```python
import torch

def _softmax(x, axis=-1):
    return torch.softmax(x, dim=axis)

def _gelu(x):
    return torch.nn.functional.gelu(x, approximate='tanh')

def _silu(x):
    return torch.nn.functional.silu(x)

def _sigmoid(x):
    return torch.sigmoid(x)

def _layernorm(x, axis=-1, eps=1e-05):
    m = x.mean(dim=axis, keepdim=True)
    v = x.var(dim=axis, keepdim=True, unbiased=False)
    return (x - m) / torch.sqrt(v + eps)

def _scatter_add(tgt, idx, src, axis=0):
    return tgt.clone().index_add_(axis, idx, src)

def _segment_sum(src, seg, n):
    out = torch.zeros((n,) + src.shape[1:], dtype=src.dtype)
    return out.index_add_(0, seg, src)

def _pad(x, pw):
    flat = tuple((v for pair in reversed(pw) for v in pair))
    return torch.nn.functional.pad(x, flat)
kernel_name = 'standalone__neighborhood_dinat__vision'

def setup_inputs():

    _w = lambda a: (lambda t: t.to(torch.bfloat16) if t.is_floating_point() else t)(torch.as_tensor(a))
    ks, dil, nh = (7, 2, 4)
    H = W = max(ks * dil, 224 // dil * dil)
    C = max(nh, 192 // nh * nh)
    return {'x': _w((lambda *s: torch.randn(*s, dtype=torch.bfloat16) * 0.2)(1, H, W, C)), 'wqkv': _w((lambda *s: torch.randn(*s, dtype=torch.bfloat16) * 0.2)(C, 3 * C)), 'wo': _w((lambda *s: torch.randn(*s, dtype=torch.bfloat16) * 0.4)(C, C)), 'rpb': _w((lambda *s: torch.randn(*s, dtype=torch.bfloat16) * 0.4)(nh, 2 * ks - 1, 2 * ks - 1))}

def reference(x, wqkv, wo, rpb):
    B, H, W, C = x.shape
    ks, dil, nh = (7, 2, 4)
    hd = C // nh
    scale = hd ** (-0.5)
    qkv = (x @ wqkv).reshape(B, H, W, 3, nh, hd)
    q = qkv[:, :, :, 0].clone()
    k = qkv[:, :, :, 1].clone()
    v = qkv[:, :, :, 2].clone()
    ih = torch.arange(H)
    iw = torch.arange(W)
    m = torch.arange(ks)
    sh = torch.clip(ih // dil - ks // 2, 0, H // dil - ks)
    sw = torch.clip(iw // dil - ks // 2, 0, W // dil - ks)
    kh = (sh[:, None] + m[None, :]) * dil + (ih % dil)[:, None]
    kw = (sw[:, None] + m[None, :]) * dil + (iw % dil)[:, None]
    rh = sh[:, None] + m[None, :] - (ih // dil)[:, None] + (ks - 1)
    rw = sw[:, None] + m[None, :] - (iw // dil)[:, None] + (ks - 1)
    ih4 = kh[:, :, None, None].clone()
    iw4 = kw[None, None, :, :].clone()
    kg = (lambda x, *d: x.permute(*d))(k[:, ih4, iw4, :, :], 0, 1, 3, 5, 2, 4, 6).reshape(B, H, W, nh, ks * ks, hd)
    vg = (lambda x, *d: x.permute(*d))(v[:, ih4, iw4, :, :], 0, 1, 3, 5, 2, 4, 6).reshape(B, H, W, nh, ks * ks, hd)
    bg = (lambda x, *d: x.permute(*d))(rpb[:, rh[:, :, None, None], rw[None, None, :, :]], 1, 3, 0, 2, 4).reshape(1, H, W, nh, ks * ks)
    logits = torch.einsum('bhwnd,bhwnkd->bhwnk', q, kg) * scale + bg
    attn = _softmax(logits, axis=-1)
    out = torch.einsum('bhwnk,bhwnkd->bhwnd', attn, vg).reshape(B, H, W, C)
    return out @ wo
```

```cpp
#include <hip/hip_runtime.h>
#include <hip/hip_bf16.h>
#include <stdint.h>

typedef __attribute__((ext_vector_type(8))) __bf16 bf16x8_t;
typedef __attribute__((ext_vector_type(4))) short s16x4_t;
typedef __attribute__((ext_vector_type(4))) float f32x4;
typedef __attribute__((ext_vector_type(4))) unsigned u32x4;
typedef __attribute__((ext_vector_type(2))) unsigned u32x2;
typedef __attribute__((ext_vector_type(2))) __bf16 bf16x2_t;

#define LDS_AS __attribute__((address_space(3)))
#define DI __device__ __forceinline__
#define SB __builtin_amdgcn_sched_barrier(0)
#define OPQ(v) asm volatile("" : "+v"(v))
#define SOPQ(v) do { v = __builtin_amdgcn_readfirstlane(v); asm volatile("" : "+s"(v)); } while (0)
#define LAM [&](int t) __attribute__((always_inline))
template <int V> struct IC { static constexpr int v = V; };
template <int I, int N, class F> __device__ __forceinline__ void sfor(F&& f) {
  if constexpr (I < N) { f(IC<I>{}); sfor<I + 1, N>(f); }
}
#define SLAM(x) [&](auto x) __attribute__((always_inline))

#define IMG 224
#define CH 192
#define ROWB 172032
#define WVOFF 0
#define KOFF 18432
#define VOFF 69632
#define TOFF 108032
#define T_HSTR 3584
#define NEGOFF (TOFF + 4 * T_HSTR)
#define DUMMY 122880
#define WKOFF 145408
#define WB1 126976
#define PBAR 4
#define WOLO 126976
#define WOHI KOFF
#define LDS_BYTES 163840
#define SLE 30720
#define SLO 0
#define WQ 69632
#define RAWB 61440
#define PFD 67584
#define XPL 10240

#define C1 0.20823509f
#define ETYPE(i) 1
#define APRIO 2
#define PPRIO 1
#define PL4 13
#define EPRIO 2
#define QPRIO 1
#define L4S 28
#define L4S3 44
#define TOG 0
#define EXN4 12
#define EXN3 9
#define TVX(i) 2
#define TVY(i) 0
#define PH do { __builtin_amdgcn_s_setprio(3); SB; } while (0)
#define PL do { __builtin_amdgcn_s_setprio(TOG); SB; } while (0)
#define LOG2E 1.4426950408889634f
#define FLAGOFF 125952
#define L_LO 0x2B800000u
#define L_HI 0x53800000u
#define NEGBF 0xF180u

DI f32x4 mfma(u32x4 a, u32x4 b, f32x4 c) {
  return __builtin_amdgcn_mfma_f32_16x16x32_bf16(__builtin_bit_cast(bf16x8_t, a), __builtin_bit_cast(bf16x8_t, b), c, 0, 0, 0);
}
DI u32x2 tr_read(const char* smem, int off) {
  s16x4_t t = __builtin_amdgcn_ds_read_tr16_b64_v4i16((LDS_AS s16x4_t*)(smem + off));
  return __builtin_bit_cast(u32x2, t);
}
DI u32x4 tr_frag(const char* smem, int off, int stride2) {
  u32x2 a = tr_read(smem, off), b = tr_read(smem, off + stride2);
  u32x4 r = {a[0], a[1], b[0], b[1]};
  return r;
}
DI void tr_lo(u32x4& r, const char* smem, int off) { u32x2 a = tr_read(smem, off); r[0] = a[0]; r[1] = a[1]; }
DI void tr_hi(u32x4& r, const char* smem, int off) { u32x2 a = tr_read(smem, off); r[2] = a[0]; r[3] = a[1]; }
DI unsigned pack2(float a, float b) {
  bf16x2_t v = {(__bf16)a, (__bf16)b};
  return __builtin_bit_cast(unsigned, v);
}
DI int clampi(int v, int lo, int hi) { return min(max(v, lo), hi); }
DI float max2(float a, float b) { return __builtin_elementwise_maximum(a, b); }
DI float max3(float a, float b, float c) { return max2(max2(a, b), c); }
DI float xmax16(float v) {
  unsigned b = __float_as_uint(v);
  auto r = __builtin_amdgcn_permlane16_swap(b, b, false, false);
  return max2(__uint_as_float(r[0]), __uint_as_float(r[1]));
}
DI float xmax32(float v) {
  unsigned b = __float_as_uint(v);
  auto r = __builtin_amdgcn_permlane32_swap(b, b, false, false);
  return max2(__uint_as_float(r[0]), __uint_as_float(r[1]));
}
#define vmov(s) ((s) | zs)

template <int NT>
DI void glds16s(const void* sbase_, unsigned voff, unsigned lds_dst) {
  unsigned long long sv = (unsigned long long)sbase_;
  unsigned slo = __builtin_amdgcn_readfirstlane((unsigned)sv), shi = __builtin_amdgcn_readfirstlane((unsigned)(sv >> 32));
  unsigned long long sbase = ((unsigned long long)shi << 32) | slo;
  lds_dst = __builtin_amdgcn_readfirstlane(lds_dst);
  if (NT)
    asm volatile("s_mov_b32 m0, %2\n\ts_nop 0\n\tglobal_load_lds_dwordx4 %0, %1 nt" :: "v"(voff), "s"(sbase), "s"(lds_dst) : "memory", "m0");
  else
    asm volatile("s_mov_b32 m0, %2\n\ts_nop 0\n\tglobal_load_lds_dwordx4 %0, %1" :: "v"(voff), "s"(sbase), "s"(lds_dst) : "memory", "m0");
}
DI void glds4s(const void* sbase_, unsigned voff, unsigned lds_dst) {
  unsigned long long sv = (unsigned long long)sbase_;
  unsigned slo = __builtin_amdgcn_readfirstlane((unsigned)sv), shi = __builtin_amdgcn_readfirstlane((unsigned)(sv >> 32));
  unsigned long long sbase = ((unsigned long long)shi << 32) | slo;
  lds_dst = __builtin_amdgcn_readfirstlane(lds_dst);
  asm volatile("s_mov_b32 m0, %2\n\ts_nop 0\n\tglobal_load_lds_dword %0, %1" :: "v"(voff), "s"(sbase), "s"(lds_dst) : "memory", "m0");
}
template <int N> DI void wait_lgkm() { __builtin_amdgcn_s_waitcnt(0xC07F | (N << 8)); }
template <int N> DI void wait_vm() { asm volatile("s_waitcnt vmcnt(%0)" :: "n"(N) : "memory"); }
DI void rawb() { asm volatile("s_barrier" ::: "memory"); }
DI void ldsb() { asm volatile("s_waitcnt lgkmcnt(0)\n\ts_barrier" ::: "memory"); }

DI void mk_lo3(int w4, int l, unsigned (&lo3)[3]) {
#pragma unroll
  for (int j = 0; j < 3; ++j) {
    int r3 = (w4 + j) % 3;
    int idx = 64 * r3 + l;
    int t = idx / 6, c = idx - 6 * t;
    int r = (t & ~12) | ((t & 4) << 1) | ((t & 8) >> 1);
    lo3[j] = r * 1152 + c * 16;
  }
}
DI void dma_w(const char* wsrc, unsigned lds_dst, int P, unsigned lo) {
  const int img = P / 18, q = P - 18 * img;
  glds16s<0>(wsrc + img * 96 + (q / 3) * 36864, lo, lds_dst + P * 1024);
}
DI void mk_lq3(int w4, int l, unsigned (&lq3)[3]) {
#pragma unroll
  for (int j = 0; j < 3; ++j) {
    const int P = w4 + 4 * j, m = (P * 43) >> 7;
    const int idx = 64 * (P - 3 * m) + l;
    const int r = (idx * 171) >> 12, pc = idx - 24 * r;
    lq3[j] = (16 * (m & 1) + 2 * (m & 2) + 8 * (r >> 2) + (r & 3)) * 1152 + (pc ^ (r & 6)) * 16;
    OPQ(lq3[j]);
  }
}
DI int p18(int w4, int i) { int P = w4 + 4 * i; return P >= 18 ? P - 3 : P; }

#define FSTAMP(k)
#define STAMP(k)
#define PPARM
#define PARG

#define TSDECL
#define TS(k)
#define TSOUT(n)
#define BWDECL
#define LDSB_X(vm) do { if (vm < 63) wait_vm<vm>(); ldsb(); } while (0)
#define LDSB_Y(vm) do { if (vm < 63) wait_vm<vm>(); ldsb(); } while (0)
#define PBW_X asm volatile("s_waitcnt vmcnt(0) lgkmcnt(%0)\n\ts_barrier" :: "n"(2 * (PBAR - 2)) : "memory")
#define PBW_Y PBW_X
#define BWOUT()
struct Geo { int A, Bc, R0, C0, or_, oc_, pa, pb; };

DI unsigned chunk_rows(int c, int or_) {
  const unsigned r3 = 0x0706u + 0x0101u * or_ + (or_ == 1 ? 0x0F200000u : 0x01000000u);
  const unsigned r4 = or_ == 1 ? 0x13121110u : (or_ == 3 ? 0x13121102u : 0x33040302u);
  unsigned r = c < 3 ? 0x09080100u + 0x01010101u * (or_ + 2 * c) : (c == 3 ? r3 : r4);
  SOPQ(r);
  return r;
}
DI void x_lane(unsigned (&xc)[3], unsigned (&xs)[3], int p, int l) {
#pragma unroll
  for (int i = 0; i < 3; ++i) {
    const int t = 8 * (i < 2 ? p + 4 * i : 8 + (p & 1)) + (l >> 3);
    const int rr = i < 2 ? (t * 205) >> 12 : 3;
    xc[i] = (t - 20 * rr) * 768 + 16 * ((l & 7) ^ ((t + 3 * rr) & 7));
    xs[i] = 8 * rr;
  }
}
DI void x_read(const char* smem, int slot, int t, int g, u32x4 (&xf)[6]) {
  int ae = slot + t * 128 + 16 * (g ^ ((t + 3 * ((t * 205) >> 12)) & 7));
  int ao = ae ^ 64;
#pragma unroll
  for (int s = 0; s < 6; ++s) xf[s] = *(const u32x4*)(smem + ((s & 1) ? ao : ae) + XPL * (s >> 1));
}

template <int NT, int D, int K0, int NX, class F>
DI void projN(const char* smem, int wb, const u32x4 (&xf)[NX][6], unsigned (&out)[NX][6], F hook) {
  u32x4 wf[D];
  f32x4 ac[3][NT];
#define PC(t) (NT == 1 ? (t) % 3 : (t) / 6)
#define PS(t) (NT == 1 ? (t) / 3 : (t) % 6)
#define PADDR(t) (wb + PS(t) * 3072 + PC(t) * 32)
#define PPACK(c, k) do { out[K0 + (k)][2 * (c)] = pack2(ac[c][k][0], ac[c][k][1]); out[K0 + (k)][2 * (c) + 1] = pack2(ac[c][k][2], ac[c][k][3]); \
                         OPQ(out[K0 + (k)][2 * (c)]); OPQ(out[K0 + (k)][2 * (c) + 1]); } while (0)
#pragma unroll
  for (int t = 0; t < D - 1; ++t) wf[t] = tr_frag(smem, PADDR(t), 768);
  SB;
  hook(-1);
  SB;
#pragma unroll
  for (int t = 0; t < 18; ++t) {
    const int c = PC(t), s = PS(t);
#pragma unroll
    for (int k = 0; k < NT; ++k) {
      ac[c][k] = mfma(wf[t % D], xf[K0 + k][s], s == 0 ? (f32x4){0.f, 0.f, 0.f, 0.f} : ac[c][k]);
      SB;
      if (t + D - 1 < 18) {
        if (k == 0) tr_lo(wf[(t + D - 1) % D], smem, PADDR(t + D - 1));
        if (k == (NT > 1 ? 1 : 0)) tr_hi(wf[(t + D - 1) % D], smem, PADDR(t + D - 1) + 768);
      }
      if (NT > 1 && k == NT - 1 && c >= 1 && s >= 1 && s <= NT) PPACK(c >= 1 ? c - 1 : 0, s >= 1 && s <= NT ? s - 1 : 0);
      SB;
    }
    hook(t);
    SB;
  }
#pragma unroll
  for (int c = (NT == 1 ? 0 : 2); c < 3; ++c)
#pragma unroll
    for (int k = 0; k < NT; ++k) PPACK(c, k);
  SB;
  hook(18);
  SB;
#undef PC
#undef PS
#undef PADDR
#undef PPACK
}

DI void aug_k(int tc, int g, unsigned& a0, unsigned& a1) {
  const unsigned m = ((~((0x7Fu << (tc & 31)) >> 6) & 0x3FFFu) | ((tc & 32) << 9)) >> (4 * g);
  a0 = ((m & 1) | ((m & 2) << 15)) * 0x3F80u;
  a1 = (((m >> 2) & 1) | ((m & 8) << 13)) * 0x3F80u;
}
DI void kv_addr(unsigned e, int c, int g, int& ka, int& va) {
  const int n = 20 * (int)(e & 31) + c;
  ka = KOFF + 128 * n + 16 * (g ^ (n & 7));
  va = VOFF + 96 * n + 8 * (g ^ ((c >> 2) & 3));
}

DI void x_addr(unsigned (&vo)[4], const unsigned (&xc)[3], const unsigned (&xs)[3], int p, unsigned rows) {
#pragma unroll
  for (int i = 0; i < 3; ++i) vo[i] = xc[i] + __builtin_amdgcn_ubfe(rows, xs[i], 8) * ROWB;
  unsigned f = vo[2], m = p < 2 ? ~0u : 0u;
  SOPQ(f); SOPQ(m);
  vo[3] = (vo[2] & m) | (f & ~m);
  OPQ(vo[0]); OPQ(vo[1]); OPQ(vo[2]); OPQ(vo[3]);
}
DI void dma_x(const char* xs, const unsigned (&vo)[4], unsigned lds_base, unsigned slot, int p, int i, bool late = false) {
  const int v = i < 6 ? i % 3 : (p < 2 ? i - 6 : 2);
  const int j = i < 3 ? p : (i < 6 ? p + 4 : 8 + (p & 1));
  glds16s<1>(xs + 128 * v, vo[i < 7 ? i / 3 : 3], lds_base + (i == 7 && p >= 2 ? PFD + 1024 : slot + v * XPL + j * 1024));
}

DI void mk_tq(const char* smem, int raw, int tid, unsigned (&tq)[4][2]) {
  const int r = min(tid, 223);
  const int seg = (r >= 55) + (r >= 110) + (r >= 167);
  const int m = seg ^ (seg >> 1);
  const int o = r - (int)((0x71A93802u >> (8 * m)) & 0xFF);
  unsigned tv[4][4];
  bool ok[4];
#pragma unroll
  for (int i = 0; i < 4; ++i) {
    const int lin = 4 * o + m + i;
    const int row = lin >> 4, dc = lin & 15;
    ok[i] = row >= 0 && row <= 12 && dc <= 12;
    const int src = raw + 2 * (clampi(row, 0, 12) * 13 + min(dc, 12));
#pragma unroll
    for (int hh = 0; hh < 4; ++hh) tv[hh][i] = *(const uint16_t*)(smem + src + hh * 338);
  }
  SB;
#pragma unroll
  for (int i = 0; i < 4; ++i)
#pragma unroll
    for (int hh = 0; hh < 4; ++hh) OPQ(tv[hh][i]);
  const unsigned m0 = (ok[0] ? 0xFFFFu : 0u) | (ok[1] ? 0xFFFF0000u : 0u), m1 = (ok[2] ? 0xFFFFu : 0u) | (ok[3] ? 0xFFFF0000u : 0u);
#pragma unroll
  for (int hh = 0; hh < 4; ++hh) {
    tq[hh][0] = (tv[hh][0] | (tv[hh][1] << 16)) & m0; tq[hh][1] = (tv[hh][2] | (tv[hh][3] << 16)) & m1;
    OPQ(tq[hh][0]); OPQ(tq[hh][1]);
  }
}

template <int KV, int LAST, class B>
DI void proj_phase(char* smem, int wb, int wbn, const u32x4 (&xf)[6][6], const int (&ka)[6], const int (&va)[6],
                   u32x4 (&wf)[3], f32x4 (&ac)[2][6], const char* dsrc, unsigned dstr, unsigned ddst, int p, const unsigned (&lo3)[3], B bar) {
  constexpr int D = 3;
  unsigned hold[6][2];
#define PADR(t) (((t) < 18 ? wb : wbn) + ((t) % 6) * 3072 + (((t) % 18) / 6) * 32)
#pragma unroll
  for (int t = 0; t < 18 + LAST; ++t) {
    const int c = t / 6, s = t % 6, ab = (c + KV) & 1;
#pragma unroll
    for (int k = 0; k < 6; ++k) {
      if (t < 18) ac[ab][k] = mfma(wf[t % D], xf[k][s], s == 0 ? (f32x4){0.f, 0.f, 0.f, 0.f} : ac[ab][k]);
      if (t + 2 < (LAST ? 18 : 20) && (k == 0 || k == 2)) {
        SB;
        if (k == 0) tr_lo(wf[(t + 2) % D], smem, PADR(t + 2));
        else tr_hi(wf[(t + 2) % D], smem, PADR(t + 2) + 768);
        SB;
      }
      if (t == 18 || (s < 3 && (k == 1 || k == 4))) {
        const int kb = t == 18 ? k : 2 * s + (k == 4 ? 1 : 0);
        const int pc = (c + 2) % 3, kv = c == 0 ? 1 - KV : KV;
        unsigned p0 = pack2(ac[ab ^ 1][kb][0], ac[ab ^ 1][kb][1]), p1 = pack2(ac[ab ^ 1][kb][2], ac[ab ^ 1][kb][3]);
        if (kv == 0) {
          u32x2 pk = {p0, p1};
          *(u32x2*)(smem + va[kb] + 32 * pc) = pk;
        } else if (pc == 0) {
          hold[kb][0] = p0; hold[kb][1] = p1;
        } else if (pc == 1) {
          u32x4 pk = {hold[kb][0], hold[kb][1], p0, p1};
          *(u32x4*)(smem + ka[kb]) = pk;
        } else {
          u32x2 pk = {p0, p1};
          *(u32x2*)(smem + (ka[kb] ^ 64)) = pk;
        }
        SB;
      }
    }
    SB;
    if (t == PBAR || t == 15) bar(t);
    if (t > PBAR && t <= PBAR + 10 && ((t - PBAR) & 1)) {
      const int i = (t - PBAR) / 2, P = p18(p, i);
      glds16s<0>(dsrc + (P / 3) * dstr, lo3[i % 3], ddst + P * 1024);
    }
    SB;
  }
#undef PADR
}

template <bool SAFE>
DI bool proj_wave(char* smem, unsigned lds_base, const char* xb, const uint16_t* __restrict__ wqkv,
                  const uint16_t* __restrict__ wo, const uint16_t* __restrict__ rpb, const Geo& G, int p, int l PPARM) {
  const int u = l & 15, g = l >> 4;
  TSDECL;
  const char* xq = xb + (size_t)G.or_ * ROWB;
  unsigned vo[3][4], lo3[3];
  int ka[6], va[6];
  unsigned ag[6][2];
  auto kv_setup = [&]() __attribute__((always_inline)) {
    const int t = 16 * p + u, rr = (t * 205) >> 12, c = t - 20 * rr;
    unsigned a0, a1;
    aug_k(c, g, a0, a1);
    const unsigned fl = g == 3 ? 0x3F80u : 0u;
#pragma unroll
    for (int k = 0; k < 6; ++k) {
      if (k == 4) {
        const unsigned e = chunk_rows(p, G.or_) >> 24;
        kv_addr(e, 4 + u, g, ka[k], va[k]);
        aug_k(4 + u + (e & 32), g, ag[k][0], ag[k][1]);
      } else {
        const unsigned e = __builtin_amdgcn_ubfe(chunk_rows(k < 4 ? k : 4, G.or_), 8 * rr, 8);
        kv_addr(e, c, g, ka[k], va[k]);
        ag[k][0] = a0;
        ag[k][1] = k < 3 ? a1 : (a1 | ((e & 32) ? fl : 0u));
      }
      OPQ(ka[k]);
      if (k >= 3) OPQ(ag[k][1]);
    }
    OPQ(ag[4][0]);
  };
  __builtin_amdgcn_s_setprio(3);
  unsigned xc[3], xs[3];
  x_lane(xc, xs, p, l);
  x_addr(vo[0], xc, xs, p, 0x09080100u);
  glds16s<0>(rpb, 16 * l, lds_base + RAWB + 1536 * p);
  glds4s(rpb, 1024 + 4 * l, lds_base + RAWB + 1536 * p + 1024);
  glds4s(rpb, min(1280 + 4 * l, 1348), lds_base + RAWB + 1536 * p + 1280);
  TS(0);
#pragma unroll
  for (int i = 0; i < 8; ++i) dma_x(xq + 2 * ROWB, vo[0], lds_base, SLO, p, i);
  {
    const int line = ((blockIdx.x >> 3) & 31) * 72 + 18 * p + min(l, 17);
    const bool lw = line < 1728;
    const unsigned long long dw = (unsigned long long)wo - (unsigned long long)wqkv - 1728ull * 128ull;
    const char* pa = (const char*)wqkv + line * 128 + (lw ? 0ull : dw);
    asm volatile("s_mov_b32 m0, %1\n\ts_nop 0\n\tglobal_load_lds_dword %0, off"
                 :: "v"(pa), "s"(__builtin_amdgcn_readfirstlane(lds_base + PFD + 256 * p)) : "memory", "m0");
  }
  TS(1);
  __builtin_amdgcn_s_setprio(0);
  mk_lo3(p, l, lo3);
#pragma unroll
  for (int j = 0; j < 3; ++j) OPQ(lo3[j]);
  const int wlane = (16 * (g >> 1) + 4 * (g & 1) + (u >> 2)) * 96 + 8 * (u & 3);
  int wbk = WKOFF + wlane;
  OPQ(wbk);
  x_addr(vo[1], xc, xs, p, 0x0706u + 0x0101u * G.or_ + (G.or_ == 1 ? 0x0F010000u : 0x01000000u));
  u32x4 xf[6][6];
  unsigned k0[6][6];
  unsigned tq[4][2];
  const int tp = 16 * p + u, te = 64 + u, tid = 64 * p + l;
  x_addr(vo[2], xc, xs, p, G.or_ == 1 ? 0x13121110u : (G.or_ == 3 ? 0x13121102u : 0x12040302u));
  kv_setup();
  SB;
  wait_vm<9>();
  mk_tq(smem, RAWB + 1536 * p, tid, tq);
  SB;
  TS(2);
#pragma unroll
  for (int i = 0; i < 5; ++i) dma_w((const char*)wqkv + 384, lds_base + WKOFF, p18(p, i), lo3[i % 3]);
  TS(3);
  __builtin_amdgcn_s_setprio(3);
  wait_vm<5>(); rawb();
  TS(4);
  x_read(smem, SLE, tp, g, xf[0]);
  x_read(smem, SLO, tp, g, xf[1]);
  x_read(smem, (p & 1) ? SLO : SLE, te, g, xf[4]);
  wait_vm<0>(); ldsb();
  TS(5);
  __builtin_amdgcn_s_setprio(QPRIO);
  projN<2, 6, 0>(smem, wbk, xf, k0, LAM {
    if (t < 0) {
#pragma unroll
      for (int i = 0; i < 4; ++i) dma_x(xq + 4 * ROWB, vo[0], lds_base, SLE, p, i, true);
    }
    else if (t < 4) dma_x(xq + 4 * ROWB, vo[0], lds_base, SLE, p, t + 4, true);
    else if (t < 12) dma_x(xb, vo[1], lds_base, SLO, p, t - 4, true);
    if (t == 11) TS(6);
  });
  TS(7);
  TS(8);
  __builtin_amdgcn_s_setprio(3);
  wait_vm<0>();
  rawb();
  TS(9);
  x_read(smem, SLE, tp, g, xf[2]);
  x_read(smem, SLO, tp, g, xf[3]);
  if (p >= 2) x_read(smem, (p & 1) ? SLO : SLE, te, g, xf[4]);
  auto k_write = [&](int k) __attribute__((always_inline)) {
    u32x4 pa = {k0[k][0], k0[k][1], k0[k][2], k0[k][3]};
    u32x4 pb = {k0[k][4], k0[k][5], ag[k][0], ag[k][1]};
    *(u32x4*)(smem + ka[k]) = pa;
    *(u32x4*)(smem + (ka[k] ^ 64)) = pb;
  };
  projN<2, 6, 2>(smem, wbk, xf, k0, LAM {
    if (t < 0) {
      ldsb();
      TS(10);
#pragma unroll
      for (int i = 0; i < 4; ++i) dma_x(xb, vo[2], lds_base, SLE, p, i, true);
      __builtin_amdgcn_s_setprio(QPRIO);
    }
    else if (t < 4) dma_x(xb, vo[2], lds_base, SLE, p, t + 4, true);
    else if (t < 9) dma_w((const char*)wqkv + 768, lds_base + WVOFF, p18(p, t - 4), lo3[(t - 4) % 3]);
  });
  projN<2, 6, 4>(smem, wbk, xf, k0, LAM {
    if (t < 0) {
      TS(11);
      __builtin_amdgcn_s_setprio(EPRIO);
      wait_vm<5>(); rawb();
      STAMP(5);
      x_read(smem, SLE, tp, g, xf[5]);
      ldsb();
    }
    else if (t % 4 == 0 && t < 16) k_write(t / 4);
    else if (t == 18) { k_write(4); k_write(5); }
  });
  f32x4 tf[4];
#pragma unroll
  for (int hh = 0; hh < 4; ++hh) {
    tf[hh] = (f32x4){__uint_as_float(tq[hh][0] << 16) * LOG2E, __uint_as_float(tq[hh][0] & 0xFFFF0000u) * LOG2E,
                     __uint_as_float(tq[hh][1] << 16) * LOG2E, __uint_as_float(tq[hh][1] & 0xFFFF0000u) * LOG2E};
    OPQ(tf[hh]);
  }
  {
    const int t = 16 * p + u, c = t - 20 * ((t * 205) >> 12);
#pragma unroll
    for (int k = 0; k < 6; ++k) {
      va[k] = VOFF + 96 * ((ka[k] - KOFF) >> 7) + 8 * (g ^ (((k == 4 ? 4 + u : c) >> 2) & 3));
      OPQ(va[k]);
    }
  }
  SB;
  STAMP(4);
  wait_vm<0>(); rawb();
  STAMP(6);
  if (tid < 224) {
#pragma unroll
    for (int hh = 0; hh < 4; ++hh) *(f32x4*)(smem + TOFF + hh * T_HSTR + 16 * tid) = tf[hh];
  } else if (tid < 226) {
    const float nb = __uint_as_float(NEGBF << 16);
    *(f32x4*)(smem + NEGOFF + 448 * (tid - 224)) = (f32x4){nb, nb, nb, nb};
  }
  SB;
  STAMP(7);
  ldsb();
#pragma unroll
  for (int i = 0; i < 5; ++i) dma_w((const char*)wqkv + 384 + 96, lds_base + WB1, p18(p, i), lo3[i % 3]);
  TSOUT(12);
  __builtin_amdgcn_s_setprio(PPRIO);
  u32x4 wf[3];
  f32x4 ac[2][6];
#pragma unroll
  for (int k = 0; k < 6; ++k)
    ac[1][k] = (f32x4){__uint_as_float(k0[k][4] << 16), __uint_as_float(k0[k][4] & 0xFFFF0000u),
                       __uint_as_float(k0[k][5] << 16), __uint_as_float(k0[k][5] & 0xFFFF0000u)};
  int wa = WVOFF + wlane, wb = WB1 + wlane, wc = WKOFF + wlane;
  unsigned da = lds_base + WKOFF, db = lds_base + WVOFF, dc = lds_base + WB1;
  wf[0] = tr_frag(smem, wa, 768);
  wf[1] = tr_frag(smem, wa + 3072, 768);
  BWDECL;
  int h = 0;
  auto barY = [&](int t) __attribute__((always_inline)) {
    if (t == PBAR) { if (h != 0) PBW_Y; }
    else if (h == 0) { wait_vm<5>(); rawb(); }
  };
  auto barX = [&](int t) __attribute__((always_inline)) { if (t == PBAR) PBW_X; };
  unsigned dstr = 36864;
  const char* dsk = (const char*)wqkv + 384 + 192;
#pragma unroll 1
  for (; h < 3; ++h) {
    OPQ(wa); OPQ(wb); OPQ(wc);
    STAMP(8 + 4 * h);
    proj_phase<0, 0>(smem, wa, wb, xf, ka, va, wf, ac, (const char*)wqkv + 768 + 96 * (h + 1), 36864, da, p, lo3, barY);
    STAMP(10 + 4 * h);
    if (h == 2) {
#pragma unroll
      for (int j = 0; j < 3; ++j) {
        int idx = 64 * ((p + j) % 3) + l;
        int row = idx / 24, pc = idx - 24 * row;
        lo3[j] = row * 384 + ((pc >> 1) ^ ((row >> 1) & 3)) * 32 + (pc & 1) * 16;
      }
      dstr = 3072;
      dsk = (const char*)wo;
    }
    proj_phase<1, 0>(smem, wb, wc, xf, ka, va, wf, ac, dsk, dstr, db, p, lo3, barX);
    dsk += 96;
    const int wt = wa; wa = wc; wc = wb; wb = wt;
    const unsigned dt = da; da = dc; dc = db; db = dt;
  }
  STAMP(20);
  proj_phase<0, 1>(smem, wa, wa, xf, ka, va, wf, ac, (const char*)wo + 18432, 3072, lds_base + WKOFF, p, lo3, barY);
  STAMP(21);
  LDSB_X(63);
  BWOUT();
  STAMP(22);
#pragma unroll
  for (int t = 0; t < 9; ++t) { int P = p + 4 * t; glds16s<0>((const char*)wo + 36864 + (P / 3) * 3072, lo3[t % 3], lds_base + WOHI + P * 1024); }
  wait_vm<0>();
  STAMP(23);
  ldsb();
  if (SAFE) return false;
  rawb();
  const u32x4 fl = *(const u32x4*)(smem + FLAGOFF);
  return __builtin_amdgcn_readfirstlane(fl[0] | fl[1] | fl[2] | fl[3]) != 0;
}

template <int NU> constexpr int g_cnt(int s) { return (s < NU - 1 ? s : NU - 1) - (s > 3 ? s - 3 : 0) + 1; }
template <int NU> constexpr int g_first(int s) { int c = 0; for (int i = 0; i < s; ++i) c += g_cnt<NU>(i); return c; }
template <int NU> constexpr int g_step(int j) { int s = 0; while (g_first<NU>(s + 1) <= j) ++s; return s; }
template <int NU> constexpr int g_unit(int j) { int s = g_step<NU>(j); return (s > 3 ? s - 3 : 0) + j - g_first<NU>(s); }
template <int NU> constexpr int g_idx(int k, int t) { return g_first<NU>(k + t) + k - (k + t > 3 ? k + t - 3 : 0); }

template <int NU> constexpr bool q_dbl(int t) { return NU == 4 || t < 72; }
template <int NU> constexpr int q_c(int t) { return q_dbl<NU>(t) ? (t % 72) / 6 : (t >= 156 ? 2 : 2 * ((t % 72) / 12) + (t & 1)); }
template <int NU> constexpr int q_s(int t) { return q_dbl<NU>(t) ? t % 6 : (t >= 156 ? t - 156 : (t % 12) / 2); }
template <int NU> constexpr int q_gi(int t) { return q_dbl<NU>(t) ? t / 6 : t / 12; }
template <int NU> constexpr int q_off(int t) { return NU == 3 && t >= 144 ? q_s<NU>(t) * 3072 + q_c<NU>(t) * 32 : (q_s<NU>(t) % 3) * 12288 + (q_c<NU>(t) >> 2) * 128; }
#define XBJ (NG - 4)
#define QD 6
#define L23S 50
#define XSP 2

template <int NU, bool SAFE>
DI bool attn_wave(char* smem, unsigned lds_base, const char* xb, const uint16_t* __restrict__ wqkv,
                  uint16_t* __restrict__ out, const Geo& G, int a, int l PPARM) {
  const int u = l & 15, g = l >> 4;
  const int j_ = a & 1, ib = a >> 1;
  TSDECL;
  __builtin_amdgcn_s_setprio(3);
  unsigned lq3[3];
  {
    unsigned vo[4], xc[3], xs[3];
    x_lane(xc, xs, a, l);
    x_addr(vo, xc, xs, a, 0x09080100u);
#pragma unroll
    for (int i = 0; i < 8; ++i) dma_x(xb + (size_t)G.or_ * ROWB, vo, lds_base, SLE, a, i);
  }
  __builtin_amdgcn_s_setprio(1);
  mk_lq3(a, l, lq3);
#define DMA_Q(i0, i1) do { SB; _Pragma("unroll") for (int i = i0; i < i1; ++i) \
    glds16s<0>((const char*)wqkv + (i / 3) * 36864, lq3[i % 3], lds_base + WQ + a * 1024 + i * 4096); SB; } while (0)
  DMA_Q(0, 6);
  const int uq = min(u, 13);
  const int qa = uq >= 7 ? 1 : 0;
  const int qb = uq - 7 * qa;
  const int tu = 20 * (2 * ib + qa) + G.oc_ + 7 * j_ + qb;
  int xa = tu * 128 + 16 * (g ^ ((tu + 3 * (2 * ib + qa)) & 7)), xo = xa ^ 64;
  OPQ(xa); OPQ(xo);
#define X_FRAG(slot, s) (*(const u32x4*)(smem + (((s) & 1) ? xo : xa) + (slot) + XPL * ((s) >> 1)))
  const int b0 = G.Bc + 7 * j_;
  const int kb0 = clampi(b0 - 3, 0, 99), kc0 = kb0 - G.C0;
  const int wlane = (16 * (g >> 1) + 4 * (g & 1) + (u >> 2)) * 96 + 8 * (u & 3);
  int wbk = WKOFF + wlane;
  OPQ(wbk);
  DMA_Q(6, 8);
  int wq[2][4];
#pragma unroll
  for (int b = 0; b < 4; ++b) {
    const int rowl = 4 * g + (u >> 2);
    wq[0][b] = WQ + rowl * 384 + 32 * (b ^ ((rowl >> 1) & 3)) + 8 * (u & 3);
    wq[1][b] = wq[0][b] + 36864;
    OPQ(wq[0][b]); OPQ(wq[1][b]);
  }
  DMA_Q(8, 10);
  unsigned augB0, augB1;
  int baseK, baseV, baseC;
  {
    const int b = b0 + qb;
    const int sbrel = clampi(b - 3, 0, 105) - G.C0;
    unsigned v[4];
#pragma unroll
    for (int jj = 0; jj < 4; ++jj) v[jj] = (sbrel == 4 * g + jj || 4 * g + jj == 14) ? NEGBF : 0u;
    augB0 = v[0] | (v[1] << 16);
    augB1 = v[2] | (v[3] << 16);
    OPQ(augB0); OPQ(augB1);
    DMA_Q(10, 12);
    const int colK = kc0 + u <= 19 ? kc0 + u : kc0 + u - 16;
    baseK = KOFF + 128 * colK + 16 * (g ^ (colK & 7));
    const int cV = kc0 + min(4 * g + (u >> 2), 12);
    baseV = VOFF + 96 * cV + 8 * ((u & 3) ^ ((cV >> 2) & 3));
    const int beta_c = kb0 - b + 6;
    const int m = beta_c & 3;
    const int om = (0x71A93802u >> (8 * m)) & 0xFF;
    baseC = TOFF + 16 * om + 4 * (beta_c - m) + 16 * g - 64 * qa + 192;
    OPQ(baseC);
  }
  const bool edge = G.or_ == (NU == 4 ? 1 : 5);
  const int pb0 = ((G.or_ - 3) >> 1) + (NU == 4 ? 0 : 4), pbs = edge ? (NU == 4 ? 3 : 1) : pb0;
  int aKa = baseK + 5120 * pb0, aKs = baseK + 5120 * pbs, aV = baseV + 3840 * pb0, aVs = baseV + 3840 * pbs;
  OPQ(aKa); OPQ(aKs); OPQ(aV); OPQ(aVs);
  int negv = NEGOFF;
  OPQ(negv);
  DMA_Q(12, 14);
  int kae = 0, vae = 0;
  unsigned ae0 = 0, ae1 = 0;
  if (NU == 3) {
    const unsigned e = chunk_rows(4, G.or_) >> 24;
    kv_addr(e, 4 + u, g, kae, vae);
    aug_k(4 + u + (e & 32), g, ae0, ae1);
    OPQ(kae); OPQ(vae); OPQ(ae0); OPQ(ae1);
  }
  DMA_Q(14, 18);
  __builtin_amdgcn_s_setprio(2);
  STAMP(1);
  u32x4 xu[NU][6];
  TS(6);
  wait_vm<18>(); rawb();
  TS(7);
#pragma unroll
  for (int s = 0; s < 6; ++s) { xu[0][s] = X_FRAG(SLE, s); xu[1][s] = X_FRAG(SLO, s); }
  unsigned Q[NU][24];
  u32x4 xe[1][6];
  unsigned ke[6];
  float oi[12];
  SB;
  TS(1);
  wait_vm<0>(); ldsb();
  STAMP(2);
  TS(2);
  {
    constexpr int D = QD, NS = NU == 4 ? 144 : 162;
    u32x4 wf[D];
    f32x4 ac[2][2];
    sfor<0, D - 1>(SLAM(tc_) { constexpr int t = decltype(tc_)::v; wf[t] = tr_frag(smem, wq[q_s<NU>(t) / 3][q_c<NU>(t) & 3] + q_off<NU>(t), 6144); });
    SB;
    sfor<0, NS + 2>(SLAM(tc_) {
      constexpr int t = decltype(tc_)::v, wi = t / 72, t1 = t % 72;
      constexpr bool dbl = q_dbl<NU>(t), kb = NU == 3 && wi == 2;
      constexpr int s = t < NS ? q_s<NU>(t) : 0, gi = q_gi<NU>(t), gs = dbl ? t % 6 : t % 12;
      constexpr int tn = t + D - 1;
      const f32x4 z = {0.f, 0.f, 0.f, 0.f};
      if constexpr (t < NS) {
        constexpr int ch = dbl || (kb && t1 >= 12) ? 0 : (t & 1);
        constexpr bool kn = NU == 3 && tn >= 144;
        const int an = (kn ? wbk : wq[(q_s<NU>(tn) / 3) & 1][q_c<NU>(tn) & 3]) + q_off<NU>(tn < NS ? tn : 0);
        if constexpr (kb) ac[gi & 1][ch] = mfma(wf[t % D], xe[0][s], s == 0 ? z : ac[gi & 1][ch]);
        else ac[gi & 1][ch] = mfma(wf[t % D], xu[2 * wi][s], s == 0 ? z : ac[gi & 1][ch]);
        SB;
        if (tn < NS) tr_lo(wf[tn % D], smem, an);
        SB;
        if constexpr (dbl) ac[gi & 1][1] = mfma(wf[t % D], xu[2 * wi + 1][s], s == 0 ? z : ac[gi & 1][1]);
        SB;
        if (tn < NS) tr_hi(wf[tn % D], smem, an + (kn ? 768 : 6144));
      }
      constexpr int tp_ = t < NS ? t - gs - 1 : NS - 1;
      sfor<0, 4>(SLAM(ec_) {
        constexpr int e = decltype(ec_)::v, k = e >> 1;
        if constexpr (t < NS ? (tp_ >= 0 && gs == 1 + e) : (t - NS == e / 2)) {
          constexpr int pg = q_gi<NU>(tp_), pw = tp_ / 72;
          const float a0 = ac[pg & 1][k][2 * (e & 1)], a1 = ac[pg & 1][k][2 * (e & 1) + 1];
          if constexpr (NU == 3 && pw == 2) {
            constexpr int o = (tp_ >= 156 ? 4 : 2 * k) + (e & 1);
            if (!(tp_ >= 156 && k == 1)) { ke[o] = pack2(a0, a1); OPQ(ke[o]); }
          } else {
            constexpr int uu = 2 * pw + (q_dbl<NU>(tp_) ? k : 0), cb = q_dbl<NU>(tp_) ? q_c<NU>(tp_) : q_c<NU>(tp_) - 1 + k;
            constexpr int o = 2 * cb + (e & 1);
            if constexpr (uu == NU - 1 && o >= 18) {
              oi[2 * (o - 18)] = a0; oi[2 * (o - 18) + 1] = a1;
              OPQ(oi[2 * (o - 18)]); OPQ(oi[2 * (o - 18) + 1]);
            } else {
              Q[uu][o] = pack2(a0 * C1, a1 * C1);
              OPQ(Q[uu][o]);
            }
          }
        }
      });
      SB;
      if (t == 0) TS(0);
      if (t == 30) TS(3);
      if (t == L23S) TS(4);
      if (t == L23S + 6 * XSP - 1 + D) TS(5);
      if (t == 72 + (NU == 4 ? L4S : L4S3)) TS(8);
      if (t == 72 + (NU == 4 ? L4S + 3 : L4S3 + D)) TS(9);
      if (t == 72 + (NU == 4 ? L4S + 4 : L4S3 + D + 1)) TS(10);
      if (t == NS + 1) TS(11);
      if (t == L23S) {
        rawb();
        STAMP(3);
      }
      if (t >= L23S && t < L23S + 6 * XSP) {
        constexpr int i = (t - L23S) / XSP;
        if ((t - L23S) % XSP == 0) xu[2][i] = X_FRAG(SLE, i);
        if (NU == 4 && (t - L23S) % XSP == XSP - 1) xu[NU - 1][i] = X_FRAG(SLO, i);
        SB;
      }
      if (t == L23S + 6 * XSP - 1 + D) { rawb(); SB; }
      if (t == 72 + (NU == 4 ? L4S : L4S3)) {
        rawb();
        STAMP(5);
        if (NU == 3) x_read(smem, SLE, 64 + u, g, xe[0]);
        SB;
      }
      if (t == 72 + (NU == 4 ? L4S + 3 : L4S3 + D)) { rawb(); SB; }
    });
  }
  if (NU == 3) {
    u32x4 pa = {ke[0], ke[1], ke[2], ke[3]};
    u32x4 pb = {ke[4], ke[5], ae0, ae1};
    *(u32x4*)(smem + kae) = pa;
    *(u32x4*)(smem + (kae ^ 64)) = pb;
  }
  SB;
  rawb();
  STAMP(6);
  u32x4 Qa[NU], Qb[NU];
  unsigned R[NU][18];
#pragma unroll
  for (int k = 0; k < NU; ++k) {
    Qa[k] = (u32x4){Q[k][0], Q[k][1], Q[k][2], Q[k][3]};
    Qb[k] = (u32x4){Q[k][4], Q[k][5], augB0, augB1};
#pragma unroll
    for (int e = 0; e < 18; ++e) R[k][e] = (k == NU - 1 && e >= 12) ? 0u : Q[k][6 + e];
  }
  STAMP(7);
  TSOUT(12);
  ldsb();
  __builtin_amdgcn_s_setprio(APRIO);

  constexpr int NG = 4 * NU, NS_ = NU + 3;
  const u32x4 ones = {0x3F803F80u, 0x3F803F80u, 0x3F803F80u, 0x3F803F80u};
  const f32x4 zero4 = {0.f, 0.f, 0.f, 0.f};
  u32x4 kf[2][4];
  f32x4 T[8];
  f32x4 S[NG][2];
  f32x4 o[NU][4];
  float inv, nt0, nt1;
  unsigned lmn = 0x3F800000u, lmx = 0x3F800000u;
#pragma unroll
  for (int db = 0; db < 3; ++db) o[NU - 1][db + 1] = (f32x4){oi[4 * db], oi[4 * db + 1], oi[4 * db + 2], oi[4 * db + 3]};
  o[NU - 1][0] = (f32x4){1.0f / C1, 0.f, 0.f, 0.f};
  int aCm, aC0, aC7, aCx, aCy, aCz;
  int hoff = 0;
#define C_ADDR() do { aCm = baseC + hoff; aC0 = qa ? negv : aCm; aC7 = qa ? aCm : negv; \
    aCy = aCm + (edge ? (NU == 4 ? 512 : -512) : 0); aCx = edge ? aCy : (NU == 4 ? aC0 : aC7); aCz = edge ? aCm : (NU == 4 ? aC7 : aC0); } while (0)
#define C_OWN(k, r) ((r) == 0 || (r) == 7 || (NU == 4 && (k) == 0 && (r) == 1) || (NU == 3 && (k) == 2 && (r) == 6))
#define C_SEL(k, r) (NU == 4 ? ((r) == 0 ? ((k) == 0 ? aCx : aC0) : ((r) == 7 ? ((k) < 2 ? aCz : aC7) : ((k) == 0 && (r) == 1 ? aCy : aCm))) \
                             : ((r) == 0 ? ((k) == 0 ? aC0 : aCz) : ((r) == 7 ? ((k) == 2 ? aCx : aC7) : ((k) == 2 && (r) == 6 ? aCy : aCm))))
#define LD_D(k, r) S[g_idx<NU>(k, (r) / 2)][(r) & 1] = *(const f32x4*)(smem + C_SEL(k, r) + 64 * (r))
#define LD_T(r) T[r] = *(const f32x4*)(smem + aCm + 64 * (r))
#define LD_K(s, i) do { const int ab_ = (s) == (NU == 4 ? 0 : 5) ? aKs : aKa; \
    kf[(s) & 1][i] = *(const u32x4*)(smem + ((((i) >> 1) ^ ((i) & 1)) ? ab_ ^ 64 : ab_) + 5120 * (s) + 2560 * ((i) & 1)); } while (0)
#define LD_V(s, db) vf[(s) & 1][db] = tr_frag(smem, ((s) == (NU == 4 ? 0 : 5) ? aVs : aV) + 3840 * (s) + 32 * (db), 1920)
#define X_PRE() do { LD_D(0, 0); if (NU == 4) LD_D(0, 1); else LD_T(1); LD_K(0, 0); LD_K(0, 1); LD_K(0, 2); LD_K(0, 3); \
    LD_T(2); LD_T(3); LD_K(1, 0); LD_K(1, 1); LD_K(1, 2); LD_K(1, 3); LD_D(1, 0); if (NU == 4) LD_T(1); } while (0)
#define NORM_A(k, i) do { \
      if ((i) == 0) { const float l_ = o[k][0][0]; inv = __builtin_amdgcn_rcpf(l_); OPQ(inv); } \
      else { const f32x4 ov_ = o[k][i]; nt0 = ov_[0] * inv; nt1 = ov_[1] * inv; OPQ(nt0); OPQ(nt1); } } while (0)
#define NORM_B(k, i) do { \
      if ((i) == 0) { const float l_ = o[k][0][0]; \
        if (!SAFE) { lmn = min(lmn, __float_as_uint(l_)); lmx = max(lmx, __float_as_uint(l_)); OPQ(lmn); OPQ(lmx); } } \
      else { const f32x4 ov_ = o[k][i]; \
        R[k][10 + 2 * (i)] = pack2(nt0, nt1); R[k][11 + 2 * (i)] = pack2(ov_[2] * inv, ov_[3] * inv); \
        OPQ(R[k][10 + 2 * (i)]); OPQ(R[k][11 + 2 * (i)]); } } while (0)
#define NORM_PART(k, i) do { NORM_A(k, i); NORM_B(k, i); } while (0)
#define ROT_A(k, i) do { \
      if ((i) == 0) { Qa[k][0] = vmov(R[k][0]); Qa[k][1] = vmov(R[k][1]); } \
      else if ((i) == 1) { Qb[k][0] = vmov(R[k][4]); Qb[k][1] = vmov(R[k][5]); } \
      else if ((i) == 2) { R[k][3] = vmov(R[k][9]); R[k][4] = vmov(R[k][10]); } \
      else { R[k][8] = vmov(R[k][14]); R[k][9] = vmov(R[k][15]); } } while (0)
#define ROT_B(k, i) do { \
      if ((i) == 0) { Qa[k][2] = vmov(R[k][2]); Qa[k][3] = vmov(R[k][3]); } \
      else if ((i) == 1) { R[k][0] = vmov(R[k][6]); R[k][1] = vmov(R[k][7]); R[k][2] = vmov(R[k][8]); } \
      else if ((i) == 2) { R[k][5] = vmov(R[k][11]); R[k][6] = vmov(R[k][12]); R[k][7] = vmov(R[k][13]); } \
      else { R[k][10] = vmov(R[k][16]); R[k][11] = vmov(R[k][17]); } } while (0)
  BWDECL;
  int wbv = WVOFF + wlane, wbc = wbk;
  wbk = WB1 + wlane;
  C_ADDR();
  if (!SAFE) X_PRE();
  SB;

#pragma unroll 1
  for (int h = 0; h < 4; ++h) {
    OPQ(aKa); OPQ(aKs); OPQ(aV); OPQ(aVs); OPQ(baseC);
    if (NU == 3) { OPQ(wbv); OPQ(wbk); OPQ(wbc); }
    unsigned zs = 0;
    asm volatile("" : "+s"(zs));
    STAMP(8 + 4 * h);
    u32x4 P[NG];
    u32x4 vf[2][3];
    if (NU == 3) {
      unsigned ve[1][6];
      PH;
      projN<1, 6, 0>(smem, wbv, xe, ve, LAM { if (t >= 2 && t < 10 && !(t & 1)) NORM_PART(NU - 1, (t - 2) / 2); });
#pragma unroll
      for (int db = 0; db < 3; ++db) { u32x2 pk = {ve[0][2 * db], ve[0][2 * db + 1]}; *(u32x2*)(smem + vae + 32 * db) = pk; }
      SB;
    }
    if constexpr (SAFE) {
      if (NU == 4) { NORM_PART(NU - 1, 0); NORM_PART(NU - 1, 1); NORM_PART(NU - 1, 2); NORM_PART(NU - 1, 3); }
      sfor<0, NU>(SLAM(kc_) {
        constexpr int k = decltype(kc_)::v;
        float pm = 0.f;
        sfor<0, 4>(SLAM(tc_) {
          constexpr int t = decltype(tc_)::v, j = g_idx<NU>(k, t);
          LD_D(k, 2 * t); LD_D(k, 2 * t + 1);
          LD_K(k + t, 0); LD_K(k + t, 1); LD_K(k + t, 2); LD_K(k + t, 3);
#pragma unroll
          for (int i = 0; i < 4; ++i) S[j][i & 1] = mfma(kf[(k + t) & 1][i], (i >> 1) ? Qb[k] : Qa[k], S[j][i & 1]);
#pragma unroll
          for (int i = 0; i < 2; ++i) {
            const float m2 = max2(max2(S[j][i][0], S[j][i][1]), max2(S[j][i][2], S[j][i][3]));
            pm = (t == 0 && i == 0) ? m2 : max2(pm, m2);
          }
          SB;
        });
        pm = xmax32(xmax16(pm));
        sfor<0, 4>(SLAM(tc_) {
          constexpr int j = g_idx<NU>(k, decltype(tc_)::v);
#pragma unroll
          for (int i = 0; i < 4; ++i)
            P[j][i] = pack2(__builtin_amdgcn_exp2f(S[j][i >> 1][2 * (i & 1)] - pm), __builtin_amdgcn_exp2f(S[j][i >> 1][2 * (i & 1) + 1] - pm));
          OPQ(P[j]);
        });
        SB;
      });
      if (h == 0) rawb();
      ldsb();
    } else {
      float ev[2][2];
      constexpr int EXN = NU == 4 ? EXN4 : EXN3;
#define E_PACK(z) do { if ((z) >= 0) { unsigned pk_ = pack2(ev[(z) & 1][0], ev[(z) & 1][1]); OPQ(pk_); P[(z) / 4][(z) % 4] = pk_; } } while (0)
#define E_HALF(z, hf) do { const int i_ = (z) % 4; \
      ev[(z) & 1][hf] = __builtin_amdgcn_exp2f(S[(z) / 4][i_ >> 1][2 * (i_ & 1) + (hf)]); OPQ(ev[(z) & 1][hf]); } while (0)
      sfor<0, NG>(SLAM(jc_) {
        constexpr int j = decltype(jc_)::v, s = g_step<NU>(j), k = g_unit<NU>(j), t = s - k;
        constexpr bool first = j == g_first<NU>(s);
        if (j == XBJ && h == 0) rawb();
        if (j == NG - 1) {
          LDSB_X(63);
          STAMP(9 + 4 * h);
          LD_V(0, 0); LD_V(0, 1); LD_V(0, 2);
          LD_V(1, 0); LD_V(1, 1); LD_V(1, 2);
          SB;
        }
        sfor<0, 4>(SLAM(ic_) {
          constexpr int i = decltype(ic_)::v, r = 2 * t + (i & 1);
          constexpr bool ex = j >= 1 && j - 1 < EXN;
          constexpr int z = 4 * (j - 1) + i;
          if (TVX((i + 3) % 4) == 0 || (j == 0 && i == 0)) PH;
          S[j][i & 1] = mfma(kf[s & 1][i], (i >> 1) ? Qb[k] : Qa[k], (i < 2 && !C_OWN(k, r)) ? T[r] : S[j][i & 1]);
          SB;
          if (TVX(i) != 3) PL;
          if (first && s >= 1 && s + 1 < NS_) LD_K(s + 1, i);
          if (j == 0) { if (i == 0) LD_T(4); if (i == 1) LD_T(5); if (i == 2) LD_D(2, 0); }
          if (j == 2) { if (i == 0) LD_T(6); if (i == 1) LD_D(0, 7); if (i == 2 && NU == 4) LD_D(NU - 1, 0); }
          if (j == 7 && i == 0) LD_D(1, 7);
          if (NU == 4 && j == 11 && i == 0) LD_D(2, 7);
          if (j == NG - 3 && i == 3) { LD_D(NU - 1, 7); if (NU == 3) LD_D(NU - 1, 6); }
          SB;
          if (ex) E_HALF(z, 0);
          if (NU == 4 && j == 0) NORM_A(NU - 1, i);
          SB;
          if (TVX(i) == 2) PH;
          if (ex) E_HALF(z, 1);
          SB;
          if (TVX(i) == 1) PH;
          if (ex) E_PACK(z - 1);
          if (NU == 4 && j == 0) NORM_B(NU - 1, i);
          SB;
        });
      });
      STAMP(10 + 4 * h);
      if (NU == 3 && h < 3) {
        unsigned ke[1][6];
        PH;
        projN<1, 6, 0>(smem, wbk, xe, ke, LAM {});
        u32x4 pa = {ke[0][0], ke[0][1], ke[0][2], ke[0][3]};
        u32x2 pb = {ke[0][4], ke[0][5]};
        *(u32x4*)(smem + kae) = pa;
        *(u32x2*)(smem + (kae ^ 64)) = pb;
        const int wt = wbv; wbv = wbc; wbc = wbk; wbk = wt;
        SB;
      }
      sfor<0, NG>(SLAM(jc_) {
        constexpr int j = decltype(jc_)::v, s = g_step<NU>(j), k = g_unit<NU>(j), t = s - k;
        constexpr bool first = j == g_first<NU>(s);
        constexpr int rk = NU == 4 ? (j == 8 ? 3 : (j >= 4 && j < 7 ? j - 4 : -1)) : (j >= 4 && j < 7 ? j - 4 : -1);
        constexpr int nk = j >= 1 && g_step<NU>(j >= 1 ? j - 1 : 0) - g_unit<NU>(j >= 1 ? j - 1 : 0) == 3 ? g_unit<NU>(j >= 1 ? j - 1 : 0) : -1;
        if (j == NG - 1) {
          if (h == 3) BWOUT();
          LDSB_Y(63);
          STAMP(11 + 4 * h);
          hoff += T_HSTR;
          C_ADDR();
          X_PRE();
          SB;
        }
        sfor<0, 4>(SLAM(ic_) {
          constexpr int i = decltype(ic_)::v;
          constexpr bool ex = EXN + j < NG;
          constexpr int z = 4 * (EXN + j) + i;
          if (TVY((i + 3) % 4) == 0 || (j == 0 && i == 0)) PH;
          o[k][i] = mfma(i ? vf[s & 1][i ? i - 1 : 0] : ones, P[j], t == 0 ? zero4 : o[k][i]);
          SB;
          if (TVY(i) != 3) PL;
          if (first && s >= 1 && s + 1 < NS_ && i < 3) LD_V(s + 1, i);
          SB;
          if (ex) E_HALF(z, 0);
          if (EXN + j == NG && i == 0) E_PACK(4 * NG - 1);
          if (rk >= 0) ROT_A(rk >= 0 ? rk : 0, i);
          if (nk >= 0) NORM_A(nk >= 0 ? nk : 0, i);
          SB;
          if (TVY(i) == 2) PH;
          if (ex) E_HALF(z, 1);
          SB;
          if (TVY(i) == 1) PH;
          if (ex) E_PACK(z - 1);
          if (rk >= 0) ROT_B(rk >= 0 ? rk : 0, i);
          if (nk >= 0) NORM_B(nk >= 0 ? nk : 0, i);
          SB;
        });
      });
    }
    if constexpr (SAFE) {
      if (NU == 3 && h < 3) {
        unsigned ke[1][6];
        projN<1, 6, 0>(smem, wbk, xe, ke, LAM {});
        u32x4 pa = {ke[0][0], ke[0][1], ke[0][2], ke[0][3]};
        u32x2 pb = {ke[0][4], ke[0][5]};
        *(u32x4*)(smem + kae) = pa;
        *(u32x2*)(smem + (kae ^ 64)) = pb;
        const int wt = wbv; wbv = wbc; wbc = wbk; wbk = wt;
        SB;
      }
      sfor<0, NU>(SLAM(kc_) {
        constexpr int k = decltype(kc_)::v;
#pragma unroll
        for (int i = 0; i < 4; ++i) { ROT_A(k, i); ROT_B(k, i); }
        sfor<0, 4>(SLAM(tc_) {
          constexpr int t = decltype(tc_)::v, j = g_idx<NU>(k, t);
          LD_V(k + t, 0); LD_V(k + t, 1); LD_V(k + t, 2);
#pragma unroll
          for (int i = 0; i < 4; ++i) o[k][i] = mfma(i ? vf[(k + t) & 1][i ? i - 1 : 0] : ones, P[j], t == 0 ? zero4 : o[k][i]);
          SB;
        });
        if (k < NU - 1) { NORM_PART(k, 0); NORM_PART(k, 1); NORM_PART(k, 2); NORM_PART(k, 3); }
        SB;
      });
      ldsb();
      hoff += T_HSTR;
      C_ADDR();
    }
  }
#pragma unroll
  for (int i = 0; i < 4; ++i) NORM_PART(NU - 1, i);
  SB;
  PH;
  STAMP(24);

  bool redo = false;
  {
    u32x4 fl;
    int l2 = l;
    OPQ(l2);
    const int u = l2 & 15, g = l2 >> 4, q4 = u >> 2, pp = u & 3;
    const int rowl = 4 * g + q4;
    int wob[2][4];
#pragma unroll
    for (int b = 0; b < 4; ++b) {
      wob[1][b] = WOHI + rowl * 384 + 32 * (b ^ ((rowl >> 1) & 3)) + 8 * pp;
      wob[0][b] = wob[1][b] + (WOLO - WOHI);
      OPQ(wob[0][b]);
    }
    const int uq = min(u, 13);
    const int qa = uq >= 7 ? 1 : 0;
    const int qb = uq - 7 * qa;
    unsigned ooff[NU];
    u32x4 ob[NU][6];
#pragma unroll
    for (int k = 0; k < NU; ++k) {
      const int tok = (2 * (G.A + 2 * (4 * ib + k) + qa) + G.pa) * IMG + 2 * (G.Bc + 7 * j_ + qb) + G.pb;
      ooff[k] = (unsigned)tok * (CH * 2) + ((g & 1) ? (32 + 8 * (g - 1)) : 8 * g);
      ob[k][0] = Qa[k];
      ob[k][1] = (u32x4){Qb[k][0], Qb[k][1], R[k][0], R[k][1]};
#pragma unroll
      for (int sg = 2; sg < 6; ++sg) ob[k][sg] = (u32x4){R[k][4 * sg - 6], R[k][4 * sg - 5], R[k][4 * sg - 4], R[k][4 * sg - 3]};
    }
    char* obase = (char*)out;
    constexpr int D = 4;
    u32x4 wa[D];
#define OFRAG(t) tr_frag(smem, (32 * ((((t) % 12) / 2) % 3)) * 384 + wob[(((t) % 12) / 2) / 3][(2 * ((t) / 12) + ((t) & 1)) & 3] + 128 * ((2 * ((t) / 12) + ((t) & 1)) >> 2), 16 * 384)
#pragma unroll
    for (int t = 0; t < D - 1; ++t) wa[t] = OFRAG(t);
    f32x4 acc[2][2][NU];
#pragma unroll
    for (int nbp = 0; nbp <= 6; ++nbp) {
#pragma unroll
      for (int t1 = 0; t1 < (nbp < 6 ? 12 : 1); ++t1) {
        const int t = 12 * nbp + t1, sg = t1 / 2, tt = t1 & 1;
        if (t + D - 1 < 72) wa[(t + D - 1) % D] = OFRAG(t + D - 1);
        if (t < 72) {
#pragma unroll
          for (int k = 0; k < NU; ++k) acc[nbp & 1][tt][k] = mfma(wa[t % D], ob[k][sg], sg == 0 ? zero4 : acc[nbp & 1][tt][k]);
        }
#pragma unroll
        for (int k = 0; k < NU; ++k) {
          if (nbp >= 1 && (t == 72 || t1 == 2 + 2 * k)) {
            const int pb_ = (nbp - 1) & 1;
            unsigned x0 = pack2(acc[pb_][0][k][0], acc[pb_][0][k][1]), x1 = pack2(acc[pb_][0][k][2], acc[pb_][0][k][3]);
            unsigned y0 = pack2(acc[pb_][1][k][0], acc[pb_][1][k][1]), y1 = pack2(acc[pb_][1][k][2], acc[pb_][1][k][3]);
            auto r0 = __builtin_amdgcn_permlane16_swap(x0, y0, false, false);
            auto r1 = __builtin_amdgcn_permlane16_swap(x1, y1, false, false);
            u32x4 st = {r0[0], r1[0], r0[1], r1[1]};
            *(u32x4*)(obase + ooff[k] + 64 * (nbp - 1)) = st;
            if (!SAFE && nbp == 1) lmx = max(lmx, (__float_as_uint(acc[0][0][k][0]) & 0x7FFFFFFFu) >= 0x7F800000u ? 0x7F800000u : 0u);
          }
        }
        if (!SAFE && nbp == 1 && t1 == 10) {
          const bool bad = lmn < L_LO || lmx > L_HI;
          *(unsigned*)(smem + FLAGOFF + 4 * a) = __builtin_amdgcn_ballot_w64(bad) != 0 ? 1u : 0u;
        }
        if (!SAFE && nbp == 2 && t1 == 4) { SB; rawb(); }
        if (!SAFE && nbp == 2 && t1 == 5) { fl = *(const u32x4*)(smem + FLAGOFF); }
        if (!SAFE && nbp == 3 && t1 == 0) { redo = __builtin_amdgcn_readfirstlane(fl[0] | fl[1] | fl[2] | fl[3]) != 0; }
        SB;
      }
    }
  }
  STAMP(25);
  return redo;
}

__global__ __launch_bounds__(512, 2) void dinat_fused(const uint16_t* __restrict__ x, const uint16_t* __restrict__ wqkv,
                                                      const uint16_t* __restrict__ wo, const uint16_t* __restrict__ rpb,
                                                      uint16_t* __restrict__ out PPARM) {
  __shared__ __attribute__((aligned(128))) char smem[LDS_BYTES];
  const unsigned lds_base = (unsigned)(size_t)(LDS_AS char*)smem;
  const int tid = threadIdx.x;
  const int w = __builtin_amdgcn_readfirstlane(tid >> 6);
  const int l = tid & 63;
  const int bidx = blockIdx.x;
  const int xcd = bidx & 7, idx = bidx >> 3;
  const int sub = 2 * (xcd & 1) + (idx & 1);
  const int ta = 2 * (xcd >> 1) + (idx >> 4), tb = (idx >> 1) & 7;
  Geo G;
  G.pa = sub >> 1; G.pb = sub & 1;
  G.A = 14 * ta; G.Bc = 14 * tb;
  G.R0 = min(max(G.A - 3, -1), 93); G.C0 = min(max(G.Bc - 3, 0), 92);
  SOPQ(G.R0); SOPQ(G.C0);
  G.or_ = G.A - G.R0; G.oc_ = G.Bc - G.C0;
  const char* xb = (const char*)x + ((long long)((2 * G.R0 + G.pa) * IMG + 2 * G.C0 + G.pb)) * (CH * 2);
  bool redo;
  STAMP(0);
  if (w >= 4) redo = proj_wave<false>(smem, lds_base, xb, wqkv, wo, rpb, G, w - 4, l PARG);
  else if (w < 2) redo = attn_wave<4, false>(smem, lds_base, xb, wqkv, out, G, w, l PARG);
  else redo = attn_wave<3, false>(smem, lds_base, xb, wqkv, out, G, w, l PARG);
  if (__builtin_expect(redo, 0)) {
    asm volatile("s_waitcnt vmcnt(0) lgkmcnt(0)\n\ts_barrier" ::: "memory");
    __builtin_amdgcn_s_setprio(0);
    int l2 = l;
    OPQ(l2);
    if (w >= 4) proj_wave<true>(smem, lds_base, xb, wqkv, wo, rpb, G, w - 4, l2 PARG);
    else if (w < 2) attn_wave<4, true>(smem, lds_base, xb, wqkv, out, G, w, l2 PARG);
    else attn_wave<3, true>(smem, lds_base, xb, wqkv, out, G, w, l2 PARG);
  }
}

extern "C" void kernel_launch(void* const* d_in, const int* in_sizes, int n_in,
                              void* d_out, int out_size, void* d_ws, size_t ws_size,
                              hipStream_t stream) {
  const uint16_t* x = (const uint16_t*)d_in[0];
  const uint16_t* wqkv = (const uint16_t*)d_in[1];
  const uint16_t* wo = (const uint16_t*)d_in[2];
  const uint16_t* rpb = (const uint16_t*)d_in[3];
  dinat_fused<<<256, 512, 0, stream>>>(x, wqkv, wo, rpb, (uint16_t*)d_out
  );
}
```

```cpp
#define DK 2
#define DCYC 0
#define DMUL 6
#include <hip/hip_runtime.h>
#include <hip/hip_bf16.h>
#include <stdint.h>

typedef __attribute__((ext_vector_type(8))) __bf16 bf16x8_t;
typedef __attribute__((ext_vector_type(4))) short s16x4_t;
typedef __attribute__((ext_vector_type(4))) float f32x4;
typedef __attribute__((ext_vector_type(4))) unsigned u32x4;
typedef __attribute__((ext_vector_type(2))) unsigned u32x2;
typedef __attribute__((ext_vector_type(2))) __bf16 bf16x2_t;

#define LDS_AS __attribute__((address_space(3)))
#define DI __device__ __forceinline__
#define SB __builtin_amdgcn_sched_barrier(0)
#define OPQ(v) asm volatile("" : "+v"(v))
#define SOPQ(v) do { v = __builtin_amdgcn_readfirstlane(v); asm volatile("" : "+s"(v)); } while (0)
#define LAM [&](int t) __attribute__((always_inline))
template <int V> struct IC { static constexpr int v = V; };
template <int I, int N, class F> __device__ __forceinline__ void sfor(F&& f) {
  if constexpr (I < N) { f(IC<I>{}); sfor<I + 1, N>(f); }
}
#define SLAM(x) [&](auto x) __attribute__((always_inline))

#define IMG 224
#define CH 192
#define ROWB 172032
#define WVOFF 0
#define KOFF 18432
#define VOFF 69632
#define TOFF 108032
#define T_HSTR 3584
#define NEGOFF (TOFF + 4 * T_HSTR)
#define DUMMY 122880
#define WKOFF 145408
#define WB1 126976
#define PBAR 4
#define WOLO 126976
#define WOHI KOFF
#define LDS_BYTES 163840
#define SLE 30720
#define SLO 0
#define WQ 69632
#define RAWB 61440
#define PFD 67584
#define XPL 10240

#define C1 0.20823509f
#define ETYPE(i) 1
#define APRIO 2
#define PPRIO 1
#define PL4 13
#define EPRIO 2
#define QPRIO 1
#define L4S 28
#define L4S3 44
#define TOG 0
#define EXN4 12
#define EXN3 9
#define TVX(i) 2
#define TVY(i) 0
#define PH do { __builtin_amdgcn_s_setprio(3); SB; } while (0)
#define PL do { __builtin_amdgcn_s_setprio(TOG); SB; } while (0)
#define LOG2E 1.4426950408889634f
#define FLAGOFF 125952
#define L_LO 0x2B800000u
#define L_HI 0x53800000u
#define NEGBF 0xF180u

DI f32x4 mfma(u32x4 a, u32x4 b, f32x4 c) {
  return __builtin_amdgcn_mfma_f32_16x16x32_bf16(__builtin_bit_cast(bf16x8_t, a), __builtin_bit_cast(bf16x8_t, b), c, 0, 0, 0);
}
DI u32x2 tr_read(const char* smem, int off) {
  s16x4_t t = __builtin_amdgcn_ds_read_tr16_b64_v4i16((LDS_AS s16x4_t*)(smem + off));
  return __builtin_bit_cast(u32x2, t);
}
DI u32x4 tr_frag(const char* smem, int off, int stride2) {
  u32x2 a = tr_read(smem, off), b = tr_read(smem, off + stride2);
  u32x4 r = {a[0], a[1], b[0], b[1]};
  return r;
}
DI void tr_lo(u32x4& r, const char* smem, int off) { u32x2 a = tr_read(smem, off); r[0] = a[0]; r[1] = a[1]; }
DI void tr_hi(u32x4& r, const char* smem, int off) { u32x2 a = tr_read(smem, off); r[2] = a[0]; r[3] = a[1]; }
DI unsigned pack2(float a, float b) {
  bf16x2_t v = {(__bf16)a, (__bf16)b};
  return __builtin_bit_cast(unsigned, v);
}
DI int clampi(int v, int lo, int hi) { return min(max(v, lo), hi); }
DI float max2(float a, float b) { return __builtin_elementwise_maximum(a, b); }
DI float max3(float a, float b, float c) { return max2(max2(a, b), c); }
DI float xmax16(float v) {
  unsigned b = __float_as_uint(v);
  auto r = __builtin_amdgcn_permlane16_swap(b, b, false, false);
  return max2(__uint_as_float(r[0]), __uint_as_float(r[1]));
}
DI float xmax32(float v) {
  unsigned b = __float_as_uint(v);
  auto r = __builtin_amdgcn_permlane32_swap(b, b, false, false);
  return max2(__uint_as_float(r[0]), __uint_as_float(r[1]));
}
#define vmov(s) ((s) | zs)

template <int NT>
DI void glds16s(const void* sbase_, unsigned voff, unsigned lds_dst) {
  unsigned long long sv = (unsigned long long)sbase_;
  unsigned slo = __builtin_amdgcn_readfirstlane((unsigned)sv), shi = __builtin_amdgcn_readfirstlane((unsigned)(sv >> 32));
  unsigned long long sbase = ((unsigned long long)shi << 32) | slo;
  lds_dst = __builtin_amdgcn_readfirstlane(lds_dst);
  if (NT)
    asm volatile("s_mov_b32 m0, %2\n\ts_nop 0\n\tglobal_load_lds_dwordx4 %0, %1 nt" :: "v"(voff), "s"(sbase), "s"(lds_dst) : "memory", "m0");
  else
    asm volatile("s_mov_b32 m0, %2\n\ts_nop 0\n\tglobal_load_lds_dwordx4 %0, %1" :: "v"(voff), "s"(sbase), "s"(lds_dst) : "memory", "m0");
}
DI void glds4s(const void* sbase_, unsigned voff, unsigned lds_dst) {
  unsigned long long sv = (unsigned long long)sbase_;
  unsigned slo = __builtin_amdgcn_readfirstlane((unsigned)sv), shi = __builtin_amdgcn_readfirstlane((unsigned)(sv >> 32));
  unsigned long long sbase = ((unsigned long long)shi << 32) | slo;
  lds_dst = __builtin_amdgcn_readfirstlane(lds_dst);
  asm volatile("s_mov_b32 m0, %2\n\ts_nop 0\n\tglobal_load_lds_dword %0, %1" :: "v"(voff), "s"(sbase), "s"(lds_dst) : "memory", "m0");
}
template <int N> DI void wait_lgkm() { __builtin_amdgcn_s_waitcnt(0xC07F | (N << 8)); }
template <int N> DI void wait_vm() { asm volatile("s_waitcnt vmcnt(%0)" :: "n"(N) : "memory"); }
DI void rawb() { asm volatile("s_barrier" ::: "memory"); }
DI void ldsb() { asm volatile("s_waitcnt lgkmcnt(0)\n\ts_barrier" ::: "memory"); }

DI void mk_lo3(int w4, int l, unsigned (&lo3)[3]) {
#pragma unroll
  for (int j = 0; j < 3; ++j) {
    int r3 = (w4 + j) % 3;
    int idx = 64 * r3 + l;
    int t = idx / 6, c = idx - 6 * t;
    int r = (t & ~12) | ((t & 4) << 1) | ((t & 8) >> 1);
    lo3[j] = r * 1152 + c * 16;
  }
}
DI void dma_w(const char* wsrc, unsigned lds_dst, int P, unsigned lo) {
  const int img = P / 18, q = P - 18 * img;
  glds16s<0>(wsrc + img * 96 + (q / 3) * 36864, lo, lds_dst + P * 1024);
}
DI void mk_lq3(int w4, int l, unsigned (&lq3)[3]) {
#pragma unroll
  for (int j = 0; j < 3; ++j) {
    const int P = w4 + 4 * j, m = (P * 43) >> 7;
    const int idx = 64 * (P - 3 * m) + l;
    const int r = (idx * 171) >> 12, pc = idx - 24 * r;
    lq3[j] = (16 * (m & 1) + 2 * (m & 2) + 8 * (r >> 2) + (r & 3)) * 1152 + (pc ^ (r & 6)) * 16;
    OPQ(lq3[j]);
  }
}
DI int p18(int w4, int i) { int P = w4 + 4 * i; return P >= 18 ? P - 3 : P; }

#define FSTAMP(k)
#define STAMP(k) do { if (__builtin_constant_p(k) && (k) == DK) { SB; dg = DCYC ? __builtin_amdgcn_s_memtime() : __builtin_amdgcn_s_memrealtime(); SB; } } while (0)
#define PPARM , unsigned long long& dg
#define PARG , dg

#define TSDECL
#define TS(k)
#define TSOUT(n)
#define BWDECL
#define LDSB_X(vm) do { if (vm < 63) wait_vm<vm>(); ldsb(); } while (0)
#define LDSB_Y(vm) do { if (vm < 63) wait_vm<vm>(); ldsb(); } while (0)
#define PBW_X asm volatile("s_waitcnt vmcnt(0) lgkmcnt(%0)\n\ts_barrier" :: "n"(2 * (PBAR - 2)) : "memory")
#define PBW_Y PBW_X
#define BWOUT()
struct Geo { int A, Bc, R0, C0, or_, oc_, pa, pb; };

DI unsigned chunk_rows(int c, int or_) {
  const unsigned r3 = 0x0706u + 0x0101u * or_ + (or_ == 1 ? 0x0F200000u : 0x01000000u);
  const unsigned r4 = or_ == 1 ? 0x13121110u : (or_ == 3 ? 0x13121102u : 0x33040302u);
  unsigned r = c < 3 ? 0x09080100u + 0x01010101u * (or_ + 2 * c) : (c == 3 ? r3 : r4);
  SOPQ(r);
  return r;
}
DI void x_lane(unsigned (&xc)[3], unsigned (&xs)[3], int p, int l) {
#pragma unroll
  for (int i = 0; i < 3; ++i) {
    const int t = 8 * (i < 2 ? p + 4 * i : 8 + (p & 1)) + (l >> 3);
    const int rr = i < 2 ? (t * 205) >> 12 : 3;
    xc[i] = (t - 20 * rr) * 768 + 16 * ((l & 7) ^ ((t + 3 * rr) & 7));
    xs[i] = 8 * rr;
  }
}
DI void x_read(const char* smem, int slot, int t, int g, u32x4 (&xf)[6]) {
  int ae = slot + t * 128 + 16 * (g ^ ((t + 3 * ((t * 205) >> 12)) & 7));
  int ao = ae ^ 64;
#pragma unroll
  for (int s = 0; s < 6; ++s) xf[s] = *(const u32x4*)(smem + ((s & 1) ? ao : ae) + XPL * (s >> 1));
}

template <int NT, int D, int K0, int NX, class F>
DI void projN(const char* smem, int wb, const u32x4 (&xf)[NX][6], unsigned (&out)[NX][6], F hook) {
  u32x4 wf[D];
  f32x4 ac[3][NT];
#define PC(t) (NT == 1 ? (t) % 3 : (t) / 6)
#define PS(t) (NT == 1 ? (t) / 3 : (t) % 6)
#define PADDR(t) (wb + PS(t) * 3072 + PC(t) * 32)
#define PPACK(c, k) do { out[K0 + (k)][2 * (c)] = pack2(ac[c][k][0], ac[c][k][1]); out[K0 + (k)][2 * (c) + 1] = pack2(ac[c][k][2], ac[c][k][3]); \
                         OPQ(out[K0 + (k)][2 * (c)]); OPQ(out[K0 + (k)][2 * (c) + 1]); } while (0)
#pragma unroll
  for (int t = 0; t < D - 1; ++t) wf[t] = tr_frag(smem, PADDR(t), 768);
  SB;
  hook(-1);
  SB;
#pragma unroll
  for (int t = 0; t < 18; ++t) {
    const int c = PC(t), s = PS(t);
#pragma unroll
    for (int k = 0; k < NT; ++k) {
      ac[c][k] = mfma(wf[t % D], xf[K0 + k][s], s == 0 ? (f32x4){0.f, 0.f, 0.f, 0.f} : ac[c][k]);
      SB;
      if (t + D - 1 < 18) {
        if (k == 0) tr_lo(wf[(t + D - 1) % D], smem, PADDR(t + D - 1));
        if (k == (NT > 1 ? 1 : 0)) tr_hi(wf[(t + D - 1) % D], smem, PADDR(t + D - 1) + 768);
      }
      if (NT > 1 && k == NT - 1 && c >= 1 && s >= 1 && s <= NT) PPACK(c >= 1 ? c - 1 : 0, s >= 1 && s <= NT ? s - 1 : 0);
      SB;
    }
    hook(t);
    SB;
  }
#pragma unroll
  for (int c = (NT == 1 ? 0 : 2); c < 3; ++c)
#pragma unroll
    for (int k = 0; k < NT; ++k) PPACK(c, k);
  SB;
  hook(18);
  SB;
#undef PC
#undef PS
#undef PADDR
#undef PPACK
}

DI void aug_k(int tc, int g, unsigned& a0, unsigned& a1) {
  const unsigned m = ((~((0x7Fu << (tc & 31)) >> 6) & 0x3FFFu) | ((tc & 32) << 9)) >> (4 * g);
  a0 = ((m & 1) | ((m & 2) << 15)) * 0x3F80u;
  a1 = (((m >> 2) & 1) | ((m & 8) << 13)) * 0x3F80u;
}
DI void kv_addr(unsigned e, int c, int g, int& ka, int& va) {
  const int n = 20 * (int)(e & 31) + c;
  ka = KOFF + 128 * n + 16 * (g ^ (n & 7));
  va = VOFF + 96 * n + 8 * (g ^ ((c >> 2) & 3));
}

DI void x_addr(unsigned (&vo)[4], const unsigned (&xc)[3], const unsigned (&xs)[3], int p, unsigned rows) {
#pragma unroll
  for (int i = 0; i < 3; ++i) vo[i] = xc[i] + __builtin_amdgcn_ubfe(rows, xs[i], 8) * ROWB;
  unsigned f = vo[2], m = p < 2 ? ~0u : 0u;
  SOPQ(f); SOPQ(m);
  vo[3] = (vo[2] & m) | (f & ~m);
  OPQ(vo[0]); OPQ(vo[1]); OPQ(vo[2]); OPQ(vo[3]);
}
DI void dma_x(const char* xs, const unsigned (&vo)[4], unsigned lds_base, unsigned slot, int p, int i, bool late = false) {
  const int v = i < 6 ? i % 3 : (p < 2 ? i - 6 : 2);
  const int j = i < 3 ? p : (i < 6 ? p + 4 : 8 + (p & 1));
  glds16s<0>(xs + 128 * v, vo[i < 7 ? i / 3 : 3], lds_base + (i == 7 && p >= 2 ? PFD + 1024 : slot + v * XPL + j * 1024));
}

DI void mk_tq(const char* smem, int raw, int tid, unsigned (&tq)[4][2]) {
  const int r = min(tid, 223);
  const int seg = (r >= 55) + (r >= 110) + (r >= 167);
  const int m = seg ^ (seg >> 1);
  const int o = r - (int)((0x71A93802u >> (8 * m)) & 0xFF);
  unsigned tv[4][4];
  bool ok[4];
#pragma unroll
  for (int i = 0; i < 4; ++i) {
    const int lin = 4 * o + m + i;
    const int row = lin >> 4, dc = lin & 15;
    ok[i] = row >= 0 && row <= 12 && dc <= 12;
    const int src = raw + 2 * (clampi(row, 0, 12) * 13 + min(dc, 12));
#pragma unroll
    for (int hh = 0; hh < 4; ++hh) tv[hh][i] = *(const uint16_t*)(smem + src + hh * 338);
  }
  SB;
#pragma unroll
  for (int i = 0; i < 4; ++i)
#pragma unroll
    for (int hh = 0; hh < 4; ++hh) OPQ(tv[hh][i]);
  const unsigned m0 = (ok[0] ? 0xFFFFu : 0u) | (ok[1] ? 0xFFFF0000u : 0u), m1 = (ok[2] ? 0xFFFFu : 0u) | (ok[3] ? 0xFFFF0000u : 0u);
#pragma unroll
  for (int hh = 0; hh < 4; ++hh) {
    tq[hh][0] = (tv[hh][0] | (tv[hh][1] << 16)) & m0; tq[hh][1] = (tv[hh][2] | (tv[hh][3] << 16)) & m1;
    OPQ(tq[hh][0]); OPQ(tq[hh][1]);
  }
}

template <int KV, int LAST, class B>
DI void proj_phase(char* smem, int wb, int wbn, const u32x4 (&xf)[6][6], const int (&ka)[6], const int (&va)[6],
                   u32x4 (&wf)[3], f32x4 (&ac)[2][6], const char* dsrc, unsigned dstr, unsigned ddst, int p, const unsigned (&lo3)[3], B bar) {
  constexpr int D = 3;
  unsigned hold[6][2];
#define PADR(t) (((t) < 18 ? wb : wbn) + ((t) % 6) * 3072 + (((t) % 18) / 6) * 32)
#pragma unroll
  for (int t = 0; t < 18 + LAST; ++t) {
    const int c = t / 6, s = t % 6, ab = (c + KV) & 1;
#pragma unroll
    for (int k = 0; k < 6; ++k) {
      if (t < 18) ac[ab][k] = mfma(wf[t % D], xf[k][s], s == 0 ? (f32x4){0.f, 0.f, 0.f, 0.f} : ac[ab][k]);
      if (t + 2 < (LAST ? 18 : 20) && (k == 0 || k == 2)) {
        SB;
        if (k == 0) tr_lo(wf[(t + 2) % D], smem, PADR(t + 2));
        else tr_hi(wf[(t + 2) % D], smem, PADR(t + 2) + 768);
        SB;
      }
      if (t == 18 || (s < 3 && (k == 1 || k == 4))) {
        const int kb = t == 18 ? k : 2 * s + (k == 4 ? 1 : 0);
        const int pc = (c + 2) % 3, kv = c == 0 ? 1 - KV : KV;
        unsigned p0 = pack2(ac[ab ^ 1][kb][0], ac[ab ^ 1][kb][1]), p1 = pack2(ac[ab ^ 1][kb][2], ac[ab ^ 1][kb][3]);
        if (kv == 0) {
          u32x2 pk = {p0, p1};
          *(u32x2*)(smem + va[kb] + 32 * pc) = pk;
        } else if (pc == 0) {
          hold[kb][0] = p0; hold[kb][1] = p1;
        } else if (pc == 1) {
          u32x4 pk = {hold[kb][0], hold[kb][1], p0, p1};
          *(u32x4*)(smem + ka[kb]) = pk;
        } else {
          u32x2 pk = {p0, p1};
          *(u32x2*)(smem + (ka[kb] ^ 64)) = pk;
        }
        SB;
      }
    }
    SB;
    if (t == PBAR || t == 15) bar(t);
    if (t > PBAR && t <= PBAR + 10 && ((t - PBAR) & 1)) {
      const int i = (t - PBAR) / 2, P = p18(p, i);
      glds16s<0>(dsrc + (P / 3) * dstr, lo3[i % 3], ddst + P * 1024);
    }
    SB;
  }
#undef PADR
}

template <bool SAFE>
DI bool proj_wave(char* smem, unsigned lds_base, const char* xb, const uint16_t* __restrict__ wqkv,
                  const uint16_t* __restrict__ wo, const uint16_t* __restrict__ rpb, const Geo& G, int p, int l PPARM) {
  const int u = l & 15, g = l >> 4;
  TSDECL;
  const char* xq = xb + (size_t)G.or_ * ROWB;
  unsigned vo[3][4], lo3[3];
  int ka[6], va[6];
  unsigned ag[6][2];
  auto kv_setup = [&]() __attribute__((always_inline)) {
    const int t = 16 * p + u, rr = (t * 205) >> 12, c = t - 20 * rr;
    unsigned a0, a1;
    aug_k(c, g, a0, a1);
    const unsigned fl = g == 3 ? 0x3F80u : 0u;
#pragma unroll
    for (int k = 0; k < 6; ++k) {
      if (k == 4) {
        const unsigned e = chunk_rows(p, G.or_) >> 24;
        kv_addr(e, 4 + u, g, ka[k], va[k]);
        aug_k(4 + u + (e & 32), g, ag[k][0], ag[k][1]);
      } else {
        const unsigned e = __builtin_amdgcn_ubfe(chunk_rows(k < 4 ? k : 4, G.or_), 8 * rr, 8);
        kv_addr(e, c, g, ka[k], va[k]);
        ag[k][0] = a0;
        ag[k][1] = k < 3 ? a1 : (a1 | ((e & 32) ? fl : 0u));
      }
      OPQ(ka[k]);
      if (k >= 3) OPQ(ag[k][1]);
    }
    OPQ(ag[4][0]);
  };
  __builtin_amdgcn_s_setprio(3);
  unsigned xc[3], xs[3];
  x_lane(xc, xs, p, l);
  x_addr(vo[0], xc, xs, p, 0x09080100u);
  glds16s<0>(rpb, 16 * l, lds_base + RAWB + 1536 * p);
  glds4s(rpb, 1024 + 4 * l, lds_base + RAWB + 1536 * p + 1024);
  glds4s(rpb, min(1280 + 4 * l, 1348), lds_base + RAWB + 1536 * p + 1280);
  TS(0);
#pragma unroll
  for (int i = 0; i < 8; ++i) dma_x(xq + 2 * ROWB, vo[0], lds_base, SLO, p, i);
  {
    const int line = ((blockIdx.x >> 3) & 31) * 72 + 18 * p + min(l, 17);
    const bool lw = line < 1728;
    const unsigned long long dw = (unsigned long long)wo - (unsigned long long)wqkv - 1728ull * 128ull;
    const char* pa = (const char*)wqkv + line * 128 + (lw ? 0ull : dw);
    asm volatile("s_mov_b32 m0, %1\n\ts_nop 0\n\tglobal_load_lds_dword %0, off"
                 :: "v"(pa), "s"(__builtin_amdgcn_readfirstlane(lds_base + PFD + 256 * p)) : "memory", "m0");
  }
  TS(1);
  __builtin_amdgcn_s_setprio(0);
  mk_lo3(p, l, lo3);
#pragma unroll
  for (int j = 0; j < 3; ++j) OPQ(lo3[j]);
  const int wlane = (16 * (g >> 1) + 4 * (g & 1) + (u >> 2)) * 96 + 8 * (u & 3);
  int wbk = WKOFF + wlane;
  OPQ(wbk);
  x_addr(vo[1], xc, xs, p, 0x0706u + 0x0101u * G.or_ + (G.or_ == 1 ? 0x0F010000u : 0x01000000u));
  u32x4 xf[6][6];
  unsigned k0[6][6];
  unsigned tq[4][2];
  const int tp = 16 * p + u, te = 64 + u, tid = 64 * p + l;
  x_addr(vo[2], xc, xs, p, G.or_ == 1 ? 0x13121110u : (G.or_ == 3 ? 0x13121102u : 0x12040302u));
  kv_setup();
  SB;
  wait_vm<9>();
  mk_tq(smem, RAWB + 1536 * p, tid, tq);
  SB;
  TS(2);
#pragma unroll
  for (int i = 0; i < 5; ++i) dma_w((const char*)wqkv + 384, lds_base + WKOFF, p18(p, i), lo3[i % 3]);
  TS(3);
  __builtin_amdgcn_s_setprio(3);
  wait_vm<5>(); rawb();
  TS(4);
  x_read(smem, SLE, tp, g, xf[0]);
  x_read(smem, SLO, tp, g, xf[1]);
  x_read(smem, (p & 1) ? SLO : SLE, te, g, xf[4]);
  wait_vm<0>(); ldsb();
  TS(5);
  __builtin_amdgcn_s_setprio(QPRIO);
  projN<2, 6, 0>(smem, wbk, xf, k0, LAM {
    if (t < 0) {
#pragma unroll
      for (int i = 0; i < 4; ++i) dma_x(xq + 4 * ROWB, vo[0], lds_base, SLE, p, i, true);
    }
    else if (t < 4) dma_x(xq + 4 * ROWB, vo[0], lds_base, SLE, p, t + 4, true);
    else if (t < 12) dma_x(xb, vo[1], lds_base, SLO, p, t - 4, true);
    if (t == 11) TS(6);
  });
  TS(7);
  TS(8);
  __builtin_amdgcn_s_setprio(3);
  wait_vm<0>();
  rawb();
  TS(9);
  x_read(smem, SLE, tp, g, xf[2]);
  x_read(smem, SLO, tp, g, xf[3]);
  if (p >= 2) x_read(smem, (p & 1) ? SLO : SLE, te, g, xf[4]);
  auto k_write = [&](int k) __attribute__((always_inline)) {
    u32x4 pa = {k0[k][0], k0[k][1], k0[k][2], k0[k][3]};
    u32x4 pb = {k0[k][4], k0[k][5], ag[k][0], ag[k][1]};
    *(u32x4*)(smem + ka[k]) = pa;
    *(u32x4*)(smem + (ka[k] ^ 64)) = pb;
  };
  projN<2, 6, 2>(smem, wbk, xf, k0, LAM {
    if (t < 0) {
      ldsb();
      TS(10);
#pragma unroll
      for (int i = 0; i < 4; ++i) dma_x(xb, vo[2], lds_base, SLE, p, i, true);
      __builtin_amdgcn_s_setprio(QPRIO);
    }
    else if (t < 4) dma_x(xb, vo[2], lds_base, SLE, p, t + 4, true);
    else if (t < 9) dma_w((const char*)wqkv + 768, lds_base + WVOFF, p18(p, t - 4), lo3[(t - 4) % 3]);
  });
  projN<2, 6, 4>(smem, wbk, xf, k0, LAM {
    if (t < 0) {
      TS(11);
      __builtin_amdgcn_s_setprio(EPRIO);
      wait_vm<5>(); rawb();
      STAMP(5);
      x_read(smem, SLE, tp, g, xf[5]);
      ldsb();
    }
    else if (t % 4 == 0 && t < 16) k_write(t / 4);
    else if (t == 18) { k_write(4); k_write(5); }
  });
  f32x4 tf[4];
#pragma unroll
  for (int hh = 0; hh < 4; ++hh) {
    tf[hh] = (f32x4){__uint_as_float(tq[hh][0] << 16) * LOG2E, __uint_as_float(tq[hh][0] & 0xFFFF0000u) * LOG2E,
                     __uint_as_float(tq[hh][1] << 16) * LOG2E, __uint_as_float(tq[hh][1] & 0xFFFF0000u) * LOG2E};
    OPQ(tf[hh]);
  }
  {
    const int t = 16 * p + u, c = t - 20 * ((t * 205) >> 12);
#pragma unroll
    for (int k = 0; k < 6; ++k) {
      va[k] = VOFF + 96 * ((ka[k] - KOFF) >> 7) + 8 * (g ^ (((k == 4 ? 4 + u : c) >> 2) & 3));
      OPQ(va[k]);
    }
  }
  SB;
  STAMP(4);
  wait_vm<0>(); rawb();
  STAMP(6);
  if (tid < 224) {
#pragma unroll
    for (int hh = 0; hh < 4; ++hh) *(f32x4*)(smem + TOFF + hh * T_HSTR + 16 * tid) = tf[hh];
  } else if (tid < 226) {
    const float nb = __uint_as_float(NEGBF << 16);
    *(f32x4*)(smem + NEGOFF + 448 * (tid - 224)) = (f32x4){nb, nb, nb, nb};
  }
  SB;
  STAMP(7);
  ldsb();
#pragma unroll
  for (int i = 0; i < 5; ++i) dma_w((const char*)wqkv + 384 + 96, lds_base + WB1, p18(p, i), lo3[i % 3]);
  TSOUT(12);
  __builtin_amdgcn_s_setprio(PPRIO);
  u32x4 wf[3];
  f32x4 ac[2][6];
#pragma unroll
  for (int k = 0; k < 6; ++k)
    ac[1][k] = (f32x4){__uint_as_float(k0[k][4] << 16), __uint_as_float(k0[k][4] & 0xFFFF0000u),
                       __uint_as_float(k0[k][5] << 16), __uint_as_float(k0[k][5] & 0xFFFF0000u)};
  int wa = WVOFF + wlane, wb = WB1 + wlane, wc = WKOFF + wlane;
  unsigned da = lds_base + WKOFF, db = lds_base + WVOFF, dc = lds_base + WB1;
  wf[0] = tr_frag(smem, wa, 768);
  wf[1] = tr_frag(smem, wa + 3072, 768);
  BWDECL;
  int h = 0;
  auto barY = [&](int t) __attribute__((always_inline)) {
    if (t == PBAR) { if (h != 0) PBW_Y; }
    else if (h == 0) { wait_vm<5>(); rawb(); }
  };
  auto barX = [&](int t) __attribute__((always_inline)) { if (t == PBAR) PBW_X; };
  unsigned dstr = 36864;
  const char* dsk = (const char*)wqkv + 384 + 192;
#pragma unroll 1
  for (; h < 3; ++h) {
    OPQ(wa); OPQ(wb); OPQ(wc);
    STAMP(8 + 4 * h);
    proj_phase<0, 0>(smem, wa, wb, xf, ka, va, wf, ac, (const char*)wqkv + 768 + 96 * (h + 1), 36864, da, p, lo3, barY);
    STAMP(10 + 4 * h);
    if (h == 2) {
#pragma unroll
      for (int j = 0; j < 3; ++j) {
        int idx = 64 * ((p + j) % 3) + l;
        int row = idx / 24, pc = idx - 24 * row;
        lo3[j] = row * 384 + ((pc >> 1) ^ ((row >> 1) & 3)) * 32 + (pc & 1) * 16;
      }
      dstr = 3072;
      dsk = (const char*)wo;
    }
    proj_phase<1, 0>(smem, wb, wc, xf, ka, va, wf, ac, dsk, dstr, db, p, lo3, barX);
    dsk += 96;
    const int wt = wa; wa = wc; wc = wb; wb = wt;
    const unsigned dt = da; da = dc; dc = db; db = dt;
  }
  STAMP(20);
  proj_phase<0, 1>(smem, wa, wa, xf, ka, va, wf, ac, (const char*)wo + 18432, 3072, lds_base + WKOFF, p, lo3, barY);
  STAMP(21);
  LDSB_X(63);
  BWOUT();
  STAMP(22);
#pragma unroll
  for (int t = 0; t < 9; ++t) { int P = p + 4 * t; glds16s<0>((const char*)wo + 36864 + (P / 3) * 3072, lo3[t % 3], lds_base + WOHI + P * 1024); }
  wait_vm<0>();
  STAMP(23);
  ldsb();
  if (SAFE) return false;
  rawb();
  const u32x4 fl = *(const u32x4*)(smem + FLAGOFF);
  return __builtin_amdgcn_readfirstlane(fl[0] | fl[1] | fl[2] | fl[3]) != 0;
}

template <int NU> constexpr int g_cnt(int s) { return (s < NU - 1 ? s : NU - 1) - (s > 3 ? s - 3 : 0) + 1; }
template <int NU> constexpr int g_first(int s) { int c = 0; for (int i = 0; i < s; ++i) c += g_cnt<NU>(i); return c; }
template <int NU> constexpr int g_step(int j) { int s = 0; while (g_first<NU>(s + 1) <= j) ++s; return s; }
template <int NU> constexpr int g_unit(int j) { int s = g_step<NU>(j); return (s > 3 ? s - 3 : 0) + j - g_first<NU>(s); }
template <int NU> constexpr int g_idx(int k, int t) { return g_first<NU>(k + t) + k - (k + t > 3 ? k + t - 3 : 0); }

template <int NU> constexpr bool q_dbl(int t) { return NU == 4 || t < 72; }
template <int NU> constexpr int q_c(int t) { return q_dbl<NU>(t) ? (t % 72) / 6 : (t >= 156 ? 2 : 2 * ((t % 72) / 12) + (t & 1)); }
template <int NU> constexpr int q_s(int t) { return q_dbl<NU>(t) ? t % 6 : (t >= 156 ? t - 156 : (t % 12) / 2); }
template <int NU> constexpr int q_gi(int t) { return q_dbl<NU>(t) ? t / 6 : t / 12; }
template <int NU> constexpr int q_off(int t) { return NU == 3 && t >= 144 ? q_s<NU>(t) * 3072 + q_c<NU>(t) * 32 : (q_s<NU>(t) % 3) * 12288 + (q_c<NU>(t) >> 2) * 128; }
#define XBJ (NG - 4)
#define QD 6
#define L23S 50
#define XSP 2

template <int NU, bool SAFE>
DI bool attn_wave(char* smem, unsigned lds_base, const char* xb, const uint16_t* __restrict__ wqkv,
                  uint16_t* __restrict__ out, const Geo& G, int a, int l PPARM) {
  const int u = l & 15, g = l >> 4;
  const int j_ = a & 1, ib = a >> 1;
  TSDECL;
  __builtin_amdgcn_s_setprio(3);
  unsigned lq3[3];
  {
    unsigned vo[4], xc[3], xs[3];
    x_lane(xc, xs, a, l);
    x_addr(vo, xc, xs, a, 0x09080100u);
#pragma unroll
    for (int i = 0; i < 8; ++i) dma_x(xb + (size_t)G.or_ * ROWB, vo, lds_base, SLE, a, i);
  }
  __builtin_amdgcn_s_setprio(1);
  mk_lq3(a, l, lq3);
#define DMA_Q(i0, i1) do { SB; _Pragma("unroll") for (int i = i0; i < i1; ++i) \
    glds16s<0>((const char*)wqkv + (i / 3) * 36864, lq3[i % 3], lds_base + WQ + a * 1024 + i * 4096); SB; } while (0)
  DMA_Q(0, 6);
  const int uq = min(u, 13);
  const int qa = uq >= 7 ? 1 : 0;
  const int qb = uq - 7 * qa;
  const int tu = 20 * (2 * ib + qa) + G.oc_ + 7 * j_ + qb;
  int xa = tu * 128 + 16 * (g ^ ((tu + 3 * (2 * ib + qa)) & 7)), xo = xa ^ 64;
  OPQ(xa); OPQ(xo);
#define X_FRAG(slot, s) (*(const u32x4*)(smem + (((s) & 1) ? xo : xa) + (slot) + XPL * ((s) >> 1)))
  const int b0 = G.Bc + 7 * j_;
  const int kb0 = clampi(b0 - 3, 0, 99), kc0 = kb0 - G.C0;
  const int wlane = (16 * (g >> 1) + 4 * (g & 1) + (u >> 2)) * 96 + 8 * (u & 3);
  int wbk = WKOFF + wlane;
  OPQ(wbk);
  DMA_Q(6, 8);
  int wq[2][4];
#pragma unroll
  for (int b = 0; b < 4; ++b) {
    const int rowl = 4 * g + (u >> 2);
    wq[0][b] = WQ + rowl * 384 + 32 * (b ^ ((rowl >> 1) & 3)) + 8 * (u & 3);
    wq[1][b] = wq[0][b] + 36864;
    OPQ(wq[0][b]); OPQ(wq[1][b]);
  }
  DMA_Q(8, 10);
  unsigned augB0, augB1;
  int baseK, baseV, baseC;
  {
    const int b = b0 + qb;
    const int sbrel = clampi(b - 3, 0, 105) - G.C0;
    unsigned v[4];
#pragma unroll
    for (int jj = 0; jj < 4; ++jj) v[jj] = (sbrel == 4 * g + jj || 4 * g + jj == 14) ? NEGBF : 0u;
    augB0 = v[0] | (v[1] << 16);
    augB1 = v[2] | (v[3] << 16);
    OPQ(augB0); OPQ(augB1);
    DMA_Q(10, 12);
    const int colK = kc0 + u <= 19 ? kc0 + u : kc0 + u - 16;
    baseK = KOFF + 128 * colK + 16 * (g ^ (colK & 7));
    const int cV = kc0 + min(4 * g + (u >> 2), 12);
    baseV = VOFF + 96 * cV + 8 * ((u & 3) ^ ((cV >> 2) & 3));
    const int beta_c = kb0 - b + 6;
    const int m = beta_c & 3;
    const int om = (0x71A93802u >> (8 * m)) & 0xFF;
    baseC = TOFF + 16 * om + 4 * (beta_c - m) + 16 * g - 64 * qa + 192;
    OPQ(baseC);
  }
  const bool edge = G.or_ == (NU == 4 ? 1 : 5);
  const int pb0 = ((G.or_ - 3) >> 1) + (NU == 4 ? 0 : 4), pbs = edge ? (NU == 4 ? 3 : 1) : pb0;
  int aKa = baseK + 5120 * pb0, aKs = baseK + 5120 * pbs, aV = baseV + 3840 * pb0, aVs = baseV + 3840 * pbs;
  OPQ(aKa); OPQ(aKs); OPQ(aV); OPQ(aVs);
  int negv = NEGOFF;
  OPQ(negv);
  DMA_Q(12, 14);
  int kae = 0, vae = 0;
  unsigned ae0 = 0, ae1 = 0;
  if (NU == 3) {
    const unsigned e = chunk_rows(4, G.or_) >> 24;
    kv_addr(e, 4 + u, g, kae, vae);
    aug_k(4 + u + (e & 32), g, ae0, ae1);
    OPQ(kae); OPQ(vae); OPQ(ae0); OPQ(ae1);
  }
  DMA_Q(14, 18);
  __builtin_amdgcn_s_setprio(2);
  STAMP(1);
  u32x4 xu[NU][6];
  TS(6);
  wait_vm<18>(); rawb();
  TS(7);
#pragma unroll
  for (int s = 0; s < 6; ++s) { xu[0][s] = X_FRAG(SLE, s); xu[1][s] = X_FRAG(SLO, s); }
  unsigned Q[NU][24];
  u32x4 xe[1][6];
  unsigned ke[6];
  float oi[12];
  SB;
  TS(1);
  wait_vm<0>(); ldsb();
  STAMP(2);
  TS(2);
  {
    constexpr int D = QD, NS = NU == 4 ? 144 : 162;
    u32x4 wf[D];
    f32x4 ac[2][2];
    sfor<0, D - 1>(SLAM(tc_) { constexpr int t = decltype(tc_)::v; wf[t] = tr_frag(smem, wq[q_s<NU>(t) / 3][q_c<NU>(t) & 3] + q_off<NU>(t), 6144); });
    SB;
    sfor<0, NS + 2>(SLAM(tc_) {
      constexpr int t = decltype(tc_)::v, wi = t / 72, t1 = t % 72;
      constexpr bool dbl = q_dbl<NU>(t), kb = NU == 3 && wi == 2;
      constexpr int s = t < NS ? q_s<NU>(t) : 0, gi = q_gi<NU>(t), gs = dbl ? t % 6 : t % 12;
      constexpr int tn = t + D - 1;
      const f32x4 z = {0.f, 0.f, 0.f, 0.f};
      if constexpr (t < NS) {
        constexpr int ch = dbl || (kb && t1 >= 12) ? 0 : (t & 1);
        constexpr bool kn = NU == 3 && tn >= 144;
        const int an = (kn ? wbk : wq[(q_s<NU>(tn) / 3) & 1][q_c<NU>(tn) & 3]) + q_off<NU>(tn < NS ? tn : 0);
        if constexpr (kb) ac[gi & 1][ch] = mfma(wf[t % D], xe[0][s], s == 0 ? z : ac[gi & 1][ch]);
        else ac[gi & 1][ch] = mfma(wf[t % D], xu[2 * wi][s], s == 0 ? z : ac[gi & 1][ch]);
        SB;
        if (tn < NS) tr_lo(wf[tn % D], smem, an);
        SB;
        if constexpr (dbl) ac[gi & 1][1] = mfma(wf[t % D], xu[2 * wi + 1][s], s == 0 ? z : ac[gi & 1][1]);
        SB;
        if (tn < NS) tr_hi(wf[tn % D], smem, an + (kn ? 768 : 6144));
      }
      constexpr int tp_ = t < NS ? t - gs - 1 : NS - 1;
      sfor<0, 4>(SLAM(ec_) {
        constexpr int e = decltype(ec_)::v, k = e >> 1;
        if constexpr (t < NS ? (tp_ >= 0 && gs == 1 + e) : (t - NS == e / 2)) {
          constexpr int pg = q_gi<NU>(tp_), pw = tp_ / 72;
          const float a0 = ac[pg & 1][k][2 * (e & 1)], a1 = ac[pg & 1][k][2 * (e & 1) + 1];
          if constexpr (NU == 3 && pw == 2) {
            constexpr int o = (tp_ >= 156 ? 4 : 2 * k) + (e & 1);
            if (!(tp_ >= 156 && k == 1)) { ke[o] = pack2(a0, a1); OPQ(ke[o]); }
          } else {
            constexpr int uu = 2 * pw + (q_dbl<NU>(tp_) ? k : 0), cb = q_dbl<NU>(tp_) ? q_c<NU>(tp_) : q_c<NU>(tp_) - 1 + k;
            constexpr int o = 2 * cb + (e & 1);
            if constexpr (uu == NU - 1 && o >= 18) {
              oi[2 * (o - 18)] = a0; oi[2 * (o - 18) + 1] = a1;
              OPQ(oi[2 * (o - 18)]); OPQ(oi[2 * (o - 18) + 1]);
            } else {
              Q[uu][o] = pack2(a0 * C1, a1 * C1);
              OPQ(Q[uu][o]);
            }
          }
        }
      });
      SB;
      if (t == 0) TS(0);
      if (t == 30) TS(3);
      if (t == L23S) TS(4);
      if (t == L23S + 6 * XSP - 1 + D) TS(5);
      if (t == 72 + (NU == 4 ? L4S : L4S3)) TS(8);
      if (t == 72 + (NU == 4 ? L4S + 3 : L4S3 + D)) TS(9);
      if (t == 72 + (NU == 4 ? L4S + 4 : L4S3 + D + 1)) TS(10);
      if (t == NS + 1) TS(11);
      if (t == L23S) {
        rawb();
        STAMP(3);
      }
      if (t >= L23S && t < L23S + 6 * XSP) {
        constexpr int i = (t - L23S) / XSP;
        if ((t - L23S) % XSP == 0) xu[2][i] = X_FRAG(SLE, i);
        if (NU == 4 && (t - L23S) % XSP == XSP - 1) xu[NU - 1][i] = X_FRAG(SLO, i);
        SB;
      }
      if (t == L23S + 6 * XSP - 1 + D) { rawb(); SB; }
      if (t == 72 + (NU == 4 ? L4S : L4S3)) {
        rawb();
        STAMP(5);
        if (NU == 3) x_read(smem, SLE, 64 + u, g, xe[0]);
        SB;
      }
      if (t == 72 + (NU == 4 ? L4S + 3 : L4S3 + D)) { rawb(); SB; }
    });
  }
  if (NU == 3) {
    u32x4 pa = {ke[0], ke[1], ke[2], ke[3]};
    u32x4 pb = {ke[4], ke[5], ae0, ae1};
    *(u32x4*)(smem + kae) = pa;
    *(u32x4*)(smem + (kae ^ 64)) = pb;
  }
  SB;
  rawb();
  STAMP(6);
  u32x4 Qa[NU], Qb[NU];
  unsigned R[NU][18];
#pragma unroll
  for (int k = 0; k < NU; ++k) {
    Qa[k] = (u32x4){Q[k][0], Q[k][1], Q[k][2], Q[k][3]};
    Qb[k] = (u32x4){Q[k][4], Q[k][5], augB0, augB1};
#pragma unroll
    for (int e = 0; e < 18; ++e) R[k][e] = (k == NU - 1 && e >= 12) ? 0u : Q[k][6 + e];
  }
  STAMP(7);
  TSOUT(12);
  ldsb();
  __builtin_amdgcn_s_setprio(APRIO);

  constexpr int NG = 4 * NU, NS_ = NU + 3;
  const u32x4 ones = {0x3F803F80u, 0x3F803F80u, 0x3F803F80u, 0x3F803F80u};
  const f32x4 zero4 = {0.f, 0.f, 0.f, 0.f};
  u32x4 kf[2][4];
  f32x4 T[8];
  f32x4 S[NG][2];
  f32x4 o[NU][4];
  float inv, nt0, nt1;
  unsigned lmn = 0x3F800000u, lmx = 0x3F800000u;
#pragma unroll
  for (int db = 0; db < 3; ++db) o[NU - 1][db + 1] = (f32x4){oi[4 * db], oi[4 * db + 1], oi[4 * db + 2], oi[4 * db + 3]};
  o[NU - 1][0] = (f32x4){1.0f / C1, 0.f, 0.f, 0.f};
  int aCm, aC0, aC7, aCx, aCy, aCz;
  int hoff = 0;
#define C_ADDR() do { aCm = baseC + hoff; aC0 = qa ? negv : aCm; aC7 = qa ? aCm : negv; \
    aCy = aCm + (edge ? (NU == 4 ? 512 : -512) : 0); aCx = edge ? aCy : (NU == 4 ? aC0 : aC7); aCz = edge ? aCm : (NU == 4 ? aC7 : aC0); } while (0)
#define C_OWN(k, r) ((r) == 0 || (r) == 7 || (NU == 4 && (k) == 0 && (r) == 1) || (NU == 3 && (k) == 2 && (r) == 6))
#define C_SEL(k, r) (NU == 4 ? ((r) == 0 ? ((k) == 0 ? aCx : aC0) : ((r) == 7 ? ((k) < 2 ? aCz : aC7) : ((k) == 0 && (r) == 1 ? aCy : aCm))) \
                             : ((r) == 0 ? ((k) == 0 ? aC0 : aCz) : ((r) == 7 ? ((k) == 2 ? aCx : aC7) : ((k) == 2 && (r) == 6 ? aCy : aCm))))
#define LD_D(k, r) S[g_idx<NU>(k, (r) / 2)][(r) & 1] = *(const f32x4*)(smem + C_SEL(k, r) + 64 * (r))
#define LD_T(r) T[r] = *(const f32x4*)(smem + aCm + 64 * (r))
#define LD_K(s, i) do { const int ab_ = (s) == (NU == 4 ? 0 : 5) ? aKs : aKa; \
    kf[(s) & 1][i] = *(const u32x4*)(smem + ((((i) >> 1) ^ ((i) & 1)) ? ab_ ^ 64 : ab_) + 5120 * (s) + 2560 * ((i) & 1)); } while (0)
#define LD_V(s, db) vf[(s) & 1][db] = tr_frag(smem, ((s) == (NU == 4 ? 0 : 5) ? aVs : aV) + 3840 * (s) + 32 * (db), 1920)
#define X_PRE() do { LD_D(0, 0); if (NU == 4) LD_D(0, 1); else LD_T(1); LD_K(0, 0); LD_K(0, 1); LD_K(0, 2); LD_K(0, 3); \
    LD_T(2); LD_T(3); LD_K(1, 0); LD_K(1, 1); LD_K(1, 2); LD_K(1, 3); LD_D(1, 0); if (NU == 4) LD_T(1); } while (0)
#define NORM_A(k, i) do { \
      if ((i) == 0) { const float l_ = o[k][0][0]; inv = __builtin_amdgcn_rcpf(l_); OPQ(inv); } \
      else { const f32x4 ov_ = o[k][i]; nt0 = ov_[0] * inv; nt1 = ov_[1] * inv; OPQ(nt0); OPQ(nt1); } } while (0)
#define NORM_B(k, i) do { \
      if ((i) == 0) { const float l_ = o[k][0][0]; \
        if (!SAFE) { lmn = min(lmn, __float_as_uint(l_)); lmx = max(lmx, __float_as_uint(l_)); OPQ(lmn); OPQ(lmx); } } \
      else { const f32x4 ov_ = o[k][i]; \
        R[k][10 + 2 * (i)] = pack2(nt0, nt1); R[k][11 + 2 * (i)] = pack2(ov_[2] * inv, ov_[3] * inv); \
        OPQ(R[k][10 + 2 * (i)]); OPQ(R[k][11 + 2 * (i)]); } } while (0)
#define NORM_PART(k, i) do { NORM_A(k, i); NORM_B(k, i); } while (0)
#define ROT_A(k, i) do { \
      if ((i) == 0) { Qa[k][0] = vmov(R[k][0]); Qa[k][1] = vmov(R[k][1]); } \
      else if ((i) == 1) { Qb[k][0] = vmov(R[k][4]); Qb[k][1] = vmov(R[k][5]); } \
      else if ((i) == 2) { R[k][3] = vmov(R[k][9]); R[k][4] = vmov(R[k][10]); } \
      else { R[k][8] = vmov(R[k][14]); R[k][9] = vmov(R[k][15]); } } while (0)
#define ROT_B(k, i) do { \
      if ((i) == 0) { Qa[k][2] = vmov(R[k][2]); Qa[k][3] = vmov(R[k][3]); } \
      else if ((i) == 1) { R[k][0] = vmov(R[k][6]); R[k][1] = vmov(R[k][7]); R[k][2] = vmov(R[k][8]); } \
      else if ((i) == 2) { R[k][5] = vmov(R[k][11]); R[k][6] = vmov(R[k][12]); R[k][7] = vmov(R[k][13]); } \
      else { R[k][10] = vmov(R[k][16]); R[k][11] = vmov(R[k][17]); } } while (0)
  BWDECL;
  int wbv = WVOFF + wlane, wbc = wbk;
  wbk = WB1 + wlane;
  C_ADDR();
  if (!SAFE) X_PRE();
  SB;

#pragma unroll 1
  for (int h = 0; h < 4; ++h) {
    OPQ(aKa); OPQ(aKs); OPQ(aV); OPQ(aVs); OPQ(baseC);
    if (NU == 3) { OPQ(wbv); OPQ(wbk); OPQ(wbc); }
    unsigned zs = 0;
    asm volatile("" : "+s"(zs));
    STAMP(8 + 4 * h);
    u32x4 P[NG];
    u32x4 vf[2][3];
    if (NU == 3) {
      unsigned ve[1][6];
      PH;
      projN<1, 6, 0>(smem, wbv, xe, ve, LAM { if (t >= 2 && t < 10 && !(t & 1)) NORM_PART(NU - 1, (t - 2) / 2); });
#pragma unroll
      for (int db = 0; db < 3; ++db) { u32x2 pk = {ve[0][2 * db], ve[0][2 * db + 1]}; *(u32x2*)(smem + vae + 32 * db) = pk; }
      SB;
    }
    if constexpr (SAFE) {
      if (NU == 4) { NORM_PART(NU - 1, 0); NORM_PART(NU - 1, 1); NORM_PART(NU - 1, 2); NORM_PART(NU - 1, 3); }
      sfor<0, NU>(SLAM(kc_) {
        constexpr int k = decltype(kc_)::v;
        float pm = 0.f;
        sfor<0, 4>(SLAM(tc_) {
          constexpr int t = decltype(tc_)::v, j = g_idx<NU>(k, t);
          LD_D(k, 2 * t); LD_D(k, 2 * t + 1);
          LD_K(k + t, 0); LD_K(k + t, 1); LD_K(k + t, 2); LD_K(k + t, 3);
#pragma unroll
          for (int i = 0; i < 4; ++i) S[j][i & 1] = mfma(kf[(k + t) & 1][i], (i >> 1) ? Qb[k] : Qa[k], S[j][i & 1]);
#pragma unroll
          for (int i = 0; i < 2; ++i) {
            const float m2 = max2(max2(S[j][i][0], S[j][i][1]), max2(S[j][i][2], S[j][i][3]));
            pm = (t == 0 && i == 0) ? m2 : max2(pm, m2);
          }
          SB;
        });
        pm = xmax32(xmax16(pm));
        sfor<0, 4>(SLAM(tc_) {
          constexpr int j = g_idx<NU>(k, decltype(tc_)::v);
#pragma unroll
          for (int i = 0; i < 4; ++i)
            P[j][i] = pack2(__builtin_amdgcn_exp2f(S[j][i >> 1][2 * (i & 1)] - pm), __builtin_amdgcn_exp2f(S[j][i >> 1][2 * (i & 1) + 1] - pm));
          OPQ(P[j]);
        });
        SB;
      });
      if (h == 0) rawb();
      ldsb();
    } else {
      float ev[2][2];
      constexpr int EXN = NU == 4 ? EXN4 : EXN3;
#define E_PACK(z) do { if ((z) >= 0) { unsigned pk_ = pack2(ev[(z) & 1][0], ev[(z) & 1][1]); OPQ(pk_); P[(z) / 4][(z) % 4] = pk_; } } while (0)
#define E_HALF(z, hf) do { const int i_ = (z) % 4; \
      ev[(z) & 1][hf] = __builtin_amdgcn_exp2f(S[(z) / 4][i_ >> 1][2 * (i_ & 1) + (hf)]); OPQ(ev[(z) & 1][hf]); } while (0)
      sfor<0, NG>(SLAM(jc_) {
        constexpr int j = decltype(jc_)::v, s = g_step<NU>(j), k = g_unit<NU>(j), t = s - k;
        constexpr bool first = j == g_first<NU>(s);
        if (j == XBJ && h == 0) rawb();
        if (j == NG - 1) {
          LDSB_X(63);
          STAMP(9 + 4 * h);
          LD_V(0, 0); LD_V(0, 1); LD_V(0, 2);
          LD_V(1, 0); LD_V(1, 1); LD_V(1, 2);
          SB;
        }
        sfor<0, 4>(SLAM(ic_) {
          constexpr int i = decltype(ic_)::v, r = 2 * t + (i & 1);
          constexpr bool ex = j >= 1 && j - 1 < EXN;
          constexpr int z = 4 * (j - 1) + i;
          if (TVX((i + 3) % 4) == 0 || (j == 0 && i == 0)) PH;
          S[j][i & 1] = mfma(kf[s & 1][i], (i >> 1) ? Qb[k] : Qa[k], (i < 2 && !C_OWN(k, r)) ? T[r] : S[j][i & 1]);
          SB;
          if (TVX(i) != 3) PL;
          if (first && s >= 1 && s + 1 < NS_) LD_K(s + 1, i);
          if (j == 0) { if (i == 0) LD_T(4); if (i == 1) LD_T(5); if (i == 2) LD_D(2, 0); }
          if (j == 2) { if (i == 0) LD_T(6); if (i == 1) LD_D(0, 7); if (i == 2 && NU == 4) LD_D(NU - 1, 0); }
          if (j == 7 && i == 0) LD_D(1, 7);
          if (NU == 4 && j == 11 && i == 0) LD_D(2, 7);
          if (j == NG - 3 && i == 3) { LD_D(NU - 1, 7); if (NU == 3) LD_D(NU - 1, 6); }
          SB;
          if (ex) E_HALF(z, 0);
          if (NU == 4 && j == 0) NORM_A(NU - 1, i);
          SB;
          if (TVX(i) == 2) PH;
          if (ex) E_HALF(z, 1);
          SB;
          if (TVX(i) == 1) PH;
          if (ex) E_PACK(z - 1);
          if (NU == 4 && j == 0) NORM_B(NU - 1, i);
          SB;
        });
      });
      STAMP(10 + 4 * h);
      if (NU == 3 && h < 3) {
        unsigned ke[1][6];
        PH;
        projN<1, 6, 0>(smem, wbk, xe, ke, LAM {});
        u32x4 pa = {ke[0][0], ke[0][1], ke[0][2], ke[0][3]};
        u32x2 pb = {ke[0][4], ke[0][5]};
        *(u32x4*)(smem + kae) = pa;
        *(u32x2*)(smem + (kae ^ 64)) = pb;
        const int wt = wbv; wbv = wbc; wbc = wbk; wbk = wt;
        SB;
      }
      sfor<0, NG>(SLAM(jc_) {
        constexpr int j = decltype(jc_)::v, s = g_step<NU>(j), k = g_unit<NU>(j), t = s - k;
        constexpr bool first = j == g_first<NU>(s);
        constexpr int rk = NU == 4 ? (j == 8 ? 3 : (j >= 4 && j < 7 ? j - 4 : -1)) : (j >= 4 && j < 7 ? j - 4 : -1);
        constexpr int nk = j >= 1 && g_step<NU>(j >= 1 ? j - 1 : 0) - g_unit<NU>(j >= 1 ? j - 1 : 0) == 3 ? g_unit<NU>(j >= 1 ? j - 1 : 0) : -1;
        if (j == NG - 1) {
          if (h == 3) BWOUT();
          LDSB_Y(63);
          STAMP(11 + 4 * h);
          hoff += T_HSTR;
          C_ADDR();
          X_PRE();
          SB;
        }
        sfor<0, 4>(SLAM(ic_) {
          constexpr int i = decltype(ic_)::v;
          constexpr bool ex = EXN + j < NG;
          constexpr int z = 4 * (EXN + j) + i;
          if (TVY((i + 3) % 4) == 0 || (j == 0 && i == 0)) PH;
          o[k][i] = mfma(i ? vf[s & 1][i ? i - 1 : 0] : ones, P[j], t == 0 ? zero4 : o[k][i]);
          SB;
          if (TVY(i) != 3) PL;
          if (first && s >= 1 && s + 1 < NS_ && i < 3) LD_V(s + 1, i);
          SB;
          if (ex) E_HALF(z, 0);
          if (EXN + j == NG && i == 0) E_PACK(4 * NG - 1);
          if (rk >= 0) ROT_A(rk >= 0 ? rk : 0, i);
          if (nk >= 0) NORM_A(nk >= 0 ? nk : 0, i);
          SB;
          if (TVY(i) == 2) PH;
          if (ex) E_HALF(z, 1);
          SB;
          if (TVY(i) == 1) PH;
          if (ex) E_PACK(z - 1);
          if (rk >= 0) ROT_B(rk >= 0 ? rk : 0, i);
          if (nk >= 0) NORM_B(nk >= 0 ? nk : 0, i);
          SB;
        });
      });
    }
    if constexpr (SAFE) {
      if (NU == 3 && h < 3) {
        unsigned ke[1][6];
        projN<1, 6, 0>(smem, wbk, xe, ke, LAM {});
        u32x4 pa = {ke[0][0], ke[0][1], ke[0][2], ke[0][3]};
        u32x2 pb = {ke[0][4], ke[0][5]};
        *(u32x4*)(smem + kae) = pa;
        *(u32x2*)(smem + (kae ^ 64)) = pb;
        const int wt = wbv; wbv = wbc; wbc = wbk; wbk = wt;
        SB;
      }
      sfor<0, NU>(SLAM(kc_) {
        constexpr int k = decltype(kc_)::v;
#pragma unroll
        for (int i = 0; i < 4; ++i) { ROT_A(k, i); ROT_B(k, i); }
        sfor<0, 4>(SLAM(tc_) {
          constexpr int t = decltype(tc_)::v, j = g_idx<NU>(k, t);
          LD_V(k + t, 0); LD_V(k + t, 1); LD_V(k + t, 2);
#pragma unroll
          for (int i = 0; i < 4; ++i) o[k][i] = mfma(i ? vf[(k + t) & 1][i ? i - 1 : 0] : ones, P[j], t == 0 ? zero4 : o[k][i]);
          SB;
        });
        if (k < NU - 1) { NORM_PART(k, 0); NORM_PART(k, 1); NORM_PART(k, 2); NORM_PART(k, 3); }
        SB;
      });
      ldsb();
      hoff += T_HSTR;
      C_ADDR();
    }
  }
#pragma unroll
  for (int i = 0; i < 4; ++i) NORM_PART(NU - 1, i);
  SB;
  PH;
  STAMP(24);

  bool redo = false;
  {
    u32x4 fl;
    int l2 = l;
    OPQ(l2);
    const int u = l2 & 15, g = l2 >> 4, q4 = u >> 2, pp = u & 3;
    const int rowl = 4 * g + q4;
    int wob[2][4];
#pragma unroll
    for (int b = 0; b < 4; ++b) {
      wob[1][b] = WOHI + rowl * 384 + 32 * (b ^ ((rowl >> 1) & 3)) + 8 * pp;
      wob[0][b] = wob[1][b] + (WOLO - WOHI);
      OPQ(wob[0][b]);
    }
    const int uq = min(u, 13);
    const int qa = uq >= 7 ? 1 : 0;
    const int qb = uq - 7 * qa;
    unsigned ooff[NU];
    u32x4 ob[NU][6];
#pragma unroll
    for (int k = 0; k < NU; ++k) {
      const int tok = (2 * (G.A + 2 * (4 * ib + k) + qa) + G.pa) * IMG + 2 * (G.Bc + 7 * j_ + qb) + G.pb;
      ooff[k] = (unsigned)tok * (CH * 2) + ((g & 1) ? (32 + 8 * (g - 1)) : 8 * g);
      ob[k][0] = Qa[k];
      ob[k][1] = (u32x4){Qb[k][0], Qb[k][1], R[k][0], R[k][1]};
#pragma unroll
      for (int sg = 2; sg < 6; ++sg) ob[k][sg] = (u32x4){R[k][4 * sg - 6], R[k][4 * sg - 5], R[k][4 * sg - 4], R[k][4 * sg - 3]};
    }
    char* obase = (char*)out;
    constexpr int D = 4;
    u32x4 wa[D];
#define OFRAG(t) tr_frag(smem, (32 * ((((t) % 12) / 2) % 3)) * 384 + wob[(((t) % 12) / 2) / 3][(2 * ((t) / 12) + ((t) & 1)) & 3] + 128 * ((2 * ((t) / 12) + ((t) & 1)) >> 2), 16 * 384)
#pragma unroll
    for (int t = 0; t < D - 1; ++t) wa[t] = OFRAG(t);
    f32x4 acc[2][2][NU];
#pragma unroll
    for (int nbp = 0; nbp <= 6; ++nbp) {
#pragma unroll
      for (int t1 = 0; t1 < (nbp < 6 ? 12 : 1); ++t1) {
        const int t = 12 * nbp + t1, sg = t1 / 2, tt = t1 & 1;
        if (t + D - 1 < 72) wa[(t + D - 1) % D] = OFRAG(t + D - 1);
        if (t < 72) {
#pragma unroll
          for (int k = 0; k < NU; ++k) acc[nbp & 1][tt][k] = mfma(wa[t % D], ob[k][sg], sg == 0 ? zero4 : acc[nbp & 1][tt][k]);
        }
#pragma unroll
        for (int k = 0; k < NU; ++k) {
          if (nbp >= 1 && (t == 72 || t1 == 2 + 2 * k)) {
            const int pb_ = (nbp - 1) & 1;
            unsigned x0 = pack2(acc[pb_][0][k][0], acc[pb_][0][k][1]), x1 = pack2(acc[pb_][0][k][2], acc[pb_][0][k][3]);
            unsigned y0 = pack2(acc[pb_][1][k][0], acc[pb_][1][k][1]), y1 = pack2(acc[pb_][1][k][2], acc[pb_][1][k][3]);
            auto r0 = __builtin_amdgcn_permlane16_swap(x0, y0, false, false);
            auto r1 = __builtin_amdgcn_permlane16_swap(x1, y1, false, false);
            u32x4 st = {r0[0], r1[0], r0[1], r1[1]};
            *(u32x4*)(obase + ooff[k] + 64 * (nbp - 1)) = st;
            if (!SAFE && nbp == 1) lmx = max(lmx, (__float_as_uint(acc[0][0][k][0]) & 0x7FFFFFFFu) >= 0x7F800000u ? 0x7F800000u : 0u);
          }
        }
        if (!SAFE && nbp == 1 && t1 == 10) {
          const bool bad = lmn < L_LO || lmx > L_HI;
          *(unsigned*)(smem + FLAGOFF + 4 * a) = __builtin_amdgcn_ballot_w64(bad) != 0 ? 1u : 0u;
        }
        if (!SAFE && nbp == 2 && t1 == 4) { SB; rawb(); }
        if (!SAFE && nbp == 2 && t1 == 5) { fl = *(const u32x4*)(smem + FLAGOFF); }
        if (!SAFE && nbp == 3 && t1 == 0) { redo = __builtin_amdgcn_readfirstlane(fl[0] | fl[1] | fl[2] | fl[3]) != 0; }
        SB;
      }
    }
  }
  STAMP(25);
  return redo;
}

__global__ __launch_bounds__(512, 2) void dinat_fused(const uint16_t* __restrict__ x, const uint16_t* __restrict__ wqkv,
                                                      const uint16_t* __restrict__ wo, const uint16_t* __restrict__ rpb,
                                                      uint16_t* __restrict__ out) {
  unsigned long long dg = 0;
  const unsigned long long dg0 = DCYC ? __builtin_amdgcn_s_memtime() : __builtin_amdgcn_s_memrealtime();
  __shared__ __attribute__((aligned(128))) char smem[LDS_BYTES];
  const unsigned lds_base = (unsigned)(size_t)(LDS_AS char*)smem;
  const int tid = threadIdx.x;
  const int w = __builtin_amdgcn_readfirstlane(tid >> 6);
  const int l = tid & 63;
  const int bidx = blockIdx.x;
  const int xcd = bidx & 7, idx = bidx >> 3;
  const int sub = 2 * (xcd & 1) + (idx & 1);
  const int ta = 2 * (xcd >> 1) + (idx >> 4), tb = (idx >> 1) & 7;
  Geo G;
  G.pa = sub >> 1; G.pb = sub & 1;
  G.A = 14 * ta; G.Bc = 14 * tb;
  G.R0 = min(max(G.A - 3, -1), 93); G.C0 = min(max(G.Bc - 3, 0), 92);
  SOPQ(G.R0); SOPQ(G.C0);
  G.or_ = G.A - G.R0; G.oc_ = G.Bc - G.C0;
  const char* xb = (const char*)x + ((long long)((2 * G.R0 + G.pa) * IMG + 2 * G.C0 + G.pb)) * (CH * 2);
  bool redo;
  STAMP(0);
  if (w >= 4) redo = proj_wave<false>(smem, lds_base, xb, wqkv, wo, rpb, G, w - 4, l PARG);
  else if (w < 2) redo = attn_wave<4, false>(smem, lds_base, xb, wqkv, out, G, w, l PARG);
  else redo = attn_wave<3, false>(smem, lds_base, xb, wqkv, out, G, w, l PARG);
  if (w == 0) {
    unsigned long long d = (dg - dg0) * DMUL / (DCYC ? 20 : 1);
    unsigned long long e = __builtin_amdgcn_s_memrealtime() + d;
    while (__builtin_amdgcn_s_memrealtime() < e) __builtin_amdgcn_s_sleep(4);
  }
  if (__builtin_expect(redo, 0)) {
    asm volatile("s_waitcnt vmcnt(0) lgkmcnt(0)\n\ts_barrier" ::: "memory");
    __builtin_amdgcn_s_setprio(0);
    int l2 = l;
    OPQ(l2);
    if (w >= 4) proj_wave<true>(smem, lds_base, xb, wqkv, wo, rpb, G, w - 4, l2 PARG);
    else if (w < 2) attn_wave<4, true>(smem, lds_base, xb, wqkv, out, G, w, l2 PARG);
    else attn_wave<3, true>(smem, lds_base, xb, wqkv, out, G, w, l2 PARG);
  }
}

extern "C" void kernel_launch(void* const* d_in, const int* in_sizes, int n_in,
                              void* d_out, int out_size, void* d_ws, size_t ws_size,
                              hipStream_t stream) {
  const uint16_t* x = (const uint16_t*)d_in[0];
  const uint16_t* wqkv = (const uint16_t*)d_in[1];
  const uint16_t* wo = (const uint16_t*)d_in[2];
  const uint16_t* rpb = (const uint16_t*)d_in[3];
  dinat_fused<<<256, 512, 0, stream>>>(x, wqkv, wo, rpb, (uint16_t*)d_out
  );
}
```

```cpp
#define POL "sc0 sc1 nt"
#define SPIN 5000
#include <hip/hip_runtime.h>
#include <hip/hip_bf16.h>
#include <stdint.h>

typedef __attribute__((ext_vector_type(8))) __bf16 bf16x8_t;
typedef __attribute__((ext_vector_type(4))) short s16x4_t;
typedef __attribute__((ext_vector_type(4))) float f32x4;
typedef __attribute__((ext_vector_type(4))) unsigned u32x4;
typedef __attribute__((ext_vector_type(2))) unsigned u32x2;
typedef __attribute__((ext_vector_type(2))) __bf16 bf16x2_t;

#define LDS_AS __attribute__((address_space(3)))
#define DI __device__ __forceinline__
#define SB __builtin_amdgcn_sched_barrier(0)
#define OPQ(v) asm volatile("" : "+v"(v))
#define SOPQ(v) do { v = __builtin_amdgcn_readfirstlane(v); asm volatile("" : "+s"(v)); } while (0)
#define LAM [&](int t) __attribute__((always_inline))
template <int V> struct IC { static constexpr int v = V; };
template <int I, int N, class F> __device__ __forceinline__ void sfor(F&& f) {
  if constexpr (I < N) { f(IC<I>{}); sfor<I + 1, N>(f); }
}
#define SLAM(x) [&](auto x) __attribute__((always_inline))

#define IMG 224
#define CH 192
#define ROWB 172032
#define WVOFF 0
#define KOFF 18432
#define VOFF 69632
#define TOFF 108032
#define T_HSTR 3584
#define NEGOFF (TOFF + 4 * T_HSTR)
#define DUMMY 122880
#define WKOFF 145408
#define WB1 126976
#define PBAR 4
#define WOLO 126976
#define WOHI KOFF
#define LDS_BYTES 163840
#define SLE 30720
#define SLO 0
#define WQ 69632
#define RAWB 61440
#define PFD 67584
#define XPL 10240

#define C1 0.20823509f
#define ETYPE(i) 1
#define APRIO 2
#define PPRIO 1
#define PL4 13
#define EPRIO 2
#define QPRIO 1
#define L4S 28
#define L4S3 44
#define TOG 0
#define EXN4 12
#define EXN3 9
#define TVX(i) 2
#define TVY(i) 0
#define PH do { __builtin_amdgcn_s_setprio(3); SB; } while (0)
#define PL do { __builtin_amdgcn_s_setprio(TOG); SB; } while (0)
#define LOG2E 1.4426950408889634f
#define FLAGOFF 125952
#define L_LO 0x2B800000u
#define L_HI 0x53800000u
#define NEGBF 0xF180u

DI f32x4 mfma(u32x4 a, u32x4 b, f32x4 c) {
  return __builtin_amdgcn_mfma_f32_16x16x32_bf16(__builtin_bit_cast(bf16x8_t, a), __builtin_bit_cast(bf16x8_t, b), c, 0, 0, 0);
}
DI u32x2 tr_read(const char* smem, int off) {
  s16x4_t t = __builtin_amdgcn_ds_read_tr16_b64_v4i16((LDS_AS s16x4_t*)(smem + off));
  return __builtin_bit_cast(u32x2, t);
}
DI u32x4 tr_frag(const char* smem, int off, int stride2) {
  u32x2 a = tr_read(smem, off), b = tr_read(smem, off + stride2);
  u32x4 r = {a[0], a[1], b[0], b[1]};
  return r;
}
DI void tr_lo(u32x4& r, const char* smem, int off) { u32x2 a = tr_read(smem, off); r[0] = a[0]; r[1] = a[1]; }
DI void tr_hi(u32x4& r, const char* smem, int off) { u32x2 a = tr_read(smem, off); r[2] = a[0]; r[3] = a[1]; }
DI unsigned pack2(float a, float b) {
  bf16x2_t v = {(__bf16)a, (__bf16)b};
  return __builtin_bit_cast(unsigned, v);
}
DI int clampi(int v, int lo, int hi) { return min(max(v, lo), hi); }
DI float max2(float a, float b) { return __builtin_elementwise_maximum(a, b); }
DI float max3(float a, float b, float c) { return max2(max2(a, b), c); }
DI float xmax16(float v) {
  unsigned b = __float_as_uint(v);
  auto r = __builtin_amdgcn_permlane16_swap(b, b, false, false);
  return max2(__uint_as_float(r[0]), __uint_as_float(r[1]));
}
DI float xmax32(float v) {
  unsigned b = __float_as_uint(v);
  auto r = __builtin_amdgcn_permlane32_swap(b, b, false, false);
  return max2(__uint_as_float(r[0]), __uint_as_float(r[1]));
}
#define vmov(s) ((s) | zs)

template <int NT>
DI void glds16s(const void* sbase_, unsigned voff, unsigned lds_dst) {
  unsigned long long sv = (unsigned long long)sbase_;
  unsigned slo = __builtin_amdgcn_readfirstlane((unsigned)sv), shi = __builtin_amdgcn_readfirstlane((unsigned)(sv >> 32));
  unsigned long long sbase = ((unsigned long long)shi << 32) | slo;
  lds_dst = __builtin_amdgcn_readfirstlane(lds_dst);
  if (NT)
    asm volatile("s_mov_b32 m0, %2\n\ts_nop 0\n\tglobal_load_lds_dwordx4 %0, %1 nt" :: "v"(voff), "s"(sbase), "s"(lds_dst) : "memory", "m0");
  else
    asm volatile("s_mov_b32 m0, %2\n\ts_nop 0\n\tglobal_load_lds_dwordx4 %0, %1" :: "v"(voff), "s"(sbase), "s"(lds_dst) : "memory", "m0");
}
DI void glds4s(const void* sbase_, unsigned voff, unsigned lds_dst) {
  unsigned long long sv = (unsigned long long)sbase_;
  unsigned slo = __builtin_amdgcn_readfirstlane((unsigned)sv), shi = __builtin_amdgcn_readfirstlane((unsigned)(sv >> 32));
  unsigned long long sbase = ((unsigned long long)shi << 32) | slo;
  lds_dst = __builtin_amdgcn_readfirstlane(lds_dst);
  asm volatile("s_mov_b32 m0, %2\n\ts_nop 0\n\tglobal_load_lds_dword %0, %1" :: "v"(voff), "s"(sbase), "s"(lds_dst) : "memory", "m0");
}
template <int N> DI void wait_lgkm() { __builtin_amdgcn_s_waitcnt(0xC07F | (N << 8)); }
template <int N> DI void wait_vm() { asm volatile("s_waitcnt vmcnt(%0)" :: "n"(N) : "memory"); }
DI void rawb() { asm volatile("s_barrier" ::: "memory"); }
DI void ldsb() { asm volatile("s_waitcnt lgkmcnt(0)\n\ts_barrier" ::: "memory"); }

DI void mk_lo3(int w4, int l, unsigned (&lo3)[3]) {
#pragma unroll
  for (int j = 0; j < 3; ++j) {
    int r3 = (w4 + j) % 3;
    int idx = 64 * r3 + l;
    int t = idx / 6, c = idx - 6 * t;
    int r = (t & ~12) | ((t & 4) << 1) | ((t & 8) >> 1);
    lo3[j] = r * 1152 + c * 16;
  }
}
DI void dma_w(const char* wsrc, unsigned lds_dst, int P, unsigned lo) {
  const int img = P / 18, q = P - 18 * img;
  glds16s<0>(wsrc + img * 96 + (q / 3) * 36864, lo, lds_dst + P * 1024);
}
DI void mk_lq3(int w4, int l, unsigned (&lq3)[3]) {
#pragma unroll
  for (int j = 0; j < 3; ++j) {
    const int P = w4 + 4 * j, m = (P * 43) >> 7;
    const int idx = 64 * (P - 3 * m) + l;
    const int r = (idx * 171) >> 12, pc = idx - 24 * r;
    lq3[j] = (16 * (m & 1) + 2 * (m & 2) + 8 * (r >> 2) + (r & 3)) * 1152 + (pc ^ (r & 6)) * 16;
    OPQ(lq3[j]);
  }
}
DI int p18(int w4, int i) { int P = w4 + 4 * i; return P >= 18 ? P - 3 : P; }

#define FSTAMP(k)
#define STAMP(k)
#define PPARM
#define PARG

#define TSDECL
#define TS(k)
#define TSOUT(n)
#define BWDECL
#define LDSB_X(vm) do { if (vm < 63) wait_vm<vm>(); ldsb(); } while (0)
#define LDSB_Y(vm) do { if (vm < 63) wait_vm<vm>(); ldsb(); } while (0)
#define PBW_X asm volatile("s_waitcnt vmcnt(0) lgkmcnt(%0)\n\ts_barrier" :: "n"(2 * (PBAR - 2)) : "memory")
#define PBW_Y PBW_X
#define BWOUT()
struct Geo { int A, Bc, R0, C0, or_, oc_, pa, pb; };

DI unsigned chunk_rows(int c, int or_) {
  const unsigned r3 = 0x0706u + 0x0101u * or_ + (or_ == 1 ? 0x0F200000u : 0x01000000u);
  const unsigned r4 = or_ == 1 ? 0x13121110u : (or_ == 3 ? 0x13121102u : 0x33040302u);
  unsigned r = c < 3 ? 0x09080100u + 0x01010101u * (or_ + 2 * c) : (c == 3 ? r3 : r4);
  SOPQ(r);
  return r;
}
DI void x_lane(unsigned (&xc)[3], unsigned (&xs)[3], int p, int l) {
#pragma unroll
  for (int i = 0; i < 3; ++i) {
    const int t = 8 * (i < 2 ? p + 4 * i : 8 + (p & 1)) + (l >> 3);
    const int rr = i < 2 ? (t * 205) >> 12 : 3;
    xc[i] = (t - 20 * rr) * 768 + 16 * ((l & 7) ^ ((t + 3 * rr) & 7));
    xs[i] = 8 * rr;
  }
}
DI void x_read(const char* smem, int slot, int t, int g, u32x4 (&xf)[6]) {
  int ae = slot + t * 128 + 16 * (g ^ ((t + 3 * ((t * 205) >> 12)) & 7));
  int ao = ae ^ 64;
#pragma unroll
  for (int s = 0; s < 6; ++s) xf[s] = *(const u32x4*)(smem + ((s & 1) ? ao : ae) + XPL * (s >> 1));
}

template <int NT, int D, int K0, int NX, class F>
DI void projN(const char* smem, int wb, const u32x4 (&xf)[NX][6], unsigned (&out)[NX][6], F hook) {
  u32x4 wf[D];
  f32x4 ac[3][NT];
#define PC(t) (NT == 1 ? (t) % 3 : (t) / 6)
#define PS(t) (NT == 1 ? (t) / 3 : (t) % 6)
#define PADDR(t) (wb + PS(t) * 3072 + PC(t) * 32)
#define PPACK(c, k) do { out[K0 + (k)][2 * (c)] = pack2(ac[c][k][0], ac[c][k][1]); out[K0 + (k)][2 * (c) + 1] = pack2(ac[c][k][2], ac[c][k][3]); \
                         OPQ(out[K0 + (k)][2 * (c)]); OPQ(out[K0 + (k)][2 * (c) + 1]); } while (0)
#pragma unroll
  for (int t = 0; t < D - 1; ++t) wf[t] = tr_frag(smem, PADDR(t), 768);
  SB;
  hook(-1);
  SB;
#pragma unroll
  for (int t = 0; t < 18; ++t) {
    const int c = PC(t), s = PS(t);
#pragma unroll
    for (int k = 0; k < NT; ++k) {
      ac[c][k] = mfma(wf[t % D], xf[K0 + k][s], s == 0 ? (f32x4){0.f, 0.f, 0.f, 0.f} : ac[c][k]);
      SB;
      if (t + D - 1 < 18) {
        if (k == 0) tr_lo(wf[(t + D - 1) % D], smem, PADDR(t + D - 1));
        if (k == (NT > 1 ? 1 : 0)) tr_hi(wf[(t + D - 1) % D], smem, PADDR(t + D - 1) + 768);
      }
      if (NT > 1 && k == NT - 1 && c >= 1 && s >= 1 && s <= NT) PPACK(c >= 1 ? c - 1 : 0, s >= 1 && s <= NT ? s - 1 : 0);
      SB;
    }
    hook(t);
    SB;
  }
#pragma unroll
  for (int c = (NT == 1 ? 0 : 2); c < 3; ++c)
#pragma unroll
    for (int k = 0; k < NT; ++k) PPACK(c, k);
  SB;
  hook(18);
  SB;
#undef PC
#undef PS
#undef PADDR
#undef PPACK
}

DI void aug_k(int tc, int g, unsigned& a0, unsigned& a1) {
  const unsigned m = ((~((0x7Fu << (tc & 31)) >> 6) & 0x3FFFu) | ((tc & 32) << 9)) >> (4 * g);
  a0 = ((m & 1) | ((m & 2) << 15)) * 0x3F80u;
  a1 = (((m >> 2) & 1) | ((m & 8) << 13)) * 0x3F80u;
}
DI void kv_addr(unsigned e, int c, int g, int& ka, int& va) {
  const int n = 20 * (int)(e & 31) + c;
  ka = KOFF + 128 * n + 16 * (g ^ (n & 7));
  va = VOFF + 96 * n + 8 * (g ^ ((c >> 2) & 3));
}

DI void x_addr(unsigned (&vo)[4], const unsigned (&xc)[3], const unsigned (&xs)[3], int p, unsigned rows) {
#pragma unroll
  for (int i = 0; i < 3; ++i) vo[i] = xc[i] + __builtin_amdgcn_ubfe(rows, xs[i], 8) * ROWB;
  unsigned f = vo[2], m = p < 2 ? ~0u : 0u;
  SOPQ(f); SOPQ(m);
  vo[3] = (vo[2] & m) | (f & ~m);
  OPQ(vo[0]); OPQ(vo[1]); OPQ(vo[2]); OPQ(vo[3]);
}
DI void dma_x(const char* xs, const unsigned (&vo)[4], unsigned lds_base, unsigned slot, int p, int i, bool late = false) {
  const int v = i < 6 ? i % 3 : (p < 2 ? i - 6 : 2);
  const int j = i < 3 ? p : (i < 6 ? p + 4 : 8 + (p & 1));
  glds16s<0>(xs + 128 * v, vo[i < 7 ? i / 3 : 3], lds_base + (i == 7 && p >= 2 ? PFD + 1024 : slot + v * XPL + j * 1024));
}

DI void mk_tq(const char* smem, int raw, int tid, unsigned (&tq)[4][2]) {
  const int r = min(tid, 223);
  const int seg = (r >= 55) + (r >= 110) + (r >= 167);
  const int m = seg ^ (seg >> 1);
  const int o = r - (int)((0x71A93802u >> (8 * m)) & 0xFF);
  unsigned tv[4][4];
  bool ok[4];
#pragma unroll
  for (int i = 0; i < 4; ++i) {
    const int lin = 4 * o + m + i;
    const int row = lin >> 4, dc = lin & 15;
    ok[i] = row >= 0 && row <= 12 && dc <= 12;
    const int src = raw + 2 * (clampi(row, 0, 12) * 13 + min(dc, 12));
#pragma unroll
    for (int hh = 0; hh < 4; ++hh) tv[hh][i] = *(const uint16_t*)(smem + src + hh * 338);
  }
  SB;
#pragma unroll
  for (int i = 0; i < 4; ++i)
#pragma unroll
    for (int hh = 0; hh < 4; ++hh) OPQ(tv[hh][i]);
  const unsigned m0 = (ok[0] ? 0xFFFFu : 0u) | (ok[1] ? 0xFFFF0000u : 0u), m1 = (ok[2] ? 0xFFFFu : 0u) | (ok[3] ? 0xFFFF0000u : 0u);
#pragma unroll
  for (int hh = 0; hh < 4; ++hh) {
    tq[hh][0] = (tv[hh][0] | (tv[hh][1] << 16)) & m0; tq[hh][1] = (tv[hh][2] | (tv[hh][3] << 16)) & m1;
    OPQ(tq[hh][0]); OPQ(tq[hh][1]);
  }
}

template <int KV, int LAST, class B>
DI void proj_phase(char* smem, int wb, int wbn, const u32x4 (&xf)[6][6], const int (&ka)[6], const int (&va)[6],
                   u32x4 (&wf)[3], f32x4 (&ac)[2][6], const char* dsrc, unsigned dstr, unsigned ddst, int p, const unsigned (&lo3)[3], B bar) {
  constexpr int D = 3;
  unsigned hold[6][2];
#define PADR(t) (((t) < 18 ? wb : wbn) + ((t) % 6) * 3072 + (((t) % 18) / 6) * 32)
#pragma unroll
  for (int t = 0; t < 18 + LAST; ++t) {
    const int c = t / 6, s = t % 6, ab = (c + KV) & 1;
#pragma unroll
    for (int k = 0; k < 6; ++k) {
      if (t < 18) ac[ab][k] = mfma(wf[t % D], xf[k][s], s == 0 ? (f32x4){0.f, 0.f, 0.f, 0.f} : ac[ab][k]);
      if (t + 2 < (LAST ? 18 : 20) && (k == 0 || k == 2)) {
        SB;
        if (k == 0) tr_lo(wf[(t + 2) % D], smem, PADR(t + 2));
        else tr_hi(wf[(t + 2) % D], smem, PADR(t + 2) + 768);
        SB;
      }
      if (t == 18 || (s < 3 && (k == 1 || k == 4))) {
        const int kb = t == 18 ? k : 2 * s + (k == 4 ? 1 : 0);
        const int pc = (c + 2) % 3, kv = c == 0 ? 1 - KV : KV;
        unsigned p0 = pack2(ac[ab ^ 1][kb][0], ac[ab ^ 1][kb][1]), p1 = pack2(ac[ab ^ 1][kb][2], ac[ab ^ 1][kb][3]);
        if (kv == 0) {
          u32x2 pk = {p0, p1};
          *(u32x2*)(smem + va[kb] + 32 * pc) = pk;
        } else if (pc == 0) {
          hold[kb][0] = p0; hold[kb][1] = p1;
        } else if (pc == 1) {
          u32x4 pk = {hold[kb][0], hold[kb][1], p0, p1};
          *(u32x4*)(smem + ka[kb]) = pk;
        } else {
          u32x2 pk = {p0, p1};
          *(u32x2*)(smem + (ka[kb] ^ 64)) = pk;
        }
        SB;
      }
    }
    SB;
    if (t == PBAR || t == 15) bar(t);
    if (t > PBAR && t <= PBAR + 10 && ((t - PBAR) & 1)) {
      const int i = (t - PBAR) / 2, P = p18(p, i);
      glds16s<0>(dsrc + (P / 3) * dstr, lo3[i % 3], ddst + P * 1024);
    }
    SB;
  }
#undef PADR
}

template <bool SAFE>
DI bool proj_wave(char* smem, unsigned lds_base, const char* xb, const uint16_t* __restrict__ wqkv,
                  const uint16_t* __restrict__ wo, const uint16_t* __restrict__ rpb, const Geo& G, int p, int l PPARM) {
  const int u = l & 15, g = l >> 4;
  TSDECL;
  const char* xq = xb + (size_t)G.or_ * ROWB;
  unsigned vo[3][4], lo3[3];
  int ka[6], va[6];
  unsigned ag[6][2];
  auto kv_setup = [&]() __attribute__((always_inline)) {
    const int t = 16 * p + u, rr = (t * 205) >> 12, c = t - 20 * rr;
    unsigned a0, a1;
    aug_k(c, g, a0, a1);
    const unsigned fl = g == 3 ? 0x3F80u : 0u;
#pragma unroll
    for (int k = 0; k < 6; ++k) {
      if (k == 4) {
        const unsigned e = chunk_rows(p, G.or_) >> 24;
        kv_addr(e, 4 + u, g, ka[k], va[k]);
        aug_k(4 + u + (e & 32), g, ag[k][0], ag[k][1]);
      } else {
        const unsigned e = __builtin_amdgcn_ubfe(chunk_rows(k < 4 ? k : 4, G.or_), 8 * rr, 8);
        kv_addr(e, c, g, ka[k], va[k]);
        ag[k][0] = a0;
        ag[k][1] = k < 3 ? a1 : (a1 | ((e & 32) ? fl : 0u));
      }
      OPQ(ka[k]);
      if (k >= 3) OPQ(ag[k][1]);
    }
    OPQ(ag[4][0]);
  };
  __builtin_amdgcn_s_setprio(3);
  unsigned xc[3], xs[3];
  x_lane(xc, xs, p, l);
  x_addr(vo[0], xc, xs, p, 0x09080100u);
  glds16s<0>(rpb, 16 * l, lds_base + RAWB + 1536 * p);
  glds4s(rpb, 1024 + 4 * l, lds_base + RAWB + 1536 * p + 1024);
  glds4s(rpb, min(1280 + 4 * l, 1348), lds_base + RAWB + 1536 * p + 1280);
  TS(0);
#pragma unroll
  for (int i = 0; i < 8; ++i) dma_x(xq + 2 * ROWB, vo[0], lds_base, SLO, p, i);
  {
    const int line = ((blockIdx.x >> 3) & 31) * 72 + 18 * p + min(l, 17);
    const bool lw = line < 1728;
    const unsigned long long dw = (unsigned long long)wo - (unsigned long long)wqkv - 1728ull * 128ull;
    const char* pa = (const char*)wqkv + line * 128 + (lw ? 0ull : dw);
    asm volatile("s_mov_b32 m0, %1\n\ts_nop 0\n\tglobal_load_lds_dword %0, off"
                 :: "v"(pa), "s"(__builtin_amdgcn_readfirstlane(lds_base + PFD + 256 * p)) : "memory", "m0");
  }
  TS(1);
  __builtin_amdgcn_s_setprio(0);
  mk_lo3(p, l, lo3);
#pragma unroll
  for (int j = 0; j < 3; ++j) OPQ(lo3[j]);
  const int wlane = (16 * (g >> 1) + 4 * (g & 1) + (u >> 2)) * 96 + 8 * (u & 3);
  int wbk = WKOFF + wlane;
  OPQ(wbk);
  x_addr(vo[1], xc, xs, p, 0x0706u + 0x0101u * G.or_ + (G.or_ == 1 ? 0x0F010000u : 0x01000000u));
  u32x4 xf[6][6];
  unsigned k0[6][6];
  unsigned tq[4][2];
  const int tp = 16 * p + u, te = 64 + u, tid = 64 * p + l;
  x_addr(vo[2], xc, xs, p, G.or_ == 1 ? 0x13121110u : (G.or_ == 3 ? 0x13121102u : 0x12040302u));
  kv_setup();
  SB;
  wait_vm<9>();
  mk_tq(smem, RAWB + 1536 * p, tid, tq);
  SB;
  TS(2);
#pragma unroll
  for (int i = 0; i < 5; ++i) dma_w((const char*)wqkv + 384, lds_base + WKOFF, p18(p, i), lo3[i % 3]);
  TS(3);
  __builtin_amdgcn_s_setprio(3);
  wait_vm<5>(); rawb();
  TS(4);
  x_read(smem, SLE, tp, g, xf[0]);
  x_read(smem, SLO, tp, g, xf[1]);
  x_read(smem, (p & 1) ? SLO : SLE, te, g, xf[4]);
  wait_vm<0>(); ldsb();
  TS(5);
  __builtin_amdgcn_s_setprio(QPRIO);
  projN<2, 6, 0>(smem, wbk, xf, k0, LAM {
    if (t < 0) {
#pragma unroll
      for (int i = 0; i < 4; ++i) dma_x(xq + 4 * ROWB, vo[0], lds_base, SLE, p, i, true);
    }
    else if (t < 4) dma_x(xq + 4 * ROWB, vo[0], lds_base, SLE, p, t + 4, true);
    else if (t < 12) dma_x(xb, vo[1], lds_base, SLO, p, t - 4, true);
    if (t == 11) TS(6);
  });
  TS(7);
  TS(8);
  __builtin_amdgcn_s_setprio(3);
  wait_vm<0>();
  rawb();
  TS(9);
  x_read(smem, SLE, tp, g, xf[2]);
  x_read(smem, SLO, tp, g, xf[3]);
  if (p >= 2) x_read(smem, (p & 1) ? SLO : SLE, te, g, xf[4]);
  auto k_write = [&](int k) __attribute__((always_inline)) {
    u32x4 pa = {k0[k][0], k0[k][1], k0[k][2], k0[k][3]};
    u32x4 pb = {k0[k][4], k0[k][5], ag[k][0], ag[k][1]};
    *(u32x4*)(smem + ka[k]) = pa;
    *(u32x4*)(smem + (ka[k] ^ 64)) = pb;
  };
  projN<2, 6, 2>(smem, wbk, xf, k0, LAM {
    if (t < 0) {
      ldsb();
      TS(10);
#pragma unroll
      for (int i = 0; i < 4; ++i) dma_x(xb, vo[2], lds_base, SLE, p, i, true);
      __builtin_amdgcn_s_setprio(QPRIO);
    }
    else if (t < 4) dma_x(xb, vo[2], lds_base, SLE, p, t + 4, true);
    else if (t < 9) dma_w((const char*)wqkv + 768, lds_base + WVOFF, p18(p, t - 4), lo3[(t - 4) % 3]);
  });
  projN<2, 6, 4>(smem, wbk, xf, k0, LAM {
    if (t < 0) {
      TS(11);
      __builtin_amdgcn_s_setprio(EPRIO);
      wait_vm<5>(); rawb();
      STAMP(5);
      x_read(smem, SLE, tp, g, xf[5]);
      ldsb();
    }
    else if (t % 4 == 0 && t < 16) k_write(t / 4);
    else if (t == 18) { k_write(4); k_write(5); }
  });
  f32x4 tf[4];
#pragma unroll
  for (int hh = 0; hh < 4; ++hh) {
    tf[hh] = (f32x4){__uint_as_float(tq[hh][0] << 16) * LOG2E, __uint_as_float(tq[hh][0] & 0xFFFF0000u) * LOG2E,
                     __uint_as_float(tq[hh][1] << 16) * LOG2E, __uint_as_float(tq[hh][1] & 0xFFFF0000u) * LOG2E};
    OPQ(tf[hh]);
  }
  {
    const int t = 16 * p + u, c = t - 20 * ((t * 205) >> 12);
#pragma unroll
    for (int k = 0; k < 6; ++k) {
      va[k] = VOFF + 96 * ((ka[k] - KOFF) >> 7) + 8 * (g ^ (((k == 4 ? 4 + u : c) >> 2) & 3));
      OPQ(va[k]);
    }
  }
  SB;
  STAMP(4);
  wait_vm<0>(); rawb();
  STAMP(6);
  if (tid < 224) {
#pragma unroll
    for (int hh = 0; hh < 4; ++hh) *(f32x4*)(smem + TOFF + hh * T_HSTR + 16 * tid) = tf[hh];
  } else if (tid < 226) {
    const float nb = __uint_as_float(NEGBF << 16);
    *(f32x4*)(smem + NEGOFF + 448 * (tid - 224)) = (f32x4){nb, nb, nb, nb};
  }
  SB;
  STAMP(7);
  ldsb();
#pragma unroll
  for (int i = 0; i < 5; ++i) dma_w((const char*)wqkv + 384 + 96, lds_base + WB1, p18(p, i), lo3[i % 3]);
  TSOUT(12);
  __builtin_amdgcn_s_setprio(PPRIO);
  u32x4 wf[3];
  f32x4 ac[2][6];
#pragma unroll
  for (int k = 0; k < 6; ++k)
    ac[1][k] = (f32x4){__uint_as_float(k0[k][4] << 16), __uint_as_float(k0[k][4] & 0xFFFF0000u),
                       __uint_as_float(k0[k][5] << 16), __uint_as_float(k0[k][5] & 0xFFFF0000u)};
  int wa = WVOFF + wlane, wb = WB1 + wlane, wc = WKOFF + wlane;
  unsigned da = lds_base + WKOFF, db = lds_base + WVOFF, dc = lds_base + WB1;
  wf[0] = tr_frag(smem, wa, 768);
  wf[1] = tr_frag(smem, wa + 3072, 768);
  BWDECL;
  int h = 0;
  auto barY = [&](int t) __attribute__((always_inline)) {
    if (t == PBAR) { if (h != 0) PBW_Y; }
    else if (h == 0) { wait_vm<5>(); rawb(); }
  };
  auto barX = [&](int t) __attribute__((always_inline)) { if (t == PBAR) PBW_X; };
  unsigned dstr = 36864;
  const char* dsk = (const char*)wqkv + 384 + 192;
#pragma unroll 1
  for (; h < 3; ++h) {
    OPQ(wa); OPQ(wb); OPQ(wc);
    STAMP(8 + 4 * h);
    proj_phase<0, 0>(smem, wa, wb, xf, ka, va, wf, ac, (const char*)wqkv + 768 + 96 * (h + 1), 36864, da, p, lo3, barY);
    STAMP(10 + 4 * h);
    if (h == 2) {
#pragma unroll
      for (int j = 0; j < 3; ++j) {
        int idx = 64 * ((p + j) % 3) + l;
        int row = idx / 24, pc = idx - 24 * row;
        lo3[j] = row * 384 + ((pc >> 1) ^ ((row >> 1) & 3)) * 32 + (pc & 1) * 16;
      }
      dstr = 3072;
      dsk = (const char*)wo;
    }
    proj_phase<1, 0>(smem, wb, wc, xf, ka, va, wf, ac, dsk, dstr, db, p, lo3, barX);
    dsk += 96;
    const int wt = wa; wa = wc; wc = wb; wb = wt;
    const unsigned dt = da; da = dc; dc = db; db = dt;
  }
  STAMP(20);
  proj_phase<0, 1>(smem, wa, wa, xf, ka, va, wf, ac, (const char*)wo + 18432, 3072, lds_base + WKOFF, p, lo3, barY);
  STAMP(21);
  LDSB_X(63);
  BWOUT();
  STAMP(22);
#pragma unroll
  for (int t = 0; t < 9; ++t) { int P = p + 4 * t; glds16s<0>((const char*)wo + 36864 + (P / 3) * 3072, lo3[t % 3], lds_base + WOHI + P * 1024); }
  wait_vm<0>();
  STAMP(23);
  ldsb();
  if (SAFE) return false;
  rawb();
  const u32x4 fl = *(const u32x4*)(smem + FLAGOFF);
  return __builtin_amdgcn_readfirstlane(fl[0] | fl[1] | fl[2] | fl[3]) != 0;
}

template <int NU> constexpr int g_cnt(int s) { return (s < NU - 1 ? s : NU - 1) - (s > 3 ? s - 3 : 0) + 1; }
template <int NU> constexpr int g_first(int s) { int c = 0; for (int i = 0; i < s; ++i) c += g_cnt<NU>(i); return c; }
template <int NU> constexpr int g_step(int j) { int s = 0; while (g_first<NU>(s + 1) <= j) ++s; return s; }
template <int NU> constexpr int g_unit(int j) { int s = g_step<NU>(j); return (s > 3 ? s - 3 : 0) + j - g_first<NU>(s); }
template <int NU> constexpr int g_idx(int k, int t) { return g_first<NU>(k + t) + k - (k + t > 3 ? k + t - 3 : 0); }

template <int NU> constexpr bool q_dbl(int t) { return NU == 4 || t < 72; }
template <int NU> constexpr int q_c(int t) { return q_dbl<NU>(t) ? (t % 72) / 6 : (t >= 156 ? 2 : 2 * ((t % 72) / 12) + (t & 1)); }
template <int NU> constexpr int q_s(int t) { return q_dbl<NU>(t) ? t % 6 : (t >= 156 ? t - 156 : (t % 12) / 2); }
template <int NU> constexpr int q_gi(int t) { return q_dbl<NU>(t) ? t / 6 : t / 12; }
template <int NU> constexpr int q_off(int t) { return NU == 3 && t >= 144 ? q_s<NU>(t) * 3072 + q_c<NU>(t) * 32 : (q_s<NU>(t) % 3) * 12288 + (q_c<NU>(t) >> 2) * 128; }
#define XBJ (NG - 4)
#define QD 6
#define L23S 50
#define XSP 2

template <int NU, bool SAFE>
DI bool attn_wave(char* smem, unsigned lds_base, const char* xb, const uint16_t* __restrict__ wqkv,
                  uint16_t* __restrict__ out, const Geo& G, int a, int l PPARM) {
  const int u = l & 15, g = l >> 4;
  const int j_ = a & 1, ib = a >> 1;
  TSDECL;
  __builtin_amdgcn_s_setprio(3);
  unsigned lq3[3];
  {
    unsigned vo[4], xc[3], xs[3];
    x_lane(xc, xs, a, l);
    x_addr(vo, xc, xs, a, 0x09080100u);
#pragma unroll
    for (int i = 0; i < 8; ++i) dma_x(xb + (size_t)G.or_ * ROWB, vo, lds_base, SLE, a, i);
  }
  __builtin_amdgcn_s_setprio(1);
  mk_lq3(a, l, lq3);
#define DMA_Q(i0, i1) do { SB; _Pragma("unroll") for (int i = i0; i < i1; ++i) \
    glds16s<0>((const char*)wqkv + (i / 3) * 36864, lq3[i % 3], lds_base + WQ + a * 1024 + i * 4096); SB; } while (0)
  DMA_Q(0, 6);
  const int uq = min(u, 13);
  const int qa = uq >= 7 ? 1 : 0;
  const int qb = uq - 7 * qa;
  const int tu = 20 * (2 * ib + qa) + G.oc_ + 7 * j_ + qb;
  int xa = tu * 128 + 16 * (g ^ ((tu + 3 * (2 * ib + qa)) & 7)), xo = xa ^ 64;
  OPQ(xa); OPQ(xo);
#define X_FRAG(slot, s) (*(const u32x4*)(smem + (((s) & 1) ? xo : xa) + (slot) + XPL * ((s) >> 1)))
  const int b0 = G.Bc + 7 * j_;
  const int kb0 = clampi(b0 - 3, 0, 99), kc0 = kb0 - G.C0;
  const int wlane = (16 * (g >> 1) + 4 * (g & 1) + (u >> 2)) * 96 + 8 * (u & 3);
  int wbk = WKOFF + wlane;
  OPQ(wbk);
  DMA_Q(6, 8);
  int wq[2][4];
#pragma unroll
  for (int b = 0; b < 4; ++b) {
    const int rowl = 4 * g + (u >> 2);
    wq[0][b] = WQ + rowl * 384 + 32 * (b ^ ((rowl >> 1) & 3)) + 8 * (u & 3);
    wq[1][b] = wq[0][b] + 36864;
    OPQ(wq[0][b]); OPQ(wq[1][b]);
  }
  DMA_Q(8, 10);
  unsigned augB0, augB1;
  int baseK, baseV, baseC;
  {
    const int b = b0 + qb;
    const int sbrel = clampi(b - 3, 0, 105) - G.C0;
    unsigned v[4];
#pragma unroll
    for (int jj = 0; jj < 4; ++jj) v[jj] = (sbrel == 4 * g + jj || 4 * g + jj == 14) ? NEGBF : 0u;
    augB0 = v[0] | (v[1] << 16);
    augB1 = v[2] | (v[3] << 16);
    OPQ(augB0); OPQ(augB1);
    DMA_Q(10, 12);
    const int colK = kc0 + u <= 19 ? kc0 + u : kc0 + u - 16;
    baseK = KOFF + 128 * colK + 16 * (g ^ (colK & 7));
    const int cV = kc0 + min(4 * g + (u >> 2), 12);
    baseV = VOFF + 96 * cV + 8 * ((u & 3) ^ ((cV >> 2) & 3));
    const int beta_c = kb0 - b + 6;
    const int m = beta_c & 3;
    const int om = (0x71A93802u >> (8 * m)) & 0xFF;
    baseC = TOFF + 16 * om + 4 * (beta_c - m) + 16 * g - 64 * qa + 192;
    OPQ(baseC);
  }
  const bool edge = G.or_ == (NU == 4 ? 1 : 5);
  const int pb0 = ((G.or_ - 3) >> 1) + (NU == 4 ? 0 : 4), pbs = edge ? (NU == 4 ? 3 : 1) : pb0;
  int aKa = baseK + 5120 * pb0, aKs = baseK + 5120 * pbs, aV = baseV + 3840 * pb0, aVs = baseV + 3840 * pbs;
  OPQ(aKa); OPQ(aKs); OPQ(aV); OPQ(aVs);
  int negv = NEGOFF;
  OPQ(negv);
  DMA_Q(12, 14);
  int kae = 0, vae = 0;
  unsigned ae0 = 0, ae1 = 0;
  if (NU == 3) {
    const unsigned e = chunk_rows(4, G.or_) >> 24;
    kv_addr(e, 4 + u, g, kae, vae);
    aug_k(4 + u + (e & 32), g, ae0, ae1);
    OPQ(kae); OPQ(vae); OPQ(ae0); OPQ(ae1);
  }
  DMA_Q(14, 18);
  __builtin_amdgcn_s_setprio(2);
  STAMP(1);
  u32x4 xu[NU][6];
  TS(6);
  wait_vm<18>(); rawb();
  TS(7);
#pragma unroll
  for (int s = 0; s < 6; ++s) { xu[0][s] = X_FRAG(SLE, s); xu[1][s] = X_FRAG(SLO, s); }
  unsigned Q[NU][24];
  u32x4 xe[1][6];
  unsigned ke[6];
  float oi[12];
  SB;
  TS(1);
  wait_vm<0>(); ldsb();
  STAMP(2);
  TS(2);
  {
    constexpr int D = QD, NS = NU == 4 ? 144 : 162;
    u32x4 wf[D];
    f32x4 ac[2][2];
    sfor<0, D - 1>(SLAM(tc_) { constexpr int t = decltype(tc_)::v; wf[t] = tr_frag(smem, wq[q_s<NU>(t) / 3][q_c<NU>(t) & 3] + q_off<NU>(t), 6144); });
    SB;
    sfor<0, NS + 2>(SLAM(tc_) {
      constexpr int t = decltype(tc_)::v, wi = t / 72, t1 = t % 72;
      constexpr bool dbl = q_dbl<NU>(t), kb = NU == 3 && wi == 2;
      constexpr int s = t < NS ? q_s<NU>(t) : 0, gi = q_gi<NU>(t), gs = dbl ? t % 6 : t % 12;
      constexpr int tn = t + D - 1;
      const f32x4 z = {0.f, 0.f, 0.f, 0.f};
      if constexpr (t < NS) {
        constexpr int ch = dbl || (kb && t1 >= 12) ? 0 : (t & 1);
        constexpr bool kn = NU == 3 && tn >= 144;
        const int an = (kn ? wbk : wq[(q_s<NU>(tn) / 3) & 1][q_c<NU>(tn) & 3]) + q_off<NU>(tn < NS ? tn : 0);
        if constexpr (kb) ac[gi & 1][ch] = mfma(wf[t % D], xe[0][s], s == 0 ? z : ac[gi & 1][ch]);
        else ac[gi & 1][ch] = mfma(wf[t % D], xu[2 * wi][s], s == 0 ? z : ac[gi & 1][ch]);
        SB;
        if (tn < NS) tr_lo(wf[tn % D], smem, an);
        SB;
        if constexpr (dbl) ac[gi & 1][1] = mfma(wf[t % D], xu[2 * wi + 1][s], s == 0 ? z : ac[gi & 1][1]);
        SB;
        if (tn < NS) tr_hi(wf[tn % D], smem, an + (kn ? 768 : 6144));
      }
      constexpr int tp_ = t < NS ? t - gs - 1 : NS - 1;
      sfor<0, 4>(SLAM(ec_) {
        constexpr int e = decltype(ec_)::v, k = e >> 1;
        if constexpr (t < NS ? (tp_ >= 0 && gs == 1 + e) : (t - NS == e / 2)) {
          constexpr int pg = q_gi<NU>(tp_), pw = tp_ / 72;
          const float a0 = ac[pg & 1][k][2 * (e & 1)], a1 = ac[pg & 1][k][2 * (e & 1) + 1];
          if constexpr (NU == 3 && pw == 2) {
            constexpr int o = (tp_ >= 156 ? 4 : 2 * k) + (e & 1);
            if (!(tp_ >= 156 && k == 1)) { ke[o] = pack2(a0, a1); OPQ(ke[o]); }
          } else {
            constexpr int uu = 2 * pw + (q_dbl<NU>(tp_) ? k : 0), cb = q_dbl<NU>(tp_) ? q_c<NU>(tp_) : q_c<NU>(tp_) - 1 + k;
            constexpr int o = 2 * cb + (e & 1);
            if constexpr (uu == NU - 1 && o >= 18) {
              oi[2 * (o - 18)] = a0; oi[2 * (o - 18) + 1] = a1;
              OPQ(oi[2 * (o - 18)]); OPQ(oi[2 * (o - 18) + 1]);
            } else {
              Q[uu][o] = pack2(a0 * C1, a1 * C1);
              OPQ(Q[uu][o]);
            }
          }
        }
      });
      SB;
      if (t == 0) TS(0);
      if (t == 30) TS(3);
      if (t == L23S) TS(4);
      if (t == L23S + 6 * XSP - 1 + D) TS(5);
      if (t == 72 + (NU == 4 ? L4S : L4S3)) TS(8);
      if (t == 72 + (NU == 4 ? L4S + 3 : L4S3 + D)) TS(9);
      if (t == 72 + (NU == 4 ? L4S + 4 : L4S3 + D + 1)) TS(10);
      if (t == NS + 1) TS(11);
      if (t == L23S) {
        rawb();
        STAMP(3);
      }
      if (t >= L23S && t < L23S + 6 * XSP) {
        constexpr int i = (t - L23S) / XSP;
        if ((t - L23S) % XSP == 0) xu[2][i] = X_FRAG(SLE, i);
        if (NU == 4 && (t - L23S) % XSP == XSP - 1) xu[NU - 1][i] = X_FRAG(SLO, i);
        SB;
      }
      if (t == L23S + 6 * XSP - 1 + D) { rawb(); SB; }
      if (t == 72 + (NU == 4 ? L4S : L4S3)) {
        rawb();
        STAMP(5);
        if (NU == 3) x_read(smem, SLE, 64 + u, g, xe[0]);
        SB;
      }
      if (t == 72 + (NU == 4 ? L4S + 3 : L4S3 + D)) { rawb(); SB; }
    });
  }
  if (NU == 3) {
    u32x4 pa = {ke[0], ke[1], ke[2], ke[3]};
    u32x4 pb = {ke[4], ke[5], ae0, ae1};
    *(u32x4*)(smem + kae) = pa;
    *(u32x4*)(smem + (kae ^ 64)) = pb;
  }
  SB;
  rawb();
  STAMP(6);
  u32x4 Qa[NU], Qb[NU];
  unsigned R[NU][18];
#pragma unroll
  for (int k = 0; k < NU; ++k) {
    Qa[k] = (u32x4){Q[k][0], Q[k][1], Q[k][2], Q[k][3]};
    Qb[k] = (u32x4){Q[k][4], Q[k][5], augB0, augB1};
#pragma unroll
    for (int e = 0; e < 18; ++e) R[k][e] = (k == NU - 1 && e >= 12) ? 0u : Q[k][6 + e];
  }
  STAMP(7);
  TSOUT(12);
  ldsb();
  __builtin_amdgcn_s_setprio(APRIO);

  constexpr int NG = 4 * NU, NS_ = NU + 3;
  const u32x4 ones = {0x3F803F80u, 0x3F803F80u, 0x3F803F80u, 0x3F803F80u};
  const f32x4 zero4 = {0.f, 0.f, 0.f, 0.f};
  u32x4 kf[2][4];
  f32x4 T[8];
  f32x4 S[NG][2];
  f32x4 o[NU][4];
  float inv, nt0, nt1;
  unsigned lmn = 0x3F800000u, lmx = 0x3F800000u;
#pragma unroll
  for (int db = 0; db < 3; ++db) o[NU - 1][db + 1] = (f32x4){oi[4 * db], oi[4 * db + 1], oi[4 * db + 2], oi[4 * db + 3]};
  o[NU - 1][0] = (f32x4){1.0f / C1, 0.f, 0.f, 0.f};
  int aCm, aC0, aC7, aCx, aCy, aCz;
  int hoff = 0;
#define C_ADDR() do { aCm = baseC + hoff; aC0 = qa ? negv : aCm; aC7 = qa ? aCm : negv; \
    aCy = aCm + (edge ? (NU == 4 ? 512 : -512) : 0); aCx = edge ? aCy : (NU == 4 ? aC0 : aC7); aCz = edge ? aCm : (NU == 4 ? aC7 : aC0); } while (0)
#define C_OWN(k, r) ((r) == 0 || (r) == 7 || (NU == 4 && (k) == 0 && (r) == 1) || (NU == 3 && (k) == 2 && (r) == 6))
#define C_SEL(k, r) (NU == 4 ? ((r) == 0 ? ((k) == 0 ? aCx : aC0) : ((r) == 7 ? ((k) < 2 ? aCz : aC7) : ((k) == 0 && (r) == 1 ? aCy : aCm))) \
                             : ((r) == 0 ? ((k) == 0 ? aC0 : aCz) : ((r) == 7 ? ((k) == 2 ? aCx : aC7) : ((k) == 2 && (r) == 6 ? aCy : aCm))))
#define LD_D(k, r) S[g_idx<NU>(k, (r) / 2)][(r) & 1] = *(const f32x4*)(smem + C_SEL(k, r) + 64 * (r))
#define LD_T(r) T[r] = *(const f32x4*)(smem + aCm + 64 * (r))
#define LD_K(s, i) do { const int ab_ = (s) == (NU == 4 ? 0 : 5) ? aKs : aKa; \
    kf[(s) & 1][i] = *(const u32x4*)(smem + ((((i) >> 1) ^ ((i) & 1)) ? ab_ ^ 64 : ab_) + 5120 * (s) + 2560 * ((i) & 1)); } while (0)
#define LD_V(s, db) vf[(s) & 1][db] = tr_frag(smem, ((s) == (NU == 4 ? 0 : 5) ? aVs : aV) + 3840 * (s) + 32 * (db), 1920)
#define X_PRE() do { LD_D(0, 0); if (NU == 4) LD_D(0, 1); else LD_T(1); LD_K(0, 0); LD_K(0, 1); LD_K(0, 2); LD_K(0, 3); \
    LD_T(2); LD_T(3); LD_K(1, 0); LD_K(1, 1); LD_K(1, 2); LD_K(1, 3); LD_D(1, 0); if (NU == 4) LD_T(1); } while (0)
#define NORM_A(k, i) do { \
      if ((i) == 0) { const float l_ = o[k][0][0]; inv = __builtin_amdgcn_rcpf(l_); OPQ(inv); } \
      else { const f32x4 ov_ = o[k][i]; nt0 = ov_[0] * inv; nt1 = ov_[1] * inv; OPQ(nt0); OPQ(nt1); } } while (0)
#define NORM_B(k, i) do { \
      if ((i) == 0) { const float l_ = o[k][0][0]; \
        if (!SAFE) { lmn = min(lmn, __float_as_uint(l_)); lmx = max(lmx, __float_as_uint(l_)); OPQ(lmn); OPQ(lmx); } } \
      else { const f32x4 ov_ = o[k][i]; \
        R[k][10 + 2 * (i)] = pack2(nt0, nt1); R[k][11 + 2 * (i)] = pack2(ov_[2] * inv, ov_[3] * inv); \
        OPQ(R[k][10 + 2 * (i)]); OPQ(R[k][11 + 2 * (i)]); } } while (0)
#define NORM_PART(k, i) do { NORM_A(k, i); NORM_B(k, i); } while (0)
#define ROT_A(k, i) do { \
      if ((i) == 0) { Qa[k][0] = vmov(R[k][0]); Qa[k][1] = vmov(R[k][1]); } \
      else if ((i) == 1) { Qb[k][0] = vmov(R[k][4]); Qb[k][1] = vmov(R[k][5]); } \
      else if ((i) == 2) { R[k][3] = vmov(R[k][9]); R[k][4] = vmov(R[k][10]); } \
      else { R[k][8] = vmov(R[k][14]); R[k][9] = vmov(R[k][15]); } } while (0)
#define ROT_B(k, i) do { \
      if ((i) == 0) { Qa[k][2] = vmov(R[k][2]); Qa[k][3] = vmov(R[k][3]); } \
      else if ((i) == 1) { R[k][0] = vmov(R[k][6]); R[k][1] = vmov(R[k][7]); R[k][2] = vmov(R[k][8]); } \
      else if ((i) == 2) { R[k][5] = vmov(R[k][11]); R[k][6] = vmov(R[k][12]); R[k][7] = vmov(R[k][13]); } \
      else { R[k][10] = vmov(R[k][16]); R[k][11] = vmov(R[k][17]); } } while (0)
  BWDECL;
  int wbv = WVOFF + wlane, wbc = wbk;
  wbk = WB1 + wlane;
  C_ADDR();
  if (!SAFE) X_PRE();
  SB;

#pragma unroll 1
  for (int h = 0; h < 4; ++h) {
    OPQ(aKa); OPQ(aKs); OPQ(aV); OPQ(aVs); OPQ(baseC);
    if (NU == 3) { OPQ(wbv); OPQ(wbk); OPQ(wbc); }
    unsigned zs = 0;
    asm volatile("" : "+s"(zs));
    STAMP(8 + 4 * h);
    u32x4 P[NG];
    u32x4 vf[2][3];
    if (NU == 3) {
      unsigned ve[1][6];
      PH;
      projN<1, 6, 0>(smem, wbv, xe, ve, LAM { if (t >= 2 && t < 10 && !(t & 1)) NORM_PART(NU - 1, (t - 2) / 2); });
#pragma unroll
      for (int db = 0; db < 3; ++db) { u32x2 pk = {ve[0][2 * db], ve[0][2 * db + 1]}; *(u32x2*)(smem + vae + 32 * db) = pk; }
      SB;
    }
    if constexpr (SAFE) {
      if (NU == 4) { NORM_PART(NU - 1, 0); NORM_PART(NU - 1, 1); NORM_PART(NU - 1, 2); NORM_PART(NU - 1, 3); }
      sfor<0, NU>(SLAM(kc_) {
        constexpr int k = decltype(kc_)::v;
        float pm = 0.f;
        sfor<0, 4>(SLAM(tc_) {
          constexpr int t = decltype(tc_)::v, j = g_idx<NU>(k, t);
          LD_D(k, 2 * t); LD_D(k, 2 * t + 1);
          LD_K(k + t, 0); LD_K(k + t, 1); LD_K(k + t, 2); LD_K(k + t, 3);
#pragma unroll
          for (int i = 0; i < 4; ++i) S[j][i & 1] = mfma(kf[(k + t) & 1][i], (i >> 1) ? Qb[k] : Qa[k], S[j][i & 1]);
#pragma unroll
          for (int i = 0; i < 2; ++i) {
            const float m2 = max2(max2(S[j][i][0], S[j][i][1]), max2(S[j][i][2], S[j][i][3]));
            pm = (t == 0 && i == 0) ? m2 : max2(pm, m2);
          }
          SB;
        });
        pm = xmax32(xmax16(pm));
        sfor<0, 4>(SLAM(tc_) {
          constexpr int j = g_idx<NU>(k, decltype(tc_)::v);
#pragma unroll
          for (int i = 0; i < 4; ++i)
            P[j][i] = pack2(__builtin_amdgcn_exp2f(S[j][i >> 1][2 * (i & 1)] - pm), __builtin_amdgcn_exp2f(S[j][i >> 1][2 * (i & 1) + 1] - pm));
          OPQ(P[j]);
        });
        SB;
      });
      if (h == 0) rawb();
      ldsb();
    } else {
      float ev[2][2];
      constexpr int EXN = NU == 4 ? EXN4 : EXN3;
#define E_PACK(z) do { if ((z) >= 0) { unsigned pk_ = pack2(ev[(z) & 1][0], ev[(z) & 1][1]); OPQ(pk_); P[(z) / 4][(z) % 4] = pk_; } } while (0)
#define E_HALF(z, hf) do { const int i_ = (z) % 4; \
      ev[(z) & 1][hf] = __builtin_amdgcn_exp2f(S[(z) / 4][i_ >> 1][2 * (i_ & 1) + (hf)]); OPQ(ev[(z) & 1][hf]); } while (0)
      sfor<0, NG>(SLAM(jc_) {
        constexpr int j = decltype(jc_)::v, s = g_step<NU>(j), k = g_unit<NU>(j), t = s - k;
        constexpr bool first = j == g_first<NU>(s);
        if (j == XBJ && h == 0) rawb();
        if (j == NG - 1) {
          LDSB_X(63);
          STAMP(9 + 4 * h);
          LD_V(0, 0); LD_V(0, 1); LD_V(0, 2);
          LD_V(1, 0); LD_V(1, 1); LD_V(1, 2);
          SB;
        }
        sfor<0, 4>(SLAM(ic_) {
          constexpr int i = decltype(ic_)::v, r = 2 * t + (i & 1);
          constexpr bool ex = j >= 1 && j - 1 < EXN;
          constexpr int z = 4 * (j - 1) + i;
          if (TVX((i + 3) % 4) == 0 || (j == 0 && i == 0)) PH;
          S[j][i & 1] = mfma(kf[s & 1][i], (i >> 1) ? Qb[k] : Qa[k], (i < 2 && !C_OWN(k, r)) ? T[r] : S[j][i & 1]);
          SB;
          if (TVX(i) != 3) PL;
          if (first && s >= 1 && s + 1 < NS_) LD_K(s + 1, i);
          if (j == 0) { if (i == 0) LD_T(4); if (i == 1) LD_T(5); if (i == 2) LD_D(2, 0); }
          if (j == 2) { if (i == 0) LD_T(6); if (i == 1) LD_D(0, 7); if (i == 2 && NU == 4) LD_D(NU - 1, 0); }
          if (j == 7 && i == 0) LD_D(1, 7);
          if (NU == 4 && j == 11 && i == 0) LD_D(2, 7);
          if (j == NG - 3 && i == 3) { LD_D(NU - 1, 7); if (NU == 3) LD_D(NU - 1, 6); }
          SB;
          if (ex) E_HALF(z, 0);
          if (NU == 4 && j == 0) NORM_A(NU - 1, i);
          SB;
          if (TVX(i) == 2) PH;
          if (ex) E_HALF(z, 1);
          SB;
          if (TVX(i) == 1) PH;
          if (ex) E_PACK(z - 1);
          if (NU == 4 && j == 0) NORM_B(NU - 1, i);
          SB;
        });
      });
      STAMP(10 + 4 * h);
      if (NU == 3 && h < 3) {
        unsigned ke[1][6];
        PH;
        projN<1, 6, 0>(smem, wbk, xe, ke, LAM {});
        u32x4 pa = {ke[0][0], ke[0][1], ke[0][2], ke[0][3]};
        u32x2 pb = {ke[0][4], ke[0][5]};
        *(u32x4*)(smem + kae) = pa;
        *(u32x2*)(smem + (kae ^ 64)) = pb;
        const int wt = wbv; wbv = wbc; wbc = wbk; wbk = wt;
        SB;
      }
      sfor<0, NG>(SLAM(jc_) {
        constexpr int j = decltype(jc_)::v, s = g_step<NU>(j), k = g_unit<NU>(j), t = s - k;
        constexpr bool first = j == g_first<NU>(s);
        constexpr int rk = NU == 4 ? (j == 8 ? 3 : (j >= 4 && j < 7 ? j - 4 : -1)) : (j >= 4 && j < 7 ? j - 4 : -1);
        constexpr int nk = j >= 1 && g_step<NU>(j >= 1 ? j - 1 : 0) - g_unit<NU>(j >= 1 ? j - 1 : 0) == 3 ? g_unit<NU>(j >= 1 ? j - 1 : 0) : -1;
        if (j == NG - 1) {
          if (h == 3) BWOUT();
          LDSB_Y(63);
          STAMP(11 + 4 * h);
          hoff += T_HSTR;
          C_ADDR();
          X_PRE();
          SB;
        }
        sfor<0, 4>(SLAM(ic_) {
          constexpr int i = decltype(ic_)::v;
          constexpr bool ex = EXN + j < NG;
          constexpr int z = 4 * (EXN + j) + i;
          if (TVY((i + 3) % 4) == 0 || (j == 0 && i == 0)) PH;
          o[k][i] = mfma(i ? vf[s & 1][i ? i - 1 : 0] : ones, P[j], t == 0 ? zero4 : o[k][i]);
          SB;
          if (TVY(i) != 3) PL;
          if (first && s >= 1 && s + 1 < NS_ && i < 3) LD_V(s + 1, i);
          SB;
          if (ex) E_HALF(z, 0);
          if (EXN + j == NG && i == 0) E_PACK(4 * NG - 1);
          if (rk >= 0) ROT_A(rk >= 0 ? rk : 0, i);
          if (nk >= 0) NORM_A(nk >= 0 ? nk : 0, i);
          SB;
          if (TVY(i) == 2) PH;
          if (ex) E_HALF(z, 1);
          SB;
          if (TVY(i) == 1) PH;
          if (ex) E_PACK(z - 1);
          if (rk >= 0) ROT_B(rk >= 0 ? rk : 0, i);
          if (nk >= 0) NORM_B(nk >= 0 ? nk : 0, i);
          SB;
        });
      });
    }
    if constexpr (SAFE) {
      if (NU == 3 && h < 3) {
        unsigned ke[1][6];
        projN<1, 6, 0>(smem, wbk, xe, ke, LAM {});
        u32x4 pa = {ke[0][0], ke[0][1], ke[0][2], ke[0][3]};
        u32x2 pb = {ke[0][4], ke[0][5]};
        *(u32x4*)(smem + kae) = pa;
        *(u32x2*)(smem + (kae ^ 64)) = pb;
        const int wt = wbv; wbv = wbc; wbc = wbk; wbk = wt;
        SB;
      }
      sfor<0, NU>(SLAM(kc_) {
        constexpr int k = decltype(kc_)::v;
#pragma unroll
        for (int i = 0; i < 4; ++i) { ROT_A(k, i); ROT_B(k, i); }
        sfor<0, 4>(SLAM(tc_) {
          constexpr int t = decltype(tc_)::v, j = g_idx<NU>(k, t);
          LD_V(k + t, 0); LD_V(k + t, 1); LD_V(k + t, 2);
#pragma unroll
          for (int i = 0; i < 4; ++i) o[k][i] = mfma(i ? vf[(k + t) & 1][i ? i - 1 : 0] : ones, P[j], t == 0 ? zero4 : o[k][i]);
          SB;
        });
        if (k < NU - 1) { NORM_PART(k, 0); NORM_PART(k, 1); NORM_PART(k, 2); NORM_PART(k, 3); }
        SB;
      });
      ldsb();
      hoff += T_HSTR;
      C_ADDR();
    }
  }
#pragma unroll
  for (int i = 0; i < 4; ++i) NORM_PART(NU - 1, i);
  SB;
  PH;
  STAMP(24);

  bool redo = false;
  {
    u32x4 fl;
    int l2 = l;
    OPQ(l2);
    const int u = l2 & 15, g = l2 >> 4, q4 = u >> 2, pp = u & 3;
    const int rowl = 4 * g + q4;
    int wob[2][4];
#pragma unroll
    for (int b = 0; b < 4; ++b) {
      wob[1][b] = WOHI + rowl * 384 + 32 * (b ^ ((rowl >> 1) & 3)) + 8 * pp;
      wob[0][b] = wob[1][b] + (WOLO - WOHI);
      OPQ(wob[0][b]);
    }
    const int uq = min(u, 13);
    const int qa = uq >= 7 ? 1 : 0;
    const int qb = uq - 7 * qa;
    unsigned ooff[NU];
    u32x4 ob[NU][6];
#pragma unroll
    for (int k = 0; k < NU; ++k) {
      const int tok = (2 * (G.A + 2 * (4 * ib + k) + qa) + G.pa) * IMG + 2 * (G.Bc + 7 * j_ + qb) + G.pb;
      ooff[k] = (unsigned)tok * (CH * 2) + ((g & 1) ? (32 + 8 * (g - 1)) : 8 * g);
      ob[k][0] = Qa[k];
      ob[k][1] = (u32x4){Qb[k][0], Qb[k][1], R[k][0], R[k][1]};
#pragma unroll
      for (int sg = 2; sg < 6; ++sg) ob[k][sg] = (u32x4){R[k][4 * sg - 6], R[k][4 * sg - 5], R[k][4 * sg - 4], R[k][4 * sg - 3]};
    }
    char* obase = (char*)out;
    constexpr int D = 4;
    u32x4 wa[D];
#define OFRAG(t) tr_frag(smem, (32 * ((((t) % 12) / 2) % 3)) * 384 + wob[(((t) % 12) / 2) / 3][(2 * ((t) / 12) + ((t) & 1)) & 3] + 128 * ((2 * ((t) / 12) + ((t) & 1)) >> 2), 16 * 384)
#pragma unroll
    for (int t = 0; t < D - 1; ++t) wa[t] = OFRAG(t);
    f32x4 acc[2][2][NU];
#pragma unroll
    for (int nbp = 0; nbp <= 6; ++nbp) {
#pragma unroll
      for (int t1 = 0; t1 < (nbp < 6 ? 12 : 1); ++t1) {
        const int t = 12 * nbp + t1, sg = t1 / 2, tt = t1 & 1;
        if (t + D - 1 < 72) wa[(t + D - 1) % D] = OFRAG(t + D - 1);
        if (t < 72) {
#pragma unroll
          for (int k = 0; k < NU; ++k) acc[nbp & 1][tt][k] = mfma(wa[t % D], ob[k][sg], sg == 0 ? zero4 : acc[nbp & 1][tt][k]);
        }
#pragma unroll
        for (int k = 0; k < NU; ++k) {
          if (nbp >= 1 && (t == 72 || t1 == 2 + 2 * k)) {
            const int pb_ = (nbp - 1) & 1;
            unsigned x0 = pack2(acc[pb_][0][k][0], acc[pb_][0][k][1]), x1 = pack2(acc[pb_][0][k][2], acc[pb_][0][k][3]);
            unsigned y0 = pack2(acc[pb_][1][k][0], acc[pb_][1][k][1]), y1 = pack2(acc[pb_][1][k][2], acc[pb_][1][k][3]);
            auto r0 = __builtin_amdgcn_permlane16_swap(x0, y0, false, false);
            auto r1 = __builtin_amdgcn_permlane16_swap(x1, y1, false, false);
            u32x4 st = {r0[0], r1[0], r0[1], r1[1]};
            asm volatile("global_store_dwordx4 %0, %1, %2 offset:%3 " POL "\n\ts_nop 1" :: "v"(ooff[k]), "v"(st), "s"(obase), "n"(64 * (nbp - 1)) : "memory");
            if (!SAFE && nbp == 1) lmx = max(lmx, (__float_as_uint(acc[0][0][k][0]) & 0x7FFFFFFFu) >= 0x7F800000u ? 0x7F800000u : 0u);
          }
        }
        if (!SAFE && nbp == 1 && t1 == 10) {
          const bool bad = lmn < L_LO || lmx > L_HI;
          *(unsigned*)(smem + FLAGOFF + 4 * a) = __builtin_amdgcn_ballot_w64(bad) != 0 ? 1u : 0u;
        }
        if (!SAFE && nbp == 2 && t1 == 4) { SB; rawb(); }
        if (!SAFE && nbp == 2 && t1 == 5) { fl = *(const u32x4*)(smem + FLAGOFF); }
        if (!SAFE && nbp == 3 && t1 == 0) { redo = __builtin_amdgcn_readfirstlane(fl[0] | fl[1] | fl[2] | fl[3]) != 0; }
        SB;
      }
    }
  }
  STAMP(25);
  return redo;
}

__global__ __launch_bounds__(512, 2) void dinat_fused(const uint16_t* __restrict__ x, const uint16_t* __restrict__ wqkv,
                                                      const uint16_t* __restrict__ wo, const uint16_t* __restrict__ rpb,
                                                      uint16_t* __restrict__ out PPARM) {
  const unsigned long long dg0 = __builtin_amdgcn_s_memrealtime();
  __shared__ __attribute__((aligned(128))) char smem[LDS_BYTES];
  const unsigned lds_base = (unsigned)(size_t)(LDS_AS char*)smem;
  const int tid = threadIdx.x;
  const int w = __builtin_amdgcn_readfirstlane(tid >> 6);
  const int l = tid & 63;
  const int bidx = blockIdx.x;
  const int xcd = bidx & 7, idx = bidx >> 3;
  const int sub = 2 * (xcd & 1) + (idx & 1);
  const int ta = 2 * (xcd >> 1) + (idx >> 4), tb = (idx >> 1) & 7;
  Geo G;
  G.pa = sub >> 1; G.pb = sub & 1;
  G.A = 14 * ta; G.Bc = 14 * tb;
  G.R0 = min(max(G.A - 3, -1), 93); G.C0 = min(max(G.Bc - 3, 0), 92);
  SOPQ(G.R0); SOPQ(G.C0);
  G.or_ = G.A - G.R0; G.oc_ = G.Bc - G.C0;
  const char* xb = (const char*)x + ((long long)((2 * G.R0 + G.pa) * IMG + 2 * G.C0 + G.pb)) * (CH * 2);
  bool redo;
  STAMP(0);
  if (w >= 4) redo = proj_wave<false>(smem, lds_base, xb, wqkv, wo, rpb, G, w - 4, l PARG);
  else if (w < 2) redo = attn_wave<4, false>(smem, lds_base, xb, wqkv, out, G, w, l PARG);
  else redo = attn_wave<3, false>(smem, lds_base, xb, wqkv, out, G, w, l PARG);
  if (w == 0 && SPIN) {
    unsigned long long e = dg0 + SPIN;
    while (__builtin_amdgcn_s_memrealtime() < e) __builtin_amdgcn_s_sleep(4);
  }
  if (__builtin_expect(redo, 0)) {
    asm volatile("s_waitcnt vmcnt(0) lgkmcnt(0)\n\ts_barrier" ::: "memory");
    __builtin_amdgcn_s_setprio(0);
    int l2 = l;
    OPQ(l2);
    if (w >= 4) proj_wave<true>(smem, lds_base, xb, wqkv, wo, rpb, G, w - 4, l2 PARG);
    else if (w < 2) attn_wave<4, true>(smem, lds_base, xb, wqkv, out, G, w, l2 PARG);
    else attn_wave<3, true>(smem, lds_base, xb, wqkv, out, G, w, l2 PARG);
  }
}

extern "C" void kernel_launch(void* const* d_in, const int* in_sizes, int n_in,
                              void* d_out, int out_size, void* d_ws, size_t ws_size,
                              hipStream_t stream) {
  const uint16_t* x = (const uint16_t*)d_in[0];
  const uint16_t* wqkv = (const uint16_t*)d_in[1];
  const uint16_t* wo = (const uint16_t*)d_in[2];
  const uint16_t* rpb = (const uint16_t*)d_in[3];
  dinat_fused<<<256, 512, 0, stream>>>(x, wqkv, wo, rpb, (uint16_t*)d_out
  );
}
```

```cpp
#define POL ""
#define SPIN 0
#define MUL 0
#include <hip/hip_runtime.h>
#include <hip/hip_bf16.h>
#include <stdint.h>

typedef __attribute__((ext_vector_type(8))) __bf16 bf16x8_t;
typedef __attribute__((ext_vector_type(4))) short s16x4_t;
typedef __attribute__((ext_vector_type(4))) float f32x4;
typedef __attribute__((ext_vector_type(4))) unsigned u32x4;
typedef __attribute__((ext_vector_type(2))) unsigned u32x2;
typedef __attribute__((ext_vector_type(2))) __bf16 bf16x2_t;

#define LDS_AS __attribute__((address_space(3)))
#define DI __device__ __forceinline__
#define SB __builtin_amdgcn_sched_barrier(0)
#define OPQ(v) asm volatile("" : "+v"(v))
#define SOPQ(v) do { v = __builtin_amdgcn_readfirstlane(v); asm volatile("" : "+s"(v)); } while (0)
#define LAM [&](int t) __attribute__((always_inline))
template <int V> struct IC { static constexpr int v = V; };
template <int I, int N, class F> __device__ __forceinline__ void sfor(F&& f) {
  if constexpr (I < N) { f(IC<I>{}); sfor<I + 1, N>(f); }
}
#define SLAM(x) [&](auto x) __attribute__((always_inline))

#define IMG 224
#define CH 192
#define ROWB 172032
#define WVOFF 0
#define KOFF 18432
#define VOFF 69632
#define TOFF 108032
#define T_HSTR 3584
#define NEGOFF (TOFF + 4 * T_HSTR)
#define DUMMY 122880
#define WKOFF 145408
#define WB1 126976
#define PBAR 4
#define WOLO 126976
#define WOHI KOFF
#define LDS_BYTES 163840
#define SLE 30720
#define SLO 0
#define WQ 69632
#define RAWB 61440
#define PFD 67584
#define XPL 10240

#define C1 0.20823509f
#define ETYPE(i) 1
#define APRIO 2
#define PPRIO 1
#define PL4 13
#define EPRIO 2
#define QPRIO 1
#define L4S 28
#define L4S3 44
#define TOG 0
#define EXN4 12
#define EXN3 9
#define TVX(i) 2
#define TVY(i) 0
#define PH do { __builtin_amdgcn_s_setprio(3); SB; } while (0)
#define PL do { __builtin_amdgcn_s_setprio(TOG); SB; } while (0)
#define LOG2E 1.4426950408889634f
#define FLAGOFF 125952
#define L_LO 0x2B800000u
#define L_HI 0x53800000u
#define NEGBF 0xF180u

DI f32x4 mfma(u32x4 a, u32x4 b, f32x4 c) {
  return __builtin_amdgcn_mfma_f32_16x16x32_bf16(__builtin_bit_cast(bf16x8_t, a), __builtin_bit_cast(bf16x8_t, b), c, 0, 0, 0);
}
DI u32x2 tr_read(const char* smem, int off) {
  s16x4_t t = __builtin_amdgcn_ds_read_tr16_b64_v4i16((LDS_AS s16x4_t*)(smem + off));
  return __builtin_bit_cast(u32x2, t);
}
DI u32x4 tr_frag(const char* smem, int off, int stride2) {
  u32x2 a = tr_read(smem, off), b = tr_read(smem, off + stride2);
  u32x4 r = {a[0], a[1], b[0], b[1]};
  return r;
}
DI void tr_lo(u32x4& r, const char* smem, int off) { u32x2 a = tr_read(smem, off); r[0] = a[0]; r[1] = a[1]; }
DI void tr_hi(u32x4& r, const char* smem, int off) { u32x2 a = tr_read(smem, off); r[2] = a[0]; r[3] = a[1]; }
DI unsigned pack2(float a, float b) {
  bf16x2_t v = {(__bf16)a, (__bf16)b};
  return __builtin_bit_cast(unsigned, v);
}
DI int clampi(int v, int lo, int hi) { return min(max(v, lo), hi); }
DI float max2(float a, float b) { return __builtin_elementwise_maximum(a, b); }
DI float max3(float a, float b, float c) { return max2(max2(a, b), c); }
DI float xmax16(float v) {
  unsigned b = __float_as_uint(v);
  auto r = __builtin_amdgcn_permlane16_swap(b, b, false, false);
  return max2(__uint_as_float(r[0]), __uint_as_float(r[1]));
}
DI float xmax32(float v) {
  unsigned b = __float_as_uint(v);
  auto r = __builtin_amdgcn_permlane32_swap(b, b, false, false);
  return max2(__uint_as_float(r[0]), __uint_as_float(r[1]));
}
#define vmov(s) ((s) | zs)

template <int NT>
DI void glds16s(const void* sbase_, unsigned voff, unsigned lds_dst) {
  unsigned long long sv = (unsigned long long)sbase_;
  unsigned slo = __builtin_amdgcn_readfirstlane((unsigned)sv), shi = __builtin_amdgcn_readfirstlane((unsigned)(sv >> 32));
  unsigned long long sbase = ((unsigned long long)shi << 32) | slo;
  lds_dst = __builtin_amdgcn_readfirstlane(lds_dst);
  if (NT)
    asm volatile("s_mov_b32 m0, %2\n\ts_nop 0\n\tglobal_load_lds_dwordx4 %0, %1 nt" :: "v"(voff), "s"(sbase), "s"(lds_dst) : "memory", "m0");
  else
    asm volatile("s_mov_b32 m0, %2\n\ts_nop 0\n\tglobal_load_lds_dwordx4 %0, %1" :: "v"(voff), "s"(sbase), "s"(lds_dst) : "memory", "m0");
}
DI void glds4s(const void* sbase_, unsigned voff, unsigned lds_dst) {
  unsigned long long sv = (unsigned long long)sbase_;
  unsigned slo = __builtin_amdgcn_readfirstlane((unsigned)sv), shi = __builtin_amdgcn_readfirstlane((unsigned)(sv >> 32));
  unsigned long long sbase = ((unsigned long long)shi << 32) | slo;
  lds_dst = __builtin_amdgcn_readfirstlane(lds_dst);
  asm volatile("s_mov_b32 m0, %2\n\ts_nop 0\n\tglobal_load_lds_dword %0, %1" :: "v"(voff), "s"(sbase), "s"(lds_dst) : "memory", "m0");
}
template <int N> DI void wait_lgkm() { __builtin_amdgcn_s_waitcnt(0xC07F | (N << 8)); }
template <int N> DI void wait_vm() { asm volatile("s_waitcnt vmcnt(%0)" :: "n"(N) : "memory"); }
DI void rawb() { asm volatile("s_barrier" ::: "memory"); }
DI void ldsb() { asm volatile("s_waitcnt lgkmcnt(0)\n\ts_barrier" ::: "memory"); }

DI void mk_lo3(int w4, int l, unsigned (&lo3)[3]) {
#pragma unroll
  for (int j = 0; j < 3; ++j) {
    int r3 = (w4 + j) % 3;
    int idx = 64 * r3 + l;
    int t = idx / 6, c = idx - 6 * t;
    int r = (t & ~12) | ((t & 4) << 1) | ((t & 8) >> 1);
    lo3[j] = r * 1152 + c * 16;
  }
}
DI void dma_w(const char* wsrc, unsigned lds_dst, int P, unsigned lo) {
  const int img = P / 18, q = P - 18 * img;
  glds16s<0>(wsrc + img * 96 + (q / 3) * 36864, lo, lds_dst + P * 1024);
}
DI void mk_lq3(int w4, int l, unsigned (&lq3)[3]) {
#pragma unroll
  for (int j = 0; j < 3; ++j) {
    const int P = w4 + 4 * j, m = (P * 43) >> 7;
    const int idx = 64 * (P - 3 * m) + l;
    const int r = (idx * 171) >> 12, pc = idx - 24 * r;
    lq3[j] = (16 * (m & 1) + 2 * (m & 2) + 8 * (r >> 2) + (r & 3)) * 1152 + (pc ^ (r & 6)) * 16;
    OPQ(lq3[j]);
  }
}
DI int p18(int w4, int i) { int P = w4 + 4 * i; return P >= 18 ? P - 3 : P; }

#define FSTAMP(k)
#define STAMP(k)
#define PPARM
#define PARG

#define TSDECL
#define TS(k)
#define TSOUT(n)
#define BWDECL
#define LDSB_X(vm) do { if (vm < 63) wait_vm<vm>(); ldsb(); } while (0)
#define LDSB_Y(vm) do { if (vm < 63) wait_vm<vm>(); ldsb(); } while (0)
#define PBW_X asm volatile("s_waitcnt vmcnt(0) lgkmcnt(%0)\n\ts_barrier" :: "n"(2 * (PBAR - 2)) : "memory")
#define PBW_Y PBW_X
#define BWOUT()
struct Geo { int A, Bc, R0, C0, or_, oc_, pa, pb; };

DI unsigned chunk_rows(int c, int or_) {
  const unsigned r3 = 0x0706u + 0x0101u * or_ + (or_ == 1 ? 0x0F200000u : 0x01000000u);
  const unsigned r4 = or_ == 1 ? 0x13121110u : (or_ == 3 ? 0x13121102u : 0x33040302u);
  unsigned r = c < 3 ? 0x09080100u + 0x01010101u * (or_ + 2 * c) : (c == 3 ? r3 : r4);
  SOPQ(r);
  return r;
}
DI void x_lane(unsigned (&xc)[3], unsigned (&xs)[3], int p, int l) {
#pragma unroll
  for (int i = 0; i < 3; ++i) {
    const int t = 8 * (i < 2 ? p + 4 * i : 8 + (p & 1)) + (l >> 3);
    const int rr = i < 2 ? (t * 205) >> 12 : 3;
    xc[i] = (t - 20 * rr) * 768 + 16 * ((l & 7) ^ ((t + 3 * rr) & 7));
    xs[i] = 8 * rr;
  }
}
DI void x_read(const char* smem, int slot, int t, int g, u32x4 (&xf)[6]) {
  int ae = slot + t * 128 + 16 * (g ^ ((t + 3 * ((t * 205) >> 12)) & 7));
  int ao = ae ^ 64;
#pragma unroll
  for (int s = 0; s < 6; ++s) xf[s] = *(const u32x4*)(smem + ((s & 1) ? ao : ae) + XPL * (s >> 1));
}

template <int NT, int D, int K0, int NX, class F>
DI void projN(const char* smem, int wb, const u32x4 (&xf)[NX][6], unsigned (&out)[NX][6], F hook) {
  u32x4 wf[D];
  f32x4 ac[3][NT];
#define PC(t) (NT == 1 ? (t) % 3 : (t) / 6)
#define PS(t) (NT == 1 ? (t) / 3 : (t) % 6)
#define PADDR(t) (wb + PS(t) * 3072 + PC(t) * 32)
#define PPACK(c, k) do { out[K0 + (k)][2 * (c)] = pack2(ac[c][k][0], ac[c][k][1]); out[K0 + (k)][2 * (c) + 1] = pack2(ac[c][k][2], ac[c][k][3]); \
                         OPQ(out[K0 + (k)][2 * (c)]); OPQ(out[K0 + (k)][2 * (c) + 1]); } while (0)
#pragma unroll
  for (int t = 0; t < D - 1; ++t) wf[t] = tr_frag(smem, PADDR(t), 768);
  SB;
  hook(-1);
  SB;
#pragma unroll
  for (int t = 0; t < 18; ++t) {
    const int c = PC(t), s = PS(t);
#pragma unroll
    for (int k = 0; k < NT; ++k) {
      ac[c][k] = mfma(wf[t % D], xf[K0 + k][s], s == 0 ? (f32x4){0.f, 0.f, 0.f, 0.f} : ac[c][k]);
      SB;
      if (t + D - 1 < 18) {
        if (k == 0) tr_lo(wf[(t + D - 1) % D], smem, PADDR(t + D - 1));
        if (k == (NT > 1 ? 1 : 0)) tr_hi(wf[(t + D - 1) % D], smem, PADDR(t + D - 1) + 768);
      }
      if (NT > 1 && k == NT - 1 && c >= 1 && s >= 1 && s <= NT) PPACK(c >= 1 ? c - 1 : 0, s >= 1 && s <= NT ? s - 1 : 0);
      SB;
    }
    hook(t);
    SB;
  }
#pragma unroll
  for (int c = (NT == 1 ? 0 : 2); c < 3; ++c)
#pragma unroll
    for (int k = 0; k < NT; ++k) PPACK(c, k);
  SB;
  hook(18);
  SB;
#undef PC
#undef PS
#undef PADDR
#undef PPACK
}

DI void aug_k(int tc, int g, unsigned& a0, unsigned& a1) {
  const unsigned m = ((~((0x7Fu << (tc & 31)) >> 6) & 0x3FFFu) | ((tc & 32) << 9)) >> (4 * g);
  a0 = ((m & 1) | ((m & 2) << 15)) * 0x3F80u;
  a1 = (((m >> 2) & 1) | ((m & 8) << 13)) * 0x3F80u;
}
DI void kv_addr(unsigned e, int c, int g, int& ka, int& va) {
  const int n = 20 * (int)(e & 31) + c;
  ka = KOFF + 128 * n + 16 * (g ^ (n & 7));
  va = VOFF + 96 * n + 8 * (g ^ ((c >> 2) & 3));
}

DI void x_addr(unsigned (&vo)[4], const unsigned (&xc)[3], const unsigned (&xs)[3], int p, unsigned rows) {
#pragma unroll
  for (int i = 0; i < 3; ++i) vo[i] = xc[i] + __builtin_amdgcn_ubfe(rows, xs[i], 8) * ROWB;
  unsigned f = vo[2], m = p < 2 ? ~0u : 0u;
  SOPQ(f); SOPQ(m);
  vo[3] = (vo[2] & m) | (f & ~m);
  OPQ(vo[0]); OPQ(vo[1]); OPQ(vo[2]); OPQ(vo[3]);
}
DI void dma_x(const char* xs, const unsigned (&vo)[4], unsigned lds_base, unsigned slot, int p, int i, bool late = false) {
  const int v = i < 6 ? i % 3 : (p < 2 ? i - 6 : 2);
  const int j = i < 3 ? p : (i < 6 ? p + 4 : 8 + (p & 1));
  glds16s<0>(xs + 128 * v, vo[i < 7 ? i / 3 : 3], lds_base + (i == 7 && p >= 2 ? PFD + 1024 : slot + v * XPL + j * 1024));
}

DI void mk_tq(const char* smem, int raw, int tid, unsigned (&tq)[4][2]) {
  const int r = min(tid, 223);
  const int seg = (r >= 55) + (r >= 110) + (r >= 167);
  const int m = seg ^ (seg >> 1);
  const int o = r - (int)((0x71A93802u >> (8 * m)) & 0xFF);
  unsigned tv[4][4];
  bool ok[4];
#pragma unroll
  for (int i = 0; i < 4; ++i) {
    const int lin = 4 * o + m + i;
    const int row = lin >> 4, dc = lin & 15;
    ok[i] = row >= 0 && row <= 12 && dc <= 12;
    const int src = raw + 2 * (clampi(row, 0, 12) * 13 + min(dc, 12));
#pragma unroll
    for (int hh = 0; hh < 4; ++hh) tv[hh][i] = *(const uint16_t*)(smem + src + hh * 338);
  }
  SB;
#pragma unroll
  for (int i = 0; i < 4; ++i)
#pragma unroll
    for (int hh = 0; hh < 4; ++hh) OPQ(tv[hh][i]);
  const unsigned m0 = (ok[0] ? 0xFFFFu : 0u) | (ok[1] ? 0xFFFF0000u : 0u), m1 = (ok[2] ? 0xFFFFu : 0u) | (ok[3] ? 0xFFFF0000u : 0u);
#pragma unroll
  for (int hh = 0; hh < 4; ++hh) {
    tq[hh][0] = (tv[hh][0] | (tv[hh][1] << 16)) & m0; tq[hh][1] = (tv[hh][2] | (tv[hh][3] << 16)) & m1;
    OPQ(tq[hh][0]); OPQ(tq[hh][1]);
  }
}

template <int KV, int LAST, class B>
DI void proj_phase(char* smem, int wb, int wbn, const u32x4 (&xf)[6][6], const int (&ka)[6], const int (&va)[6],
                   u32x4 (&wf)[3], f32x4 (&ac)[2][6], const char* dsrc, unsigned dstr, unsigned ddst, int p, const unsigned (&lo3)[3], B bar) {
  constexpr int D = 3;
  unsigned hold[6][2];
#define PADR(t) (((t) < 18 ? wb : wbn) + ((t) % 6) * 3072 + (((t) % 18) / 6) * 32)
#pragma unroll
  for (int t = 0; t < 18 + LAST; ++t) {
    const int c = t / 6, s = t % 6, ab = (c + KV) & 1;
#pragma unroll
    for (int k = 0; k < 6; ++k) {
      if (t < 18) ac[ab][k] = mfma(wf[t % D], xf[k][s], s == 0 ? (f32x4){0.f, 0.f, 0.f, 0.f} : ac[ab][k]);
      if (t + 2 < (LAST ? 18 : 20) && (k == 0 || k == 2)) {
        SB;
        if (k == 0) tr_lo(wf[(t + 2) % D], smem, PADR(t + 2));
        else tr_hi(wf[(t + 2) % D], smem, PADR(t + 2) + 768);
        SB;
      }
      if (t == 18 || (s < 3 && (k == 1 || k == 4))) {
        const int kb = t == 18 ? k : 2 * s + (k == 4 ? 1 : 0);
        const int pc = (c + 2) % 3, kv = c == 0 ? 1 - KV : KV;
        unsigned p0 = pack2(ac[ab ^ 1][kb][0], ac[ab ^ 1][kb][1]), p1 = pack2(ac[ab ^ 1][kb][2], ac[ab ^ 1][kb][3]);
        if (kv == 0) {
          u32x2 pk = {p0, p1};
          *(u32x2*)(smem + va[kb] + 32 * pc) = pk;
        } else if (pc == 0) {
          hold[kb][0] = p0; hold[kb][1] = p1;
        } else if (pc == 1) {
          u32x4 pk = {hold[kb][0], hold[kb][1], p0, p1};
          *(u32x4*)(smem + ka[kb]) = pk;
        } else {
          u32x2 pk = {p0, p1};
          *(u32x2*)(smem + (ka[kb] ^ 64)) = pk;
        }
        SB;
      }
    }
    SB;
    if (t == PBAR || t == 15) bar(t);
    if (t > PBAR && t <= PBAR + 10 && ((t - PBAR) & 1)) {
      const int i = (t - PBAR) / 2, P = p18(p, i);
      glds16s<0>(dsrc + (P / 3) * dstr, lo3[i % 3], ddst + P * 1024);
    }
    SB;
  }
#undef PADR
}

template <bool SAFE>
DI bool proj_wave(char* smem, unsigned lds_base, const char* xb, const uint16_t* __restrict__ wqkv,
                  const uint16_t* __restrict__ wo, const uint16_t* __restrict__ rpb, const Geo& G, int p, int l PPARM) {
  const int u = l & 15, g = l >> 4;
  TSDECL;
  const char* xq = xb + (size_t)G.or_ * ROWB;
  unsigned vo[3][4], lo3[3];
  int ka[6], va[6];
  unsigned ag[6][2];
  auto kv_setup = [&]() __attribute__((always_inline)) {
    const int t = 16 * p + u, rr = (t * 205) >> 12, c = t - 20 * rr;
    unsigned a0, a1;
    aug_k(c, g, a0, a1);
    const unsigned fl = g == 3 ? 0x3F80u : 0u;
#pragma unroll
    for (int k = 0; k < 6; ++k) {
      if (k == 4) {
        const unsigned e = chunk_rows(p, G.or_) >> 24;
        kv_addr(e, 4 + u, g, ka[k], va[k]);
        aug_k(4 + u + (e & 32), g, ag[k][0], ag[k][1]);
      } else {
        const unsigned e = __builtin_amdgcn_ubfe(chunk_rows(k < 4 ? k : 4, G.or_), 8 * rr, 8);
        kv_addr(e, c, g, ka[k], va[k]);
        ag[k][0] = a0;
        ag[k][1] = k < 3 ? a1 : (a1 | ((e & 32) ? fl : 0u));
      }
      OPQ(ka[k]);
      if (k >= 3) OPQ(ag[k][1]);
    }
    OPQ(ag[4][0]);
  };
  __builtin_amdgcn_s_setprio(3);
  unsigned xc[3], xs[3];
  x_lane(xc, xs, p, l);
  x_addr(vo[0], xc, xs, p, 0x09080100u);
  glds16s<0>(rpb, 16 * l, lds_base + RAWB + 1536 * p);
  glds4s(rpb, 1024 + 4 * l, lds_base + RAWB + 1536 * p + 1024);
  glds4s(rpb, min(1280 + 4 * l, 1348), lds_base + RAWB + 1536 * p + 1280);
  TS(0);
#pragma unroll
  for (int i = 0; i < 8; ++i) dma_x(xq + 2 * ROWB, vo[0], lds_base, SLO, p, i);
  {
    const int line = ((blockIdx.x >> 3) & 31) * 72 + 18 * p + min(l, 17);
    const bool lw = line < 1728;
    const unsigned long long dw = (unsigned long long)wo - (unsigned long long)wqkv - 1728ull * 128ull;
    const char* pa = (const char*)wqkv + line * 128 + (lw ? 0ull : dw);
    asm volatile("s_mov_b32 m0, %1\n\ts_nop 0\n\tglobal_load_lds_dword %0, off"
                 :: "v"(pa), "s"(__builtin_amdgcn_readfirstlane(lds_base + PFD + 256 * p)) : "memory", "m0");
  }
  TS(1);
  __builtin_amdgcn_s_setprio(0);
  mk_lo3(p, l, lo3);
#pragma unroll
  for (int j = 0; j < 3; ++j) OPQ(lo3[j]);
  const int wlane = (16 * (g >> 1) + 4 * (g & 1) + (u >> 2)) * 96 + 8 * (u & 3);
  int wbk = WKOFF + wlane;
  OPQ(wbk);
  x_addr(vo[1], xc, xs, p, 0x0706u + 0x0101u * G.or_ + (G.or_ == 1 ? 0x0F010000u : 0x01000000u));
  u32x4 xf[6][6];
  unsigned k0[6][6];
  unsigned tq[4][2];
  const int tp = 16 * p + u, te = 64 + u, tid = 64 * p + l;
  x_addr(vo[2], xc, xs, p, G.or_ == 1 ? 0x13121110u : (G.or_ == 3 ? 0x13121102u : 0x12040302u));
  kv_setup();
  SB;
  wait_vm<9>();
  mk_tq(smem, RAWB + 1536 * p, tid, tq);
  SB;
  TS(2);
#pragma unroll
  for (int i = 0; i < 5; ++i) dma_w((const char*)wqkv + 384, lds_base + WKOFF, p18(p, i), lo3[i % 3]);
  TS(3);
  __builtin_amdgcn_s_setprio(3);
  wait_vm<5>(); rawb();
  TS(4);
  x_read(smem, SLE, tp, g, xf[0]);
  x_read(smem, SLO, tp, g, xf[1]);
  x_read(smem, (p & 1) ? SLO : SLE, te, g, xf[4]);
  wait_vm<0>(); ldsb();
  TS(5);
  __builtin_amdgcn_s_setprio(QPRIO);
  projN<2, 6, 0>(smem, wbk, xf, k0, LAM {
    if (t < 0) {
#pragma unroll
      for (int i = 0; i < 4; ++i) dma_x(xq + 4 * ROWB, vo[0], lds_base, SLE, p, i, true);
    }
    else if (t < 4) dma_x(xq + 4 * ROWB, vo[0], lds_base, SLE, p, t + 4, true);
    else if (t < 12) dma_x(xb, vo[1], lds_base, SLO, p, t - 4, true);
    if (t == 11) TS(6);
  });
  TS(7);
  TS(8);
  __builtin_amdgcn_s_setprio(3);
  wait_vm<0>();
  rawb();
  TS(9);
  x_read(smem, SLE, tp, g, xf[2]);
  x_read(smem, SLO, tp, g, xf[3]);
  if (p >= 2) x_read(smem, (p & 1) ? SLO : SLE, te, g, xf[4]);
  auto k_write = [&](int k) __attribute__((always_inline)) {
    u32x4 pa = {k0[k][0], k0[k][1], k0[k][2], k0[k][3]};
    u32x4 pb = {k0[k][4], k0[k][5], ag[k][0], ag[k][1]};
    *(u32x4*)(smem + ka[k]) = pa;
    *(u32x4*)(smem + (ka[k] ^ 64)) = pb;
  };
  projN<2, 6, 2>(smem, wbk, xf, k0, LAM {
    if (t < 0) {
      ldsb();
      TS(10);
#pragma unroll
      for (int i = 0; i < 4; ++i) dma_x(xb, vo[2], lds_base, SLE, p, i, true);
      __builtin_amdgcn_s_setprio(QPRIO);
    }
    else if (t < 4) dma_x(xb, vo[2], lds_base, SLE, p, t + 4, true);
    else if (t < 9) dma_w((const char*)wqkv + 768, lds_base + WVOFF, p18(p, t - 4), lo3[(t - 4) % 3]);
  });
  projN<2, 6, 4>(smem, wbk, xf, k0, LAM {
    if (t < 0) {
      TS(11);
      __builtin_amdgcn_s_setprio(EPRIO);
      wait_vm<5>(); rawb();
      STAMP(5);
      x_read(smem, SLE, tp, g, xf[5]);
      ldsb();
    }
    else if (t % 4 == 0 && t < 16) k_write(t / 4);
    else if (t == 18) { k_write(4); k_write(5); }
  });
  f32x4 tf[4];
#pragma unroll
  for (int hh = 0; hh < 4; ++hh) {
    tf[hh] = (f32x4){__uint_as_float(tq[hh][0] << 16) * LOG2E, __uint_as_float(tq[hh][0] & 0xFFFF0000u) * LOG2E,
                     __uint_as_float(tq[hh][1] << 16) * LOG2E, __uint_as_float(tq[hh][1] & 0xFFFF0000u) * LOG2E};
    OPQ(tf[hh]);
  }
  {
    const int t = 16 * p + u, c = t - 20 * ((t * 205) >> 12);
#pragma unroll
    for (int k = 0; k < 6; ++k) {
      va[k] = VOFF + 96 * ((ka[k] - KOFF) >> 7) + 8 * (g ^ (((k == 4 ? 4 + u : c) >> 2) & 3));
      OPQ(va[k]);
    }
  }
  SB;
  STAMP(4);
  wait_vm<0>(); rawb();
  STAMP(6);
  if (tid < 224) {
#pragma unroll
    for (int hh = 0; hh < 4; ++hh) *(f32x4*)(smem + TOFF + hh * T_HSTR + 16 * tid) = tf[hh];
  } else if (tid < 226) {
    const float nb = __uint_as_float(NEGBF << 16);
    *(f32x4*)(smem + NEGOFF + 448 * (tid - 224)) = (f32x4){nb, nb, nb, nb};
  }
  SB;
  STAMP(7);
  ldsb();
#pragma unroll
  for (int i = 0; i < 5; ++i) dma_w((const char*)wqkv + 384 + 96, lds_base + WB1, p18(p, i), lo3[i % 3]);
  TSOUT(12);
  __builtin_amdgcn_s_setprio(PPRIO);
  u32x4 wf[3];
  f32x4 ac[2][6];
#pragma unroll
  for (int k = 0; k < 6; ++k)
    ac[1][k] = (f32x4){__uint_as_float(k0[k][4] << 16), __uint_as_float(k0[k][4] & 0xFFFF0000u),
                       __uint_as_float(k0[k][5] << 16), __uint_as_float(k0[k][5] & 0xFFFF0000u)};
  int wa = WVOFF + wlane, wb = WB1 + wlane, wc = WKOFF + wlane;
  unsigned da = lds_base + WKOFF, db = lds_base + WVOFF, dc = lds_base + WB1;
  wf[0] = tr_frag(smem, wa, 768);
  wf[1] = tr_frag(smem, wa + 3072, 768);
  BWDECL;
  int h = 0;
  auto barY = [&](int t) __attribute__((always_inline)) {
    if (t == PBAR) { if (h != 0) PBW_Y; }
    else if (h == 0) { wait_vm<5>(); rawb(); }
  };
  auto barX = [&](int t) __attribute__((always_inline)) { if (t == PBAR) PBW_X; };
  unsigned dstr = 36864;
  const char* dsk = (const char*)wqkv + 384 + 192;
#pragma unroll 1
  for (; h < 3; ++h) {
    OPQ(wa); OPQ(wb); OPQ(wc);
    STAMP(8 + 4 * h);
    proj_phase<0, 0>(smem, wa, wb, xf, ka, va, wf, ac, (const char*)wqkv + 768 + 96 * (h + 1), 36864, da, p, lo3, barY);
    STAMP(10 + 4 * h);
    if (h == 2) {
#pragma unroll
      for (int j = 0; j < 3; ++j) {
        int idx = 64 * ((p + j) % 3) + l;
        int row = idx / 24, pc = idx - 24 * row;
        lo3[j] = row * 384 + ((pc >> 1) ^ ((row >> 1) & 3)) * 32 + (pc & 1) * 16;
      }
      dstr = 3072;
      dsk = (const char*)wo;
    }
    proj_phase<1, 0>(smem, wb, wc, xf, ka, va, wf, ac, dsk, dstr, db, p, lo3, barX);
    dsk += 96;
    const int wt = wa; wa = wc; wc = wb; wb = wt;
    const unsigned dt = da; da = dc; dc = db; db = dt;
  }
  STAMP(20);
  proj_phase<0, 1>(smem, wa, wa, xf, ka, va, wf, ac, (const char*)wo + 18432, 3072, lds_base + WKOFF, p, lo3, barY);
  STAMP(21);
  LDSB_X(63);
  BWOUT();
  STAMP(22);
#pragma unroll
  for (int t = 0; t < 9; ++t) { int P = p + 4 * t; glds16s<0>((const char*)wo + 36864 + (P / 3) * 3072, lo3[t % 3], lds_base + WOHI + P * 1024); }
  wait_vm<0>();
  STAMP(23);
  ldsb();
  if (SAFE) return false;
  rawb();
  const u32x4 fl = *(const u32x4*)(smem + FLAGOFF);
  return __builtin_amdgcn_readfirstlane(fl[0] | fl[1] | fl[2] | fl[3]) != 0;
}

template <int NU> constexpr int g_cnt(int s) { return (s < NU - 1 ? s : NU - 1) - (s > 3 ? s - 3 : 0) + 1; }
template <int NU> constexpr int g_first(int s) { int c = 0; for (int i = 0; i < s; ++i) c += g_cnt<NU>(i); return c; }
template <int NU> constexpr int g_step(int j) { int s = 0; while (g_first<NU>(s + 1) <= j) ++s; return s; }
template <int NU> constexpr int g_unit(int j) { int s = g_step<NU>(j); return (s > 3 ? s - 3 : 0) + j - g_first<NU>(s); }
template <int NU> constexpr int g_idx(int k, int t) { return g_first<NU>(k + t) + k - (k + t > 3 ? k + t - 3 : 0); }

template <int NU> constexpr bool q_dbl(int t) { return NU == 4 || t < 72; }
template <int NU> constexpr int q_c(int t) { return q_dbl<NU>(t) ? (t % 72) / 6 : (t >= 156 ? 2 : 2 * ((t % 72) / 12) + (t & 1)); }
template <int NU> constexpr int q_s(int t) { return q_dbl<NU>(t) ? t % 6 : (t >= 156 ? t - 156 : (t % 12) / 2); }
template <int NU> constexpr int q_gi(int t) { return q_dbl<NU>(t) ? t / 6 : t / 12; }
template <int NU> constexpr int q_off(int t) { return NU == 3 && t >= 144 ? q_s<NU>(t) * 3072 + q_c<NU>(t) * 32 : (q_s<NU>(t) % 3) * 12288 + (q_c<NU>(t) >> 2) * 128; }
#define XBJ (NG - 4)
#define QD 6
#define L23S 50
#define XSP 2

template <int NU, bool SAFE>
DI bool attn_wave(char* smem, unsigned lds_base, const char* xb, const uint16_t* __restrict__ wqkv,
                  uint16_t* __restrict__ out, const Geo& G, int a, int l PPARM) {
  const int u = l & 15, g = l >> 4;
  const int j_ = a & 1, ib = a >> 1;
  TSDECL;
  __builtin_amdgcn_s_setprio(3);
  unsigned lq3[3];
  {
    unsigned vo[4], xc[3], xs[3];
    x_lane(xc, xs, a, l);
    x_addr(vo, xc, xs, a, 0x09080100u);
#pragma unroll
    for (int i = 0; i < 8; ++i) dma_x(xb + (size_t)G.or_ * ROWB, vo, lds_base, SLE, a, i);
  }
  __builtin_amdgcn_s_setprio(1);
  mk_lq3(a, l, lq3);
#define DMA_Q(i0, i1) do { SB; _Pragma("unroll") for (int i = i0; i < i1; ++i) \
    glds16s<0>((const char*)wqkv + (i / 3) * 36864, lq3[i % 3], lds_base + WQ + a * 1024 + i * 4096); SB; } while (0)
  DMA_Q(0, 6);
  const int uq = min(u, 13);
  const int qa = uq >= 7 ? 1 : 0;
  const int qb = uq - 7 * qa;
  const int tu = 20 * (2 * ib + qa) + G.oc_ + 7 * j_ + qb;
  int xa = tu * 128 + 16 * (g ^ ((tu + 3 * (2 * ib + qa)) & 7)), xo = xa ^ 64;
  OPQ(xa); OPQ(xo);
#define X_FRAG(slot, s) (*(const u32x4*)(smem + (((s) & 1) ? xo : xa) + (slot) + XPL * ((s) >> 1)))
  const int b0 = G.Bc + 7 * j_;
  const int kb0 = clampi(b0 - 3, 0, 99), kc0 = kb0 - G.C0;
  const int wlane = (16 * (g >> 1) + 4 * (g & 1) + (u >> 2)) * 96 + 8 * (u & 3);
  int wbk = WKOFF + wlane;
  OPQ(wbk);
  DMA_Q(6, 8);
  int wq[2][4];
#pragma unroll
  for (int b = 0; b < 4; ++b) {
    const int rowl = 4 * g + (u >> 2);
    wq[0][b] = WQ + rowl * 384 + 32 * (b ^ ((rowl >> 1) & 3)) + 8 * (u & 3);
    wq[1][b] = wq[0][b] + 36864;
    OPQ(wq[0][b]); OPQ(wq[1][b]);
  }
  DMA_Q(8, 10);
  unsigned augB0, augB1;
  int baseK, baseV, baseC;
  {
    const int b = b0 + qb;
    const int sbrel = clampi(b - 3, 0, 105) - G.C0;
    unsigned v[4];
#pragma unroll
    for (int jj = 0; jj < 4; ++jj) v[jj] = (sbrel == 4 * g + jj || 4 * g + jj == 14) ? NEGBF : 0u;
    augB0 = v[0] | (v[1] << 16);
    augB1 = v[2] | (v[3] << 16);
    OPQ(augB0); OPQ(augB1);
    DMA_Q(10, 12);
    const int colK = kc0 + u <= 19 ? kc0 + u : kc0 + u - 16;
    baseK = KOFF + 128 * colK + 16 * (g ^ (colK & 7));
    const int cV = kc0 + min(4 * g + (u >> 2), 12);
    baseV = VOFF + 96 * cV + 8 * ((u & 3) ^ ((cV >> 2) & 3));
    const int beta_c = kb0 - b + 6;
    const int m = beta_c & 3;
    const int om = (0x71A93802u >> (8 * m)) & 0xFF;
    baseC = TOFF + 16 * om + 4 * (beta_c - m) + 16 * g - 64 * qa + 192;
    OPQ(baseC);
  }
  const bool edge = G.or_ == (NU == 4 ? 1 : 5);
  const int pb0 = ((G.or_ - 3) >> 1) + (NU == 4 ? 0 : 4), pbs = edge ? (NU == 4 ? 3 : 1) : pb0;
  int aKa = baseK + 5120 * pb0, aKs = baseK + 5120 * pbs, aV = baseV + 3840 * pb0, aVs = baseV + 3840 * pbs;
  OPQ(aKa); OPQ(aKs); OPQ(aV); OPQ(aVs);
  int negv = NEGOFF;
  OPQ(negv);
  DMA_Q(12, 14);
  int kae = 0, vae = 0;
  unsigned ae0 = 0, ae1 = 0;
  if (NU == 3) {
    const unsigned e = chunk_rows(4, G.or_) >> 24;
    kv_addr(e, 4 + u, g, kae, vae);
    aug_k(4 + u + (e & 32), g, ae0, ae1);
    OPQ(kae); OPQ(vae); OPQ(ae0); OPQ(ae1);
  }
  DMA_Q(14, 18);
  __builtin_amdgcn_s_setprio(2);
  STAMP(1);
  u32x4 xu[NU][6];
  TS(6);
  wait_vm<18>(); rawb();
  TS(7);
#pragma unroll
  for (int s = 0; s < 6; ++s) { xu[0][s] = X_FRAG(SLE, s); xu[1][s] = X_FRAG(SLO, s); }
  unsigned Q[NU][24];
  u32x4 xe[1][6];
  unsigned ke[6];
  float oi[12];
  SB;
  TS(1);
  wait_vm<0>(); ldsb();
  STAMP(2);
  TS(2);
  {
    constexpr int D = QD, NS = NU == 4 ? 144 : 162;
    u32x4 wf[D];
    f32x4 ac[2][2];
    sfor<0, D - 1>(SLAM(tc_) { constexpr int t = decltype(tc_)::v; wf[t] = tr_frag(smem, wq[q_s<NU>(t) / 3][q_c<NU>(t) & 3] + q_off<NU>(t), 6144); });
    SB;
    sfor<0, NS + 2>(SLAM(tc_) {
      constexpr int t = decltype(tc_)::v, wi = t / 72, t1 = t % 72;
      constexpr bool dbl = q_dbl<NU>(t), kb = NU == 3 && wi == 2;
      constexpr int s = t < NS ? q_s<NU>(t) : 0, gi = q_gi<NU>(t), gs = dbl ? t % 6 : t % 12;
      constexpr int tn = t + D - 1;
      const f32x4 z = {0.f, 0.f, 0.f, 0.f};
      if constexpr (t < NS) {
        constexpr int ch = dbl || (kb && t1 >= 12) ? 0 : (t & 1);
        constexpr bool kn = NU == 3 && tn >= 144;
        const int an = (kn ? wbk : wq[(q_s<NU>(tn) / 3) & 1][q_c<NU>(tn) & 3]) + q_off<NU>(tn < NS ? tn : 0);
        if constexpr (kb) ac[gi & 1][ch] = mfma(wf[t % D], xe[0][s], s == 0 ? z : ac[gi & 1][ch]);
        else ac[gi & 1][ch] = mfma(wf[t % D], xu[2 * wi][s], s == 0 ? z : ac[gi & 1][ch]);
        SB;
        if (tn < NS) tr_lo(wf[tn % D], smem, an);
        SB;
        if constexpr (dbl) ac[gi & 1][1] = mfma(wf[t % D], xu[2 * wi + 1][s], s == 0 ? z : ac[gi & 1][1]);
        SB;
        if (tn < NS) tr_hi(wf[tn % D], smem, an + (kn ? 768 : 6144));
      }
      constexpr int tp_ = t < NS ? t - gs - 1 : NS - 1;
      sfor<0, 4>(SLAM(ec_) {
        constexpr int e = decltype(ec_)::v, k = e >> 1;
        if constexpr (t < NS ? (tp_ >= 0 && gs == 1 + e) : (t - NS == e / 2)) {
          constexpr int pg = q_gi<NU>(tp_), pw = tp_ / 72;
          const float a0 = ac[pg & 1][k][2 * (e & 1)], a1 = ac[pg & 1][k][2 * (e & 1) + 1];
          if constexpr (NU == 3 && pw == 2) {
            constexpr int o = (tp_ >= 156 ? 4 : 2 * k) + (e & 1);
            if (!(tp_ >= 156 && k == 1)) { ke[o] = pack2(a0, a1); OPQ(ke[o]); }
          } else {
            constexpr int uu = 2 * pw + (q_dbl<NU>(tp_) ? k : 0), cb = q_dbl<NU>(tp_) ? q_c<NU>(tp_) : q_c<NU>(tp_) - 1 + k;
            constexpr int o = 2 * cb + (e & 1);
            if constexpr (uu == NU - 1 && o >= 18) {
              oi[2 * (o - 18)] = a0; oi[2 * (o - 18) + 1] = a1;
              OPQ(oi[2 * (o - 18)]); OPQ(oi[2 * (o - 18) + 1]);
            } else {
              Q[uu][o] = pack2(a0 * C1, a1 * C1);
              OPQ(Q[uu][o]);
            }
          }
        }
      });
      SB;
      if (t == 0) TS(0);
      if (t == 30) TS(3);
      if (t == L23S) TS(4);
      if (t == L23S + 6 * XSP - 1 + D) TS(5);
      if (t == 72 + (NU == 4 ? L4S : L4S3)) TS(8);
      if (t == 72 + (NU == 4 ? L4S + 3 : L4S3 + D)) TS(9);
      if (t == 72 + (NU == 4 ? L4S + 4 : L4S3 + D + 1)) TS(10);
      if (t == NS + 1) TS(11);
      if (t == L23S) {
        rawb();
        STAMP(3);
      }
      if (t >= L23S && t < L23S + 6 * XSP) {
        constexpr int i = (t - L23S) / XSP;
        if ((t - L23S) % XSP == 0) xu[2][i] = X_FRAG(SLE, i);
        if (NU == 4 && (t - L23S) % XSP == XSP - 1) xu[NU - 1][i] = X_FRAG(SLO, i);
        SB;
      }
      if (t == L23S + 6 * XSP - 1 + D) { rawb(); SB; }
      if (t == 72 + (NU == 4 ? L4S : L4S3)) {
        rawb();
        STAMP(5);
        if (NU == 3) x_read(smem, SLE, 64 + u, g, xe[0]);
        SB;
      }
      if (t == 72 + (NU == 4 ? L4S + 3 : L4S3 + D)) { rawb(); SB; }
    });
  }
  if (NU == 3) {
    u32x4 pa = {ke[0], ke[1], ke[2], ke[3]};
    u32x4 pb = {ke[4], ke[5], ae0, ae1};
    *(u32x4*)(smem + kae) = pa;
    *(u32x4*)(smem + (kae ^ 64)) = pb;
  }
  SB;
  rawb();
  STAMP(6);
  u32x4 Qa[NU], Qb[NU];
  unsigned R[NU][18];
#pragma unroll
  for (int k = 0; k < NU; ++k) {
    Qa[k] = (u32x4){Q[k][0], Q[k][1], Q[k][2], Q[k][3]};
    Qb[k] = (u32x4){Q[k][4], Q[k][5], augB0, augB1};
#pragma unroll
    for (int e = 0; e < 18; ++e) R[k][e] = (k == NU - 1 && e >= 12) ? 0u : Q[k][6 + e];
  }
  STAMP(7);
  TSOUT(12);
  ldsb();
  __builtin_amdgcn_s_setprio(APRIO);

  constexpr int NG = 4 * NU, NS_ = NU + 3;
  const u32x4 ones = {0x3F803F80u, 0x3F803F80u, 0x3F803F80u, 0x3F803F80u};
  const f32x4 zero4 = {0.f, 0.f, 0.f, 0.f};
  u32x4 kf[2][4];
  f32x4 T[8];
  f32x4 S[NG][2];
  f32x4 o[NU][4];
  float inv, nt0, nt1;
  unsigned lmn = 0x3F800000u, lmx = 0x3F800000u;
#pragma unroll
  for (int db = 0; db < 3; ++db) o[NU - 1][db + 1] = (f32x4){oi[4 * db], oi[4 * db + 1], oi[4 * db + 2], oi[4 * db + 3]};
  o[NU - 1][0] = (f32x4){1.0f / C1, 0.f, 0.f, 0.f};
  int aCm, aC0, aC7, aCx, aCy, aCz;
  int hoff = 0;
#define C_ADDR() do { aCm = baseC + hoff; aC0 = qa ? negv : aCm; aC7 = qa ? aCm : negv; \
    aCy = aCm + (edge ? (NU == 4 ? 512 : -512) : 0); aCx = edge ? aCy : (NU == 4 ? aC0 : aC7); aCz = edge ? aCm : (NU == 4 ? aC7 : aC0); } while (0)
#define C_OWN(k, r) ((r) == 0 || (r) == 7 || (NU == 4 && (k) == 0 && (r) == 1) || (NU == 3 && (k) == 2 && (r) == 6))
#define C_SEL(k, r) (NU == 4 ? ((r) == 0 ? ((k) == 0 ? aCx : aC0) : ((r) == 7 ? ((k) < 2 ? aCz : aC7) : ((k) == 0 && (r) == 1 ? aCy : aCm))) \
                             : ((r) == 0 ? ((k) == 0 ? aC0 : aCz) : ((r) == 7 ? ((k) == 2 ? aCx : aC7) : ((k) == 2 && (r) == 6 ? aCy : aCm))))
#define LD_D(k, r) S[g_idx<NU>(k, (r) / 2)][(r) & 1] = *(const f32x4*)(smem + C_SEL(k, r) + 64 * (r))
#define LD_T(r) T[r] = *(const f32x4*)(smem + aCm + 64 * (r))
#define LD_K(s, i) do { const int ab_ = (s) == (NU == 4 ? 0 : 5) ? aKs : aKa; \
    kf[(s) & 1][i] = *(const u32x4*)(smem + ((((i) >> 1) ^ ((i) & 1)) ? ab_ ^ 64 : ab_) + 5120 * (s) + 2560 * ((i) & 1)); } while (0)
#define LD_V(s, db) vf[(s) & 1][db] = tr_frag(smem, ((s) == (NU == 4 ? 0 : 5) ? aVs : aV) + 3840 * (s) + 32 * (db), 1920)
#define X_PRE() do { LD_D(0, 0); if (NU == 4) LD_D(0, 1); else LD_T(1); LD_K(0, 0); LD_K(0, 1); LD_K(0, 2); LD_K(0, 3); \
    LD_T(2); LD_T(3); LD_K(1, 0); LD_K(1, 1); LD_K(1, 2); LD_K(1, 3); LD_D(1, 0); if (NU == 4) LD_T(1); } while (0)
#define NORM_A(k, i) do { \
      if ((i) == 0) { const float l_ = o[k][0][0]; inv = __builtin_amdgcn_rcpf(l_); OPQ(inv); } \
      else { const f32x4 ov_ = o[k][i]; nt0 = ov_[0] * inv; nt1 = ov_[1] * inv; OPQ(nt0); OPQ(nt1); } } while (0)
#define NORM_B(k, i) do { \
      if ((i) == 0) { const float l_ = o[k][0][0]; \
        if (!SAFE) { lmn = min(lmn, __float_as_uint(l_)); lmx = max(lmx, __float_as_uint(l_)); OPQ(lmn); OPQ(lmx); } } \
      else { const f32x4 ov_ = o[k][i]; \
        R[k][10 + 2 * (i)] = pack2(nt0, nt1); R[k][11 + 2 * (i)] = pack2(ov_[2] * inv, ov_[3] * inv); \
        OPQ(R[k][10 + 2 * (i)]); OPQ(R[k][11 + 2 * (i)]); } } while (0)
#define NORM_PART(k, i) do { NORM_A(k, i); NORM_B(k, i); } while (0)
#define ROT_A(k, i) do { \
      if ((i) == 0) { Qa[k][0] = vmov(R[k][0]); Qa[k][1] = vmov(R[k][1]); } \
      else if ((i) == 1) { Qb[k][0] = vmov(R[k][4]); Qb[k][1] = vmov(R[k][5]); } \
      else if ((i) == 2) { R[k][3] = vmov(R[k][9]); R[k][4] = vmov(R[k][10]); } \
      else { R[k][8] = vmov(R[k][14]); R[k][9] = vmov(R[k][15]); } } while (0)
#define ROT_B(k, i) do { \
      if ((i) == 0) { Qa[k][2] = vmov(R[k][2]); Qa[k][3] = vmov(R[k][3]); } \
      else if ((i) == 1) { R[k][0] = vmov(R[k][6]); R[k][1] = vmov(R[k][7]); R[k][2] = vmov(R[k][8]); } \
      else if ((i) == 2) { R[k][5] = vmov(R[k][11]); R[k][6] = vmov(R[k][12]); R[k][7] = vmov(R[k][13]); } \
      else { R[k][10] = vmov(R[k][16]); R[k][11] = vmov(R[k][17]); } } while (0)
  BWDECL;
  int wbv = WVOFF + wlane, wbc = wbk;
  wbk = WB1 + wlane;
  C_ADDR();
  if (!SAFE) X_PRE();
  SB;

#pragma unroll 1
  for (int h = 0; h < 4; ++h) {
    OPQ(aKa); OPQ(aKs); OPQ(aV); OPQ(aVs); OPQ(baseC);
    if (NU == 3) { OPQ(wbv); OPQ(wbk); OPQ(wbc); }
    unsigned zs = 0;
    asm volatile("" : "+s"(zs));
    STAMP(8 + 4 * h);
    u32x4 P[NG];
    u32x4 vf[2][3];
    if (NU == 3) {
      unsigned ve[1][6];
      PH;
      projN<1, 6, 0>(smem, wbv, xe, ve, LAM { if (t >= 2 && t < 10 && !(t & 1)) NORM_PART(NU - 1, (t - 2) / 2); });
#pragma unroll
      for (int db = 0; db < 3; ++db) { u32x2 pk = {ve[0][2 * db], ve[0][2 * db + 1]}; *(u32x2*)(smem + vae + 32 * db) = pk; }
      SB;
    }
    if constexpr (SAFE) {
      if (NU == 4) { NORM_PART(NU - 1, 0); NORM_PART(NU - 1, 1); NORM_PART(NU - 1, 2); NORM_PART(NU - 1, 3); }
      sfor<0, NU>(SLAM(kc_) {
        constexpr int k = decltype(kc_)::v;
        float pm = 0.f;
        sfor<0, 4>(SLAM(tc_) {
          constexpr int t = decltype(tc_)::v, j = g_idx<NU>(k, t);
          LD_D(k, 2 * t); LD_D(k, 2 * t + 1);
          LD_K(k + t, 0); LD_K(k + t, 1); LD_K(k + t, 2); LD_K(k + t, 3);
#pragma unroll
          for (int i = 0; i < 4; ++i) S[j][i & 1] = mfma(kf[(k + t) & 1][i], (i >> 1) ? Qb[k] : Qa[k], S[j][i & 1]);
#pragma unroll
          for (int i = 0; i < 2; ++i) {
            const float m2 = max2(max2(S[j][i][0], S[j][i][1]), max2(S[j][i][2], S[j][i][3]));
            pm = (t == 0 && i == 0) ? m2 : max2(pm, m2);
          }
          SB;
        });
        pm = xmax32(xmax16(pm));
        sfor<0, 4>(SLAM(tc_) {
          constexpr int j = g_idx<NU>(k, decltype(tc_)::v);
#pragma unroll
          for (int i = 0; i < 4; ++i)
            P[j][i] = pack2(__builtin_amdgcn_exp2f(S[j][i >> 1][2 * (i & 1)] - pm), __builtin_amdgcn_exp2f(S[j][i >> 1][2 * (i & 1) + 1] - pm));
          OPQ(P[j]);
        });
        SB;
      });
      if (h == 0) rawb();
      ldsb();
    } else {
      float ev[2][2];
      constexpr int EXN = NU == 4 ? EXN4 : EXN3;
#define E_PACK(z) do { if ((z) >= 0) { unsigned pk_ = pack2(ev[(z) & 1][0], ev[(z) & 1][1]); OPQ(pk_); P[(z) / 4][(z) % 4] = pk_; } } while (0)
#define E_HALF(z, hf) do { const int i_ = (z) % 4; \
      ev[(z) & 1][hf] = __builtin_amdgcn_exp2f(S[(z) / 4][i_ >> 1][2 * (i_ & 1) + (hf)]); OPQ(ev[(z) & 1][hf]); } while (0)
      sfor<0, NG>(SLAM(jc_) {
        constexpr int j = decltype(jc_)::v, s = g_step<NU>(j), k = g_unit<NU>(j), t = s - k;
        constexpr bool first = j == g_first<NU>(s);
        if (j == XBJ && h == 0) rawb();
        if (j == NG - 1) {
          LDSB_X(63);
          STAMP(9 + 4 * h);
          LD_V(0, 0); LD_V(0, 1); LD_V(0, 2);
          LD_V(1, 0); LD_V(1, 1); LD_V(1, 2);
          SB;
        }
        sfor<0, 4>(SLAM(ic_) {
          constexpr int i = decltype(ic_)::v, r = 2 * t + (i & 1);
          constexpr bool ex = j >= 1 && j - 1 < EXN;
          constexpr int z = 4 * (j - 1) + i;
          if (TVX((i + 3) % 4) == 0 || (j == 0 && i == 0)) PH;
          S[j][i & 1] = mfma(kf[s & 1][i], (i >> 1) ? Qb[k] : Qa[k], (i < 2 && !C_OWN(k, r)) ? T[r] : S[j][i & 1]);
          SB;
          if (TVX(i) != 3) PL;
          if (first && s >= 1 && s + 1 < NS_) LD_K(s + 1, i);
          if (j == 0) { if (i == 0) LD_T(4); if (i == 1) LD_T(5); if (i == 2) LD_D(2, 0); }
          if (j == 2) { if (i == 0) LD_T(6); if (i == 1) LD_D(0, 7); if (i == 2 && NU == 4) LD_D(NU - 1, 0); }
          if (j == 7 && i == 0) LD_D(1, 7);
          if (NU == 4 && j == 11 && i == 0) LD_D(2, 7);
          if (j == NG - 3 && i == 3) { LD_D(NU - 1, 7); if (NU == 3) LD_D(NU - 1, 6); }
          SB;
          if (ex) E_HALF(z, 0);
          if (NU == 4 && j == 0) NORM_A(NU - 1, i);
          SB;
          if (TVX(i) == 2) PH;
          if (ex) E_HALF(z, 1);
          SB;
          if (TVX(i) == 1) PH;
          if (ex) E_PACK(z - 1);
          if (NU == 4 && j == 0) NORM_B(NU - 1, i);
          SB;
        });
      });
      STAMP(10 + 4 * h);
      if (NU == 3 && h < 3) {
        unsigned ke[1][6];
        PH;
        projN<1, 6, 0>(smem, wbk, xe, ke, LAM {});
        u32x4 pa = {ke[0][0], ke[0][1], ke[0][2], ke[0][3]};
        u32x2 pb = {ke[0][4], ke[0][5]};
        *(u32x4*)(smem + kae) = pa;
        *(u32x2*)(smem + (kae ^ 64)) = pb;
        const int wt = wbv; wbv = wbc; wbc = wbk; wbk = wt;
        SB;
      }
      sfor<0, NG>(SLAM(jc_) {
        constexpr int j = decltype(jc_)::v, s = g_step<NU>(j), k = g_unit<NU>(j), t = s - k;
        constexpr bool first = j == g_first<NU>(s);
        constexpr int rk = NU == 4 ? (j == 8 ? 3 : (j >= 4 && j < 7 ? j - 4 : -1)) : (j >= 4 && j < 7 ? j - 4 : -1);
        constexpr int nk = j >= 1 && g_step<NU>(j >= 1 ? j - 1 : 0) - g_unit<NU>(j >= 1 ? j - 1 : 0) == 3 ? g_unit<NU>(j >= 1 ? j - 1 : 0) : -1;
        if (j == NG - 1) {
          if (h == 3) BWOUT();
          LDSB_Y(63);
          STAMP(11 + 4 * h);
          hoff += T_HSTR;
          C_ADDR();
          X_PRE();
          SB;
        }
        sfor<0, 4>(SLAM(ic_) {
          constexpr int i = decltype(ic_)::v;
          constexpr bool ex = EXN + j < NG;
          constexpr int z = 4 * (EXN + j) + i;
          if (TVY((i + 3) % 4) == 0 || (j == 0 && i == 0)) PH;
          o[k][i] = mfma(i ? vf[s & 1][i ? i - 1 : 0] : ones, P[j], t == 0 ? zero4 : o[k][i]);
          SB;
          if (TVY(i) != 3) PL;
          if (first && s >= 1 && s + 1 < NS_ && i < 3) LD_V(s + 1, i);
          SB;
          if (ex) E_HALF(z, 0);
          if (EXN + j == NG && i == 0) E_PACK(4 * NG - 1);
          if (rk >= 0) ROT_A(rk >= 0 ? rk : 0, i);
          if (nk >= 0) NORM_A(nk >= 0 ? nk : 0, i);
          SB;
          if (TVY(i) == 2) PH;
          if (ex) E_HALF(z, 1);
          SB;
          if (TVY(i) == 1) PH;
          if (ex) E_PACK(z - 1);
          if (rk >= 0) ROT_B(rk >= 0 ? rk : 0, i);
          if (nk >= 0) NORM_B(nk >= 0 ? nk : 0, i);
          SB;
        });
      });
    }
    if constexpr (SAFE) {
      if (NU == 3 && h < 3) {
        unsigned ke[1][6];
        projN<1, 6, 0>(smem, wbk, xe, ke, LAM {});
        u32x4 pa = {ke[0][0], ke[0][1], ke[0][2], ke[0][3]};
        u32x2 pb = {ke[0][4], ke[0][5]};
        *(u32x4*)(smem + kae) = pa;
        *(u32x2*)(smem + (kae ^ 64)) = pb;
        const int wt = wbv; wbv = wbc; wbc = wbk; wbk = wt;
        SB;
      }
      sfor<0, NU>(SLAM(kc_) {
        constexpr int k = decltype(kc_)::v;
#pragma unroll
        for (int i = 0; i < 4; ++i) { ROT_A(k, i); ROT_B(k, i); }
        sfor<0, 4>(SLAM(tc_) {
          constexpr int t = decltype(tc_)::v, j = g_idx<NU>(k, t);
          LD_V(k + t, 0); LD_V(k + t, 1); LD_V(k + t, 2);
#pragma unroll
          for (int i = 0; i < 4; ++i) o[k][i] = mfma(i ? vf[(k + t) & 1][i ? i - 1 : 0] : ones, P[j], t == 0 ? zero4 : o[k][i]);
          SB;
        });
        if (k < NU - 1) { NORM_PART(k, 0); NORM_PART(k, 1); NORM_PART(k, 2); NORM_PART(k, 3); }
        SB;
      });
      ldsb();
      hoff += T_HSTR;
      C_ADDR();
    }
  }
#pragma unroll
  for (int i = 0; i < 4; ++i) NORM_PART(NU - 1, i);
  SB;
  PH;
  STAMP(24);

  bool redo = false;
  {
    u32x4 fl;
    int l2 = l;
    OPQ(l2);
    const int u = l2 & 15, g = l2 >> 4, q4 = u >> 2, pp = u & 3;
    const int rowl = 4 * g + q4;
    int wob[2][4];
#pragma unroll
    for (int b = 0; b < 4; ++b) {
      wob[1][b] = WOHI + rowl * 384 + 32 * (b ^ ((rowl >> 1) & 3)) + 8 * pp;
      wob[0][b] = wob[1][b] + (WOLO - WOHI);
      OPQ(wob[0][b]);
    }
    const int uq = min(u, 13);
    const int qa = uq >= 7 ? 1 : 0;
    const int qb = uq - 7 * qa;
    unsigned ooff[NU];
    u32x4 ob[NU][6];
#pragma unroll
    for (int k = 0; k < NU; ++k) {
      const int tok = (2 * (G.A + 2 * (4 * ib + k) + qa) + G.pa) * IMG + 2 * (G.Bc + 7 * j_ + qb) + G.pb;
      ooff[k] = (unsigned)tok * (CH * 2) + ((g & 1) ? (32 + 8 * (g - 1)) : 8 * g);
      ob[k][0] = Qa[k];
      ob[k][1] = (u32x4){Qb[k][0], Qb[k][1], R[k][0], R[k][1]};
#pragma unroll
      for (int sg = 2; sg < 6; ++sg) ob[k][sg] = (u32x4){R[k][4 * sg - 6], R[k][4 * sg - 5], R[k][4 * sg - 4], R[k][4 * sg - 3]};
    }
    char* obase = (char*)out;
    constexpr int D = 4;
    u32x4 wa[D];
#define OFRAG(t) tr_frag(smem, (32 * ((((t) % 12) / 2) % 3)) * 384 + wob[(((t) % 12) / 2) / 3][(2 * ((t) / 12) + ((t) & 1)) & 3] + 128 * ((2 * ((t) / 12) + ((t) & 1)) >> 2), 16 * 384)
#pragma unroll
    for (int t = 0; t < D - 1; ++t) wa[t] = OFRAG(t);
    f32x4 acc[2][2][NU];
#pragma unroll
    for (int nbp = 0; nbp <= 6; ++nbp) {
#pragma unroll
      for (int t1 = 0; t1 < (nbp < 6 ? 12 : 1); ++t1) {
        const int t = 12 * nbp + t1, sg = t1 / 2, tt = t1 & 1;
        if (t + D - 1 < 72) wa[(t + D - 1) % D] = OFRAG(t + D - 1);
        if (t < 72) {
#pragma unroll
          for (int k = 0; k < NU; ++k) acc[nbp & 1][tt][k] = mfma(wa[t % D], ob[k][sg], sg == 0 ? zero4 : acc[nbp & 1][tt][k]);
        }
#pragma unroll
        for (int k = 0; k < NU; ++k) {
          if (nbp >= 1 && (t == 72 || t1 == 2 + 2 * k)) {
            const int pb_ = (nbp - 1) & 1;
            unsigned x0 = pack2(acc[pb_][0][k][0], acc[pb_][0][k][1]), x1 = pack2(acc[pb_][0][k][2], acc[pb_][0][k][3]);
            unsigned y0 = pack2(acc[pb_][1][k][0], acc[pb_][1][k][1]), y1 = pack2(acc[pb_][1][k][2], acc[pb_][1][k][3]);
            auto r0 = __builtin_amdgcn_permlane16_swap(x0, y0, false, false);
            auto r1 = __builtin_amdgcn_permlane16_swap(x1, y1, false, false);
            u32x4 st = {r0[0], r1[0], r0[1], r1[1]};
            asm volatile("global_store_dwordx4 %0, %1, %2 offset:%3 " POL "\n\ts_nop 1" :: "v"(ooff[k]), "v"(st), "s"(obase), "n"(64 * (nbp - 1)) : "memory");
            if (!SAFE && nbp == 1) lmx = max(lmx, (__float_as_uint(acc[0][0][k][0]) & 0x7FFFFFFFu) >= 0x7F800000u ? 0x7F800000u : 0u);
          }
        }
        if (!SAFE && nbp == 1 && t1 == 10) {
          const bool bad = lmn < L_LO || lmx > L_HI;
          *(unsigned*)(smem + FLAGOFF + 4 * a) = __builtin_amdgcn_ballot_w64(bad) != 0 ? 1u : 0u;
        }
        if (!SAFE && nbp == 2 && t1 == 4) { SB; rawb(); }
        if (!SAFE && nbp == 2 && t1 == 5) { fl = *(const u32x4*)(smem + FLAGOFF); }
        if (!SAFE && nbp == 3 && t1 == 0) { redo = __builtin_amdgcn_readfirstlane(fl[0] | fl[1] | fl[2] | fl[3]) != 0; }
        SB;
      }
    }
  }
  STAMP(25);
  return redo;
}

__global__ __launch_bounds__(512, 2) void dinat_fused(const uint16_t* __restrict__ x_, const uint16_t* __restrict__ wqkv_,
                                                      const uint16_t* __restrict__ wo_, const uint16_t* __restrict__ rpb_,
                                                      uint16_t* __restrict__ out_) {
  unsigned long long dg0, dg1;
  const uint16_t* x; const uint16_t* wqkv; const uint16_t* wo; const uint16_t* rpb; uint16_t* out;
  {
    auto ka = __builtin_amdgcn_kernarg_segment_ptr();
    asm volatile("s_memrealtime %0\n\ts_waitcnt lgkmcnt(0)\n\ts_load_dwordx2 %2, %7, 0\n\ts_load_dwordx2 %3, %7, 8\n\ts_load_dwordx2 %4, %7, 16\n\ts_load_dwordx2 %5, %7, 24\n\ts_load_dwordx2 %6, %7, 32\n\ts_waitcnt lgkmcnt(0)\n\ts_memrealtime %1\n\ts_waitcnt lgkmcnt(0)"
      : "=&s"(dg0), "=&s"(dg1), "=&s"(x), "=&s"(wqkv), "=&s"(wo), "=&s"(rpb), "=&s"(out) : "s"(ka) : "memory");
  }
  __shared__ __attribute__((aligned(128))) char smem[LDS_BYTES];
  const unsigned lds_base = (unsigned)(size_t)(LDS_AS char*)smem;
  const int tid = threadIdx.x;
  const int w = __builtin_amdgcn_readfirstlane(tid >> 6);
  const int l = tid & 63;
  const int bidx = blockIdx.x;
  const int xcd = bidx & 7, idx = bidx >> 3;
  const int sub = 2 * (xcd & 1) + (idx & 1);
  const int ta = 2 * (xcd >> 1) + (idx >> 4), tb = (idx >> 1) & 7;
  Geo G;
  G.pa = sub >> 1; G.pb = sub & 1;
  G.A = 14 * ta; G.Bc = 14 * tb;
  G.R0 = min(max(G.A - 3, -1), 93); G.C0 = min(max(G.Bc - 3, 0), 92);
  SOPQ(G.R0); SOPQ(G.C0);
  G.or_ = G.A - G.R0; G.oc_ = G.Bc - G.C0;
  const char* xb = (const char*)x + ((long long)((2 * G.R0 + G.pa) * IMG + 2 * G.C0 + G.pb)) * (CH * 2);
  bool redo;
  STAMP(0);
  if (w >= 4) redo = proj_wave<false>(smem, lds_base, xb, wqkv, wo, rpb, G, w - 4, l PARG);
  else if (w < 2) redo = attn_wave<4, false>(smem, lds_base, xb, wqkv, out, G, w, l PARG);
  else redo = attn_wave<3, false>(smem, lds_base, xb, wqkv, out, G, w, l PARG);
  if (w == 0) {
    unsigned long long e = SPIN ? dg0 + SPIN : __builtin_amdgcn_s_memrealtime() + (dg1 - dg0) * MUL;
    while (__builtin_amdgcn_s_memrealtime() < e) __builtin_amdgcn_s_sleep(4);
  }
  if (__builtin_expect(redo, 0)) {
    asm volatile("s_waitcnt vmcnt(0) lgkmcnt(0)\n\ts_barrier" ::: "memory");
    __builtin_amdgcn_s_setprio(0);
    int l2 = l;
    OPQ(l2);
    if (w >= 4) proj_wave<true>(smem, lds_base, xb, wqkv, wo, rpb, G, w - 4, l2 PARG);
    else if (w < 2) attn_wave<4, true>(smem, lds_base, xb, wqkv, out, G, w, l2 PARG);
    else attn_wave<3, true>(smem, lds_base, xb, wqkv, out, G, w, l2 PARG);
  }
}

extern "C" void kernel_launch(void* const* d_in, const int* in_sizes, int n_in,
                              void* d_out, int out_size, void* d_ws, size_t ws_size,
                              hipStream_t stream) {
  const uint16_t* x = (const uint16_t*)d_in[0];
  const uint16_t* wqkv = (const uint16_t*)d_in[1];
  const uint16_t* wo = (const uint16_t*)d_in[2];
  const uint16_t* rpb = (const uint16_t*)d_in[3];
  dinat_fused<<<256, 512, 0, stream>>>(x, wqkv, wo, rpb, (uint16_t*)d_out
  );
}
```

```cpp
#include <hip/hip_runtime.h>
#include <hip/hip_bf16.h>
#include <stdint.h>

typedef __attribute__((ext_vector_type(4))) float f32x4;
typedef __attribute__((ext_vector_type(4))) unsigned u32x4;
typedef __attribute__((ext_vector_type(8))) unsigned u32x8;

#define LDS_AS __attribute__((address_space(3)))
#define DI __device__ __forceinline__
#define SB __builtin_amdgcn_sched_barrier(0)
#define OPQ(v) asm volatile("" : "+v"(v))
#define UNI(v) __builtin_amdgcn_readfirstlane(v)

#define TOFF 0
#define TE 1216
#define RAW 2048
#define PFD 3584
#define O45 2048
#define FLAG 34304
#define TMPA 28672
#define BB 24576
#define PFC 375
#define XR 102400
#define TMPB 53248
#define RESA 4096
#define RESB 77824
#define NREG 35
#define UD0 0x1312111003020100ULL
#define UD1 0x0504090807060504ULL
#define UD2 0x2928191817161514ULL
#define UD3 0x1D1C1B1A0D0C0B0AULL
#define UD4 0x2120252423222120ULL
#define UD5 0x4342414033323130ULL
#define UD6 0x2726393837363534ULL
#define UD7 0x3D3C3B3A2D2C2B2AULL
#define UD8 0x4D4C4B4A49484746ULL
#define UD9 0x6362616053525150ULL
#define UD10 0x4544595857565554ULL
#define UD11 0x6D6C6B6A5D5C5B5AULL
#define UD12 0x6564696867666564ULL
#define UD13 0x6564696867666564ULL
#define UD14 0x6564696867666564ULL
#define UD15 0x6564696867666564ULL
#define CLOB_A "v10", "v11", "v24", "v25", "v26", "v27", "v28", "v29", "v30", "v31", "v32", "v33", "v34", "v35", "v36", "v37", "v38", "v39", "v40", "v41", "v42", "v43", "v44", "v45", "v46", "v47", "v48", "v49", "v50", "v51", "v52", "v53", "v54", "v55", "v56", "v57", "v58", "v59", "v60", "v61", "v62", "v63", "v64", "v65", "v66", "v67", "v68", "v69", "v70", "v71", "v72", "v73", "v74", "v75", "v76", "v77", "v78", "v79", "v80", "v81", "v82", "v83", "v84", "v85", "v86", "v87", "v88", "v89", "v90", "v91", "v92", "v93", "v94", "v95", "v96", "v97", "v98", "v99", "v100", "v101", "v102", "v103", "v104", "v105", "v106", "v107", "v108", "v109", "v110", "v111", "v112", "v113", "v114", "v115", "v116", "v117", "v118", "v119", "v120", "v121", "v122", "v123", "v124", "v125", "v126", "v127", "v128", "v129", "v130", "v131", "v132", "v133", "v134", "v135", "v136", "v137", "v138", "v139", "v140", "v141", "v142", "v143", "v144", "v145", "v146", "v147", "v148", "v149", "v150", "v151", "v152", "v153", "v154", "v155", "v156", "v157", "v158", "v159", "v160", "v161", "v162", "v163", "v164", "v165", "v166", "v167", "v168", "v169", "v170", "v171", "v172", "v173", "v174", "v175", "v176", "v177", "v178", "v179", "v180", "v181", "v182", "v183", "v184", "v185", "v186", "v187", "v188", "v189", "v190", "v191", "v192", "v193", "v194", "v195", "v196", "v197", "v198", "v199", "v200", "v201", "v202", "v203", "v204", "v205", "v206", "v207", "v208", "v209", "v210", "v211", "v212", "v213", "v214", "v215", "v216", "v217", "v218", "v219", "v220", "v221", "v222", "v223", "v224", "v225", "v226", "v227", "v228", "v229", "v230", "v231", "v232", "v233", "v234", "v235", "v236", "v237", "v238", "v239", "v240", "v241", "v242", "v243", "v244", "v245", "v246", "v247", "v248", "v249", "v250", "v251", "v252", "v253", "v254", "v255"
#define CLOB_B "v9", "v20", "v21", "v22", "v23", "v24", "v25", "v26", "v27", "v28", "v29", "v30", "v31", "v32", "v33", "v34", "v35", "v36", "v37", "v38", "v39", "v40", "v41", "v42", "v43", "v44", "v45", "v46", "v47", "v48", "v49", "v50", "v51", "v52", "v53", "v54", "v55", "v56", "v57", "v58", "v59", "v60", "v61", "v62", "v63", "v64", "v65", "v66", "v67", "v68", "v69", "v70", "v71", "v72", "v73", "v74", "v75", "v76", "v77", "v78", "v79", "v80", "v81", "v82", "v83", "v84", "v85", "v86", "v87", "v88", "v89", "v90", "v91", "v92", "v93", "v94", "v95", "v96", "v97", "v98", "v99", "v100", "v101", "v102", "v103", "v104", "v105", "v106", "v107", "v108", "v109", "v110", "v111", "v112", "v113", "v114", "v115", "v116", "v117", "v118", "v119", "v120", "v121", "v122", "v123", "v124", "v125", "v126", "v127", "v128", "v129", "v130", "v131", "v132", "v133", "v134", "v135", "v136", "v137", "v138", "v139", "v140", "v141", "v142", "v143", "v144", "v145", "v146", "v147", "v148", "v149", "v150", "v151", "v152", "v153", "v154", "v155", "v156", "v157", "v158", "v159", "v160", "v161", "v162", "v163", "v164", "v165", "v166", "v167", "v168", "v169", "v170", "v171", "v172", "v173", "v174", "v175", "v176", "v177", "v178", "v179", "v180", "v181", "v182", "v183", "v184", "v185", "v186", "v187", "v188", "v189", "v190", "v191", "v192", "v193", "v194", "v195", "v196", "v197", "v198", "v199", "v200", "v201", "v202", "v203", "v204", "v205", "v206", "v207", "v208", "v209", "v210", "v211", "v212", "v213", "v214", "v215", "v216", "v217", "v218", "v219", "v220", "v221", "v222", "v223", "v224", "v225", "v226", "v227", "v228", "v229", "v230", "v231", "v232", "v233", "v234", "v235", "v236", "v237", "v238", "v239", "v240", "v241", "v242", "v243", "v244", "v245", "v246", "v247", "v248", "v249", "v250", "v251", "v252", "v253", "v254", "v255"
#define LDS_BYTES 163840
#define LOG2E 1.4426950408889634f
#define LP(T, o) ((LDS_AS T*)(size_t)(unsigned)(o))

DI const char* uni(const void* p) {
  unsigned long long sv = (unsigned long long)p;
  unsigned lo = UNI((unsigned)sv), hi = UNI((unsigned)(sv >> 32));
  return (const char*)(((unsigned long long)hi << 32) | lo);
}
#define DMA(src, voff, dst) asm volatile("s_mov_b32 m0, %2\n\ts_nop 0\n\tglobal_load_lds_dwordx4 %0, %1" :: "v"(voff), "s"(src), "s"(dst) : "memory", "m0")
#define DMA4(src, voff, dst) asm volatile("s_mov_b32 m0, %2\n\ts_nop 0\n\tglobal_load_lds_dword %0, %1" :: "v"(voff), "s"(src), "s"(dst) : "memory", "m0")
#define RAWB() asm volatile("s_barrier" ::: "memory")
#define WVM(n) asm volatile("s_waitcnt vmcnt(%0)" :: "n"(n) : "memory")
#define PSTART
#define PEND
#define PPARM
#define SPARM
#define SARG
#define PCLOB

struct Geo { int t0, sr, sc, er, ec, so; unsigned long long pc; };
DI int tok_idx(const Geo& G, int hr, int hc) {
  return G.t0 + G.sr * (hr - (hr >= 17 ? G.er : 0)) + G.sc * (hc - (hc >= 17 ? G.ec : 0));
}
template <int STR, int HB>
DI void mk_lo(int l, unsigned (&lo)[3], int h = 0) {
#pragma unroll
  for (int i = 0; i < 3; ++i) {
    const int idx = 64 * i + l, t = (idx * 171) >> 10, c = idx - 6 * t;
    const int r = HB ? ((t >> 3) & 1) * HB + ((t >> 1) & 8) + (t & 7) : ((t & ~12) | ((t & 4) << 1) | ((t & 8) >> 1));
    lo[i] = r * STR + c * 16 + (HB ? (c < 4 ? 64 * h : 192 + 32 * h) : 0);
    OPQ(lo[i]);
  }
}

DI int lane_id() {
  int l;
  asm volatile("v_mbcnt_lo_u32_b32 %0, -1, 0\n\tv_mbcnt_hi_u32_b32 %0, -1, %0" : "=v"(l));
  return l;
}
#define A_HEAD \
  const int l = lane_id() & 63; \
  const int u = l & 15, g = l >> 4; \
  unsigned lo3[3]; \
  const char* wq_; \
  const unsigned sW = UNI(TMPA + 6144 * h); \
  unsigned wtm, xa, xo, lh, gr, tdst, tneg; \
  u32x8 bv; u32x4 sp
#define A_SMALL() do { \
  const int cu = (blockIdx.x >> 3) & 31, line = cu * 72 + 18 * h + l; \
  unsigned po = 128 * (l >= 18 ? min(16 * cu + 4 * h + min(l - 18, 3), PFC) : (line < 1728 ? line : line - 1728)); \
  const int rb = (338 * h) & ~3; \
  unsigned r0 = min(rb + 4 * l, 1348), r1 = min(rb + 128 + 4 * l, 1348); \
  OPQ(po); OPQ(r0); OPQ(r1); \
  SB; \
  const unsigned long long pb = l >= 18 ? G.pc : (line < 1728 ? (unsigned long long)wqkv : (unsigned long long)wo); \
  asm volatile("s_mov_b32 m0, %1\n\ts_nop 0\n\tglobal_load_lds_dword %0, off" :: "v"(pb + po), "s"(PFD) : "memory", "m0"); \
  const char* rp = uni(rpb); \
  wq_ = uni((const char*)wqkv + 96 * h); \
  DMA4(rp, r0, UNI(RAW + 384 * h)); \
  DMA4(rp, r1, UNI(RAW + 384 * h + 128)); \
} while (0)
#define A_TABLES() do { \
  mk_lo<1152, 0>(l, lo3); \
  const unsigned wlane = (16 * (g >> 1) + 4 * (g & 1) + (u >> 2)) * 96 + 8 * (u & 3); \
  wtm = 6144 * h + wlane; \
  xa = XR + u * 128 + 16 * (g ^ (u >> 1)); xo = xa ^ 64; lh = 16 * l + 6144 * h; gr = 6144 * h + 336 * g; \
  *LP(uint16_t, PFD + 256) = 0xF180; \
  const int lv = min(l, 39), row = lv >> 2, qd = lv & 3; \
  const int rho = row >= 7 ? row - 7 : row + 3; \
  const int src = RAW + 384 * h + ((338 * h) & 2) + 26 * (G.sr < 0 ? 12 - rho : rho) + (G.sc < 0 ? 24 : 0); \
  tdst = sW + 64 + 336 * row + (row >= 7 ? 32 : 0) + 16 * qd; \
  tneg = sW + (l < 10 ? 384 + 336 * l + (l >= 7 ? 32 : 0) : (l < 14 ? 16 * (l - 10) : (l < 16 ? 2416 + 16 * (l - 14) : 3456 + 16 * (l & 1)))); \
  _Pragma("unroll") for (int e = 0; e < 7; ++e) bv[e] = (unsigned)(4 * qd + e - 3) <= 6u ? src + 2 * G.sc * (4 * qd + e) : PFD + 256; \
  bv[7] = 0; \
  _Pragma("unroll") for (int i = 0; i < 4; ++i) sp[i] = (qd < 3 && i <= qd + 1) ? src + 2 * G.sc * (8 - qd + i - (i >= 1 ? 7 : 0)) : PFD + 256; \
  OPQ(wtm); OPQ(xa); OPQ(xo); OPQ(lh); OPQ(gr); OPQ(tdst); OPQ(tneg); OPQ(bv); OPQ(sp); \
} while (0)
#define ASM_A(txt) asm volatile("s_waitcnt lgkmcnt(0)\n" txt \
  : "+{v0}"(lo3[0]), "+{v1}"(lo3[1]), "+{v2}"(lo3[2]), "+{v3}"(wtm), "+{v4}"(xa), "+{v5}"(xo), "+{v6}"(lh), "+{v7}"(gr), "+{v8}"(tdst), "+{v9}"(tneg), \
    "+{v[12:19]}"(bv), "+{v[20:23]}"(sp) \
  : "{s[46:47]}"(wq_), "{s50}"(sW), "{s[58:59]}"(G.er ? 0xFFFFFFFFFFFF0000ULL : 0ULL) \
  : CLOB_A, "s42", "s43", "s60", "s61", "s68", "s69", "s70", "s71", "s72", "s73", "vcc", "scc", "m0", "memory" PCLOB)

#define B_HEAD \
  const int l = lane_id() & 63; \
  const int u = l & 15, g = l >> 4; \
  const bool ph = h >= 2; \
  const int tk = 8 * (h & 1) + (l >> 3); \
  unsigned ck16 = 16 * ((l & 7) ^ (tk >> 1)); \
  unsigned xl = (G.t0 + G.sr * (tk >> 2) + G.sc * (tk & 3)) * 768 + ck16; \
  OPQ(xl); \
  const unsigned sXB = UNI(6144 * (h >> 1) + 1024 * (h & 1)), sW = UNI(TMPB + 6144 * h); \
  SB; \
  const char* xs = uni((const char*)x + G.so); \
  unsigned lo3[3], lo4[3], lo5[3]; \
  unsigned wtm, wtp, xa, xo, l16, tabx, lh, g8, g16
#define XDIR(a0, b0, a1, b1, dst) do { \
  const unsigned xv = xl + UNI(3072 * (G.sr * (ph ? a1 : a0) + G.sc * (ph ? b1 : b0))); \
  DMA(xs, xv, sXB + (dst)); DMA(xs + 128, xv, sXB + (dst) + 2048); DMA(xs + 256, xv, sXB + (dst) + 4096); \
} while (0)
#define B_SETUP() do { \
  const unsigned wrow = BB + 6144 * h + (16 * (g >> 1) + 4 * (g & 1) + (u >> 2)) * 96; \
  wtm = wrow + 8 * (u & 3); wtp = wrow + 16 * (u & 3); \
  xa = XR + u * 128 + 16 * (g ^ (u >> 1)); xo = xa ^ 64; l16 = 16 * l; tabx = 2 * tk; lh = l16 + BB + 6144 * h; g8 = 8 * g; g16 = 16 * g; \
  mk_lo<384, 16>(l, lo4, h); mk_lo<384, 48>(l, lo5, h); mk_lo<1152, 0>(l, lo3); \
  _Pragma("unroll") for (int i = 0; i < 2; ++i) { \
    const int e = min(256 * i + 64 * h + l, 399), blk = e >> 4, a = (blk * 205) >> 10, b = blk - 5 * a; \
    *LP(uint16_t, TOFF + 2 * e) = tok_idx(G, 4 * a + ((e >> 2) & 3), 4 * b + (e & 3)); } \
  const unsigned long long d0 = h == 0 ? UD0 : (h == 1 ? UD4 : (h == 2 ? UD8 : UD12)), d1 = h == 0 ? UD1 : (h == 1 ? UD5 : (h == 2 ? UD9 : UD13)); \
  const unsigned long long d2 = h == 0 ? UD2 : (h == 1 ? UD6 : (h == 2 ? UD10 : UD14)), d3 = h == 0 ? UD3 : (h == 1 ? UD7 : (h == 2 ? UD11 : UD15)); \
  const unsigned long long d = g < 2 ? (g == 0 ? d0 : d1) : (g == 2 ? d2 : d3); \
  const int by = (int)(d >> (8 * (u >> 1))) & 255, c = by & 15, r = 2 * (by >> 4) + (u & 1); \
  const int e = 16 * min(4 * h + g, 12) + u; \
  *LP(uint16_t, TOFF + 800 + 2 * e) = G.t0 + G.sr * (r + 3) + G.sc * (c + 3); \
  const int cp = 64 * (c & 3) - 16 * (c >> 2); \
  const int dl = (G.ec && c >= 11) ? 16 * c + 16 - cp : 0; \
  *LP(unsigned, TE + 4 * e) = (unsigned)(TMPA + 64 + cp - 336 * r) | ((unsigned)dl << 16); \
} while (0)
#define ASM_B(txt) asm volatile("s_waitcnt lgkmcnt(0)\n" txt \
  : "+{v0}"(lo3[0]), "+{v1}"(lo3[1]), "+{v2}"(lo3[2]), "+{v3}"(lo4[0]), "+{v4}"(lo4[1]), "+{v5}"(lo4[2]), "+{v6}"(lo5[0]), "+{v7}"(lo5[1]), "+{v8}"(lo5[2]), \
    "+{v10}"(wtm), "+{v11}"(wtp), "+{v19}"(g8), "+{v12}"(xa), "+{v13}"(xo), "+{v14}"(l16), "+{v15}"(tabx), "+{v16}"(ck16), "+{v17}"(lh), "+{v18}"(g16) \
  : "{s[46:47]}"(uni((const char*)wqkv + 96 * h)), "{s[48:49]}"(uni(wo)), "{s50}"(sW), "{s51}"(sXB), \
    "{s54}"(UNI(O45 + 1024 * (h >> 1) + 8 * (h & 1))), "{s55}"(UNI(ph ? ~0u : 0u)), "{s[56:57]}"(xs), "{s65}"(UNI(FLAG + 4 * h)), \
    "{s[68:69]}"(uni((const char*)out + G.so + 64 * h)), "{s[70:71]}"(uni((const char*)out + G.so + 256 + 32 * h)) \
  : CLOB_B, "s40", "s41", "s42", "s43", "s58", "s59", "s60", "s61", "s62", "s63", "s64", "vcc", "scc", "m0", "memory" PCLOB)

DI void waveA_fast(const uint16_t* wqkv, const uint16_t* wo, const uint16_t* rpb, const Geo& G, int h SPARM) {
  A_HEAD;
  A_TABLES(); A_SMALL(); RAWB(); SB;
  DMA(wq_ + 384, lo3[0], sW + (0)); SB;
  DMA(wq_ + 384, lo3[1], sW + (1024)); SB;
  DMA(wq_ + 384, lo3[2], sW + (2048)); SB;
  DMA(wq_ + 37248, lo3[0], sW + (3072)); SB;
  DMA(wq_ + 37248, lo3[1], sW + (4096)); SB;
  DMA(wq_ + 37248, lo3[2], sW + (5120)); SB;
  DMA(wq_ + 74112, lo3[0], sW + (-24576)); SB;
  DMA(wq_ + 74112, lo3[1], sW + (-23552)); SB;
  DMA(wq_ + 74112, lo3[2], sW + (-22528)); SB;
  DMA(wq_ + 110976, lo3[0], sW + (-21504)); SB;
  DMA(wq_ + 110976, lo3[1], sW + (-20480)); SB;
  DMA(wq_ + 110976, lo3[2], sW + (-19456)); SB;
  DMA(wq_ + 147840, lo3[0], sW + (110592)); SB;
  DMA(wq_ + 147840, lo3[1], sW + (111616)); SB;
  DMA(wq_ + 147840, lo3[2], sW + (112640)); SB;
  DMA(wq_ + 184704, lo3[0], sW + (113664)); SB;
  DMA(wq_ + 184704, lo3[1], sW + (114688)); SB;
  DMA(wq_ + 184704, lo3[2], sW + (115712)); SB;
  ASM_A(
"s_setprio 1\n"
"s_mov_b32 s60, 0x3e553b95\n"
"s_mov_b32 s61, 0x3fb8aa3b\n"
"v_add_u32 v11, 77824, v3\n"
"v_mov_b32 v24, 0\n"
"v_mov_b32 v25, 0\n"
"s_barrier\n"
"s_waitcnt vmcnt(15)\n"
"ds_read_b64_tr_b16 v[28:29], v3 offset:28672\n"
"ds_read_b64_tr_b16 v[30:31], v3 offset:29440\n"
"s_waitcnt vmcnt(12)\n"
"ds_read_b128 v[178:181], v4 offset:0\n"
"ds_read_b64_tr_b16 v[32:33], v3 offset:28704\n"
"ds_read_b64_tr_b16 v[34:35], v3 offset:29472\n"
"ds_read_b64_tr_b16 v[36:37], v3 offset:28736\n"
"ds_read_b64_tr_b16 v[38:39], v3 offset:29504\n"
"ds_read_b128 v[182:185], v4 offset:6144\n"
"ds_read_b128 v[186:189], v4 offset:12288\n"
"ds_read_b64_tr_b16 v[40:41], v3 offset:31744\n"
"ds_read_b64_tr_b16 v[42:43], v3 offset:32512\n"
"ds_read_b64_tr_b16 v[44:45], v3 offset:31776\n"
"ds_read_b64_tr_b16 v[46:47], v3 offset:32544\n"
"ds_read_b64_tr_b16 v[48:49], v3 offset:31808\n"
"ds_read_b64_tr_b16 v[50:51], v3 offset:32576\n"
"ds_read_b128 v[190:193], v4 offset:18432\n"
"s_waitcnt lgkmcnt(13)\n"
"v_mfma_f32_16x16x32_bf16 v[196:199], v[28:31], v[178:181], 0\n"
"s_waitcnt lgkmcnt(11)\n"
"v_mfma_f32_16x16x32_bf16 v[212:215], v[32:35], v[178:181], 0\n"
"s_waitcnt lgkmcnt(9)\n"
"v_mfma_f32_16x16x32_bf16 v[216:219], v[36:39], v[178:181], 0\n"
"s_waitcnt lgkmcnt(8)\n"
"ds_read_b128 v[178:181], v4 offset:24576\n"
"v_mfma_f32_16x16x32_bf16 v[202:205], v[28:31], v[182:185], 0\n"
"v_mfma_f32_16x16x32_bf16 v[220:223], v[32:35], v[182:185], 0\n"
"v_mfma_f32_16x16x32_bf16 v[236:239], v[36:39], v[182:185], 0\n"
"s_waitcnt lgkmcnt(8)\n"
"ds_read_b128 v[182:185], v4 offset:30720\n"
"v_mfma_f32_16x16x32_bf16 v[226:229], v[28:31], v[186:189], 0\n"
"v_mfma_f32_16x16x32_bf16 v[240:243], v[32:35], v[186:189], 0\n"
"v_mfma_f32_16x16x32_bf16 v[244:247], v[36:39], v[186:189], 0\n"
"s_waitcnt lgkmcnt(2)\n"
"ds_read_b128 v[186:189], v5 offset:0\n"
"v_mfma_f32_16x16x32_bf16 v[232:235], v[28:31], v[190:193], 0\n"
"v_mfma_f32_16x16x32_bf16 v[248:251], v[32:35], v[190:193], 0\n"
"v_mfma_f32_16x16x32_bf16 v[252:255], v[36:39], v[190:193], 0\n"
"s_waitcnt lgkmcnt(2)\n"
"ds_read_b128 v[190:193], v5 offset:6144\n"
"v_mfma_f32_16x16x32_bf16 v[208:211], v[28:31], v[178:181], 0\n"
"s_waitcnt vmcnt(9)\n"
"ds_read_b64_tr_b16 v[52:53], v3 offset:4096\n"
"ds_read_b64_tr_b16 v[54:55], v3 offset:4864\n"
"v_mfma_f32_16x16x32_bf16 v[100:103], v[32:35], v[178:181], 0\n"
"ds_read_b64_tr_b16 v[56:57], v3 offset:4128\n"
"ds_read_b64_tr_b16 v[58:59], v3 offset:4896\n"
"v_mfma_f32_16x16x32_bf16 v[104:107], v[36:39], v[178:181], 0\n"
"s_waitcnt lgkmcnt(6)\n"
"ds_read_b64_tr_b16 v[60:61], v3 offset:4160\n"
"ds_read_b64_tr_b16 v[62:63], v3 offset:4928\n"
"v_mfma_f32_16x16x32_bf16 v[178:181], v[28:31], v[182:185], 0\n"
"ds_read_b128 v[72:75], v5 offset:12288\n"
"v_mfma_f32_16x16x32_bf16 v[108:111], v[32:35], v[182:185], 0\n"
"v_mfma_f32_16x16x32_bf16 v[112:115], v[36:39], v[182:185], 0\n"
"s_waitcnt lgkmcnt(8)\n"
"ds_read_b128 v[182:185], v5 offset:18432\n"
"v_mfma_f32_16x16x32_bf16 v[196:199], v[40:43], v[186:189], v[196:199]\n"
"v_mfma_f32_16x16x32_bf16 v[212:215], v[44:47], v[186:189], v[212:215]\n"
"v_mfma_f32_16x16x32_bf16 v[216:219], v[48:51], v[186:189], v[216:219]\n"
"s_waitcnt lgkmcnt(8)\n"
"ds_read_b128 v[186:189], v5 offset:24576\n"
"v_mfma_f32_16x16x32_bf16 v[202:205], v[40:43], v[190:193], v[202:205]\n"
"v_mfma_f32_16x16x32_bf16 v[220:223], v[44:47], v[190:193], v[220:223]\n"
"v_mfma_f32_16x16x32_bf16 v[236:239], v[48:51], v[190:193], v[236:239]\n"
"s_waitcnt lgkmcnt(2)\n"
"ds_read_b128 v[190:193], v5 offset:30720\n"
"v_mfma_f32_16x16x32_bf16 v[226:229], v[40:43], v[72:75], v[226:229]\n"
"s_waitcnt vmcnt(6)\n"
"ds_read_b64_tr_b16 v[64:65], v3 offset:7168\n"
"ds_read_b64_tr_b16 v[66:67], v3 offset:7936\n"
"v_mfma_f32_16x16x32_bf16 v[240:243], v[44:47], v[72:75], v[240:243]\n"
"ds_read_b64_tr_b16 v[68:69], v3 offset:7200\n"
"ds_read_b64_tr_b16 v[70:71], v3 offset:7968\n"
"v_mfma_f32_16x16x32_bf16 v[244:247], v[48:51], v[72:75], v[244:247]\n"
"s_waitcnt lgkmcnt(6)\n"
"ds_read_b64_tr_b16 v[72:73], v3 offset:7232\n"
"ds_read_b64_tr_b16 v[74:75], v3 offset:8000\n"
"v_mfma_f32_16x16x32_bf16 v[232:235], v[40:43], v[182:185], v[232:235]\n"
"ds_read_b128 v[84:87], v4 offset:2048\n"
"v_mfma_f32_16x16x32_bf16 v[248:251], v[44:47], v[182:185], v[248:251]\n"
"v_mfma_f32_16x16x32_bf16 v[252:255], v[48:51], v[182:185], v[252:255]\n"
"s_waitcnt lgkmcnt(8)\n"
"ds_read_b128 v[182:185], v4 offset:8192\n"
"v_mfma_f32_16x16x32_bf16 v[208:211], v[40:43], v[186:189], v[208:211]\n"
"v_mfma_f32_16x16x32_bf16 v[100:103], v[44:47], v[186:189], v[100:103]\n"
"v_mfma_f32_16x16x32_bf16 v[104:107], v[48:51], v[186:189], v[104:107]\n"
"s_waitcnt lgkmcnt(8)\n"
"ds_read_b128 v[186:189], v4 offset:14336\n"
"v_mfma_f32_16x16x32_bf16 v[178:181], v[40:43], v[190:193], v[178:181]\n"
"v_mfma_f32_16x16x32_bf16 v[108:111], v[44:47], v[190:193], v[108:111]\n"
"v_mfma_f32_16x16x32_bf16 v[112:115], v[48:51], v[190:193], v[112:115]\n"
"s_waitcnt lgkmcnt(2)\n"
"ds_read_b128 v[190:193], v4 offset:20480\n"
"v_mfma_f32_16x16x32_bf16 v[196:199], v[52:55], v[84:87], v[196:199]\n"
"s_waitcnt vmcnt(3)\n"
"ds_read_b64_tr_b16 v[76:77], v11 offset:61440\n"
"ds_read_b64_tr_b16 v[78:79], v11 offset:62208\n"
"v_mfma_f32_16x16x32_bf16 v[212:215], v[56:59], v[84:87], v[212:215]\n"
"ds_read_b64_tr_b16 v[80:81], v11 offset:61472\n"
"ds_read_b64_tr_b16 v[82:83], v11 offset:62240\n"
"v_mfma_f32_16x16x32_bf16 v[216:219], v[60:63], v[84:87], v[216:219]\n"
"s_waitcnt lgkmcnt(6)\n"
"ds_read_b64_tr_b16 v[84:85], v11 offset:61504\n"
"ds_read_b64_tr_b16 v[86:87], v11 offset:62272\n"
"v_mfma_f32_16x16x32_bf16 v[202:205], v[52:55], v[182:185], v[202:205]\n"
"ds_read_b128 v[96:99], v4 offset:26624\n"
"v_mfma_f32_16x16x32_bf16 v[220:223], v[56:59], v[182:185], v[220:223]\n"
"v_mfma_f32_16x16x32_bf16 v[236:239], v[60:63], v[182:185], v[236:239]\n"
"s_waitcnt lgkmcnt(8)\n"
"ds_read_b128 v[182:185], v4 offset:32768\n"
"v_mfma_f32_16x16x32_bf16 v[226:229], v[52:55], v[186:189], v[226:229]\n"
"v_mfma_f32_16x16x32_bf16 v[240:243], v[56:59], v[186:189], v[240:243]\n"
"v_mfma_f32_16x16x32_bf16 v[244:247], v[60:63], v[186:189], v[244:247]\n"
"s_waitcnt lgkmcnt(8)\n"
"ds_read_b128 v[186:189], v5 offset:2048\n"
"v_mfma_f32_16x16x32_bf16 v[232:235], v[52:55], v[190:193], v[232:235]\n"
"v_mfma_f32_16x16x32_bf16 v[248:251], v[56:59], v[190:193], v[248:251]\n"
"v_mfma_f32_16x16x32_bf16 v[252:255], v[60:63], v[190:193], v[252:255]\n"
"s_waitcnt lgkmcnt(2)\n"
"ds_read_b128 v[190:193], v5 offset:8192\n"
"v_mfma_f32_16x16x32_bf16 v[208:211], v[52:55], v[96:99], v[208:211]\n"
"s_waitcnt vmcnt(0)\n"
"ds_read_b64_tr_b16 v[88:89], v11 offset:64512\n"
"ds_read_b64_tr_b16 v[90:91], v11 offset:65280\n"
"v_mfma_f32_16x16x32_bf16 v[100:103], v[56:59], v[96:99], v[100:103]\n"
"ds_read_b64_tr_b16 v[92:93], v11 offset:64544\n"
"ds_read_b64_tr_b16 v[94:95], v11 offset:65312\n"
"v_mfma_f32_16x16x32_bf16 v[104:107], v[60:63], v[96:99], v[104:107]\n"
"s_waitcnt lgkmcnt(6)\n"
"ds_read_b64_tr_b16 v[96:97], v11 offset:64576\n"
"ds_read_b64_tr_b16 v[98:99], v11 offset:65344\n"
"v_mfma_f32_16x16x32_bf16 v[178:181], v[52:55], v[182:185], v[178:181]\n"
"ds_read_b128 v[116:119], v5 offset:14336\n"
"v_mfma_f32_16x16x32_bf16 v[108:111], v[56:59], v[182:185], v[108:111]\n"
"v_mfma_f32_16x16x32_bf16 v[112:115], v[60:63], v[182:185], v[112:115]\n"
"s_waitcnt lgkmcnt(8)\n"
"ds_read_b128 v[182:185], v5 offset:20480\n"
"v_mfma_f32_16x16x32_bf16 v[196:199], v[64:67], v[186:189], v[196:199]\n"
"v_mfma_f32_16x16x32_bf16 v[212:215], v[68:71], v[186:189], v[212:215]\n"
"v_mfma_f32_16x16x32_bf16 v[216:219], v[72:75], v[186:189], v[216:219]\n"
"s_waitcnt lgkmcnt(8)\n"
"ds_read_b128 v[186:189], v5 offset:26624\n"
"v_mfma_f32_16x16x32_bf16 v[202:205], v[64:67], v[190:193], v[202:205]\n"
"s_waitcnt lgkmcnt(3)\n"
"s_barrier\n"
"v_mfma_f32_16x16x32_bf16 v[220:223], v[68:71], v[190:193], v[220:223]\n"
"v_mfma_f32_16x16x32_bf16 v[236:239], v[72:75], v[190:193], v[236:239]\n"
"s_waitcnt lgkmcnt(2)\n"
"ds_read_b128 v[190:193], v5 offset:32768\n"
"v_mfma_f32_16x16x32_bf16 v[226:229], v[64:67], v[116:119], v[226:229]\n"
"v_mfma_f32_16x16x32_bf16 v[240:243], v[68:71], v[116:119], v[240:243]\n"
"v_mfma_f32_16x16x32_bf16 v[244:247], v[72:75], v[116:119], v[244:247]\n"
"s_waitcnt lgkmcnt(2)\n"
"ds_read_b128 v[116:119], v4 offset:4096\n"
"v_mfma_f32_16x16x32_bf16 v[232:235], v[64:67], v[182:185], v[232:235]\n"
"v_mfma_f32_16x16x32_bf16 v[248:251], v[68:71], v[182:185], v[248:251]\n"
"v_mfma_f32_16x16x32_bf16 v[252:255], v[72:75], v[182:185], v[252:255]\n"
"s_waitcnt lgkmcnt(2)\n"
"ds_read_b128 v[182:185], v4 offset:10240\n"
"v_mfma_f32_16x16x32_bf16 v[208:211], v[64:67], v[186:189], v[208:211]\n"
"v_mfma_f32_16x16x32_bf16 v[100:103], v[68:71], v[186:189], v[100:103]\n"
"ds_read_b128 v[120:123], v5 offset:4096\n"
"v_mfma_f32_16x16x32_bf16 v[104:107], v[72:75], v[186:189], v[104:107]\n"
"s_waitcnt lgkmcnt(3)\n"
"ds_read_b128 v[186:189], v5 offset:10240\n"
"v_mfma_f32_16x16x32_bf16 v[178:181], v[64:67], v[190:193], v[178:181]\n"
"ds_read_b128 v[124:127], v4 offset:16384\n"
"v_mfma_f32_16x16x32_bf16 v[108:111], v[68:71], v[190:193], v[108:111]\n"
"ds_read_b128 v[128:131], v5 offset:16384\n"
"v_mfma_f32_16x16x32_bf16 v[112:115], v[72:75], v[190:193], v[112:115]\n"
"s_waitcnt lgkmcnt(5)\n"
"ds_read_b128 v[190:193], v4 offset:22528\n"
"v_mfma_f32_16x16x32_bf16 v[196:199], v[76:79], v[116:119], v[196:199]\n"
"ds_read_b128 v[132:135], v5 offset:22528\n"
"v_mfma_f32_16x16x32_bf16 v[212:215], v[80:83], v[116:119], v[212:215]\n"
"ds_read_b128 v[136:139], v4 offset:28672\n"
"ds_read_b128 v[140:143], v5 offset:28672\n"
"ds_read_b128 v[144:147], v4 offset:34816\n"
"ds_read_b128 v[148:151], v5 offset:34816\n"
"v_mfma_f32_16x16x32_bf16 v[216:219], v[84:87], v[116:119], v[216:219]\n"
"s_waitcnt lgkmcnt(10)\n"
"v_mfma_f32_16x16x32_bf16 v[202:205], v[76:79], v[182:185], v[202:205]\n"
"v_mfma_f32_16x16x32_bf16 v[220:223], v[80:83], v[182:185], v[220:223]\n"
"v_mfma_f32_16x16x32_bf16 v[236:239], v[84:87], v[182:185], v[236:239]\n"
"s_waitcnt lgkmcnt(7)\n"
"v_mfma_f32_16x16x32_bf16 v[226:229], v[76:79], v[124:127], v[226:229]\n"
"v_mfma_f32_16x16x32_bf16 v[240:243], v[80:83], v[124:127], v[240:243]\n"
"v_mfma_f32_16x16x32_bf16 v[244:247], v[84:87], v[124:127], v[244:247]\n"
"s_waitcnt lgkmcnt(5)\n"
"v_mfma_f32_16x16x32_bf16 v[232:235], v[76:79], v[190:193], v[232:235]\n"
"v_mfma_f32_16x16x32_bf16 v[248:251], v[80:83], v[190:193], v[248:251]\n"
"v_mfma_f32_16x16x32_bf16 v[252:255], v[84:87], v[190:193], v[252:255]\n"
"s_waitcnt lgkmcnt(3)\n"
"v_mfma_f32_16x16x32_bf16 v[208:211], v[76:79], v[136:139], v[208:211]\n"
"v_mfma_f32_16x16x32_bf16 v[100:103], v[80:83], v[136:139], v[100:103]\n"
"v_mfma_f32_16x16x32_bf16 v[104:107], v[84:87], v[136:139], v[104:107]\n"
"s_waitcnt lgkmcnt(1)\n"
"v_mfma_f32_16x16x32_bf16 v[178:181], v[76:79], v[144:147], v[178:181]\n"
"v_mfma_f32_16x16x32_bf16 v[108:111], v[80:83], v[144:147], v[108:111]\n"
"v_mfma_f32_16x16x32_bf16 v[112:115], v[84:87], v[144:147], v[112:115]\n"
"v_mfma_f32_16x16x32_bf16 v[196:199], v[88:91], v[120:123], v[196:199]\n"
"v_mfma_f32_16x16x32_bf16 v[212:215], v[92:95], v[120:123], v[212:215]\n"
"v_mfma_f32_16x16x32_bf16 v[216:219], v[96:99], v[120:123], v[216:219]\n"
"v_mfma_f32_16x16x32_bf16 v[202:205], v[88:91], v[186:189], v[202:205]\n"
"v_mfma_f32_16x16x32_bf16 v[220:223], v[92:95], v[186:189], v[220:223]\n"
"v_mfma_f32_16x16x32_bf16 v[236:239], v[96:99], v[186:189], v[236:239]\n"
"v_mfma_f32_16x16x32_bf16 v[226:229], v[88:91], v[128:131], v[226:229]\n"
"v_mfma_f32_16x16x32_bf16 v[240:243], v[92:95], v[128:131], v[240:243]\n"
"v_mfma_f32_16x16x32_bf16 v[244:247], v[96:99], v[128:131], v[244:247]\n"
"v_cvt_pk_bf16_f32 v196, v196, v197\n"
"v_cvt_pk_bf16_f32 v197, v198, v199\n"
"v_cvt_pk_bf16_f32 v198, v212, v213\n"
"v_cvt_pk_bf16_f32 v199, v214, v215\n"
"v_cvt_pk_bf16_f32 v200, v216, v217\n"
"v_cvt_pk_bf16_f32 v201, v218, v219\n"
"v_cvt_pk_bf16_f32 v202, v202, v203\n"
"v_cvt_pk_bf16_f32 v203, v204, v205\n"
"v_cvt_pk_bf16_f32 v204, v220, v221\n"
"v_cvt_pk_bf16_f32 v205, v222, v223\n"
"v_cvt_pk_bf16_f32 v206, v236, v237\n"
"v_cvt_pk_bf16_f32 v207, v238, v239\n"
"v_cvt_pk_bf16_f32 v226, v226, v227\n"
"v_cvt_pk_bf16_f32 v227, v228, v229\n"
"v_mfma_f32_16x16x32_bf16 v[232:235], v[88:91], v[132:135], v[232:235]\n"
"v_cvt_pk_bf16_f32 v228, v240, v241\n"
"v_cvt_pk_bf16_f32 v229, v242, v243\n"
"v_mfma_f32_16x16x32_bf16 v[248:251], v[92:95], v[132:135], v[248:251]\n"
"v_cvt_pk_bf16_f32 v230, v244, v245\n"
"v_cvt_pk_bf16_f32 v231, v246, v247\n"
"v_mfma_f32_16x16x32_bf16 v[252:255], v[96:99], v[132:135], v[252:255]\n"
"v_mfma_f32_16x16x32_bf16 v[208:211], v[88:91], v[140:143], v[208:211]\n"
"v_mfma_f32_16x16x32_bf16 v[100:103], v[92:95], v[140:143], v[100:103]\n"
"v_cvt_pk_bf16_f32 v232, v232, v233\n"
"v_cvt_pk_bf16_f32 v233, v234, v235\n"
"v_mfma_f32_16x16x32_bf16 v[104:107], v[96:99], v[140:143], v[104:107]\n"
"s_waitcnt lgkmcnt(0)\n"
"v_cvt_pk_bf16_f32 v234, v248, v249\n"
"v_cvt_pk_bf16_f32 v235, v250, v251\n"
"v_mfma_f32_16x16x32_bf16 v[178:181], v[88:91], v[148:151], v[178:181]\n"
"v_cvt_pk_bf16_f32 v236, v252, v253\n"
"v_cvt_pk_bf16_f32 v237, v254, v255\n"
"v_cvt_pk_bf16_f32 v208, v208, v209\n"
"v_cvt_pk_bf16_f32 v209, v210, v211\n"
"v_cvt_pk_bf16_f32 v210, v100, v101\n"
"v_cvt_pk_bf16_f32 v211, v102, v103\n"
"v_mfma_f32_16x16x32_bf16 v[108:111], v[92:95], v[148:151], v[108:111]\n"
"v_cvt_pk_bf16_f32 v212, v104, v105\n"
"v_cvt_pk_bf16_f32 v213, v106, v107\n"
"v_mfma_f32_16x16x32_bf16 v[112:115], v[96:99], v[148:151], v[112:115]\n"
"s_add_u32 s42, s46, 147456\n"
"s_addc_u32 s43, s47, 0\n"
"s_add_u32 m0, s50, 4294942720\n"
"s_nop 0\n"
"global_load_lds_dwordx4 v0, s[42:43]\n"
"s_add_u32 m0, s50, 4294943744\n"
"s_nop 0\n"
"global_load_lds_dwordx4 v1, s[42:43]\n"
"s_add_u32 m0, s50, 4294944768\n"
"s_nop 0\n"
"global_load_lds_dwordx4 v2, s[42:43]\n"
"s_add_u32 s42, s46, 184320\n"
"s_addc_u32 s43, s47, 0\n"
"s_add_u32 m0, s50, 4294945792\n"
"s_nop 0\n"
"global_load_lds_dwordx4 v0, s[42:43]\n"
"v_cvt_pk_bf16_f32 v178, v178, v179\n"
"s_add_u32 m0, s50, 4294946816\n"
"s_nop 0\n"
"global_load_lds_dwordx4 v1, s[42:43]\n"
"s_add_u32 m0, s50, 4294947840\n"
"s_nop 0\n"
"global_load_lds_dwordx4 v2, s[42:43]\n"
"s_barrier\n"
"s_add_u32 s42, s46, 0\n"
"s_addc_u32 s43, s47, 0\n"
"s_add_u32 m0, s50, 24576\n"
"s_nop 0\n"
"global_load_lds_dwordx4 v0, s[42:43]\n"
"s_add_u32 m0, s50, 25600\n"
"s_nop 0\n"
"global_load_lds_dwordx4 v1, s[42:43]\n"
"s_add_u32 m0, s50, 26624\n"
"s_nop 0\n"
"global_load_lds_dwordx4 v2, s[42:43]\n"
"s_add_u32 s42, s46, 36864\n"
"s_addc_u32 s43, s47, 0\n"
"s_add_u32 m0, s50, 27648\n"
"s_nop 0\n"
"global_load_lds_dwordx4 v0, s[42:43]\n"
"v_cvt_pk_bf16_f32 v179, v180, v181\n"
"s_add_u32 m0, s50, 28672\n"
"s_nop 0\n"
"global_load_lds_dwordx4 v1, s[42:43]\n"
"s_add_u32 m0, s50, 29696\n"
"s_nop 0\n"
"global_load_lds_dwordx4 v2, s[42:43]\n"
"s_add_u32 s42, s46, 73728\n"
"s_addc_u32 s43, s47, 0\n"
"s_add_u32 m0, s50, 49152\n"
"s_nop 0\n"
"global_load_lds_dwordx4 v0, s[42:43]\n"
"v_cvt_pk_bf16_f32 v180, v108, v109\n"
"s_add_u32 m0, s50, 50176\n"
"s_nop 0\n"
"global_load_lds_dwordx4 v1, s[42:43]\n"
"s_add_u32 m0, s50, 51200\n"
"s_nop 0\n"
"global_load_lds_dwordx4 v2, s[42:43]\n"
"s_add_u32 s42, s46, 110592\n"
"s_addc_u32 s43, s47, 0\n"
"s_add_u32 m0, s50, 52224\n"
"s_nop 0\n"
"global_load_lds_dwordx4 v0, s[42:43]\n"
"v_cvt_pk_bf16_f32 v181, v110, v111\n"
"s_add_u32 m0, s50, 53248\n"
"s_nop 0\n"
"global_load_lds_dwordx4 v1, s[42:43]\n"
"s_add_u32 m0, s50, 54272\n"
"s_nop 0\n"
"global_load_lds_dwordx4 v2, s[42:43]\n"
"ds_read_u16 v13, v13 offset:0\n"
"v_cvt_pk_bf16_f32 v182, v112, v113\n"
"ds_read_u16 v23, v23 offset:0\n"
"v_cvt_pk_bf16_f32 v183, v114, v115\n"
"ds_read_u16 v14, v14 offset:0\n"
"ds_read_u16 v15, v15 offset:0\n"
"ds_read_u16 v16, v16 offset:0\n"
"ds_read_u16 v17, v17 offset:0\n"
"ds_read_u16 v18, v18 offset:0\n"
"ds_read_u16 v12, v12 offset:0\n"
"s_waitcnt lgkmcnt(7)\n"
"v_lshlrev_b32 v13, 16, v13\n"
"ds_read_u16 v20, v20 offset:0\n"
"s_waitcnt lgkmcnt(7)\n"
"v_lshlrev_b32 v23, 16, v23\n"
"ds_read_u16 v21, v21 offset:0\n"
"v_mul_f32 v13, s61, v13\n"
"ds_read_u16 v22, v22 offset:0\n"
"s_waitcnt lgkmcnt(8)\n"
"v_lshlrev_b32 v14, 16, v14\n"
"v_mul_f32 v23, s61, v23\n"
"v_mul_f32 v14, s61, v14\n"
"s_waitcnt lgkmcnt(7)\n"
"v_lshlrev_b32 v15, 16, v15\n"
"v_mov_b32 v184, v13\n"
"v_mul_f32 v15, s61, v15\n"
"s_waitcnt lgkmcnt(6)\n"
"v_lshlrev_b32 v16, 16, v16\n"
"v_mov_b32 v185, v14\n"
"v_mul_f32 v16, s61, v16\n"
"s_waitcnt lgkmcnt(5)\n"
"v_lshlrev_b32 v17, 16, v17\n"
"v_mov_b32 v186, v15\n"
"v_mul_f32 v17, s61, v17\n"
"s_waitcnt lgkmcnt(4)\n"
"v_lshlrev_b32 v18, 16, v18\n"
"v_mov_b32 v187, v16\n"
"v_mul_f32 v18, s61, v18\n"
"v_mov_b32 v188, v17\n"
"v_mov_b32 v189, v18\n"
"v_mov_b32 v190, 0xf1800000\n"
"v_mov_b32 v191, 0xf1800000\n"
"v_mov_b32 v192, 0xf1800000\n"
"v_mov_b32 v193, 0xf1800000\n"
"s_waitcnt vmcnt(15)\n"
"ds_read_b64_tr_b16 v[214:215], v3 offset:4160\n"
"ds_read_b64_tr_b16 v[216:217], v3 offset:4928\n"
"s_waitcnt vmcnt(12)\n"
"ds_read_b64_tr_b16 v[100:101], v3 offset:4096\n"
"ds_read_b64_tr_b16 v[102:103], v3 offset:4864\n"
"ds_read_b64_tr_b16 v[104:105], v3 offset:4128\n"
"ds_read_b64_tr_b16 v[106:107], v3 offset:4896\n"
"ds_read_b64_tr_b16 v[108:109], v3 offset:7168\n"
"ds_read_b64_tr_b16 v[110:111], v3 offset:7936\n"
"ds_read_b64_tr_b16 v[112:113], v3 offset:7200\n"
"ds_read_b64_tr_b16 v[114:115], v3 offset:7968\n"
"ds_read_b64_tr_b16 v[218:219], v3 offset:7232\n"
"ds_read_b64_tr_b16 v[220:221], v3 offset:8000\n"
"s_waitcnt lgkmcnt(15)\n"
"v_lshlrev_b32 v12, 16, v12\n"
"s_waitcnt lgkmcnt(14)\n"
"v_lshlrev_b32 v20, 16, v20\n"
"s_waitcnt lgkmcnt(13)\n"
"v_lshlrev_b32 v21, 16, v21\n"
"s_waitcnt lgkmcnt(12)\n"
"v_lshlrev_b32 v22, 16, v22\n"
"v_mul_f32 v12, s61, v12\n"
"v_mul_f32 v20, s61, v20\n"
"v_mul_f32 v21, s61, v21\n"
"v_mul_f32 v22, s61, v22\n"
"s_waitcnt lgkmcnt(0)\n"
"s_barrier\n"
"s_waitcnt vmcnt(9)\n"
"ds_read_b64_tr_b16 v[116:117], v3 offset:53248\n"
"ds_read_b64_tr_b16 v[118:119], v3 offset:54016\n"
"ds_read_b128 v[222:225], v4 offset:36864\n"
"ds_read_b64_tr_b16 v[120:121], v3 offset:53280\n"
"ds_read_b64_tr_b16 v[122:123], v3 offset:54048\n"
"ds_read_b64_tr_b16 v[238:239], v3 offset:53312\n"
"ds_read_b64_tr_b16 v[240:241], v3 offset:54080\n"
"s_waitcnt vmcnt(6)\n"
"ds_read_b64_tr_b16 v[124:125], v3 offset:56320\n"
"ds_read_b64_tr_b16 v[126:127], v3 offset:57088\n"
"ds_read_b128 v[242:245], v5 offset:36864\n"
"ds_read_b64_tr_b16 v[128:129], v3 offset:56352\n"
"ds_read_b64_tr_b16 v[130:131], v3 offset:57120\n"
"ds_write_b128 v8, v[186:189] offset:0\n"
"s_waitcnt lgkmcnt(10)\n"
"v_mfma_f32_16x16x32_bf16 v[246:249], v[116:119], v[222:225], 0\n"
"s_waitcnt lgkmcnt(8)\n"
"ds_read_b64_tr_b16 v[250:251], v3 offset:56384\n"
"ds_read_b64_tr_b16 v[252:253], v3 offset:57152\n"
"v_mfma_f32_16x16x32_bf16 v[140:143], v[120:123], v[222:225], 0\n"
"s_waitcnt vmcnt(3)\n"
"ds_read_b64_tr_b16 v[0:1], v11 offset:0\n"
"ds_read_b64_tr_b16 v[2:3], v11 offset:768\n"
"s_waitcnt lgkmcnt(10)\n"
"v_mfma_f32_16x16x32_bf16 v[144:147], v[238:241], v[222:225], 0\n"
"s_waitcnt lgkmcnt(7)\n"
"ds_read_b128 v[222:225], v4 offset:38912\n"
"v_mfma_f32_16x16x32_bf16 v[246:249], v[124:127], v[242:245], v[246:249]\n"
"ds_write_b128 v8, v[14:17] offset:64\n"
"ds_read_b128 v[148:151], v5 offset:38912\n"
"ds_read_b64_tr_b16 v[16:17], v11 offset:32\n"
"ds_read_b64_tr_b16 v[18:19], v11 offset:800\n"
"s_waitcnt lgkmcnt(10)\n"
"v_mfma_f32_16x16x32_bf16 v[140:143], v[128:131], v[242:245], v[140:143]\n"
"ds_read_b64_tr_b16 v[152:153], v11 offset:64\n"
"ds_read_b64_tr_b16 v[154:155], v11 offset:832\n"
"s_waitcnt vmcnt(0)\n"
"ds_write_b128 v8, v[184:187] offset:128\n"
"s_waitcnt lgkmcnt(10)\n"
"v_mfma_f32_16x16x32_bf16 v[144:147], v[250:253], v[242:245], v[144:147]\n"
"s_waitcnt lgkmcnt(7)\n"
"ds_read_b64_tr_b16 v[132:133], v11 offset:3072\n"
"ds_read_b64_tr_b16 v[134:135], v11 offset:3840\n"
"v_mfma_f32_16x16x32_bf16 v[246:249], v[0:3], v[222:225], v[246:249]\n"
"ds_read_b64_tr_b16 v[136:137], v11 offset:3104\n"
"ds_read_b64_tr_b16 v[138:139], v11 offset:3872\n"
"ds_write_b128 v8, v[12:15] offset:192\n"
"ds_read_b128 v[184:187], v4 offset:40960\n"
"s_waitcnt lgkmcnt(9)\n"
"v_mfma_f32_16x16x32_bf16 v[140:143], v[16:19], v[222:225], v[140:143]\n"
"s_waitcnt lgkmcnt(7)\n"
"ds_read_b64_tr_b16 v[242:243], v11 offset:3136\n"
"ds_read_b64_tr_b16 v[244:245], v11 offset:3904\n"
"v_mfma_f32_16x16x32_bf16 v[144:147], v[152:155], v[222:225], v[144:147]\n"
"ds_write_b128 v8, v[20:23] offset:256\n"
"s_waitcnt lgkmcnt(7)\n"
"v_mfma_f32_16x16x32_bf16 v[246:249], v[132:135], v[148:151], v[246:249]\n"
"ds_write_b128 v9, v[190:193] offset:0\n"
"ds_read_b128 v[188:191], v5 offset:40960\n"
"s_waitcnt lgkmcnt(7)\n"
"v_mfma_f32_16x16x32_bf16 v[140:143], v[136:139], v[148:151], v[140:143]\n"
"s_waitcnt lgkmcnt(3)\n"
"s_barrier\n"
"v_mfma_f32_16x16x32_bf16 v[144:147], v[242:245], v[148:151], v[144:147]\n"
"v_mfma_f32_16x16x32_bf16 v[246:249], v[100:103], v[184:187], v[246:249]\n"
"ds_read_b128 v[192:195], v4 offset:43008\n"
"v_mfma_f32_16x16x32_bf16 v[140:143], v[104:107], v[184:187], v[140:143]\n"
"ds_write_b128 v6, v[238:241] offset:4096\n"
"v_bfe_u32 v241, v6, 2, 6\n"
"v_mfma_f32_16x16x32_bf16 v[144:147], v[214:217], v[184:187], v[144:147]\n"
"s_waitcnt lgkmcnt(2)\n"
"ds_read_b32 v241, v241 offset:1216\n"
"v_mfma_f32_16x16x32_bf16 v[246:249], v[108:111], v[188:191], v[246:249]\n"
"s_add_u32 s68, s50, 0\n"
"ds_read_b128 v[184:187], v5 offset:43008\n"
"v_mfma_f32_16x16x32_bf16 v[140:143], v[112:115], v[188:191], v[140:143]\n"
"ds_write_b128 v6, v[250:253] offset:5120\n"
"v_mfma_f32_16x16x32_bf16 v[144:147], v[218:221], v[188:191], v[144:147]\n"
"ds_write_b128 v6, v[152:155] offset:6144\n"
"s_waitcnt lgkmcnt(3)\n"
"v_add_u32_sdwa v225, v241, v7 dst_sel:DWORD dst_unused:UNUSED_PAD src0_sel:WORD_0 src1_sel:DWORD\n"
"v_mfma_f32_16x16x32_bf16 v[238:241], v[28:31], v[192:195], 0\n"
"ds_read_b128 v[188:191], v4 offset:45056\n"
"v_max_u32 v224, s68, v225\n"
"v_mfma_f32_16x16x32_bf16 v[250:253], v[32:35], v[192:195], 0\n"
"ds_write_b128 v6, v[242:245] offset:7168\n"
"v_mul_f32 v246, s60, v246\n"
"v_mfma_f32_16x16x32_bf16 v[8:11], v[36:39], v[192:195], 0\n"
"s_waitcnt lgkmcnt(4)\n"
"ds_read_b128 v[192:195], v224 offset:16\n"
"v_mul_f32 v247, s60, v247\n"
"v_mfma_f32_16x16x32_bf16 v[238:241], v[40:43], v[184:187], v[238:241]\n"
"ds_write_b128 v6, v[214:217] offset:8192\n"
"v_cvt_pk_bf16_f32 v246, v246, v247\n"
"v_mfma_f32_16x16x32_bf16 v[250:253], v[44:47], v[184:187], v[250:253]\n"
"ds_write_b128 v6, v[218:221] offset:9216\n"
"v_mul_f32 v248, s60, v248\n"
"v_mfma_f32_16x16x32_bf16 v[8:11], v[48:51], v[184:187], v[8:11]\n"
"s_waitcnt lgkmcnt(4)\n"
"ds_read_b128 v[184:187], v5 offset:45056\n"
"v_mul_f32 v249, s60, v249\n"
"v_mfma_f32_16x16x32_bf16 v[238:241], v[52:55], v[188:191], v[238:241]\n"
"v_cvt_pk_bf16_f32 v247, v248, v249\n"
"v_mul_f32 v140, s60, v140\n"
"v_mfma_f32_16x16x32_bf16 v[250:253], v[56:59], v[188:191], v[250:253]\n"
"v_mul_f32 v141, s60, v141\n"
"v_mul_f32 v142, s60, v142\n"
"v_mul_f32 v143, s60, v143\n"
"v_cvt_pk_bf16_f32 v248, v140, v141\n"
"v_cvt_pk_bf16_f32 v249, v142, v143\n"
"v_mul_f32 v144, s60, v144\n"
"v_mul_f32 v145, s60, v145\n"
"v_mul_f32 v146, s60, v146\n"
"s_waitcnt lgkmcnt(3)\n"
"v_mfma_f32_16x16x32_bf16 v[192:195], v[202:205], v[246:249], v[192:195]\n"
"ds_read_b128 v[214:217], v4 offset:47104\n"
"v_mul_f32 v147, s60, v147\n"
"v_cvt_pk_bf16_f32 v26, v144, v145\n"
"v_cvt_pk_bf16_f32 v27, v146, v147\n"
"s_nop 1\n"
"v_mfma_f32_16x16x32_bf16 v[192:195], v[204:207], v[24:27], v[192:195]\n"
"v_mfma_f32_16x16x32_bf16 v[8:11], v[60:63], v[188:191], v[8:11]\n"
"s_waitcnt lgkmcnt(1)\n"
"v_mfma_f32_16x16x32_bf16 v[238:241], v[64:67], v[184:187], v[238:241]\n"
"ds_read_b128 v[188:191], v5 offset:47104\n"
"v_mfma_f32_16x16x32_bf16 v[250:253], v[68:71], v[184:187], v[250:253]\n"
"ds_read_b128 v[218:221], v224 offset:32\n"
"v_mfma_f32_16x16x32_bf16 v[8:11], v[72:75], v[184:187], v[8:11]\n"
"s_waitcnt lgkmcnt(2)\n"
"v_mfma_f32_16x16x32_bf16 v[238:241], v[76:79], v[214:217], v[238:241]\n"
"v_mfma_f32_16x16x32_bf16 v[250:253], v[80:83], v[214:217], v[250:253]\n"
"v_exp_f32 v192, v192\n"
"v_mfma_f32_16x16x32_bf16 v[8:11], v[84:87], v[214:217], v[8:11]\n"
"s_add_u32 s69, s50, 1040\n"
"ds_read_b128 v[214:217], v4 offset:49152\n"
"v_exp_f32 v193, v193\n"
"v_exp_f32 v194, v194\n"
"s_waitcnt lgkmcnt(2)\n"
"v_mfma_f32_16x16x32_bf16 v[238:241], v[88:91], v[188:191], v[238:241]\n"
"s_waitcnt lgkmcnt(1)\n"
"v_exp_f32 v195, v195\n"
"v_mfma_f32_16x16x32_bf16 v[218:221], v[208:211], v[246:249], v[218:221]\n"
"v_cvt_pk_bf16_f32 v192, v192, v193\n"
"v_cvt_pk_bf16_f32 v193, v194, v195\n"
"v_mfma_f32_16x16x32_bf16 v[218:221], v[210:213], v[24:27], v[218:221]\n"
"s_nop 7\n"
"ds_read_b128 v[220:223], v5 offset:49152\n"
"v_min_u32 v245, s69, v225\n"
"v_mfma_f32_16x16x32_bf16 v[250:253], v[92:95], v[188:191], v[250:253]\n"
"ds_write_b64 v6, v[192:193] offset:53248\n"
"v_cvt_pk_bf16_f32 v238, v238, v239\n"
"v_mfma_f32_16x16x32_bf16 v[8:11], v[96:99], v[188:191], v[8:11]\n"
"s_waitcnt lgkmcnt(2)\n"
"ds_read_b128 v[188:191], v245 offset:1344\n"
"v_cvt_pk_bf16_f32 v239, v240, v241\n"
"v_mfma_f32_16x16x32_bf16 v[184:187], v[28:31], v[214:217], 0\n"
"v_exp_f32 v218, v218\n"
"v_mfma_f32_16x16x32_bf16 v[12:15], v[32:35], v[214:217], 0\n"
"v_cvt_pk_bf16_f32 v240, v250, v251\n"
"v_cvt_pk_bf16_f32 v241, v252, v253\n"
"v_mfma_f32_16x16x32_bf16 v[250:253], v[36:39], v[214:217], 0\n"
"s_waitcnt lgkmcnt(2)\n"
"ds_read_b128 v[192:195], v4 offset:51200\n"
"v_cvt_pk_bf16_f32 v242, v8, v9\n"
"v_cvt_pk_bf16_f32 v243, v10, v11\n"
"v_mfma_f32_16x16x32_bf16 v[184:187], v[40:43], v[220:223], v[184:187]\n"
"v_exp_f32 v219, v219\n"
"v_mfma_f32_16x16x32_bf16 v[12:15], v[44:47], v[220:223], v[12:15]\n"
"s_waitcnt lgkmcnt(1)\n"
"v_cvt_pk_bf16_f32 v218, v218, v219\n"
"v_mov_b32 v219, 0\n"
"v_mfma_f32_16x16x32_bf16 v[188:191], v[226:229], v[246:249], v[188:191]\n"
"ds_read_b128 v[214:217], v5 offset:51200\n"
"v_mfma_f32_16x16x32_bf16 v[188:191], v[228:231], v[24:27], v[188:191]\n"
"ds_write_b64 v6, v[218:219] offset:53256\n"
"v_mfma_f32_16x16x32_bf16 v[250:253], v[48:51], v[220:223], v[250:253]\n"
"s_waitcnt lgkmcnt(2)\n"
"v_mfma_f32_16x16x32_bf16 v[184:187], v[52:55], v[192:195], v[184:187]\n"
"ds_read_b128 v[218:221], v245 offset:1360\n"
"v_mfma_f32_16x16x32_bf16 v[12:15], v[56:59], v[192:195], v[12:15]\n"
"v_mfma_f32_16x16x32_bf16 v[250:253], v[60:63], v[192:195], v[250:253]\n"
"s_waitcnt lgkmcnt(2)\n"
"ds_read_b128 v[192:195], v4 offset:53248\n"
"v_mfma_f32_16x16x32_bf16 v[184:187], v[64:67], v[214:217], v[184:187]\n"
"v_exp_f32 v188, v188\n"
"v_mfma_f32_16x16x32_bf16 v[12:15], v[68:71], v[214:217], v[12:15]\n"
"v_exp_f32 v189, v189\n"
"v_mfma_f32_16x16x32_bf16 v[250:253], v[72:75], v[214:217], v[250:253]\n"
"s_waitcnt lgkmcnt(1)\n"
"ds_read_b128 v[214:217], v5 offset:53248\n"
"v_cvt_pk_bf16_f32 v188, v188, v189\n"
"v_exp_f32 v190, v190\n"
"v_mfma_f32_16x16x32_bf16 v[218:221], v[232:235], v[246:249], v[218:221]\n"
"v_exp_f32 v191, v191\n"
"v_mfma_f32_16x16x32_bf16 v[218:221], v[234:237], v[24:27], v[218:221]\n"
"s_waitcnt lgkmcnt(1)\n"
"v_cvt_pk_bf16_f32 v189, v190, v191\n"
"v_mfma_f32_16x16x32_bf16 v[184:187], v[76:79], v[192:195], v[184:187]\n"
"ds_read_b128 v[8:11], v4 offset:55296\n"
"v_mfma_f32_16x16x32_bf16 v[12:15], v[80:83], v[192:195], v[12:15]\n"
"ds_read_b128 v[20:23], v224 offset:0\n"
"v_mfma_f32_16x16x32_bf16 v[250:253], v[84:87], v[192:195], v[250:253]\n"
"ds_write_b64 v6, v[188:189] offset:54272\n"
"s_waitcnt lgkmcnt(3)\n"
"v_mfma_f32_16x16x32_bf16 v[184:187], v[88:91], v[214:217], v[184:187]\n"
"v_exp_f32 v218, v218\n"
"v_mfma_f32_16x16x32_bf16 v[12:15], v[92:95], v[214:217], v[12:15]\n"
"v_exp_f32 v219, v219\n"
"v_mfma_f32_16x16x32_bf16 v[250:253], v[96:99], v[214:217], v[250:253]\n"
"s_add_u32 s70, s50, 4294966992\n"
"ds_read_b128 v[214:217], v5 offset:55296\n"
"v_cvt_pk_bf16_f32 v218, v218, v219\n"
"v_exp_f32 v220, v220\n"
"v_cvt_pk_bf16_f32 v184, v184, v185\n"
"v_cvt_pk_bf16_f32 v185, v186, v187\n"
"s_waitcnt lgkmcnt(3)\n"
"v_mfma_f32_16x16x32_bf16 v[190:193], v[28:31], v[8:11], 0\n"
"s_waitcnt lgkmcnt(2)\n"
"v_cvt_pk_bf16_f32 v186, v12, v13\n"
"v_cvt_pk_bf16_f32 v187, v14, v15\n"
"v_mfma_f32_16x16x32_bf16 v[20:23], v[196:199], v[246:249], v[20:23]\n"
"v_cvt_pk_bf16_f32 v188, v250, v251\n"
"v_cvt_pk_bf16_f32 v189, v252, v253\n"
"v_mfma_f32_16x16x32_bf16 v[20:23], v[198:201], v[24:27], v[20:23]\n"
"ds_read_b128 v[194:197], v4 offset:57344\n"
"v_min_u32 v244, s70, v225\n"
"v_mfma_f32_16x16x32_bf16 v[198:201], v[32:35], v[8:11], 0\n"
"v_exp_f32 v221, v221\n"
"v_mfma_f32_16x16x32_bf16 v[222:225], v[36:39], v[8:11], 0\n"
"s_waitcnt lgkmcnt(1)\n"
"ds_read_b128 v[250:253], v244 offset:2688\n"
"v_cvt_pk_bf16_f32 v219, v220, v221\n"
"v_mfma_f32_16x16x32_bf16 v[190:193], v[40:43], v[214:217], v[190:193]\n"
"ds_write_b64 v6, v[218:219] offset:54280\n"
"v_mfma_f32_16x16x32_bf16 v[198:201], v[44:47], v[214:217], v[198:201]\n"
"v_exp_f32 v20, v20\n"
"v_mfma_f32_16x16x32_bf16 v[222:225], v[48:51], v[214:217], v[222:225]\n"
"s_waitcnt lgkmcnt(2)\n"
"ds_read_b128 v[214:217], v5 offset:57344\n"
"v_exp_f32 v21, v21\n"
"v_mfma_f32_16x16x32_bf16 v[190:193], v[52:55], v[194:197], v[190:193]\n"
"s_waitcnt lgkmcnt(1)\n"
"s_barrier\n"
"v_exp_f32 v22, v22\n"
"v_mfma_f32_16x16x32_bf16 v[198:201], v[56:59], v[194:197], v[198:201]\n"
"v_exp_f32 v23, v23\n"
"v_mfma_f32_16x16x32_bf16 v[250:253], v[178:181], v[246:249], v[250:253]\n"
"ds_read_b128 v[218:221], v4 offset:59392\n"
"v_cvt_pk_bf16_f32 v20, v20, v21\n"
"v_mfma_f32_16x16x32_bf16 v[250:253], v[180:183], v[24:27], v[250:253]\n"
"v_cvt_pk_bf16_f32 v21, v22, v23\n"
"v_mfma_f32_16x16x32_bf16 v[222:225], v[60:63], v[194:197], v[222:225]\n"
"ds_write_b64 v6, v[20:21] offset:58376\n"
"s_waitcnt lgkmcnt(2)\n"
"v_mfma_f32_16x16x32_bf16 v[190:193], v[64:67], v[214:217], v[190:193]\n"
"ds_read_b128 v[8:11], v245 offset:1376\n"
"v_mfma_f32_16x16x32_bf16 v[198:201], v[68:71], v[214:217], v[198:201]\n"
"v_mfma_f32_16x16x32_bf16 v[222:225], v[72:75], v[214:217], v[222:225]\n"
"s_waitcnt lgkmcnt(2)\n"
"ds_read_b128 v[194:197], v5 offset:59392\n"
"v_exp_f32 v250, v250\n"
"v_mfma_f32_16x16x32_bf16 v[190:193], v[76:79], v[218:221], v[190:193]\n"
"v_exp_f32 v251, v251\n"
"v_mfma_f32_16x16x32_bf16 v[198:201], v[80:83], v[218:221], v[198:201]\n"
"v_exp_f32 v252, v252\n"
"v_mfma_f32_16x16x32_bf16 v[222:225], v[84:87], v[218:221], v[222:225]\n"
"s_waitcnt lgkmcnt(1)\n"
"ds_read_b128 v[214:217], v4 offset:12288\n"
"v_cvt_pk_bf16_f32 v250, v250, v251\n"
"v_exp_f32 v253, v253\n"
"v_mfma_f32_16x16x32_bf16 v[8:11], v[238:241], v[246:249], v[8:11]\n"
"v_cvt_pk_bf16_f32 v251, v252, v253\n"
"v_mfma_f32_16x16x32_bf16 v[8:11], v[240:243], v[24:27], v[8:11]\n"
"s_waitcnt lgkmcnt(1)\n"
"ds_read_b128 v[218:221], v6 offset:4096\n"
"v_mfma_f32_16x16x32_bf16 v[190:193], v[88:91], v[194:197], v[190:193]\n"
"ds_read_b128 v[252:255], v5 offset:12288\n"
"v_mfma_f32_16x16x32_bf16 v[198:201], v[92:95], v[194:197], v[198:201]\n"
"ds_read_b128 v[12:15], v244 offset:2704\n"
"v_mfma_f32_16x16x32_bf16 v[222:225], v[96:99], v[194:197], v[222:225]\n"
"ds_write_b64 v6, v[250:251] offset:57344\n"
"s_waitcnt lgkmcnt(4)\n"
"v_mfma_f32_16x16x32_bf16 v[20:23], v[116:119], v[214:217], 0\n"
"v_exp_f32 v8, v8\n"
"v_mfma_f32_16x16x32_bf16 v[140:143], v[120:123], v[214:217], 0\n"
"ds_read_b128 v[144:147], v6 offset:5120\n"
"v_cvt_pk_bf16_f32 v190, v190, v191\n"
"v_cvt_pk_bf16_f32 v191, v192, v193\n"
"v_cvt_pk_bf16_f32 v192, v198, v199\n"
"v_cvt_pk_bf16_f32 v193, v200, v201\n"
"v_cvt_pk_bf16_f32 v194, v222, v223\n"
"v_cvt_pk_bf16_f32 v195, v224, v225\n"
"s_waitcnt lgkmcnt(4)\n"
"v_mfma_f32_16x16x32_bf16 v[196:199], v[218:221], v[214:217], 0\n"
"s_waitcnt lgkmcnt(3)\n"
"ds_read_b128 v[214:217], v4 offset:14336\n"
"v_mfma_f32_16x16x32_bf16 v[20:23], v[124:127], v[252:255], v[20:23]\n"
"s_waitcnt lgkmcnt(3)\n"
"v_exp_f32 v9, v9\n"
"v_mfma_f32_16x16x32_bf16 v[12:15], v[184:187], v[246:249], v[12:15]\n"
"ds_read_b128 v[218:221], v6 offset:6144\n"
"v_cvt_pk_bf16_f32 v8, v8, v9\n"
"v_mfma_f32_16x16x32_bf16 v[12:15], v[186:189], v[24:27], v[12:15]\n"
"ds_read_b128 v[222:225], v5 offset:14336\n"
"v_mov_b32 v9, 0\n"
"v_mfma_f32_16x16x32_bf16 v[140:143], v[128:131], v[252:255], v[140:143]\n"
"ds_write_b64 v6, v[8:9] offset:33280\n"
"s_waitcnt lgkmcnt(4)\n"
"v_mfma_f32_16x16x32_bf16 v[196:199], v[144:147], v[252:255], v[196:199]\n"
"s_waitcnt lgkmcnt(3)\n"
"ds_read_b128 v[250:253], v244 offset:2720\n"
"v_mfma_f32_16x16x32_bf16 v[20:23], v[0:3], v[214:217], v[20:23]\n"
"v_mfma_f32_16x16x32_bf16 v[140:143], v[16:19], v[214:217], v[140:143]\n"
"s_waitcnt lgkmcnt(3)\n"
"ds_read_b128 v[8:11], v6 offset:7168\n"
"v_mfma_f32_16x16x32_bf16 v[196:199], v[218:221], v[214:217], v[196:199]\n"
"s_waitcnt lgkmcnt(3)\n"
"ds_read_b128 v[214:217], v4 offset:16384\n"
"v_mfma_f32_16x16x32_bf16 v[20:23], v[132:135], v[222:225], v[20:23]\n"
"v_exp_f32 v12, v12\n"
"v_mfma_f32_16x16x32_bf16 v[140:143], v[136:139], v[222:225], v[140:143]\n"
"s_waitcnt lgkmcnt(2)\n"
"ds_read_b128 v[218:221], v6 offset:8192\n"
"v_exp_f32 v13, v13\n"
"v_mfma_f32_16x16x32_bf16 v[250:253], v[190:193], v[246:249], v[250:253]\n"
"ds_read_b128 v[244:247], v5 offset:16384\n"
"v_cvt_pk_bf16_f32 v12, v12, v13\n"
"v_mfma_f32_16x16x32_bf16 v[250:253], v[192:195], v[24:27], v[250:253]\n"
"s_waitcnt lgkmcnt(3)\n"
"v_exp_f32 v14, v14\n"
"v_exp_f32 v15, v15\n"
"v_mfma_f32_16x16x32_bf16 v[196:199], v[8:11], v[222:225], v[196:199]\n"
"s_waitcnt lgkmcnt(2)\n"
"ds_read_b128 v[222:225], v6 offset:9216\n"
"v_cvt_pk_bf16_f32 v13, v14, v15\n"
"v_mfma_f32_16x16x32_bf16 v[20:23], v[100:103], v[214:217], v[20:23]\n"
"ds_read_b128 v[252:255], v4 offset:0\n"
"v_mfma_f32_16x16x32_bf16 v[140:143], v[104:107], v[214:217], v[140:143]\n"
"ds_write_b64 v6, v[12:13] offset:57352\n"
"v_bfe_u32 v201, v6, 2, 6\n"
"s_waitcnt lgkmcnt(4)\n"
"v_mfma_f32_16x16x32_bf16 v[196:199], v[218:221], v[214:217], v[196:199]\n"
"s_waitcnt lgkmcnt(3)\n"
"ds_read_b32 v201, v201 offset:1280\n"
"v_mfma_f32_16x16x32_bf16 v[20:23], v[108:111], v[244:247], v[20:23]\n"
"s_add_u32 s71, s50, 4294967280\n"
"ds_read_b128 v[218:221], v5 offset:0\n"
"v_mfma_f32_16x16x32_bf16 v[140:143], v[112:115], v[244:247], v[140:143]\n"
"s_waitcnt lgkmcnt(4)\n"
"v_exp_f32 v250, v250\n"
"v_exp_f32 v251, v251\n"
"v_mfma_f32_16x16x32_bf16 v[196:199], v[222:225], v[244:247], v[196:199]\n"
"s_waitcnt lgkmcnt(3)\n"
"v_cvt_pk_bf16_f32 v250, v250, v251\n"
"v_mov_b32 v251, 0\n"
"v_mfma_f32_16x16x32_bf16 v[214:217], v[28:31], v[252:255], 0\n"
"ds_read_b128 v[222:225], v4 offset:2048\n"
"v_mul_f32 v20, s60, v20\n"
"v_mfma_f32_16x16x32_bf16 v[8:11], v[32:35], v[252:255], 0\n"
"ds_write_b64 v6, v[250:251] offset:58368\n"
"s_waitcnt lgkmcnt(3)\n"
"v_add_u32_sdwa v200, v201, v7 dst_sel:DWORD dst_unused:UNUSED_PAD src0_sel:WORD_0 src1_sel:DWORD\n"
"v_mfma_f32_16x16x32_bf16 v[248:251], v[36:39], v[252:255], 0\n"
"s_waitcnt lgkmcnt(2)\n"
"v_max_u32 v201, s71, v200\n"
"v_mul_f32 v21, s60, v21\n"
"v_mfma_f32_16x16x32_bf16 v[214:217], v[40:43], v[218:221], v[214:217]\n"
"ds_read_b128 v[244:247], v201 offset:16\n"
"v_cvt_pk_bf16_f32 v20, v20, v21\n"
"v_mfma_f32_16x16x32_bf16 v[8:11], v[44:47], v[218:221], v[8:11]\n"
"v_mul_f32 v22, s60, v22\n"
"v_mul_f32 v23, s60, v23\n"
"v_mfma_f32_16x16x32_bf16 v[248:251], v[48:51], v[218:221], v[248:251]\n"
"s_waitcnt lgkmcnt(1)\n"
"s_barrier\n"
"ds_read_b128 v[218:221], v5 offset:2048\n"
"v_cvt_pk_bf16_f32 v21, v22, v23\n"
"v_mul_f32 v140, s60, v140\n"
"v_mul_f32 v141, s60, v141\n"
"v_mfma_f32_16x16x32_bf16 v[214:217], v[52:55], v[222:225], v[214:217]\n"
"v_mul_f32 v142, s60, v142\n"
"v_mul_f32 v143, s60, v143\n"
"v_mfma_f32_16x16x32_bf16 v[8:11], v[56:59], v[222:225], v[8:11]\n"
"v_cvt_pk_bf16_f32 v22, v140, v141\n"
"v_cvt_pk_bf16_f32 v23, v142, v143\n"
"v_mul_f32 v196, s60, v196\n"
"v_mul_f32 v197, s60, v197\n"
"v_mul_f32 v198, s60, v198\n"
"v_mul_f32 v199, s60, v199\n"
"s_waitcnt lgkmcnt(1)\n"
"v_mfma_f32_16x16x32_bf16 v[244:247], v[202:205], v[20:23], v[244:247]\n"
"ds_read_b128 v[252:255], v4 offset:4096\n"
"v_cvt_pk_bf16_f32 v26, v196, v197\n"
"v_cvt_pk_bf16_f32 v27, v198, v199\n"
"s_nop 1\n"
"v_mfma_f32_16x16x32_bf16 v[244:247], v[204:207], v[24:27], v[244:247]\n"
"v_mfma_f32_16x16x32_bf16 v[248:251], v[60:63], v[222:225], v[248:251]\n"
"s_waitcnt lgkmcnt(1)\n"
"v_mfma_f32_16x16x32_bf16 v[214:217], v[64:67], v[218:221], v[214:217]\n"
"ds_read_b128 v[196:199], v5 offset:4096\n"
"v_mfma_f32_16x16x32_bf16 v[8:11], v[68:71], v[218:221], v[8:11]\n"
"ds_read_b128 v[222:225], v201 offset:32\n"
"v_mfma_f32_16x16x32_bf16 v[248:251], v[72:75], v[218:221], v[248:251]\n"
"s_waitcnt lgkmcnt(2)\n"
"v_mfma_f32_16x16x32_bf16 v[214:217], v[76:79], v[252:255], v[214:217]\n"
"v_mfma_f32_16x16x32_bf16 v[8:11], v[80:83], v[252:255], v[8:11]\n"
"v_exp_f32 v244, v244\n"
"v_mfma_f32_16x16x32_bf16 v[248:251], v[84:87], v[252:255], v[248:251]\n"
"s_waitcnt lgkmcnt(1)\n"
"ds_read_b128 v[218:221], v4 offset:6144\n"
"v_exp_f32 v245, v245\n"
"v_exp_f32 v246, v246\n"
"v_mfma_f32_16x16x32_bf16 v[214:217], v[88:91], v[196:199], v[214:217]\n"
"s_add_u32 s72, s50, 1024\n"
"v_exp_f32 v247, v247\n"
"s_waitcnt lgkmcnt(1)\n"
"v_mfma_f32_16x16x32_bf16 v[222:225], v[208:211], v[20:23], v[222:225]\n"
"v_cvt_pk_bf16_f32 v244, v244, v245\n"
"v_cvt_pk_bf16_f32 v245, v246, v247\n"
"v_mfma_f32_16x16x32_bf16 v[222:225], v[210:213], v[24:27], v[222:225]\n"
"ds_read_b128 v[252:255], v5 offset:6144\n"
"v_mfma_f32_16x16x32_bf16 v[8:11], v[92:95], v[196:199], v[8:11]\n"
"ds_write_b64 v6, v[244:245] offset:55296\n"
"v_cvt_pk_bf16_f32 v214, v214, v215\n"
"v_mfma_f32_16x16x32_bf16 v[248:251], v[96:99], v[196:199], v[248:251]\n"
"s_waitcnt lgkmcnt(2)\n"
"v_cvt_pk_bf16_f32 v215, v216, v217\n"
"v_min_u32 v177, s72, v200\n"
"v_mfma_f32_16x16x32_bf16 v[244:247], v[28:31], v[218:221], 0\n"
"ds_read_b128 v[196:199], v177 offset:1376\n"
"v_mfma_f32_16x16x32_bf16 v[12:15], v[32:35], v[218:221], 0\n"
"v_cvt_pk_bf16_f32 v216, v8, v9\n"
"v_cvt_pk_bf16_f32 v217, v10, v11\n"
"v_mfma_f32_16x16x32_bf16 v[8:11], v[36:39], v[218:221], 0\n"
"s_waitcnt lgkmcnt(2)\n"
"ds_read_b128 v[140:143], v4 offset:8192\n"
"v_cvt_pk_bf16_f32 v218, v248, v249\n"
"v_cvt_pk_bf16_f32 v219, v250, v251\n"
"v_mfma_f32_16x16x32_bf16 v[244:247], v[40:43], v[252:255], v[244:247]\n"
"v_exp_f32 v222, v222\n"
"v_mfma_f32_16x16x32_bf16 v[12:15], v[44:47], v[252:255], v[12:15]\n"
"v_exp_f32 v223, v223\n"
"v_mfma_f32_16x16x32_bf16 v[8:11], v[48:51], v[252:255], v[8:11]\n"
"s_waitcnt lgkmcnt(1)\n"
"ds_read_b128 v[248:251], v5 offset:8192\n"
"v_cvt_pk_bf16_f32 v222, v222, v223\n"
"v_exp_f32 v224, v224\n"
"v_mfma_f32_16x16x32_bf16 v[196:199], v[238:241], v[20:23], v[196:199]\n"
"v_exp_f32 v225, v225\n"
"v_mfma_f32_16x16x32_bf16 v[196:199], v[240:243], v[24:27], v[196:199]\n"
"s_waitcnt lgkmcnt(1)\n"
"v_cvt_pk_bf16_f32 v223, v224, v225\n"
"v_mfma_f32_16x16x32_bf16 v[244:247], v[52:55], v[140:143], v[244:247]\n"
"ds_read_b128 v[252:255], v4 offset:10240\n"
"v_mfma_f32_16x16x32_bf16 v[12:15], v[56:59], v[140:143], v[12:15]\n"
"ds_read_b128 v[144:147], v177 offset:1360\n"
"v_mfma_f32_16x16x32_bf16 v[8:11], v[60:63], v[140:143], v[8:11]\n"
"ds_write_b64 v6, v[222:223] offset:55304\n"
"s_waitcnt lgkmcnt(3)\n"
"v_mfma_f32_16x16x32_bf16 v[244:247], v[64:67], v[248:251], v[244:247]\n"
"v_exp_f32 v196, v196\n"
"v_mfma_f32_16x16x32_bf16 v[12:15], v[68:71], v[248:251], v[12:15]\n"
"v_exp_f32 v197, v197\n"
"v_mfma_f32_16x16x32_bf16 v[8:11], v[72:75], v[248:251], v[8:11]\n"
"s_waitcnt lgkmcnt(2)\n"
"ds_read_b128 v[220:223], v5 offset:10240\n"
"v_cvt_pk_bf16_f32 v196, v196, v197\n"
"v_exp_f32 v198, v198\n"
"v_mfma_f32_16x16x32_bf16 v[244:247], v[76:79], v[252:255], v[244:247]\n"
"s_waitcnt lgkmcnt(2)\n"
"v_exp_f32 v199, v199\n"
"v_mfma_f32_16x16x32_bf16 v[144:147], v[232:235], v[20:23], v[144:147]\n"
"v_cvt_pk_bf16_f32 v197, v198, v199\n"
"v_mfma_f32_16x16x32_bf16 v[144:147], v[234:237], v[24:27], v[144:147]\n"
"ds_read_b128 v[140:143], v4 offset:18432\n"
"v_mfma_f32_16x16x32_bf16 v[12:15], v[80:83], v[252:255], v[12:15]\n"
"ds_write_b64 v6, v[196:197] offset:56320\n"
"v_mfma_f32_16x16x32_bf16 v[8:11], v[84:87], v[252:255], v[8:11]\n"
"s_waitcnt lgkmcnt(2)\n"
"ds_read_b128 v[196:199], v6 offset:4096\n"
"v_mfma_f32_16x16x32_bf16 v[244:247], v[88:91], v[220:223], v[244:247]\n"
"ds_read_b128 v[250:253], v201 offset:48\n"
"v_mfma_f32_16x16x32_bf16 v[12:15], v[92:95], v[220:223], v[12:15]\n"
"v_exp_f32 v144, v144\n"
"v_mfma_f32_16x16x32_bf16 v[8:11], v[96:99], v[220:223], v[8:11]\n"
"s_waitcnt lgkmcnt(2)\n"
"s_barrier\n"
"ds_read_b128 v[220:223], v5 offset:18432\n"
"v_exp_f32 v145, v145\n"
"v_mfma_f32_16x16x32_bf16 v[148:151], v[116:119], v[140:143], 0\n"
"v_cvt_pk_bf16_f32 v244, v244, v245\n"
"v_cvt_pk_bf16_f32 v245, v246, v247\n"
"v_cvt_pk_bf16_f32 v246, v12, v13\n"
"v_cvt_pk_bf16_f32 v247, v14, v15\n"
"v_mfma_f32_16x16x32_bf16 v[12:15], v[120:123], v[140:143], 0\n"
"ds_read_b128 v[152:155], v6 offset:5120\n"
"v_cvt_pk_bf16_f32 v248, v8, v9\n"
"v_cvt_pk_bf16_f32 v249, v10, v11\n"
"v_cvt_pk_bf16_f32 v144, v144, v145\n"
"s_waitcnt lgkmcnt(3)\n"
"v_mfma_f32_16x16x32_bf16 v[8:11], v[196:199], v[140:143], 0\n"
"s_waitcnt lgkmcnt(2)\n"
"ds_read_b128 v[196:199], v4 offset:20480\n"
"v_mfma_f32_16x16x32_bf16 v[250:253], v[214:217], v[20:23], v[250:253]\n"
"v_exp_f32 v146, v146\n"
"v_mfma_f32_16x16x32_bf16 v[250:253], v[216:219], v[24:27], v[250:253]\n"
"s_waitcnt lgkmcnt(2)\n"
"ds_read_b128 v[140:143], v6 offset:6144\n"
"v_mfma_f32_16x16x32_bf16 v[148:151], v[124:127], v[220:223], v[148:151]\n"
"ds_read_b128 v[156:159], v5 offset:20480\n"
"v_mfma_f32_16x16x32_bf16 v[12:15], v[128:131], v[220:223], v[12:15]\n"
"s_waitcnt lgkmcnt(3)\n"
"v_exp_f32 v147, v147\n"
"v_mfma_f32_16x16x32_bf16 v[8:11], v[152:155], v[220:223], v[8:11]\n"
"ds_read_b128 v[220:223], v177 offset:1392\n"
"v_cvt_pk_bf16_f32 v145, v146, v147\n"
"ds_write_b64 v6, v[144:145] offset:57352\n"
"s_waitcnt lgkmcnt(4)\n"
"v_mfma_f32_16x16x32_bf16 v[148:151], v[0:3], v[196:199], v[148:151]\n"
"v_exp_f32 v250, v250\n"
"v_mfma_f32_16x16x32_bf16 v[12:15], v[16:19], v[196:199], v[12:15]\n"
"s_waitcnt lgkmcnt(3)\n"
"ds_read_b128 v[144:147], v6 offset:7168\n"
"v_mfma_f32_16x16x32_bf16 v[8:11], v[140:143], v[196:199], v[8:11]\n"
"s_waitcnt lgkmcnt(3)\n"
"ds_read_b128 v[196:199], v4 offset:22528\n"
"v_mfma_f32_16x16x32_bf16 v[148:151], v[132:135], v[156:159], v[148:151]\n"
"v_exp_f32 v251, v251\n"
"v_mfma_f32_16x16x32_bf16 v[12:15], v[136:139], v[156:159], v[12:15]\n"
"s_waitcnt lgkmcnt(3)\n"
"ds_read_b128 v[140:143], v6 offset:8192\n"
"v_cvt_pk_bf16_f32 v250, v250, v251\n"
"v_mfma_f32_16x16x32_bf16 v[220:223], v[244:247], v[20:23], v[220:223]\n"
"ds_read_b128 v[20:23], v5 offset:22528\n"
"v_mfma_f32_16x16x32_bf16 v[220:223], v[246:249], v[24:27], v[220:223]\n"
"s_waitcnt lgkmcnt(3)\n"
"v_exp_f32 v252, v252\n"
"v_exp_f32 v253, v253\n"
"v_mfma_f32_16x16x32_bf16 v[8:11], v[144:147], v[156:159], v[8:11]\n"
"s_waitcnt lgkmcnt(2)\n"
"ds_read_b128 v[144:147], v6 offset:9216\n"
"v_cvt_pk_bf16_f32 v251, v252, v253\n"
"v_mfma_f32_16x16x32_bf16 v[148:151], v[100:103], v[196:199], v[148:151]\n"
"ds_read_b128 v[252:255], v4 offset:24576\n"
"v_mfma_f32_16x16x32_bf16 v[12:15], v[104:107], v[196:199], v[12:15]\n"
"ds_write_b64 v6, v[250:251] offset:57344\n"
"v_bfe_u32 v251, v6, 2, 6\n"
"s_waitcnt lgkmcnt(4)\n"
"v_mfma_f32_16x16x32_bf16 v[8:11], v[140:143], v[196:199], v[8:11]\n"
"s_waitcnt lgkmcnt(3)\n"
"ds_read_b32 v251, v251 offset:1344\n"
"v_mfma_f32_16x16x32_bf16 v[148:151], v[108:111], v[20:23], v[148:151]\n"
"s_add_u32 s69, s50, 4294966976\n"
"ds_read_b128 v[140:143], v5 offset:24576\n"
"v_mfma_f32_16x16x32_bf16 v[12:15], v[112:115], v[20:23], v[12:15]\n"
"s_waitcnt lgkmcnt(4)\n"
"v_exp_f32 v220, v220\n"
"v_exp_f32 v221, v221\n"
"v_mfma_f32_16x16x32_bf16 v[8:11], v[144:147], v[20:23], v[8:11]\n"
"s_waitcnt lgkmcnt(3)\n"
"v_exp_f32 v222, v222\n"
"v_mfma_f32_16x16x32_bf16 v[196:199], v[28:31], v[252:255], 0\n"
"ds_read_b128 v[20:23], v4 offset:26624\n"
"v_cvt_pk_bf16_f32 v220, v220, v221\n"
"v_mfma_f32_16x16x32_bf16 v[144:147], v[32:35], v[252:255], 0\n"
"v_exp_f32 v223, v223\n"
"v_mfma_f32_16x16x32_bf16 v[152:155], v[36:39], v[252:255], 0\n"
"s_waitcnt lgkmcnt(2)\n"
"v_cvt_pk_bf16_f32 v221, v222, v223\n"
"v_add_u32_sdwa v250, v251, v7 dst_sel:DWORD dst_unused:UNUSED_PAD src0_sel:WORD_0 src1_sel:DWORD\n"
"ds_write_b64 v6, v[220:221] offset:56328\n"
"v_min_u32 v251, s69, v250\n"
"s_waitcnt lgkmcnt(2)\n"
"v_mfma_f32_16x16x32_bf16 v[196:199], v[40:43], v[140:143], v[196:199]\n"
"ds_read_b128 v[220:223], v251 offset:2704\n"
"v_mul_f32 v148, s60, v148\n"
"v_mfma_f32_16x16x32_bf16 v[144:147], v[44:47], v[140:143], v[144:147]\n"
"v_mul_f32 v149, s60, v149\n"
"v_cvt_pk_bf16_f32 v148, v148, v149\n"
"v_mfma_f32_16x16x32_bf16 v[152:155], v[48:51], v[140:143], v[152:155]\n"
"s_waitcnt lgkmcnt(2)\n"
"ds_read_b128 v[252:255], v5 offset:26624\n"
"v_mul_f32 v150, s60, v150\n"
"v_mfma_f32_16x16x32_bf16 v[196:199], v[52:55], v[20:23], v[196:199]\n"
"s_waitcnt lgkmcnt(2)\n"
"s_barrier\n"
"v_mul_f32 v151, s60, v151\n"
"v_cvt_pk_bf16_f32 v149, v150, v151\n"
"v_mfma_f32_16x16x32_bf16 v[144:147], v[56:59], v[20:23], v[144:147]\n"
"v_mul_f32 v12, s60, v12\n"
"v_mul_f32 v13, s60, v13\n"
"v_mul_f32 v14, s60, v14\n"
"v_mul_f32 v15, s60, v15\n"
"v_cvt_pk_bf16_f32 v150, v12, v13\n"
"v_cvt_pk_bf16_f32 v151, v14, v15\n"
"v_mul_f32 v8, s60, v8\n"
"v_mul_f32 v9, s60, v9\n"
"s_waitcnt lgkmcnt(1)\n"
"v_mfma_f32_16x16x32_bf16 v[220:223], v[184:187], v[148:151], v[220:223]\n"
"ds_read_b128 v[12:15], v4 offset:28672\n"
"v_mul_f32 v10, s60, v10\n"
"v_mul_f32 v11, s60, v11\n"
"v_cvt_pk_bf16_f32 v26, v8, v9\n"
"v_cvt_pk_bf16_f32 v27, v10, v11\n"
"s_nop 1\n"
"v_mfma_f32_16x16x32_bf16 v[220:223], v[186:189], v[24:27], v[220:223]\n"
"v_mfma_f32_16x16x32_bf16 v[152:155], v[60:63], v[20:23], v[152:155]\n"
"s_waitcnt lgkmcnt(1)\n"
"ds_read_b128 v[8:11], v251 offset:2720\n"
"v_mfma_f32_16x16x32_bf16 v[196:199], v[64:67], v[252:255], v[196:199]\n"
"v_mfma_f32_16x16x32_bf16 v[144:147], v[68:71], v[252:255], v[144:147]\n"
"s_add_u32 s73, s50, 4294967280\n"
"v_mfma_f32_16x16x32_bf16 v[152:155], v[72:75], v[252:255], v[152:155]\n"
"s_waitcnt lgkmcnt(1)\n"
"ds_read_b128 v[252:255], v5 offset:28672\n"
"v_mfma_f32_16x16x32_bf16 v[196:199], v[76:79], v[12:15], v[196:199]\n"
"v_exp_f32 v220, v220\n"
"v_mfma_f32_16x16x32_bf16 v[144:147], v[80:83], v[12:15], v[144:147]\n"
"s_waitcnt lgkmcnt(1)\n"
"v_max_u32 v225, s73, v250\n"
"v_exp_f32 v221, v221\n"
"v_exp_f32 v222, v222\n"
"v_mfma_f32_16x16x32_bf16 v[8:11], v[190:193], v[148:151], v[8:11]\n"
"ds_read_b128 v[20:23], v4 offset:30720\n"
"v_cvt_pk_bf16_f32 v220, v220, v221\n"
"v_mfma_f32_16x16x32_bf16 v[8:11], v[192:195], v[24:27], v[8:11]\n"
"ds_read_b128 v[140:143], v225 offset:32\n"
"v_mfma_f32_16x16x32_bf16 v[152:155], v[84:87], v[12:15], v[152:155]\n"
"s_waitcnt lgkmcnt(2)\n"
"v_exp_f32 v223, v223\n"
"v_mfma_f32_16x16x32_bf16 v[196:199], v[88:91], v[252:255], v[196:199]\n"
"v_cvt_pk_bf16_f32 v221, v222, v223\n"
"v_mfma_f32_16x16x32_bf16 v[144:147], v[92:95], v[252:255], v[144:147]\n"
"ds_write_b64 v6, v[220:221] offset:53248\n"
"v_mfma_f32_16x16x32_bf16 v[152:155], v[96:99], v[252:255], v[152:155]\n"
"s_waitcnt lgkmcnt(2)\n"
"ds_read_b128 v[252:255], v5 offset:30720\n"
"v_exp_f32 v8, v8\n"
"v_mfma_f32_16x16x32_bf16 v[220:223], v[28:31], v[20:23], 0\n"
"v_exp_f32 v9, v9\n"
"v_cvt_pk_bf16_f32 v196, v196, v197\n"
"v_cvt_pk_bf16_f32 v197, v198, v199\n"
"v_cvt_pk_bf16_f32 v198, v144, v145\n"
"v_cvt_pk_bf16_f32 v199, v146, v147\n"
"s_waitcnt lgkmcnt(2)\n"
"v_mfma_f32_16x16x32_bf16 v[140:143], v[208:211], v[148:151], v[140:143]\n"
"v_cvt_pk_bf16_f32 v200, v152, v153\n"
"v_cvt_pk_bf16_f32 v201, v154, v155\n"
"v_mfma_f32_16x16x32_bf16 v[140:143], v[210:213], v[24:27], v[140:143]\n"
"ds_read_b128 v[12:15], v225 offset:48\n"
"v_cvt_pk_bf16_f32 v8, v8, v9\n"
"v_mfma_f32_16x16x32_bf16 v[144:147], v[32:35], v[20:23], 0\n"
"v_exp_f32 v10, v10\n"
"v_mfma_f32_16x16x32_bf16 v[152:155], v[36:39], v[20:23], 0\n"
"s_add_u32 s68, s50, 4294965936\n"
"ds_read_b128 v[20:23], v4 offset:32768\n"
"s_waitcnt lgkmcnt(2)\n"
"v_mfma_f32_16x16x32_bf16 v[220:223], v[40:43], v[252:255], v[220:223]\n"
"v_exp_f32 v11, v11\n"
"v_mfma_f32_16x16x32_bf16 v[144:147], v[44:47], v[252:255], v[144:147]\n"
"v_cvt_pk_bf16_f32 v9, v10, v11\n"
"v_mfma_f32_16x16x32_bf16 v[152:155], v[48:51], v[252:255], v[152:155]\n"
"ds_write_b64 v6, v[8:9] offset:53256\n"
"ds_read_b128 v[252:255], v5 offset:32768\n"
"v_max_u32 v177, s68, v250\n"
"s_waitcnt lgkmcnt(3)\n"
"v_mfma_f32_16x16x32_bf16 v[12:15], v[214:217], v[148:151], v[12:15]\n"
"v_exp_f32 v140, v140\n"
"v_mfma_f32_16x16x32_bf16 v[12:15], v[216:219], v[24:27], v[12:15]\n"
"s_waitcnt lgkmcnt(2)\n"
"ds_read_b128 v[8:11], v177 offset:1392\n"
"v_mfma_f32_16x16x32_bf16 v[220:223], v[52:55], v[20:23], v[220:223]\n"
"v_exp_f32 v141, v141\n"
"v_mfma_f32_16x16x32_bf16 v[144:147], v[56:59], v[20:23], v[144:147]\n"
"v_exp_f32 v142, v142\n"
"v_mfma_f32_16x16x32_bf16 v[152:155], v[60:63], v[20:23], v[152:155]\n"
"s_waitcnt lgkmcnt(1)\n"
"ds_read_b128 v[20:23], v4 offset:34816\n"
"v_cvt_pk_bf16_f32 v140, v140, v141\n"
"v_exp_f32 v143, v143\n"
"v_mfma_f32_16x16x32_bf16 v[220:223], v[64:67], v[252:255], v[220:223]\n"
"v_cvt_pk_bf16_f32 v141, v142, v143\n"
"v_mfma_f32_16x16x32_bf16 v[144:147], v[68:71], v[252:255], v[144:147]\n"
"ds_write_b64 v6, v[140:141] offset:54272\n"
"s_waitcnt lgkmcnt(2)\n"
"v_mfma_f32_16x16x32_bf16 v[8:11], v[244:247], v[148:151], v[8:11]\n"
"ds_read_b128 v[140:143], v5 offset:34816\n"
"v_mfma_f32_16x16x32_bf16 v[8:11], v[246:249], v[24:27], v[8:11]\n"
"ds_read_b128 v[156:159], v225 offset:16\n"
"v_mfma_f32_16x16x32_bf16 v[152:155], v[72:75], v[252:255], v[152:155]\n"
"s_waitcnt lgkmcnt(3)\n"
"v_exp_f32 v12, v12\n"
"v_mfma_f32_16x16x32_bf16 v[220:223], v[76:79], v[20:23], v[220:223]\n"
"v_exp_f32 v13, v13\n"
"v_exp_f32 v14, v14\n"
"v_mfma_f32_16x16x32_bf16 v[144:147], v[80:83], v[20:23], v[144:147]\n"
"v_exp_f32 v15, v15\n"
"v_cvt_pk_bf16_f32 v12, v12, v13\n"
"v_cvt_pk_bf16_f32 v13, v14, v15\n"
"v_mfma_f32_16x16x32_bf16 v[152:155], v[84:87], v[20:23], v[152:155]\n"
"ds_write_b64 v6, v[12:13] offset:54280\n"
"ds_read_b128 v[252:255], v4 offset:36864\n"
"s_waitcnt lgkmcnt(3)\n"
"v_mfma_f32_16x16x32_bf16 v[220:223], v[88:91], v[140:143], v[220:223]\n"
"s_waitcnt lgkmcnt(2)\n"
"v_exp_f32 v8, v8\n"
"v_mfma_f32_16x16x32_bf16 v[156:159], v[202:205], v[148:151], v[156:159]\n"
"ds_read_b128 v[12:15], v6 offset:4096\n"
"v_mfma_f32_16x16x32_bf16 v[156:159], v[204:207], v[24:27], v[156:159]\n"
"ds_read_b128 v[202:205], v177 offset:1360\n"
"v_mfma_f32_16x16x32_bf16 v[144:147], v[92:95], v[140:143], v[144:147]\n"
"v_exp_f32 v9, v9\n"
"v_mfma_f32_16x16x32_bf16 v[152:155], v[96:99], v[140:143], v[152:155]\n"
"ds_read_b128 v[20:23], v5 offset:36864\n"
"v_cvt_pk_bf16_f32 v220, v220, v221\n"
"v_exp_f32 v10, v10\n"
"v_cvt_pk_bf16_f32 v221, v222, v223\n"
"v_cvt_pk_bf16_f32 v8, v8, v9\n"
"s_waitcnt lgkmcnt(3)\n"
"v_mfma_f32_16x16x32_bf16 v[140:143], v[116:119], v[252:255], 0\n"
"v_exp_f32 v11, v11\n"
"v_cvt_pk_bf16_f32 v222, v144, v145\n"
"v_cvt_pk_bf16_f32 v9, v10, v11\n"
"ds_write_b64 v6, v[8:9] offset:33280\n"
"v_cvt_pk_bf16_f32 v223, v146, v147\n"
"v_mfma_f32_16x16x32_bf16 v[8:11], v[120:123], v[252:255], 0\n"
"s_waitcnt lgkmcnt(3)\n"
"ds_read_b128 v[144:147], v6 offset:5120\n"
"v_cvt_pk_bf16_f32 v224, v152, v153\n"
"v_cvt_pk_bf16_f32 v225, v154, v155\n"
"v_mfma_f32_16x16x32_bf16 v[152:155], v[12:15], v[252:255], 0\n"
"s_waitcnt lgkmcnt(1)\n"
"ds_read_b128 v[252:255], v4 offset:38912\n"
"s_barrier\n"
"v_mfma_f32_16x16x32_bf16 v[202:205], v[232:235], v[148:151], v[202:205]\n"
"v_exp_f32 v156, v156\n"
"v_mfma_f32_16x16x32_bf16 v[202:205], v[234:237], v[24:27], v[202:205]\n"
"ds_read_b128 v[12:15], v177 offset:1376\n"
"v_mfma_f32_16x16x32_bf16 v[140:143], v[124:127], v[20:23], v[140:143]\n"
"v_exp_f32 v157, v157\n"
"v_mfma_f32_16x16x32_bf16 v[8:11], v[128:131], v[20:23], v[8:11]\n"
"s_waitcnt lgkmcnt(2)\n"
"ds_read_b128 v[160:163], v6 offset:6144\n"
"v_cvt_pk_bf16_f32 v156, v156, v157\n"
"v_exp_f32 v158, v158\n"
"v_mfma_f32_16x16x32_bf16 v[152:155], v[144:147], v[20:23], v[152:155]\n"
"s_waitcnt lgkmcnt(2)\n"
"ds_read_b128 v[20:23], v5 offset:38912\n"
"v_exp_f32 v159, v159\n"
"v_mfma_f32_16x16x32_bf16 v[140:143], v[0:3], v[252:255], v[140:143]\n"
"v_cvt_pk_bf16_f32 v157, v158, v159\n"
"v_mfma_f32_16x16x32_bf16 v[8:11], v[16:19], v[252:255], v[8:11]\n"
"ds_write_b64 v6, v[156:157] offset:57352\n"
"ds_read_b128 v[144:147], v6 offset:7168\n"
"s_waitcnt lgkmcnt(4)\n"
"v_mfma_f32_16x16x32_bf16 v[12:15], v[238:241], v[148:151], v[12:15]\n"
"ds_read_b128 v[156:159], v4 offset:40960\n"
"v_mfma_f32_16x16x32_bf16 v[12:15], v[240:243], v[24:27], v[12:15]\n"
"s_waitcnt lgkmcnt(4)\n"
"ds_read_b128 v[164:167], v251 offset:2736\n"
"v_mfma_f32_16x16x32_bf16 v[152:155], v[160:163], v[252:255], v[152:155]\n"
"s_waitcnt lgkmcnt(4)\n"
"v_exp_f32 v202, v202\n"
"v_mfma_f32_16x16x32_bf16 v[140:143], v[132:135], v[20:23], v[140:143]\n"
"v_exp_f32 v203, v203\n"
"v_exp_f32 v204, v204\n"
"v_mfma_f32_16x16x32_bf16 v[8:11], v[136:139], v[20:23], v[8:11]\n"
"s_waitcnt lgkmcnt(2)\n"
"ds_read_b128 v[250:253], v6 offset:8192\n"
"v_cvt_pk_bf16_f32 v202, v202, v203\n"
"v_exp_f32 v205, v205\n"
"v_mfma_f32_16x16x32_bf16 v[152:155], v[144:147], v[20:23], v[152:155]\n"
"s_waitcnt lgkmcnt(2)\n"
"ds_read_b128 v[20:23], v5 offset:40960\n"
"v_cvt_pk_bf16_f32 v203, v204, v205\n"
"v_mfma_f32_16x16x32_bf16 v[140:143], v[100:103], v[156:159], v[140:143]\n"
"ds_write_b64 v6, v[202:203] offset:58368\n"
"s_waitcnt lgkmcnt(3)\n"
"v_mfma_f32_16x16x32_bf16 v[164:167], v[196:199], v[148:151], v[164:167]\n"
"ds_read_b128 v[202:205], v6 offset:9216\n"
"v_mfma_f32_16x16x32_bf16 v[164:167], v[198:201], v[24:27], v[164:167]\n"
"ds_read_b128 v[144:147], v4 offset:49152\n"
"v_mfma_f32_16x16x32_bf16 v[8:11], v[104:107], v[156:159], v[8:11]\n"
"s_waitcnt lgkmcnt(4)\n"
"v_exp_f32 v12, v12\n"
"v_mfma_f32_16x16x32_bf16 v[152:155], v[250:253], v[156:159], v[152:155]\n"
"s_waitcnt lgkmcnt(3)\n"
"v_exp_f32 v13, v13\n"
"v_exp_f32 v14, v14\n"
"v_mfma_f32_16x16x32_bf16 v[140:143], v[108:111], v[20:23], v[140:143]\n"
"s_add_u32 s70, s50, 4294967264\n"
"ds_read_b128 v[148:151], v5 offset:49152\n"
"v_cvt_pk_bf16_f32 v12, v12, v13\n"
"v_exp_f32 v15, v15\n"
"v_mfma_f32_16x16x32_bf16 v[8:11], v[112:115], v[20:23], v[8:11]\n"
"s_waitcnt lgkmcnt(2)\n"
"v_cvt_pk_bf16_f32 v13, v14, v15\n"
"v_bfe_u32 v177, v6, 2, 6\n"
"v_mfma_f32_16x16x32_bf16 v[152:155], v[202:205], v[20:23], v[152:155]\n"
"ds_write_b64 v6, v[12:13] offset:58376\n"
"ds_read_b32 v177, v177 offset:1408\n"
"s_waitcnt lgkmcnt(3)\n"
"v_mfma_f32_16x16x32_bf16 v[250:253], v[28:31], v[144:147], 0\n"
"ds_read_b128 v[202:205], v4 offset:51200\n"
"v_mul_f32 v140, s60, v140\n"
"v_mfma_f32_16x16x32_bf16 v[12:15], v[32:35], v[144:147], 0\n"
"v_exp_f32 v164, v164\n"
"v_mfma_f32_16x16x32_bf16 v[20:23], v[36:39], v[144:147], 0\n"
"s_waitcnt lgkmcnt(3)\n"
"v_exp_f32 v165, v165\n"
"v_exp_f32 v166, v166\n"
"v_mfma_f32_16x16x32_bf16 v[250:253], v[40:43], v[148:151], v[250:253]\n"
"v_exp_f32 v167, v167\n"
"v_cvt_pk_bf16_f32 v164, v164, v165\n"
"v_cvt_pk_bf16_f32 v165, v166, v167\n"
"ds_write_b64 v6, v[164:165] offset:57344\n"
"s_waitcnt lgkmcnt(2)\n"
"v_add_u32_sdwa v176, v177, v7 dst_sel:DWORD dst_unused:UNUSED_PAD src0_sel:WORD_0 src1_sel:DWORD\n"
"s_nop 0\n"
"v_max_u32 v207, s70, v176\n"
"v_mul_f32 v141, s60, v141\n"
"v_mfma_f32_16x16x32_bf16 v[12:15], v[44:47], v[148:151], v[12:15]\n"
"ds_read_b128 v[144:147], v207 offset:48\n"
"v_cvt_pk_bf16_f32 v140, v140, v141\n"
"v_mfma_f32_16x16x32_bf16 v[20:23], v[48:51], v[148:151], v[20:23]\n"
"s_waitcnt lgkmcnt(2)\n"
"ds_read_b128 v[148:151], v5 offset:51200\n"
"v_mul_f32 v142, s60, v142\n"
"v_mfma_f32_16x16x32_bf16 v[250:253], v[52:55], v[202:205], v[250:253]\n"
"s_waitcnt lgkmcnt(2)\n"
"s_barrier\n"
"v_mul_f32 v143, s60, v143\n"
"v_cvt_pk_bf16_f32 v141, v142, v143\n"
"v_mfma_f32_16x16x32_bf16 v[12:15], v[56:59], v[202:205], v[12:15]\n"
"v_mul_f32 v8, s60, v8\n"
"v_mul_f32 v9, s60, v9\n"
"v_mul_f32 v10, s60, v10\n"
"v_mul_f32 v11, s60, v11\n"
"v_cvt_pk_bf16_f32 v142, v8, v9\n"
"v_cvt_pk_bf16_f32 v143, v10, v11\n"
"v_mul_f32 v152, s60, v152\n"
"v_mul_f32 v153, s60, v153\n"
"v_mul_f32 v154, s60, v154\n"
"v_mul_f32 v155, s60, v155\n"
"s_waitcnt lgkmcnt(1)\n"
"v_mfma_f32_16x16x32_bf16 v[144:147], v[214:217], v[140:143], v[144:147]\n"
"ds_read_b128 v[8:11], v4 offset:53248\n"
"v_cvt_pk_bf16_f32 v26, v152, v153\n"
"v_cvt_pk_bf16_f32 v27, v154, v155\n"
"v_add_u32_sdwa v177, sext(v177), v176 dst_sel:DWORD dst_unused:UNUSED_PAD src0_sel:WORD_1 src1_sel:DWORD\n"
"s_nop 0\n"
"v_max_u32 v206, s70, v177\n"
"v_mfma_f32_16x16x32_bf16 v[144:147], v[216:219], v[24:27], v[144:147]\n"
"v_mfma_f32_16x16x32_bf16 v[20:23], v[60:63], v[202:205], v[20:23]\n"
"s_waitcnt lgkmcnt(1)\n"
"ds_read_b128 v[214:217], v206 offset:64\n"
"v_mfma_f32_16x16x32_bf16 v[250:253], v[64:67], v[148:151], v[250:253]\n"
"v_mfma_f32_16x16x32_bf16 v[12:15], v[68:71], v[148:151], v[12:15]\n"
"s_add_u32 s72, s50, 4294966960\n"
"v_mfma_f32_16x16x32_bf16 v[20:23], v[72:75], v[148:151], v[20:23]\n"
"s_waitcnt lgkmcnt(1)\n"
"ds_read_b128 v[202:205], v5 offset:53248\n"
"v_mfma_f32_16x16x32_bf16 v[250:253], v[76:79], v[8:11], v[250:253]\n"
"v_exp_f32 v144, v144\n"
"v_mfma_f32_16x16x32_bf16 v[12:15], v[80:83], v[8:11], v[12:15]\n"
"s_waitcnt lgkmcnt(1)\n"
"v_min_u32 v206, s72, v176\n"
"v_exp_f32 v145, v145\n"
"v_exp_f32 v146, v146\n"
"v_mfma_f32_16x16x32_bf16 v[214:217], v[220:223], v[140:143], v[214:217]\n"
"ds_read_b128 v[218:221], v4 offset:55296\n"
"v_cvt_pk_bf16_f32 v144, v144, v145\n"
"v_mfma_f32_16x16x32_bf16 v[214:217], v[222:225], v[24:27], v[214:217]\n"
"ds_read_b128 v[222:225], v206 offset:2720\n"
"v_mfma_f32_16x16x32_bf16 v[20:23], v[84:87], v[8:11], v[20:23]\n"
"s_waitcnt lgkmcnt(2)\n"
"v_exp_f32 v147, v147\n"
"v_mfma_f32_16x16x32_bf16 v[250:253], v[88:91], v[202:205], v[250:253]\n"
"v_cvt_pk_bf16_f32 v145, v146, v147\n"
"v_mfma_f32_16x16x32_bf16 v[12:15], v[92:95], v[202:205], v[12:15]\n"
"ds_write_b64 v6, v[144:145] offset:55296\n"
"v_mfma_f32_16x16x32_bf16 v[20:23], v[96:99], v[202:205], v[20:23]\n"
"s_waitcnt lgkmcnt(2)\n"
"ds_read_b128 v[8:11], v5 offset:55296\n"
"v_exp_f32 v214, v214\n"
"v_mfma_f32_16x16x32_bf16 v[202:205], v[28:31], v[218:221], 0\n"
"v_exp_f32 v215, v215\n"
"v_cvt_pk_bf16_f32 v250, v250, v251\n"
"v_cvt_pk_bf16_f32 v251, v252, v253\n"
"v_cvt_pk_bf16_f32 v252, v12, v13\n"
"v_cvt_pk_bf16_f32 v253, v14, v15\n"
"s_waitcnt lgkmcnt(2)\n"
"v_mfma_f32_16x16x32_bf16 v[222:225], v[190:193], v[140:143], v[222:225]\n"
"v_cvt_pk_bf16_f32 v254, v20, v21\n"
"v_cvt_pk_bf16_f32 v255, v22, v23\n"
"v_mfma_f32_16x16x32_bf16 v[222:225], v[192:195], v[24:27], v[222:225]\n"
"ds_read_b128 v[12:15], v206 offset:2736\n"
"v_cvt_pk_bf16_f32 v214, v214, v215\n"
"v_mfma_f32_16x16x32_bf16 v[20:23], v[32:35], v[218:221], 0\n"
"v_exp_f32 v216, v216\n"
"v_mfma_f32_16x16x32_bf16 v[144:147], v[36:39], v[218:221], 0\n"
"s_waitcnt lgkmcnt(1)\n"
"ds_read_b128 v[218:221], v4 offset:57344\n"
"v_mfma_f32_16x16x32_bf16 v[202:205], v[40:43], v[8:11], v[202:205]\n"
"v_exp_f32 v217, v217\n"
"v_mfma_f32_16x16x32_bf16 v[20:23], v[44:47], v[8:11], v[20:23]\n"
"v_cvt_pk_bf16_f32 v215, v216, v217\n"
"v_mov_b32 v222, 0\n"
"v_mfma_f32_16x16x32_bf16 v[144:147], v[48:51], v[8:11], v[144:147]\n"
"ds_write_b64 v6, v[214:215] offset:55304\n"
"ds_read_b128 v[214:217], v5 offset:57344\n"
"v_exp_f32 v224, v224\n"
"s_waitcnt lgkmcnt(3)\n"
"v_mfma_f32_16x16x32_bf16 v[12:15], v[196:199], v[140:143], v[12:15]\n"
"v_exp_f32 v225, v225\n"
"v_mfma_f32_16x16x32_bf16 v[12:15], v[198:201], v[24:27], v[12:15]\n"
"s_waitcnt lgkmcnt(2)\n"
"ds_read_b128 v[8:11], v207 offset:32\n"
"v_cvt_pk_bf16_f32 v223, v224, v225\n"
"v_mfma_f32_16x16x32_bf16 v[202:205], v[52:55], v[218:221], v[202:205]\n"
"ds_write_b64 v6, v[222:223] offset:56320\n"
"v_mfma_f32_16x16x32_bf16 v[20:23], v[56:59], v[218:221], v[20:23]\n"
"s_add_u32 s71, s50, 1008\n"
"v_mfma_f32_16x16x32_bf16 v[144:147], v[60:63], v[218:221], v[144:147]\n"
"s_waitcnt lgkmcnt(2)\n"
"ds_read_b128 v[218:221], v4 offset:59392\n"
"v_mfma_f32_16x16x32_bf16 v[202:205], v[64:67], v[214:217], v[202:205]\n"
"v_exp_f32 v12, v12\n"
"v_mfma_f32_16x16x32_bf16 v[20:23], v[68:71], v[214:217], v[20:23]\n"
"s_waitcnt lgkmcnt(2)\n"
"v_min_u32 v225, s71, v176\n"
"v_exp_f32 v13, v13\n"
"v_mfma_f32_16x16x32_bf16 v[8:11], v[208:211], v[140:143], v[8:11]\n"
"ds_read_b128 v[206:209], v5 offset:59392\n"
"v_cvt_pk_bf16_f32 v12, v12, v13\n"
"v_mfma_f32_16x16x32_bf16 v[8:11], v[210:213], v[24:27], v[8:11]\n"
"ds_read_b128 v[210:213], v225 offset:1376\n"
"v_mfma_f32_16x16x32_bf16 v[144:147], v[72:75], v[214:217], v[144:147]\n"
"s_waitcnt lgkmcnt(2)\n"
"v_exp_f32 v14, v14\n"
"v_exp_f32 v15, v15\n"
"v_mfma_f32_16x16x32_bf16 v[202:205], v[76:79], v[218:221], v[202:205]\n"
"v_cvt_pk_bf16_f32 v13, v14, v15\n"
"v_mfma_f32_16x16x32_bf16 v[20:23], v[80:83], v[218:221], v[20:23]\n"
"ds_write_b64 v6, v[12:13] offset:56328\n"
"v_mov_b32 v8, 0\n"
"v_mfma_f32_16x16x32_bf16 v[144:147], v[84:87], v[218:221], v[144:147]\n"
"s_waitcnt lgkmcnt(2)\n"
"ds_read_b128 v[214:217], v4 offset:43008\n"
"v_mfma_f32_16x16x32_bf16 v[202:205], v[88:91], v[206:209], v[202:205]\n"
"s_waitcnt lgkmcnt(2)\n"
"v_exp_f32 v10, v10\n"
"v_mfma_f32_16x16x32_bf16 v[210:213], v[238:241], v[140:143], v[210:213]\n"
"s_waitcnt lgkmcnt(1)\n"
"ds_read_b128 v[218:221], v6 offset:4096\n"
"s_barrier\n"
"v_mfma_f32_16x16x32_bf16 v[210:213], v[240:243], v[24:27], v[210:213]\n"
"ds_read_b128 v[12:15], v225 offset:1392\n"
"v_mfma_f32_16x16x32_bf16 v[20:23], v[92:95], v[206:209], v[20:23]\n"
"v_exp_f32 v11, v11\n"
"v_mfma_f32_16x16x32_bf16 v[144:147], v[96:99], v[206:209], v[144:147]\n"
"ds_read_b128 v[222:225], v5 offset:43008\n"
"v_cvt_pk_bf16_f32 v202, v202, v203\n"
"v_cvt_pk_bf16_f32 v203, v204, v205\n"
"v_cvt_pk_bf16_f32 v9, v10, v11\n"
"ds_write_b64 v6, v[8:9] offset:57352\n"
"v_mov_b32 v210, 0\n"
"s_waitcnt lgkmcnt(4)\n"
"v_mfma_f32_16x16x32_bf16 v[8:11], v[116:119], v[214:217], 0\n"
"v_cvt_pk_bf16_f32 v204, v20, v21\n"
"v_cvt_pk_bf16_f32 v205, v22, v23\n"
"v_mfma_f32_16x16x32_bf16 v[20:23], v[120:123], v[214:217], 0\n"
"s_waitcnt lgkmcnt(3)\n"
"ds_read_b128 v[148:151], v6 offset:5120\n"
"v_cvt_pk_bf16_f32 v206, v144, v145\n"
"v_cvt_pk_bf16_f32 v207, v146, v147\n"
"v_mfma_f32_16x16x32_bf16 v[144:147], v[218:221], v[214:217], 0\n"
"s_waitcnt lgkmcnt(3)\n"
"ds_read_b128 v[214:217], v4 offset:45056\n"
"v_min_u32 v211, s71, v177\n"
"v_mfma_f32_16x16x32_bf16 v[12:15], v[244:247], v[140:143], v[12:15]\n"
"v_exp_f32 v212, v212\n"
"v_mfma_f32_16x16x32_bf16 v[12:15], v[246:249], v[24:27], v[12:15]\n"
"s_waitcnt lgkmcnt(3)\n"
"ds_read_b128 v[218:221], v211 offset:1408\n"
"v_mfma_f32_16x16x32_bf16 v[8:11], v[124:127], v[222:225], v[8:11]\n"
"v_exp_f32 v213, v213\n"
"v_mfma_f32_16x16x32_bf16 v[20:23], v[128:131], v[222:225], v[20:23]\n"
"ds_read_b128 v[152:155], v6 offset:6144\n"
"v_cvt_pk_bf16_f32 v211, v212, v213\n"
"ds_write_b64 v6, v[210:211] offset:58368\n"
"s_waitcnt lgkmcnt(4)\n"
"v_mfma_f32_16x16x32_bf16 v[144:147], v[148:151], v[222:225], v[144:147]\n"
"s_waitcnt lgkmcnt(3)\n"
"ds_read_b128 v[208:211], v5 offset:45056\n"
"v_mfma_f32_16x16x32_bf16 v[8:11], v[0:3], v[214:217], v[8:11]\n"
"v_exp_f32 v12, v12\n"
"v_mfma_f32_16x16x32_bf16 v[20:23], v[16:19], v[214:217], v[20:23]\n"
"s_waitcnt lgkmcnt(3)\n"
"ds_read_b128 v[222:225], v6 offset:7168\n"
"v_min_u32 v176, s72, v177\n"
"v_mfma_f32_16x16x32_bf16 v[218:221], v[250:253], v[140:143], v[218:221]\n"
"ds_read_b128 v[148:151], v4 offset:47104\n"
"v_mfma_f32_16x16x32_bf16 v[218:221], v[252:255], v[24:27], v[218:221]\n"
"s_waitcnt lgkmcnt(4)\n"
"ds_read_b128 v[156:159], v176 offset:2752\n"
"v_mfma_f32_16x16x32_bf16 v[144:147], v[152:155], v[214:217], v[144:147]\n"
"s_waitcnt lgkmcnt(3)\n"
"v_exp_f32 v13, v13\n"
"v_exp_f32 v14, v14\n"
"v_exp_f32 v15, v15\n"
"v_mfma_f32_16x16x32_bf16 v[8:11], v[132:135], v[208:211], v[8:11]\n"
"v_cvt_pk_bf16_f32 v12, v12, v13\n"
"v_cvt_pk_bf16_f32 v13, v14, v15\n"
"v_mfma_f32_16x16x32_bf16 v[20:23], v[136:139], v[208:211], v[20:23]\n"
"ds_write_b64 v6, v[12:13] offset:58376\n"
"ds_read_b128 v[212:215], v6 offset:8192\n"
"s_waitcnt lgkmcnt(4)\n"
"v_mfma_f32_16x16x32_bf16 v[144:147], v[222:225], v[208:211], v[144:147]\n"
"s_waitcnt lgkmcnt(3)\n"
"ds_read_b128 v[208:211], v5 offset:47104\n"
"v_mfma_f32_16x16x32_bf16 v[8:11], v[100:103], v[148:151], v[8:11]\n"
"s_waitcnt lgkmcnt(3)\n"
"v_exp_f32 v218, v218\n"
"v_mfma_f32_16x16x32_bf16 v[156:159], v[202:205], v[140:143], v[156:159]\n"
"ds_read_b128 v[222:225], v6 offset:9216\n"
"v_mfma_f32_16x16x32_bf16 v[156:159], v[204:207], v[24:27], v[156:159]\n"
"ds_read_b128 v[12:15], v4 offset:0\n"
"v_mfma_f32_16x16x32_bf16 v[20:23], v[104:107], v[148:151], v[20:23]\n"
"s_waitcnt lgkmcnt(3)\n"
"v_exp_f32 v219, v219\n"
"v_exp_f32 v220, v220\n"
"v_exp_f32 v221, v221\n"
"v_mfma_f32_16x16x32_bf16 v[144:147], v[212:215], v[148:151], v[144:147]\n"
"s_waitcnt lgkmcnt(2)\n"
"v_cvt_pk_bf16_f32 v218, v218, v219\n"
"v_cvt_pk_bf16_f32 v219, v220, v221\n"
"v_mfma_f32_16x16x32_bf16 v[8:11], v[108:111], v[208:211], v[8:11]\n"
"s_add_u32 s68, s50, 4294966992\n"
"ds_write_b64 v6, v[218:219] offset:33288\n"
"v_bfe_u32 v177, v6, 2, 6\n"
"v_mfma_f32_16x16x32_bf16 v[20:23], v[112:115], v[208:211], v[20:23]\n"
"s_waitcnt lgkmcnt(2)\n"
"ds_read_b32 v177, v177 offset:1472\n"
"v_mfma_f32_16x16x32_bf16 v[144:147], v[222:225], v[208:211], v[144:147]\n"
"s_waitcnt lgkmcnt(2)\n"
"ds_read_b128 v[212:215], v5 offset:0\n"
"v_mfma_f32_16x16x32_bf16 v[208:211], v[28:31], v[12:15], 0\n"
"ds_read_b128 v[216:219], v4 offset:2048\n"
"v_mul_f32 v8, s60, v8\n"
"v_mfma_f32_16x16x32_bf16 v[220:223], v[32:35], v[12:15], 0\n"
"v_exp_f32 v156, v156\n"
"v_mfma_f32_16x16x32_bf16 v[140:143], v[36:39], v[12:15], 0\n"
"s_waitcnt lgkmcnt(2)\n"
"v_exp_f32 v157, v157\n"
"v_exp_f32 v158, v158\n"
"v_exp_f32 v159, v159\n"
"v_add_u32_sdwa v176, v177, v7 dst_sel:DWORD dst_unused:UNUSED_PAD src0_sel:WORD_0 src1_sel:DWORD\n"
"s_nop 0\n"
"v_min_u32 v177, s68, v176\n"
"ds_read_b128 v[12:15], v177 offset:2688\n"
"v_cvt_pk_bf16_f32 v156, v156, v157\n"
"s_waitcnt lgkmcnt(2)\n"
"v_mfma_f32_16x16x32_bf16 v[208:211], v[40:43], v[212:215], v[208:211]\n"
"v_cvt_pk_bf16_f32 v157, v158, v159\n"
"v_mul_f32 v9, s60, v9\n"
"v_mfma_f32_16x16x32_bf16 v[220:223], v[44:47], v[212:215], v[220:223]\n"
"ds_write_b64 v6, v[156:157] offset:57344\n"
"v_cvt_pk_bf16_f32 v8, v8, v9\n"
"v_mfma_f32_16x16x32_bf16 v[140:143], v[48:51], v[212:215], v[140:143]\n"
"s_waitcnt lgkmcnt(2)\n"
"ds_read_b128 v[212:215], v5 offset:2048\n"
"v_mul_f32 v10, s60, v10\n"
"v_mfma_f32_16x16x32_bf16 v[208:211], v[52:55], v[216:219], v[208:211]\n"
"v_mul_f32 v11, s60, v11\n"
"v_cvt_pk_bf16_f32 v9, v10, v11\n"
"v_mfma_f32_16x16x32_bf16 v[220:223], v[56:59], v[216:219], v[220:223]\n"
"s_waitcnt lgkmcnt(1)\n"
"v_mul_f32 v20, s60, v20\n"
"v_mul_f32 v21, s60, v21\n"
"s_barrier\n"
"v_mul_f32 v22, s60, v22\n"
"v_mul_f32 v23, s60, v23\n"
"v_cvt_pk_bf16_f32 v10, v20, v21\n"
"v_cvt_pk_bf16_f32 v11, v22, v23\n"
"v_mul_f32 v144, s60, v144\n"
"v_mul_f32 v145, s60, v145\n"
"v_mfma_f32_16x16x32_bf16 v[12:15], v[178:181], v[8:11], v[12:15]\n"
"ds_read_b128 v[20:23], v4 offset:4096\n"
"v_mul_f32 v146, s60, v146\n"
"v_mul_f32 v147, s60, v147\n"
"v_cvt_pk_bf16_f32 v26, v144, v145\n"
"v_cvt_pk_bf16_f32 v27, v146, v147\n"
"s_nop 1\n"
"v_mfma_f32_16x16x32_bf16 v[12:15], v[180:183], v[24:27], v[12:15]\n"
"v_mfma_f32_16x16x32_bf16 v[140:143], v[60:63], v[216:219], v[140:143]\n"
"s_waitcnt lgkmcnt(1)\n"
"v_mfma_f32_16x16x32_bf16 v[208:211], v[64:67], v[212:215], v[208:211]\n"
"ds_read_b128 v[216:219], v5 offset:4096\n"
"v_mfma_f32_16x16x32_bf16 v[220:223], v[68:71], v[212:215], v[220:223]\n"
"ds_read_b128 v[144:147], v177 offset:2704\n"
"v_mfma_f32_16x16x32_bf16 v[140:143], v[72:75], v[212:215], v[140:143]\n"
"s_waitcnt lgkmcnt(2)\n"
"v_mfma_f32_16x16x32_bf16 v[208:211], v[76:79], v[20:23], v[208:211]\n"
"v_mfma_f32_16x16x32_bf16 v[220:223], v[80:83], v[20:23], v[220:223]\n"
"v_exp_f32 v12, v12\n"
"v_mfma_f32_16x16x32_bf16 v[140:143], v[84:87], v[20:23], v[140:143]\n"
"s_waitcnt lgkmcnt(1)\n"
"ds_read_b128 v[20:23], v4 offset:6144\n"
"v_exp_f32 v13, v13\n"
"v_exp_f32 v14, v14\n"
"v_mfma_f32_16x16x32_bf16 v[208:211], v[88:91], v[216:219], v[208:211]\n"
"s_add_u32 s73, s50, 4294965952\n"
"v_exp_f32 v15, v15\n"
"s_waitcnt lgkmcnt(1)\n"
"v_mfma_f32_16x16x32_bf16 v[144:147], v[184:187], v[8:11], v[144:147]\n"
"v_cvt_pk_bf16_f32 v12, v12, v13\n"
"v_cvt_pk_bf16_f32 v13, v14, v15\n"
"v_mfma_f32_16x16x32_bf16 v[144:147], v[186:189], v[24:27], v[144:147]\n"
"ds_read_b128 v[148:151], v5 offset:6144\n"
"v_mfma_f32_16x16x32_bf16 v[220:223], v[92:95], v[216:219], v[220:223]\n"
"ds_write_b64 v6, v[12:13] offset:53248\n"
"v_cvt_pk_bf16_f32 v208, v208, v209\n"
"v_mfma_f32_16x16x32_bf16 v[140:143], v[96:99], v[216:219], v[140:143]\n"
"s_waitcnt lgkmcnt(2)\n"
"v_cvt_pk_bf16_f32 v209, v210, v211\n"
"v_max_u32 v225, s73, v176\n"
"v_mfma_f32_16x16x32_bf16 v[214:217], v[28:31], v[20:23], 0\n"
"ds_read_b128 v[12:15], v225 offset:1344\n"
"v_mfma_f32_16x16x32_bf16 v[152:155], v[32:35], v[20:23], 0\n"
"v_cvt_pk_bf16_f32 v210, v220, v221\n"
"v_cvt_pk_bf16_f32 v211, v222, v223\n"
"v_mfma_f32_16x16x32_bf16 v[220:223], v[36:39], v[20:23], 0\n"
"s_waitcnt lgkmcnt(2)\n"
"ds_read_b128 v[20:23], v4 offset:8192\n"
"v_cvt_pk_bf16_f32 v212, v140, v141\n"
"v_cvt_pk_bf16_f32 v213, v142, v143\n"
"v_mfma_f32_16x16x32_bf16 v[214:217], v[40:43], v[148:151], v[214:217]\n"
"v_exp_f32 v144, v144\n"
"v_mfma_f32_16x16x32_bf16 v[152:155], v[44:47], v[148:151], v[152:155]\n"
"v_exp_f32 v145, v145\n"
"v_mfma_f32_16x16x32_bf16 v[220:223], v[48:51], v[148:151], v[220:223]\n"
"s_waitcnt lgkmcnt(1)\n"
"ds_read_b128 v[140:143], v5 offset:8192\n"
"v_cvt_pk_bf16_f32 v144, v144, v145\n"
"v_exp_f32 v146, v146\n"
"v_mfma_f32_16x16x32_bf16 v[12:15], v[226:229], v[8:11], v[12:15]\n"
"v_exp_f32 v147, v147\n"
"v_mfma_f32_16x16x32_bf16 v[12:15], v[228:231], v[24:27], v[12:15]\n"
"s_waitcnt lgkmcnt(1)\n"
"v_cvt_pk_bf16_f32 v145, v146, v147\n"
"v_mfma_f32_16x16x32_bf16 v[214:217], v[52:55], v[20:23], v[214:217]\n"
"ds_read_b128 v[148:151], v4 offset:10240\n"
"v_mfma_f32_16x16x32_bf16 v[152:155], v[56:59], v[20:23], v[152:155]\n"
"ds_read_b128 v[156:159], v225 offset:1360\n"
"v_mfma_f32_16x16x32_bf16 v[220:223], v[60:63], v[20:23], v[220:223]\n"
"ds_write_b64 v6, v[144:145] offset:53256\n"
"s_waitcnt lgkmcnt(3)\n"
"v_mfma_f32_16x16x32_bf16 v[214:217], v[64:67], v[140:143], v[214:217]\n"
"v_exp_f32 v12, v12\n"
"v_mfma_f32_16x16x32_bf16 v[152:155], v[68:71], v[140:143], v[152:155]\n"
"v_exp_f32 v13, v13\n"
"v_mfma_f32_16x16x32_bf16 v[220:223], v[72:75], v[140:143], v[220:223]\n"
"s_waitcnt lgkmcnt(2)\n"
"ds_read_b128 v[20:23], v5 offset:10240\n"
"v_cvt_pk_bf16_f32 v12, v12, v13\n"
"v_exp_f32 v14, v14\n"
"v_mfma_f32_16x16x32_bf16 v[214:217], v[76:79], v[148:151], v[214:217]\n"
"s_waitcnt lgkmcnt(2)\n"
"v_exp_f32 v15, v15\n"
"v_mfma_f32_16x16x32_bf16 v[156:159], v[232:235], v[8:11], v[156:159]\n"
"v_cvt_pk_bf16_f32 v13, v14, v15\n"
"v_mfma_f32_16x16x32_bf16 v[156:159], v[234:237], v[24:27], v[156:159]\n"
"ds_read_b128 v[140:143], v4 offset:18432\n"
"v_mfma_f32_16x16x32_bf16 v[152:155], v[80:83], v[148:151], v[152:155]\n"
"ds_write_b64 v6, v[12:13] offset:54272\n"
"v_mfma_f32_16x16x32_bf16 v[220:223], v[84:87], v[148:151], v[220:223]\n"
"s_waitcnt lgkmcnt(2)\n"
"ds_read_b128 v[12:15], v6 offset:4096\n"
"v_mfma_f32_16x16x32_bf16 v[214:217], v[88:91], v[20:23], v[214:217]\n"
"ds_read_b128 v[144:147], v225 offset:1376\n"
"v_mfma_f32_16x16x32_bf16 v[152:155], v[92:95], v[20:23], v[152:155]\n"
"v_exp_f32 v156, v156\n"
"v_mfma_f32_16x16x32_bf16 v[220:223], v[96:99], v[20:23], v[220:223]\n"
"s_waitcnt lgkmcnt(2)\n"
"s_barrier\n"
"ds_read_b128 v[20:23], v5 offset:18432\n"
"v_exp_f32 v157, v157\n"
"v_mfma_f32_16x16x32_bf16 v[148:151], v[116:119], v[140:143], 0\n"
"v_cvt_pk_bf16_f32 v214, v214, v215\n"
"v_cvt_pk_bf16_f32 v215, v216, v217\n"
"v_cvt_pk_bf16_f32 v216, v152, v153\n"
"v_cvt_pk_bf16_f32 v217, v154, v155\n"
"v_mfma_f32_16x16x32_bf16 v[152:155], v[120:123], v[140:143], 0\n"
"ds_read_b128 v[160:163], v6 offset:5120\n"
"v_cvt_pk_bf16_f32 v218, v220, v221\n"
"v_cvt_pk_bf16_f32 v219, v222, v223\n"
"v_cvt_pk_bf16_f32 v156, v156, v157\n"
"s_waitcnt lgkmcnt(3)\n"
"v_mfma_f32_16x16x32_bf16 v[164:167], v[12:15], v[140:143], 0\n"
"s_waitcnt lgkmcnt(2)\n"
"ds_read_b128 v[220:223], v4 offset:20480\n"
"v_mfma_f32_16x16x32_bf16 v[144:147], v[238:241], v[8:11], v[144:147]\n"
"v_exp_f32 v158, v158\n"
"v_mfma_f32_16x16x32_bf16 v[144:147], v[240:243], v[24:27], v[144:147]\n"
"s_waitcnt lgkmcnt(2)\n"
"ds_read_b128 v[12:15], v6 offset:6144\n"
"v_mfma_f32_16x16x32_bf16 v[148:151], v[124:127], v[20:23], v[148:151]\n"
"ds_read_b128 v[140:143], v5 offset:20480\n"
"v_mfma_f32_16x16x32_bf16 v[152:155], v[128:131], v[20:23], v[152:155]\n"
"s_waitcnt lgkmcnt(3)\n"
"v_exp_f32 v159, v159\n"
"v_mfma_f32_16x16x32_bf16 v[164:167], v[160:163], v[20:23], v[164:167]\n"
"ds_read_b128 v[20:23], v177 offset:2720\n"
"v_cvt_pk_bf16_f32 v157, v158, v159\n"
"ds_write_b64 v6, v[156:157] offset:54280\n"
"s_waitcnt lgkmcnt(4)\n"
"v_mfma_f32_16x16x32_bf16 v[148:151], v[0:3], v[220:223], v[148:151]\n"
"v_exp_f32 v144, v144\n"
"v_mfma_f32_16x16x32_bf16 v[152:155], v[16:19], v[220:223], v[152:155]\n"
"s_waitcnt lgkmcnt(3)\n"
"ds_read_b128 v[156:159], v6 offset:7168\n"
"v_mfma_f32_16x16x32_bf16 v[164:167], v[12:15], v[220:223], v[164:167]\n"
"s_waitcnt lgkmcnt(3)\n"
"ds_read_b128 v[220:223], v4 offset:22528\n"
"v_mfma_f32_16x16x32_bf16 v[148:151], v[132:135], v[140:143], v[148:151]\n"
"v_exp_f32 v145, v145\n"
"v_mfma_f32_16x16x32_bf16 v[152:155], v[136:139], v[140:143], v[152:155]\n"
"s_waitcnt lgkmcnt(3)\n"
"ds_read_b128 v[12:15], v6 offset:8192\n"
"v_cvt_pk_bf16_f32 v144, v144, v145\n"
"v_mfma_f32_16x16x32_bf16 v[20:23], v[190:193], v[8:11], v[20:23]\n"
"ds_read_b128 v[8:11], v5 offset:22528\n"
"v_mfma_f32_16x16x32_bf16 v[20:23], v[192:195], v[24:27], v[20:23]\n"
"s_waitcnt lgkmcnt(3)\n"
"v_exp_f32 v146, v146\n"
"v_exp_f32 v147, v147\n"
"v_mfma_f32_16x16x32_bf16 v[164:167], v[156:159], v[140:143], v[164:167]\n"
"s_waitcnt lgkmcnt(2)\n"
"ds_read_b128 v[140:143], v6 offset:9216\n"
"v_cvt_pk_bf16_f32 v145, v146, v147\n"
"v_mfma_f32_16x16x32_bf16 v[148:151], v[100:103], v[220:223], v[148:151]\n"
"ds_read_b128 v[156:159], v4 offset:12288\n"
"v_mfma_f32_16x16x32_bf16 v[152:155], v[104:107], v[220:223], v[152:155]\n"
"ds_write_b64 v6, v[144:145] offset:57352\n"
"v_bfe_u32 v225, v6, 2, 6\n"
"s_waitcnt lgkmcnt(4)\n"
"v_mfma_f32_16x16x32_bf16 v[164:167], v[12:15], v[220:223], v[164:167]\n"
"s_waitcnt lgkmcnt(3)\n"
"ds_read_b32 v225, v225 offset:1536\n"
"v_mfma_f32_16x16x32_bf16 v[148:151], v[108:111], v[8:11], v[148:151]\n"
"s_add_u32 s70, s50, 4294964608\n"
"ds_read_b128 v[12:15], v5 offset:12288\n"
"v_mfma_f32_16x16x32_bf16 v[152:155], v[112:115], v[8:11], v[152:155]\n"
"s_waitcnt lgkmcnt(4)\n"
"v_exp_f32 v20, v20\n"
"v_exp_f32 v21, v21\n"
"v_mfma_f32_16x16x32_bf16 v[164:167], v[140:143], v[8:11], v[164:167]\n"
"s_waitcnt lgkmcnt(3)\n"
"v_exp_f32 v22, v22\n"
"v_mfma_f32_16x16x32_bf16 v[220:223], v[28:31], v[156:159], 0\n"
"ds_read_b128 v[8:11], v4 offset:14336\n"
"v_cvt_pk_bf16_f32 v20, v20, v21\n"
"v_mfma_f32_16x16x32_bf16 v[140:143], v[32:35], v[156:159], 0\n"
"v_exp_f32 v23, v23\n"
"v_mfma_f32_16x16x32_bf16 v[144:147], v[36:39], v[156:159], 0\n"
"s_waitcnt lgkmcnt(2)\n"
"v_cvt_pk_bf16_f32 v21, v22, v23\n"
"v_add_u32_sdwa v177, v225, v7 dst_sel:DWORD dst_unused:UNUSED_PAD src0_sel:WORD_0 src1_sel:DWORD\n"
"ds_write_b64 v6, v[20:21] offset:57344\n"
"v_max_u32 v176, s70, v177\n"
"s_waitcnt lgkmcnt(2)\n"
"v_mfma_f32_16x16x32_bf16 v[220:223], v[40:43], v[12:15], v[220:223]\n"
"ds_read_b128 v[20:23], v176 offset:2688\n"
"v_mul_f32 v148, s60, v148\n"
"v_mfma_f32_16x16x32_bf16 v[140:143], v[44:47], v[12:15], v[140:143]\n"
"v_mul_f32 v149, s60, v149\n"
"v_cvt_pk_bf16_f32 v148, v148, v149\n"
"v_mfma_f32_16x16x32_bf16 v[144:147], v[48:51], v[12:15], v[144:147]\n"
"s_waitcnt lgkmcnt(2)\n"
"ds_read_b128 v[12:15], v5 offset:14336\n"
"v_mul_f32 v150, s60, v150\n"
"v_mfma_f32_16x16x32_bf16 v[220:223], v[52:55], v[8:11], v[220:223]\n"
"s_waitcnt lgkmcnt(2)\n"
"s_barrier\n"
"v_mul_f32 v151, s60, v151\n"
"v_cvt_pk_bf16_f32 v149, v150, v151\n"
"v_mfma_f32_16x16x32_bf16 v[140:143], v[56:59], v[8:11], v[140:143]\n"
"v_mul_f32 v152, s60, v152\n"
"v_mul_f32 v153, s60, v153\n"
"v_mul_f32 v154, s60, v154\n"
"v_mul_f32 v155, s60, v155\n"
"v_cvt_pk_bf16_f32 v150, v152, v153\n"
"v_cvt_pk_bf16_f32 v151, v154, v155\n"
"v_mul_f32 v164, s60, v164\n"
"v_mul_f32 v165, s60, v165\n"
"s_waitcnt lgkmcnt(1)\n"
"v_mfma_f32_16x16x32_bf16 v[20:23], v[178:181], v[148:151], v[20:23]\n"
"s_add_u32 s71, s50, 4294965648\n"
"ds_read_b128 v[152:155], v176 offset:2704\n"
"v_mul_f32 v166, s60, v166\n"
"v_mul_f32 v167, s60, v167\n"
"v_cvt_pk_bf16_f32 v26, v164, v165\n"
"v_cvt_pk_bf16_f32 v27, v166, v167\n"
"s_nop 1\n"
"v_mfma_f32_16x16x32_bf16 v[20:23], v[180:183], v[24:27], v[20:23]\n"
"v_mfma_f32_16x16x32_bf16 v[144:147], v[60:63], v[8:11], v[144:147]\n"
"s_waitcnt lgkmcnt(1)\n"
"ds_read_b128 v[8:11], v4 offset:16384\n"
"v_mfma_f32_16x16x32_bf16 v[220:223], v[64:67], v[12:15], v[220:223]\n"
"v_min_u32 v175, s71, v177\n"
"v_mfma_f32_16x16x32_bf16 v[140:143], v[68:71], v[12:15], v[140:143]\n"
"v_mfma_f32_16x16x32_bf16 v[144:147], v[72:75], v[12:15], v[144:147]\n"
"s_waitcnt lgkmcnt(1)\n"
"ds_read_b128 v[12:15], v175 offset:4032\n"
"v_mfma_f32_16x16x32_bf16 v[152:155], v[184:187], v[148:151], v[152:155]\n"
"v_exp_f32 v20, v20\n"
"v_mfma_f32_16x16x32_bf16 v[152:155], v[186:189], v[24:27], v[152:155]\n"
"s_waitcnt lgkmcnt(1)\n"
"ds_read_b128 v[156:159], v5 offset:16384\n"
"v_exp_f32 v21, v21\n"
"v_exp_f32 v22, v22\n"
"v_mfma_f32_16x16x32_bf16 v[220:223], v[76:79], v[8:11], v[220:223]\n"
"v_exp_f32 v23, v23\n"
"v_mfma_f32_16x16x32_bf16 v[140:143], v[80:83], v[8:11], v[140:143]\n"
"v_cvt_pk_bf16_f32 v20, v20, v21\n"
"v_cvt_pk_bf16_f32 v21, v22, v23\n"
"v_mfma_f32_16x16x32_bf16 v[144:147], v[84:87], v[8:11], v[144:147]\n"
"ds_write_b64 v6, v[20:21] offset:55296\n"
"ds_read_b128 v[8:11], v175 offset:4048\n"
"s_waitcnt lgkmcnt(3)\n"
"v_mfma_f32_16x16x32_bf16 v[12:15], v[208:211], v[148:151], v[12:15]\n"
"v_exp_f32 v152, v152\n"
"v_mfma_f32_16x16x32_bf16 v[12:15], v[210:213], v[24:27], v[12:15]\n"
"s_waitcnt lgkmcnt(2)\n"
"ds_read_b128 v[20:23], v4 offset:30720\n"
"v_mfma_f32_16x16x32_bf16 v[220:223], v[88:91], v[156:159], v[220:223]\n"
"v_exp_f32 v153, v153\n"
"v_mfma_f32_16x16x32_bf16 v[140:143], v[92:95], v[156:159], v[140:143]\n"
"ds_read_b128 v[160:163], v6 offset:4096\n"
"v_cvt_pk_bf16_f32 v152, v152, v153\n"
"v_exp_f32 v154, v154\n"
"v_mfma_f32_16x16x32_bf16 v[144:147], v[96:99], v[156:159], v[144:147]\n"
"s_add_u32 s69, s50, 4294965952\n"
"ds_read_b128 v[156:159], v176 offset:2720\n"
"v_cvt_pk_bf16_f32 v220, v220, v221\n"
"v_exp_f32 v155, v155\n"
"v_cvt_pk_bf16_f32 v221, v222, v223\n"
"v_cvt_pk_bf16_f32 v153, v154, v155\n"
"s_waitcnt lgkmcnt(3)\n"
"v_mfma_f32_16x16x32_bf16 v[8:11], v[214:217], v[148:151], v[8:11]\n"
"ds_write_b64 v6, v[152:153] offset:55304\n"
"v_cvt_pk_bf16_f32 v222, v140, v141\n"
"v_cvt_pk_bf16_f32 v223, v142, v143\n"
"v_cvt_pk_bf16_f32 v224, v144, v145\n"
"v_mfma_f32_16x16x32_bf16 v[8:11], v[216:219], v[24:27], v[8:11]\n"
"s_waitcnt lgkmcnt(3)\n"
"ds_read_b128 v[140:143], v5 offset:30720\n"
"v_cvt_pk_bf16_f32 v225, v146, v147\n"
"v_mfma_f32_16x16x32_bf16 v[144:147], v[116:119], v[20:23], 0\n"
"v_exp_f32 v12, v12\n"
"v_mfma_f32_16x16x32_bf16 v[152:155], v[120:123], v[20:23], 0\n"
"ds_read_b128 v[164:167], v6 offset:5120\n"
"v_max_u32 v174, s69, v177\n"
"v_exp_f32 v13, v13\n"
"v_exp_f32 v14, v14\n"
"s_waitcnt lgkmcnt(4)\n"
"v_mfma_f32_16x16x32_bf16 v[168:171], v[160:163], v[20:23], 0\n"
"s_waitcnt lgkmcnt(3)\n"
"ds_read_b128 v[20:23], v174 offset:1344\n"
"v_cvt_pk_bf16_f32 v12, v12, v13\n"
"v_exp_f32 v15, v15\n"
"v_mfma_f32_16x16x32_bf16 v[156:159], v[190:193], v[148:151], v[156:159]\n"
"v_cvt_pk_bf16_f32 v13, v14, v15\n"
"v_mfma_f32_16x16x32_bf16 v[156:159], v[192:195], v[24:27], v[156:159]\n"
"ds_write_b64 v6, v[12:13] offset:56320\n"
"ds_read_b128 v[12:15], v4 offset:32768\n"
"s_waitcnt lgkmcnt(4)\n"
"v_mfma_f32_16x16x32_bf16 v[144:147], v[124:127], v[140:143], v[144:147]\n"
"v_exp_f32 v8, v8\n"
"v_mfma_f32_16x16x32_bf16 v[152:155], v[128:131], v[140:143], v[152:155]\n"
"s_waitcnt lgkmcnt(3)\n"
"ds_read_b128 v[160:163], v6 offset:6144\n"
"v_exp_f32 v9, v9\n"
"v_exp_f32 v10, v10\n"
"v_mfma_f32_16x16x32_bf16 v[168:171], v[164:167], v[140:143], v[168:171]\n"
"s_waitcnt lgkmcnt(3)\n"
"ds_read_b128 v[140:143], v174 offset:1360\n"
"v_cvt_pk_bf16_f32 v8, v8, v9\n"
"v_exp_f32 v11, v11\n"
"v_mfma_f32_16x16x32_bf16 v[20:23], v[226:229], v[148:151], v[20:23]\n"
"v_cvt_pk_bf16_f32 v9, v10, v11\n"
"v_mfma_f32_16x16x32_bf16 v[20:23], v[228:231], v[24:27], v[20:23]\n"
"ds_write_b64 v6, v[8:9] offset:56328\n"
"ds_read_b128 v[226:229], v5 offset:32768\n"
"s_waitcnt lgkmcnt(4)\n"
"v_mfma_f32_16x16x32_bf16 v[144:147], v[0:3], v[12:15], v[144:147]\n"
"v_exp_f32 v156, v156\n"
"v_mfma_f32_16x16x32_bf16 v[152:155], v[16:19], v[12:15], v[152:155]\n"
"s_waitcnt lgkmcnt(3)\n"
"ds_read_b128 v[8:11], v6 offset:7168\n"
"v_mfma_f32_16x16x32_bf16 v[168:171], v[160:163], v[12:15], v[168:171]\n"
"s_waitcnt lgkmcnt(3)\n"
"ds_read_b128 v[12:15], v174 offset:1376\n"
"v_exp_f32 v157, v157\n"
"v_mfma_f32_16x16x32_bf16 v[140:143], v[232:235], v[148:151], v[140:143]\n"
"v_cvt_pk_bf16_f32 v156, v156, v157\n"
"v_mov_b32 v157, 0\n"
"v_mfma_f32_16x16x32_bf16 v[140:143], v[234:237], v[24:27], v[140:143]\n"
"ds_write_b64 v6, v[156:157] offset:33288\n"
"ds_read_b128 v[156:159], v4 offset:34816\n"
"s_waitcnt lgkmcnt(4)\n"
"v_mfma_f32_16x16x32_bf16 v[144:147], v[132:135], v[226:229], v[144:147]\n"
"v_exp_f32 v20, v20\n"
"v_mfma_f32_16x16x32_bf16 v[152:155], v[136:139], v[226:229], v[152:155]\n"
"s_waitcnt lgkmcnt(1)\n"
"ds_read_b128 v[160:163], v6 offset:8192\n"
"v_exp_f32 v21, v21\n"
"s_barrier\n"
"v_exp_f32 v22, v22\n"
"v_mfma_f32_16x16x32_bf16 v[168:171], v[8:11], v[226:229], v[168:171]\n"
"ds_read_b128 v[8:11], v175 offset:4064\n"
"v_cvt_pk_bf16_f32 v20, v20, v21\n"
"v_exp_f32 v23, v23\n"
"v_mfma_f32_16x16x32_bf16 v[12:15], v[238:241], v[148:151], v[12:15]\n"
"v_cvt_pk_bf16_f32 v21, v22, v23\n"
"v_mfma_f32_16x16x32_bf16 v[12:15], v[240:243], v[24:27], v[12:15]\n"
"ds_write_b64 v6, v[20:21] offset:57352\n"
"ds_read_b128 v[226:229], v5 offset:34816\n"
"s_waitcnt lgkmcnt(4)\n"
"v_mfma_f32_16x16x32_bf16 v[144:147], v[100:103], v[156:159], v[144:147]\n"
"v_exp_f32 v140, v140\n"
"v_mfma_f32_16x16x32_bf16 v[152:155], v[104:107], v[156:159], v[152:155]\n"
"s_waitcnt lgkmcnt(3)\n"
"ds_read_b128 v[20:23], v6 offset:9216\n"
"v_exp_f32 v141, v141\n"
"v_exp_f32 v142, v142\n"
"v_mfma_f32_16x16x32_bf16 v[168:171], v[160:163], v[156:159], v[168:171]\n"
"s_waitcnt lgkmcnt(3)\n"
"ds_read_b128 v[156:159], v4 offset:24576\n"
"v_cvt_pk_bf16_f32 v140, v140, v141\n"
"v_exp_f32 v143, v143\n"
"v_mfma_f32_16x16x32_bf16 v[8:11], v[220:223], v[148:151], v[8:11]\n"
"v_cvt_pk_bf16_f32 v141, v142, v143\n"
"v_bfe_u32 v177, v6, 2, 6\n"
"v_mfma_f32_16x16x32_bf16 v[8:11], v[222:225], v[24:27], v[8:11]\n"
"ds_write_b64 v6, v[140:141] offset:58368\n"
"ds_read_b32 v177, v177 offset:1600\n"
"s_waitcnt lgkmcnt(4)\n"
"v_mfma_f32_16x16x32_bf16 v[144:147], v[108:111], v[226:229], v[144:147]\n"
"s_add_u32 s73, s50, 4294966976\n"
"ds_read_b128 v[140:143], v5 offset:24576\n"
"v_mfma_f32_16x16x32_bf16 v[152:155], v[112:115], v[226:229], v[152:155]\n"
"s_waitcnt lgkmcnt(4)\n"
"v_exp_f32 v12, v12\n"
"v_mfma_f32_16x16x32_bf16 v[168:171], v[20:23], v[226:229], v[168:171]\n"
"s_waitcnt lgkmcnt(3)\n"
"v_exp_f32 v13, v13\n"
"v_mfma_f32_16x16x32_bf16 v[226:229], v[28:31], v[156:159], 0\n"
"ds_read_b128 v[20:23], v4 offset:26624\n"
"v_cvt_pk_bf16_f32 v12, v12, v13\n"
"v_mov_b32 v13, 0\n"
"v_mul_f32 v144, s60, v144\n"
"v_mfma_f32_16x16x32_bf16 v[148:151], v[32:35], v[156:159], 0\n"
"ds_write_b64 v6, v[12:13] offset:58376\n"
"s_waitcnt lgkmcnt(3)\n"
"v_add_u32_sdwa v176, v177, v7 dst_sel:DWORD dst_unused:UNUSED_PAD src0_sel:WORD_0 src1_sel:DWORD\n"
"v_mfma_f32_16x16x32_bf16 v[12:15], v[36:39], v[156:159], 0\n"
"s_waitcnt lgkmcnt(2)\n"
"v_min_u32 v177, s73, v176\n"
"v_mul_f32 v145, s60, v145\n"
"v_mfma_f32_16x16x32_bf16 v[226:229], v[40:43], v[140:143], v[226:229]\n"
"ds_read_b128 v[156:159], v177 offset:2704\n"
"v_cvt_pk_bf16_f32 v144, v144, v145\n"
"v_mfma_f32_16x16x32_bf16 v[148:151], v[44:47], v[140:143], v[148:151]\n"
"v_exp_f32 v8, v8\n"
"v_mfma_f32_16x16x32_bf16 v[12:15], v[48:51], v[140:143], v[12:15]\n"
"s_waitcnt lgkmcnt(2)\n"
"ds_read_b128 v[140:143], v5 offset:26624\n"
"v_mul_f32 v146, s60, v146\n"
"v_exp_f32 v9, v9\n"
"v_mfma_f32_16x16x32_bf16 v[226:229], v[52:55], v[20:23], v[226:229]\n"
"v_cvt_pk_bf16_f32 v8, v8, v9\n"
"v_mov_b32 v9, 0\n"
"v_mfma_f32_16x16x32_bf16 v[148:151], v[56:59], v[20:23], v[148:151]\n"
"ds_write_b64 v6, v[8:9] offset:57344\n"
"v_mul_f32 v147, s60, v147\n"
"v_cvt_pk_bf16_f32 v145, v146, v147\n"
"v_mul_f32 v152, s60, v152\n"
"v_mul_f32 v153, s60, v153\n"
"v_mul_f32 v154, s60, v154\n"
"v_mul_f32 v155, s60, v155\n"
"v_cvt_pk_bf16_f32 v146, v152, v153\n"
"s_waitcnt lgkmcnt(0)\n"
"s_barrier\n"
"v_cvt_pk_bf16_f32 v147, v154, v155\n"
"v_mul_f32 v168, s60, v168\n"
"v_mul_f32 v169, s60, v169\n"
"v_mul_f32 v170, s60, v170\n"
"v_mfma_f32_16x16x32_bf16 v[156:159], v[184:187], v[144:147], v[156:159]\n"
"s_add_u32 s68, s50, 4294965632\n"
"ds_read_b128 v[8:11], v177 offset:2720\n"
"v_mul_f32 v171, s60, v171\n"
"v_cvt_pk_bf16_f32 v26, v168, v169\n"
"v_cvt_pk_bf16_f32 v27, v170, v171\n"
"s_nop 1\n"
"v_mfma_f32_16x16x32_bf16 v[156:159], v[186:189], v[24:27], v[156:159]\n"
"v_mfma_f32_16x16x32_bf16 v[12:15], v[60:63], v[20:23], v[12:15]\n"
"ds_read_b128 v[20:23], v4 offset:28672\n"
"v_mfma_f32_16x16x32_bf16 v[226:229], v[64:67], v[140:143], v[226:229]\n"
"v_min_u32 v175, s68, v176\n"
"v_mfma_f32_16x16x32_bf16 v[148:151], v[68:71], v[140:143], v[148:151]\n"
"v_mfma_f32_16x16x32_bf16 v[12:15], v[72:75], v[140:143], v[12:15]\n"
"s_waitcnt lgkmcnt(1)\n"
"ds_read_b128 v[140:143], v175 offset:4048\n"
"v_mfma_f32_16x16x32_bf16 v[8:11], v[190:193], v[144:147], v[8:11]\n"
"v_exp_f32 v156, v156\n"
"v_mfma_f32_16x16x32_bf16 v[8:11], v[192:195], v[24:27], v[8:11]\n"
"s_waitcnt lgkmcnt(1)\n"
"ds_read_b128 v[152:155], v5 offset:28672\n"
"v_exp_f32 v157, v157\n"
"v_exp_f32 v158, v158\n"
"v_mfma_f32_16x16x32_bf16 v[226:229], v[76:79], v[20:23], v[226:229]\n"
"v_exp_f32 v159, v159\n"
"v_mfma_f32_16x16x32_bf16 v[148:151], v[80:83], v[20:23], v[148:151]\n"
"v_cvt_pk_bf16_f32 v156, v156, v157\n"
"v_cvt_pk_bf16_f32 v157, v158, v159\n"
"v_mfma_f32_16x16x32_bf16 v[12:15], v[84:87], v[20:23], v[12:15]\n"
"ds_write_b64 v6, v[156:157] offset:53248\n"
"ds_read_b128 v[20:23], v175 offset:4064\n"
"s_waitcnt lgkmcnt(3)\n"
"v_mfma_f32_16x16x32_bf16 v[140:143], v[214:217], v[144:147], v[140:143]\n"
"v_exp_f32 v8, v8\n"
"v_mfma_f32_16x16x32_bf16 v[140:143], v[216:219], v[24:27], v[140:143]\n"
"s_waitcnt lgkmcnt(2)\n"
"ds_read_b128 v[156:159], v4 offset:43008\n"
"v_mfma_f32_16x16x32_bf16 v[226:229], v[88:91], v[152:155], v[226:229]\n"
"v_exp_f32 v9, v9\n"
"v_mfma_f32_16x16x32_bf16 v[148:151], v[92:95], v[152:155], v[148:151]\n"
"ds_read_b128 v[160:163], v6 offset:4096\n"
"v_cvt_pk_bf16_f32 v8, v8, v9\n"
"v_exp_f32 v10, v10\n"
"v_mfma_f32_16x16x32_bf16 v[12:15], v[96:99], v[152:155], v[12:15]\n"
"s_add_u32 s72, s50, 4294965936\n"
"ds_read_b128 v[152:155], v177 offset:2736\n"
"v_cvt_pk_bf16_f32 v226, v226, v227\n"
"v_exp_f32 v11, v11\n"
"v_cvt_pk_bf16_f32 v227, v228, v229\n"
"v_cvt_pk_bf16_f32 v9, v10, v11\n"
"s_waitcnt lgkmcnt(3)\n"
"v_mfma_f32_16x16x32_bf16 v[20:23], v[220:223], v[144:147], v[20:23]\n"
"ds_write_b64 v6, v[8:9] offset:53256\n"
"v_cvt_pk_bf16_f32 v228, v148, v149\n"
"v_cvt_pk_bf16_f32 v229, v150, v151\n"
"v_cvt_pk_bf16_f32 v230, v12, v13\n"
"v_mfma_f32_16x16x32_bf16 v[20:23], v[222:225], v[24:27], v[20:23]\n"
"s_waitcnt lgkmcnt(3)\n"
"ds_read_b128 v[8:11], v5 offset:43008\n"
"v_cvt_pk_bf16_f32 v231, v14, v15\n"
"v_mfma_f32_16x16x32_bf16 v[12:15], v[116:119], v[156:159], 0\n"
"v_exp_f32 v140, v140\n"
"v_mfma_f32_16x16x32_bf16 v[148:151], v[120:123], v[156:159], 0\n"
"ds_read_b128 v[164:167], v6 offset:5120\n"
"v_max_u32 v174, s72, v176\n"
"v_exp_f32 v141, v141\n"
"v_exp_f32 v142, v142\n"
"s_waitcnt lgkmcnt(4)\n"
"v_mfma_f32_16x16x32_bf16 v[168:171], v[160:163], v[156:159], 0\n"
"s_waitcnt lgkmcnt(3)\n"
"ds_read_b128 v[156:159], v174 offset:1360\n"
"v_cvt_pk_bf16_f32 v140, v140, v141\n"
"v_exp_f32 v143, v143\n"
"v_mfma_f32_16x16x32_bf16 v[152:155], v[196:199], v[144:147], v[152:155]\n"
"v_cvt_pk_bf16_f32 v141, v142, v143\n"
"v_mfma_f32_16x16x32_bf16 v[152:155], v[198:201], v[24:27], v[152:155]\n"
"ds_write_b64 v6, v[140:141] offset:54272\n"
"ds_read_b128 v[140:143], v4 offset:45056\n"
"s_waitcnt lgkmcnt(4)\n"
"v_mfma_f32_16x16x32_bf16 v[12:15], v[124:127], v[8:11], v[12:15]\n"
"v_exp_f32 v20, v20\n"
"v_mfma_f32_16x16x32_bf16 v[148:151], v[128:131], v[8:11], v[148:151]\n"
"s_waitcnt lgkmcnt(3)\n"
"ds_read_b128 v[160:163], v6 offset:6144\n"
"v_exp_f32 v21, v21\n"
"v_exp_f32 v22, v22\n"
"v_mfma_f32_16x16x32_bf16 v[168:171], v[164:167], v[8:11], v[168:171]\n"
"s_waitcnt lgkmcnt(3)\n"
"ds_read_b128 v[8:11], v174 offset:1376\n"
"v_cvt_pk_bf16_f32 v20, v20, v21\n"
"v_exp_f32 v23, v23\n"
"v_mfma_f32_16x16x32_bf16 v[156:159], v[232:235], v[144:147], v[156:159]\n"
"v_cvt_pk_bf16_f32 v21, v22, v23\n"
"v_mfma_f32_16x16x32_bf16 v[156:159], v[234:237], v[24:27], v[156:159]\n"
"ds_write_b64 v6, v[20:21] offset:54280\n"
"ds_read_b128 v[232:235], v5 offset:45056\n"
"s_waitcnt lgkmcnt(4)\n"
"v_mfma_f32_16x16x32_bf16 v[12:15], v[0:3], v[140:143], v[12:15]\n"
"v_exp_f32 v152, v152\n"
"v_mfma_f32_16x16x32_bf16 v[148:151], v[16:19], v[140:143], v[148:151]\n"
"s_waitcnt lgkmcnt(3)\n"
"ds_read_b128 v[20:23], v6 offset:7168\n"
"v_exp_f32 v153, v153\n"
"v_exp_f32 v154, v154\n"
"v_mfma_f32_16x16x32_bf16 v[168:171], v[160:163], v[140:143], v[168:171]\n"
"s_waitcnt lgkmcnt(3)\n"
"ds_read_b128 v[140:143], v174 offset:1392\n"
"v_cvt_pk_bf16_f32 v152, v152, v153\n"
"v_exp_f32 v155, v155\n"
"v_mfma_f32_16x16x32_bf16 v[8:11], v[238:241], v[144:147], v[8:11]\n"
"v_cvt_pk_bf16_f32 v153, v154, v155\n"
"v_mfma_f32_16x16x32_bf16 v[8:11], v[240:243], v[24:27], v[8:11]\n"
"ds_write_b64 v6, v[152:153] offset:33280\n"
"ds_read_b128 v[152:155], v4 offset:47104\n"
"s_waitcnt lgkmcnt(4)\n"
"v_mfma_f32_16x16x32_bf16 v[12:15], v[132:135], v[232:235], v[12:15]\n"
"v_exp_f32 v156, v156\n"
"v_mfma_f32_16x16x32_bf16 v[148:151], v[136:139], v[232:235], v[148:151]\n"
"s_waitcnt lgkmcnt(1)\n"
"ds_read_b128 v[160:163], v6 offset:8192\n"
"v_exp_f32 v157, v157\n"
"s_barrier\n"
"v_exp_f32 v158, v158\n"
"v_mfma_f32_16x16x32_bf16 v[168:171], v[20:23], v[232:235], v[168:171]\n"
"ds_read_b128 v[20:23], v175 offset:4080\n"
"v_cvt_pk_bf16_f32 v156, v156, v157\n"
"v_exp_f32 v159, v159\n"
"v_mfma_f32_16x16x32_bf16 v[140:143], v[244:247], v[144:147], v[140:143]\n"
"v_cvt_pk_bf16_f32 v157, v158, v159\n"
"v_mfma_f32_16x16x32_bf16 v[140:143], v[246:249], v[24:27], v[140:143]\n"
"ds_write_b64 v6, v[156:157] offset:57352\n"
"ds_read_b128 v[232:235], v5 offset:47104\n"
"s_waitcnt lgkmcnt(4)\n"
"v_mfma_f32_16x16x32_bf16 v[12:15], v[100:103], v[152:155], v[12:15]\n"
"v_exp_f32 v8, v8\n"
"v_mfma_f32_16x16x32_bf16 v[148:151], v[104:107], v[152:155], v[148:151]\n"
"s_waitcnt lgkmcnt(3)\n"
"ds_read_b128 v[156:159], v6 offset:9216\n"
"v_exp_f32 v9, v9\n"
"v_exp_f32 v10, v10\n"
"v_mfma_f32_16x16x32_bf16 v[168:171], v[160:163], v[152:155], v[168:171]\n"
"s_waitcnt lgkmcnt(3)\n"
"ds_read_b128 v[152:155], v4 offset:36864\n"
"v_cvt_pk_bf16_f32 v8, v8, v9\n"
"v_exp_f32 v11, v11\n"
"v_mfma_f32_16x16x32_bf16 v[20:23], v[226:229], v[144:147], v[20:23]\n"
"v_cvt_pk_bf16_f32 v9, v10, v11\n"
"v_bfe_u32 v177, v6, 2, 6\n"
"v_mfma_f32_16x16x32_bf16 v[20:23], v[228:231], v[24:27], v[20:23]\n"
"ds_write_b64 v6, v[8:9] offset:58368\n"
"ds_read_b32 v177, v177 offset:1664\n"
"s_waitcnt lgkmcnt(4)\n"
"v_mfma_f32_16x16x32_bf16 v[12:15], v[108:111], v[232:235], v[12:15]\n"
"s_add_u32 s69, s50, 4294965920\n"
"ds_read_b128 v[8:11], v5 offset:36864\n"
"v_mfma_f32_16x16x32_bf16 v[148:151], v[112:115], v[232:235], v[148:151]\n"
"s_waitcnt lgkmcnt(4)\n"
"v_exp_f32 v140, v140\n"
"v_exp_f32 v141, v141\n"
"v_exp_f32 v142, v142\n"
"v_mfma_f32_16x16x32_bf16 v[168:171], v[156:159], v[232:235], v[168:171]\n"
"s_waitcnt lgkmcnt(3)\n"
"v_exp_f32 v143, v143\n"
"v_cvt_pk_bf16_f32 v140, v140, v141\n"
"v_cvt_pk_bf16_f32 v141, v142, v143\n"
"v_mfma_f32_16x16x32_bf16 v[232:235], v[28:31], v[152:155], 0\n"
"ds_write_b64 v6, v[140:141] offset:58376\n"
"ds_read_b128 v[140:143], v4 offset:38912\n"
"v_mfma_f32_16x16x32_bf16 v[144:147], v[32:35], v[152:155], 0\n"
"s_waitcnt lgkmcnt(3)\n"
"v_add_u32_sdwa v176, v177, v7 dst_sel:DWORD dst_unused:UNUSED_PAD src0_sel:WORD_0 src1_sel:DWORD\n"
"s_nop 0\n"
"v_max_u32 v175, s69, v176\n"
"v_mfma_f32_16x16x32_bf16 v[156:159], v[36:39], v[152:155], 0\n"
"s_waitcnt lgkmcnt(2)\n"
"ds_read_b128 v[152:155], v175 offset:1392\n"
"v_mul_f32 v12, s60, v12\n"
"v_mfma_f32_16x16x32_bf16 v[232:235], v[40:43], v[8:11], v[232:235]\n"
"v_exp_f32 v20, v20\n"
"v_mfma_f32_16x16x32_bf16 v[144:147], v[44:47], v[8:11], v[144:147]\n"
"v_exp_f32 v21, v21\n"
"v_exp_f32 v22, v22\n"
"v_mfma_f32_16x16x32_bf16 v[156:159], v[48:51], v[8:11], v[156:159]\n"
"s_waitcnt lgkmcnt(1)\n"
"ds_read_b128 v[8:11], v5 offset:38912\n"
"v_cvt_pk_bf16_f32 v20, v20, v21\n"
"v_exp_f32 v23, v23\n"
"v_mfma_f32_16x16x32_bf16 v[232:235], v[52:55], v[140:143], v[232:235]\n"
"v_cvt_pk_bf16_f32 v21, v22, v23\n"
"v_mul_f32 v13, s60, v13\n"
"v_mfma_f32_16x16x32_bf16 v[144:147], v[56:59], v[140:143], v[144:147]\n"
"ds_write_b64 v6, v[20:21] offset:57344\n"
"v_cvt_pk_bf16_f32 v12, v12, v13\n"
"v_mul_f32 v14, s60, v14\n"
"v_mul_f32 v15, s60, v15\n"
"v_cvt_pk_bf16_f32 v13, v14, v15\n"
"v_mul_f32 v148, s60, v148\n"
"v_mul_f32 v149, s60, v149\n"
"v_mul_f32 v150, s60, v150\n"
"s_waitcnt lgkmcnt(0)\n"
"s_barrier\n"
"v_mul_f32 v151, s60, v151\n"
"v_cvt_pk_bf16_f32 v14, v148, v149\n"
"v_cvt_pk_bf16_f32 v15, v150, v151\n"
"v_mul_f32 v168, s60, v168\n"
"v_mul_f32 v169, s60, v169\n"
"v_mul_f32 v170, s60, v170\n"
"v_mfma_f32_16x16x32_bf16 v[152:155], v[244:247], v[12:15], v[152:155]\n"
"s_add_u32 s70, s50, 4294966960\n"
"v_mul_f32 v171, s60, v171\n"
"v_cvt_pk_bf16_f32 v26, v168, v169\n"
"v_cvt_pk_bf16_f32 v27, v170, v171\n"
"v_add_u32_sdwa v177, sext(v177), v176 dst_sel:DWORD dst_unused:UNUSED_PAD src0_sel:WORD_1 src1_sel:DWORD\n"
"s_nop 0\n"
"v_max_u32 v174, s69, v177\n"
"v_mfma_f32_16x16x32_bf16 v[152:155], v[246:249], v[24:27], v[152:155]\n"
"ds_read_b128 v[244:247], v174 offset:1408\n"
"v_mfma_f32_16x16x32_bf16 v[156:159], v[60:63], v[140:143], v[156:159]\n"
"ds_read_b128 v[20:23], v4 offset:40960\n"
"v_mfma_f32_16x16x32_bf16 v[232:235], v[64:67], v[8:11], v[232:235]\n"
"v_min_u32 v174, s70, v176\n"
"v_mfma_f32_16x16x32_bf16 v[144:147], v[68:71], v[8:11], v[144:147]\n"
"v_mfma_f32_16x16x32_bf16 v[156:159], v[72:75], v[8:11], v[156:159]\n"
"ds_read_b128 v[8:11], v174 offset:2720\n"
"v_exp_f32 v152, v152\n"
"v_exp_f32 v153, v153\n"
"v_exp_f32 v154, v154\n"
"s_waitcnt lgkmcnt(2)\n"
"v_mfma_f32_16x16x32_bf16 v[244:247], v[250:253], v[12:15], v[244:247]\n"
"v_exp_f32 v155, v155\n"
"v_mfma_f32_16x16x32_bf16 v[244:247], v[252:255], v[24:27], v[244:247]\n"
"s_waitcnt lgkmcnt(1)\n"
"ds_read_b128 v[248:251], v5 offset:40960\n"
"v_cvt_pk_bf16_f32 v152, v152, v153\n"
"v_mfma_f32_16x16x32_bf16 v[232:235], v[76:79], v[20:23], v[232:235]\n"
"v_cvt_pk_bf16_f32 v153, v154, v155\n"
"v_mfma_f32_16x16x32_bf16 v[144:147], v[80:83], v[20:23], v[144:147]\n"
"ds_write_b64 v6, v[152:153] offset:55296\n"
"v_mfma_f32_16x16x32_bf16 v[156:159], v[84:87], v[20:23], v[156:159]\n"
"s_waitcnt lgkmcnt(2)\n"
"ds_read_b128 v[252:255], v174 offset:2736\n"
"v_mfma_f32_16x16x32_bf16 v[8:11], v[190:193], v[12:15], v[8:11]\n"
"v_exp_f32 v244, v244\n"
"v_mfma_f32_16x16x32_bf16 v[8:11], v[192:195], v[24:27], v[8:11]\n"
"s_waitcnt lgkmcnt(2)\n"
"ds_read_b128 v[20:23], v4 offset:49152\n"
"v_exp_f32 v245, v245\n"
"v_mfma_f32_16x16x32_bf16 v[232:235], v[88:91], v[248:251], v[232:235]\n"
"v_exp_f32 v246, v246\n"
"v_mfma_f32_16x16x32_bf16 v[144:147], v[92:95], v[248:251], v[144:147]\n"
"ds_read_b128 v[140:143], v6 offset:4096\n"
"v_cvt_pk_bf16_f32 v244, v244, v245\n"
"v_mfma_f32_16x16x32_bf16 v[156:159], v[96:99], v[248:251], v[156:159]\n"
"v_exp_f32 v247, v247\n"
"v_min_u32 v174, s70, v177\n"
"v_cvt_pk_bf16_f32 v245, v246, v247\n"
"ds_write_b64 v6, v[244:245] offset:55304\n"
"v_cvt_pk_bf16_f32 v232, v232, v233\n"
"ds_read_b128 v[244:247], v174 offset:2752\n"
"v_cvt_pk_bf16_f32 v233, v234, v235\n"
"s_waitcnt lgkmcnt(4)\n"
"v_mfma_f32_16x16x32_bf16 v[252:255], v[196:199], v[12:15], v[252:255]\n"
"v_cvt_pk_bf16_f32 v234, v144, v145\n"
"v_cvt_pk_bf16_f32 v235, v146, v147\n"
"v_mfma_f32_16x16x32_bf16 v[252:255], v[198:201], v[24:27], v[252:255]\n"
"ds_read_b128 v[248:251], v5 offset:49152\n"
"v_cvt_pk_bf16_f32 v236, v156, v157\n"
"v_cvt_pk_bf16_f32 v237, v158, v159\n"
"v_mov_b32 v8, 0\n"
"s_waitcnt lgkmcnt(4)\n"
"v_mfma_f32_16x16x32_bf16 v[144:147], v[116:119], v[20:23], 0\n"
"v_exp_f32 v10, v10\n"
"v_mfma_f32_16x16x32_bf16 v[148:151], v[120:123], v[20:23], 0\n"
"s_waitcnt lgkmcnt(3)\n"
"ds_read_b128 v[152:155], v6 offset:5120\n"
"v_mfma_f32_16x16x32_bf16 v[156:159], v[140:143], v[20:23], 0\n"
"s_add_u32 s71, s50, 4294965616\n"
"ds_read_b128 v[20:23], v175 offset:1376\n"
"v_exp_f32 v11, v11\n"
"s_waitcnt lgkmcnt(3)\n"
"v_mfma_f32_16x16x32_bf16 v[244:247], v[202:205], v[12:15], v[244:247]\n"
"v_cvt_pk_bf16_f32 v9, v10, v11\n"
"v_mfma_f32_16x16x32_bf16 v[244:247], v[204:207], v[24:27], v[244:247]\n"
"ds_write_b64 v6, v[8:9] offset:56320\n"
"ds_read_b128 v[8:11], v4 offset:51200\n"
"s_waitcnt lgkmcnt(4)\n"
"v_mfma_f32_16x16x32_bf16 v[144:147], v[124:127], v[248:251], v[144:147]\n"
"v_exp_f32 v252, v252\n"
"v_mfma_f32_16x16x32_bf16 v[148:151], v[128:131], v[248:251], v[148:151]\n"
"ds_read_b128 v[140:143], v6 offset:6144\n"
"v_min_u32 v175, s71, v176\n"
"v_exp_f32 v253, v253\n"
"v_exp_f32 v254, v254\n"
"s_waitcnt lgkmcnt(4)\n"
"v_mfma_f32_16x16x32_bf16 v[156:159], v[152:155], v[248:251], v[156:159]\n"
"s_waitcnt lgkmcnt(3)\n"
"ds_read_b128 v[248:251], v175 offset:4064\n"
"v_cvt_pk_bf16_f32 v252, v252, v253\n"
"v_exp_f32 v255, v255\n"
"v_mfma_f32_16x16x32_bf16 v[20:23], v[238:241], v[12:15], v[20:23]\n"
"v_cvt_pk_bf16_f32 v253, v254, v255\n"
"v_mfma_f32_16x16x32_bf16 v[20:23], v[240:243], v[24:27], v[20:23]\n"
"ds_write_b64 v6, v[252:253] offset:56328\n"
"ds_read_b128 v[238:241], v5 offset:51200\n"
"s_waitcnt lgkmcnt(4)\n"
"v_mfma_f32_16x16x32_bf16 v[144:147], v[0:3], v[8:11], v[144:147]\n"
"v_exp_f32 v244, v244\n"
"v_mfma_f32_16x16x32_bf16 v[148:151], v[16:19], v[8:11], v[148:151]\n"
"s_waitcnt lgkmcnt(3)\n"
"ds_read_b128 v[252:255], v6 offset:7168\n"
"v_exp_f32 v245, v245\n"
"v_exp_f32 v246, v246\n"
"v_mfma_f32_16x16x32_bf16 v[156:159], v[140:143], v[8:11], v[156:159]\n"
"s_waitcnt lgkmcnt(3)\n"
"ds_read_b128 v[8:11], v175 offset:4080\n"
"v_cvt_pk_bf16_f32 v244, v244, v245\n"
"v_exp_f32 v247, v247\n"
"v_mfma_f32_16x16x32_bf16 v[248:251], v[220:223], v[12:15], v[248:251]\n"
"v_cvt_pk_bf16_f32 v245, v246, v247\n"
"v_mov_b32 v20, 0\n"
"v_mfma_f32_16x16x32_bf16 v[248:251], v[222:225], v[24:27], v[248:251]\n"
"ds_write_b64 v6, v[244:245] offset:33288\n"
"ds_read_b128 v[242:245], v4 offset:53248\n"
"s_waitcnt lgkmcnt(4)\n"
"v_mfma_f32_16x16x32_bf16 v[144:147], v[132:135], v[238:241], v[144:147]\n"
"v_exp_f32 v22, v22\n"
"v_mfma_f32_16x16x32_bf16 v[148:151], v[136:139], v[238:241], v[148:151]\n"
"s_waitcnt lgkmcnt(3)\n"
"ds_read_b128 v[140:143], v6 offset:8192\n"
"v_min_u32 v21, s71, v177\n"
"v_mfma_f32_16x16x32_bf16 v[156:159], v[252:255], v[238:241], v[156:159]\n"
"s_waitcnt lgkmcnt(2)\n"
"ds_read_b128 v[252:255], v21 offset:4096\n"
"s_barrier\n"
"v_exp_f32 v23, v23\n"
"v_mfma_f32_16x16x32_bf16 v[8:11], v[226:229], v[12:15], v[8:11]\n"
"v_cvt_pk_bf16_f32 v21, v22, v23\n"
"v_mov_b32 v248, 0\n"
"v_mfma_f32_16x16x32_bf16 v[8:11], v[228:231], v[24:27], v[8:11]\n"
"ds_write_b64 v6, v[20:21] offset:58376\n"
"ds_read_b128 v[238:241], v5 offset:53248\n"
"s_waitcnt lgkmcnt(4)\n"
"v_mfma_f32_16x16x32_bf16 v[144:147], v[100:103], v[242:245], v[144:147]\n"
"v_exp_f32 v250, v250\n"
"v_mfma_f32_16x16x32_bf16 v[148:151], v[104:107], v[242:245], v[148:151]\n"
"s_waitcnt lgkmcnt(3)\n"
"ds_read_b128 v[20:23], v6 offset:9216\n"
"v_mfma_f32_16x16x32_bf16 v[156:159], v[140:143], v[242:245], v[156:159]\n"
"s_waitcnt lgkmcnt(3)\n"
"ds_read_b128 v[242:245], v4 offset:0\n"
"v_exp_f32 v251, v251\n"
"v_mfma_f32_16x16x32_bf16 v[252:255], v[232:235], v[12:15], v[252:255]\n"
"v_cvt_pk_bf16_f32 v249, v250, v251\n"
"v_bfe_u32 v177, v6, 2, 6\n"
"v_mfma_f32_16x16x32_bf16 v[252:255], v[234:237], v[24:27], v[252:255]\n"
"ds_write_b64 v6, v[248:249] offset:57344\n"
"ds_read_b32 v177, v177 offset:1728\n"
"s_waitcnt lgkmcnt(4)\n"
"v_mfma_f32_16x16x32_bf16 v[144:147], v[108:111], v[238:241], v[144:147]\n"
"s_add_u32 s72, s50, 4294964592\n"
"ds_read_b128 v[246:249], v5 offset:0\n"
"v_mfma_f32_16x16x32_bf16 v[148:151], v[112:115], v[238:241], v[148:151]\n"
"s_waitcnt lgkmcnt(4)\n"
"v_exp_f32 v8, v8\n"
"v_exp_f32 v9, v9\n"
"v_exp_f32 v10, v10\n"
"v_mfma_f32_16x16x32_bf16 v[156:159], v[20:23], v[238:241], v[156:159]\n"
"s_waitcnt lgkmcnt(3)\n"
"v_exp_f32 v11, v11\n"
"v_cvt_pk_bf16_f32 v8, v8, v9\n"
"v_cvt_pk_bf16_f32 v9, v10, v11\n"
"v_mfma_f32_16x16x32_bf16 v[238:241], v[28:31], v[242:245], 0\n"
"ds_write_b64 v6, v[8:9] offset:57352\n"
"ds_read_b128 v[8:11], v4 offset:2048\n"
"v_mfma_f32_16x16x32_bf16 v[12:15], v[32:35], v[242:245], 0\n"
"s_waitcnt lgkmcnt(3)\n"
"v_add_u32_sdwa v176, v177, v7 dst_sel:DWORD dst_unused:UNUSED_PAD src0_sel:WORD_0 src1_sel:DWORD\n"
"s_nop 0\n"
"v_max_u32 v251, s72, v176\n"
"v_mfma_f32_16x16x32_bf16 v[20:23], v[36:39], v[242:245], 0\n"
"s_waitcnt lgkmcnt(2)\n"
"ds_read_b128 v[242:245], v251 offset:2704\n"
"v_mul_f32 v144, s60, v144\n"
"v_mfma_f32_16x16x32_bf16 v[238:241], v[40:43], v[246:249], v[238:241]\n"
"v_exp_f32 v252, v252\n"
"v_mfma_f32_16x16x32_bf16 v[12:15], v[44:47], v[246:249], v[12:15]\n"
"v_exp_f32 v253, v253\n"
"v_exp_f32 v254, v254\n"
"v_mfma_f32_16x16x32_bf16 v[20:23], v[48:51], v[246:249], v[20:23]\n"
"s_waitcnt lgkmcnt(1)\n"
"ds_read_b128 v[246:249], v5 offset:2048\n"
"v_cvt_pk_bf16_f32 v252, v252, v253\n"
"v_exp_f32 v255, v255\n"
"v_mfma_f32_16x16x32_bf16 v[238:241], v[52:55], v[8:11], v[238:241]\n"
"v_cvt_pk_bf16_f32 v253, v254, v255\n"
"v_mul_f32 v145, s60, v145\n"
"v_mfma_f32_16x16x32_bf16 v[12:15], v[56:59], v[8:11], v[12:15]\n"
"ds_write_b64 v6, v[252:253] offset:58368\n"
"v_cvt_pk_bf16_f32 v144, v144, v145\n"
"v_mul_f32 v146, s60, v146\n"
"v_mul_f32 v147, s60, v147\n"
"v_cvt_pk_bf16_f32 v145, v146, v147\n"
"v_mul_f32 v148, s60, v148\n"
"v_mul_f32 v149, s60, v149\n"
"v_mul_f32 v150, s60, v150\n"
"s_waitcnt lgkmcnt(0)\n"
"s_barrier\n"
"v_mul_f32 v151, s60, v151\n"
"v_cvt_pk_bf16_f32 v146, v148, v149\n"
"v_cvt_pk_bf16_f32 v147, v150, v151\n"
"v_mul_f32 v156, s60, v156\n"
"v_mul_f32 v157, s60, v157\n"
"v_mul_f32 v158, s60, v158\n"
"v_mfma_f32_16x16x32_bf16 v[242:245], v[184:187], v[144:147], v[242:245]\n"
"ds_read_b128 v[252:255], v4 offset:4096\n"
"v_mul_f32 v159, s60, v159\n"
"v_cvt_pk_bf16_f32 v26, v156, v157\n"
"v_cvt_pk_bf16_f32 v27, v158, v159\n"
"v_add_u32_sdwa v177, sext(v177), v176 dst_sel:DWORD dst_unused:UNUSED_PAD src0_sel:WORD_1 src1_sel:DWORD\n"
"s_nop 0\n"
"v_mfma_f32_16x16x32_bf16 v[242:245], v[186:189], v[24:27], v[242:245]\n"
"v_mfma_f32_16x16x32_bf16 v[20:23], v[60:63], v[8:11], v[20:23]\n"
"ds_read_b128 v[8:11], v251 offset:2720\n"
"v_mfma_f32_16x16x32_bf16 v[238:241], v[64:67], v[246:249], v[238:241]\n"
"v_mfma_f32_16x16x32_bf16 v[12:15], v[68:71], v[246:249], v[12:15]\n"
"v_mfma_f32_16x16x32_bf16 v[20:23], v[72:75], v[246:249], v[20:23]\n"
"s_waitcnt lgkmcnt(1)\n"
"ds_read_b128 v[246:249], v5 offset:4096\n"
"v_mfma_f32_16x16x32_bf16 v[238:241], v[76:79], v[252:255], v[238:241]\n"
"v_mfma_f32_16x16x32_bf16 v[12:15], v[80:83], v[252:255], v[12:15]\n"
"v_exp_f32 v244, v244\n"
"v_exp_f32 v245, v245\n"
"v_mov_b32 v242, 0\n"
"v_cvt_pk_bf16_f32 v243, v244, v245\n"
"s_waitcnt lgkmcnt(1)\n"
"v_mfma_f32_16x16x32_bf16 v[8:11], v[190:193], v[144:147], v[8:11]\n"
"ds_read_b128 v[140:143], v4 offset:6144\n"
"v_mfma_f32_16x16x32_bf16 v[8:11], v[192:195], v[24:27], v[8:11]\n"
"ds_write_b64 v6, v[242:243] offset:53248\n"
"v_mfma_f32_16x16x32_bf16 v[20:23], v[84:87], v[252:255], v[20:23]\n"
"s_waitcnt lgkmcnt(2)\n"
"ds_read_b128 v[252:255], v251 offset:2736\n"
"v_mfma_f32_16x16x32_bf16 v[238:241], v[88:91], v[246:249], v[238:241]\n"
"v_mfma_f32_16x16x32_bf16 v[12:15], v[92:95], v[246:249], v[12:15]\n"
"v_mfma_f32_16x16x32_bf16 v[20:23], v[96:99], v[246:249], v[20:23]\n"
"s_waitcnt lgkmcnt(2)\n"
"ds_read_b128 v[248:251], v5 offset:6144\n"
"v_mfma_f32_16x16x32_bf16 v[244:247], v[28:31], v[140:143], 0\n"
"v_exp_f32 v8, v8\n"
"v_mfma_f32_16x16x32_bf16 v[148:151], v[32:35], v[140:143], 0\n"
"v_exp_f32 v9, v9\n"
"v_exp_f32 v10, v10\n"
"v_cvt_pk_bf16_f32 v238, v238, v239\n"
"v_cvt_pk_bf16_f32 v239, v240, v241\n"
"v_cvt_pk_bf16_f32 v240, v12, v13\n"
"v_cvt_pk_bf16_f32 v241, v14, v15\n"
"v_cvt_pk_bf16_f32 v242, v20, v21\n"
"v_cvt_pk_bf16_f32 v243, v22, v23\n"
"s_waitcnt lgkmcnt(1)\n"
"v_mfma_f32_16x16x32_bf16 v[252:255], v[196:199], v[144:147], v[252:255]\n"
"ds_read_b128 v[12:15], v4 offset:8192\n"
"v_max_u32 v175, s72, v177\n"
"v_mfma_f32_16x16x32_bf16 v[252:255], v[198:201], v[24:27], v[252:255]\n"
"v_exp_f32 v11, v11\n"
"v_mfma_f32_16x16x32_bf16 v[20:23], v[36:39], v[140:143], 0\n"
"s_waitcnt lgkmcnt(1)\n"
"ds_read_b128 v[140:143], v175 offset:2752\n"
"v_cvt_pk_bf16_f32 v8, v8, v9\n"
"v_mfma_f32_16x16x32_bf16 v[244:247], v[40:43], v[248:251], v[244:247]\n"
"v_cvt_pk_bf16_f32 v9, v10, v11\n"
"v_mfma_f32_16x16x32_bf16 v[148:151], v[44:47], v[248:251], v[148:151]\n"
"s_add_u32 s73, s50, 4294965632\n"
"ds_write_b64 v6, v[8:9] offset:53256\n"
"v_mfma_f32_16x16x32_bf16 v[20:23], v[48:51], v[248:251], v[20:23]\n"
"s_waitcnt lgkmcnt(2)\n"
"ds_read_b128 v[248:251], v5 offset:8192\n"
"v_exp_f32 v252, v252\n"
"v_mfma_f32_16x16x32_bf16 v[244:247], v[52:55], v[12:15], v[244:247]\n"
"v_exp_f32 v253, v253\n"
"v_mfma_f32_16x16x32_bf16 v[148:151], v[56:59], v[12:15], v[148:151]\n"
"s_waitcnt lgkmcnt(2)\n"
"v_min_u32 v175, s73, v176\n"
"v_cvt_pk_bf16_f32 v252, v252, v253\n"
"v_mfma_f32_16x16x32_bf16 v[140:143], v[202:205], v[144:147], v[140:143]\n"
"ds_read_b128 v[8:11], v4 offset:10240\n"
"v_mfma_f32_16x16x32_bf16 v[140:143], v[204:207], v[24:27], v[140:143]\n"
"ds_read_b128 v[152:155], v175 offset:4048\n"
"v_mfma_f32_16x16x32_bf16 v[20:23], v[60:63], v[12:15], v[20:23]\n"
"s_waitcnt lgkmcnt(2)\n"
"v_exp_f32 v254, v254\n"
"v_exp_f32 v255, v255\n"
"v_mfma_f32_16x16x32_bf16 v[244:247], v[64:67], v[248:251], v[244:247]\n"
"v_cvt_pk_bf16_f32 v253, v254, v255\n"
"v_mfma_f32_16x16x32_bf16 v[148:151], v[68:71], v[248:251], v[148:151]\n"
"ds_write_b64 v6, v[252:253] offset:54272\n"
"v_mfma_f32_16x16x32_bf16 v[20:23], v[72:75], v[248:251], v[20:23]\n"
"s_waitcnt lgkmcnt(2)\n"
"ds_read_b128 v[248:251], v5 offset:10240\n"
"v_mfma_f32_16x16x32_bf16 v[244:247], v[76:79], v[8:11], v[244:247]\n"
"s_waitcnt lgkmcnt(2)\n"
"v_exp_f32 v140, v140\n"
"v_mfma_f32_16x16x32_bf16 v[152:155], v[214:217], v[144:147], v[152:155]\n"
"v_exp_f32 v141, v141\n"
"v_mfma_f32_16x16x32_bf16 v[152:155], v[216:219], v[24:27], v[152:155]\n"
"ds_read_b128 v[252:255], v4 offset:12288\n"
"v_cvt_pk_bf16_f32 v140, v140, v141\n"
"v_mfma_f32_16x16x32_bf16 v[148:151], v[80:83], v[8:11], v[148:151]\n"
"ds_read_b128 v[12:15], v175 offset:4064\n"
"v_mfma_f32_16x16x32_bf16 v[20:23], v[84:87], v[8:11], v[20:23]\n"
"s_waitcnt lgkmcnt(2)\n"
"v_exp_f32 v142, v142\n"
"v_exp_f32 v143, v143\n"
"v_mfma_f32_16x16x32_bf16 v[244:247], v[88:91], v[248:251], v[244:247]\n"
"v_cvt_pk_bf16_f32 v141, v142, v143\n"
"v_mov_b32 v152, 0\n"
"v_mfma_f32_16x16x32_bf16 v[148:151], v[92:95], v[248:251], v[148:151]\n"
"ds_write_b64 v6, v[140:141] offset:54280\n"
"ds_read_b128 v[8:11], v6 offset:4096\n"
"v_mfma_f32_16x16x32_bf16 v[20:23], v[96:99], v[248:251], v[20:23]\n"
"s_waitcnt lgkmcnt(3)\n"
"ds_read_b128 v[140:143], v5 offset:12288\n"
"v_mfma_f32_16x16x32_bf16 v[156:159], v[116:119], v[252:255], 0\n"
"s_waitcnt lgkmcnt(3)\n"
"v_cvt_pk_bf16_f32 v244, v244, v245\n"
"v_cvt_pk_bf16_f32 v245, v246, v247\n"
"v_mfma_f32_16x16x32_bf16 v[12:15], v[220:223], v[144:147], v[12:15]\n"
"s_waitcnt lgkmcnt(2)\n"
"ds_read_b128 v[160:163], v6 offset:5120\n"
"v_cvt_pk_bf16_f32 v246, v148, v149\n"
"s_barrier\n"
"v_cvt_pk_bf16_f32 v247, v150, v151\n"
"v_cvt_pk_bf16_f32 v248, v20, v21\n"
"v_cvt_pk_bf16_f32 v249, v22, v23\n"
"v_mfma_f32_16x16x32_bf16 v[12:15], v[222:225], v[24:27], v[12:15]\n"
"ds_read_b128 v[20:23], v4 offset:14336\n"
"v_mfma_f32_16x16x32_bf16 v[148:151], v[120:123], v[252:255], 0\n"
"s_waitcnt lgkmcnt(3)\n"
"ds_read_b128 v[164:167], v175 offset:4080\n"
"v_mfma_f32_16x16x32_bf16 v[168:171], v[8:11], v[252:255], 0\n"
"s_waitcnt lgkmcnt(3)\n"
"v_exp_f32 v154, v154\n"
"v_mfma_f32_16x16x32_bf16 v[156:159], v[124:127], v[140:143], v[156:159]\n"
"v_exp_f32 v155, v155\n"
"v_mfma_f32_16x16x32_bf16 v[148:151], v[128:131], v[140:143], v[148:151]\n"
"s_waitcnt lgkmcnt(2)\n"
"ds_read_b128 v[250:253], v6 offset:6144\n"
"v_cvt_pk_bf16_f32 v153, v154, v155\n"
"v_mfma_f32_16x16x32_bf16 v[168:171], v[160:163], v[140:143], v[168:171]\n"
"ds_write_b64 v6, v[152:153] offset:57344\n"
"ds_read_b128 v[8:11], v5 offset:14336\n"
"v_exp_f32 v12, v12\n"
"s_waitcnt lgkmcnt(4)\n"
"v_mfma_f32_16x16x32_bf16 v[156:159], v[0:3], v[20:23], v[156:159]\n"
"s_waitcnt lgkmcnt(3)\n"
"v_exp_f32 v13, v13\n"
"v_mfma_f32_16x16x32_bf16 v[164:167], v[226:229], v[144:147], v[164:167]\n"
"ds_read_b128 v[140:143], v6 offset:7168\n"
"v_min_u32 v176, s73, v177\n"
"v_mfma_f32_16x16x32_bf16 v[164:167], v[228:231], v[24:27], v[164:167]\n"
"ds_read_b128 v[152:155], v4 offset:16384\n"
"v_cvt_pk_bf16_f32 v12, v12, v13\n"
"v_mfma_f32_16x16x32_bf16 v[148:151], v[16:19], v[20:23], v[148:151]\n"
"s_waitcnt lgkmcnt(4)\n"
"ds_read_b128 v[160:163], v176 offset:4096\n"
"v_mfma_f32_16x16x32_bf16 v[168:171], v[250:253], v[20:23], v[168:171]\n"
"s_waitcnt lgkmcnt(3)\n"
"v_exp_f32 v14, v14\n"
"v_exp_f32 v15, v15\n"
"v_mfma_f32_16x16x32_bf16 v[156:159], v[132:135], v[8:11], v[156:159]\n"
"v_cvt_pk_bf16_f32 v13, v14, v15\n"
"v_mfma_f32_16x16x32_bf16 v[148:151], v[136:139], v[8:11], v[148:151]\n"
"ds_write_b64 v6, v[12:13] offset:57352\n"
"ds_read_b128 v[250:253], v6 offset:8192\n"
"s_waitcnt lgkmcnt(4)\n"
"v_mfma_f32_16x16x32_bf16 v[168:171], v[140:143], v[8:11], v[168:171]\n"
"s_waitcnt lgkmcnt(3)\n"
"ds_read_b128 v[8:11], v5 offset:16384\n"
"v_mfma_f32_16x16x32_bf16 v[156:159], v[100:103], v[152:155], v[156:159]\n"
"s_waitcnt lgkmcnt(3)\n"
"v_exp_f32 v164, v164\n"
"v_mfma_f32_16x16x32_bf16 v[160:163], v[232:235], v[144:147], v[160:163]\n"
"ds_read_b128 v[12:15], v6 offset:9216\n"
"v_mfma_f32_16x16x32_bf16 v[160:163], v[234:237], v[24:27], v[160:163]\n"
"ds_read_b128 v[20:23], v4 offset:55296\n"
"v_mfma_f32_16x16x32_bf16 v[148:151], v[104:107], v[152:155], v[148:151]\n"
"s_waitcnt lgkmcnt(3)\n"
"v_exp_f32 v165, v165\n"
"v_exp_f32 v166, v166\n"
"v_exp_f32 v167, v167\n"
"v_mfma_f32_16x16x32_bf16 v[168:171], v[250:253], v[152:155], v[168:171]\n"
"s_waitcnt lgkmcnt(2)\n"
"v_cvt_pk_bf16_f32 v164, v164, v165\n"
"v_cvt_pk_bf16_f32 v165, v166, v167\n"
"v_mfma_f32_16x16x32_bf16 v[156:159], v[108:111], v[8:11], v[156:159]\n"
"s_add_u32 s68, s50, 4294964608\n"
"ds_write_b64 v6, v[164:165] offset:58368\n"
"v_bfe_u32 v177, v6, 2, 6\n"
"v_mfma_f32_16x16x32_bf16 v[148:151], v[112:115], v[8:11], v[148:151]\n"
"s_waitcnt lgkmcnt(2)\n"
"ds_read_b32 v177, v177 offset:1792\n"
"v_mfma_f32_16x16x32_bf16 v[168:171], v[12:15], v[8:11], v[168:171]\n"
"s_waitcnt lgkmcnt(2)\n"
"ds_read_b128 v[8:11], v5 offset:55296\n"
"v_mfma_f32_16x16x32_bf16 v[250:253], v[28:31], v[20:23], 0\n"
"ds_read_b128 v[12:15], v4 offset:57344\n"
"v_mul_f32 v156, s60, v156\n"
"v_mfma_f32_16x16x32_bf16 v[140:143], v[32:35], v[20:23], 0\n"
"v_exp_f32 v160, v160\n"
"v_mfma_f32_16x16x32_bf16 v[144:147], v[36:39], v[20:23], 0\n"
"s_waitcnt lgkmcnt(2)\n"
"v_exp_f32 v161, v161\n"
"v_exp_f32 v162, v162\n"
"v_exp_f32 v163, v163\n"
"v_add_u32_sdwa v255, v177, v7 dst_sel:DWORD dst_unused:UNUSED_PAD src0_sel:WORD_0 src1_sel:DWORD\n"
"s_nop 0\n"
"v_max_u32 v177, s68, v255\n"
"ds_read_b128 v[20:23], v177 offset:2688\n"
"v_cvt_pk_bf16_f32 v160, v160, v161\n"
"s_waitcnt lgkmcnt(2)\n"
"v_mfma_f32_16x16x32_bf16 v[250:253], v[40:43], v[8:11], v[250:253]\n"
"s_add_u32 s69, s50, 4294963264\n"
"v_cvt_pk_bf16_f32 v161, v162, v163\n"
"v_mul_f32 v157, s60, v157\n"
"v_mfma_f32_16x16x32_bf16 v[140:143], v[44:47], v[8:11], v[140:143]\n"
"ds_write_b64 v6, v[160:161] offset:58376\n"
"v_cvt_pk_bf16_f32 v156, v156, v157\n"
"v_mfma_f32_16x16x32_bf16 v[144:147], v[48:51], v[8:11], v[144:147]\n"
"s_waitcnt lgkmcnt(2)\n"
"ds_read_b128 v[8:11], v5 offset:57344\n"
"v_mul_f32 v158, s60, v158\n"
"v_mfma_f32_16x16x32_bf16 v[250:253], v[52:55], v[12:15], v[250:253]\n"
"v_mul_f32 v159, s60, v159\n"
"v_cvt_pk_bf16_f32 v157, v158, v159\n"
"v_mfma_f32_16x16x32_bf16 v[140:143], v[56:59], v[12:15], v[140:143]\n"
"s_waitcnt lgkmcnt(1)\n"
"v_mul_f32 v148, s60, v148\n"
"v_mul_f32 v149, s60, v149\n"
"s_barrier\n"
"v_mul_f32 v150, s60, v150\n"
"v_mul_f32 v151, s60, v151\n"
"v_cvt_pk_bf16_f32 v158, v148, v149\n"
"v_cvt_pk_bf16_f32 v159, v150, v151\n"
"v_mul_f32 v168, s60, v168\n"
"v_mul_f32 v169, s60, v169\n"
"v_mfma_f32_16x16x32_bf16 v[20:23], v[178:181], v[156:159], v[20:23]\n"
"v_mul_f32 v170, s60, v170\n"
"v_mul_f32 v171, s60, v171\n"
"v_cvt_pk_bf16_f32 v26, v168, v169\n"
"v_cvt_pk_bf16_f32 v27, v170, v171\n"
"v_max_u32 v176, s69, v255\n"
"s_nop 0\n"
"v_mfma_f32_16x16x32_bf16 v[20:23], v[180:183], v[24:27], v[20:23]\n"
"ds_read_b128 v[178:181], v176 offset:4032\n"
"v_mfma_f32_16x16x32_bf16 v[144:147], v[60:63], v[12:15], v[144:147]\n"
"s_waitcnt lgkmcnt(1)\n"
"ds_read_b128 v[12:15], v4 offset:59392\n"
"v_mfma_f32_16x16x32_bf16 v[250:253], v[64:67], v[8:11], v[250:253]\n"
"v_mfma_f32_16x16x32_bf16 v[140:143], v[68:71], v[8:11], v[140:143]\n"
"v_mfma_f32_16x16x32_bf16 v[144:147], v[72:75], v[8:11], v[144:147]\n"
"s_waitcnt lgkmcnt(1)\n"
"ds_read_b128 v[8:11], v177 offset:2704\n"
"v_mfma_f32_16x16x32_bf16 v[178:181], v[208:211], v[156:159], v[178:181]\n"
"v_exp_f32 v20, v20\n"
"v_mfma_f32_16x16x32_bf16 v[178:181], v[210:213], v[24:27], v[178:181]\n"
"s_waitcnt lgkmcnt(1)\n"
"ds_read_b128 v[208:211], v5 offset:59392\n"
"v_mfma_f32_16x16x32_bf16 v[250:253], v[76:79], v[12:15], v[250:253]\n"
"v_exp_f32 v21, v21\n"
"v_mfma_f32_16x16x32_bf16 v[140:143], v[80:83], v[12:15], v[140:143]\n"
"v_exp_f32 v22, v22\n"
"v_mfma_f32_16x16x32_bf16 v[144:147], v[84:87], v[12:15], v[144:147]\n"
"s_add_u32 s70, s50, 4294964304\n"
"ds_read_b128 v[12:15], v176 offset:4048\n"
"v_cvt_pk_bf16_f32 v20, v20, v21\n"
"v_exp_f32 v23, v23\n"
"s_waitcnt lgkmcnt(2)\n"
"v_mfma_f32_16x16x32_bf16 v[8:11], v[184:187], v[156:159], v[8:11]\n"
"v_cvt_pk_bf16_f32 v21, v22, v23\n"
"v_mfma_f32_16x16x32_bf16 v[8:11], v[186:189], v[24:27], v[8:11]\n"
"ds_write_b64 v6, v[20:21] offset:55296\n"
"ds_read_b128 v[20:23], v4 offset:24576\n"
"s_waitcnt lgkmcnt(3)\n"
"v_mfma_f32_16x16x32_bf16 v[250:253], v[88:91], v[208:211], v[250:253]\n"
"v_exp_f32 v178, v178\n"
"v_mfma_f32_16x16x32_bf16 v[140:143], v[92:95], v[208:211], v[140:143]\n"
"ds_read_b128 v[148:151], v6 offset:4096\n"
"v_min_u32 v213, s70, v255\n"
"s_mov_b64 exec, s[58:59]\n"
"v_add_u32 v213, 1040, v213\n"
"s_mov_b64 exec, -1\n"
"v_exp_f32 v179, v179\n"
"v_exp_f32 v180, v180\n"
"v_mfma_f32_16x16x32_bf16 v[144:147], v[96:99], v[208:211], v[144:147]\n"
"ds_read_b128 v[208:211], v213 offset:5376\n"
"v_cvt_pk_bf16_f32 v178, v178, v179\n"
"v_exp_f32 v181, v181\n"
"v_cvt_pk_bf16_f32 v250, v250, v251\n"
"v_cvt_pk_bf16_f32 v179, v180, v181\n"
"ds_write_b64 v6, v[178:179] offset:56320\n"
"v_cvt_pk_bf16_f32 v251, v252, v253\n"
"s_waitcnt lgkmcnt(5)\n"
"v_mfma_f32_16x16x32_bf16 v[12:15], v[214:217], v[156:159], v[12:15]\n"
"v_cvt_pk_bf16_f32 v252, v140, v141\n"
"v_cvt_pk_bf16_f32 v253, v142, v143\n"
"v_mfma_f32_16x16x32_bf16 v[12:15], v[216:219], v[24:27], v[12:15]\n"
"s_waitcnt lgkmcnt(3)\n"
"ds_read_b128 v[178:181], v5 offset:24576\n"
"v_cvt_pk_bf16_f32 v254, v144, v145\n"
"v_cvt_pk_bf16_f32 v255, v146, v147\n"
"v_mfma_f32_16x16x32_bf16 v[140:143], v[116:119], v[20:23], 0\n"
"v_exp_f32 v8, v8\n"
"v_mfma_f32_16x16x32_bf16 v[144:147], v[120:123], v[20:23], 0\n"
"s_waitcnt lgkmcnt(3)\n"
"ds_read_b128 v[152:155], v6 offset:5120\n"
"v_exp_f32 v9, v9\n"
"v_exp_f32 v10, v10\n"
"v_mfma_f32_16x16x32_bf16 v[160:163], v[148:151], v[20:23], 0\n"
"s_waitcnt lgkmcnt(3)\n"
"ds_read_b128 v[20:23], v177 offset:2720\n"
"v_cvt_pk_bf16_f32 v8, v8, v9\n"
"v_exp_f32 v11, v11\n"
"v_mfma_f32_16x16x32_bf16 v[208:211], v[238:241], v[156:159], v[208:211]\n"
"v_cvt_pk_bf16_f32 v9, v10, v11\n"
"v_mfma_f32_16x16x32_bf16 v[208:211], v[240:243], v[24:27], v[208:211]\n"
"ds_write_b64 v6, v[8:9] offset:55304\n"
"ds_read_b128 v[238:241], v4 offset:26624\n"
"s_waitcnt lgkmcnt(4)\n"
"v_mfma_f32_16x16x32_bf16 v[140:143], v[124:127], v[178:181], v[140:143]\n"
"v_exp_f32 v12, v12\n"
"v_mfma_f32_16x16x32_bf16 v[144:147], v[128:131], v[178:181], v[144:147]\n"
"s_waitcnt lgkmcnt(3)\n"
"ds_read_b128 v[8:11], v6 offset:6144\n"
"v_exp_f32 v13, v13\n"
"v_exp_f32 v14, v14\n"
"v_mfma_f32_16x16x32_bf16 v[160:163], v[152:155], v[178:181], v[160:163]\n"
"s_waitcnt lgkmcnt(3)\n"
"ds_read_b128 v[178:181], v176 offset:4064\n"
"v_cvt_pk_bf16_f32 v12, v12, v13\n"
"v_exp_f32 v15, v15\n"
"v_mfma_f32_16x16x32_bf16 v[20:23], v[190:193], v[156:159], v[20:23]\n"
"v_cvt_pk_bf16_f32 v13, v14, v15\n"
"v_mfma_f32_16x16x32_bf16 v[20:23], v[192:195], v[24:27], v[20:23]\n"
"ds_write_b64 v6, v[12:13] offset:56328\n"
"ds_read_b128 v[12:15], v5 offset:26624\n"
"s_waitcnt lgkmcnt(4)\n"
"v_mfma_f32_16x16x32_bf16 v[140:143], v[0:3], v[238:241], v[140:143]\n"
"v_exp_f32 v208, v208\n"
"v_mfma_f32_16x16x32_bf16 v[144:147], v[16:19], v[238:241], v[144:147]\n"
"s_waitcnt lgkmcnt(1)\n"
"ds_read_b128 v[148:151], v6 offset:7168\n"
"v_exp_f32 v209, v209\n"
"s_barrier\n"
"v_exp_f32 v210, v210\n"
"v_mfma_f32_16x16x32_bf16 v[160:163], v[8:11], v[238:241], v[160:163]\n"
"ds_read_b128 v[238:241], v213 offset:5392\n"
"v_cvt_pk_bf16_f32 v208, v208, v209\n"
"v_exp_f32 v211, v211\n"
"v_mfma_f32_16x16x32_bf16 v[178:181], v[220:223], v[156:159], v[178:181]\n"
"v_cvt_pk_bf16_f32 v209, v210, v211\n"
"v_mfma_f32_16x16x32_bf16 v[178:181], v[222:225], v[24:27], v[178:181]\n"
"ds_write_b64 v6, v[208:209] offset:57344\n"
"s_nop 6\n"
"ds_read_b128 v[180:183], v4 offset:28672\n"
"s_waitcnt lgkmcnt(4)\n"
"v_mfma_f32_16x16x32_bf16 v[140:143], v[132:135], v[12:15], v[140:143]\n"
"v_exp_f32 v20, v20\n"
"v_mfma_f32_16x16x32_bf16 v[144:147], v[136:139], v[12:15], v[144:147]\n"
"s_waitcnt lgkmcnt(3)\n"
"ds_read_b128 v[208:211], v6 offset:8192\n"
"v_mfma_f32_16x16x32_bf16 v[160:163], v[148:151], v[12:15], v[160:163]\n"
"s_waitcnt lgkmcnt(3)\n"
"ds_read_b128 v[8:11], v213 offset:5408\n"
"v_exp_f32 v21, v21\n"
"v_mfma_f32_16x16x32_bf16 v[238:241], v[244:247], v[156:159], v[238:241]\n"
"v_cvt_pk_bf16_f32 v20, v20, v21\n"
"v_mov_b32 v21, 0\n"
"v_mfma_f32_16x16x32_bf16 v[238:241], v[246:249], v[24:27], v[238:241]\n"
"ds_write_b64 v6, v[20:21] offset:58368\n"
"ds_read_b128 v[12:15], v5 offset:28672\n"
"s_waitcnt lgkmcnt(4)\n"
"v_mfma_f32_16x16x32_bf16 v[140:143], v[100:103], v[180:183], v[140:143]\n"
"v_exp_f32 v178, v178\n"
"v_mfma_f32_16x16x32_bf16 v[144:147], v[104:107], v[180:183], v[144:147]\n"
"s_waitcnt lgkmcnt(3)\n"
"ds_read_b128 v[20:23], v6 offset:9216\n"
"v_mfma_f32_16x16x32_bf16 v[160:163], v[208:211], v[180:183], v[160:163]\n"
"s_waitcnt lgkmcnt(3)\n"
"ds_read_b128 v[208:211], v4 offset:18432\n"
"v_exp_f32 v179, v179\n"
"v_mfma_f32_16x16x32_bf16 v[8:11], v[250:253], v[156:159], v[8:11]\n"
"v_cvt_pk_bf16_f32 v178, v178, v179\n"
"v_mov_b32 v179, 0\n"
"v_mfma_f32_16x16x32_bf16 v[8:11], v[252:255], v[24:27], v[8:11]\n"
"ds_write_b64 v6, v[178:179] offset:58376\n"
"v_bfe_u32 v213, v6, 2, 6\n"
"s_waitcnt lgkmcnt(3)\n"
"v_mfma_f32_16x16x32_bf16 v[140:143], v[108:111], v[12:15], v[140:143]\n"
"s_add_u32 s71, s50, 4294964592\n"
"ds_read_b32 v213, v213 offset:1856\n"
"v_mfma_f32_16x16x32_bf16 v[144:147], v[112:115], v[12:15], v[144:147]\n"
"ds_read_b128 v[148:151], v5 offset:18432\n"
"v_exp_f32 v238, v238\n"
"v_exp_f32 v239, v239\n"
"v_exp_f32 v240, v240\n"
"s_waitcnt lgkmcnt(4)\n"
"v_mfma_f32_16x16x32_bf16 v[160:163], v[20:23], v[12:15], v[160:163]\n"
"s_waitcnt lgkmcnt(3)\n"
"v_exp_f32 v241, v241\n"
"v_cvt_pk_bf16_f32 v238, v238, v239\n"
"v_cvt_pk_bf16_f32 v239, v240, v241\n"
"v_mfma_f32_16x16x32_bf16 v[178:181], v[28:31], v[208:211], 0\n"
"ds_write_b64 v6, v[238:239] offset:57352\n"
"ds_read_b128 v[238:241], v4 offset:20480\n"
"v_mfma_f32_16x16x32_bf16 v[12:15], v[32:35], v[208:211], 0\n"
"s_waitcnt lgkmcnt(3)\n"
"v_add_u32_sdwa v212, v213, v7 dst_sel:DWORD dst_unused:UNUSED_PAD src0_sel:WORD_0 src1_sel:DWORD\n"
"s_nop 0\n"
"v_max_u32 v213, s71, v212\n"
"v_mfma_f32_16x16x32_bf16 v[20:23], v[36:39], v[208:211], 0\n"
"s_waitcnt lgkmcnt(2)\n"
"ds_read_b128 v[208:211], v213 offset:2720\n"
"v_mul_f32 v140, s60, v140\n"
"v_mfma_f32_16x16x32_bf16 v[178:181], v[40:43], v[148:151], v[178:181]\n"
"v_exp_f32 v8, v8\n"
"v_mfma_f32_16x16x32_bf16 v[12:15], v[44:47], v[148:151], v[12:15]\n"
"v_exp_f32 v9, v9\n"
"v_mfma_f32_16x16x32_bf16 v[20:23], v[48:51], v[148:151], v[20:23]\n"
"ds_read_b128 v[148:151], v5 offset:20480\n"
"v_cvt_pk_bf16_f32 v8, v8, v9\n"
"v_mov_b32 v9, 0\n"
"v_mul_f32 v141, s60, v141\n"
"ds_write_b64 v6, v[8:9] offset:33288\n"
"v_cvt_pk_bf16_f32 v140, v140, v141\n"
"s_waitcnt lgkmcnt(3)\n"
"v_mfma_f32_16x16x32_bf16 v[178:181], v[52:55], v[238:241], v[178:181]\n"
"v_mul_f32 v142, s60, v142\n"
"v_mul_f32 v143, s60, v143\n"
"v_mfma_f32_16x16x32_bf16 v[12:15], v[56:59], v[238:241], v[12:15]\n"
"v_cvt_pk_bf16_f32 v141, v142, v143\n"
"v_mul_f32 v144, s60, v144\n"
"v_mul_f32 v145, s60, v145\n"
"v_mul_f32 v146, s60, v146\n"
"s_waitcnt lgkmcnt(0)\n"
"s_barrier\n"
"v_mul_f32 v147, s60, v147\n"
"v_cvt_pk_bf16_f32 v142, v144, v145\n"
"v_cvt_pk_bf16_f32 v143, v146, v147\n"
"v_mul_f32 v160, s60, v160\n"
"v_mul_f32 v161, s60, v161\n"
"v_mul_f32 v162, s60, v162\n"
"v_mfma_f32_16x16x32_bf16 v[208:211], v[190:193], v[140:143], v[208:211]\n"
"s_add_u32 s72, s50, 4294965632\n"
"ds_read_b128 v[8:11], v213 offset:2736\n"
"v_mul_f32 v163, s60, v163\n"
"v_cvt_pk_bf16_f32 v26, v160, v161\n"
"v_cvt_pk_bf16_f32 v27, v162, v163\n"
"s_nop 1\n"
"v_mfma_f32_16x16x32_bf16 v[208:211], v[192:195], v[24:27], v[208:211]\n"
"v_mfma_f32_16x16x32_bf16 v[20:23], v[60:63], v[238:241], v[20:23]\n"
"ds_read_b128 v[238:241], v4 offset:22528\n"
"v_mfma_f32_16x16x32_bf16 v[178:181], v[64:67], v[148:151], v[178:181]\n"
"v_min_u32 v183, s72, v212\n"
"v_mfma_f32_16x16x32_bf16 v[12:15], v[68:71], v[148:151], v[12:15]\n"
"v_mfma_f32_16x16x32_bf16 v[20:23], v[72:75], v[148:151], v[20:23]\n"
"s_waitcnt lgkmcnt(1)\n"
"ds_read_b128 v[144:147], v183 offset:4048\n"
"v_mfma_f32_16x16x32_bf16 v[8:11], v[196:199], v[140:143], v[8:11]\n"
"v_exp_f32 v208, v208\n"
"v_mfma_f32_16x16x32_bf16 v[8:11], v[198:201], v[24:27], v[8:11]\n"
"s_waitcnt lgkmcnt(1)\n"
"ds_read_b128 v[148:151], v5 offset:22528\n"
"v_exp_f32 v209, v209\n"
"v_exp_f32 v210, v210\n"
"v_mfma_f32_16x16x32_bf16 v[178:181], v[76:79], v[238:241], v[178:181]\n"
"v_exp_f32 v211, v211\n"
"v_mfma_f32_16x16x32_bf16 v[12:15], v[80:83], v[238:241], v[12:15]\n"
"v_cvt_pk_bf16_f32 v208, v208, v209\n"
"v_cvt_pk_bf16_f32 v209, v210, v211\n"
"v_mfma_f32_16x16x32_bf16 v[20:23], v[84:87], v[238:241], v[20:23]\n"
"ds_write_b64 v6, v[208:209] offset:53248\n"
"ds_read_b128 v[208:211], v183 offset:4064\n"
"s_waitcnt lgkmcnt(3)\n"
"v_mfma_f32_16x16x32_bf16 v[144:147], v[214:217], v[140:143], v[144:147]\n"
"v_exp_f32 v8, v8\n"
"v_mfma_f32_16x16x32_bf16 v[144:147], v[216:219], v[24:27], v[144:147]\n"
"s_waitcnt lgkmcnt(2)\n"
"ds_read_b128 v[238:241], v4 offset:36864\n"
"v_mfma_f32_16x16x32_bf16 v[178:181], v[88:91], v[148:151], v[178:181]\n"
"v_exp_f32 v9, v9\n"
"v_mfma_f32_16x16x32_bf16 v[12:15], v[92:95], v[148:151], v[12:15]\n"
"ds_read_b128 v[152:155], v6 offset:4096\n"
"v_cvt_pk_bf16_f32 v8, v8, v9\n"
"v_exp_f32 v10, v10\n"
"v_mfma_f32_16x16x32_bf16 v[20:23], v[96:99], v[148:151], v[20:23]\n"
"ds_read_b128 v[148:151], v183 offset:4080\n"
"v_exp_f32 v11, v11\n"
"v_cvt_pk_bf16_f32 v178, v178, v179\n"
"v_cvt_pk_bf16_f32 v9, v10, v11\n"
"ds_write_b64 v6, v[8:9] offset:53256\n"
"v_cvt_pk_bf16_f32 v179, v180, v181\n"
"s_waitcnt lgkmcnt(4)\n"
"v_mfma_f32_16x16x32_bf16 v[208:211], v[220:223], v[140:143], v[208:211]\n"
"v_cvt_pk_bf16_f32 v180, v12, v13\n"
"v_cvt_pk_bf16_f32 v181, v14, v15\n"
"v_mfma_f32_16x16x32_bf16 v[208:211], v[222:225], v[24:27], v[208:211]\n"
"s_waitcnt lgkmcnt(3)\n"
"ds_read_b128 v[8:11], v5 offset:36864\n"
"v_cvt_pk_bf16_f32 v182, v20, v21\n"
"v_cvt_pk_bf16_f32 v183, v22, v23\n"
"v_mfma_f32_16x16x32_bf16 v[12:15], v[116:119], v[238:241], 0\n"
"v_exp_f32 v144, v144\n"
"v_mfma_f32_16x16x32_bf16 v[20:23], v[120:123], v[238:241], 0\n"
"s_waitcnt lgkmcnt(3)\n"
"ds_read_b128 v[156:159], v6 offset:5120\n"
"v_exp_f32 v145, v145\n"
"v_exp_f32 v146, v146\n"
"v_mfma_f32_16x16x32_bf16 v[160:163], v[152:155], v[238:241], 0\n"
"s_add_u32 s73, s50, 4294964288\n"
"ds_read_b128 v[238:241], v213 offset:2704\n"
"v_cvt_pk_bf16_f32 v144, v144, v145\n"
"v_exp_f32 v147, v147\n"
"s_waitcnt lgkmcnt(4)\n"
"v_mfma_f32_16x16x32_bf16 v[148:151], v[226:229], v[140:143], v[148:151]\n"
"v_cvt_pk_bf16_f32 v145, v146, v147\n"
"v_mfma_f32_16x16x32_bf16 v[148:151], v[228:231], v[24:27], v[148:151]\n"
"ds_write_b64 v6, v[144:145] offset:54272\n"
"ds_read_b128 v[144:147], v4 offset:38912\n"
"s_waitcnt lgkmcnt(4)\n"
"v_mfma_f32_16x16x32_bf16 v[12:15], v[124:127], v[8:11], v[12:15]\n"
"v_exp_f32 v208, v208\n"
"v_mfma_f32_16x16x32_bf16 v[20:23], v[128:131], v[8:11], v[20:23]\n"
"ds_read_b128 v[152:155], v6 offset:6144\n"
"v_min_u32 v213, s73, v212\n"
"s_mov_b64 exec, s[58:59]\n"
"v_add_u32 v213, 1040, v213\n"
"s_mov_b64 exec, -1\n"
"v_exp_f32 v209, v209\n"
"v_exp_f32 v210, v210\n"
"s_waitcnt lgkmcnt(4)\n"
"v_mfma_f32_16x16x32_bf16 v[160:163], v[156:159], v[8:11], v[160:163]\n"
"s_waitcnt lgkmcnt(3)\n"
"ds_read_b128 v[8:11], v213 offset:5392\n"
"v_cvt_pk_bf16_f32 v208, v208, v209\n"
"v_exp_f32 v211, v211\n"
"v_mfma_f32_16x16x32_bf16 v[238:241], v[184:187], v[140:143], v[238:241]\n"
"v_cvt_pk_bf16_f32 v209, v210, v211\n"
"v_mfma_f32_16x16x32_bf16 v[238:241], v[186:189], v[24:27], v[238:241]\n"
"ds_write_b64 v6, v[208:209] offset:54280\n"
"ds_read_b128 v[184:187], v5 offset:38912\n"
"s_waitcnt lgkmcnt(4)\n"
"v_mfma_f32_16x16x32_bf16 v[12:15], v[0:3], v[144:147], v[12:15]\n"
"v_exp_f32 v148, v148\n"
"v_mfma_f32_16x16x32_bf16 v[20:23], v[16:19], v[144:147], v[20:23]\n"
"s_waitcnt lgkmcnt(3)\n"
"ds_read_b128 v[208:211], v6 offset:7168\n"
"v_exp_f32 v149, v149\n"
"v_exp_f32 v150, v150\n"
"v_mfma_f32_16x16x32_bf16 v[160:163], v[152:155], v[144:147], v[160:163]\n"
"s_waitcnt lgkmcnt(3)\n"
"ds_read_b128 v[144:147], v213 offset:5408\n"
"v_cvt_pk_bf16_f32 v148, v148, v149\n"
"v_exp_f32 v151, v151\n"
"v_mfma_f32_16x16x32_bf16 v[8:11], v[244:247], v[140:143], v[8:11]\n"
"v_cvt_pk_bf16_f32 v149, v150, v151\n"
"v_mfma_f32_16x16x32_bf16 v[8:11], v[246:249], v[24:27], v[8:11]\n"
"ds_write_b64 v6, v[148:149] offset:33280\n"
"ds_read_b128 v[148:151], v4 offset:40960\n"
"s_waitcnt lgkmcnt(4)\n"
"v_mfma_f32_16x16x32_bf16 v[12:15], v[132:135], v[184:187], v[12:15]\n"
"v_exp_f32 v238, v238\n"
"v_mfma_f32_16x16x32_bf16 v[20:23], v[136:139], v[184:187], v[20:23]\n"
"s_waitcnt lgkmcnt(1)\n"
"ds_read_b128 v[152:155], v6 offset:8192\n"
"v_exp_f32 v239, v239\n"
"s_barrier\n"
"v_exp_f32 v240, v240\n"
"v_mfma_f32_16x16x32_bf16 v[160:163], v[208:211], v[184:187], v[160:163]\n"
"ds_read_b128 v[208:211], v213 offset:5424\n"
"v_cvt_pk_bf16_f32 v238, v238, v239\n"
"v_exp_f32 v241, v241\n"
"v_mfma_f32_16x16x32_bf16 v[144:147], v[250:253], v[140:143], v[144:147]\n"
"v_cvt_pk_bf16_f32 v239, v240, v241\n"
"v_mfma_f32_16x16x32_bf16 v[144:147], v[252:255], v[24:27], v[144:147]\n"
"ds_write_b64 v6, v[238:239] offset:57352\n"
"ds_read_b128 v[184:187], v5 offset:40960\n"
"s_waitcnt lgkmcnt(4)\n"
"v_mfma_f32_16x16x32_bf16 v[12:15], v[100:103], v[148:151], v[12:15]\n"
"v_exp_f32 v8, v8\n"
"v_mfma_f32_16x16x32_bf16 v[20:23], v[104:107], v[148:151], v[20:23]\n"
"s_waitcnt lgkmcnt(3)\n"
"ds_read_b128 v[238:241], v6 offset:9216\n"
"v_exp_f32 v9, v9\n"
"v_exp_f32 v10, v10\n"
"v_mfma_f32_16x16x32_bf16 v[160:163], v[152:155], v[148:151], v[160:163]\n"
"s_waitcnt lgkmcnt(3)\n"
"ds_read_b128 v[148:151], v4 offset:30720\n"
"v_cvt_pk_bf16_f32 v8, v8, v9\n"
"v_exp_f32 v11, v11\n"
"v_mfma_f32_16x16x32_bf16 v[208:211], v[178:181], v[140:143], v[208:211]\n"
"v_cvt_pk_bf16_f32 v9, v10, v11\n"
"v_bfe_u32 v213, v6, 2, 6\n"
"v_mfma_f32_16x16x32_bf16 v[208:211], v[180:183], v[24:27], v[208:211]\n"
"ds_write_b64 v6, v[8:9] offset:58368\n"
"ds_read_b32 v213, v213 offset:1920\n"
"s_waitcnt lgkmcnt(4)\n"
"v_mfma_f32_16x16x32_bf16 v[12:15], v[108:111], v[184:187], v[12:15]\n"
"s_add_u32 s68, s50, 4294964576\n"
"ds_read_b128 v[8:11], v5 offset:30720\n"
"v_mfma_f32_16x16x32_bf16 v[20:23], v[112:115], v[184:187], v[20:23]\n"
"s_waitcnt lgkmcnt(4)\n"
"v_exp_f32 v144, v144\n"
"v_exp_f32 v145, v145\n"
"v_exp_f32 v146, v146\n"
"v_mfma_f32_16x16x32_bf16 v[160:163], v[238:241], v[184:187], v[160:163]\n"
"s_waitcnt lgkmcnt(3)\n"
"v_exp_f32 v147, v147\n"
"v_cvt_pk_bf16_f32 v144, v144, v145\n"
"v_cvt_pk_bf16_f32 v145, v146, v147\n"
"v_mfma_f32_16x16x32_bf16 v[184:187], v[28:31], v[148:151], 0\n"
"ds_write_b64 v6, v[144:145] offset:58376\n"
"ds_read_b128 v[238:241], v4 offset:32768\n"
"v_mfma_f32_16x16x32_bf16 v[28:31], v[32:35], v[148:151], 0\n"
"s_waitcnt lgkmcnt(3)\n"
"v_add_u32_sdwa v212, v213, v7 dst_sel:DWORD dst_unused:UNUSED_PAD src0_sel:WORD_0 src1_sel:DWORD\n"
"s_nop 0\n"
"v_max_u32 v243, s68, v212\n"
"v_mfma_f32_16x16x32_bf16 v[32:35], v[36:39], v[148:151], 0\n"
"s_waitcnt lgkmcnt(2)\n"
"ds_read_b128 v[36:39], v243 offset:2720\n"
"v_mul_f32 v12, s60, v12\n"
"v_mfma_f32_16x16x32_bf16 v[184:187], v[40:43], v[8:11], v[184:187]\n"
"v_exp_f32 v208, v208\n"
"v_mfma_f32_16x16x32_bf16 v[28:31], v[44:47], v[8:11], v[28:31]\n"
"v_exp_f32 v209, v209\n"
"v_exp_f32 v210, v210\n"
"v_mfma_f32_16x16x32_bf16 v[32:35], v[48:51], v[8:11], v[32:35]\n"
"s_waitcnt lgkmcnt(1)\n"
"ds_read_b128 v[8:11], v5 offset:32768\n"
"v_cvt_pk_bf16_f32 v208, v208, v209\n"
"v_exp_f32 v211, v211\n"
"v_mfma_f32_16x16x32_bf16 v[184:187], v[52:55], v[238:241], v[184:187]\n"
"v_cvt_pk_bf16_f32 v209, v210, v211\n"
"v_mul_f32 v13, s60, v13\n"
"v_mfma_f32_16x16x32_bf16 v[28:31], v[56:59], v[238:241], v[28:31]\n"
"ds_write_b64 v6, v[208:209] offset:57344\n"
"v_cvt_pk_bf16_f32 v12, v12, v13\n"
"v_mul_f32 v14, s60, v14\n"
"v_mul_f32 v15, s60, v15\n"
"v_cvt_pk_bf16_f32 v13, v14, v15\n"
"v_mul_f32 v20, s60, v20\n"
"v_mul_f32 v21, s60, v21\n"
"v_mul_f32 v22, s60, v22\n"
"s_waitcnt lgkmcnt(0)\n"
"s_barrier\n"
"v_mul_f32 v23, s60, v23\n"
"v_cvt_pk_bf16_f32 v14, v20, v21\n"
"v_cvt_pk_bf16_f32 v15, v22, v23\n"
"v_mul_f32 v160, s60, v160\n"
"v_mul_f32 v161, s60, v161\n"
"v_mul_f32 v162, s60, v162\n"
"v_mfma_f32_16x16x32_bf16 v[36:39], v[190:193], v[12:15], v[36:39]\n"
"ds_read_b128 v[188:191], v243 offset:2736\n"
"v_mul_f32 v163, s60, v163\n"
"v_cvt_pk_bf16_f32 v26, v160, v161\n"
"v_cvt_pk_bf16_f32 v27, v162, v163\n"
"v_add_u32_sdwa v213, sext(v213), v212 dst_sel:DWORD dst_unused:UNUSED_PAD src0_sel:WORD_1 src1_sel:DWORD\n"
"s_nop 0\n"
"v_mfma_f32_16x16x32_bf16 v[36:39], v[192:195], v[24:27], v[36:39]\n"
"v_mfma_f32_16x16x32_bf16 v[32:35], v[60:63], v[238:241], v[32:35]\n"
"ds_read_b128 v[192:195], v4 offset:34816\n"
"v_mfma_f32_16x16x32_bf16 v[184:187], v[64:67], v[8:11], v[184:187]\n"
"v_max_u32 v239, s68, v213\n"
"v_mfma_f32_16x16x32_bf16 v[28:31], v[68:71], v[8:11], v[28:31]\n"
"v_mfma_f32_16x16x32_bf16 v[32:35], v[72:75], v[8:11], v[32:35]\n"
"s_add_u32 s69, s50, 4294963232\n"
"ds_read_b128 v[208:211], v239 offset:2752\n"
"v_mov_b32 v36, 0\n"
"s_waitcnt lgkmcnt(2)\n"
"v_mfma_f32_16x16x32_bf16 v[188:191], v[196:199], v[12:15], v[188:191]\n"
"v_exp_f32 v38, v38\n"
"v_mfma_f32_16x16x32_bf16 v[188:191], v[198:201], v[24:27], v[188:191]\n"
"s_waitcnt lgkmcnt(1)\n"
"ds_read_b128 v[196:199], v5 offset:34816\n"
"v_exp_f32 v39, v39\n"
"v_mfma_f32_16x16x32_bf16 v[184:187], v[76:79], v[192:195], v[184:187]\n"
"v_cvt_pk_bf16_f32 v37, v38, v39\n"
"v_max_u32 v239, s69, v213\n"
"v_mfma_f32_16x16x32_bf16 v[28:31], v[80:83], v[192:195], v[28:31]\n"
"ds_write_b64 v6, v[36:37] offset:55296\n"
"v_mfma_f32_16x16x32_bf16 v[32:35], v[84:87], v[192:195], v[32:35]\n"
"s_add_u32 s70, s50, 4294964272\n"
"ds_read_b128 v[192:195], v239 offset:4096\n"
"s_waitcnt lgkmcnt(3)\n"
"v_mfma_f32_16x16x32_bf16 v[208:211], v[202:205], v[12:15], v[208:211]\n"
"v_exp_f32 v188, v188\n"
"v_mfma_f32_16x16x32_bf16 v[208:211], v[204:207], v[24:27], v[208:211]\n"
"s_waitcnt lgkmcnt(2)\n"
"ds_read_b128 v[200:203], v4 offset:43008\n"
"v_min_u32 v177, s70, v212\n"
"v_mfma_f32_16x16x32_bf16 v[184:187], v[88:91], v[196:199], v[184:187]\n"
"v_exp_f32 v189, v189\n"
"v_mfma_f32_16x16x32_bf16 v[28:31], v[92:95], v[196:199], v[28:31]\n"
"ds_read_b128 v[204:207], v6 offset:4096\n"
"v_min_u32 v176, s70, v213\n"
"s_mov_b64 exec, s[58:59]\n"
"v_add_u32 v177, 1040, v177\n"
"v_add_u32 v176, 1040, v176\n"
"s_mov_b64 exec, -1\n"
"v_exp_f32 v190, v190\n"
"v_mfma_f32_16x16x32_bf16 v[32:35], v[96:99], v[196:199], v[32:35]\n"
"ds_read_b128 v[196:199], v177 offset:5408\n"
"v_cvt_pk_bf16_f32 v188, v188, v189\n"
"v_exp_f32 v191, v191\n"
"v_cvt_pk_bf16_f32 v184, v184, v185\n"
"v_cvt_pk_bf16_f32 v189, v190, v191\n"
"ds_write_b64 v6, v[188:189] offset:55304\n"
"v_cvt_pk_bf16_f32 v185, v186, v187\n"
"s_waitcnt lgkmcnt(4)\n"
"v_mfma_f32_16x16x32_bf16 v[192:195], v[232:235], v[12:15], v[192:195]\n"
"v_cvt_pk_bf16_f32 v186, v28, v29\n"
"v_cvt_pk_bf16_f32 v187, v30, v31\n"
"v_mfma_f32_16x16x32_bf16 v[192:195], v[234:237], v[24:27], v[192:195]\n"
"s_waitcnt lgkmcnt(3)\n"
"ds_read_b128 v[232:235], v5 offset:43008\n"
"v_cvt_pk_bf16_f32 v188, v32, v33\n"
"v_cvt_pk_bf16_f32 v189, v34, v35\n"
"v_mfma_f32_16x16x32_bf16 v[236:239], v[116:119], v[200:203], 0\n"
"v_exp_f32 v208, v208\n"
"v_mfma_f32_16x16x32_bf16 v[240:243], v[120:123], v[200:203], 0\n"
"ds_read_b128 v[8:11], v6 offset:5120\n"
"v_max_u32 v213, s69, v212\n"
"v_exp_f32 v209, v209\n"
"v_exp_f32 v210, v210\n"
"s_waitcnt lgkmcnt(4)\n"
"v_mfma_f32_16x16x32_bf16 v[20:23], v[204:207], v[200:203], 0\n"
"s_waitcnt lgkmcnt(3)\n"
"ds_read_b128 v[200:203], v213 offset:4064\n"
"v_cvt_pk_bf16_f32 v208, v208, v209\n"
"v_exp_f32 v211, v211\n"
"v_mfma_f32_16x16x32_bf16 v[196:199], v[250:253], v[12:15], v[196:199]\n"
"v_cvt_pk_bf16_f32 v209, v210, v211\n"
"v_mfma_f32_16x16x32_bf16 v[196:199], v[252:255], v[24:27], v[196:199]\n"
"ds_write_b64 v6, v[208:209] offset:56320\n"
"ds_read_b128 v[204:207], v4 offset:45056\n"
"s_waitcnt lgkmcnt(4)\n"
"v_mfma_f32_16x16x32_bf16 v[236:239], v[124:127], v[232:235], v[236:239]\n"
"v_exp_f32 v192, v192\n"
"v_mfma_f32_16x16x32_bf16 v[240:243], v[128:131], v[232:235], v[240:243]\n"
"s_waitcnt lgkmcnt(3)\n"
"ds_read_b128 v[208:211], v6 offset:6144\n"
"v_exp_f32 v193, v193\n"
"v_exp_f32 v194, v194\n"
"v_mfma_f32_16x16x32_bf16 v[20:23], v[8:11], v[232:235], v[20:23]\n"
"s_waitcnt lgkmcnt(3)\n"
"ds_read_b128 v[232:235], v213 offset:4080\n"
"v_cvt_pk_bf16_f32 v192, v192, v193\n"
"v_exp_f32 v195, v195\n"
"v_mfma_f32_16x16x32_bf16 v[200:203], v[220:223], v[12:15], v[200:203]\n"
"v_cvt_pk_bf16_f32 v193, v194, v195\n"
"v_mov_b32 v196, 0\n"
"v_mfma_f32_16x16x32_bf16 v[200:203], v[222:225], v[24:27], v[200:203]\n"
"ds_write_b64 v6, v[192:193] offset:56328\n"
"ds_read_b128 v[190:193], v5 offset:45056\n"
"s_waitcnt lgkmcnt(4)\n"
"v_mfma_f32_16x16x32_bf16 v[236:239], v[0:3], v[204:207], v[236:239]\n"
"v_exp_f32 v198, v198\n"
"v_mfma_f32_16x16x32_bf16 v[240:243], v[16:19], v[204:207], v[240:243]\n"
"s_waitcnt lgkmcnt(3)\n"
"ds_read_b128 v[0:3], v6 offset:7168\n"
"v_mfma_f32_16x16x32_bf16 v[20:23], v[208:211], v[204:207], v[20:23]\n"
"s_waitcnt lgkmcnt(3)\n"
"ds_read_b128 v[204:207], v177 offset:5424\n"
"v_mov_b32 v200, 0\n"
"v_exp_f32 v199, v199\n"
"v_mfma_f32_16x16x32_bf16 v[232:235], v[226:229], v[12:15], v[232:235]\n"
"v_cvt_pk_bf16_f32 v197, v198, v199\n"
"v_mfma_f32_16x16x32_bf16 v[232:235], v[228:231], v[24:27], v[232:235]\n"
"ds_write_b64 v6, v[196:197] offset:33288\n"
"ds_read_b128 v[194:197], v4 offset:47104\n"
"s_waitcnt lgkmcnt(4)\n"
"v_mfma_f32_16x16x32_bf16 v[236:239], v[132:135], v[190:193], v[236:239]\n"
"v_exp_f32 v202, v202\n"
"v_mfma_f32_16x16x32_bf16 v[240:243], v[136:139], v[190:193], v[240:243]\n"
"s_waitcnt lgkmcnt(3)\n"
"ds_read_b128 v[208:211], v6 offset:8192\n"
"v_bfe_u32 v4, v6, 2, 6\n"
"v_mfma_f32_16x16x32_bf16 v[20:23], v[0:3], v[190:193], v[20:23]\n"
"s_add_u32 s71, s50, 4294963248\n"
"ds_read_b32 v4, v4 offset:1984\n"
"s_waitcnt lgkmcnt(3)\n"
"s_barrier\n"
"ds_read_b128 v[190:193], v176 offset:5440\n"
"v_exp_f32 v203, v203\n"
"v_mfma_f32_16x16x32_bf16 v[204:207], v[178:181], v[12:15], v[204:207]\n"
"v_cvt_pk_bf16_f32 v201, v202, v203\n"
"v_mfma_f32_16x16x32_bf16 v[204:207], v[180:183], v[24:27], v[204:207]\n"
"ds_write_b64 v6, v[200:201] offset:58368\n"
"ds_read_b128 v[198:201], v5 offset:47104\n"
"s_waitcnt lgkmcnt(5)\n"
"v_mfma_f32_16x16x32_bf16 v[236:239], v[100:103], v[194:197], v[236:239]\n"
"v_exp_f32 v232, v232\n"
"v_mfma_f32_16x16x32_bf16 v[240:243], v[104:107], v[194:197], v[240:243]\n"
"s_add_u32 s72, s50, 4294964288\n"
"ds_read_b128 v[0:3], v6 offset:9216\n"
"s_waitcnt lgkmcnt(4)\n"
"v_add_u32_sdwa v5, v4, v7 dst_sel:DWORD dst_unused:UNUSED_PAD src0_sel:WORD_0 src1_sel:DWORD\n"
"v_exp_f32 v233, v233\n"
"v_exp_f32 v234, v234\n"
"v_mfma_f32_16x16x32_bf16 v[20:23], v[208:211], v[194:197], v[20:23]\n"
"s_waitcnt lgkmcnt(3)\n"
"v_exp_f32 v235, v235\n"
"v_cvt_pk_bf16_f32 v232, v232, v233\n"
"v_cvt_pk_bf16_f32 v233, v234, v235\n"
"v_mfma_f32_16x16x32_bf16 v[190:193], v[184:187], v[12:15], v[190:193]\n"
"ds_write_b64 v6, v[232:233] offset:58376\n"
"v_max_u32 v7, s71, v5\n"
"v_mfma_f32_16x16x32_bf16 v[190:193], v[186:189], v[24:27], v[190:193]\n"
"s_waitcnt lgkmcnt(2)\n"
"ds_read_b128 v[184:187], v7 offset:4048\n"
"v_min_u32 v4, s72, v5\n"
"v_mfma_f32_16x16x32_bf16 v[236:239], v[108:111], v[198:201], v[236:239]\n"
"s_mov_b64 exec, s[58:59]\n"
"v_add_u32 v4, 1040, v4\n"
"s_mov_b64 exec, -1\n"
"v_mfma_f32_16x16x32_bf16 v[240:243], v[112:115], v[198:201], v[240:243]\n"
"ds_read_b128 v[194:197], v7 offset:4064\n"
"ds_read_b128 v[208:211], v4 offset:5392\n"
"v_exp_f32 v204, v204\n"
"v_exp_f32 v205, v205\n"
"v_mul_f32 v236, s60, v236\n"
"v_mul_f32 v237, s60, v237\n"
"v_exp_f32 v206, v206\n"
"s_waitcnt lgkmcnt(4)\n"
"v_mfma_f32_16x16x32_bf16 v[20:23], v[0:3], v[198:201], v[20:23]\n"
"v_cvt_pk_bf16_f32 v236, v236, v237\n"
"v_mul_f32 v238, s60, v238\n"
"v_mul_f32 v239, s60, v239\n"
"v_cvt_pk_bf16_f32 v237, v238, v239\n"
"v_exp_f32 v207, v207\n"
"v_mul_f32 v240, s60, v240\n"
"v_mul_f32 v241, s60, v241\n"
"v_mul_f32 v242, s60, v242\n"
"v_mul_f32 v243, s60, v243\n"
"v_cvt_pk_bf16_f32 v238, v240, v241\n"
"v_cvt_pk_bf16_f32 v239, v242, v243\n"
"v_cvt_pk_bf16_f32 v204, v204, v205\n"
"v_cvt_pk_bf16_f32 v205, v206, v207\n"
"s_waitcnt lgkmcnt(2)\n"
"v_mfma_f32_16x16x32_bf16 v[184:187], v[214:217], v[236:239], v[184:187]\n"
"ds_write_b64 v6, v[204:205] offset:57344\n"
"v_mul_f32 v20, s60, v20\n"
"ds_read_b128 v[198:201], v7 offset:4080\n"
"v_mul_f32 v21, s60, v21\n"
"v_mul_f32 v22, s60, v22\n"
"v_mul_f32 v23, s60, v23\n"
"v_cvt_pk_bf16_f32 v26, v20, v21\n"
"v_cvt_pk_bf16_f32 v27, v22, v23\n"
"v_exp_f32 v190, v190\n"
"v_exp_f32 v191, v191\n"
"v_mfma_f32_16x16x32_bf16 v[184:187], v[216:219], v[24:27], v[184:187]\n"
"s_waitcnt lgkmcnt(3)\n"
"v_exp_f32 v192, v192\n"
"v_mfma_f32_16x16x32_bf16 v[194:197], v[220:223], v[236:239], v[194:197]\n"
"ds_read_b128 v[202:205], v4 offset:5408\n"
"v_cvt_pk_bf16_f32 v190, v190, v191\n"
"v_mfma_f32_16x16x32_bf16 v[194:197], v[222:225], v[24:27], v[194:197]\n"
"s_waitcnt lgkmcnt(3)\n"
"v_exp_f32 v193, v193\n"
"v_mfma_f32_16x16x32_bf16 v[208:211], v[244:247], v[236:239], v[208:211]\n"
"ds_read_b128 v[212:215], v4 offset:5424\n"
"v_cvt_pk_bf16_f32 v191, v192, v193\n"
"v_mfma_f32_16x16x32_bf16 v[208:211], v[246:249], v[24:27], v[208:211]\n"
"ds_write_b64 v6, v[190:191] offset:57352\n"
"v_exp_f32 v184, v184\n"
"v_exp_f32 v185, v185\n"
"v_exp_f32 v186, v186\n"
"s_waitcnt lgkmcnt(3)\n"
"v_mfma_f32_16x16x32_bf16 v[198:201], v[226:229], v[236:239], v[198:201]\n"
"s_waitcnt lgkmcnt(0)\n"
"s_barrier\n"
"v_exp_f32 v187, v187\n"
"v_cvt_pk_bf16_f32 v184, v184, v185\n"
"v_cvt_pk_bf16_f32 v185, v186, v187\n"
"ds_write_b64 v6, v[184:185] offset:53248\n"
"v_exp_f32 v194, v194\n"
"v_mfma_f32_16x16x32_bf16 v[198:201], v[228:231], v[24:27], v[198:201]\n"
"v_exp_f32 v195, v195\n"
"v_exp_f32 v196, v196\n"
"v_exp_f32 v197, v197\n"
"v_mfma_f32_16x16x32_bf16 v[202:205], v[250:253], v[236:239], v[202:205]\n"
"v_exp_f32 v208, v208\n"
"v_cvt_pk_bf16_f32 v194, v194, v195\n"
"v_cvt_pk_bf16_f32 v195, v196, v197\n"
"ds_write_b64 v6, v[194:195] offset:53256\n"
"v_exp_f32 v209, v209\n"
"v_mfma_f32_16x16x32_bf16 v[202:205], v[252:255], v[24:27], v[202:205]\n"
"v_exp_f32 v210, v210\n"
"v_exp_f32 v211, v211\n"
"v_cvt_pk_bf16_f32 v208, v208, v209\n"
"v_cvt_pk_bf16_f32 v209, v210, v211\n"
"v_mfma_f32_16x16x32_bf16 v[212:215], v[178:181], v[236:239], v[212:215]\n"
"ds_write_b64 v6, v[208:209] offset:54272\n"
"v_mfma_f32_16x16x32_bf16 v[212:215], v[180:183], v[24:27], v[212:215]\n"
"s_waitcnt lgkmcnt(0)\n"
"v_exp_f32 v198, v198\n"
"v_exp_f32 v199, v199\n"
"v_exp_f32 v200, v200\n"
"s_barrier\n"
"v_exp_f32 v201, v201\n"
"v_cvt_pk_bf16_f32 v198, v198, v199\n"
"v_cvt_pk_bf16_f32 v199, v200, v201\n"
"ds_write_b64 v6, v[198:199] offset:57344\n"
"v_exp_f32 v202, v202\n"
"v_exp_f32 v203, v203\n"
"v_exp_f32 v204, v204\n"
"v_exp_f32 v205, v205\n"
"v_cvt_pk_bf16_f32 v202, v202, v203\n"
"v_cvt_pk_bf16_f32 v203, v204, v205\n"
"ds_write_b64 v6, v[202:203] offset:54280\n"
"v_exp_f32 v212, v212\n"
"v_exp_f32 v213, v213\n"
"v_exp_f32 v214, v214\n"
"v_exp_f32 v215, v215\n"
"v_cvt_pk_bf16_f32 v212, v212, v213\n"
"v_cvt_pk_bf16_f32 v213, v214, v215\n"
"ds_write_b64 v6, v[212:213] offset:57352\n"
"s_waitcnt lgkmcnt(0)\n"
"s_barrier\n"
"s_barrier\n"
"s_barrier\n"
"s_barrier\n"
  );
}
DI void waveB_fast(const uint16_t* x, const uint16_t* wqkv, const uint16_t* wo, uint16_t* out, const Geo& G, int h SPARM) {
  B_HEAD;
  XDIR(0, 0, 0, 1, 102400); SB;
  XDIR(1, 0, 1, 1, 114688); SB;
  XDIR(0, 2, 2, 0, 126976); SB;
  RAWB(); B_SETUP(); SB;
  ASM_B(
"s_setprio 0\n"
"s_movk_i32 s40, 768\n"
"s_mov_b64 s[62:63], 0\n"
"s_movk_i32 s64, 0x207\n"
"s_add_u32 s58, s56, 128\n"
"s_addc_u32 s59, s57, 0\n"
"s_add_u32 s60, s56, 256\n"
"s_addc_u32 s61, s57, 0\n"
"s_waitcnt vmcnt(0)\n"
"s_barrier\n"
"s_add_u32 s42, s46, 768\n"
"s_addc_u32 s43, s47, 0\n"
"s_add_u32 m0, s50, 0\n"
"s_nop 0\n"
"global_load_lds_dwordx4 v0, s[42:43]\n"
"s_add_u32 m0, s50, 1024\n"
"s_nop 0\n"
"global_load_lds_dwordx4 v1, s[42:43]\n"
"s_add_u32 m0, s50, 2048\n"
"s_nop 0\n"
"global_load_lds_dwordx4 v2, s[42:43]\n"
"s_add_u32 s42, s46, 37632\n"
"s_addc_u32 s43, s47, 0\n"
"s_add_u32 m0, s50, 3072\n"
"s_nop 0\n"
"global_load_lds_dwordx4 v0, s[42:43]\n"
"s_add_u32 m0, s50, 4096\n"
"s_nop 0\n"
"global_load_lds_dwordx4 v1, s[42:43]\n"
"s_add_u32 m0, s50, 5120\n"
"s_nop 0\n"
"global_load_lds_dwordx4 v2, s[42:43]\n"
"s_add_u32 s42, s46, 74496\n"
"s_addc_u32 s43, s47, 0\n"
"s_add_u32 m0, s50, 24576\n"
"s_nop 0\n"
"global_load_lds_dwordx4 v0, s[42:43]\n"
"s_add_u32 m0, s50, 25600\n"
"s_nop 0\n"
"global_load_lds_dwordx4 v1, s[42:43]\n"
"s_add_u32 m0, s50, 26624\n"
"s_nop 0\n"
"global_load_lds_dwordx4 v2, s[42:43]\n"
"s_add_u32 s42, s46, 111360\n"
"s_addc_u32 s43, s47, 0\n"
"s_add_u32 m0, s50, 27648\n"
"s_nop 0\n"
"global_load_lds_dwordx4 v0, s[42:43]\n"
"s_add_u32 m0, s50, 28672\n"
"s_nop 0\n"
"global_load_lds_dwordx4 v1, s[42:43]\n"
"s_add_u32 m0, s50, 29696\n"
"s_nop 0\n"
"global_load_lds_dwordx4 v2, s[42:43]\n"
"s_barrier\n"
"s_add_u32 s42, s46, 148224\n"
"s_addc_u32 s43, s47, 0\n"
"s_add_u32 m0, s50, 4294942720\n"
"s_nop 0\n"
"global_load_lds_dwordx4 v0, s[42:43]\n"
"s_add_u32 m0, s50, 4294943744\n"
"s_nop 0\n"
"global_load_lds_dwordx4 v1, s[42:43]\n"
"s_add_u32 m0, s50, 4294944768\n"
"s_nop 0\n"
"global_load_lds_dwordx4 v2, s[42:43]\n"
"s_add_u32 s42, s46, 185088\n"
"s_addc_u32 s43, s47, 0\n"
"s_add_u32 m0, s50, 4294945792\n"
"s_nop 0\n"
"global_load_lds_dwordx4 v0, s[42:43]\n"
"s_add_u32 m0, s50, 4294946816\n"
"s_nop 0\n"
"global_load_lds_dwordx4 v1, s[42:43]\n"
"s_add_u32 m0, s50, 4294947840\n"
"s_nop 0\n"
"global_load_lds_dwordx4 v2, s[42:43]\n"
"s_waitcnt vmcnt(15)\n"
"ds_read_b64_tr_b16 v[20:21], v10 offset:28736\n"
"ds_read_b64_tr_b16 v[22:23], v10 offset:29504\n"
"s_andn2_b32 s41, 576, s55\n"
"v_add_u32 v9, s41, v15\n"
"ds_read_u16 v9, v9 offset:224\n"
"ds_read_b64_tr_b16 v[24:25], v10 offset:28672\n"
"ds_read_b64_tr_b16 v[26:27], v10 offset:29440\n"
"ds_read_b128 v[178:181], v12 offset:0\n"
"ds_read_b64_tr_b16 v[28:29], v10 offset:28704\n"
"ds_read_b64_tr_b16 v[30:31], v10 offset:29472\n"
"ds_read_b128 v[182:185], v12 offset:6144\n"
"ds_read_b128 v[186:189], v12 offset:12288\n"
"ds_read_b128 v[190:193], v13 offset:0\n"
"s_waitcnt lgkmcnt(8)\n"
"v_mad_u32_u24 v9, v9, s40, v16\n"
"s_add_u32 m0, s51, 139264\n"
"s_nop 0\n"
"global_load_lds_dwordx4 v9, s[56:57]\n"
"s_add_u32 m0, s51, 141312\n"
"s_nop 0\n"
"global_load_lds_dwordx4 v9, s[58:59]\n"
"s_add_u32 m0, s51, 143360\n"
"s_nop 0\n"
"global_load_lds_dwordx4 v9, s[60:61]\n"
"s_waitcnt vmcnt(15)\n"
"ds_read_b64_tr_b16 v[32:33], v10 offset:31808\n"
"ds_read_b64_tr_b16 v[34:35], v10 offset:32576\n"
"s_and_b32 s41, 32, s55\n"
"v_add_u32 v9, s41, v15\n"
"ds_read_u16 v9, v9 offset:352\n"
"ds_read_b64_tr_b16 v[36:37], v10 offset:31744\n"
"ds_read_b64_tr_b16 v[38:39], v10 offset:32512\n"
"ds_read_b64_tr_b16 v[40:41], v10 offset:31776\n"
"ds_read_b64_tr_b16 v[42:43], v10 offset:32544\n"
"s_waitcnt lgkmcnt(4)\n"
"v_mad_u32_u24 v9, v9, s40, v16\n"
"s_add_u32 m0, s51, 151552\n"
"s_nop 0\n"
"global_load_lds_dwordx4 v9, s[56:57]\n"
"s_add_u32 m0, s51, 153600\n"
"s_nop 0\n"
"global_load_lds_dwordx4 v9, s[58:59]\n"
"s_add_u32 m0, s51, 155648\n"
"s_nop 0\n"
"global_load_lds_dwordx4 v9, s[60:61]\n"
"v_mfma_f32_16x16x32_bf16 v[194:197], v[178:181], v[24:27], 0\n"
"s_waitcnt vmcnt(15)\n"
"ds_read_b64_tr_b16 v[44:45], v10 offset:53248\n"
"ds_read_b64_tr_b16 v[46:47], v10 offset:54016\n"
"v_mfma_f32_16x16x32_bf16 v[198:201], v[178:181], v[28:31], 0\n"
"ds_read_b128 v[202:205], v13 offset:6144\n"
"v_mfma_f32_16x16x32_bf16 v[206:209], v[178:181], v[20:23], 0\n"
"ds_read_b64_tr_b16 v[48:49], v10 offset:53280\n"
"ds_read_b64_tr_b16 v[50:51], v10 offset:54048\n"
"v_mfma_f32_16x16x32_bf16 v[178:181], v[182:185], v[24:27], 0\n"
"ds_read_b64_tr_b16 v[52:53], v10 offset:53312\n"
"ds_read_b64_tr_b16 v[54:55], v10 offset:54080\n"
"v_mfma_f32_16x16x32_bf16 v[214:217], v[182:185], v[28:31], 0\n"
"ds_read_b128 v[210:213], v13 offset:12288\n"
"v_mfma_f32_16x16x32_bf16 v[218:221], v[182:185], v[20:23], 0\n"
"v_mfma_f32_16x16x32_bf16 v[222:225], v[186:189], v[24:27], 0\n"
"v_mfma_f32_16x16x32_bf16 v[226:229], v[186:189], v[28:31], 0\n"
"ds_read_b128 v[182:185], v12 offset:2048\n"
"v_mfma_f32_16x16x32_bf16 v[230:233], v[186:189], v[20:23], 0\n"
"s_waitcnt lgkmcnt(11)\n"
"v_mfma_f32_16x16x32_bf16 v[194:197], v[190:193], v[36:39], v[194:197]\n"
"s_waitcnt lgkmcnt(9)\n"
"v_mfma_f32_16x16x32_bf16 v[198:201], v[190:193], v[40:43], v[198:201]\n"
"ds_read_b128 v[186:189], v12 offset:8192\n"
"v_mfma_f32_16x16x32_bf16 v[206:209], v[190:193], v[32:35], v[206:209]\n"
"s_waitcnt lgkmcnt(7)\n"
"v_mfma_f32_16x16x32_bf16 v[178:181], v[202:205], v[36:39], v[178:181]\n"
"v_mfma_f32_16x16x32_bf16 v[214:217], v[202:205], v[40:43], v[214:217]\n"
"ds_read_b128 v[190:193], v12 offset:14336\n"
"v_mfma_f32_16x16x32_bf16 v[218:221], v[202:205], v[32:35], v[218:221]\n"
"s_waitcnt lgkmcnt(3)\n"
"v_mfma_f32_16x16x32_bf16 v[222:225], v[210:213], v[36:39], v[222:225]\n"
"s_waitcnt vmcnt(12)\n"
"ds_read_b64_tr_b16 v[56:57], v10 offset:56320\n"
"ds_read_b64_tr_b16 v[58:59], v10 offset:57088\n"
"v_mfma_f32_16x16x32_bf16 v[226:229], v[210:213], v[40:43], v[226:229]\n"
"ds_read_b128 v[202:205], v13 offset:2048\n"
"v_mfma_f32_16x16x32_bf16 v[230:233], v[210:213], v[32:35], v[230:233]\n"
"s_waitcnt lgkmcnt(5)\n"
"ds_read_b64_tr_b16 v[60:61], v10 offset:56352\n"
"ds_read_b64_tr_b16 v[62:63], v10 offset:57120\n"
"v_mfma_f32_16x16x32_bf16 v[194:197], v[182:185], v[44:47], v[194:197]\n"
"ds_read_b64_tr_b16 v[64:65], v10 offset:56384\n"
"ds_read_b64_tr_b16 v[66:67], v10 offset:57152\n"
"v_mfma_f32_16x16x32_bf16 v[198:201], v[182:185], v[48:51], v[198:201]\n"
"ds_read_b128 v[210:213], v13 offset:8192\n"
"v_mfma_f32_16x16x32_bf16 v[206:209], v[182:185], v[52:55], v[206:209]\n"
"s_waitcnt lgkmcnt(9)\n"
"v_mfma_f32_16x16x32_bf16 v[178:181], v[186:189], v[44:47], v[178:181]\n"
"v_mfma_f32_16x16x32_bf16 v[214:217], v[186:189], v[48:51], v[214:217]\n"
"ds_read_b128 v[182:185], v13 offset:14336\n"
"v_mfma_f32_16x16x32_bf16 v[218:221], v[186:189], v[52:55], v[218:221]\n"
"s_waitcnt lgkmcnt(9)\n"
"v_mfma_f32_16x16x32_bf16 v[222:225], v[190:193], v[44:47], v[222:225]\n"
"s_waitcnt vmcnt(9)\n"
"ds_read_b64_tr_b16 v[68:69], v10 offset:4096\n"
"ds_read_b64_tr_b16 v[70:71], v10 offset:4864\n"
"v_mfma_f32_16x16x32_bf16 v[226:229], v[190:193], v[48:51], v[226:229]\n"
"s_waitcnt lgkmcnt(4)\n"
"ds_read_b128 v[186:189], v12 offset:4096\n"
"s_barrier\n"
"v_mfma_f32_16x16x32_bf16 v[230:233], v[190:193], v[52:55], v[230:233]\n"
"ds_read_b64_tr_b16 v[72:73], v10 offset:4128\n"
"ds_read_b64_tr_b16 v[74:75], v10 offset:4896\n"
"v_mfma_f32_16x16x32_bf16 v[194:197], v[202:205], v[56:59], v[194:197]\n"
"ds_read_b64_tr_b16 v[76:77], v10 offset:4160\n"
"ds_read_b64_tr_b16 v[78:79], v10 offset:4928\n"
"v_mfma_f32_16x16x32_bf16 v[198:201], v[202:205], v[60:63], v[198:201]\n"
"ds_read_b128 v[190:193], v12 offset:10240\n"
"v_mfma_f32_16x16x32_bf16 v[206:209], v[202:205], v[64:67], v[206:209]\n"
"s_waitcnt lgkmcnt(9)\n"
"v_mfma_f32_16x16x32_bf16 v[178:181], v[210:213], v[56:59], v[178:181]\n"
"v_mfma_f32_16x16x32_bf16 v[214:217], v[210:213], v[60:63], v[214:217]\n"
"ds_read_b128 v[202:205], v12 offset:16384\n"
"v_mfma_f32_16x16x32_bf16 v[218:221], v[210:213], v[64:67], v[218:221]\n"
"s_waitcnt lgkmcnt(9)\n"
"v_mfma_f32_16x16x32_bf16 v[222:225], v[182:185], v[56:59], v[222:225]\n"
"s_waitcnt vmcnt(6)\n"
"ds_read_b64_tr_b16 v[80:81], v10 offset:7168\n"
"ds_read_b64_tr_b16 v[82:83], v10 offset:7936\n"
"v_mfma_f32_16x16x32_bf16 v[226:229], v[182:185], v[60:63], v[226:229]\n"
"ds_read_b128 v[210:213], v13 offset:4096\n"
"v_mfma_f32_16x16x32_bf16 v[230:233], v[182:185], v[64:67], v[230:233]\n"
"s_waitcnt lgkmcnt(9)\n"
"ds_read_b64_tr_b16 v[84:85], v10 offset:7200\n"
"ds_read_b64_tr_b16 v[86:87], v10 offset:7968\n"
"v_mfma_f32_16x16x32_bf16 v[194:197], v[186:189], v[68:71], v[194:197]\n"
"s_waitcnt lgkmcnt(9)\n"
"ds_read_b64_tr_b16 v[88:89], v10 offset:7232\n"
"ds_read_b64_tr_b16 v[90:91], v10 offset:8000\n"
"v_mfma_f32_16x16x32_bf16 v[198:201], v[186:189], v[72:75], v[198:201]\n"
"s_waitcnt lgkmcnt(9)\n"
"ds_read_b128 v[182:185], v13 offset:10240\n"
"v_mfma_f32_16x16x32_bf16 v[206:209], v[186:189], v[76:79], v[206:209]\n"
"s_waitcnt lgkmcnt(9)\n"
"v_mfma_f32_16x16x32_bf16 v[178:181], v[190:193], v[68:71], v[178:181]\n"
"v_mfma_f32_16x16x32_bf16 v[214:217], v[190:193], v[72:75], v[214:217]\n"
"ds_read_b128 v[186:189], v13 offset:16384\n"
"v_mfma_f32_16x16x32_bf16 v[218:221], v[190:193], v[76:79], v[218:221]\n"
"s_waitcnt lgkmcnt(9)\n"
"v_mfma_f32_16x16x32_bf16 v[222:225], v[202:205], v[68:71], v[222:225]\n"
"v_mfma_f32_16x16x32_bf16 v[226:229], v[202:205], v[72:75], v[226:229]\n"
"ds_read_b128 v[190:193], v12 offset:18432\n"
"v_mfma_f32_16x16x32_bf16 v[230:233], v[202:205], v[76:79], v[230:233]\n"
"s_waitcnt lgkmcnt(7)\n"
"v_mfma_f32_16x16x32_bf16 v[194:197], v[210:213], v[80:83], v[194:197]\n"
"s_waitcnt lgkmcnt(5)\n"
"v_mfma_f32_16x16x32_bf16 v[198:201], v[210:213], v[84:87], v[198:201]\n"
"s_waitcnt lgkmcnt(3)\n"
"ds_read_b128 v[202:205], v13 offset:18432\n"
"v_mfma_f32_16x16x32_bf16 v[206:209], v[210:213], v[88:91], v[206:209]\n"
"s_waitcnt lgkmcnt(3)\n"
"v_mfma_f32_16x16x32_bf16 v[178:181], v[182:185], v[80:83], v[178:181]\n"
"v_mfma_f32_16x16x32_bf16 v[214:217], v[182:185], v[84:87], v[214:217]\n"
"ds_read_b128 v[240:243], v12 offset:20480\n"
"v_mfma_f32_16x16x32_bf16 v[218:221], v[182:185], v[88:91], v[218:221]\n"
"s_waitcnt lgkmcnt(3)\n"
"v_mfma_f32_16x16x32_bf16 v[222:225], v[186:189], v[80:83], v[222:225]\n"
"v_cvt_pk_bf16_f32 v184, v194, v195\n"
"v_cvt_pk_bf16_f32 v185, v196, v197\n"
"v_cvt_pk_bf16_f32 v210, v198, v199\n"
"v_cvt_pk_bf16_f32 v211, v200, v201\n"
"v_mfma_f32_16x16x32_bf16 v[226:229], v[186:189], v[84:87], v[226:229]\n"
"ds_read_b128 v[196:199], v13 offset:20480\n"
"v_cvt_pk_bf16_f32 v236, v206, v207\n"
"v_cvt_pk_bf16_f32 v237, v208, v209\n"
"v_mfma_f32_16x16x32_bf16 v[230:233], v[186:189], v[88:91], v[230:233]\n"
"s_waitcnt lgkmcnt(3)\n"
"v_cvt_pk_bf16_f32 v186, v178, v179\n"
"v_cvt_pk_bf16_f32 v187, v180, v181\n"
"v_mfma_f32_16x16x32_bf16 v[178:181], v[190:193], v[24:27], 0\n"
"v_cvt_pk_bf16_f32 v212, v214, v215\n"
"v_cvt_pk_bf16_f32 v213, v216, v217\n"
"v_mfma_f32_16x16x32_bf16 v[206:209], v[190:193], v[28:31], 0\n"
"ds_read_b128 v[214:217], v12 offset:22528\n"
"v_cvt_pk_bf16_f32 v238, v218, v219\n"
"v_cvt_pk_bf16_f32 v239, v220, v221\n"
"v_mfma_f32_16x16x32_bf16 v[250:253], v[190:193], v[20:23], 0\n"
"s_waitcnt lgkmcnt(3)\n"
"v_cvt_pk_bf16_f32 v194, v222, v223\n"
"v_cvt_pk_bf16_f32 v195, v224, v225\n"
"v_mfma_f32_16x16x32_bf16 v[178:181], v[202:205], v[36:39], v[178:181]\n"
"v_cvt_pk_bf16_f32 v220, v226, v227\n"
"v_cvt_pk_bf16_f32 v221, v228, v229\n"
"v_mfma_f32_16x16x32_bf16 v[206:209], v[202:205], v[40:43], v[206:209]\n"
"ds_read_b128 v[188:191], v13 offset:22528\n"
"v_cvt_pk_bf16_f32 v246, v230, v231\n"
"v_cvt_pk_bf16_f32 v247, v232, v233\n"
"v_mfma_f32_16x16x32_bf16 v[250:253], v[202:205], v[32:35], v[250:253]\n"
"s_waitcnt lgkmcnt(3)\n"
"v_mfma_f32_16x16x32_bf16 v[178:181], v[240:243], v[44:47], v[178:181]\n"
"v_mfma_f32_16x16x32_bf16 v[206:209], v[240:243], v[48:51], v[206:209]\n"
"ds_read_b128 v[200:203], v12 offset:24576\n"
"v_mfma_f32_16x16x32_bf16 v[250:253], v[240:243], v[52:55], v[250:253]\n"
"s_waitcnt lgkmcnt(3)\n"
"v_mfma_f32_16x16x32_bf16 v[178:181], v[196:199], v[56:59], v[178:181]\n"
"v_mfma_f32_16x16x32_bf16 v[206:209], v[196:199], v[60:63], v[206:209]\n"
"ds_read_b128 v[224:227], v13 offset:24576\n"
"v_mfma_f32_16x16x32_bf16 v[250:253], v[196:199], v[64:67], v[250:253]\n"
"s_waitcnt lgkmcnt(3)\n"
"v_mfma_f32_16x16x32_bf16 v[178:181], v[214:217], v[68:71], v[178:181]\n"
"v_mfma_f32_16x16x32_bf16 v[206:209], v[214:217], v[72:75], v[206:209]\n"
"ds_read_b128 v[228:231], v12 offset:26624\n"
"v_mfma_f32_16x16x32_bf16 v[250:253], v[214:217], v[76:79], v[250:253]\n"
"s_waitcnt lgkmcnt(3)\n"
"v_mfma_f32_16x16x32_bf16 v[178:181], v[188:191], v[80:83], v[178:181]\n"
"v_mfma_f32_16x16x32_bf16 v[206:209], v[188:191], v[84:87], v[206:209]\n"
"ds_read_b128 v[214:217], v13 offset:26624\n"
"v_mfma_f32_16x16x32_bf16 v[250:253], v[188:191], v[88:91], v[250:253]\n"
"s_waitcnt lgkmcnt(3)\n"
"v_mfma_f32_16x16x32_bf16 v[190:193], v[200:203], v[24:27], 0\n"
"v_mfma_f32_16x16x32_bf16 v[232:235], v[200:203], v[28:31], 0\n"
"ds_read_b128 v[240:243], v12 offset:28672\n"
"v_mfma_f32_16x16x32_bf16 v[92:95], v[200:203], v[20:23], 0\n"
"s_waitcnt lgkmcnt(3)\n"
"v_mfma_f32_16x16x32_bf16 v[190:193], v[224:227], v[36:39], v[190:193]\n"
"v_cvt_pk_bf16_f32 v196, v178, v179\n"
"v_cvt_pk_bf16_f32 v197, v180, v181\n"
"v_cvt_pk_bf16_f32 v222, v206, v207\n"
"v_cvt_pk_bf16_f32 v223, v208, v209\n"
"v_mfma_f32_16x16x32_bf16 v[232:235], v[224:227], v[40:43], v[232:235]\n"
"ds_read_b128 v[178:181], v13 offset:28672\n"
"v_cvt_pk_bf16_f32 v248, v250, v251\n"
"v_cvt_pk_bf16_f32 v249, v252, v253\n"
"v_mfma_f32_16x16x32_bf16 v[92:95], v[224:227], v[32:35], v[92:95]\n"
"s_waitcnt lgkmcnt(3)\n"
"v_mfma_f32_16x16x32_bf16 v[190:193], v[228:231], v[44:47], v[190:193]\n"
"v_mfma_f32_16x16x32_bf16 v[232:235], v[228:231], v[48:51], v[232:235]\n"
"ds_read_b128 v[198:201], v12 offset:30720\n"
"v_mfma_f32_16x16x32_bf16 v[92:95], v[228:231], v[52:55], v[92:95]\n"
"s_waitcnt lgkmcnt(3)\n"
"v_mfma_f32_16x16x32_bf16 v[190:193], v[214:217], v[56:59], v[190:193]\n"
"v_mfma_f32_16x16x32_bf16 v[232:235], v[214:217], v[60:63], v[232:235]\n"
"ds_read_b128 v[202:205], v13 offset:30720\n"
"v_mfma_f32_16x16x32_bf16 v[92:95], v[214:217], v[64:67], v[92:95]\n"
"s_waitcnt lgkmcnt(3)\n"
"v_mfma_f32_16x16x32_bf16 v[190:193], v[240:243], v[68:71], v[190:193]\n"
"v_mfma_f32_16x16x32_bf16 v[232:235], v[240:243], v[72:75], v[232:235]\n"
"ds_read_b128 v[206:209], v12 offset:32768\n"
"v_mfma_f32_16x16x32_bf16 v[92:95], v[240:243], v[76:79], v[92:95]\n"
"s_waitcnt lgkmcnt(3)\n"
"v_mfma_f32_16x16x32_bf16 v[190:193], v[178:181], v[80:83], v[190:193]\n"
"v_mfma_f32_16x16x32_bf16 v[232:235], v[178:181], v[84:87], v[232:235]\n"
"ds_read_b128 v[216:219], v13 offset:32768\n"
"v_mfma_f32_16x16x32_bf16 v[92:95], v[178:181], v[88:91], v[92:95]\n"
"s_waitcnt lgkmcnt(3)\n"
"v_mfma_f32_16x16x32_bf16 v[180:183], v[198:201], v[24:27], 0\n"
"v_mfma_f32_16x16x32_bf16 v[224:227], v[198:201], v[28:31], 0\n"
"ds_read_b128 v[228:231], v12 offset:34816\n"
"v_mfma_f32_16x16x32_bf16 v[242:245], v[198:201], v[20:23], 0\n"
"s_waitcnt lgkmcnt(3)\n"
"v_mfma_f32_16x16x32_bf16 v[180:183], v[202:205], v[36:39], v[180:183]\n"
"v_cvt_pk_bf16_f32 v188, v190, v191\n"
"v_cvt_pk_bf16_f32 v189, v192, v193\n"
"v_cvt_pk_bf16_f32 v214, v232, v233\n"
"v_cvt_pk_bf16_f32 v215, v234, v235\n"
"v_mfma_f32_16x16x32_bf16 v[224:227], v[202:205], v[40:43], v[224:227]\n"
"ds_read_b128 v[190:193], v13 offset:34816\n"
"v_cvt_pk_bf16_f32 v240, v92, v93\n"
"v_cvt_pk_bf16_f32 v241, v94, v95\n"
"v_mfma_f32_16x16x32_bf16 v[242:245], v[202:205], v[32:35], v[242:245]\n"
"s_waitcnt lgkmcnt(3)\n"
"v_mfma_f32_16x16x32_bf16 v[180:183], v[206:209], v[44:47], v[180:183]\n"
"v_mfma_f32_16x16x32_bf16 v[224:227], v[206:209], v[48:51], v[224:227]\n"
"v_mfma_f32_16x16x32_bf16 v[242:245], v[206:209], v[52:55], v[242:245]\n"
"s_waitcnt lgkmcnt(2)\n"
"v_mfma_f32_16x16x32_bf16 v[180:183], v[216:219], v[56:59], v[180:183]\n"
"v_mfma_f32_16x16x32_bf16 v[224:227], v[216:219], v[60:63], v[224:227]\n"
"v_mfma_f32_16x16x32_bf16 v[242:245], v[216:219], v[64:67], v[242:245]\n"
"s_and_b32 s41, 160, s55\n"
"v_add_u32 v9, s41, v15\n"
"s_waitcnt lgkmcnt(1)\n"
"v_mfma_f32_16x16x32_bf16 v[180:183], v[228:231], v[68:71], v[180:183]\n"
"ds_read_u16 v9, v9 offset:96\n"
"v_mfma_f32_16x16x32_bf16 v[224:227], v[228:231], v[72:75], v[224:227]\n"
"v_mfma_f32_16x16x32_bf16 v[242:245], v[228:231], v[76:79], v[242:245]\n"
"s_waitcnt vmcnt(0)\n"
"s_barrier\n"
"ds_read_b128 v[198:201], v12 offset:43008\n"
"s_waitcnt lgkmcnt(2)\n"
"v_mfma_f32_16x16x32_bf16 v[180:183], v[190:193], v[80:83], v[180:183]\n"
"ds_read_b128 v[206:209], v13 offset:43008\n"
"v_mfma_f32_16x16x32_bf16 v[224:227], v[190:193], v[84:87], v[224:227]\n"
"v_mfma_f32_16x16x32_bf16 v[242:245], v[190:193], v[88:91], v[242:245]\n"
"s_waitcnt lgkmcnt(2)\n"
"ds_read_b128 v[190:193], v12 offset:45056\n"
"v_mad_u32_u24 v9, v9, s40, v16\n"
"s_waitcnt lgkmcnt(2)\n"
"v_mfma_f32_16x16x32_bf16 v[216:219], v[198:201], v[24:27], 0\n"
"v_mfma_f32_16x16x32_bf16 v[232:235], v[198:201], v[28:31], 0\n"
"v_mfma_f32_16x16x32_bf16 v[252:255], v[198:201], v[20:23], 0\n"
"s_waitcnt lgkmcnt(1)\n"
"ds_read_b128 v[198:201], v13 offset:45056\n"
"v_cvt_pk_bf16_f32 v178, v180, v181\n"
"v_cvt_pk_bf16_f32 v179, v182, v183\n"
"v_mfma_f32_16x16x32_bf16 v[216:219], v[206:209], v[36:39], v[216:219]\n"
"v_cvt_pk_bf16_f32 v204, v224, v225\n"
"v_cvt_pk_bf16_f32 v205, v226, v227\n"
"v_mfma_f32_16x16x32_bf16 v[232:235], v[206:209], v[40:43], v[232:235]\n"
"v_cvt_pk_bf16_f32 v230, v242, v243\n"
"v_cvt_pk_bf16_f32 v231, v244, v245\n"
"v_mfma_f32_16x16x32_bf16 v[252:255], v[206:209], v[32:35], v[252:255]\n"
"s_waitcnt lgkmcnt(1)\n"
"ds_read_b128 v[180:183], v12 offset:47104\n"
"v_mfma_f32_16x16x32_bf16 v[216:219], v[190:193], v[44:47], v[216:219]\n"
"v_mfma_f32_16x16x32_bf16 v[232:235], v[190:193], v[48:51], v[232:235]\n"
"s_add_u32 m0, s51, 102400\n"
"s_nop 0\n"
"global_load_lds_dwordx4 v9, s[56:57]\n"
"v_mfma_f32_16x16x32_bf16 v[252:255], v[190:193], v[52:55], v[252:255]\n"
"s_waitcnt lgkmcnt(1)\n"
"ds_read_b128 v[190:193], v13 offset:47104\n"
"v_mfma_f32_16x16x32_bf16 v[216:219], v[198:201], v[56:59], v[216:219]\n"
"v_mfma_f32_16x16x32_bf16 v[232:235], v[198:201], v[60:63], v[232:235]\n"
"s_add_u32 m0, s51, 104448\n"
"s_nop 0\n"
"global_load_lds_dwordx4 v9, s[58:59]\n"
"v_mfma_f32_16x16x32_bf16 v[252:255], v[198:201], v[64:67], v[252:255]\n"
"s_waitcnt lgkmcnt(1)\n"
"ds_read_b128 v[198:201], v12 offset:49152\n"
"v_mfma_f32_16x16x32_bf16 v[216:219], v[180:183], v[68:71], v[216:219]\n"
"v_mfma_f32_16x16x32_bf16 v[232:235], v[180:183], v[72:75], v[232:235]\n"
"s_add_u32 m0, s51, 106496\n"
"s_nop 0\n"
"global_load_lds_dwordx4 v9, s[60:61]\n"
"s_and_b32 s41, 32, s55\n"
"v_add_u32 v9, s41, v15\n"
"v_mfma_f32_16x16x32_bf16 v[252:255], v[180:183], v[76:79], v[252:255]\n"
"s_waitcnt lgkmcnt(1)\n"
"ds_read_u16 v9, v9 offset:832\n"
"v_mfma_f32_16x16x32_bf16 v[216:219], v[190:193], v[80:83], v[216:219]\n"
"ds_read_b128 v[180:183], v13 offset:49152\n"
"v_mfma_f32_16x16x32_bf16 v[232:235], v[190:193], v[84:87], v[232:235]\n"
"v_mfma_f32_16x16x32_bf16 v[252:255], v[190:193], v[88:91], v[252:255]\n"
"s_waitcnt lgkmcnt(2)\n"
"ds_read_b128 v[190:193], v12 offset:51200\n"
"v_mfma_f32_16x16x32_bf16 v[206:209], v[198:201], v[24:27], 0\n"
"v_mfma_f32_16x16x32_bf16 v[226:229], v[198:201], v[28:31], 0\n"
"v_mfma_f32_16x16x32_bf16 v[242:245], v[198:201], v[20:23], 0\n"
"s_barrier\n"
"s_add_u32 s42, s48, 0\n"
"s_addc_u32 s43, s49, 0\n"
"s_add_u32 m0, s50, 24576\n"
"s_nop 0\n"
"global_load_lds_dwordx4 v3, s[42:43]\n"
"s_waitcnt lgkmcnt(2)\n"
"v_mad_u32_u24 v9, v9, s40, v16\n"
"v_cvt_pk_bf16_f32 v198, v216, v217\n"
"v_cvt_pk_bf16_f32 v199, v218, v219\n"
"s_waitcnt lgkmcnt(1)\n"
"v_mfma_f32_16x16x32_bf16 v[206:209], v[180:183], v[36:39], v[206:209]\n"
"s_add_u32 m0, s51, 114688\n"
"s_nop 0\n"
"global_load_lds_dwordx4 v9, s[56:57]\n"
"s_add_u32 m0, s50, 25600\n"
"s_nop 0\n"
"global_load_lds_dwordx4 v4, s[42:43]\n"
"ds_read_b128 v[200:203], v13 offset:51200\n"
"v_cvt_pk_bf16_f32 v224, v232, v233\n"
"v_cvt_pk_bf16_f32 v225, v234, v235\n"
"v_mfma_f32_16x16x32_bf16 v[226:229], v[180:183], v[40:43], v[226:229]\n"
"v_cvt_pk_bf16_f32 v250, v252, v253\n"
"v_cvt_pk_bf16_f32 v251, v254, v255\n"
"v_mfma_f32_16x16x32_bf16 v[242:245], v[180:183], v[32:35], v[242:245]\n"
"s_add_u32 m0, s50, 26624\n"
"s_nop 0\n"
"global_load_lds_dwordx4 v5, s[42:43]\n"
"s_add_u32 m0, s51, 116736\n"
"s_nop 0\n"
"global_load_lds_dwordx4 v9, s[58:59]\n"
"s_waitcnt lgkmcnt(1)\n"
"v_mfma_f32_16x16x32_bf16 v[206:209], v[190:193], v[44:47], v[206:209]\n"
"ds_read_b128 v[180:183], v12 offset:53248\n"
"s_add_u32 s42, s48, 18432\n"
"s_addc_u32 s43, s49, 0\n"
"s_add_u32 m0, s50, 27648\n"
"s_nop 0\n"
"global_load_lds_dwordx4 v3, s[42:43]\n"
"v_mfma_f32_16x16x32_bf16 v[226:229], v[190:193], v[48:51], v[226:229]\n"
"s_add_u32 m0, s51, 118784\n"
"s_nop 0\n"
"global_load_lds_dwordx4 v9, s[60:61]\n"
"s_add_u32 m0, s50, 28672\n"
"s_nop 0\n"
"global_load_lds_dwordx4 v4, s[42:43]\n"
"v_mfma_f32_16x16x32_bf16 v[242:245], v[190:193], v[52:55], v[242:245]\n"
"s_waitcnt lgkmcnt(1)\n"
"ds_read_b128 v[190:193], v13 offset:53248\n"
"v_mfma_f32_16x16x32_bf16 v[206:209], v[200:203], v[56:59], v[206:209]\n"
"s_add_u32 m0, s50, 29696\n"
"s_nop 0\n"
"global_load_lds_dwordx4 v5, s[42:43]\n"
"s_andn2_b32 s41, 288, s55\n"
"v_add_u32 v9, s41, v15\n"
"v_mfma_f32_16x16x32_bf16 v[226:229], v[200:203], v[60:63], v[226:229]\n"
"ds_read_u16 v9, v9 offset:128\n"
"v_mfma_f32_16x16x32_bf16 v[242:245], v[200:203], v[64:67], v[242:245]\n"
"s_waitcnt lgkmcnt(2)\n"
"ds_read_b128 v[200:203], v12 offset:55296\n"
"v_mfma_f32_16x16x32_bf16 v[206:209], v[180:183], v[68:71], v[206:209]\n"
"v_mfma_f32_16x16x32_bf16 v[226:229], v[180:183], v[72:75], v[226:229]\n"
"v_mfma_f32_16x16x32_bf16 v[242:245], v[180:183], v[76:79], v[242:245]\n"
"s_waitcnt lgkmcnt(2)\n"
"ds_read_b128 v[216:219], v13 offset:55296\n"
"v_mfma_f32_16x16x32_bf16 v[206:209], v[190:193], v[80:83], v[206:209]\n"
"v_mfma_f32_16x16x32_bf16 v[226:229], v[190:193], v[84:87], v[226:229]\n"
"v_mfma_f32_16x16x32_bf16 v[242:245], v[190:193], v[88:91], v[242:245]\n"
"s_waitcnt lgkmcnt(2)\n"
"ds_read_b128 v[190:193], v12 offset:57344\n"
"v_mad_u32_u24 v9, v9, s40, v16\n"
"s_add_u32 m0, s51, 126976\n"
"s_nop 0\n"
"global_load_lds_dwordx4 v9, s[56:57]\n"
"s_waitcnt lgkmcnt(2)\n"
"v_mfma_f32_16x16x32_bf16 v[252:255], v[200:203], v[24:27], 0\n"
"v_mfma_f32_16x16x32_bf16 v[108:111], v[200:203], v[28:31], 0\n"
"v_cvt_pk_bf16_f32 v180, v206, v207\n"
"v_cvt_pk_bf16_f32 v181, v208, v209\n"
"v_mfma_f32_16x16x32_bf16 v[112:115], v[200:203], v[20:23], 0\n"
"s_waitcnt lgkmcnt(1)\n"
"ds_read_b128 v[200:203], v13 offset:57344\n"
"v_cvt_pk_bf16_f32 v206, v226, v227\n"
"v_mfma_f32_16x16x32_bf16 v[252:255], v[216:219], v[36:39], v[252:255]\n"
"s_add_u32 m0, s51, 129024\n"
"s_nop 0\n"
"global_load_lds_dwordx4 v9, s[58:59]\n"
"v_cvt_pk_bf16_f32 v207, v228, v229\n"
"v_mfma_f32_16x16x32_bf16 v[108:111], v[216:219], v[40:43], v[108:111]\n"
"s_waitcnt vmcnt(4)\n"
"s_barrier\n"
"s_waitcnt vmcnt(7)\n"
"v_cvt_pk_bf16_f32 v232, v242, v243\n"
"v_cvt_pk_bf16_f32 v233, v244, v245\n"
"v_mfma_f32_16x16x32_bf16 v[112:115], v[216:219], v[32:35], v[112:115]\n"
"s_waitcnt lgkmcnt(1)\n"
"ds_read_b64_tr_b16 v[92:93], v11 offset:53248\n"
"ds_read_b64_tr_b16 v[94:95], v11 offset:54016\n"
"v_mfma_f32_16x16x32_bf16 v[252:255], v[190:193], v[44:47], v[252:255]\n"
"s_add_u32 m0, s51, 131072\n"
"s_nop 0\n"
"global_load_lds_dwordx4 v9, s[60:61]\n"
"ds_read_b64_tr_b16 v[96:97], v11 offset:53256\n"
"ds_read_b64_tr_b16 v[98:99], v11 offset:54024\n"
"v_mfma_f32_16x16x32_bf16 v[108:111], v[190:193], v[48:51], v[108:111]\n"
"ds_read_b64_tr_b16 v[226:227], v10 offset:53312\n"
"ds_read_b64_tr_b16 v[228:229], v10 offset:54080\n"
"s_waitcnt vmcnt(3)\n"
"ds_read_b64_tr_b16 v[100:101], v11 offset:56320\n"
"ds_read_b64_tr_b16 v[102:103], v11 offset:57088\n"
"v_mfma_f32_16x16x32_bf16 v[112:115], v[190:193], v[52:55], v[112:115]\n"
"s_waitcnt lgkmcnt(8)\n"
"ds_read_b64_tr_b16 v[104:105], v11 offset:56328\n"
"ds_read_b64_tr_b16 v[106:107], v11 offset:57096\n"
"v_mfma_f32_16x16x32_bf16 v[252:255], v[200:203], v[56:59], v[252:255]\n"
"ds_read_b64_tr_b16 v[132:133], v10 offset:56384\n"
"ds_read_b64_tr_b16 v[134:135], v10 offset:57152\n"
"v_mfma_f32_16x16x32_bf16 v[108:111], v[200:203], v[60:63], v[108:111]\n"
"ds_read_b128 v[190:193], v12 offset:59392\n"
"ds_read_b128 v[216:219], v13 offset:59392\n"
"v_mfma_f32_16x16x32_bf16 v[112:115], v[200:203], v[64:67], v[112:115]\n"
"s_waitcnt lgkmcnt(8)\n"
"s_add_u32 s42, s48, 36864\n"
"s_addc_u32 s43, s49, 0\n"
"s_add_u32 m0, s50, 24576\n"
"s_nop 0\n"
"global_load_lds_dwordx4 v3, s[42:43]\n"
"s_add_u32 m0, s50, 25600\n"
"s_nop 0\n"
"global_load_lds_dwordx4 v4, s[42:43]\n"
"s_waitcnt lgkmcnt(1)\n"
"v_mfma_f32_16x16x32_bf16 v[252:255], v[190:193], v[68:71], v[252:255]\n"
"v_mfma_f32_16x16x32_bf16 v[108:111], v[190:193], v[72:75], v[108:111]\n"
"s_add_u32 m0, s50, 26624\n"
"s_nop 0\n"
"global_load_lds_dwordx4 v5, s[42:43]\n"
"v_mfma_f32_16x16x32_bf16 v[112:115], v[190:193], v[76:79], v[112:115]\n"
"s_waitcnt lgkmcnt(0)\n"
"v_mov_b32 v190, 0x3f803f80\n"
"v_mov_b32 v191, 0x3f803f80\n"
"v_mfma_f32_16x16x32_bf16 v[252:255], v[216:219], v[80:83], v[252:255]\n"
"v_mov_b32 v192, 0x3f803f80\n"
"v_mov_b32 v193, 0x3f803f80\n"
"s_add_u32 s42, s48, 55296\n"
"s_addc_u32 s43, s49, 0\n"
"s_add_u32 m0, s50, 27648\n"
"s_nop 0\n"
"global_load_lds_dwordx4 v3, s[42:43]\n"
"v_mfma_f32_16x16x32_bf16 v[108:111], v[216:219], v[84:87], v[108:111]\n"
"v_mfma_f32_16x16x32_bf16 v[112:115], v[216:219], v[88:91], v[112:115]\n"
"s_add_u32 m0, s50, 28672\n"
"s_nop 0\n"
"global_load_lds_dwordx4 v4, s[42:43]\n"
"v_cvt_pk_bf16_f32 v182, v252, v253\n"
"v_cvt_pk_bf16_f32 v183, v254, v255\n"
"s_add_u32 m0, s50, 29696\n"
"s_nop 0\n"
"global_load_lds_dwordx4 v5, s[42:43]\n"
"s_and_b32 s41, 32, s55\n"
"v_add_u32 v9, s41, v15\n"
"s_barrier\n"
"s_waitcnt vmcnt(3)\n"
"ds_read_b64_tr_b16 v[0:1], v11 offset:53248\n"
"ds_read_b64_tr_b16 v[2:3], v11 offset:54016\n"
"v_cvt_pk_bf16_f32 v208, v108, v109\n"
"v_cvt_pk_bf16_f32 v209, v110, v111\n"
"v_cvt_pk_bf16_f32 v234, v112, v113\n"
"v_cvt_pk_bf16_f32 v235, v114, v115\n"
"ds_read_b64_tr_b16 v[108:109], v11 offset:53256\n"
"ds_read_b64_tr_b16 v[110:111], v11 offset:54024\n"
"ds_read_b64_tr_b16 v[200:201], v10 offset:53312\n"
"ds_read_b64_tr_b16 v[202:203], v10 offset:54080\n"
"s_waitcnt vmcnt(0)\n"
"ds_read_b64_tr_b16 v[112:113], v11 offset:56320\n"
"ds_read_b64_tr_b16 v[114:115], v11 offset:57088\n"
"ds_read_b64_tr_b16 v[116:117], v11 offset:56328\n"
"ds_read_b64_tr_b16 v[118:119], v11 offset:57096\n"
"ds_read_b64_tr_b16 v[252:253], v10 offset:56384\n"
"ds_read_b64_tr_b16 v[254:255], v10 offset:57152\n"
"ds_read_u16 v9, v9 offset:896\n"
"ds_read_b128 v[216:219], v17 offset:28672\n"
"ds_read_b128 v[242:245], v17 offset:29696\n"
"s_waitcnt lgkmcnt(9)\n"
"s_add_u32 s42, s48, 12288\n"
"s_addc_u32 s43, s49, 0\n"
"s_add_u32 m0, s50, 24576\n"
"s_nop 0\n"
"global_load_lds_dwordx4 v6, s[42:43]\n"
"s_waitcnt lgkmcnt(2)\n"
"v_mad_u32_u24 v9, v9, s40, v16\n"
"s_add_u32 m0, s50, 25600\n"
"s_nop 0\n"
"global_load_lds_dwordx4 v7, s[42:43]\n"
"s_add_u32 m0, s51, 139264\n"
"s_nop 0\n"
"global_load_lds_dwordx4 v9, s[56:57]\n"
"s_waitcnt lgkmcnt(1)\n"
"v_mfma_f32_16x16x32_bf16 v[120:123], v[190:193], v[216:219], 0\n"
"s_add_u32 m0, s50, 26624\n"
"s_nop 0\n"
"global_load_lds_dwordx4 v8, s[42:43]\n"
"v_mfma_f32_16x16x32_bf16 v[136:139], v[186:189], v[216:219], 0\n"
"ds_read_b128 v[124:127], v17 offset:32768\n"
"s_add_u32 m0, s51, 141312\n"
"s_nop 0\n"
"global_load_lds_dwordx4 v9, s[58:59]\n"
"s_add_u32 s42, s48, 49152\n"
"s_addc_u32 s43, s49, 0\n"
"s_add_u32 m0, s50, 27648\n"
"s_nop 0\n"
"global_load_lds_dwordx4 v6, s[42:43]\n"
"v_mfma_f32_16x16x32_bf16 v[128:131], v[212:215], v[216:219], 0\n"
"s_add_u32 m0, s51, 143360\n"
"s_nop 0\n"
"global_load_lds_dwordx4 v9, s[60:61]\n"
"s_add_u32 m0, s50, 28672\n"
"s_nop 0\n"
"global_load_lds_dwordx4 v7, s[42:43]\n"
"v_mfma_f32_16x16x32_bf16 v[4:7], v[238:241], v[216:219], 0\n"
"s_waitcnt lgkmcnt(1)\n"
"v_mfma_f32_16x16x32_bf16 v[120:123], v[190:193], v[242:245], v[120:123]\n"
"s_add_u32 m0, s50, 29696\n"
"s_nop 0\n"
"global_load_lds_dwordx4 v8, s[42:43]\n"
"v_mfma_f32_16x16x32_bf16 v[136:139], v[194:197], v[242:245], v[136:139]\n"
"s_and_b32 s41, 160, s55\n"
"v_add_u32 v177, s41, v15\n"
"v_mfma_f32_16x16x32_bf16 v[128:131], v[220:223], v[242:245], v[128:131]\n"
"ds_read_b128 v[216:219], v17 offset:33792\n"
"v_mfma_f32_16x16x32_bf16 v[4:7], v[246:249], v[242:245], v[4:7]\n"
"s_waitcnt lgkmcnt(1)\n"
"ds_read_u16 v177, v177 offset:288\n"
"v_mfma_f32_16x16x32_bf16 v[120:123], v[190:193], v[124:127], v[120:123]\n"
"ds_read_b64 v[244:245], v17 offset:8704\n"
"v_mfma_f32_16x16x32_bf16 v[136:139], v[178:181], v[124:127], v[136:139]\n"
"v_mfma_f32_16x16x32_bf16 v[128:131], v[204:207], v[124:127], v[128:131]\n"
"v_mov_b32 v242, 0\n"
"v_mov_b32 v243, 0\n"
"v_mfma_f32_16x16x32_bf16 v[4:7], v[230:233], v[124:127], v[4:7]\n"
"s_waitcnt lgkmcnt(2)\n"
"v_mfma_f32_16x16x32_bf16 v[120:123], v[190:193], v[216:219], v[120:123]\n"
"s_waitcnt lgkmcnt(1)\n"
"v_mad_u32_u24 v177, v177, s40, v16\n"
"s_waitcnt lgkmcnt(0)\n"
"v_mfma_f32_16x16x32_bf16 v[120:123], v[190:193], v[242:245], v[120:123]\n"
"s_add_u32 m0, s51, 151552\n"
"s_nop 0\n"
"global_load_lds_dwordx4 v177, s[56:57]\n"
"v_mfma_f32_16x16x32_bf16 v[4:7], v[234:237], v[216:219], v[4:7]\n"
"ds_read_b128 v[190:193], v12 offset:0\n"
"v_mfma_f32_16x16x32_bf16 v[4:7], v[248:251], v[242:245], v[4:7]\n"
"v_mfma_f32_16x16x32_bf16 v[136:139], v[182:185], v[216:219], v[136:139]\n"
"ds_read_b128 v[124:127], v13 offset:0\n"
"s_add_u32 m0, s51, 153600\n"
"s_nop 0\n"
"global_load_lds_dwordx4 v177, s[58:59]\n"
"v_rcp_f32 v176, v120\n"
"v_mfma_f32_16x16x32_bf16 v[136:139], v[196:199], v[242:245], v[136:139]\n"
"v_add_u32 v120, 0xd4800000, v120\n"
"v_cmp_lt_u32 vcc, 0x28000000, v120\n"
"v_mfma_f32_16x16x32_bf16 v[128:131], v[208:211], v[216:219], v[128:131]\n"
"v_mul_f32 v4, v4, v176\n"
"v_mul_f32 v5, v5, v176\n"
"v_mfma_f32_16x16x32_bf16 v[128:131], v[222:225], v[242:245], v[128:131]\n"
"s_add_u32 m0, s51, 155648\n"
"s_nop 0\n"
"global_load_lds_dwordx4 v177, s[60:61]\n"
"v_cvt_pk_bf16_f32 v244, v4, v5\n"
"s_waitcnt lgkmcnt(1)\n"
"v_mfma_f32_16x16x32_bf16 v[216:219], v[190:193], v[24:27], 0\n"
"s_or_b64 s[62:63], s[62:63], vcc\n"
"ds_read_b128 v[120:123], v12 offset:2048\n"
"v_mul_f32 v6, v6, v176\n"
"v_mfma_f32_16x16x32_bf16 v[140:143], v[190:193], v[28:31], 0\n"
"v_mul_f32 v7, v7, v176\n"
"v_cvt_pk_bf16_f32 v245, v6, v7\n"
"v_mfma_f32_16x16x32_bf16 v[144:147], v[190:193], v[20:23], 0\n"
"s_waitcnt lgkmcnt(1)\n"
"ds_read_b128 v[190:193], v13 offset:2048\n"
"v_mul_f32 v136, v136, v176\n"
"v_mfma_f32_16x16x32_bf16 v[216:219], v[124:127], v[36:39], v[216:219]\n"
"v_mul_f32 v137, v137, v176\n"
"v_cvt_pk_bf16_f32 v136, v136, v137\n"
"v_mfma_f32_16x16x32_bf16 v[140:143], v[124:127], v[40:43], v[140:143]\n"
"v_mul_f32 v138, v138, v176\n"
"v_mul_f32 v139, v139, v176\n"
"v_mfma_f32_16x16x32_bf16 v[144:147], v[124:127], v[32:35], v[144:147]\n"
"ds_read_b128 v[4:7], v12 offset:4096\n"
"v_cvt_pk_bf16_f32 v137, v138, v139\n"
"v_mul_f32 v128, v128, v176\n"
"v_mul_f32 v129, v129, v176\n"
"v_cvt_pk_bf16_f32 v138, v128, v129\n"
"v_mul_f32 v130, v130, v176\n"
"v_mul_f32 v131, v131, v176\n"
"v_cvt_pk_bf16_f32 v139, v130, v131\n"
"s_waitcnt lgkmcnt(2)\n"
"v_mfma_f32_16x16x32_bf16 v[216:219], v[120:123], v[44:47], v[216:219]\n"
"v_mfma_f32_16x16x32_bf16 v[140:143], v[120:123], v[48:51], v[140:143]\n"
"v_mfma_f32_16x16x32_bf16 v[144:147], v[120:123], v[52:55], v[144:147]\n"
"s_waitcnt lgkmcnt(1)\n"
"ds_read_b128 v[128:131], v13 offset:4096\n"
"v_mfma_f32_16x16x32_bf16 v[216:219], v[190:193], v[56:59], v[216:219]\n"
"v_mfma_f32_16x16x32_bf16 v[140:143], v[190:193], v[60:63], v[140:143]\n"
"v_mfma_f32_16x16x32_bf16 v[144:147], v[190:193], v[64:67], v[144:147]\n"
"s_waitcnt lgkmcnt(1)\n"
"ds_read_b128 v[148:151], v12 offset:6144\n"
"v_mfma_f32_16x16x32_bf16 v[216:219], v[4:7], v[68:71], v[216:219]\n"
"v_mfma_f32_16x16x32_bf16 v[140:143], v[4:7], v[72:75], v[140:143]\n"
"v_mfma_f32_16x16x32_bf16 v[144:147], v[4:7], v[76:79], v[144:147]\n"
"s_waitcnt vmcnt(18)\n"
"s_barrier\n"
"ds_read_b128 v[152:155], v13 offset:6144\n"
"s_waitcnt vmcnt(8)\n"
"ds_read_b64_tr_b16 v[4:5], v11 offset:53248\n"
"ds_read_b64_tr_b16 v[6:7], v11 offset:54016\n"
"s_waitcnt lgkmcnt(4)\n"
"v_mfma_f32_16x16x32_bf16 v[216:219], v[128:131], v[80:83], v[216:219]\n"
"ds_read_b64_tr_b16 v[120:121], v11 offset:53256\n"
"ds_read_b64_tr_b16 v[122:123], v11 offset:54024\n"
"v_mfma_f32_16x16x32_bf16 v[140:143], v[128:131], v[84:87], v[140:143]\n"
"ds_read_b64_tr_b16 v[156:157], v10 offset:53312\n"
"ds_read_b64_tr_b16 v[158:159], v10 offset:54080\n"
"s_waitcnt vmcnt(3)\n"
"ds_read_b64_tr_b16 v[124:125], v11 offset:56320\n"
"ds_read_b64_tr_b16 v[126:127], v11 offset:57088\n"
"v_mfma_f32_16x16x32_bf16 v[144:147], v[128:131], v[88:91], v[144:147]\n"
"s_waitcnt lgkmcnt(9)\n"
"ds_read_b64_tr_b16 v[128:129], v11 offset:56328\n"
"ds_read_b64_tr_b16 v[130:131], v11 offset:57096\n"
"v_mfma_f32_16x16x32_bf16 v[160:163], v[148:151], v[24:27], 0\n"
"ds_read_b64_tr_b16 v[164:165], v10 offset:56384\n"
"ds_read_b64_tr_b16 v[166:167], v10 offset:57152\n"
"v_mfma_f32_16x16x32_bf16 v[8:11], v[148:151], v[28:31], 0\n"
"ds_read_b128 v[168:171], v12 offset:8192\n"
"v_cvt_pk_bf16_f32 v190, v216, v217\n"
"v_mfma_f32_16x16x32_bf16 v[172:175], v[148:151], v[20:23], 0\n"
"s_waitcnt lgkmcnt(13)\n"
"ds_read_b128 v[148:151], v13 offset:8192\n"
"v_cvt_pk_bf16_f32 v191, v218, v219\n"
"v_mfma_f32_16x16x32_bf16 v[160:163], v[152:155], v[36:39], v[160:163]\n"
"ds_write_b128 v17, v[136:139] offset:7680\n"
"v_cvt_pk_bf16_f32 v216, v140, v141\n"
"v_cvt_pk_bf16_f32 v217, v142, v143\n"
"v_mfma_f32_16x16x32_bf16 v[8:11], v[152:155], v[40:43], v[8:11]\n"
"v_cvt_pk_bf16_f32 v242, v144, v145\n"
"v_cvt_pk_bf16_f32 v243, v146, v147\n"
"v_mfma_f32_16x16x32_bf16 v[172:175], v[152:155], v[32:35], v[172:175]\n"
"ds_read_b128 v[136:139], v12 offset:10240\n"
"v_add_u32 v237, s54, v14\n"
"ds_write_b64 v237, v[244:245] offset:0\n"
"s_waitcnt lgkmcnt(4)\n"
"v_mfma_f32_16x16x32_bf16 v[160:163], v[168:171], v[44:47], v[160:163]\n"
"v_mfma_f32_16x16x32_bf16 v[8:11], v[168:171], v[48:51], v[8:11]\n"
"ds_write_b128 v17, v[226:229] offset:53248\n"
"v_mfma_f32_16x16x32_bf16 v[172:175], v[168:171], v[52:55], v[172:175]\n"
"s_waitcnt lgkmcnt(4)\n"
"ds_read_b128 v[226:229], v13 offset:10240\n"
"v_mfma_f32_16x16x32_bf16 v[160:163], v[148:151], v[56:59], v[160:163]\n"
"ds_write_b128 v17, v[132:135] offset:54272\n"
"v_mfma_f32_16x16x32_bf16 v[8:11], v[148:151], v[60:63], v[8:11]\n"
"ds_write_b128 v17, v[200:203] offset:55296\n"
"v_mfma_f32_16x16x32_bf16 v[172:175], v[148:151], v[64:67], v[172:175]\n"
"ds_write_b128 v17, v[252:255] offset:56320\n"
"ds_write_b128 v17, v[156:159] offset:57344\n"
"ds_write_b128 v17, v[164:167] offset:58368\n"
"s_waitcnt lgkmcnt(8)\n"
"v_mfma_f32_16x16x32_bf16 v[160:163], v[136:139], v[68:71], v[160:163]\n"
"v_mfma_f32_16x16x32_bf16 v[8:11], v[136:139], v[72:75], v[8:11]\n"
"v_mfma_f32_16x16x32_bf16 v[172:175], v[136:139], v[76:79], v[172:175]\n"
"s_waitcnt lgkmcnt(5)\n"
"v_mov_b32 v132, 0x3f803f80\n"
"v_mov_b32 v133, 0x3f803f80\n"
"v_mfma_f32_16x16x32_bf16 v[160:163], v[226:229], v[80:83], v[160:163]\n"
"s_waitcnt vmcnt(5)\n"
"s_waitcnt lgkmcnt(0)\n"
"s_barrier\n"
"ds_read_b128 v[136:139], v17 offset:30720\n"
"v_mov_b32 v134, 0x3f803f80\n"
"v_mfma_f32_16x16x32_bf16 v[8:11], v[226:229], v[84:87], v[8:11]\n"
"s_and_b32 s41, 32, s55\n"
"v_add_u32 v245, s41, v15\n"
"v_mov_b32 v135, 0x3f803f80\n"
"v_mfma_f32_16x16x32_bf16 v[172:175], v[226:229], v[88:91], v[172:175]\n"
"ds_read_b128 v[140:143], v17 offset:31744\n"
"v_cvt_pk_bf16_f32 v200, v160, v161\n"
"v_cvt_pk_bf16_f32 v201, v162, v163\n"
"ds_read_b128 v[146:149], v17 offset:32768\n"
"v_cvt_pk_bf16_f32 v226, v8, v9\n"
"v_cvt_pk_bf16_f32 v227, v10, v11\n"
"ds_read_u16 v245, v245 offset:480\n"
"s_waitcnt lgkmcnt(3)\n"
"v_mfma_f32_16x16x32_bf16 v[8:11], v[132:135], v[136:139], 0\n"
"v_cvt_pk_bf16_f32 v252, v172, v173\n"
"v_cvt_pk_bf16_f32 v253, v174, v175\n"
"v_mfma_f32_16x16x32_bf16 v[152:155], v[186:189], v[136:139], 0\n"
"v_mfma_f32_16x16x32_bf16 v[156:159], v[212:215], v[136:139], 0\n"
"v_mfma_f32_16x16x32_bf16 v[160:163], v[238:241], v[136:139], 0\n"
"s_waitcnt lgkmcnt(2)\n"
"v_mfma_f32_16x16x32_bf16 v[8:11], v[132:135], v[140:143], v[8:11]\n"
"s_waitcnt lgkmcnt(0)\n"
"v_mad_u32_u24 v245, v245, s40, v16\n"
"v_mov_b32 v144, 0\n"
"v_mfma_f32_16x16x32_bf16 v[8:11], v[132:135], v[146:149], v[8:11]\n"
"s_add_u32 m0, s51, 102400\n"
"s_nop 0\n"
"global_load_lds_dwordx4 v245, s[56:57]\n"
"v_mfma_f32_16x16x32_bf16 v[160:163], v[250:253], v[140:143], v[160:163]\n"
"v_mov_b32 v145, 0\n"
"v_mov_b32 v150, 0\n"
"v_mov_b32 v151, 0\n"
"v_mfma_f32_16x16x32_bf16 v[160:163], v[240:243], v[144:147], v[160:163]\n"
"s_nop 0\n"
"v_mfma_f32_16x16x32_bf16 v[160:163], v[248:251], v[148:151], v[160:163]\n"
"ds_read_b128 v[132:135], v14 offset:32256\n"
"s_add_u32 m0, s51, 104448\n"
"s_nop 0\n"
"global_load_lds_dwordx4 v245, s[58:59]\n"
"v_mfma_f32_16x16x32_bf16 v[152:155], v[198:201], v[140:143], v[152:155]\n"
"v_rcp_f32 v244, v8\n"
"v_mfma_f32_16x16x32_bf16 v[152:155], v[188:191], v[144:147], v[152:155]\n"
"ds_read_b128 v[136:139], v17 offset:53248\n"
"v_add_u32 v8, 0xd4800000, v8\n"
"v_mfma_f32_16x16x32_bf16 v[152:155], v[196:199], v[148:151], v[152:155]\n"
"ds_read_b128 v[164:167], v14 offset:38400\n"
"v_cmp_lt_u32 vcc, 0x28000000, v8\n"
"v_mfma_f32_16x16x32_bf16 v[156:159], v[224:227], v[140:143], v[156:159]\n"
"s_add_u32 m0, s51, 106496\n"
"s_nop 0\n"
"global_load_lds_dwordx4 v245, s[60:61]\n"
"v_mfma_f32_16x16x32_bf16 v[156:159], v[214:217], v[144:147], v[156:159]\n"
"s_or_b64 s[62:63], s[62:63], vcc\n"
"ds_read_b128 v[8:11], v17 offset:54272\n"
"v_mul_f32 v160, v160, v244\n"
"v_mfma_f32_16x16x32_bf16 v[156:159], v[222:225], v[148:151], v[156:159]\n"
"s_waitcnt lgkmcnt(3)\n"
"ds_read_b128 v[140:143], v14 offset:44544\n"
"v_mul_f32 v161, v161, v244\n"
"v_cvt_pk_bf16_f32 v254, v160, v161\n"
"v_mfma_f32_16x16x32_bf16 v[144:147], v[92:95], v[132:135], 0\n"
"v_mul_f32 v162, v162, v244\n"
"v_mul_f32 v163, v163, v244\n"
"v_mfma_f32_16x16x32_bf16 v[148:151], v[96:99], v[132:135], 0\n"
"s_and_b32 s41, 416, s55\n"
"v_add_u32 v245, s41, v15\n"
"v_cvt_pk_bf16_f32 v255, v162, v163\n"
"s_waitcnt lgkmcnt(3)\n"
"v_mfma_f32_16x16x32_bf16 v[160:163], v[136:139], v[132:135], 0\n"
"s_waitcnt lgkmcnt(2)\n"
"ds_read_b128 v[132:135], v17 offset:55296\n"
"v_mul_f32 v152, v152, v244\n"
"v_mfma_f32_16x16x32_bf16 v[144:147], v[100:103], v[164:167], v[144:147]\n"
"ds_read_u16 v245, v245 offset:544\n"
"v_mul_f32 v153, v153, v244\n"
"v_cvt_pk_bf16_f32 v152, v152, v153\n"
"v_mfma_f32_16x16x32_bf16 v[148:151], v[104:107], v[164:167], v[148:151]\n"
"s_waitcnt lgkmcnt(3)\n"
"ds_read_b128 v[136:139], v14 offset:50688\n"
"v_mul_f32 v154, v154, v244\n"
"v_mfma_f32_16x16x32_bf16 v[160:163], v[8:11], v[164:167], v[160:163]\n"
"s_waitcnt lgkmcnt(3)\n"
"ds_read_b128 v[8:11], v17 offset:56320\n"
"v_mul_f32 v155, v155, v244\n"
"v_cvt_pk_bf16_f32 v153, v154, v155\n"
"v_mfma_f32_16x16x32_bf16 v[144:147], v[0:3], v[140:143], v[144:147]\n"
"ds_read_b128 v[164:167], v14 offset:2048\n"
"v_mul_f32 v156, v156, v244\n"
"v_mfma_f32_16x16x32_bf16 v[148:151], v[108:111], v[140:143], v[148:151]\n"
"ds_read_b128 v[168:171], v17 offset:57344\n"
"v_mul_f32 v157, v157, v244\n"
"v_cvt_pk_bf16_f32 v154, v156, v157\n"
"v_mul_f32 v158, v158, v244\n"
"v_mul_f32 v159, v159, v244\n"
"v_cvt_pk_bf16_f32 v155, v158, v159\n"
"s_waitcnt lgkmcnt(5)\n"
"v_mfma_f32_16x16x32_bf16 v[160:163], v[132:135], v[140:143], v[160:163]\n"
"s_waitcnt lgkmcnt(4)\n"
"ds_read_b128 v[132:135], v14 offset:3072\n"
"v_mad_u32_u24 v245, v245, s40, v16\n"
"s_waitcnt lgkmcnt(4)\n"
"v_mfma_f32_16x16x32_bf16 v[144:147], v[112:115], v[136:139], v[144:147]\n"
"s_add_u32 m0, s51, 114688\n"
"s_nop 0\n"
"global_load_lds_dwordx4 v245, s[56:57]\n"
"v_mfma_f32_16x16x32_bf16 v[148:151], v[116:119], v[136:139], v[148:151]\n"
"s_waitcnt lgkmcnt(3)\n"
"ds_read_b128 v[140:143], v17 offset:58368\n"
"v_mfma_f32_16x16x32_bf16 v[160:163], v[8:11], v[136:139], v[160:163]\n"
"s_waitcnt lgkmcnt(3)\n"
"ds_read_b128 v[8:11], v12 offset:24576\n"
"v_mfma_f32_16x16x32_bf16 v[144:147], v[4:7], v[164:167], v[144:147]\n"
"v_mfma_f32_16x16x32_bf16 v[148:151], v[120:123], v[164:167], v[148:151]\n"
"s_add_u32 m0, s51, 116736\n"
"s_nop 0\n"
"global_load_lds_dwordx4 v245, s[58:59]\n"
"s_waitcnt lgkmcnt(3)\n"
"v_mfma_f32_16x16x32_bf16 v[160:163], v[168:171], v[164:167], v[160:163]\n"
"s_waitcnt lgkmcnt(2)\n"
"ds_read_b128 v[136:139], v13 offset:24576\n"
"v_bfe_u32 v244, v14, 3, 5\n"
"v_mfma_f32_16x16x32_bf16 v[144:147], v[124:127], v[132:135], v[144:147]\n"
"ds_read_u16 v244, v244 offset:800\n"
"v_mfma_f32_16x16x32_bf16 v[148:151], v[128:131], v[132:135], v[148:151]\n"
"s_waitcnt vmcnt(5)\n"
"s_barrier\n"
"s_add_u32 m0, s51, 118784\n"
"s_nop 0\n"
"global_load_lds_dwordx4 v245, s[60:61]\n"
"s_waitcnt lgkmcnt(3)\n"
"v_mfma_f32_16x16x32_bf16 v[160:163], v[140:143], v[132:135], v[160:163]\n"
"s_waitcnt lgkmcnt(2)\n"
"ds_read_b128 v[132:135], v12 offset:26624\n"
"v_mfma_f32_16x16x32_bf16 v[140:143], v[8:11], v[24:27], 0\n"
"ds_write_b128 v17, v[152:155] offset:7680\n"
"v_mfma_f32_16x16x32_bf16 v[152:155], v[8:11], v[28:31], 0\n"
"v_cmp_class_f32_e64 vcc, v144, s64\n"
"v_cvt_pk_bf16_f32 v144, v144, v145\n"
"v_mfma_f32_16x16x32_bf16 v[156:159], v[8:11], v[20:23], 0\n"
"s_waitcnt lgkmcnt(3)\n"
"ds_read_b128 v[8:11], v13 offset:26624\n"
"v_cvt_pk_bf16_f32 v145, v146, v147\n"
"v_mfma_f32_16x16x32_bf16 v[140:143], v[136:139], v[36:39], v[140:143]\n"
"s_or_b64 s[62:63], s[62:63], vcc\n"
"s_waitcnt lgkmcnt(3)\n"
"v_mad_u32_u24 v245, v244, s40, v18\n"
"v_cvt_pk_bf16_f32 v146, v148, v149\n"
"v_mfma_f32_16x16x32_bf16 v[152:155], v[136:139], v[40:43], v[152:155]\n"
"v_cvt_pk_bf16_f32 v147, v150, v151\n"
"v_cvt_pk_bf16_f32 v160, v160, v161\n"
"v_mfma_f32_16x16x32_bf16 v[156:159], v[136:139], v[32:35], v[156:159]\n"
"global_store_dwordx4 v245, v[144:147], s[68:69] nt\n"
"s_waitcnt lgkmcnt(2)\n"
"v_mfma_f32_16x16x32_bf16 v[140:143], v[132:135], v[44:47], v[140:143]\n"
"ds_read_b128 v[136:139], v12 offset:28672\n"
"v_add_u32 v245, s54, v14\n"
"v_mfma_f32_16x16x32_bf16 v[152:155], v[132:135], v[48:51], v[152:155]\n"
"ds_write_b64 v245, v[254:255] offset:0\n"
"v_mfma_f32_16x16x32_bf16 v[156:159], v[132:135], v[52:55], v[156:159]\n"
"s_waitcnt lgkmcnt(2)\n"
"ds_read_b128 v[132:135], v13 offset:28672\n"
"v_mad_u32_u24 v244, v244, s40, v19\n"
"v_mfma_f32_16x16x32_bf16 v[140:143], v[8:11], v[56:59], v[140:143]\n"
"v_cvt_pk_bf16_f32 v161, v162, v163\n"
"v_mfma_f32_16x16x32_bf16 v[152:155], v[8:11], v[60:63], v[152:155]\n"
"global_store_dwordx2 v244, v[160:161], s[70:71] nt\n"
"v_mfma_f32_16x16x32_bf16 v[156:159], v[8:11], v[64:67], v[156:159]\n"
"s_waitcnt lgkmcnt(2)\n"
"ds_read_b128 v[8:11], v12 offset:30720\n"
"v_mfma_f32_16x16x32_bf16 v[140:143], v[136:139], v[68:71], v[140:143]\n"
"v_mfma_f32_16x16x32_bf16 v[152:155], v[136:139], v[72:75], v[152:155]\n"
"v_mfma_f32_16x16x32_bf16 v[156:159], v[136:139], v[76:79], v[156:159]\n"
"s_waitcnt lgkmcnt(1)\n"
"ds_read_b128 v[136:139], v13 offset:30720\n"
"v_mfma_f32_16x16x32_bf16 v[140:143], v[132:135], v[80:83], v[140:143]\n"
"v_mfma_f32_16x16x32_bf16 v[152:155], v[132:135], v[84:87], v[152:155]\n"
"v_mfma_f32_16x16x32_bf16 v[156:159], v[132:135], v[88:91], v[156:159]\n"
"s_waitcnt lgkmcnt(1)\n"
"ds_read_b128 v[132:135], v12 offset:32768\n"
"v_mfma_f32_16x16x32_bf16 v[144:147], v[8:11], v[24:27], 0\n"
"v_mfma_f32_16x16x32_bf16 v[148:151], v[8:11], v[28:31], 0\n"
"v_mfma_f32_16x16x32_bf16 v[160:163], v[8:11], v[20:23], 0\n"
"s_waitcnt lgkmcnt(1)\n"
"ds_read_b128 v[8:11], v13 offset:32768\n"
"v_mfma_f32_16x16x32_bf16 v[144:147], v[136:139], v[36:39], v[144:147]\n"
"v_cvt_pk_bf16_f32 v184, v140, v141\n"
"v_cvt_pk_bf16_f32 v185, v142, v143\n"
"v_cvt_pk_bf16_f32 v210, v152, v153\n"
"v_cvt_pk_bf16_f32 v211, v154, v155\n"
"v_mfma_f32_16x16x32_bf16 v[148:151], v[136:139], v[40:43], v[148:151]\n"
"v_cvt_pk_bf16_f32 v236, v156, v157\n"
"v_cvt_pk_bf16_f32 v237, v158, v159\n"
"v_mfma_f32_16x16x32_bf16 v[160:163], v[136:139], v[32:35], v[160:163]\n"
"s_waitcnt lgkmcnt(1)\n"
"ds_read_b128 v[136:139], v12 offset:34816\n"
"v_mfma_f32_16x16x32_bf16 v[144:147], v[132:135], v[44:47], v[144:147]\n"
"v_mfma_f32_16x16x32_bf16 v[148:151], v[132:135], v[48:51], v[148:151]\n"
"v_mfma_f32_16x16x32_bf16 v[160:163], v[132:135], v[52:55], v[160:163]\n"
"s_waitcnt lgkmcnt(1)\n"
"ds_read_b128 v[132:135], v13 offset:34816\n"
"v_mfma_f32_16x16x32_bf16 v[144:147], v[8:11], v[56:59], v[144:147]\n"
"v_mfma_f32_16x16x32_bf16 v[148:151], v[8:11], v[60:63], v[148:151]\n"
"v_mfma_f32_16x16x32_bf16 v[160:163], v[8:11], v[64:67], v[160:163]\n"
"s_waitcnt lgkmcnt(1)\n"
"v_mfma_f32_16x16x32_bf16 v[144:147], v[136:139], v[68:71], v[144:147]\n"
"v_mfma_f32_16x16x32_bf16 v[148:151], v[136:139], v[72:75], v[148:151]\n"
"v_mfma_f32_16x16x32_bf16 v[160:163], v[136:139], v[76:79], v[160:163]\n"
"s_waitcnt lgkmcnt(0)\n"
"v_mov_b32 v8, 0x3f803f80\n"
"v_mov_b32 v9, 0x3f803f80\n"
"v_mfma_f32_16x16x32_bf16 v[144:147], v[132:135], v[80:83], v[144:147]\n"
"s_barrier\n"
"ds_read_b128 v[136:139], v17 offset:28672\n"
"v_mov_b32 v10, 0x3f803f80\n"
"v_mfma_f32_16x16x32_bf16 v[148:151], v[132:135], v[84:87], v[148:151]\n"
"s_and_b32 s41, 416, s55\n"
"v_add_u32 v255, s41, v15\n"
"v_mov_b32 v11, 0x3f803f80\n"
"v_mfma_f32_16x16x32_bf16 v[160:163], v[132:135], v[88:91], v[160:163]\n"
"ds_read_b128 v[132:135], v17 offset:29696\n"
"v_cvt_pk_bf16_f32 v192, v144, v145\n"
"v_cvt_pk_bf16_f32 v193, v146, v147\n"
"ds_read_u16 v255, v255 offset:576\n"
"v_cvt_pk_bf16_f32 v218, v148, v149\n"
"v_cvt_pk_bf16_f32 v219, v150, v151\n"
"s_waitcnt lgkmcnt(2)\n"
"v_mfma_f32_16x16x32_bf16 v[140:143], v[8:11], v[136:139], 0\n"
"v_mfma_f32_16x16x32_bf16 v[144:147], v[180:183], v[136:139], 0\n"
"ds_read_b128 v[148:151], v17 offset:32768\n"
"v_cvt_pk_bf16_f32 v244, v160, v161\n"
"v_mfma_f32_16x16x32_bf16 v[152:155], v[206:209], v[136:139], 0\n"
"v_cvt_pk_bf16_f32 v245, v162, v163\n"
"v_mfma_f32_16x16x32_bf16 v[156:159], v[232:235], v[136:139], 0\n"
"s_waitcnt lgkmcnt(1)\n"
"v_mad_u32_u24 v255, v255, s40, v16\n"
"v_mfma_f32_16x16x32_bf16 v[140:143], v[8:11], v[132:135], v[140:143]\n"
"s_add_u32 m0, s51, 126976\n"
"s_nop 0\n"
"global_load_lds_dwordx4 v255, s[56:57]\n"
"v_mfma_f32_16x16x32_bf16 v[144:147], v[188:191], v[132:135], v[144:147]\n"
"ds_read_b128 v[136:139], v17 offset:33792\n"
"v_mfma_f32_16x16x32_bf16 v[152:155], v[214:217], v[132:135], v[152:155]\n"
"ds_read_b64 v[162:163], v17 offset:8704\n"
"v_mfma_f32_16x16x32_bf16 v[156:159], v[240:243], v[132:135], v[156:159]\n"
"s_waitcnt lgkmcnt(2)\n"
"v_mfma_f32_16x16x32_bf16 v[140:143], v[8:11], v[148:151], v[140:143]\n"
"s_add_u32 m0, s51, 129024\n"
"s_nop 0\n"
"global_load_lds_dwordx4 v255, s[58:59]\n"
"v_mfma_f32_16x16x32_bf16 v[144:147], v[184:187], v[148:151], v[144:147]\n"
"v_mfma_f32_16x16x32_bf16 v[152:155], v[210:213], v[148:151], v[152:155]\n"
"v_mov_b32 v160, 0\n"
"v_mov_b32 v161, 0\n"
"v_mfma_f32_16x16x32_bf16 v[156:159], v[236:239], v[148:151], v[156:159]\n"
"s_waitcnt lgkmcnt(1)\n"
"v_mfma_f32_16x16x32_bf16 v[140:143], v[8:11], v[136:139], v[140:143]\n"
"s_add_u32 m0, s51, 131072\n"
"s_nop 0\n"
"global_load_lds_dwordx4 v255, s[60:61]\n"
"s_waitcnt lgkmcnt(0)\n"
"v_mfma_f32_16x16x32_bf16 v[140:143], v[8:11], v[160:163], v[140:143]\n"
"ds_read_b128 v[8:11], v14 offset:32256\n"
"v_mfma_f32_16x16x32_bf16 v[156:159], v[248:251], v[136:139], v[156:159]\n"
"v_mfma_f32_16x16x32_bf16 v[156:159], v[250:253], v[160:163], v[156:159]\n"
"ds_read_b128 v[132:135], v17 offset:53248\n"
"v_mfma_f32_16x16x32_bf16 v[144:147], v[196:199], v[136:139], v[144:147]\n"
"ds_read_b128 v[148:151], v14 offset:38400\n"
"v_mfma_f32_16x16x32_bf16 v[144:147], v[198:201], v[160:163], v[144:147]\n"
"v_mfma_f32_16x16x32_bf16 v[152:155], v[222:225], v[136:139], v[152:155]\n"
"ds_read_b128 v[136:139], v17 offset:54272\n"
"v_rcp_f32 v255, v140\n"
"v_add_u32 v140, 0xd4800000, v140\n"
"v_cmp_lt_u32 vcc, 0x28000000, v140\n"
"v_mfma_f32_16x16x32_bf16 v[152:155], v[224:227], v[160:163], v[152:155]\n"
"ds_read_b128 v[140:143], v14 offset:44544\n"
"v_mul_f32 v156, v156, v255\n"
"v_mul_f32 v157, v157, v255\n"
"v_cvt_pk_bf16_f32 v238, v156, v157\n"
"s_waitcnt lgkmcnt(4)\n"
"v_mfma_f32_16x16x32_bf16 v[160:163], v[92:95], v[8:11], 0\n"
"s_or_b64 s[62:63], s[62:63], vcc\n"
"v_mul_f32 v158, v158, v255\n"
"v_mul_f32 v159, v159, v255\n"
"v_mfma_f32_16x16x32_bf16 v[164:167], v[96:99], v[8:11], 0\n"
"s_waitcnt lgkmcnt(3)\n"
"ds_read_b128 v[168:171], v17 offset:55296\n"
"v_cvt_pk_bf16_f32 v239, v158, v159\n"
"v_mfma_f32_16x16x32_bf16 v[156:159], v[132:135], v[8:11], 0\n"
"s_waitcnt lgkmcnt(3)\n"
"ds_read_b128 v[8:11], v14 offset:50688\n"
"v_mul_f32 v144, v144, v255\n"
"v_mfma_f32_16x16x32_bf16 v[160:163], v[100:103], v[148:151], v[160:163]\n"
"v_mul_f32 v145, v145, v255\n"
"v_cvt_pk_bf16_f32 v144, v144, v145\n"
"v_mfma_f32_16x16x32_bf16 v[164:167], v[104:107], v[148:151], v[164:167]\n"
"s_waitcnt lgkmcnt(3)\n"
"ds_read_b128 v[132:135], v17 offset:56320\n"
"v_mul_f32 v146, v146, v255\n"
"v_mfma_f32_16x16x32_bf16 v[156:159], v[136:139], v[148:151], v[156:159]\n"
"s_waitcnt lgkmcnt(3)\n"
"ds_read_b128 v[136:139], v14 offset:2048\n"
"v_mul_f32 v147, v147, v255\n"
"v_cvt_pk_bf16_f32 v145, v146, v147\n"
"v_mfma_f32_16x16x32_bf16 v[160:163], v[0:3], v[140:143], v[160:163]\n"
"v_mul_f32 v152, v152, v255\n"
"v_mul_f32 v153, v153, v255\n"
"v_mfma_f32_16x16x32_bf16 v[164:167], v[108:111], v[140:143], v[164:167]\n"
"s_waitcnt lgkmcnt(3)\n"
"ds_read_b128 v[148:151], v17 offset:57344\n"
"v_cvt_pk_bf16_f32 v146, v152, v153\n"
"v_mul_f32 v154, v154, v255\n"
"v_mfma_f32_16x16x32_bf16 v[156:159], v[168:171], v[140:143], v[156:159]\n"
"s_waitcnt lgkmcnt(3)\n"
"ds_read_b128 v[140:143], v14 offset:3072\n"
"v_mul_f32 v155, v155, v255\n"
"v_mfma_f32_16x16x32_bf16 v[160:163], v[112:115], v[8:11], v[160:163]\n"
"v_cvt_pk_bf16_f32 v147, v154, v155\n"
"v_mfma_f32_16x16x32_bf16 v[164:167], v[116:119], v[8:11], v[164:167]\n"
"s_waitcnt lgkmcnt(3)\n"
"ds_read_b128 v[152:155], v17 offset:58368\n"
"v_mfma_f32_16x16x32_bf16 v[156:159], v[132:135], v[8:11], v[156:159]\n"
"s_waitcnt lgkmcnt(3)\n"
"ds_read_b128 v[8:11], v12 offset:49152\n"
"v_mfma_f32_16x16x32_bf16 v[160:163], v[4:7], v[136:139], v[160:163]\n"
"v_mfma_f32_16x16x32_bf16 v[164:167], v[120:123], v[136:139], v[164:167]\n"
"s_waitcnt lgkmcnt(3)\n"
"v_mfma_f32_16x16x32_bf16 v[156:159], v[148:151], v[136:139], v[156:159]\n"
"s_waitcnt lgkmcnt(2)\n"
"ds_read_b128 v[132:135], v13 offset:49152\n"
"v_bfe_u32 v255, v14, 3, 5\n"
"v_mfma_f32_16x16x32_bf16 v[160:163], v[124:127], v[140:143], v[160:163]\n"
"ds_read_u16 v255, v255 offset:832\n"
"v_mfma_f32_16x16x32_bf16 v[164:167], v[128:131], v[140:143], v[164:167]\n"
"s_waitcnt vmcnt(8)\n"
"s_barrier\n"
"s_waitcnt lgkmcnt(3)\n"
"v_mfma_f32_16x16x32_bf16 v[156:159], v[152:155], v[140:143], v[156:159]\n"
"s_waitcnt lgkmcnt(2)\n"
"ds_read_b128 v[136:139], v12 offset:51200\n"
"v_mfma_f32_16x16x32_bf16 v[140:143], v[8:11], v[24:27], 0\n"
"ds_write_b128 v17, v[144:147] offset:7680\n"
"v_mfma_f32_16x16x32_bf16 v[144:147], v[8:11], v[28:31], 0\n"
"v_cmp_class_f32_e64 vcc, v160, s64\n"
"v_cvt_pk_bf16_f32 v160, v160, v161\n"
"v_mfma_f32_16x16x32_bf16 v[148:151], v[8:11], v[20:23], 0\n"
"s_waitcnt lgkmcnt(3)\n"
"ds_read_b128 v[8:11], v13 offset:51200\n"
"v_cvt_pk_bf16_f32 v161, v162, v163\n"
"v_mfma_f32_16x16x32_bf16 v[140:143], v[132:135], v[36:39], v[140:143]\n"
"s_or_b64 s[62:63], s[62:63], vcc\n"
"v_cvt_pk_bf16_f32 v162, v164, v165\n"
"s_waitcnt lgkmcnt(3)\n"
"v_mad_u32_u24 v254, v255, s40, v18\n"
"v_mfma_f32_16x16x32_bf16 v[144:147], v[132:135], v[40:43], v[144:147]\n"
"v_cvt_pk_bf16_f32 v163, v166, v167\n"
"v_cvt_pk_bf16_f32 v156, v156, v157\n"
"v_mfma_f32_16x16x32_bf16 v[148:151], v[132:135], v[32:35], v[148:151]\n"
"global_store_dwordx4 v254, v[160:163], s[68:69] nt\n"
"s_waitcnt lgkmcnt(2)\n"
"v_mfma_f32_16x16x32_bf16 v[140:143], v[136:139], v[44:47], v[140:143]\n"
"ds_read_b128 v[132:135], v12 offset:53248\n"
"v_add_u32 v254, s54, v14\n"
"v_mfma_f32_16x16x32_bf16 v[144:147], v[136:139], v[48:51], v[144:147]\n"
"ds_write_b64 v254, v[238:239] offset:0\n"
"v_mfma_f32_16x16x32_bf16 v[148:151], v[136:139], v[52:55], v[148:151]\n"
"s_waitcnt lgkmcnt(2)\n"
"ds_read_b128 v[136:139], v13 offset:53248\n"
"v_mad_u32_u24 v255, v255, s40, v19\n"
"v_mfma_f32_16x16x32_bf16 v[140:143], v[8:11], v[56:59], v[140:143]\n"
"v_cvt_pk_bf16_f32 v157, v158, v159\n"
"v_mfma_f32_16x16x32_bf16 v[144:147], v[8:11], v[60:63], v[144:147]\n"
"global_store_dwordx2 v255, v[156:157], s[70:71] nt\n"
"v_mfma_f32_16x16x32_bf16 v[148:151], v[8:11], v[64:67], v[148:151]\n"
"s_waitcnt lgkmcnt(2)\n"
"ds_read_b128 v[8:11], v12 offset:55296\n"
"v_mfma_f32_16x16x32_bf16 v[140:143], v[132:135], v[68:71], v[140:143]\n"
"v_mfma_f32_16x16x32_bf16 v[144:147], v[132:135], v[72:75], v[144:147]\n"
"v_mfma_f32_16x16x32_bf16 v[148:151], v[132:135], v[76:79], v[148:151]\n"
"s_waitcnt lgkmcnt(1)\n"
"ds_read_b128 v[132:135], v13 offset:55296\n"
"v_mfma_f32_16x16x32_bf16 v[140:143], v[136:139], v[80:83], v[140:143]\n"
"v_mfma_f32_16x16x32_bf16 v[144:147], v[136:139], v[84:87], v[144:147]\n"
"v_mfma_f32_16x16x32_bf16 v[148:151], v[136:139], v[88:91], v[148:151]\n"
"s_waitcnt lgkmcnt(1)\n"
"ds_read_b128 v[136:139], v12 offset:57344\n"
"v_mfma_f32_16x16x32_bf16 v[152:155], v[8:11], v[24:27], 0\n"
"v_mfma_f32_16x16x32_bf16 v[156:159], v[8:11], v[28:31], 0\n"
"v_mfma_f32_16x16x32_bf16 v[160:163], v[8:11], v[20:23], 0\n"
"s_waitcnt lgkmcnt(1)\n"
"ds_read_b128 v[8:11], v13 offset:57344\n"
"v_mfma_f32_16x16x32_bf16 v[152:155], v[132:135], v[36:39], v[152:155]\n"
"v_cvt_pk_bf16_f32 v202, v140, v141\n"
"v_cvt_pk_bf16_f32 v203, v142, v143\n"
"v_cvt_pk_bf16_f32 v228, v144, v145\n"
"v_cvt_pk_bf16_f32 v229, v146, v147\n"
"v_mfma_f32_16x16x32_bf16 v[156:159], v[132:135], v[40:43], v[156:159]\n"
"v_cvt_pk_bf16_f32 v254, v148, v149\n"
"v_cvt_pk_bf16_f32 v255, v150, v151\n"
"v_mfma_f32_16x16x32_bf16 v[160:163], v[132:135], v[32:35], v[160:163]\n"
"s_waitcnt lgkmcnt(1)\n"
"ds_read_b128 v[132:135], v12 offset:59392\n"
"v_mfma_f32_16x16x32_bf16 v[152:155], v[136:139], v[44:47], v[152:155]\n"
"v_mfma_f32_16x16x32_bf16 v[156:159], v[136:139], v[48:51], v[156:159]\n"
"v_mfma_f32_16x16x32_bf16 v[160:163], v[136:139], v[52:55], v[160:163]\n"
"s_waitcnt lgkmcnt(1)\n"
"ds_read_b128 v[136:139], v13 offset:59392\n"
"v_mfma_f32_16x16x32_bf16 v[152:155], v[8:11], v[56:59], v[152:155]\n"
"v_mfma_f32_16x16x32_bf16 v[156:159], v[8:11], v[60:63], v[156:159]\n"
"v_mfma_f32_16x16x32_bf16 v[160:163], v[8:11], v[64:67], v[160:163]\n"
"s_waitcnt lgkmcnt(1)\n"
"v_mfma_f32_16x16x32_bf16 v[152:155], v[132:135], v[68:71], v[152:155]\n"
"v_mfma_f32_16x16x32_bf16 v[156:159], v[132:135], v[72:75], v[156:159]\n"
"v_mfma_f32_16x16x32_bf16 v[160:163], v[132:135], v[76:79], v[160:163]\n"
"s_waitcnt lgkmcnt(0)\n"
"v_mov_b32 v8, 0x3f803f80\n"
"v_mov_b32 v9, 0x3f803f80\n"
"v_mfma_f32_16x16x32_bf16 v[152:155], v[136:139], v[80:83], v[152:155]\n"
"s_waitcnt vmcnt(7)\n"
"s_barrier\n"
"ds_read_b128 v[132:135], v17 offset:30720\n"
"v_mov_b32 v10, 0x3f803f80\n"
"v_mfma_f32_16x16x32_bf16 v[156:159], v[136:139], v[84:87], v[156:159]\n"
"s_and_b32 s41, 416, s55\n"
"v_add_u32 v177, s41, v15\n"
"v_mov_b32 v11, 0x3f803f80\n"
"v_mfma_f32_16x16x32_bf16 v[160:163], v[136:139], v[88:91], v[160:163]\n"
"ds_read_b128 v[136:139], v17 offset:31744\n"
"v_cvt_pk_bf16_f32 v186, v152, v153\n"
"v_cvt_pk_bf16_f32 v187, v154, v155\n"
"ds_read_u16 v177, v177 offset:608\n"
"v_cvt_pk_bf16_f32 v212, v156, v157\n"
"v_cvt_pk_bf16_f32 v213, v158, v159\n"
"s_waitcnt lgkmcnt(2)\n"
"v_mfma_f32_16x16x32_bf16 v[140:143], v[8:11], v[132:135], 0\n"
"v_mfma_f32_16x16x32_bf16 v[144:147], v[190:193], v[132:135], 0\n"
"ds_read_b128 v[190:193], v17 offset:32768\n"
"v_cvt_pk_bf16_f32 v238, v160, v161\n"
"v_mfma_f32_16x16x32_bf16 v[148:151], v[216:219], v[132:135], 0\n"
"v_cvt_pk_bf16_f32 v239, v162, v163\n"
"v_mfma_f32_16x16x32_bf16 v[216:219], v[242:245], v[132:135], 0\n"
"s_waitcnt lgkmcnt(1)\n"
"v_mad_u32_u24 v177, v177, s40, v16\n"
"v_mfma_f32_16x16x32_bf16 v[140:143], v[8:11], v[136:139], v[140:143]\n"
"s_add_u32 m0, s51, 139264\n"
"s_nop 0\n"
"global_load_lds_dwordx4 v177, s[56:57]\n"
"v_mfma_f32_16x16x32_bf16 v[144:147], v[182:185], v[136:139], v[144:147]\n"
"ds_read_b128 v[242:245], v17 offset:33792\n"
"v_mfma_f32_16x16x32_bf16 v[148:151], v[208:211], v[136:139], v[148:151]\n"
"ds_read_b64 v[134:135], v17 offset:8712\n"
"v_mfma_f32_16x16x32_bf16 v[216:219], v[234:237], v[136:139], v[216:219]\n"
"s_waitcnt lgkmcnt(2)\n"
"v_mfma_f32_16x16x32_bf16 v[140:143], v[8:11], v[190:193], v[140:143]\n"
"s_add_u32 m0, s51, 141312\n"
"s_nop 0\n"
"global_load_lds_dwordx4 v177, s[58:59]\n"
"v_mfma_f32_16x16x32_bf16 v[144:147], v[186:189], v[190:193], v[144:147]\n"
"v_mfma_f32_16x16x32_bf16 v[148:151], v[212:215], v[190:193], v[148:151]\n"
"v_mov_b32 v132, 0\n"
"v_mov_b32 v133, 0\n"
"v_mfma_f32_16x16x32_bf16 v[216:219], v[238:241], v[190:193], v[216:219]\n"
"s_waitcnt lgkmcnt(1)\n"
"v_mfma_f32_16x16x32_bf16 v[140:143], v[8:11], v[242:245], v[140:143]\n"
"s_add_u32 m0, s51, 143360\n"
"s_nop 0\n"
"global_load_lds_dwordx4 v177, s[60:61]\n"
"s_waitcnt lgkmcnt(0)\n"
"v_mfma_f32_16x16x32_bf16 v[140:143], v[8:11], v[132:135], v[140:143]\n"
"ds_read_b128 v[188:191], v14 offset:32256\n"
"v_mfma_f32_16x16x32_bf16 v[216:219], v[250:253], v[242:245], v[216:219]\n"
"v_mfma_f32_16x16x32_bf16 v[216:219], v[252:255], v[132:135], v[216:219]\n"
"ds_read_b128 v[8:11], v17 offset:53248\n"
"v_mfma_f32_16x16x32_bf16 v[144:147], v[198:201], v[242:245], v[144:147]\n"
"s_andn2_b32 s41, 352, s55\n"
"v_add_u32 v215, s41, v15\n"
"v_mfma_f32_16x16x32_bf16 v[144:147], v[200:203], v[132:135], v[144:147]\n"
"ds_read_b128 v[136:139], v14 offset:38400\n"
"v_rcp_f32 v214, v140\n"
"v_mfma_f32_16x16x32_bf16 v[148:151], v[224:227], v[242:245], v[148:151]\n"
"ds_read_u16 v215, v215 offset:704\n"
"v_add_u32 v140, 0xd4800000, v140\n"
"v_cmp_lt_u32 vcc, 0x28000000, v140\n"
"v_mfma_f32_16x16x32_bf16 v[148:151], v[226:229], v[132:135], v[148:151]\n"
"ds_read_b128 v[240:243], v17 offset:54272\n"
"v_mul_f32 v216, v216, v214\n"
"v_mul_f32 v217, v217, v214\n"
"v_cvt_pk_bf16_f32 v244, v216, v217\n"
"s_waitcnt lgkmcnt(4)\n"
"v_mfma_f32_16x16x32_bf16 v[132:135], v[92:95], v[188:191], 0\n"
"s_or_b64 s[62:63], s[62:63], vcc\n"
"ds_read_b128 v[140:143], v14 offset:44544\n"
"v_mul_f32 v218, v218, v214\n"
"v_mfma_f32_16x16x32_bf16 v[152:155], v[96:99], v[188:191], 0\n"
"s_waitcnt lgkmcnt(4)\n"
"ds_read_b128 v[156:159], v17 offset:55296\n"
"v_mul_f32 v219, v219, v214\n"
"v_cvt_pk_bf16_f32 v245, v218, v219\n"
"v_mfma_f32_16x16x32_bf16 v[216:219], v[8:11], v[188:191], 0\n"
"s_waitcnt lgkmcnt(4)\n"
"ds_read_b128 v[188:191], v14 offset:50688\n"
"v_mul_f32 v144, v144, v214\n"
"v_mul_f32 v145, v145, v214\n"
"v_mfma_f32_16x16x32_bf16 v[132:135], v[100:103], v[136:139], v[132:135]\n"
"v_cvt_pk_bf16_f32 v144, v144, v145\n"
"v_mul_f32 v146, v146, v214\n"
"v_mfma_f32_16x16x32_bf16 v[152:155], v[104:107], v[136:139], v[152:155]\n"
"s_waitcnt lgkmcnt(4)\n"
"ds_read_b128 v[8:11], v17 offset:56320\n"
"v_mad_u32_u24 v215, v215, s40, v16\n"
"v_mul_f32 v147, v147, v214\n"
"v_cvt_pk_bf16_f32 v145, v146, v147\n"
"s_waitcnt lgkmcnt(4)\n"
"v_mfma_f32_16x16x32_bf16 v[216:219], v[240:243], v[136:139], v[216:219]\n"
"s_add_u32 m0, s51, 151552\n"
"s_nop 0\n"
"global_load_lds_dwordx4 v215, s[56:57]\n"
"s_waitcnt lgkmcnt(3)\n"
"v_mfma_f32_16x16x32_bf16 v[132:135], v[0:3], v[140:143], v[132:135]\n"
"ds_read_b128 v[240:243], v14 offset:2048\n"
"v_mul_f32 v148, v148, v214\n"
"v_mfma_f32_16x16x32_bf16 v[152:155], v[108:111], v[140:143], v[152:155]\n"
"s_waitcnt lgkmcnt(3)\n"
"ds_read_b128 v[136:139], v17 offset:57344\n"
"v_mul_f32 v149, v149, v214\n"
"v_cvt_pk_bf16_f32 v146, v148, v149\n"
"v_mfma_f32_16x16x32_bf16 v[216:219], v[156:159], v[140:143], v[216:219]\n"
"s_waitcnt lgkmcnt(3)\n"
"ds_read_b128 v[140:143], v14 offset:3072\n"
"v_mul_f32 v150, v150, v214\n"
"v_mfma_f32_16x16x32_bf16 v[132:135], v[112:115], v[188:191], v[132:135]\n"
"s_add_u32 m0, s51, 153600\n"
"s_nop 0\n"
"global_load_lds_dwordx4 v215, s[58:59]\n"
"v_mul_f32 v151, v151, v214\n"
"v_cvt_pk_bf16_f32 v147, v150, v151\n"
"v_mfma_f32_16x16x32_bf16 v[152:155], v[116:119], v[188:191], v[152:155]\n"
"s_waitcnt lgkmcnt(3)\n"
"ds_read_b128 v[148:151], v17 offset:58368\n"
"v_mfma_f32_16x16x32_bf16 v[216:219], v[8:11], v[188:191], v[216:219]\n"
"ds_read_b128 v[188:191], v12 offset:0\n"
"s_add_u32 m0, s51, 155648\n"
"s_nop 0\n"
"global_load_lds_dwordx4 v215, s[60:61]\n"
"s_waitcnt lgkmcnt(4)\n"
"v_mfma_f32_16x16x32_bf16 v[132:135], v[4:7], v[240:243], v[132:135]\n"
"v_mfma_f32_16x16x32_bf16 v[152:155], v[120:123], v[240:243], v[152:155]\n"
"s_waitcnt lgkmcnt(3)\n"
"v_mfma_f32_16x16x32_bf16 v[216:219], v[136:139], v[240:243], v[216:219]\n"
"s_waitcnt lgkmcnt(2)\n"
"ds_read_b128 v[240:243], v13 offset:0\n"
"v_bfe_u32 v215, v14, 3, 5\n"
"v_mfma_f32_16x16x32_bf16 v[132:135], v[124:127], v[140:143], v[132:135]\n"
"ds_read_u16 v215, v215 offset:864\n"
"v_mfma_f32_16x16x32_bf16 v[152:155], v[128:131], v[140:143], v[152:155]\n"
"s_barrier\n"
"s_waitcnt lgkmcnt(3)\n"
"v_mfma_f32_16x16x32_bf16 v[216:219], v[148:151], v[140:143], v[216:219]\n"
"s_waitcnt lgkmcnt(2)\n"
"ds_read_b128 v[8:11], v12 offset:2048\n"
"v_mfma_f32_16x16x32_bf16 v[136:139], v[188:191], v[24:27], 0\n"
"ds_write_b128 v17, v[144:147] offset:7680\n"
"v_mfma_f32_16x16x32_bf16 v[140:143], v[188:191], v[28:31], 0\n"
"v_cmp_class_f32_e64 vcc, v132, s64\n"
"v_cvt_pk_bf16_f32 v132, v132, v133\n"
"v_mfma_f32_16x16x32_bf16 v[144:147], v[188:191], v[20:23], 0\n"
"s_waitcnt lgkmcnt(3)\n"
"ds_read_b128 v[188:191], v13 offset:2048\n"
"v_cvt_pk_bf16_f32 v133, v134, v135\n"
"v_mfma_f32_16x16x32_bf16 v[136:139], v[240:243], v[36:39], v[136:139]\n"
"s_or_b64 s[62:63], s[62:63], vcc\n"
"v_cvt_pk_bf16_f32 v134, v152, v153\n"
"s_waitcnt lgkmcnt(3)\n"
"v_mad_u32_u24 v214, v215, s40, v18\n"
"v_mfma_f32_16x16x32_bf16 v[140:143], v[240:243], v[40:43], v[140:143]\n"
"v_cvt_pk_bf16_f32 v135, v154, v155\n"
"v_cvt_pk_bf16_f32 v216, v216, v217\n"
"v_mfma_f32_16x16x32_bf16 v[144:147], v[240:243], v[32:35], v[144:147]\n"
"global_store_dwordx4 v214, v[132:135], s[68:69] nt\n"
"s_waitcnt lgkmcnt(2)\n"
"v_mfma_f32_16x16x32_bf16 v[136:139], v[8:11], v[44:47], v[136:139]\n"
"ds_read_b128 v[240:243], v12 offset:4096\n"
"v_add_u32 v217, s54, v14\n"
"v_mfma_f32_16x16x32_bf16 v[140:143], v[8:11], v[48:51], v[140:143]\n"
"ds_write_b64 v217, v[244:245] offset:0\n"
"v_mfma_f32_16x16x32_bf16 v[144:147], v[8:11], v[52:55], v[144:147]\n"
"s_waitcnt lgkmcnt(2)\n"
"ds_read_b128 v[8:11], v13 offset:4096\n"
"v_mad_u32_u24 v215, v215, s40, v19\n"
"v_mfma_f32_16x16x32_bf16 v[136:139], v[188:191], v[56:59], v[136:139]\n"
"v_cvt_pk_bf16_f32 v217, v218, v219\n"
"v_mfma_f32_16x16x32_bf16 v[140:143], v[188:191], v[60:63], v[140:143]\n"
"global_store_dwordx2 v215, v[216:217], s[70:71] nt\n"
"v_mfma_f32_16x16x32_bf16 v[144:147], v[188:191], v[64:67], v[144:147]\n"
"s_waitcnt lgkmcnt(2)\n"
"ds_read_b128 v[188:191], v12 offset:6144\n"
"v_mfma_f32_16x16x32_bf16 v[136:139], v[240:243], v[68:71], v[136:139]\n"
"v_mfma_f32_16x16x32_bf16 v[140:143], v[240:243], v[72:75], v[140:143]\n"
"v_mfma_f32_16x16x32_bf16 v[144:147], v[240:243], v[76:79], v[144:147]\n"
"s_waitcnt lgkmcnt(1)\n"
"ds_read_b128 v[216:219], v13 offset:6144\n"
"v_mfma_f32_16x16x32_bf16 v[136:139], v[8:11], v[80:83], v[136:139]\n"
"v_mfma_f32_16x16x32_bf16 v[140:143], v[8:11], v[84:87], v[140:143]\n"
"v_mfma_f32_16x16x32_bf16 v[144:147], v[8:11], v[88:91], v[144:147]\n"
"s_waitcnt lgkmcnt(1)\n"
"ds_read_b128 v[242:245], v12 offset:8192\n"
"v_mfma_f32_16x16x32_bf16 v[8:11], v[188:191], v[24:27], 0\n"
"v_mfma_f32_16x16x32_bf16 v[132:135], v[188:191], v[28:31], 0\n"
"v_mfma_f32_16x16x32_bf16 v[148:151], v[188:191], v[20:23], 0\n"
"s_waitcnt lgkmcnt(1)\n"
"ds_read_b128 v[190:193], v13 offset:8192\n"
"v_mfma_f32_16x16x32_bf16 v[8:11], v[216:219], v[36:39], v[8:11]\n"
"v_cvt_pk_bf16_f32 v188, v136, v137\n"
"v_cvt_pk_bf16_f32 v189, v138, v139\n"
"v_cvt_pk_bf16_f32 v214, v140, v141\n"
"v_cvt_pk_bf16_f32 v215, v142, v143\n"
"v_mfma_f32_16x16x32_bf16 v[132:135], v[216:219], v[40:43], v[132:135]\n"
"v_cvt_pk_bf16_f32 v240, v144, v145\n"
"v_cvt_pk_bf16_f32 v241, v146, v147\n"
"v_mfma_f32_16x16x32_bf16 v[148:151], v[216:219], v[32:35], v[148:151]\n"
"s_waitcnt lgkmcnt(1)\n"
"ds_read_b128 v[216:219], v12 offset:10240\n"
"v_mfma_f32_16x16x32_bf16 v[8:11], v[242:245], v[44:47], v[8:11]\n"
"v_mfma_f32_16x16x32_bf16 v[132:135], v[242:245], v[48:51], v[132:135]\n"
"v_mfma_f32_16x16x32_bf16 v[148:151], v[242:245], v[52:55], v[148:151]\n"
"s_waitcnt lgkmcnt(1)\n"
"ds_read_b128 v[242:245], v13 offset:10240\n"
"v_mfma_f32_16x16x32_bf16 v[8:11], v[190:193], v[56:59], v[8:11]\n"
"v_mfma_f32_16x16x32_bf16 v[132:135], v[190:193], v[60:63], v[132:135]\n"
"v_mfma_f32_16x16x32_bf16 v[148:151], v[190:193], v[64:67], v[148:151]\n"
"s_waitcnt lgkmcnt(1)\n"
"v_mfma_f32_16x16x32_bf16 v[8:11], v[216:219], v[68:71], v[8:11]\n"
"v_mfma_f32_16x16x32_bf16 v[132:135], v[216:219], v[72:75], v[132:135]\n"
"v_mfma_f32_16x16x32_bf16 v[148:151], v[216:219], v[76:79], v[148:151]\n"
"s_waitcnt lgkmcnt(0)\n"
"v_mov_b32 v136, 0x3f803f80\n"
"v_mov_b32 v137, 0x3f803f80\n"
"v_mfma_f32_16x16x32_bf16 v[8:11], v[242:245], v[80:83], v[8:11]\n"
"s_waitcnt vmcnt(10)\n"
"s_barrier\n"
"ds_read_b128 v[140:143], v17 offset:28672\n"
"v_mov_b32 v138, 0x3f803f80\n"
"v_mfma_f32_16x16x32_bf16 v[132:135], v[242:245], v[84:87], v[132:135]\n"
"s_and_b32 s41, 32, s55\n"
"v_add_u32 v193, s41, v15\n"
"v_mov_b32 v139, 0x3f803f80\n"
"v_mfma_f32_16x16x32_bf16 v[148:151], v[242:245], v[88:91], v[148:151]\n"
"ds_read_b128 v[144:147], v17 offset:29696\n"
"v_cvt_pk_bf16_f32 v190, v8, v9\n"
"v_cvt_pk_bf16_f32 v191, v10, v11\n"
"ds_read_b128 v[154:157], v17 offset:32768\n"
"v_cvt_pk_bf16_f32 v216, v132, v133\n"
"v_cvt_pk_bf16_f32 v217, v134, v135\n"
"ds_read_u16 v193, v193 offset:640\n"
"s_waitcnt lgkmcnt(3)\n"
"v_mfma_f32_16x16x32_bf16 v[8:11], v[136:139], v[140:143], 0\n"
"v_cvt_pk_bf16_f32 v242, v148, v149\n"
"v_cvt_pk_bf16_f32 v243, v150, v151\n"
"v_mfma_f32_16x16x32_bf16 v[132:135], v[178:181], v[140:143], 0\n"
"v_mfma_f32_16x16x32_bf16 v[148:151], v[204:207], v[140:143], 0\n"
"v_mfma_f32_16x16x32_bf16 v[160:163], v[230:233], v[140:143], 0\n"
"s_waitcnt lgkmcnt(2)\n"
"v_mfma_f32_16x16x32_bf16 v[8:11], v[136:139], v[144:147], v[8:11]\n"
"s_waitcnt lgkmcnt(0)\n"
"v_mad_u32_u24 v193, v193, s40, v16\n"
"v_mov_b32 v152, 0\n"
"v_mfma_f32_16x16x32_bf16 v[8:11], v[136:139], v[154:157], v[8:11]\n"
"s_add_u32 m0, s51, 102400\n"
"s_nop 0\n"
"global_load_lds_dwordx4 v193, s[56:57]\n"
"v_mfma_f32_16x16x32_bf16 v[160:163], v[246:249], v[144:147], v[160:163]\n"
"v_mov_b32 v153, 0\n"
"v_mov_b32 v158, 0\n"
"v_mov_b32 v159, 0\n"
"v_mfma_f32_16x16x32_bf16 v[160:163], v[232:235], v[152:155], v[160:163]\n"
"s_nop 0\n"
"v_mfma_f32_16x16x32_bf16 v[160:163], v[250:253], v[156:159], v[160:163]\n"
"ds_read_b128 v[136:139], v14 offset:32256\n"
"s_add_u32 m0, s51, 104448\n"
"s_nop 0\n"
"global_load_lds_dwordx4 v193, s[58:59]\n"
"v_mfma_f32_16x16x32_bf16 v[132:135], v[194:197], v[144:147], v[132:135]\n"
"v_rcp_f32 v192, v8\n"
"v_mfma_f32_16x16x32_bf16 v[132:135], v[180:183], v[152:155], v[132:135]\n"
"ds_read_b128 v[140:143], v17 offset:53248\n"
"v_add_u32 v8, 0xd4800000, v8\n"
"v_mfma_f32_16x16x32_bf16 v[132:135], v[198:201], v[156:159], v[132:135]\n"
"ds_read_b128 v[164:167], v14 offset:38400\n"
"v_cmp_lt_u32 vcc, 0x28000000, v8\n"
"v_mfma_f32_16x16x32_bf16 v[148:151], v[220:223], v[144:147], v[148:151]\n"
"s_add_u32 m0, s51, 106496\n"
"s_nop 0\n"
"global_load_lds_dwordx4 v193, s[60:61]\n"
"v_mfma_f32_16x16x32_bf16 v[148:151], v[206:209], v[152:155], v[148:151]\n"
"s_or_b64 s[62:63], s[62:63], vcc\n"
"ds_read_b128 v[8:11], v17 offset:54272\n"
"v_mul_f32 v160, v160, v192\n"
"v_mfma_f32_16x16x32_bf16 v[148:151], v[224:227], v[156:159], v[148:151]\n"
"s_waitcnt lgkmcnt(3)\n"
"ds_read_b128 v[144:147], v14 offset:44544\n"
"v_mul_f32 v161, v161, v192\n"
"v_cvt_pk_bf16_f32 v244, v160, v161\n"
"v_mfma_f32_16x16x32_bf16 v[152:155], v[92:95], v[136:139], 0\n"
"v_mul_f32 v162, v162, v192\n"
"v_mul_f32 v163, v163, v192\n"
"v_mfma_f32_16x16x32_bf16 v[156:159], v[96:99], v[136:139], 0\n"
"s_waitcnt lgkmcnt(3)\n"
"ds_read_b128 v[168:171], v17 offset:55296\n"
"v_cvt_pk_bf16_f32 v245, v162, v163\n"
"v_mfma_f32_16x16x32_bf16 v[160:163], v[140:143], v[136:139], 0\n"
"s_waitcnt lgkmcnt(3)\n"
"ds_read_b128 v[136:139], v14 offset:50688\n"
"v_mul_f32 v132, v132, v192\n"
"v_mfma_f32_16x16x32_bf16 v[152:155], v[100:103], v[164:167], v[152:155]\n"
"v_mul_f32 v133, v133, v192\n"
"v_cvt_pk_bf16_f32 v132, v132, v133\n"
"v_mfma_f32_16x16x32_bf16 v[156:159], v[104:107], v[164:167], v[156:159]\n"
"s_waitcnt lgkmcnt(3)\n"
"ds_read_b128 v[140:143], v17 offset:56320\n"
"v_mul_f32 v134, v134, v192\n"
"v_mul_f32 v135, v135, v192\n"
"v_mfma_f32_16x16x32_bf16 v[160:163], v[8:11], v[164:167], v[160:163]\n"
"s_waitcnt lgkmcnt(3)\n"
"ds_read_b128 v[8:11], v14 offset:2048\n"
"v_cvt_pk_bf16_f32 v133, v134, v135\n"
"v_mfma_f32_16x16x32_bf16 v[152:155], v[0:3], v[144:147], v[152:155]\n"
"v_mul_f32 v148, v148, v192\n"
"v_mul_f32 v149, v149, v192\n"
"v_mfma_f32_16x16x32_bf16 v[156:159], v[108:111], v[144:147], v[156:159]\n"
"s_waitcnt lgkmcnt(3)\n"
"ds_read_b128 v[164:167], v17 offset:57344\n"
"v_cvt_pk_bf16_f32 v134, v148, v149\n"
"v_mul_f32 v150, v150, v192\n"
"v_mfma_f32_16x16x32_bf16 v[160:163], v[168:171], v[144:147], v[160:163]\n"
"s_waitcnt lgkmcnt(3)\n"
"ds_read_b128 v[144:147], v14 offset:3072\n"
"v_mul_f32 v151, v151, v192\n"
"v_mfma_f32_16x16x32_bf16 v[152:155], v[112:115], v[136:139], v[152:155]\n"
"v_cvt_pk_bf16_f32 v135, v150, v151\n"
"v_mfma_f32_16x16x32_bf16 v[156:159], v[116:119], v[136:139], v[156:159]\n"
"s_waitcnt lgkmcnt(3)\n"
"ds_read_b128 v[148:151], v17 offset:58368\n"
"v_mfma_f32_16x16x32_bf16 v[160:163], v[140:143], v[136:139], v[160:163]\n"
"s_waitcnt lgkmcnt(3)\n"
"ds_read_b128 v[136:139], v12 offset:12288\n"
"v_mfma_f32_16x16x32_bf16 v[152:155], v[4:7], v[8:11], v[152:155]\n"
"v_mfma_f32_16x16x32_bf16 v[156:159], v[120:123], v[8:11], v[156:159]\n"
"s_waitcnt lgkmcnt(3)\n"
"v_mfma_f32_16x16x32_bf16 v[160:163], v[164:167], v[8:11], v[160:163]\n"
"s_waitcnt lgkmcnt(2)\n"
"ds_read_b128 v[8:11], v13 offset:12288\n"
"v_bfe_u32 v219, v14, 3, 5\n"
"v_mfma_f32_16x16x32_bf16 v[152:155], v[124:127], v[144:147], v[152:155]\n"
"ds_read_u16 v219, v219 offset:896\n"
"v_mfma_f32_16x16x32_bf16 v[156:159], v[128:131], v[144:147], v[156:159]\n"
"s_barrier\n"
"s_waitcnt lgkmcnt(3)\n"
"v_mfma_f32_16x16x32_bf16 v[160:163], v[148:151], v[144:147], v[160:163]\n"
"s_waitcnt lgkmcnt(2)\n"
"ds_read_b128 v[140:143], v12 offset:14336\n"
"v_mfma_f32_16x16x32_bf16 v[144:147], v[136:139], v[24:27], 0\n"
"ds_write_b128 v17, v[132:135] offset:7680\n"
"v_mfma_f32_16x16x32_bf16 v[132:135], v[136:139], v[28:31], 0\n"
"v_cmp_class_f32_e64 vcc, v152, s64\n"
"v_cvt_pk_bf16_f32 v152, v152, v153\n"
"v_mfma_f32_16x16x32_bf16 v[148:151], v[136:139], v[20:23], 0\n"
"s_waitcnt lgkmcnt(3)\n"
"ds_read_b128 v[136:139], v13 offset:14336\n"
"v_cvt_pk_bf16_f32 v153, v154, v155\n"
"v_mfma_f32_16x16x32_bf16 v[144:147], v[8:11], v[36:39], v[144:147]\n"
"s_or_b64 s[62:63], s[62:63], vcc\n"
"v_cvt_pk_bf16_f32 v154, v156, v157\n"
"s_waitcnt lgkmcnt(3)\n"
"v_mad_u32_u24 v218, v219, s40, v18\n"
"v_mfma_f32_16x16x32_bf16 v[132:135], v[8:11], v[40:43], v[132:135]\n"
"v_cvt_pk_bf16_f32 v155, v158, v159\n"
"v_cvt_pk_bf16_f32 v160, v160, v161\n"
"v_mfma_f32_16x16x32_bf16 v[148:151], v[8:11], v[32:35], v[148:151]\n"
"global_store_dwordx4 v218, v[152:155], s[68:69] nt\n"
"s_waitcnt lgkmcnt(2)\n"
"v_mfma_f32_16x16x32_bf16 v[144:147], v[140:143], v[44:47], v[144:147]\n"
"ds_read_b128 v[8:11], v12 offset:16384\n"
"v_add_u32 v218, s54, v14\n"
"v_mfma_f32_16x16x32_bf16 v[132:135], v[140:143], v[48:51], v[132:135]\n"
"ds_write_b64 v218, v[244:245] offset:0\n"
"v_mfma_f32_16x16x32_bf16 v[148:151], v[140:143], v[52:55], v[148:151]\n"
"s_waitcnt lgkmcnt(2)\n"
"ds_read_b128 v[140:143], v13 offset:16384\n"
"v_mad_u32_u24 v219, v219, s40, v19\n"
"v_mfma_f32_16x16x32_bf16 v[144:147], v[136:139], v[56:59], v[144:147]\n"
"v_cvt_pk_bf16_f32 v161, v162, v163\n"
"v_mfma_f32_16x16x32_bf16 v[132:135], v[136:139], v[60:63], v[132:135]\n"
"global_store_dwordx2 v219, v[160:161], s[70:71] nt\n"
"v_mfma_f32_16x16x32_bf16 v[148:151], v[136:139], v[64:67], v[148:151]\n"
"s_waitcnt lgkmcnt(2)\n"
"v_mfma_f32_16x16x32_bf16 v[144:147], v[8:11], v[68:71], v[144:147]\n"
"v_mfma_f32_16x16x32_bf16 v[132:135], v[8:11], v[72:75], v[132:135]\n"
"v_mfma_f32_16x16x32_bf16 v[148:151], v[8:11], v[76:79], v[148:151]\n"
"s_waitcnt lgkmcnt(0)\n"
"v_mov_b32 v8, 0x3f803f80\n"
"v_mov_b32 v9, 0x3f803f80\n"
"v_mfma_f32_16x16x32_bf16 v[144:147], v[140:143], v[80:83], v[144:147]\n"
"s_waitcnt vmcnt(10)\n"
"s_barrier\n"
"ds_read_b128 v[136:139], v17 offset:30720\n"
"v_mov_b32 v10, 0x3f803f80\n"
"v_mfma_f32_16x16x32_bf16 v[132:135], v[140:143], v[84:87], v[132:135]\n"
"s_andn2_b32 s41, 352, s55\n"
"v_add_u32 v177, s41, v15\n"
"v_mov_b32 v11, 0x3f803f80\n"
"v_mfma_f32_16x16x32_bf16 v[148:151], v[140:143], v[88:91], v[148:151]\n"
"ds_read_b128 v[140:143], v17 offset:31744\n"
"v_cvt_pk_bf16_f32 v192, v144, v145\n"
"v_cvt_pk_bf16_f32 v193, v146, v147\n"
"ds_read_u16 v177, v177 offset:736\n"
"v_cvt_pk_bf16_f32 v218, v132, v133\n"
"v_cvt_pk_bf16_f32 v219, v134, v135\n"
"s_waitcnt lgkmcnt(2)\n"
"v_mfma_f32_16x16x32_bf16 v[132:135], v[8:11], v[136:139], 0\n"
"v_mfma_f32_16x16x32_bf16 v[144:147], v[178:181], v[136:139], 0\n"
"ds_read_b128 v[152:155], v17 offset:32768\n"
"v_cvt_pk_bf16_f32 v244, v148, v149\n"
"v_mfma_f32_16x16x32_bf16 v[156:159], v[204:207], v[136:139], 0\n"
"v_cvt_pk_bf16_f32 v245, v150, v151\n"
"v_mfma_f32_16x16x32_bf16 v[148:151], v[230:233], v[136:139], 0\n"
"s_waitcnt lgkmcnt(1)\n"
"v_mad_u32_u24 v177, v177, s40, v16\n"
"v_mfma_f32_16x16x32_bf16 v[132:135], v[8:11], v[140:143], v[132:135]\n"
"s_add_u32 m0, s51, 114688\n"
"s_nop 0\n"
"global_load_lds_dwordx4 v177, s[56:57]\n"
"v_mfma_f32_16x16x32_bf16 v[144:147], v[188:191], v[140:143], v[144:147]\n"
"ds_read_b128 v[136:139], v17 offset:33792\n"
"v_mfma_f32_16x16x32_bf16 v[156:159], v[214:217], v[140:143], v[156:159]\n"
"ds_read_b64 v[162:163], v17 offset:8712\n"
"v_mfma_f32_16x16x32_bf16 v[148:151], v[240:243], v[140:143], v[148:151]\n"
"s_waitcnt lgkmcnt(2)\n"
"v_mfma_f32_16x16x32_bf16 v[132:135], v[8:11], v[152:155], v[132:135]\n"
"s_add_u32 m0, s51, 116736\n"
"s_nop 0\n"
"global_load_lds_dwordx4 v177, s[58:59]\n"
"v_mfma_f32_16x16x32_bf16 v[144:147], v[192:195], v[152:155], v[144:147]\n"
"v_mfma_f32_16x16x32_bf16 v[156:159], v[218:221], v[152:155], v[156:159]\n"
"v_mov_b32 v160, 0\n"
"v_mov_b32 v161, 0\n"
"v_mfma_f32_16x16x32_bf16 v[148:151], v[244:247], v[152:155], v[148:151]\n"
"s_waitcnt lgkmcnt(1)\n"
"v_mfma_f32_16x16x32_bf16 v[132:135], v[8:11], v[136:139], v[132:135]\n"
"s_add_u32 m0, s51, 118784\n"
"s_nop 0\n"
"global_load_lds_dwordx4 v177, s[60:61]\n"
"s_waitcnt lgkmcnt(0)\n"
"v_mfma_f32_16x16x32_bf16 v[132:135], v[8:11], v[160:163], v[132:135]\n"
"ds_read_b128 v[8:11], v14 offset:32256\n"
"v_mfma_f32_16x16x32_bf16 v[148:151], v[248:251], v[136:139], v[148:151]\n"
"v_mfma_f32_16x16x32_bf16 v[148:151], v[232:235], v[160:163], v[148:151]\n"
"ds_read_b128 v[140:143], v17 offset:53248\n"
"v_mfma_f32_16x16x32_bf16 v[144:147], v[196:199], v[136:139], v[144:147]\n"
"ds_read_b128 v[152:155], v14 offset:38400\n"
"v_mfma_f32_16x16x32_bf16 v[144:147], v[180:183], v[160:163], v[144:147]\n"
"v_mfma_f32_16x16x32_bf16 v[156:159], v[222:225], v[136:139], v[156:159]\n"
"ds_read_b128 v[136:139], v17 offset:54272\n"
"v_rcp_f32 v247, v132\n"
"v_add_u32 v132, 0xd4800000, v132\n"
"v_cmp_lt_u32 vcc, 0x28000000, v132\n"
"v_mfma_f32_16x16x32_bf16 v[156:159], v[206:209], v[160:163], v[156:159]\n"
"ds_read_b128 v[132:135], v14 offset:44544\n"
"v_mul_f32 v148, v148, v247\n"
"v_mul_f32 v149, v149, v247\n"
"v_cvt_pk_bf16_f32 v220, v148, v149\n"
"s_waitcnt lgkmcnt(4)\n"
"v_mfma_f32_16x16x32_bf16 v[160:163], v[92:95], v[8:11], 0\n"
"s_or_b64 s[62:63], s[62:63], vcc\n"
"v_mul_f32 v150, v150, v247\n"
"v_mul_f32 v151, v151, v247\n"
"v_mfma_f32_16x16x32_bf16 v[164:167], v[96:99], v[8:11], 0\n"
"s_waitcnt lgkmcnt(3)\n"
"ds_read_b128 v[168:171], v17 offset:55296\n"
"v_cvt_pk_bf16_f32 v221, v150, v151\n"
"v_mfma_f32_16x16x32_bf16 v[148:151], v[140:143], v[8:11], 0\n"
"s_waitcnt lgkmcnt(3)\n"
"ds_read_b128 v[8:11], v14 offset:50688\n"
"v_mul_f32 v144, v144, v247\n"
"v_mfma_f32_16x16x32_bf16 v[160:163], v[100:103], v[152:155], v[160:163]\n"
"v_mul_f32 v145, v145, v247\n"
"v_cvt_pk_bf16_f32 v144, v144, v145\n"
"v_mfma_f32_16x16x32_bf16 v[164:167], v[104:107], v[152:155], v[164:167]\n"
"s_waitcnt lgkmcnt(3)\n"
"ds_read_b128 v[140:143], v17 offset:56320\n"
"v_mul_f32 v146, v146, v247\n"
"v_mfma_f32_16x16x32_bf16 v[148:151], v[136:139], v[152:155], v[148:151]\n"
"s_waitcnt lgkmcnt(3)\n"
"ds_read_b128 v[136:139], v14 offset:2048\n"
"v_mul_f32 v147, v147, v247\n"
"v_cvt_pk_bf16_f32 v145, v146, v147\n"
"v_mfma_f32_16x16x32_bf16 v[160:163], v[0:3], v[132:135], v[160:163]\n"
"v_mul_f32 v156, v156, v247\n"
"v_mul_f32 v157, v157, v247\n"
"v_mfma_f32_16x16x32_bf16 v[164:167], v[108:111], v[132:135], v[164:167]\n"
"s_waitcnt lgkmcnt(3)\n"
"ds_read_b128 v[152:155], v17 offset:57344\n"
"v_cvt_pk_bf16_f32 v146, v156, v157\n"
"v_mul_f32 v158, v158, v247\n"
"v_mfma_f32_16x16x32_bf16 v[148:151], v[168:171], v[132:135], v[148:151]\n"
"s_waitcnt lgkmcnt(3)\n"
"ds_read_b128 v[132:135], v14 offset:3072\n"
"v_mul_f32 v159, v159, v247\n"
"v_mfma_f32_16x16x32_bf16 v[160:163], v[112:115], v[8:11], v[160:163]\n"
"v_cvt_pk_bf16_f32 v147, v158, v159\n"
"v_mfma_f32_16x16x32_bf16 v[164:167], v[116:119], v[8:11], v[164:167]\n"
"s_waitcnt lgkmcnt(3)\n"
"ds_read_b128 v[156:159], v17 offset:58368\n"
"v_mfma_f32_16x16x32_bf16 v[148:151], v[140:143], v[8:11], v[148:151]\n"
"s_waitcnt lgkmcnt(3)\n"
"ds_read_b128 v[8:11], v12 offset:24576\n"
"v_mfma_f32_16x16x32_bf16 v[160:163], v[4:7], v[136:139], v[160:163]\n"
"v_mfma_f32_16x16x32_bf16 v[164:167], v[120:123], v[136:139], v[164:167]\n"
"s_waitcnt lgkmcnt(3)\n"
"v_mfma_f32_16x16x32_bf16 v[148:151], v[152:155], v[136:139], v[148:151]\n"
"s_waitcnt lgkmcnt(2)\n"
"ds_read_b128 v[136:139], v13 offset:24576\n"
"v_bfe_u32 v247, v14, 3, 5\n"
"v_mfma_f32_16x16x32_bf16 v[160:163], v[124:127], v[132:135], v[160:163]\n"
"ds_read_u16 v247, v247 offset:928\n"
"v_mfma_f32_16x16x32_bf16 v[164:167], v[128:131], v[132:135], v[164:167]\n"
"s_barrier\n"
"s_waitcnt lgkmcnt(3)\n"
"v_mfma_f32_16x16x32_bf16 v[148:151], v[156:159], v[132:135], v[148:151]\n"
"s_waitcnt lgkmcnt(2)\n"
"ds_read_b128 v[132:135], v12 offset:26624\n"
"v_mfma_f32_16x16x32_bf16 v[140:143], v[8:11], v[24:27], 0\n"
"ds_write_b128 v17, v[144:147] offset:7680\n"
"v_mfma_f32_16x16x32_bf16 v[144:147], v[8:11], v[28:31], 0\n"
"v_cmp_class_f32_e64 vcc, v160, s64\n"
"v_cvt_pk_bf16_f32 v160, v160, v161\n"
"v_mfma_f32_16x16x32_bf16 v[152:155], v[8:11], v[20:23], 0\n"
"s_waitcnt lgkmcnt(3)\n"
"ds_read_b128 v[8:11], v13 offset:26624\n"
"v_cvt_pk_bf16_f32 v161, v162, v163\n"
"v_mfma_f32_16x16x32_bf16 v[140:143], v[136:139], v[36:39], v[140:143]\n"
"s_or_b64 s[62:63], s[62:63], vcc\n"
"v_cvt_pk_bf16_f32 v162, v164, v165\n"
"s_waitcnt lgkmcnt(3)\n"
"v_mad_u32_u24 v246, v247, s40, v18\n"
"v_mfma_f32_16x16x32_bf16 v[144:147], v[136:139], v[40:43], v[144:147]\n"
"v_cvt_pk_bf16_f32 v163, v166, v167\n"
"v_cvt_pk_bf16_f32 v148, v148, v149\n"
"v_mfma_f32_16x16x32_bf16 v[152:155], v[136:139], v[32:35], v[152:155]\n"
"global_store_dwordx4 v246, v[160:163], s[68:69] nt\n"
"s_waitcnt lgkmcnt(2)\n"
"v_mfma_f32_16x16x32_bf16 v[140:143], v[132:135], v[44:47], v[140:143]\n"
"ds_read_b128 v[136:139], v12 offset:28672\n"
"v_add_u32 v246, s54, v14\n"
"v_mfma_f32_16x16x32_bf16 v[144:147], v[132:135], v[48:51], v[144:147]\n"
"ds_write_b64 v246, v[220:221] offset:0\n"
"v_mfma_f32_16x16x32_bf16 v[152:155], v[132:135], v[52:55], v[152:155]\n"
"s_waitcnt lgkmcnt(2)\n"
"ds_read_b128 v[132:135], v13 offset:28672\n"
"v_mad_u32_u24 v247, v247, s40, v19\n"
"v_mfma_f32_16x16x32_bf16 v[140:143], v[8:11], v[56:59], v[140:143]\n"
"v_cvt_pk_bf16_f32 v149, v150, v151\n"
"v_mfma_f32_16x16x32_bf16 v[144:147], v[8:11], v[60:63], v[144:147]\n"
"global_store_dwordx2 v247, v[148:149], s[70:71] nt\n"
"v_mfma_f32_16x16x32_bf16 v[152:155], v[8:11], v[64:67], v[152:155]\n"
"s_waitcnt lgkmcnt(2)\n"
"v_mfma_f32_16x16x32_bf16 v[140:143], v[136:139], v[68:71], v[140:143]\n"
"v_mfma_f32_16x16x32_bf16 v[144:147], v[136:139], v[72:75], v[144:147]\n"
"v_mfma_f32_16x16x32_bf16 v[152:155], v[136:139], v[76:79], v[152:155]\n"
"s_waitcnt lgkmcnt(0)\n"
"v_mov_b32 v8, 0x3f803f80\n"
"v_mov_b32 v9, 0x3f803f80\n"
"v_mfma_f32_16x16x32_bf16 v[140:143], v[132:135], v[80:83], v[140:143]\n"
"s_waitcnt vmcnt(12)\n"
"s_barrier\n"
"ds_read_b128 v[136:139], v17 offset:28672\n"
"v_mov_b32 v10, 0x3f803f80\n"
"v_mfma_f32_16x16x32_bf16 v[144:147], v[132:135], v[84:87], v[144:147]\n"
"s_andn2_b32 s41, 352, s55\n"
"v_add_u32 v177, s41, v15\n"
"v_mov_b32 v11, 0x3f803f80\n"
"v_mfma_f32_16x16x32_bf16 v[152:155], v[132:135], v[88:91], v[152:155]\n"
"ds_read_b128 v[132:135], v17 offset:29696\n"
"v_cvt_pk_bf16_f32 v194, v140, v141\n"
"v_cvt_pk_bf16_f32 v195, v142, v143\n"
"ds_read_u16 v177, v177 offset:768\n"
"v_cvt_pk_bf16_f32 v220, v144, v145\n"
"v_cvt_pk_bf16_f32 v221, v146, v147\n"
"s_waitcnt lgkmcnt(2)\n"
"v_mfma_f32_16x16x32_bf16 v[140:143], v[8:11], v[136:139], 0\n"
"v_mfma_f32_16x16x32_bf16 v[144:147], v[180:183], v[136:139], 0\n"
"ds_read_b128 v[148:151], v17 offset:32768\n"
"v_cvt_pk_bf16_f32 v246, v152, v153\n"
"v_mfma_f32_16x16x32_bf16 v[156:159], v[206:209], v[136:139], 0\n"
"v_cvt_pk_bf16_f32 v247, v154, v155\n"
"v_mfma_f32_16x16x32_bf16 v[152:155], v[232:235], v[136:139], 0\n"
"s_waitcnt lgkmcnt(1)\n"
"v_mad_u32_u24 v177, v177, s40, v16\n"
"v_mfma_f32_16x16x32_bf16 v[140:143], v[8:11], v[132:135], v[140:143]\n"
"s_add_u32 m0, s51, 126976\n"
"s_nop 0\n"
"global_load_lds_dwordx4 v177, s[56:57]\n"
"v_mfma_f32_16x16x32_bf16 v[144:147], v[190:193], v[132:135], v[144:147]\n"
"ds_read_b128 v[136:139], v17 offset:33792\n"
"v_mfma_f32_16x16x32_bf16 v[156:159], v[216:219], v[132:135], v[156:159]\n"
"ds_read_b64 v[162:163], v17 offset:8704\n"
"v_mfma_f32_16x16x32_bf16 v[152:155], v[242:245], v[132:135], v[152:155]\n"
"s_waitcnt lgkmcnt(2)\n"
"v_mfma_f32_16x16x32_bf16 v[140:143], v[8:11], v[148:151], v[140:143]\n"
"s_add_u32 m0, s51, 129024\n"
"s_nop 0\n"
"global_load_lds_dwordx4 v177, s[58:59]\n"
"v_mfma_f32_16x16x32_bf16 v[144:147], v[194:197], v[148:151], v[144:147]\n"
"v_mfma_f32_16x16x32_bf16 v[156:159], v[220:223], v[148:151], v[156:159]\n"
"v_mov_b32 v160, 0\n"
"v_mov_b32 v161, 0\n"
"v_mfma_f32_16x16x32_bf16 v[152:155], v[246:249], v[148:151], v[152:155]\n"
"s_waitcnt lgkmcnt(1)\n"
"v_mfma_f32_16x16x32_bf16 v[140:143], v[8:11], v[136:139], v[140:143]\n"
"s_add_u32 m0, s51, 131072\n"
"s_nop 0\n"
"global_load_lds_dwordx4 v177, s[60:61]\n"
"s_waitcnt lgkmcnt(0)\n"
"v_mfma_f32_16x16x32_bf16 v[140:143], v[8:11], v[160:163], v[140:143]\n"
"ds_read_b128 v[8:11], v14 offset:32256\n"
"v_mfma_f32_16x16x32_bf16 v[152:155], v[250:253], v[136:139], v[152:155]\n"
"v_mfma_f32_16x16x32_bf16 v[152:155], v[234:237], v[160:163], v[152:155]\n"
"ds_read_b128 v[132:135], v17 offset:53248\n"
"v_mfma_f32_16x16x32_bf16 v[144:147], v[198:201], v[136:139], v[144:147]\n"
"ds_read_b128 v[148:151], v14 offset:38400\n"
"v_mfma_f32_16x16x32_bf16 v[144:147], v[182:185], v[160:163], v[144:147]\n"
"v_mfma_f32_16x16x32_bf16 v[156:159], v[224:227], v[136:139], v[156:159]\n"
"ds_read_b128 v[136:139], v17 offset:54272\n"
"v_rcp_f32 v249, v140\n"
"v_add_u32 v140, 0xd4800000, v140\n"
"v_cmp_lt_u32 vcc, 0x28000000, v140\n"
"v_mfma_f32_16x16x32_bf16 v[156:159], v[208:211], v[160:163], v[156:159]\n"
"ds_read_b128 v[140:143], v14 offset:44544\n"
"v_mul_f32 v152, v152, v249\n"
"v_mul_f32 v153, v153, v249\n"
"v_cvt_pk_bf16_f32 v222, v152, v153\n"
"s_waitcnt lgkmcnt(4)\n"
"v_mfma_f32_16x16x32_bf16 v[160:163], v[92:95], v[8:11], 0\n"
"s_or_b64 s[62:63], s[62:63], vcc\n"
"v_mul_f32 v154, v154, v249\n"
"v_mul_f32 v155, v155, v249\n"
"v_mfma_f32_16x16x32_bf16 v[164:167], v[96:99], v[8:11], 0\n"
"s_waitcnt lgkmcnt(3)\n"
"ds_read_b128 v[168:171], v17 offset:55296\n"
"v_cvt_pk_bf16_f32 v223, v154, v155\n"
"v_mfma_f32_16x16x32_bf16 v[152:155], v[132:135], v[8:11], 0\n"
"s_waitcnt lgkmcnt(3)\n"
"ds_read_b128 v[8:11], v14 offset:50688\n"
"v_mul_f32 v144, v144, v249\n"
"v_mfma_f32_16x16x32_bf16 v[160:163], v[100:103], v[148:151], v[160:163]\n"
"v_mul_f32 v145, v145, v249\n"
"v_cvt_pk_bf16_f32 v144, v144, v145\n"
"v_mfma_f32_16x16x32_bf16 v[164:167], v[104:107], v[148:151], v[164:167]\n"
"s_waitcnt lgkmcnt(3)\n"
"ds_read_b128 v[132:135], v17 offset:56320\n"
"v_mul_f32 v146, v146, v249\n"
"v_mfma_f32_16x16x32_bf16 v[152:155], v[136:139], v[148:151], v[152:155]\n"
"s_waitcnt lgkmcnt(3)\n"
"ds_read_b128 v[136:139], v14 offset:2048\n"
"v_mul_f32 v147, v147, v249\n"
"v_cvt_pk_bf16_f32 v145, v146, v147\n"
"v_mfma_f32_16x16x32_bf16 v[160:163], v[0:3], v[140:143], v[160:163]\n"
"v_mul_f32 v156, v156, v249\n"
"v_mul_f32 v157, v157, v249\n"
"v_mfma_f32_16x16x32_bf16 v[164:167], v[108:111], v[140:143], v[164:167]\n"
"s_waitcnt lgkmcnt(3)\n"
"ds_read_b128 v[148:151], v17 offset:57344\n"
"v_cvt_pk_bf16_f32 v146, v156, v157\n"
"v_mul_f32 v158, v158, v249\n"
"v_mfma_f32_16x16x32_bf16 v[152:155], v[168:171], v[140:143], v[152:155]\n"
"s_waitcnt lgkmcnt(3)\n"
"ds_read_b128 v[140:143], v14 offset:3072\n"
"v_mul_f32 v159, v159, v249\n"
"v_mfma_f32_16x16x32_bf16 v[160:163], v[112:115], v[8:11], v[160:163]\n"
"v_cvt_pk_bf16_f32 v147, v158, v159\n"
"v_mfma_f32_16x16x32_bf16 v[164:167], v[116:119], v[8:11], v[164:167]\n"
"s_waitcnt lgkmcnt(3)\n"
"ds_read_b128 v[156:159], v17 offset:58368\n"
"v_mfma_f32_16x16x32_bf16 v[152:155], v[132:135], v[8:11], v[152:155]\n"
"s_waitcnt lgkmcnt(3)\n"
"ds_read_b128 v[8:11], v12 offset:36864\n"
"v_mfma_f32_16x16x32_bf16 v[160:163], v[4:7], v[136:139], v[160:163]\n"
"v_mfma_f32_16x16x32_bf16 v[164:167], v[120:123], v[136:139], v[164:167]\n"
"s_waitcnt lgkmcnt(3)\n"
"v_mfma_f32_16x16x32_bf16 v[152:155], v[148:151], v[136:139], v[152:155]\n"
"s_waitcnt lgkmcnt(2)\n"
"ds_read_b128 v[132:135], v13 offset:36864\n"
"v_bfe_u32 v249, v14, 3, 5\n"
"v_mfma_f32_16x16x32_bf16 v[160:163], v[124:127], v[140:143], v[160:163]\n"
"ds_read_u16 v249, v249 offset:960\n"
"v_mfma_f32_16x16x32_bf16 v[164:167], v[128:131], v[140:143], v[164:167]\n"
"s_waitcnt vmcnt(10)\n"
"s_barrier\n"
"s_waitcnt lgkmcnt(3)\n"
"v_mfma_f32_16x16x32_bf16 v[152:155], v[156:159], v[140:143], v[152:155]\n"
"s_waitcnt lgkmcnt(2)\n"
"ds_read_b128 v[136:139], v12 offset:38912\n"
"v_mfma_f32_16x16x32_bf16 v[140:143], v[8:11], v[24:27], 0\n"
"ds_write_b128 v17, v[144:147] offset:7680\n"
"v_mfma_f32_16x16x32_bf16 v[144:147], v[8:11], v[28:31], 0\n"
"v_cmp_class_f32_e64 vcc, v160, s64\n"
"v_cvt_pk_bf16_f32 v160, v160, v161\n"
"v_mfma_f32_16x16x32_bf16 v[148:151], v[8:11], v[20:23], 0\n"
"s_waitcnt lgkmcnt(3)\n"
"ds_read_b128 v[8:11], v13 offset:38912\n"
"v_cvt_pk_bf16_f32 v161, v162, v163\n"
"v_mfma_f32_16x16x32_bf16 v[140:143], v[132:135], v[36:39], v[140:143]\n"
"s_or_b64 s[62:63], s[62:63], vcc\n"
"v_cvt_pk_bf16_f32 v162, v164, v165\n"
"s_waitcnt lgkmcnt(3)\n"
"v_mad_u32_u24 v248, v249, s40, v18\n"
"v_mfma_f32_16x16x32_bf16 v[144:147], v[132:135], v[40:43], v[144:147]\n"
"v_cvt_pk_bf16_f32 v163, v166, v167\n"
"v_cvt_pk_bf16_f32 v152, v152, v153\n"
"v_mfma_f32_16x16x32_bf16 v[148:151], v[132:135], v[32:35], v[148:151]\n"
"global_store_dwordx4 v248, v[160:163], s[68:69] nt\n"
"s_waitcnt lgkmcnt(2)\n"
"v_mfma_f32_16x16x32_bf16 v[140:143], v[136:139], v[44:47], v[140:143]\n"
"ds_read_b128 v[132:135], v12 offset:40960\n"
"v_add_u32 v248, s54, v14\n"
"v_mfma_f32_16x16x32_bf16 v[144:147], v[136:139], v[48:51], v[144:147]\n"
"ds_write_b64 v248, v[222:223] offset:0\n"
"v_mfma_f32_16x16x32_bf16 v[148:151], v[136:139], v[52:55], v[148:151]\n"
"s_waitcnt lgkmcnt(2)\n"
"ds_read_b128 v[136:139], v13 offset:40960\n"
"v_mad_u32_u24 v249, v249, s40, v19\n"
"v_mfma_f32_16x16x32_bf16 v[140:143], v[8:11], v[56:59], v[140:143]\n"
"v_cvt_pk_bf16_f32 v153, v154, v155\n"
"v_mfma_f32_16x16x32_bf16 v[144:147], v[8:11], v[60:63], v[144:147]\n"
"global_store_dwordx2 v249, v[152:153], s[70:71] nt\n"
"v_mfma_f32_16x16x32_bf16 v[148:151], v[8:11], v[64:67], v[148:151]\n"
"s_waitcnt lgkmcnt(2)\n"
"v_mfma_f32_16x16x32_bf16 v[140:143], v[132:135], v[68:71], v[140:143]\n"
"v_mfma_f32_16x16x32_bf16 v[144:147], v[132:135], v[72:75], v[144:147]\n"
"v_mfma_f32_16x16x32_bf16 v[148:151], v[132:135], v[76:79], v[148:151]\n"
"s_waitcnt lgkmcnt(0)\n"
"v_mov_b32 v8, 0x3f803f80\n"
"v_mov_b32 v9, 0x3f803f80\n"
"v_mfma_f32_16x16x32_bf16 v[140:143], v[136:139], v[80:83], v[140:143]\n"
"s_waitcnt vmcnt(7)\n"
"s_barrier\n"
"ds_read_b128 v[132:135], v17 offset:30720\n"
"v_mov_b32 v10, 0x3f803f80\n"
"v_mfma_f32_16x16x32_bf16 v[144:147], v[136:139], v[84:87], v[144:147]\n"
"s_and_b32 s41, 32, s55\n"
"v_add_u32 v177, s41, v15\n"
"v_mov_b32 v11, 0x3f803f80\n"
"v_mfma_f32_16x16x32_bf16 v[148:151], v[136:139], v[88:91], v[148:151]\n"
"ds_read_b128 v[136:139], v17 offset:31744\n"
"v_cvt_pk_bf16_f32 v196, v140, v141\n"
"v_cvt_pk_bf16_f32 v197, v142, v143\n"
"ds_read_u16 v177, v177 offset:1152\n"
"v_cvt_pk_bf16_f32 v222, v144, v145\n"
"v_cvt_pk_bf16_f32 v223, v146, v147\n"
"s_waitcnt lgkmcnt(2)\n"
"v_mfma_f32_16x16x32_bf16 v[140:143], v[8:11], v[132:135], 0\n"
"v_mfma_f32_16x16x32_bf16 v[144:147], v[200:203], v[132:135], 0\n"
"ds_read_b128 v[200:203], v17 offset:32768\n"
"v_cvt_pk_bf16_f32 v248, v148, v149\n"
"v_mfma_f32_16x16x32_bf16 v[152:155], v[226:229], v[132:135], 0\n"
"v_cvt_pk_bf16_f32 v249, v150, v151\n"
"v_mfma_f32_16x16x32_bf16 v[226:229], v[252:255], v[132:135], 0\n"
"s_waitcnt lgkmcnt(1)\n"
"v_mad_u32_u24 v177, v177, s40, v16\n"
"v_mfma_f32_16x16x32_bf16 v[140:143], v[8:11], v[136:139], v[140:143]\n"
"s_add_u32 m0, s51, 139264\n"
"s_nop 0\n"
"global_load_lds_dwordx4 v177, s[56:57]\n"
"v_mfma_f32_16x16x32_bf16 v[144:147], v[182:185], v[136:139], v[144:147]\n"
"ds_read_b128 v[252:255], v17 offset:33792\n"
"v_mfma_f32_16x16x32_bf16 v[152:155], v[208:211], v[136:139], v[152:155]\n"
"ds_read_b64 v[134:135], v17 offset:8712\n"
"v_mfma_f32_16x16x32_bf16 v[226:229], v[234:237], v[136:139], v[226:229]\n"
"s_waitcnt lgkmcnt(2)\n"
"v_mfma_f32_16x16x32_bf16 v[140:143], v[8:11], v[200:203], v[140:143]\n"
"s_add_u32 m0, s51, 141312\n"
"s_nop 0\n"
"global_load_lds_dwordx4 v177, s[58:59]\n"
"v_mfma_f32_16x16x32_bf16 v[144:147], v[192:195], v[200:203], v[144:147]\n"
"v_mfma_f32_16x16x32_bf16 v[152:155], v[218:221], v[200:203], v[152:155]\n"
"v_mov_b32 v132, 0\n"
"v_mov_b32 v133, 0\n"
"v_mfma_f32_16x16x32_bf16 v[226:229], v[244:247], v[200:203], v[226:229]\n"
"s_waitcnt lgkmcnt(1)\n"
"v_mfma_f32_16x16x32_bf16 v[140:143], v[8:11], v[252:255], v[140:143]\n"
"s_add_u32 m0, s51, 143360\n"
"s_nop 0\n"
"global_load_lds_dwordx4 v177, s[60:61]\n"
"s_waitcnt lgkmcnt(0)\n"
"v_mfma_f32_16x16x32_bf16 v[140:143], v[8:11], v[132:135], v[140:143]\n"
"ds_read_b128 v[200:203], v14 offset:32256\n"
"v_mfma_f32_16x16x32_bf16 v[226:229], v[248:251], v[252:255], v[226:229]\n"
"v_mfma_f32_16x16x32_bf16 v[226:229], v[236:239], v[132:135], v[226:229]\n"
"ds_read_b128 v[8:11], v17 offset:53248\n"
"v_mfma_f32_16x16x32_bf16 v[144:147], v[196:199], v[252:255], v[144:147]\n"
"ds_read_b128 v[136:139], v14 offset:38400\n"
"v_mfma_f32_16x16x32_bf16 v[144:147], v[184:187], v[132:135], v[144:147]\n"
"v_mfma_f32_16x16x32_bf16 v[152:155], v[222:225], v[252:255], v[152:155]\n"
"ds_read_b128 v[250:253], v17 offset:54272\n"
"v_rcp_f32 v16, v140\n"
"v_add_u32 v140, 0xd4800000, v140\n"
"v_cmp_lt_u32 vcc, 0x28000000, v140\n"
"v_mfma_f32_16x16x32_bf16 v[152:155], v[210:213], v[132:135], v[152:155]\n"
"ds_read_b128 v[132:135], v14 offset:44544\n"
"v_mul_f32 v226, v226, v16\n"
"v_mul_f32 v227, v227, v16\n"
"v_cvt_pk_bf16_f32 v198, v226, v227\n"
"s_waitcnt lgkmcnt(4)\n"
"v_mfma_f32_16x16x32_bf16 v[224:227], v[92:95], v[200:203], 0\n"
"s_or_b64 s[62:63], s[62:63], vcc\n"
"v_mul_f32 v228, v228, v16\n"
"v_mul_f32 v229, v229, v16\n"
"v_mfma_f32_16x16x32_bf16 v[140:143], v[96:99], v[200:203], 0\n"
"s_waitcnt lgkmcnt(3)\n"
"ds_read_b128 v[148:151], v17 offset:55296\n"
"v_cvt_pk_bf16_f32 v199, v228, v229\n"
"v_mfma_f32_16x16x32_bf16 v[156:159], v[8:11], v[200:203], 0\n"
"s_waitcnt lgkmcnt(3)\n"
"ds_read_b128 v[200:203], v14 offset:50688\n"
"v_mul_f32 v144, v144, v16\n"
"v_mfma_f32_16x16x32_bf16 v[224:227], v[100:103], v[136:139], v[224:227]\n"
"v_mul_f32 v145, v145, v16\n"
"v_cvt_pk_bf16_f32 v144, v144, v145\n"
"v_mfma_f32_16x16x32_bf16 v[140:143], v[104:107], v[136:139], v[140:143]\n"
"s_waitcnt lgkmcnt(3)\n"
"ds_read_b128 v[8:11], v17 offset:56320\n"
"v_mul_f32 v146, v146, v16\n"
"v_mfma_f32_16x16x32_bf16 v[156:159], v[250:253], v[136:139], v[156:159]\n"
"s_waitcnt lgkmcnt(3)\n"
"ds_read_b128 v[250:253], v14 offset:2048\n"
"v_mul_f32 v147, v147, v16\n"
"v_cvt_pk_bf16_f32 v145, v146, v147\n"
"v_mfma_f32_16x16x32_bf16 v[224:227], v[0:3], v[132:135], v[224:227]\n"
"v_mul_f32 v152, v152, v16\n"
"v_mul_f32 v153, v153, v16\n"
"v_mfma_f32_16x16x32_bf16 v[140:143], v[108:111], v[132:135], v[140:143]\n"
"s_waitcnt lgkmcnt(3)\n"
"ds_read_b128 v[136:139], v17 offset:57344\n"
"v_cvt_pk_bf16_f32 v146, v152, v153\n"
"v_mul_f32 v154, v154, v16\n"
"v_mfma_f32_16x16x32_bf16 v[156:159], v[148:151], v[132:135], v[156:159]\n"
"s_waitcnt lgkmcnt(3)\n"
"ds_read_b128 v[132:135], v14 offset:3072\n"
"v_mul_f32 v155, v155, v16\n"
"v_mfma_f32_16x16x32_bf16 v[224:227], v[112:115], v[200:203], v[224:227]\n"
"v_cvt_pk_bf16_f32 v147, v154, v155\n"
"v_mfma_f32_16x16x32_bf16 v[140:143], v[116:119], v[200:203], v[140:143]\n"
"s_waitcnt lgkmcnt(3)\n"
"ds_read_b128 v[148:151], v17 offset:58368\n"
"v_mfma_f32_16x16x32_bf16 v[156:159], v[8:11], v[200:203], v[156:159]\n"
"s_waitcnt lgkmcnt(3)\n"
"ds_read_b128 v[200:203], v12 offset:0\n"
"v_mfma_f32_16x16x32_bf16 v[224:227], v[4:7], v[250:253], v[224:227]\n"
"v_mfma_f32_16x16x32_bf16 v[140:143], v[120:123], v[250:253], v[140:143]\n"
"s_waitcnt lgkmcnt(3)\n"
"v_mfma_f32_16x16x32_bf16 v[156:159], v[136:139], v[250:253], v[156:159]\n"
"s_waitcnt lgkmcnt(2)\n"
"ds_read_b128 v[250:253], v13 offset:0\n"
"v_bfe_u32 v16, v14, 3, 5\n"
"v_mfma_f32_16x16x32_bf16 v[224:227], v[124:127], v[132:135], v[224:227]\n"
"ds_read_u16 v16, v16 offset:992\n"
"v_mfma_f32_16x16x32_bf16 v[140:143], v[128:131], v[132:135], v[140:143]\n"
"s_barrier\n"
"s_waitcnt lgkmcnt(3)\n"
"v_mfma_f32_16x16x32_bf16 v[156:159], v[148:151], v[132:135], v[156:159]\n"
"s_waitcnt lgkmcnt(2)\n"
"ds_read_b128 v[8:11], v12 offset:2048\n"
"v_mfma_f32_16x16x32_bf16 v[132:135], v[200:203], v[24:27], 0\n"
"ds_write_b128 v17, v[144:147] offset:7680\n"
"v_mfma_f32_16x16x32_bf16 v[136:139], v[200:203], v[28:31], 0\n"
"v_cmp_class_f32_e64 vcc, v224, s64\n"
"v_cvt_pk_bf16_f32 v224, v224, v225\n"
"v_mfma_f32_16x16x32_bf16 v[144:147], v[200:203], v[20:23], 0\n"
"s_waitcnt lgkmcnt(3)\n"
"ds_read_b128 v[200:203], v13 offset:2048\n"
"v_cvt_pk_bf16_f32 v225, v226, v227\n"
"v_mfma_f32_16x16x32_bf16 v[132:135], v[250:253], v[36:39], v[132:135]\n"
"s_or_b64 s[62:63], s[62:63], vcc\n"
"v_cvt_pk_bf16_f32 v226, v140, v141\n"
"s_waitcnt lgkmcnt(3)\n"
"v_mad_u32_u24 v15, v16, s40, v18\n"
"v_mfma_f32_16x16x32_bf16 v[136:139], v[250:253], v[40:43], v[136:139]\n"
"v_cvt_pk_bf16_f32 v227, v142, v143\n"
"v_cvt_pk_bf16_f32 v156, v156, v157\n"
"v_mfma_f32_16x16x32_bf16 v[144:147], v[250:253], v[32:35], v[144:147]\n"
"global_store_dwordx4 v15, v[224:227], s[68:69] nt\n"
"s_waitcnt lgkmcnt(2)\n"
"v_mfma_f32_16x16x32_bf16 v[132:135], v[8:11], v[44:47], v[132:135]\n"
"ds_read_b128 v[224:227], v12 offset:4096\n"
"v_add_u32 v157, s54, v14\n"
"v_mfma_f32_16x16x32_bf16 v[136:139], v[8:11], v[48:51], v[136:139]\n"
"ds_write_b64 v157, v[198:199] offset:0\n"
"v_mfma_f32_16x16x32_bf16 v[144:147], v[8:11], v[52:55], v[144:147]\n"
"s_waitcnt lgkmcnt(2)\n"
"ds_read_b128 v[250:253], v13 offset:4096\n"
"v_mad_u32_u24 v16, v16, s40, v19\n"
"v_mfma_f32_16x16x32_bf16 v[132:135], v[200:203], v[56:59], v[132:135]\n"
"v_cvt_pk_bf16_f32 v157, v158, v159\n"
"v_mfma_f32_16x16x32_bf16 v[136:139], v[200:203], v[60:63], v[136:139]\n"
"global_store_dwordx2 v16, v[156:157], s[70:71] nt\n"
"v_mfma_f32_16x16x32_bf16 v[144:147], v[200:203], v[64:67], v[144:147]\n"
"s_waitcnt lgkmcnt(2)\n"
"ds_read_b128 v[198:201], v12 offset:6144\n"
"v_mfma_f32_16x16x32_bf16 v[132:135], v[224:227], v[68:71], v[132:135]\n"
"v_mfma_f32_16x16x32_bf16 v[136:139], v[224:227], v[72:75], v[136:139]\n"
"v_mfma_f32_16x16x32_bf16 v[144:147], v[224:227], v[76:79], v[144:147]\n"
"s_waitcnt lgkmcnt(1)\n"
"ds_read_b128 v[226:229], v13 offset:6144\n"
"v_mfma_f32_16x16x32_bf16 v[132:135], v[250:253], v[80:83], v[132:135]\n"
"v_mfma_f32_16x16x32_bf16 v[136:139], v[250:253], v[84:87], v[136:139]\n"
"v_mfma_f32_16x16x32_bf16 v[144:147], v[250:253], v[88:91], v[144:147]\n"
"s_waitcnt lgkmcnt(1)\n"
"ds_read_b128 v[252:255], v12 offset:8192\n"
"v_mfma_f32_16x16x32_bf16 v[8:11], v[198:201], v[24:27], 0\n"
"v_mfma_f32_16x16x32_bf16 v[140:143], v[198:201], v[28:31], 0\n"
"v_mfma_f32_16x16x32_bf16 v[148:151], v[198:201], v[20:23], 0\n"
"s_waitcnt lgkmcnt(1)\n"
"ds_read_b128 v[200:203], v13 offset:8192\n"
"v_mfma_f32_16x16x32_bf16 v[8:11], v[226:229], v[36:39], v[8:11]\n"
"v_cvt_pk_bf16_f32 v198, v132, v133\n"
"v_cvt_pk_bf16_f32 v199, v134, v135\n"
"v_cvt_pk_bf16_f32 v224, v136, v137\n"
"v_cvt_pk_bf16_f32 v225, v138, v139\n"
"v_mfma_f32_16x16x32_bf16 v[140:143], v[226:229], v[40:43], v[140:143]\n"
"v_cvt_pk_bf16_f32 v250, v144, v145\n"
"v_cvt_pk_bf16_f32 v251, v146, v147\n"
"v_mfma_f32_16x16x32_bf16 v[148:151], v[226:229], v[32:35], v[148:151]\n"
"s_waitcnt lgkmcnt(1)\n"
"ds_read_b128 v[226:229], v12 offset:10240\n"
"v_mfma_f32_16x16x32_bf16 v[8:11], v[252:255], v[44:47], v[8:11]\n"
"v_mfma_f32_16x16x32_bf16 v[140:143], v[252:255], v[48:51], v[140:143]\n"
"v_mfma_f32_16x16x32_bf16 v[148:151], v[252:255], v[52:55], v[148:151]\n"
"s_waitcnt lgkmcnt(1)\n"
"ds_read_b128 v[252:255], v13 offset:10240\n"
"v_mfma_f32_16x16x32_bf16 v[8:11], v[200:203], v[56:59], v[8:11]\n"
"v_mfma_f32_16x16x32_bf16 v[140:143], v[200:203], v[60:63], v[140:143]\n"
"v_mfma_f32_16x16x32_bf16 v[148:151], v[200:203], v[64:67], v[148:151]\n"
"s_waitcnt lgkmcnt(1)\n"
"v_mfma_f32_16x16x32_bf16 v[8:11], v[226:229], v[68:71], v[8:11]\n"
"v_mfma_f32_16x16x32_bf16 v[140:143], v[226:229], v[72:75], v[140:143]\n"
"v_mfma_f32_16x16x32_bf16 v[148:151], v[226:229], v[76:79], v[148:151]\n"
"s_waitcnt lgkmcnt(0)\n"
"v_mov_b32 v132, 0x3f803f80\n"
"v_mov_b32 v133, 0x3f803f80\n"
"v_mfma_f32_16x16x32_bf16 v[8:11], v[252:255], v[80:83], v[8:11]\n"
"s_waitcnt vmcnt(7)\n"
"s_barrier\n"
"ds_read_b128 v[136:139], v17 offset:28672\n"
"v_mov_b32 v134, 0x3f803f80\n"
"v_mfma_f32_16x16x32_bf16 v[140:143], v[252:255], v[84:87], v[140:143]\n"
"ds_read_b128 v[144:147], v17 offset:29696\n"
"v_mov_b32 v135, 0x3f803f80\n"
"v_mfma_f32_16x16x32_bf16 v[148:151], v[252:255], v[88:91], v[148:151]\n"
"s_waitcnt lgkmcnt(1)\n"
"v_mfma_f32_16x16x32_bf16 v[152:155], v[132:135], v[136:139], 0\n"
"v_cvt_pk_bf16_f32 v200, v8, v9\n"
"v_cvt_pk_bf16_f32 v201, v10, v11\n"
"v_mfma_f32_16x16x32_bf16 v[8:11], v[180:183], v[136:139], 0\n"
"ds_read_b128 v[156:159], v17 offset:32768\n"
"v_cvt_pk_bf16_f32 v226, v140, v141\n"
"v_mfma_f32_16x16x32_bf16 v[160:163], v[206:209], v[136:139], 0\n"
"ds_read_b128 v[164:167], v17 offset:33792\n"
"v_cvt_pk_bf16_f32 v227, v142, v143\n"
"v_mfma_f32_16x16x32_bf16 v[140:143], v[232:235], v[136:139], 0\n"
"s_waitcnt lgkmcnt(2)\n"
"v_cvt_pk_bf16_f32 v252, v148, v149\n"
"v_cvt_pk_bf16_f32 v253, v150, v151\n"
"v_mfma_f32_16x16x32_bf16 v[152:155], v[132:135], v[144:147], v[152:155]\n"
"v_mfma_f32_16x16x32_bf16 v[8:11], v[184:187], v[144:147], v[8:11]\n"
"v_mfma_f32_16x16x32_bf16 v[160:163], v[210:213], v[144:147], v[160:163]\n"
"v_mfma_f32_16x16x32_bf16 v[140:143], v[236:239], v[144:147], v[140:143]\n"
"s_waitcnt lgkmcnt(1)\n"
"v_mfma_f32_16x16x32_bf16 v[152:155], v[132:135], v[156:159], v[152:155]\n"
"s_waitcnt lgkmcnt(0)\n"
"v_mfma_f32_16x16x32_bf16 v[152:155], v[132:135], v[164:167], v[152:155]\n"
"ds_read_b128 v[132:135], v14 offset:32256\n"
"v_mfma_f32_16x16x32_bf16 v[140:143], v[242:245], v[156:159], v[140:143]\n"
"v_mfma_f32_16x16x32_bf16 v[140:143], v[246:249], v[164:167], v[140:143]\n"
"ds_read_b128 v[136:139], v17 offset:53248\n"
"v_mfma_f32_16x16x32_bf16 v[8:11], v[190:193], v[156:159], v[8:11]\n"
"ds_read_b128 v[144:147], v14 offset:38400\n"
"v_mfma_f32_16x16x32_bf16 v[8:11], v[194:197], v[164:167], v[8:11]\n"
"v_mfma_f32_16x16x32_bf16 v[160:163], v[216:219], v[156:159], v[160:163]\n"
"ds_read_b128 v[148:151], v17 offset:54272\n"
"v_rcp_f32 v16, v152\n"
"v_add_u32 v152, 0xd4800000, v152\n"
"v_cmp_lt_u32 vcc, 0x28000000, v152\n"
"v_mfma_f32_16x16x32_bf16 v[160:163], v[220:223], v[164:167], v[160:163]\n"
"ds_read_b128 v[152:155], v14 offset:44544\n"
"v_mul_f32 v140, v140, v16\n"
"v_mul_f32 v141, v141, v16\n"
"v_cvt_pk_bf16_f32 v254, v140, v141\n"
"s_waitcnt lgkmcnt(4)\n"
"v_mfma_f32_16x16x32_bf16 v[156:159], v[92:95], v[132:135], 0\n"
"s_or_b64 s[62:63], s[62:63], vcc\n"
"v_mul_f32 v142, v142, v16\n"
"v_mul_f32 v143, v143, v16\n"
"v_mfma_f32_16x16x32_bf16 v[164:167], v[96:99], v[132:135], 0\n"
"s_waitcnt lgkmcnt(3)\n"
"ds_read_b128 v[168:171], v17 offset:55296\n"
"v_cvt_pk_bf16_f32 v255, v142, v143\n"
"v_mfma_f32_16x16x32_bf16 v[140:143], v[136:139], v[132:135], 0\n"
"s_waitcnt lgkmcnt(3)\n"
"ds_read_b128 v[132:135], v14 offset:50688\n"
"v_mul_f32 v8, v8, v16\n"
"v_mfma_f32_16x16x32_bf16 v[156:159], v[100:103], v[144:147], v[156:159]\n"
"v_mul_f32 v9, v9, v16\n"
"v_cvt_pk_bf16_f32 v8, v8, v9\n"
"v_mfma_f32_16x16x32_bf16 v[164:167], v[104:107], v[144:147], v[164:167]\n"
"s_waitcnt lgkmcnt(3)\n"
"ds_read_b128 v[136:139], v17 offset:56320\n"
"v_mul_f32 v10, v10, v16\n"
"v_mfma_f32_16x16x32_bf16 v[140:143], v[148:151], v[144:147], v[140:143]\n"
"s_waitcnt lgkmcnt(3)\n"
"ds_read_b128 v[144:147], v14 offset:2048\n"
"v_mul_f32 v11, v11, v16\n"
"v_cvt_pk_bf16_f32 v9, v10, v11\n"
"v_mfma_f32_16x16x32_bf16 v[156:159], v[0:3], v[152:155], v[156:159]\n"
"v_mul_f32 v160, v160, v16\n"
"v_mul_f32 v161, v161, v16\n"
"v_mfma_f32_16x16x32_bf16 v[164:167], v[108:111], v[152:155], v[164:167]\n"
"s_waitcnt lgkmcnt(3)\n"
"ds_read_b128 v[148:151], v17 offset:57344\n"
"v_cvt_pk_bf16_f32 v10, v160, v161\n"
"v_mul_f32 v162, v162, v16\n"
"v_mfma_f32_16x16x32_bf16 v[140:143], v[168:171], v[152:155], v[140:143]\n"
"s_waitcnt lgkmcnt(3)\n"
"ds_read_b128 v[152:155], v14 offset:3072\n"
"v_mul_f32 v163, v163, v16\n"
"v_mfma_f32_16x16x32_bf16 v[156:159], v[112:115], v[132:135], v[156:159]\n"
"v_cvt_pk_bf16_f32 v11, v162, v163\n"
"v_mfma_f32_16x16x32_bf16 v[164:167], v[116:119], v[132:135], v[164:167]\n"
"s_waitcnt lgkmcnt(3)\n"
"ds_read_b128 v[160:163], v17 offset:58368\n"
"v_mfma_f32_16x16x32_bf16 v[140:143], v[136:139], v[132:135], v[140:143]\n"
"s_waitcnt lgkmcnt(3)\n"
"ds_read_b128 v[132:135], v12 offset:55296\n"
"v_mfma_f32_16x16x32_bf16 v[156:159], v[4:7], v[144:147], v[156:159]\n"
"v_mfma_f32_16x16x32_bf16 v[164:167], v[120:123], v[144:147], v[164:167]\n"
"s_waitcnt lgkmcnt(3)\n"
"v_mfma_f32_16x16x32_bf16 v[140:143], v[148:151], v[144:147], v[140:143]\n"
"s_waitcnt lgkmcnt(2)\n"
"ds_read_b128 v[136:139], v13 offset:55296\n"
"v_bfe_u32 v16, v14, 3, 5\n"
"v_mfma_f32_16x16x32_bf16 v[156:159], v[124:127], v[152:155], v[156:159]\n"
"ds_read_u16 v16, v16 offset:1024\n"
"v_mfma_f32_16x16x32_bf16 v[164:167], v[128:131], v[152:155], v[164:167]\n"
"s_barrier\n"
"s_waitcnt lgkmcnt(3)\n"
"v_mfma_f32_16x16x32_bf16 v[140:143], v[160:163], v[152:155], v[140:143]\n"
"s_waitcnt lgkmcnt(2)\n"
"ds_read_b128 v[144:147], v12 offset:57344\n"
"v_mfma_f32_16x16x32_bf16 v[148:151], v[132:135], v[24:27], 0\n"
"ds_write_b128 v17, v[8:11] offset:7680\n"
"v_mfma_f32_16x16x32_bf16 v[8:11], v[132:135], v[28:31], 0\n"
"v_cmp_class_f32_e64 vcc, v156, s64\n"
"v_cvt_pk_bf16_f32 v156, v156, v157\n"
"v_mfma_f32_16x16x32_bf16 v[152:155], v[132:135], v[20:23], 0\n"
"s_waitcnt lgkmcnt(3)\n"
"ds_read_b128 v[132:135], v13 offset:57344\n"
"v_cvt_pk_bf16_f32 v157, v158, v159\n"
"v_mfma_f32_16x16x32_bf16 v[148:151], v[136:139], v[36:39], v[148:151]\n"
"s_or_b64 s[62:63], s[62:63], vcc\n"
"v_cvt_pk_bf16_f32 v158, v164, v165\n"
"s_waitcnt lgkmcnt(3)\n"
"v_mad_u32_u24 v15, v16, s40, v18\n"
"v_mfma_f32_16x16x32_bf16 v[8:11], v[136:139], v[40:43], v[8:11]\n"
"v_cvt_pk_bf16_f32 v159, v166, v167\n"
"v_cvt_pk_bf16_f32 v140, v140, v141\n"
"v_mfma_f32_16x16x32_bf16 v[152:155], v[136:139], v[32:35], v[152:155]\n"
"global_store_dwordx4 v15, v[156:159], s[68:69] nt\n"
"s_waitcnt lgkmcnt(2)\n"
"v_mfma_f32_16x16x32_bf16 v[148:151], v[144:147], v[44:47], v[148:151]\n"
"ds_read_b128 v[136:139], v12 offset:59392\n"
"v_add_u32 v141, s54, v14\n"
"v_mfma_f32_16x16x32_bf16 v[8:11], v[144:147], v[48:51], v[8:11]\n"
"ds_write_b64 v141, v[254:255] offset:0\n"
"v_mfma_f32_16x16x32_bf16 v[152:155], v[144:147], v[52:55], v[152:155]\n"
"s_waitcnt lgkmcnt(2)\n"
"ds_read_b128 v[144:147], v13 offset:59392\n"
"v_mad_u32_u24 v16, v16, s40, v19\n"
"v_mfma_f32_16x16x32_bf16 v[148:151], v[132:135], v[56:59], v[148:151]\n"
"v_cvt_pk_bf16_f32 v141, v142, v143\n"
"v_mfma_f32_16x16x32_bf16 v[8:11], v[132:135], v[60:63], v[8:11]\n"
"global_store_dwordx2 v16, v[140:141], s[70:71] nt\n"
"v_mfma_f32_16x16x32_bf16 v[152:155], v[132:135], v[64:67], v[152:155]\n"
"s_waitcnt lgkmcnt(2)\n"
"v_mfma_f32_16x16x32_bf16 v[148:151], v[136:139], v[68:71], v[148:151]\n"
"v_mfma_f32_16x16x32_bf16 v[8:11], v[136:139], v[72:75], v[8:11]\n"
"v_mfma_f32_16x16x32_bf16 v[152:155], v[136:139], v[76:79], v[152:155]\n"
"s_waitcnt lgkmcnt(0)\n"
"v_mov_b32 v132, 0x3f803f80\n"
"v_mov_b32 v133, 0x3f803f80\n"
"v_mfma_f32_16x16x32_bf16 v[148:151], v[144:147], v[80:83], v[148:151]\n"
"s_waitcnt vmcnt(4)\n"
"s_barrier\n"
"ds_read_b128 v[136:139], v17 offset:30720\n"
"v_mov_b32 v134, 0x3f803f80\n"
"v_mfma_f32_16x16x32_bf16 v[8:11], v[144:147], v[84:87], v[8:11]\n"
"ds_read_b128 v[140:143], v17 offset:31744\n"
"v_mov_b32 v135, 0x3f803f80\n"
"v_mfma_f32_16x16x32_bf16 v[152:155], v[144:147], v[88:91], v[152:155]\n"
"s_waitcnt lgkmcnt(1)\n"
"v_mfma_f32_16x16x32_bf16 v[144:147], v[132:135], v[136:139], 0\n"
"v_cvt_pk_bf16_f32 v202, v148, v149\n"
"v_cvt_pk_bf16_f32 v203, v150, v151\n"
"v_mfma_f32_16x16x32_bf16 v[148:151], v[178:181], v[136:139], 0\n"
"ds_read_b128 v[156:159], v17 offset:32768\n"
"v_cvt_pk_bf16_f32 v228, v8, v9\n"
"v_mfma_f32_16x16x32_bf16 v[160:163], v[204:207], v[136:139], 0\n"
"v_cvt_pk_bf16_f32 v229, v10, v11\n"
"v_cvt_pk_bf16_f32 v254, v152, v153\n"
"v_mfma_f32_16x16x32_bf16 v[8:11], v[230:233], v[136:139], 0\n"
"s_waitcnt lgkmcnt(1)\n"
"v_cvt_pk_bf16_f32 v255, v154, v155\n"
"v_mfma_f32_16x16x32_bf16 v[144:147], v[132:135], v[140:143], v[144:147]\n"
"v_mfma_f32_16x16x32_bf16 v[148:151], v[188:191], v[140:143], v[148:151]\n"
"ds_read_b128 v[166:169], v17 offset:33792\n"
"v_mfma_f32_16x16x32_bf16 v[160:163], v[214:217], v[140:143], v[160:163]\n"
"ds_read_b64 v[138:139], v17 offset:8712\n"
"v_mfma_f32_16x16x32_bf16 v[8:11], v[240:243], v[140:143], v[8:11]\n"
"s_waitcnt lgkmcnt(2)\n"
"v_mfma_f32_16x16x32_bf16 v[144:147], v[132:135], v[156:159], v[144:147]\n"
"v_mfma_f32_16x16x32_bf16 v[148:151], v[198:201], v[156:159], v[148:151]\n"
"v_mfma_f32_16x16x32_bf16 v[160:163], v[224:227], v[156:159], v[160:163]\n"
"v_mov_b32 v136, 0\n"
"v_mov_b32 v137, 0\n"
"v_mfma_f32_16x16x32_bf16 v[8:11], v[250:253], v[156:159], v[8:11]\n"
"s_waitcnt lgkmcnt(1)\n"
"v_mov_b32 v164, 0\n"
"v_mov_b32 v165, 0\n"
"v_mfma_f32_16x16x32_bf16 v[144:147], v[132:135], v[166:169], v[144:147]\n"
"s_waitcnt lgkmcnt(0)\n"
"v_mov_b32 v170, 0\n"
"v_mov_b32 v171, 0\n"
"v_mfma_f32_16x16x32_bf16 v[144:147], v[132:135], v[136:139], v[144:147]\n"
"v_mfma_f32_16x16x32_bf16 v[8:11], v[232:235], v[164:167], v[8:11]\n"
"v_mfma_f32_16x16x32_bf16 v[8:11], v[244:247], v[168:171], v[8:11]\n"
"v_mfma_f32_16x16x32_bf16 v[8:11], v[252:255], v[136:139], v[8:11]\n"
"ds_read_b128 v[132:135], v14 offset:32256\n"
"v_mfma_f32_16x16x32_bf16 v[148:151], v[180:183], v[164:167], v[148:151]\n"
"v_mfma_f32_16x16x32_bf16 v[148:151], v[192:195], v[168:171], v[148:151]\n"
"ds_read_b128 v[140:143], v17 offset:53248\n"
"v_mfma_f32_16x16x32_bf16 v[148:151], v[200:203], v[136:139], v[148:151]\n"
"ds_read_b128 v[152:155], v14 offset:38400\n"
"v_rcp_f32 v16, v144\n"
"v_mfma_f32_16x16x32_bf16 v[160:163], v[206:209], v[164:167], v[160:163]\n"
"v_add_u32 v144, 0xd4800000, v144\n"
"v_cmp_lt_u32 vcc, 0x28000000, v144\n"
"v_mfma_f32_16x16x32_bf16 v[160:163], v[218:221], v[168:171], v[160:163]\n"
"ds_read_b128 v[144:147], v17 offset:54272\n"
"v_mul_f32 v8, v8, v16\n"
"v_mfma_f32_16x16x32_bf16 v[160:163], v[226:229], v[136:139], v[160:163]\n"
"s_or_b64 s[62:63], s[62:63], vcc\n"
"ds_read_b128 v[136:139], v14 offset:44544\n"
"v_mul_f32 v9, v9, v16\n"
"v_cvt_pk_bf16_f32 v240, v8, v9\n"
"s_waitcnt lgkmcnt(4)\n"
"v_mfma_f32_16x16x32_bf16 v[156:159], v[92:95], v[132:135], 0\n"
"v_mul_f32 v10, v10, v16\n"
"v_mul_f32 v11, v11, v16\n"
"v_mfma_f32_16x16x32_bf16 v[164:167], v[96:99], v[132:135], 0\n"
"s_waitcnt lgkmcnt(3)\n"
"ds_read_b128 v[168:171], v17 offset:55296\n"
"v_cvt_pk_bf16_f32 v241, v10, v11\n"
"v_mfma_f32_16x16x32_bf16 v[8:11], v[140:143], v[132:135], 0\n"
"s_waitcnt lgkmcnt(3)\n"
"ds_read_b128 v[132:135], v14 offset:50688\n"
"v_mul_f32 v148, v148, v16\n"
"v_mfma_f32_16x16x32_bf16 v[156:159], v[100:103], v[152:155], v[156:159]\n"
"v_mul_f32 v149, v149, v16\n"
"v_cvt_pk_bf16_f32 v148, v148, v149\n"
"v_mfma_f32_16x16x32_bf16 v[164:167], v[104:107], v[152:155], v[164:167]\n"
"s_waitcnt lgkmcnt(3)\n"
"ds_read_b128 v[140:143], v17 offset:56320\n"
"v_mul_f32 v150, v150, v16\n"
"v_mul_f32 v151, v151, v16\n"
"v_mfma_f32_16x16x32_bf16 v[8:11], v[144:147], v[152:155], v[8:11]\n"
"s_waitcnt lgkmcnt(3)\n"
"ds_read_b128 v[144:147], v14 offset:2048\n"
"v_cvt_pk_bf16_f32 v149, v150, v151\n"
"v_mfma_f32_16x16x32_bf16 v[156:159], v[0:3], v[136:139], v[156:159]\n"
"v_mul_f32 v160, v160, v16\n"
"v_mul_f32 v161, v161, v16\n"
"v_mfma_f32_16x16x32_bf16 v[164:167], v[108:111], v[136:139], v[164:167]\n"
"s_waitcnt lgkmcnt(3)\n"
"ds_read_b128 v[152:155], v17 offset:57344\n"
"v_cvt_pk_bf16_f32 v150, v160, v161\n"
"v_mul_f32 v162, v162, v16\n"
"v_mfma_f32_16x16x32_bf16 v[8:11], v[168:171], v[136:139], v[8:11]\n"
"s_waitcnt lgkmcnt(3)\n"
"ds_read_b128 v[136:139], v14 offset:3072\n"
"v_mul_f32 v163, v163, v16\n"
"v_mfma_f32_16x16x32_bf16 v[156:159], v[112:115], v[132:135], v[156:159]\n"
"v_cvt_pk_bf16_f32 v151, v162, v163\n"
"v_mfma_f32_16x16x32_bf16 v[164:167], v[116:119], v[132:135], v[164:167]\n"
"s_waitcnt lgkmcnt(3)\n"
"ds_read_b128 v[160:163], v17 offset:58368\n"
"v_mfma_f32_16x16x32_bf16 v[8:11], v[140:143], v[132:135], v[8:11]\n"
"s_waitcnt lgkmcnt(3)\n"
"ds_read_b128 v[132:135], v12 offset:18432\n"
"v_mfma_f32_16x16x32_bf16 v[156:159], v[4:7], v[144:147], v[156:159]\n"
"v_mfma_f32_16x16x32_bf16 v[164:167], v[120:123], v[144:147], v[164:167]\n"
"s_waitcnt lgkmcnt(3)\n"
"v_mfma_f32_16x16x32_bf16 v[8:11], v[152:155], v[144:147], v[8:11]\n"
"s_waitcnt lgkmcnt(2)\n"
"ds_read_b128 v[140:143], v13 offset:18432\n"
"v_bfe_u32 v16, v14, 3, 5\n"
"v_mfma_f32_16x16x32_bf16 v[156:159], v[124:127], v[136:139], v[156:159]\n"
"ds_read_u16 v16, v16 offset:1056\n"
"v_mfma_f32_16x16x32_bf16 v[164:167], v[128:131], v[136:139], v[164:167]\n"
"s_barrier\n"
"s_waitcnt lgkmcnt(3)\n"
"v_mfma_f32_16x16x32_bf16 v[8:11], v[160:163], v[136:139], v[8:11]\n"
"s_waitcnt lgkmcnt(2)\n"
"ds_read_b128 v[136:139], v12 offset:20480\n"
"v_mfma_f32_16x16x32_bf16 v[144:147], v[132:135], v[24:27], 0\n"
"ds_write_b128 v17, v[148:151] offset:7680\n"
"v_mfma_f32_16x16x32_bf16 v[148:151], v[132:135], v[28:31], 0\n"
"v_cmp_class_f32_e64 vcc, v156, s64\n"
"v_cvt_pk_bf16_f32 v156, v156, v157\n"
"v_mfma_f32_16x16x32_bf16 v[152:155], v[132:135], v[20:23], 0\n"
"s_waitcnt lgkmcnt(3)\n"
"ds_read_b128 v[132:135], v13 offset:20480\n"
"v_cvt_pk_bf16_f32 v157, v158, v159\n"
"v_mfma_f32_16x16x32_bf16 v[144:147], v[140:143], v[36:39], v[144:147]\n"
"s_or_b64 s[62:63], s[62:63], vcc\n"
"v_cvt_pk_bf16_f32 v158, v164, v165\n"
"s_waitcnt lgkmcnt(3)\n"
"v_mad_u32_u24 v15, v16, s40, v18\n"
"v_mfma_f32_16x16x32_bf16 v[148:151], v[140:143], v[40:43], v[148:151]\n"
"v_cvt_pk_bf16_f32 v159, v166, v167\n"
"v_cvt_pk_bf16_f32 v8, v8, v9\n"
"v_mfma_f32_16x16x32_bf16 v[152:155], v[140:143], v[32:35], v[152:155]\n"
"global_store_dwordx4 v15, v[156:159], s[68:69] nt\n"
"s_waitcnt lgkmcnt(2)\n"
"v_mfma_f32_16x16x32_bf16 v[144:147], v[136:139], v[44:47], v[144:147]\n"
"ds_read_b128 v[140:143], v12 offset:22528\n"
"v_add_u32 v15, s54, v14\n"
"v_mfma_f32_16x16x32_bf16 v[148:151], v[136:139], v[48:51], v[148:151]\n"
"ds_write_b64 v15, v[240:241] offset:0\n"
"v_mfma_f32_16x16x32_bf16 v[152:155], v[136:139], v[52:55], v[152:155]\n"
"s_waitcnt lgkmcnt(2)\n"
"ds_read_b128 v[136:139], v13 offset:22528\n"
"v_mad_u32_u24 v16, v16, s40, v19\n"
"v_mfma_f32_16x16x32_bf16 v[144:147], v[132:135], v[56:59], v[144:147]\n"
"v_cvt_pk_bf16_f32 v9, v10, v11\n"
"v_mfma_f32_16x16x32_bf16 v[148:151], v[132:135], v[60:63], v[148:151]\n"
"global_store_dwordx2 v16, v[8:9], s[70:71] nt\n"
"v_mfma_f32_16x16x32_bf16 v[152:155], v[132:135], v[64:67], v[152:155]\n"
"s_waitcnt lgkmcnt(2)\n"
"v_mfma_f32_16x16x32_bf16 v[144:147], v[140:143], v[68:71], v[144:147]\n"
"v_mfma_f32_16x16x32_bf16 v[148:151], v[140:143], v[72:75], v[148:151]\n"
"v_mfma_f32_16x16x32_bf16 v[152:155], v[140:143], v[76:79], v[152:155]\n"
"s_waitcnt lgkmcnt(0)\n"
"v_mov_b32 v8, 0x3f803f80\n"
"v_mov_b32 v9, 0x3f803f80\n"
"v_mfma_f32_16x16x32_bf16 v[144:147], v[136:139], v[80:83], v[144:147]\n"
"s_barrier\n"
"ds_read_b128 v[132:135], v17 offset:28672\n"
"v_mov_b32 v10, 0x3f803f80\n"
"v_mfma_f32_16x16x32_bf16 v[148:151], v[136:139], v[84:87], v[148:151]\n"
"ds_read_b128 v[140:143], v17 offset:29696\n"
"v_mov_b32 v11, 0x3f803f80\n"
"v_mfma_f32_16x16x32_bf16 v[152:155], v[136:139], v[88:91], v[152:155]\n"
"s_waitcnt lgkmcnt(1)\n"
"v_mfma_f32_16x16x32_bf16 v[136:139], v[8:11], v[132:135], 0\n"
"v_cvt_pk_bf16_f32 v178, v144, v145\n"
"v_cvt_pk_bf16_f32 v179, v146, v147\n"
"v_mfma_f32_16x16x32_bf16 v[144:147], v[182:185], v[132:135], 0\n"
"ds_read_b128 v[156:159], v17 offset:32768\n"
"v_cvt_pk_bf16_f32 v204, v148, v149\n"
"v_mfma_f32_16x16x32_bf16 v[160:163], v[208:211], v[132:135], 0\n"
"v_cvt_pk_bf16_f32 v205, v150, v151\n"
"v_cvt_pk_bf16_f32 v230, v152, v153\n"
"v_mfma_f32_16x16x32_bf16 v[148:151], v[234:237], v[132:135], 0\n"
"s_waitcnt lgkmcnt(1)\n"
"v_cvt_pk_bf16_f32 v231, v154, v155\n"
"v_mfma_f32_16x16x32_bf16 v[136:139], v[8:11], v[140:143], v[136:139]\n"
"v_mfma_f32_16x16x32_bf16 v[144:147], v[190:193], v[140:143], v[144:147]\n"
"ds_read_b128 v[132:135], v17 offset:33792\n"
"v_mfma_f32_16x16x32_bf16 v[160:163], v[216:219], v[140:143], v[160:163]\n"
"ds_read_b64 v[154:155], v17 offset:8704\n"
"v_mfma_f32_16x16x32_bf16 v[148:151], v[242:245], v[140:143], v[148:151]\n"
"s_waitcnt lgkmcnt(2)\n"
"v_mfma_f32_16x16x32_bf16 v[136:139], v[8:11], v[156:159], v[136:139]\n"
"v_mfma_f32_16x16x32_bf16 v[144:147], v[178:181], v[156:159], v[144:147]\n"
"v_mfma_f32_16x16x32_bf16 v[160:163], v[204:207], v[156:159], v[160:163]\n"
"v_mov_b32 v152, 0\n"
"v_mov_b32 v153, 0\n"
"v_mfma_f32_16x16x32_bf16 v[148:151], v[230:233], v[156:159], v[148:151]\n"
"s_waitcnt lgkmcnt(1)\n"
"v_mfma_f32_16x16x32_bf16 v[136:139], v[8:11], v[132:135], v[136:139]\n"
"s_waitcnt lgkmcnt(0)\n"
"v_mfma_f32_16x16x32_bf16 v[136:139], v[8:11], v[152:155], v[136:139]\n"
"ds_read_b128 v[8:11], v14 offset:32256\n"
"v_mfma_f32_16x16x32_bf16 v[148:151], v[252:255], v[132:135], v[148:151]\n"
"v_mfma_f32_16x16x32_bf16 v[148:151], v[244:247], v[152:155], v[148:151]\n"
"ds_read_b128 v[140:143], v17 offset:53248\n"
"v_mfma_f32_16x16x32_bf16 v[144:147], v[200:203], v[132:135], v[144:147]\n"
"ds_read_b128 v[156:159], v14 offset:38400\n"
"v_mfma_f32_16x16x32_bf16 v[144:147], v[192:195], v[152:155], v[144:147]\n"
"v_mfma_f32_16x16x32_bf16 v[160:163], v[226:229], v[132:135], v[160:163]\n"
"ds_read_b128 v[132:135], v17 offset:54272\n"
"v_rcp_f32 v16, v136\n"
"v_add_u32 v136, 0xd4800000, v136\n"
"v_cmp_lt_u32 vcc, 0x28000000, v136\n"
"v_mfma_f32_16x16x32_bf16 v[160:163], v[218:221], v[152:155], v[160:163]\n"
"ds_read_b128 v[136:139], v14 offset:44544\n"
"v_mul_f32 v148, v148, v16\n"
"v_mul_f32 v149, v149, v16\n"
"v_cvt_pk_bf16_f32 v240, v148, v149\n"
"s_waitcnt lgkmcnt(4)\n"
"v_mfma_f32_16x16x32_bf16 v[152:155], v[92:95], v[8:11], 0\n"
"s_or_b64 s[62:63], s[62:63], vcc\n"
"v_mul_f32 v150, v150, v16\n"
"v_mul_f32 v151, v151, v16\n"
"v_mfma_f32_16x16x32_bf16 v[164:167], v[96:99], v[8:11], 0\n"
"s_waitcnt lgkmcnt(3)\n"
"ds_read_b128 v[168:171], v17 offset:55296\n"
"v_cvt_pk_bf16_f32 v241, v150, v151\n"
"v_mfma_f32_16x16x32_bf16 v[148:151], v[140:143], v[8:11], 0\n"
"s_waitcnt lgkmcnt(3)\n"
"ds_read_b128 v[8:11], v14 offset:50688\n"
"v_mul_f32 v144, v144, v16\n"
"v_mfma_f32_16x16x32_bf16 v[152:155], v[100:103], v[156:159], v[152:155]\n"
"v_mul_f32 v145, v145, v16\n"
"v_cvt_pk_bf16_f32 v144, v144, v145\n"
"v_mfma_f32_16x16x32_bf16 v[164:167], v[104:107], v[156:159], v[164:167]\n"
"s_waitcnt lgkmcnt(3)\n"
"ds_read_b128 v[140:143], v17 offset:56320\n"
"v_mul_f32 v146, v146, v16\n"
"v_mfma_f32_16x16x32_bf16 v[148:151], v[132:135], v[156:159], v[148:151]\n"
"s_waitcnt lgkmcnt(3)\n"
"ds_read_b128 v[132:135], v14 offset:2048\n"
"v_mul_f32 v147, v147, v16\n"
"v_cvt_pk_bf16_f32 v145, v146, v147\n"
"v_mfma_f32_16x16x32_bf16 v[152:155], v[0:3], v[136:139], v[152:155]\n"
"v_mul_f32 v160, v160, v16\n"
"v_mul_f32 v161, v161, v16\n"
"v_mfma_f32_16x16x32_bf16 v[164:167], v[108:111], v[136:139], v[164:167]\n"
"s_waitcnt lgkmcnt(3)\n"
"ds_read_b128 v[156:159], v17 offset:57344\n"
"v_cvt_pk_bf16_f32 v146, v160, v161\n"
"v_mul_f32 v162, v162, v16\n"
"v_mfma_f32_16x16x32_bf16 v[148:151], v[168:171], v[136:139], v[148:151]\n"
"s_waitcnt lgkmcnt(3)\n"
"ds_read_b128 v[136:139], v14 offset:3072\n"
"v_mul_f32 v163, v163, v16\n"
"v_mfma_f32_16x16x32_bf16 v[152:155], v[112:115], v[8:11], v[152:155]\n"
"v_cvt_pk_bf16_f32 v147, v162, v163\n"
"v_mfma_f32_16x16x32_bf16 v[164:167], v[116:119], v[8:11], v[164:167]\n"
"s_waitcnt lgkmcnt(3)\n"
"ds_read_b128 v[160:163], v17 offset:58368\n"
"v_mfma_f32_16x16x32_bf16 v[148:151], v[140:143], v[8:11], v[148:151]\n"
"s_waitcnt lgkmcnt(3)\n"
"ds_read_b128 v[8:11], v12 offset:30720\n"
"v_mfma_f32_16x16x32_bf16 v[152:155], v[4:7], v[132:135], v[152:155]\n"
"v_mfma_f32_16x16x32_bf16 v[164:167], v[120:123], v[132:135], v[164:167]\n"
"s_waitcnt lgkmcnt(3)\n"
"v_mfma_f32_16x16x32_bf16 v[148:151], v[156:159], v[132:135], v[148:151]\n"
"s_waitcnt lgkmcnt(2)\n"
"ds_read_b128 v[132:135], v13 offset:30720\n"
"v_bfe_u32 v16, v14, 3, 5\n"
"v_mfma_f32_16x16x32_bf16 v[152:155], v[124:127], v[136:139], v[152:155]\n"
"ds_read_u16 v16, v16 offset:1088\n"
"v_mfma_f32_16x16x32_bf16 v[164:167], v[128:131], v[136:139], v[164:167]\n"
"s_barrier\n"
"s_waitcnt lgkmcnt(3)\n"
"v_mfma_f32_16x16x32_bf16 v[148:151], v[160:163], v[136:139], v[148:151]\n"
"s_waitcnt lgkmcnt(2)\n"
"ds_read_b128 v[136:139], v12 offset:32768\n"
"v_mfma_f32_16x16x32_bf16 v[140:143], v[8:11], v[24:27], 0\n"
"ds_write_b128 v17, v[144:147] offset:7680\n"
"v_mfma_f32_16x16x32_bf16 v[24:27], v[8:11], v[28:31], 0\n"
"v_cmp_class_f32_e64 vcc, v152, s64\n"
"v_cvt_pk_bf16_f32 v152, v152, v153\n"
"v_mfma_f32_16x16x32_bf16 v[28:31], v[8:11], v[20:23], 0\n"
"s_waitcnt lgkmcnt(3)\n"
"ds_read_b128 v[8:11], v13 offset:32768\n"
"v_cvt_pk_bf16_f32 v153, v154, v155\n"
"v_mfma_f32_16x16x32_bf16 v[140:143], v[132:135], v[36:39], v[140:143]\n"
"s_or_b64 s[62:63], s[62:63], vcc\n"
"v_cvt_pk_bf16_f32 v154, v164, v165\n"
"s_waitcnt lgkmcnt(3)\n"
"v_mad_u32_u24 v15, v16, s40, v18\n"
"v_mfma_f32_16x16x32_bf16 v[24:27], v[132:135], v[40:43], v[24:27]\n"
"v_cvt_pk_bf16_f32 v155, v166, v167\n"
"v_cvt_pk_bf16_f32 v148, v148, v149\n"
"v_mfma_f32_16x16x32_bf16 v[28:31], v[132:135], v[32:35], v[28:31]\n"
"global_store_dwordx4 v15, v[152:155], s[68:69] nt\n"
"s_waitcnt lgkmcnt(2)\n"
"v_mfma_f32_16x16x32_bf16 v[140:143], v[136:139], v[44:47], v[140:143]\n"
"ds_read_b128 v[20:23], v12 offset:34816\n"
"v_add_u32 v149, s54, v14\n"
"v_mfma_f32_16x16x32_bf16 v[24:27], v[136:139], v[48:51], v[24:27]\n"
"ds_write_b64 v149, v[240:241] offset:0\n"
"v_mfma_f32_16x16x32_bf16 v[28:31], v[136:139], v[52:55], v[28:31]\n"
"s_waitcnt lgkmcnt(2)\n"
"ds_read_b128 v[32:35], v13 offset:34816\n"
"v_mad_u32_u24 v16, v16, s40, v19\n"
"v_mfma_f32_16x16x32_bf16 v[140:143], v[8:11], v[56:59], v[140:143]\n"
"v_cvt_pk_bf16_f32 v149, v150, v151\n"
"v_mfma_f32_16x16x32_bf16 v[24:27], v[8:11], v[60:63], v[24:27]\n"
"global_store_dwordx2 v16, v[148:149], s[70:71] nt\n"
"v_mfma_f32_16x16x32_bf16 v[28:31], v[8:11], v[64:67], v[28:31]\n"
"s_waitcnt lgkmcnt(2)\n"
"v_mfma_f32_16x16x32_bf16 v[140:143], v[20:23], v[68:71], v[140:143]\n"
"v_mfma_f32_16x16x32_bf16 v[24:27], v[20:23], v[72:75], v[24:27]\n"
"v_mfma_f32_16x16x32_bf16 v[28:31], v[20:23], v[76:79], v[28:31]\n"
"s_waitcnt lgkmcnt(0)\n"
"v_mov_b32 v8, 0x3f803f80\n"
"v_mov_b32 v9, 0x3f803f80\n"
"v_mfma_f32_16x16x32_bf16 v[140:143], v[32:35], v[80:83], v[140:143]\n"
"s_barrier\n"
"ds_read_b128 v[20:23], v17 offset:30720\n"
"v_mov_b32 v10, 0x3f803f80\n"
"v_mfma_f32_16x16x32_bf16 v[24:27], v[32:35], v[84:87], v[24:27]\n"
"ds_read_b128 v[36:39], v17 offset:32768\n"
"v_mov_b32 v11, 0x3f803f80\n"
"v_mfma_f32_16x16x32_bf16 v[28:31], v[32:35], v[88:91], v[28:31]\n"
"s_waitcnt lgkmcnt(1)\n"
"v_mfma_f32_16x16x32_bf16 v[32:35], v[8:11], v[20:23], 0\n"
"v_cvt_pk_bf16_f32 v180, v140, v141\n"
"v_cvt_pk_bf16_f32 v181, v142, v143\n"
"v_mfma_f32_16x16x32_bf16 v[40:43], v[182:185], v[20:23], 0\n"
"ds_read_b128 v[44:47], v17 offset:33792\n"
"v_cvt_pk_bf16_f32 v206, v24, v25\n"
"v_mfma_f32_16x16x32_bf16 v[48:51], v[208:211], v[20:23], 0\n"
"v_cvt_pk_bf16_f32 v207, v26, v27\n"
"v_cvt_pk_bf16_f32 v232, v28, v29\n"
"v_mfma_f32_16x16x32_bf16 v[24:27], v[234:237], v[20:23], 0\n"
"s_waitcnt lgkmcnt(1)\n"
"v_cvt_pk_bf16_f32 v233, v30, v31\n"
"v_mfma_f32_16x16x32_bf16 v[32:35], v[8:11], v[36:39], v[32:35]\n"
"v_mfma_f32_16x16x32_bf16 v[40:43], v[178:181], v[36:39], v[40:43]\n"
"ds_read_b128 v[54:57], v17 offset:31744\n"
"v_mfma_f32_16x16x32_bf16 v[48:51], v[204:207], v[36:39], v[48:51]\n"
"ds_read_b64 v[22:23], v17 offset:8712\n"
"v_mfma_f32_16x16x32_bf16 v[24:27], v[230:233], v[36:39], v[24:27]\n"
"s_waitcnt lgkmcnt(2)\n"
"v_mfma_f32_16x16x32_bf16 v[32:35], v[8:11], v[44:47], v[32:35]\n"
"v_mfma_f32_16x16x32_bf16 v[40:43], v[192:195], v[44:47], v[40:43]\n"
"v_mfma_f32_16x16x32_bf16 v[48:51], v[218:221], v[44:47], v[48:51]\n"
"v_mov_b32 v20, 0\n"
"v_mov_b32 v21, 0\n"
"v_mfma_f32_16x16x32_bf16 v[24:27], v[244:247], v[44:47], v[24:27]\n"
"s_waitcnt lgkmcnt(1)\n"
"v_mov_b32 v52, 0\n"
"v_mov_b32 v53, 0\n"
"v_mfma_f32_16x16x32_bf16 v[32:35], v[8:11], v[54:57], v[32:35]\n"
"s_waitcnt lgkmcnt(0)\n"
"v_mov_b32 v58, 0\n"
"v_mov_b32 v59, 0\n"
"v_mfma_f32_16x16x32_bf16 v[32:35], v[8:11], v[20:23], v[32:35]\n"
"v_mfma_f32_16x16x32_bf16 v[24:27], v[236:239], v[52:55], v[24:27]\n"
"v_mfma_f32_16x16x32_bf16 v[24:27], v[248:251], v[56:59], v[24:27]\n"
"v_mfma_f32_16x16x32_bf16 v[24:27], v[252:255], v[20:23], v[24:27]\n"
"ds_read_b128 v[234:237], v14 offset:32256\n"
"v_mfma_f32_16x16x32_bf16 v[40:43], v[184:187], v[52:55], v[40:43]\n"
"v_mfma_f32_16x16x32_bf16 v[40:43], v[196:199], v[56:59], v[40:43]\n"
"ds_read_b128 v[182:185], v17 offset:53248\n"
"v_mfma_f32_16x16x32_bf16 v[40:43], v[200:203], v[20:23], v[40:43]\n"
"ds_read_b128 v[186:189], v14 offset:38400\n"
"v_rcp_f32 v16, v32\n"
"v_mfma_f32_16x16x32_bf16 v[48:51], v[210:213], v[52:55], v[48:51]\n"
"v_add_u32 v32, 0xd4800000, v32\n"
"v_cmp_lt_u32 vcc, 0x28000000, v32\n"
"v_mfma_f32_16x16x32_bf16 v[48:51], v[222:225], v[56:59], v[48:51]\n"
"ds_read_b128 v[196:199], v17 offset:54272\n"
"v_mul_f32 v24, v24, v16\n"
"v_mfma_f32_16x16x32_bf16 v[48:51], v[226:229], v[20:23], v[48:51]\n"
"s_or_b64 s[62:63], s[62:63], vcc\n"
"ds_read_b128 v[208:211], v14 offset:44544\n"
"v_mul_f32 v25, v25, v16\n"
"v_cvt_pk_bf16_f32 v240, v24, v25\n"
"s_waitcnt lgkmcnt(4)\n"
"v_mfma_f32_16x16x32_bf16 v[212:215], v[92:95], v[234:237], 0\n"
"v_mul_f32 v26, v26, v16\n"
"v_mul_f32 v27, v27, v16\n"
"v_mfma_f32_16x16x32_bf16 v[222:225], v[96:99], v[234:237], 0\n"
"s_waitcnt lgkmcnt(3)\n"
"ds_read_b128 v[248:251], v17 offset:55296\n"
"v_cvt_pk_bf16_f32 v241, v26, v27\n"
"v_mfma_f32_16x16x32_bf16 v[8:11], v[182:185], v[234:237], 0\n"
"s_waitcnt lgkmcnt(3)\n"
"ds_read_b128 v[182:185], v14 offset:50688\n"
"v_mul_f32 v40, v40, v16\n"
"v_mfma_f32_16x16x32_bf16 v[212:215], v[100:103], v[186:189], v[212:215]\n"
"v_mul_f32 v41, v41, v16\n"
"v_cvt_pk_bf16_f32 v40, v40, v41\n"
"v_mfma_f32_16x16x32_bf16 v[222:225], v[104:107], v[186:189], v[222:225]\n"
"s_waitcnt lgkmcnt(3)\n"
"ds_read_b128 v[234:237], v17 offset:56320\n"
"v_mul_f32 v42, v42, v16\n"
"v_mul_f32 v43, v43, v16\n"
"v_mfma_f32_16x16x32_bf16 v[8:11], v[196:199], v[186:189], v[8:11]\n"
"s_waitcnt lgkmcnt(3)\n"
"ds_read_b128 v[186:189], v14 offset:2048\n"
"v_cvt_pk_bf16_f32 v41, v42, v43\n"
"v_mfma_f32_16x16x32_bf16 v[212:215], v[0:3], v[208:211], v[212:215]\n"
"v_mul_f32 v48, v48, v16\n"
"v_mul_f32 v49, v49, v16\n"
"v_mfma_f32_16x16x32_bf16 v[222:225], v[108:111], v[208:211], v[222:225]\n"
"s_waitcnt lgkmcnt(3)\n"
"ds_read_b128 v[196:199], v17 offset:57344\n"
"v_cvt_pk_bf16_f32 v42, v48, v49\n"
"v_mul_f32 v50, v50, v16\n"
"v_mfma_f32_16x16x32_bf16 v[8:11], v[248:251], v[208:211], v[8:11]\n"
"s_waitcnt lgkmcnt(3)\n"
"ds_read_b128 v[208:211], v14 offset:3072\n"
"v_mul_f32 v51, v51, v16\n"
"v_mfma_f32_16x16x32_bf16 v[212:215], v[112:115], v[182:185], v[212:215]\n"
"v_cvt_pk_bf16_f32 v43, v50, v51\n"
"v_mfma_f32_16x16x32_bf16 v[222:225], v[116:119], v[182:185], v[222:225]\n"
"s_waitcnt lgkmcnt(3)\n"
"ds_read_b128 v[248:251], v17 offset:58368\n"
"v_add_u32 v16, s54, v14\n"
"v_mfma_f32_16x16x32_bf16 v[8:11], v[234:237], v[182:185], v[8:11]\n"
"s_waitcnt lgkmcnt(3)\n"
"v_mfma_f32_16x16x32_bf16 v[212:215], v[4:7], v[186:189], v[212:215]\n"
"v_mfma_f32_16x16x32_bf16 v[222:225], v[120:123], v[186:189], v[222:225]\n"
"s_waitcnt lgkmcnt(2)\n"
"v_mfma_f32_16x16x32_bf16 v[8:11], v[196:199], v[186:189], v[8:11]\n"
"s_waitcnt lgkmcnt(1)\n"
"v_bfe_u32 v15, v14, 3, 5\n"
"v_mov_b32 v182, 0x3f803f80\n"
"v_mov_b32 v183, 0x3f803f80\n"
"v_mfma_f32_16x16x32_bf16 v[212:215], v[124:127], v[208:211], v[212:215]\n"
"ds_read_u16 v15, v15 offset:1120\n"
"v_mov_b32 v184, 0x3f803f80\n"
"v_mfma_f32_16x16x32_bf16 v[222:225], v[128:131], v[208:211], v[222:225]\n"
"s_barrier\n"
"ds_write_b64 v16, v[240:241] offset:0\n"
"ds_write_b128 v17, v[40:43] offset:7680\n"
"s_waitcnt lgkmcnt(3)\n"
"v_mfma_f32_16x16x32_bf16 v[8:11], v[248:251], v[208:211], v[8:11]\n"
"v_mov_b32 v185, 0x3f803f80\n"
"v_cmp_class_f32_e64 vcc, v212, s64\n"
"v_cvt_pk_bf16_f32 v212, v212, v213\n"
"v_cvt_pk_bf16_f32 v213, v214, v215\n"
"v_cvt_pk_bf16_f32 v214, v222, v223\n"
"v_cvt_pk_bf16_f32 v215, v224, v225\n"
"s_or_b64 s[62:63], s[62:63], vcc\n"
"s_waitcnt lgkmcnt(0)\n"
"s_barrier\n"
"ds_read_b128 v[186:189], v17 offset:28672\n"
"v_cvt_pk_bf16_f32 v8, v8, v9\n"
"v_cvt_pk_bf16_f32 v9, v10, v11\n"
"v_mad_u32_u24 v16, v15, s40, v18\n"
"v_mad_u32_u24 v15, v15, s40, v19\n"
"ds_read_b128 v[196:199], v17 offset:29696\n"
"ds_read_b128 v[236:239], v17 offset:32768\n"
"global_store_dwordx4 v16, v[212:215], s[68:69] nt\n"
"s_waitcnt lgkmcnt(2)\n"
"v_mfma_f32_16x16x32_bf16 v[208:211], v[182:185], v[186:189], 0\n"
"v_mfma_f32_16x16x32_bf16 v[212:215], v[190:193], v[186:189], 0\n"
"v_mfma_f32_16x16x32_bf16 v[222:225], v[216:219], v[186:189], 0\n"
"v_mfma_f32_16x16x32_bf16 v[248:251], v[242:245], v[186:189], 0\n"
"s_waitcnt lgkmcnt(1)\n"
"v_mfma_f32_16x16x32_bf16 v[208:211], v[182:185], v[196:199], v[208:211]\n"
"s_waitcnt lgkmcnt(0)\n"
"v_mov_b32 v234, 0\n"
"v_mov_b32 v235, 0\n"
"v_mfma_f32_16x16x32_bf16 v[208:211], v[182:185], v[236:239], v[208:211]\n"
"v_mov_b32 v240, 0\n"
"v_mov_b32 v241, 0\n"
"v_mfma_f32_16x16x32_bf16 v[248:251], v[252:255], v[196:199], v[248:251]\n"
"global_store_dwordx2 v15, v[8:9], s[70:71] nt\n"
"v_mfma_f32_16x16x32_bf16 v[248:251], v[244:247], v[234:237], v[248:251]\n"
"v_mfma_f32_16x16x32_bf16 v[248:251], v[230:233], v[238:241], v[248:251]\n"
"ds_read_b128 v[182:185], v14 offset:32256\n"
"v_mfma_f32_16x16x32_bf16 v[212:215], v[200:203], v[196:199], v[212:215]\n"
"v_rcp_f32 v16, v208\n"
"v_mfma_f32_16x16x32_bf16 v[212:215], v[192:195], v[234:237], v[212:215]\n"
"ds_read_b128 v[186:189], v17 offset:53248\n"
"v_add_u32 v208, 0xd4800000, v208\n"
"v_mfma_f32_16x16x32_bf16 v[212:215], v[178:181], v[238:241], v[212:215]\n"
"ds_read_b128 v[178:181], v14 offset:38400\n"
"v_cmp_lt_u32 vcc, 0x28000000, v208\n"
"v_mfma_f32_16x16x32_bf16 v[222:225], v[226:229], v[196:199], v[222:225]\n"
"v_mul_f32 v248, v248, v16\n"
"v_mul_f32 v249, v249, v16\n"
"v_mfma_f32_16x16x32_bf16 v[222:225], v[218:221], v[234:237], v[222:225]\n"
"s_or_b64 s[62:63], s[62:63], vcc\n"
"ds_read_b128 v[190:193], v17 offset:54272\n"
"v_cvt_pk_bf16_f32 v12, v248, v249\n"
"v_mfma_f32_16x16x32_bf16 v[222:225], v[204:207], v[238:241], v[222:225]\n"
"ds_read_b128 v[194:197], v14 offset:44544\n"
"v_mul_f32 v250, v250, v16\n"
"v_mul_f32 v251, v251, v16\n"
"v_cvt_pk_bf16_f32 v13, v250, v251\n"
"s_waitcnt lgkmcnt(4)\n"
"v_mfma_f32_16x16x32_bf16 v[198:201], v[92:95], v[182:185], 0\n"
"v_mul_f32 v212, v212, v16\n"
"v_mul_f32 v213, v213, v16\n"
"v_mfma_f32_16x16x32_bf16 v[202:205], v[96:99], v[182:185], 0\n"
"s_waitcnt lgkmcnt(3)\n"
"ds_read_b128 v[206:209], v17 offset:55296\n"
"v_cvt_pk_bf16_f32 v212, v212, v213\n"
"v_mfma_f32_16x16x32_bf16 v[216:219], v[186:189], v[182:185], 0\n"
"s_waitcnt lgkmcnt(3)\n"
"ds_read_b128 v[182:185], v14 offset:50688\n"
"v_mul_f32 v214, v214, v16\n"
"v_mfma_f32_16x16x32_bf16 v[198:201], v[100:103], v[178:181], v[198:201]\n"
"v_mul_f32 v215, v215, v16\n"
"v_cvt_pk_bf16_f32 v213, v214, v215\n"
"v_mfma_f32_16x16x32_bf16 v[202:205], v[104:107], v[178:181], v[202:205]\n"
"s_waitcnt lgkmcnt(3)\n"
"ds_read_b128 v[186:189], v17 offset:56320\n"
"v_mul_f32 v222, v222, v16\n"
"v_mfma_f32_16x16x32_bf16 v[216:219], v[190:193], v[178:181], v[216:219]\n"
"s_waitcnt lgkmcnt(3)\n"
"ds_read_b128 v[178:181], v14 offset:2048\n"
"v_mul_f32 v223, v223, v16\n"
"v_mfma_f32_16x16x32_bf16 v[198:201], v[0:3], v[194:197], v[198:201]\n"
"v_cvt_pk_bf16_f32 v214, v222, v223\n"
"v_mul_f32 v224, v224, v16\n"
"v_mfma_f32_16x16x32_bf16 v[202:205], v[108:111], v[194:197], v[202:205]\n"
"ds_read_b128 v[190:193], v17 offset:57344\n"
"v_mul_f32 v225, v225, v16\n"
"v_add_u32 v16, s54, v14\n"
"v_cvt_pk_bf16_f32 v215, v224, v225\n"
"s_waitcnt lgkmcnt(4)\n"
"v_mfma_f32_16x16x32_bf16 v[216:219], v[206:209], v[194:197], v[216:219]\n"
"s_waitcnt lgkmcnt(3)\n"
"ds_read_b128 v[194:197], v14 offset:3072\n"
"v_mfma_f32_16x16x32_bf16 v[198:201], v[112:115], v[182:185], v[198:201]\n"
"v_mfma_f32_16x16x32_bf16 v[202:205], v[116:119], v[182:185], v[202:205]\n"
"s_waitcnt lgkmcnt(3)\n"
"ds_read_b128 v[206:209], v17 offset:58368\n"
"v_mfma_f32_16x16x32_bf16 v[216:219], v[186:189], v[182:185], v[216:219]\n"
"s_waitcnt lgkmcnt(3)\n"
"v_mfma_f32_16x16x32_bf16 v[198:201], v[4:7], v[178:181], v[198:201]\n"
"v_mfma_f32_16x16x32_bf16 v[202:205], v[120:123], v[178:181], v[202:205]\n"
"s_waitcnt lgkmcnt(2)\n"
"ds_read_b128 v[182:185], v17 offset:53248\n"
"v_mfma_f32_16x16x32_bf16 v[216:219], v[190:193], v[178:181], v[216:219]\n"
"s_waitcnt lgkmcnt(2)\n"
"v_bfe_u32 v15, v14, 3, 5\n"
"ds_read_u16 v15, v15 offset:1152\n"
"v_mfma_f32_16x16x32_bf16 v[198:201], v[124:127], v[194:197], v[198:201]\n"
"v_mfma_f32_16x16x32_bf16 v[202:205], v[128:131], v[194:197], v[202:205]\n"
"s_barrier\n"
"ds_write_b64 v16, v[12:13] offset:0\n"
"ds_write_b128 v17, v[212:215] offset:7680\n"
"s_waitcnt lgkmcnt(4)\n"
"v_mfma_f32_16x16x32_bf16 v[216:219], v[206:209], v[194:197], v[216:219]\n"
"s_waitcnt lgkmcnt(0)\n"
"ds_read_b128 v[178:181], v17 offset:54272\n"
"s_barrier\n"
"ds_read_b128 v[186:189], v14 offset:32256\n"
"v_cmp_class_f32_e64 vcc, v198, s64\n"
"v_cvt_pk_bf16_f32 v198, v198, v199\n"
"v_cvt_pk_bf16_f32 v199, v200, v201\n"
"v_mad_u32_u24 v16, v15, s40, v18\n"
"v_cvt_pk_bf16_f32 v200, v202, v203\n"
"v_cvt_pk_bf16_f32 v201, v204, v205\n"
"s_or_b64 s[62:63], s[62:63], vcc\n"
"v_cvt_pk_bf16_f32 v216, v216, v217\n"
"v_mad_u32_u24 v15, v15, s40, v19\n"
"v_cvt_pk_bf16_f32 v217, v218, v219\n"
"ds_read_b128 v[190:193], v14 offset:38400\n"
"ds_read_b128 v[194:197], v14 offset:44544\n"
"global_store_dwordx4 v16, v[198:201], s[68:69] nt\n"
"s_waitcnt lgkmcnt(2)\n"
"s_nop 0\n"
"v_mfma_f32_16x16x32_bf16 v[198:201], v[92:95], v[186:189], 0\n"
"v_mfma_f32_16x16x32_bf16 v[202:205], v[96:99], v[186:189], 0\n"
"ds_read_b128 v[206:209], v17 offset:55296\n"
"v_mfma_f32_16x16x32_bf16 v[210:213], v[182:185], v[186:189], 0\n"
"s_waitcnt lgkmcnt(2)\n"
"ds_read_b128 v[182:185], v14 offset:50688\n"
"v_mfma_f32_16x16x32_bf16 v[198:201], v[100:103], v[190:193], v[198:201]\n"
"v_mfma_f32_16x16x32_bf16 v[202:205], v[104:107], v[190:193], v[202:205]\n"
"ds_read_b128 v[186:189], v17 offset:56320\n"
"v_mfma_f32_16x16x32_bf16 v[210:213], v[178:181], v[190:193], v[210:213]\n"
"s_waitcnt lgkmcnt(3)\n"
"ds_read_b128 v[178:181], v14 offset:2048\n"
"v_mfma_f32_16x16x32_bf16 v[198:201], v[0:3], v[194:197], v[198:201]\n"
"global_store_dwordx2 v15, v[216:217], s[70:71] nt\n"
"v_mfma_f32_16x16x32_bf16 v[202:205], v[108:111], v[194:197], v[202:205]\n"
"s_waitcnt lgkmcnt(3)\n"
"ds_read_b128 v[190:193], v17 offset:57344\n"
"v_mfma_f32_16x16x32_bf16 v[210:213], v[206:209], v[194:197], v[210:213]\n"
"s_waitcnt lgkmcnt(3)\n"
"ds_read_b128 v[194:197], v14 offset:3072\n"
"v_mfma_f32_16x16x32_bf16 v[198:201], v[112:115], v[182:185], v[198:201]\n"
"v_mfma_f32_16x16x32_bf16 v[202:205], v[116:119], v[182:185], v[202:205]\n"
"s_waitcnt lgkmcnt(3)\n"
"ds_read_b128 v[206:209], v17 offset:58368\n"
"v_mfma_f32_16x16x32_bf16 v[210:213], v[186:189], v[182:185], v[210:213]\n"
"s_waitcnt lgkmcnt(3)\n"
"v_mfma_f32_16x16x32_bf16 v[198:201], v[4:7], v[178:181], v[198:201]\n"
"v_mfma_f32_16x16x32_bf16 v[202:205], v[120:123], v[178:181], v[202:205]\n"
"s_waitcnt lgkmcnt(2)\n"
"v_mfma_f32_16x16x32_bf16 v[210:213], v[190:193], v[178:181], v[210:213]\n"
"s_waitcnt lgkmcnt(1)\n"
"v_bfe_u32 v215, v14, 3, 5\n"
"v_mfma_f32_16x16x32_bf16 v[198:201], v[124:127], v[194:197], v[198:201]\n"
"ds_read_u16 v215, v215 offset:1184\n"
"v_mfma_f32_16x16x32_bf16 v[202:205], v[128:131], v[194:197], v[202:205]\n"
"s_waitcnt lgkmcnt(1)\n"
"v_mfma_f32_16x16x32_bf16 v[210:213], v[206:209], v[194:197], v[210:213]\n"
"s_waitcnt lgkmcnt(0)\n"
"v_mad_u32_u24 v214, v215, s40, v18\n"
"v_mad_u32_u24 v215, v215, s40, v19\n"
"s_nop 0\n"
"v_cmp_class_f32_e64 vcc, v198, s64\n"
"s_or_b64 s[62:63], s[62:63], vcc\n"
"s_cmp_lg_u64 s[62:63], 0\n"
"s_cselect_b32 s41, 1, 0\n"
"v_mov_b32 v208, s65\n"
"v_mov_b32 v209, s41\n"
"ds_write_b32 v208, v209 offset:0\n"
"v_cvt_pk_bf16_f32 v198, v198, v199\n"
"v_cvt_pk_bf16_f32 v199, v200, v201\n"
"v_cvt_pk_bf16_f32 v200, v202, v203\n"
"v_cvt_pk_bf16_f32 v201, v204, v205\n"
"v_cvt_pk_bf16_f32 v210, v210, v211\n"
"v_cvt_pk_bf16_f32 v211, v212, v213\n"
"global_store_dwordx4 v214, v[198:201], s[68:69] nt\n"
"global_store_dwordx2 v215, v[210:211], s[70:71] nt\n"
"s_waitcnt lgkmcnt(0)\n"
"s_barrier\n"
  );
}
DI void waveA_safe(const uint16_t* wqkv, const uint16_t* wo, const uint16_t* rpb, const Geo& G, int h) {
  A_HEAD;
  A_TABLES(); A_SMALL(); RAWB(); SB;
  DMA(wq_ + 384, lo3[0], sW + (0)); SB;
  DMA(wq_ + 384, lo3[1], sW + (1024)); SB;
  DMA(wq_ + 384, lo3[2], sW + (2048)); SB;
  DMA(wq_ + 37248, lo3[0], sW + (3072)); SB;
  DMA(wq_ + 37248, lo3[1], sW + (4096)); SB;
  DMA(wq_ + 37248, lo3[2], sW + (5120)); SB;
  DMA(wq_ + 74112, lo3[0], sW + (-24576)); SB;
  DMA(wq_ + 74112, lo3[1], sW + (-23552)); SB;
  DMA(wq_ + 74112, lo3[2], sW + (-22528)); SB;
  DMA(wq_ + 110976, lo3[0], sW + (-21504)); SB;
  DMA(wq_ + 110976, lo3[1], sW + (-20480)); SB;
  DMA(wq_ + 110976, lo3[2], sW + (-19456)); SB;
  DMA(wq_ + 147840, lo3[0], sW + (110592)); SB;
  DMA(wq_ + 147840, lo3[1], sW + (111616)); SB;
  DMA(wq_ + 147840, lo3[2], sW + (112640)); SB;
  DMA(wq_ + 184704, lo3[0], sW + (113664)); SB;
  DMA(wq_ + 184704, lo3[1], sW + (114688)); SB;
  DMA(wq_ + 184704, lo3[2], sW + (115712)); SB;
  ASM_A(
"s_setprio 1\n"
"s_mov_b32 s60, 0x3e553b95\n"
"s_mov_b32 s61, 0x3fb8aa3b\n"
"v_add_u32 v11, 77824, v3\n"
"v_mov_b32 v24, 0\n"
"v_mov_b32 v25, 0\n"
"s_barrier\n"
"s_waitcnt vmcnt(15)\n"
"ds_read_b64_tr_b16 v[28:29], v3 offset:28672\n"
"ds_read_b64_tr_b16 v[30:31], v3 offset:29440\n"
"s_waitcnt vmcnt(12)\n"
"ds_read_b128 v[178:181], v4 offset:0\n"
"ds_read_b64_tr_b16 v[32:33], v3 offset:28704\n"
"ds_read_b64_tr_b16 v[34:35], v3 offset:29472\n"
"ds_read_b64_tr_b16 v[36:37], v3 offset:28736\n"
"ds_read_b64_tr_b16 v[38:39], v3 offset:29504\n"
"ds_read_b128 v[182:185], v4 offset:6144\n"
"ds_read_b128 v[186:189], v4 offset:12288\n"
"ds_read_b64_tr_b16 v[40:41], v3 offset:31744\n"
"ds_read_b64_tr_b16 v[42:43], v3 offset:32512\n"
"ds_read_b64_tr_b16 v[44:45], v3 offset:31776\n"
"ds_read_b64_tr_b16 v[46:47], v3 offset:32544\n"
"ds_read_b64_tr_b16 v[48:49], v3 offset:31808\n"
"ds_read_b64_tr_b16 v[50:51], v3 offset:32576\n"
"ds_read_b128 v[190:193], v4 offset:18432\n"
"s_waitcnt lgkmcnt(13)\n"
"v_mfma_f32_16x16x32_bf16 v[196:199], v[28:31], v[178:181], 0\n"
"s_waitcnt lgkmcnt(11)\n"
"v_mfma_f32_16x16x32_bf16 v[212:215], v[32:35], v[178:181], 0\n"
"s_waitcnt lgkmcnt(9)\n"
"v_mfma_f32_16x16x32_bf16 v[216:219], v[36:39], v[178:181], 0\n"
"s_waitcnt lgkmcnt(8)\n"
"ds_read_b128 v[220:223], v4 offset:24576\n"
"v_mfma_f32_16x16x32_bf16 v[202:205], v[28:31], v[182:185], 0\n"
"v_mfma_f32_16x16x32_bf16 v[236:239], v[32:35], v[182:185], 0\n"
"v_mfma_f32_16x16x32_bf16 v[240:243], v[36:39], v[182:185], 0\n"
"s_waitcnt lgkmcnt(8)\n"
"ds_read_b128 v[244:247], v4 offset:30720\n"
"v_mfma_f32_16x16x32_bf16 v[226:229], v[28:31], v[186:189], 0\n"
"v_mfma_f32_16x16x32_bf16 v[248:251], v[32:35], v[186:189], 0\n"
"v_mfma_f32_16x16x32_bf16 v[252:255], v[36:39], v[186:189], 0\n"
"s_waitcnt lgkmcnt(2)\n"
"ds_read_b128 v[64:67], v5 offset:0\n"
"v_mfma_f32_16x16x32_bf16 v[232:235], v[28:31], v[190:193], 0\n"
"v_mfma_f32_16x16x32_bf16 v[182:185], v[32:35], v[190:193], 0\n"
"v_mfma_f32_16x16x32_bf16 v[100:103], v[36:39], v[190:193], 0\n"
"s_waitcnt lgkmcnt(2)\n"
"ds_read_b128 v[68:71], v5 offset:6144\n"
"v_mfma_f32_16x16x32_bf16 v[208:211], v[28:31], v[220:223], 0\n"
"s_waitcnt vmcnt(9)\n"
"ds_read_b64_tr_b16 v[52:53], v3 offset:4096\n"
"ds_read_b64_tr_b16 v[54:55], v3 offset:4864\n"
"v_mfma_f32_16x16x32_bf16 v[186:189], v[32:35], v[220:223], 0\n"
"ds_read_b64_tr_b16 v[56:57], v3 offset:4128\n"
"ds_read_b64_tr_b16 v[58:59], v3 offset:4896\n"
"v_mfma_f32_16x16x32_bf16 v[190:193], v[36:39], v[220:223], 0\n"
"s_waitcnt lgkmcnt(6)\n"
"ds_read_b64_tr_b16 v[60:61], v3 offset:4160\n"
"ds_read_b64_tr_b16 v[62:63], v3 offset:4928\n"
"v_mfma_f32_16x16x32_bf16 v[178:181], v[28:31], v[244:247], 0\n"
"ds_read_b128 v[76:79], v5 offset:12288\n"
"v_mfma_f32_16x16x32_bf16 v[104:107], v[32:35], v[244:247], 0\n"
"v_mfma_f32_16x16x32_bf16 v[108:111], v[36:39], v[244:247], 0\n"
"s_waitcnt lgkmcnt(8)\n"
"ds_read_b128 v[220:223], v5 offset:18432\n"
"v_mfma_f32_16x16x32_bf16 v[196:199], v[40:43], v[64:67], v[196:199]\n"
"v_mfma_f32_16x16x32_bf16 v[212:215], v[44:47], v[64:67], v[212:215]\n"
"v_mfma_f32_16x16x32_bf16 v[216:219], v[48:51], v[64:67], v[216:219]\n"
"s_waitcnt lgkmcnt(8)\n"
"ds_read_b128 v[80:83], v5 offset:24576\n"
"v_mfma_f32_16x16x32_bf16 v[202:205], v[40:43], v[68:71], v[202:205]\n"
"v_mfma_f32_16x16x32_bf16 v[236:239], v[44:47], v[68:71], v[236:239]\n"
"v_mfma_f32_16x16x32_bf16 v[240:243], v[48:51], v[68:71], v[240:243]\n"
"s_waitcnt lgkmcnt(2)\n"
"ds_read_b128 v[244:247], v5 offset:30720\n"
"v_mfma_f32_16x16x32_bf16 v[226:229], v[40:43], v[76:79], v[226:229]\n"
"s_waitcnt vmcnt(6)\n"
"ds_read_b64_tr_b16 v[64:65], v3 offset:7168\n"
"ds_read_b64_tr_b16 v[66:67], v3 offset:7936\n"
"v_mfma_f32_16x16x32_bf16 v[248:251], v[44:47], v[76:79], v[248:251]\n"
"ds_read_b64_tr_b16 v[68:69], v3 offset:7200\n"
"ds_read_b64_tr_b16 v[70:71], v3 offset:7968\n"
"v_mfma_f32_16x16x32_bf16 v[252:255], v[48:51], v[76:79], v[252:255]\n"
"s_waitcnt lgkmcnt(6)\n"
"ds_read_b64_tr_b16 v[72:73], v3 offset:7232\n"
"ds_read_b64_tr_b16 v[74:75], v3 offset:8000\n"
"v_mfma_f32_16x16x32_bf16 v[232:235], v[40:43], v[220:223], v[232:235]\n"
"ds_read_b128 v[88:91], v4 offset:2048\n"
"v_mfma_f32_16x16x32_bf16 v[182:185], v[44:47], v[220:223], v[182:185]\n"
"v_mfma_f32_16x16x32_bf16 v[100:103], v[48:51], v[220:223], v[100:103]\n"
"s_waitcnt lgkmcnt(8)\n"
"ds_read_b128 v[92:95], v4 offset:8192\n"
"v_mfma_f32_16x16x32_bf16 v[208:211], v[40:43], v[80:83], v[208:211]\n"
"v_mfma_f32_16x16x32_bf16 v[186:189], v[44:47], v[80:83], v[186:189]\n"
"v_mfma_f32_16x16x32_bf16 v[190:193], v[48:51], v[80:83], v[190:193]\n"
"s_waitcnt lgkmcnt(8)\n"
"ds_read_b128 v[96:99], v4 offset:14336\n"
"v_mfma_f32_16x16x32_bf16 v[178:181], v[40:43], v[244:247], v[178:181]\n"
"v_mfma_f32_16x16x32_bf16 v[104:107], v[44:47], v[244:247], v[104:107]\n"
"v_mfma_f32_16x16x32_bf16 v[108:111], v[48:51], v[244:247], v[108:111]\n"
"s_waitcnt lgkmcnt(2)\n"
"ds_read_b128 v[220:223], v4 offset:20480\n"
"v_mfma_f32_16x16x32_bf16 v[196:199], v[52:55], v[88:91], v[196:199]\n"
"s_waitcnt vmcnt(3)\n"
"ds_read_b64_tr_b16 v[76:77], v11 offset:61440\n"
"ds_read_b64_tr_b16 v[78:79], v11 offset:62208\n"
"v_mfma_f32_16x16x32_bf16 v[212:215], v[56:59], v[88:91], v[212:215]\n"
"ds_read_b64_tr_b16 v[80:81], v11 offset:61472\n"
"ds_read_b64_tr_b16 v[82:83], v11 offset:62240\n"
"v_mfma_f32_16x16x32_bf16 v[216:219], v[60:63], v[88:91], v[216:219]\n"
"s_waitcnt lgkmcnt(6)\n"
"ds_read_b64_tr_b16 v[84:85], v11 offset:61504\n"
"ds_read_b64_tr_b16 v[86:87], v11 offset:62272\n"
"v_mfma_f32_16x16x32_bf16 v[202:205], v[52:55], v[92:95], v[202:205]\n"
"ds_read_b128 v[244:247], v4 offset:26624\n"
"v_mfma_f32_16x16x32_bf16 v[236:239], v[56:59], v[92:95], v[236:239]\n"
"v_mfma_f32_16x16x32_bf16 v[240:243], v[60:63], v[92:95], v[240:243]\n"
"s_waitcnt lgkmcnt(8)\n"
"ds_read_b128 v[112:115], v4 offset:32768\n"
"v_mfma_f32_16x16x32_bf16 v[226:229], v[52:55], v[96:99], v[226:229]\n"
"v_mfma_f32_16x16x32_bf16 v[248:251], v[56:59], v[96:99], v[248:251]\n"
"v_mfma_f32_16x16x32_bf16 v[252:255], v[60:63], v[96:99], v[252:255]\n"
"s_waitcnt lgkmcnt(8)\n"
"ds_read_b128 v[116:119], v5 offset:2048\n"
"v_mfma_f32_16x16x32_bf16 v[232:235], v[52:55], v[220:223], v[232:235]\n"
"v_mfma_f32_16x16x32_bf16 v[182:185], v[56:59], v[220:223], v[182:185]\n"
"v_mfma_f32_16x16x32_bf16 v[100:103], v[60:63], v[220:223], v[100:103]\n"
"s_waitcnt lgkmcnt(2)\n"
"ds_read_b128 v[120:123], v5 offset:8192\n"
"v_mfma_f32_16x16x32_bf16 v[208:211], v[52:55], v[244:247], v[208:211]\n"
"s_waitcnt vmcnt(0)\n"
"ds_read_b64_tr_b16 v[88:89], v11 offset:64512\n"
"ds_read_b64_tr_b16 v[90:91], v11 offset:65280\n"
"v_mfma_f32_16x16x32_bf16 v[186:189], v[56:59], v[244:247], v[186:189]\n"
"ds_read_b64_tr_b16 v[92:93], v11 offset:64544\n"
"ds_read_b64_tr_b16 v[94:95], v11 offset:65312\n"
"v_mfma_f32_16x16x32_bf16 v[190:193], v[60:63], v[244:247], v[190:193]\n"
"s_waitcnt lgkmcnt(6)\n"
"ds_read_b64_tr_b16 v[96:97], v11 offset:64576\n"
"ds_read_b64_tr_b16 v[98:99], v11 offset:65344\n"
"v_mfma_f32_16x16x32_bf16 v[178:181], v[52:55], v[112:115], v[178:181]\n"
"ds_read_b128 v[220:223], v5 offset:14336\n"
"v_mfma_f32_16x16x32_bf16 v[104:107], v[56:59], v[112:115], v[104:107]\n"
"v_mfma_f32_16x16x32_bf16 v[108:111], v[60:63], v[112:115], v[108:111]\n"
"s_waitcnt lgkmcnt(8)\n"
"ds_read_b128 v[244:247], v5 offset:20480\n"
"v_mfma_f32_16x16x32_bf16 v[196:199], v[64:67], v[116:119], v[196:199]\n"
"v_mfma_f32_16x16x32_bf16 v[212:215], v[68:71], v[116:119], v[212:215]\n"
"v_mfma_f32_16x16x32_bf16 v[216:219], v[72:75], v[116:119], v[216:219]\n"
"s_waitcnt lgkmcnt(8)\n"
"ds_read_b128 v[124:127], v5 offset:26624\n"
"v_mfma_f32_16x16x32_bf16 v[202:205], v[64:67], v[120:123], v[202:205]\n"
"s_waitcnt lgkmcnt(3)\n"
"s_barrier\n"
"v_mfma_f32_16x16x32_bf16 v[236:239], v[68:71], v[120:123], v[236:239]\n"
"v_mfma_f32_16x16x32_bf16 v[240:243], v[72:75], v[120:123], v[240:243]\n"
"s_waitcnt lgkmcnt(2)\n"
"ds_read_b128 v[112:115], v5 offset:32768\n"
"v_mfma_f32_16x16x32_bf16 v[226:229], v[64:67], v[220:223], v[226:229]\n"
"v_mfma_f32_16x16x32_bf16 v[248:251], v[68:71], v[220:223], v[248:251]\n"
"v_mfma_f32_16x16x32_bf16 v[252:255], v[72:75], v[220:223], v[252:255]\n"
"s_waitcnt lgkmcnt(2)\n"
"ds_read_b128 v[116:119], v4 offset:4096\n"
"v_mfma_f32_16x16x32_bf16 v[232:235], v[64:67], v[244:247], v[232:235]\n"
"v_mfma_f32_16x16x32_bf16 v[182:185], v[68:71], v[244:247], v[182:185]\n"
"v_mfma_f32_16x16x32_bf16 v[100:103], v[72:75], v[244:247], v[100:103]\n"
"s_waitcnt lgkmcnt(2)\n"
"ds_read_b128 v[120:123], v4 offset:10240\n"
"v_mfma_f32_16x16x32_bf16 v[208:211], v[64:67], v[124:127], v[208:211]\n"
"v_mfma_f32_16x16x32_bf16 v[186:189], v[68:71], v[124:127], v[186:189]\n"
"ds_read_b128 v[220:223], v5 offset:4096\n"
"v_mfma_f32_16x16x32_bf16 v[190:193], v[72:75], v[124:127], v[190:193]\n"
"s_waitcnt lgkmcnt(3)\n"
"ds_read_b128 v[128:131], v5 offset:10240\n"
"v_mfma_f32_16x16x32_bf16 v[178:181], v[64:67], v[112:115], v[178:181]\n"
"ds_read_b128 v[244:247], v4 offset:16384\n"
"v_mfma_f32_16x16x32_bf16 v[104:107], v[68:71], v[112:115], v[104:107]\n"
"ds_read_b128 v[132:135], v5 offset:16384\n"
"v_mfma_f32_16x16x32_bf16 v[108:111], v[72:75], v[112:115], v[108:111]\n"
"s_waitcnt lgkmcnt(5)\n"
"ds_read_b128 v[124:127], v4 offset:22528\n"
"v_mfma_f32_16x16x32_bf16 v[196:199], v[76:79], v[116:119], v[196:199]\n"
"ds_read_b128 v[136:139], v5 offset:22528\n"
"v_mfma_f32_16x16x32_bf16 v[212:215], v[80:83], v[116:119], v[212:215]\n"
"ds_read_b128 v[140:143], v4 offset:28672\n"
"ds_read_b128 v[144:147], v5 offset:28672\n"
"v_mfma_f32_16x16x32_bf16 v[216:219], v[84:87], v[116:119], v[216:219]\n"
"s_waitcnt lgkmcnt(8)\n"
"ds_read_b128 v[112:115], v4 offset:34816\n"
"ds_read_b128 v[148:151], v5 offset:34816\n"
"v_mfma_f32_16x16x32_bf16 v[202:205], v[76:79], v[120:123], v[202:205]\n"
"v_mfma_f32_16x16x32_bf16 v[236:239], v[80:83], v[120:123], v[236:239]\n"
"v_mfma_f32_16x16x32_bf16 v[240:243], v[84:87], v[120:123], v[240:243]\n"
"s_waitcnt lgkmcnt(7)\n"
"v_mfma_f32_16x16x32_bf16 v[226:229], v[76:79], v[244:247], v[226:229]\n"
"v_mfma_f32_16x16x32_bf16 v[248:251], v[80:83], v[244:247], v[248:251]\n"
"v_mfma_f32_16x16x32_bf16 v[252:255], v[84:87], v[244:247], v[252:255]\n"
"s_waitcnt lgkmcnt(5)\n"
"v_mfma_f32_16x16x32_bf16 v[232:235], v[76:79], v[124:127], v[232:235]\n"
"v_mfma_f32_16x16x32_bf16 v[182:185], v[80:83], v[124:127], v[182:185]\n"
"v_mfma_f32_16x16x32_bf16 v[100:103], v[84:87], v[124:127], v[100:103]\n"
"s_waitcnt lgkmcnt(3)\n"
"v_mfma_f32_16x16x32_bf16 v[208:211], v[76:79], v[140:143], v[208:211]\n"
"v_mfma_f32_16x16x32_bf16 v[186:189], v[80:83], v[140:143], v[186:189]\n"
"v_mfma_f32_16x16x32_bf16 v[190:193], v[84:87], v[140:143], v[190:193]\n"
"s_waitcnt lgkmcnt(1)\n"
"v_mfma_f32_16x16x32_bf16 v[178:181], v[76:79], v[112:115], v[178:181]\n"
"v_mfma_f32_16x16x32_bf16 v[104:107], v[80:83], v[112:115], v[104:107]\n"
"v_mfma_f32_16x16x32_bf16 v[108:111], v[84:87], v[112:115], v[108:111]\n"
"v_mfma_f32_16x16x32_bf16 v[196:199], v[88:91], v[220:223], v[196:199]\n"
"v_mfma_f32_16x16x32_bf16 v[212:215], v[92:95], v[220:223], v[212:215]\n"
"v_mfma_f32_16x16x32_bf16 v[216:219], v[96:99], v[220:223], v[216:219]\n"
"v_mfma_f32_16x16x32_bf16 v[202:205], v[88:91], v[128:131], v[202:205]\n"
"v_mfma_f32_16x16x32_bf16 v[236:239], v[92:95], v[128:131], v[236:239]\n"
"v_mfma_f32_16x16x32_bf16 v[240:243], v[96:99], v[128:131], v[240:243]\n"
"v_mfma_f32_16x16x32_bf16 v[226:229], v[88:91], v[132:135], v[226:229]\n"
"v_mfma_f32_16x16x32_bf16 v[248:251], v[92:95], v[132:135], v[248:251]\n"
"v_mfma_f32_16x16x32_bf16 v[252:255], v[96:99], v[132:135], v[252:255]\n"
"v_cvt_pk_bf16_f32 v196, v196, v197\n"
"v_cvt_pk_bf16_f32 v197, v198, v199\n"
"v_cvt_pk_bf16_f32 v198, v212, v213\n"
"v_cvt_pk_bf16_f32 v199, v214, v215\n"
"v_cvt_pk_bf16_f32 v200, v216, v217\n"
"v_cvt_pk_bf16_f32 v201, v218, v219\n"
"v_cvt_pk_bf16_f32 v202, v202, v203\n"
"v_cvt_pk_bf16_f32 v203, v204, v205\n"
"v_cvt_pk_bf16_f32 v204, v236, v237\n"
"v_cvt_pk_bf16_f32 v205, v238, v239\n"
"v_cvt_pk_bf16_f32 v206, v240, v241\n"
"v_cvt_pk_bf16_f32 v207, v242, v243\n"
"v_cvt_pk_bf16_f32 v226, v226, v227\n"
"v_cvt_pk_bf16_f32 v227, v228, v229\n"
"v_mfma_f32_16x16x32_bf16 v[232:235], v[88:91], v[136:139], v[232:235]\n"
"v_cvt_pk_bf16_f32 v228, v248, v249\n"
"v_cvt_pk_bf16_f32 v229, v250, v251\n"
"v_mfma_f32_16x16x32_bf16 v[182:185], v[92:95], v[136:139], v[182:185]\n"
"v_cvt_pk_bf16_f32 v230, v252, v253\n"
"v_cvt_pk_bf16_f32 v231, v254, v255\n"
"v_mfma_f32_16x16x32_bf16 v[100:103], v[96:99], v[136:139], v[100:103]\n"
"v_mfma_f32_16x16x32_bf16 v[208:211], v[88:91], v[144:147], v[208:211]\n"
"v_mfma_f32_16x16x32_bf16 v[186:189], v[92:95], v[144:147], v[186:189]\n"
"v_cvt_pk_bf16_f32 v232, v232, v233\n"
"v_cvt_pk_bf16_f32 v233, v234, v235\n"
"v_mfma_f32_16x16x32_bf16 v[190:193], v[96:99], v[144:147], v[190:193]\n"
"s_waitcnt lgkmcnt(0)\n"
"v_cvt_pk_bf16_f32 v234, v182, v183\n"
"v_cvt_pk_bf16_f32 v235, v184, v185\n"
"v_mfma_f32_16x16x32_bf16 v[178:181], v[88:91], v[148:151], v[178:181]\n"
"v_cvt_pk_bf16_f32 v236, v100, v101\n"
"v_cvt_pk_bf16_f32 v237, v102, v103\n"
"v_cvt_pk_bf16_f32 v208, v208, v209\n"
"v_cvt_pk_bf16_f32 v209, v210, v211\n"
"v_cvt_pk_bf16_f32 v210, v186, v187\n"
"v_cvt_pk_bf16_f32 v211, v188, v189\n"
"v_mfma_f32_16x16x32_bf16 v[104:107], v[92:95], v[148:151], v[104:107]\n"
"v_cvt_pk_bf16_f32 v212, v190, v191\n"
"v_cvt_pk_bf16_f32 v213, v192, v193\n"
"v_mfma_f32_16x16x32_bf16 v[108:111], v[96:99], v[148:151], v[108:111]\n"
"s_add_u32 s42, s46, 147456\n"
"s_addc_u32 s43, s47, 0\n"
"s_add_u32 m0, s50, 4294942720\n"
"s_nop 0\n"
"global_load_lds_dwordx4 v0, s[42:43]\n"
"s_add_u32 m0, s50, 4294943744\n"
"s_nop 0\n"
"global_load_lds_dwordx4 v1, s[42:43]\n"
"s_add_u32 m0, s50, 4294944768\n"
"s_nop 0\n"
"global_load_lds_dwordx4 v2, s[42:43]\n"
"s_add_u32 s42, s46, 184320\n"
"s_addc_u32 s43, s47, 0\n"
"s_add_u32 m0, s50, 4294945792\n"
"s_nop 0\n"
"global_load_lds_dwordx4 v0, s[42:43]\n"
"s_add_u32 m0, s50, 4294946816\n"
"s_nop 0\n"
"global_load_lds_dwordx4 v1, s[42:43]\n"
"s_add_u32 m0, s50, 4294947840\n"
"s_nop 0\n"
"global_load_lds_dwordx4 v2, s[42:43]\n"
"s_barrier\n"
"s_add_u32 s42, s46, 0\n"
"s_addc_u32 s43, s47, 0\n"
"s_add_u32 m0, s50, 24576\n"
"s_nop 0\n"
"global_load_lds_dwordx4 v0, s[42:43]\n"
"s_add_u32 m0, s50, 25600\n"
"s_nop 0\n"
"global_load_lds_dwordx4 v1, s[42:43]\n"
"s_add_u32 m0, s50, 26624\n"
"s_nop 0\n"
"global_load_lds_dwordx4 v2, s[42:43]\n"
"s_add_u32 s42, s46, 36864\n"
"s_addc_u32 s43, s47, 0\n"
"s_add_u32 m0, s50, 27648\n"
"s_nop 0\n"
"global_load_lds_dwordx4 v0, s[42:43]\n"
"s_add_u32 m0, s50, 28672\n"
"s_nop 0\n"
"global_load_lds_dwordx4 v1, s[42:43]\n"
"s_add_u32 m0, s50, 29696\n"
"s_nop 0\n"
"global_load_lds_dwordx4 v2, s[42:43]\n"
"s_add_u32 s42, s46, 73728\n"
"s_addc_u32 s43, s47, 0\n"
"s_add_u32 m0, s50, 49152\n"
"s_nop 0\n"
"global_load_lds_dwordx4 v0, s[42:43]\n"
"s_add_u32 m0, s50, 50176\n"
"s_nop 0\n"
"global_load_lds_dwordx4 v1, s[42:43]\n"
"s_add_u32 m0, s50, 51200\n"
"s_nop 0\n"
"global_load_lds_dwordx4 v2, s[42:43]\n"
"s_add_u32 s42, s46, 110592\n"
"s_addc_u32 s43, s47, 0\n"
"s_add_u32 m0, s50, 52224\n"
"s_nop 0\n"
"global_load_lds_dwordx4 v0, s[42:43]\n"
"s_add_u32 m0, s50, 53248\n"
"s_nop 0\n"
"global_load_lds_dwordx4 v1, s[42:43]\n"
"s_add_u32 m0, s50, 54272\n"
"s_nop 0\n"
"global_load_lds_dwordx4 v2, s[42:43]\n"
"ds_read_u16 v13, v13 offset:0\n"
"v_cvt_pk_bf16_f32 v178, v178, v179\n"
"v_cvt_pk_bf16_f32 v179, v180, v181\n"
"v_cvt_pk_bf16_f32 v180, v104, v105\n"
"v_cvt_pk_bf16_f32 v181, v106, v107\n"
"v_cvt_pk_bf16_f32 v182, v108, v109\n"
"v_cvt_pk_bf16_f32 v183, v110, v111\n"
"ds_read_u16 v23, v23 offset:0\n"
"ds_read_u16 v14, v14 offset:0\n"
"ds_read_u16 v15, v15 offset:0\n"
"ds_read_u16 v16, v16 offset:0\n"
"ds_read_u16 v17, v17 offset:0\n"
"ds_read_u16 v18, v18 offset:0\n"
"ds_read_u16 v12, v12 offset:0\n"
"ds_read_u16 v20, v20 offset:0\n"
"ds_read_u16 v21, v21 offset:0\n"
"ds_read_u16 v22, v22 offset:0\n"
"s_waitcnt lgkmcnt(10)\n"
"v_lshlrev_b32 v13, 16, v13\n"
"v_mul_f32 v13, s61, v13\n"
"s_waitcnt lgkmcnt(9)\n"
"v_lshlrev_b32 v23, 16, v23\n"
"v_mul_f32 v23, s61, v23\n"
"s_waitcnt lgkmcnt(8)\n"
"v_lshlrev_b32 v14, 16, v14\n"
"v_mul_f32 v14, s61, v14\n"
"s_waitcnt lgkmcnt(7)\n"
"v_lshlrev_b32 v15, 16, v15\n"
"v_mov_b32 v184, v13\n"
"v_mul_f32 v15, s61, v15\n"
"s_waitcnt lgkmcnt(6)\n"
"v_lshlrev_b32 v16, 16, v16\n"
"v_mov_b32 v185, v14\n"
"v_mul_f32 v16, s61, v16\n"
"s_waitcnt lgkmcnt(5)\n"
"v_lshlrev_b32 v17, 16, v17\n"
"v_mov_b32 v186, v15\n"
"v_mul_f32 v17, s61, v17\n"
"s_waitcnt lgkmcnt(4)\n"
"v_lshlrev_b32 v18, 16, v18\n"
"v_mov_b32 v187, v16\n"
"v_mul_f32 v18, s61, v18\n"
"v_mov_b32 v188, v17\n"
"v_mov_b32 v189, v18\n"
"v_mov_b32 v190, 0xf1800000\n"
"v_mov_b32 v191, 0xf1800000\n"
"v_mov_b32 v192, 0xf1800000\n"
"v_mov_b32 v193, 0xf1800000\n"
"s_waitcnt vmcnt(15)\n"
"s_waitcnt lgkmcnt(3)\n"
"v_lshlrev_b32 v12, 16, v12\n"
"s_waitcnt lgkmcnt(2)\n"
"v_lshlrev_b32 v20, 16, v20\n"
"s_waitcnt lgkmcnt(1)\n"
"v_lshlrev_b32 v21, 16, v21\n"
"s_waitcnt lgkmcnt(0)\n"
"v_lshlrev_b32 v22, 16, v22\n"
"v_mul_f32 v12, s61, v12\n"
"v_mul_f32 v20, s61, v20\n"
"v_mul_f32 v21, s61, v21\n"
"v_mul_f32 v22, s61, v22\n"
"ds_read_b64_tr_b16 v[214:215], v3 offset:4160\n"
"ds_read_b64_tr_b16 v[216:217], v3 offset:4928\n"
"s_waitcnt vmcnt(12)\n"
"ds_read_b64_tr_b16 v[100:101], v3 offset:4096\n"
"ds_read_b64_tr_b16 v[102:103], v3 offset:4864\n"
"ds_read_b64_tr_b16 v[104:105], v3 offset:4128\n"
"ds_read_b64_tr_b16 v[106:107], v3 offset:4896\n"
"ds_read_b64_tr_b16 v[108:109], v3 offset:7168\n"
"ds_read_b64_tr_b16 v[110:111], v3 offset:7936\n"
"ds_read_b64_tr_b16 v[112:113], v3 offset:7200\n"
"ds_read_b64_tr_b16 v[114:115], v3 offset:7968\n"
"ds_read_b64_tr_b16 v[218:219], v3 offset:7232\n"
"ds_read_b64_tr_b16 v[220:221], v3 offset:8000\n"
"s_waitcnt lgkmcnt(0)\n"
"s_barrier\n"
"s_waitcnt vmcnt(9)\n"
"ds_read_b64_tr_b16 v[116:117], v3 offset:53248\n"
"ds_read_b64_tr_b16 v[118:119], v3 offset:54016\n"
"ds_read_b128 v[222:225], v4 offset:36864\n"
"ds_read_b64_tr_b16 v[120:121], v3 offset:53280\n"
"ds_read_b64_tr_b16 v[122:123], v3 offset:54048\n"
"ds_read_b64_tr_b16 v[238:239], v3 offset:53312\n"
"ds_read_b64_tr_b16 v[240:241], v3 offset:54080\n"
"s_waitcnt vmcnt(6)\n"
"ds_read_b64_tr_b16 v[124:125], v3 offset:56320\n"
"ds_read_b64_tr_b16 v[126:127], v3 offset:57088\n"
"ds_read_b128 v[242:245], v5 offset:36864\n"
"ds_read_b64_tr_b16 v[128:129], v3 offset:56352\n"
"ds_read_b64_tr_b16 v[130:131], v3 offset:57120\n"
"ds_write_b128 v8, v[186:189] offset:0\n"
"ds_read_b64_tr_b16 v[246:247], v3 offset:56384\n"
"ds_read_b64_tr_b16 v[248:249], v3 offset:57152\n"
"s_waitcnt vmcnt(3)\n"
"ds_read_b64_tr_b16 v[0:1], v11 offset:0\n"
"ds_read_b64_tr_b16 v[2:3], v11 offset:768\n"
"s_waitcnt lgkmcnt(14)\n"
"v_mfma_f32_16x16x32_bf16 v[250:253], v[116:119], v[222:225], 0\n"
"s_waitcnt lgkmcnt(12)\n"
"ds_read_b128 v[140:143], v4 offset:38912\n"
"v_mfma_f32_16x16x32_bf16 v[144:147], v[120:123], v[222:225], 0\n"
"ds_write_b128 v8, v[14:17] offset:64\n"
"s_waitcnt lgkmcnt(12)\n"
"v_mfma_f32_16x16x32_bf16 v[148:151], v[238:241], v[222:225], 0\n"
"s_waitcnt lgkmcnt(9)\n"
"ds_read_b64_tr_b16 v[16:17], v11 offset:32\n"
"ds_read_b64_tr_b16 v[18:19], v11 offset:800\n"
"v_mfma_f32_16x16x32_bf16 v[250:253], v[124:127], v[242:245], v[250:253]\n"
"s_waitcnt lgkmcnt(9)\n"
"ds_read_b64_tr_b16 v[152:153], v11 offset:64\n"
"ds_read_b64_tr_b16 v[154:155], v11 offset:832\n"
"v_mfma_f32_16x16x32_bf16 v[144:147], v[128:131], v[242:245], v[144:147]\n"
"s_waitcnt vmcnt(0)\n"
"ds_write_b128 v8, v[184:187] offset:128\n"
"ds_read_b64_tr_b16 v[132:133], v11 offset:3072\n"
"ds_read_b64_tr_b16 v[134:135], v11 offset:3840\n"
"s_waitcnt lgkmcnt(11)\n"
"v_mfma_f32_16x16x32_bf16 v[148:151], v[246:249], v[242:245], v[148:151]\n"
"s_waitcnt lgkmcnt(8)\n"
"ds_read_b128 v[222:225], v5 offset:38912\n"
"v_mfma_f32_16x16x32_bf16 v[250:253], v[0:3], v[140:143], v[250:253]\n"
"ds_read_b64_tr_b16 v[136:137], v11 offset:3104\n"
"ds_read_b64_tr_b16 v[138:139], v11 offset:3872\n"
"ds_read_b64_tr_b16 v[184:185], v11 offset:3136\n"
"ds_read_b64_tr_b16 v[186:187], v11 offset:3904\n"
"ds_read_b128 v[242:245], v4 offset:40960\n"
"s_waitcnt lgkmcnt(11)\n"
"v_mfma_f32_16x16x32_bf16 v[144:147], v[16:19], v[140:143], v[144:147]\n"
"ds_write_b128 v8, v[12:15] offset:192\n"
"ds_write_b128 v8, v[20:23] offset:256\n"
"s_waitcnt lgkmcnt(11)\n"
"v_mfma_f32_16x16x32_bf16 v[148:151], v[152:155], v[140:143], v[148:151]\n"
"ds_write_b128 v9, v[190:193] offset:0\n"
"s_waitcnt lgkmcnt(8)\n"
"v_mfma_f32_16x16x32_bf16 v[250:253], v[132:135], v[222:225], v[250:253]\n"
"s_waitcnt lgkmcnt(6)\n"
"ds_read_b128 v[8:11], v5 offset:40960\n"
"v_mfma_f32_16x16x32_bf16 v[144:147], v[136:139], v[222:225], v[144:147]\n"
"s_waitcnt lgkmcnt(5)\n"
"s_barrier\n"
"v_mfma_f32_16x16x32_bf16 v[148:151], v[184:187], v[222:225], v[148:151]\n"
"s_waitcnt lgkmcnt(4)\n"
"v_mfma_f32_16x16x32_bf16 v[250:253], v[100:103], v[242:245], v[250:253]\n"
"ds_read_b128 v[188:191], v4 offset:43008\n"
"v_mfma_f32_16x16x32_bf16 v[144:147], v[104:107], v[242:245], v[144:147]\n"
"ds_write_b128 v6, v[238:241] offset:4096\n"
"v_mfma_f32_16x16x32_bf16 v[148:151], v[214:217], v[242:245], v[148:151]\n"
"ds_write_b128 v6, v[246:249] offset:5120\n"
"s_waitcnt lgkmcnt(3)\n"
"v_mfma_f32_16x16x32_bf16 v[250:253], v[108:111], v[8:11], v[250:253]\n"
"ds_read_b128 v[192:195], v5 offset:43008\n"
"v_mfma_f32_16x16x32_bf16 v[144:147], v[112:115], v[8:11], v[144:147]\n"
"ds_write_b128 v6, v[152:155] offset:6144\n"
"v_mfma_f32_16x16x32_bf16 v[148:151], v[218:221], v[8:11], v[148:151]\n"
"s_waitcnt lgkmcnt(4)\n"
"v_mfma_f32_16x16x32_bf16 v[238:241], v[28:31], v[188:191], 0\n"
"ds_read_b128 v[222:225], v4 offset:45056\n"
"v_mfma_f32_16x16x32_bf16 v[244:247], v[32:35], v[188:191], 0\n"
"ds_write_b128 v6, v[184:187] offset:7168\n"
"v_mfma_f32_16x16x32_bf16 v[12:15], v[36:39], v[188:191], 0\n"
"ds_write_b128 v6, v[214:217] offset:8192\n"
"s_waitcnt lgkmcnt(4)\n"
"v_mfma_f32_16x16x32_bf16 v[238:241], v[40:43], v[192:195], v[238:241]\n"
"ds_read_b128 v[8:11], v5 offset:45056\n"
"v_mul_f32 v250, s60, v250\n"
"v_mfma_f32_16x16x32_bf16 v[244:247], v[44:47], v[192:195], v[244:247]\n"
"ds_write_b128 v6, v[218:221] offset:9216\n"
"v_mfma_f32_16x16x32_bf16 v[12:15], v[48:51], v[192:195], v[12:15]\n"
"s_waitcnt lgkmcnt(4)\n"
"v_mul_f32 v251, s60, v251\n"
"v_cvt_pk_bf16_f32 v250, v250, v251\n"
"v_mfma_f32_16x16x32_bf16 v[238:241], v[52:55], v[222:225], v[238:241]\n"
"ds_read_b128 v[188:191], v4 offset:47104\n"
"v_mul_f32 v252, s60, v252\n"
"v_mfma_f32_16x16x32_bf16 v[244:247], v[56:59], v[222:225], v[244:247]\n"
"v_mul_f32 v253, s60, v253\n"
"v_cvt_pk_bf16_f32 v251, v252, v253\n"
"v_mfma_f32_16x16x32_bf16 v[12:15], v[60:63], v[222:225], v[12:15]\n"
"v_mul_f32 v144, s60, v144\n"
"v_mul_f32 v145, s60, v145\n"
"v_mul_f32 v146, s60, v146\n"
"v_mul_f32 v147, s60, v147\n"
"v_cvt_pk_bf16_f32 v252, v144, v145\n"
"s_waitcnt lgkmcnt(2)\n"
"v_mfma_f32_16x16x32_bf16 v[238:241], v[64:67], v[8:11], v[238:241]\n"
"ds_read_b128 v[192:195], v5 offset:47104\n"
"v_cvt_pk_bf16_f32 v253, v146, v147\n"
"v_mfma_f32_16x16x32_bf16 v[244:247], v[68:71], v[8:11], v[244:247]\n"
"v_mul_f32 v148, s60, v148\n"
"v_mul_f32 v149, s60, v149\n"
"v_mfma_f32_16x16x32_bf16 v[12:15], v[72:75], v[8:11], v[12:15]\n"
"v_mul_f32 v150, s60, v150\n"
"v_mul_f32 v151, s60, v151\n"
"v_cvt_pk_bf16_f32 v26, v148, v149\n"
"v_cvt_pk_bf16_f32 v27, v150, v151\n"
"s_waitcnt lgkmcnt(1)\n"
"v_mfma_f32_16x16x32_bf16 v[238:241], v[76:79], v[188:191], v[238:241]\n"
"ds_read_b128 v[214:217], v4 offset:49152\n"
"v_mfma_f32_16x16x32_bf16 v[244:247], v[80:83], v[188:191], v[244:247]\n"
"v_mfma_f32_16x16x32_bf16 v[12:15], v[84:87], v[188:191], v[12:15]\n"
"s_waitcnt lgkmcnt(1)\n"
"v_mfma_f32_16x16x32_bf16 v[238:241], v[88:91], v[192:195], v[238:241]\n"
"ds_read_b128 v[218:221], v5 offset:49152\n"
"v_mfma_f32_16x16x32_bf16 v[244:247], v[92:95], v[192:195], v[244:247]\n"
"v_mfma_f32_16x16x32_bf16 v[12:15], v[96:99], v[192:195], v[12:15]\n"
"s_waitcnt lgkmcnt(1)\n"
"v_mfma_f32_16x16x32_bf16 v[184:187], v[28:31], v[214:217], 0\n"
"ds_read_b128 v[222:225], v4 offset:51200\n"
"v_mfma_f32_16x16x32_bf16 v[8:11], v[32:35], v[214:217], 0\n"
"v_mfma_f32_16x16x32_bf16 v[20:23], v[36:39], v[214:217], 0\n"
"s_waitcnt lgkmcnt(1)\n"
"v_mfma_f32_16x16x32_bf16 v[184:187], v[40:43], v[218:221], v[184:187]\n"
"ds_read_b128 v[188:191], v5 offset:51200\n"
"v_cvt_pk_bf16_f32 v238, v238, v239\n"
"v_cvt_pk_bf16_f32 v239, v240, v241\n"
"v_cvt_pk_bf16_f32 v240, v244, v245\n"
"v_cvt_pk_bf16_f32 v241, v246, v247\n"
"v_cvt_pk_bf16_f32 v242, v12, v13\n"
"v_cvt_pk_bf16_f32 v243, v14, v15\n"
"v_mfma_f32_16x16x32_bf16 v[8:11], v[44:47], v[218:221], v[8:11]\n"
"v_mfma_f32_16x16x32_bf16 v[20:23], v[48:51], v[218:221], v[20:23]\n"
"s_waitcnt lgkmcnt(1)\n"
"v_mfma_f32_16x16x32_bf16 v[184:187], v[52:55], v[222:225], v[184:187]\n"
"ds_read_b128 v[214:217], v4 offset:53248\n"
"v_mfma_f32_16x16x32_bf16 v[8:11], v[56:59], v[222:225], v[8:11]\n"
"v_mfma_f32_16x16x32_bf16 v[20:23], v[60:63], v[222:225], v[20:23]\n"
"s_waitcnt lgkmcnt(1)\n"
"v_mfma_f32_16x16x32_bf16 v[184:187], v[64:67], v[188:191], v[184:187]\n"
"ds_read_b128 v[244:247], v5 offset:53248\n"
"v_mfma_f32_16x16x32_bf16 v[8:11], v[68:71], v[188:191], v[8:11]\n"
"v_mfma_f32_16x16x32_bf16 v[20:23], v[72:75], v[188:191], v[20:23]\n"
"s_waitcnt lgkmcnt(1)\n"
"v_mfma_f32_16x16x32_bf16 v[184:187], v[76:79], v[214:217], v[184:187]\n"
"ds_read_b128 v[218:221], v4 offset:55296\n"
"v_mfma_f32_16x16x32_bf16 v[8:11], v[80:83], v[214:217], v[8:11]\n"
"v_mfma_f32_16x16x32_bf16 v[20:23], v[84:87], v[214:217], v[20:23]\n"
"s_waitcnt lgkmcnt(1)\n"
"v_mfma_f32_16x16x32_bf16 v[184:187], v[88:91], v[244:247], v[184:187]\n"
"ds_read_b128 v[222:225], v5 offset:55296\n"
"v_mfma_f32_16x16x32_bf16 v[8:11], v[92:95], v[244:247], v[8:11]\n"
"v_mfma_f32_16x16x32_bf16 v[20:23], v[96:99], v[244:247], v[20:23]\n"
"s_waitcnt lgkmcnt(1)\n"
"v_mfma_f32_16x16x32_bf16 v[190:193], v[28:31], v[218:221], 0\n"
"ds_read_b128 v[12:15], v4 offset:57344\n"
"v_mfma_f32_16x16x32_bf16 v[214:217], v[32:35], v[218:221], 0\n"
"v_mfma_f32_16x16x32_bf16 v[140:143], v[36:39], v[218:221], 0\n"
"s_waitcnt lgkmcnt(1)\n"
"v_mfma_f32_16x16x32_bf16 v[190:193], v[40:43], v[222:225], v[190:193]\n"
"ds_read_b128 v[144:147], v5 offset:57344\n"
"v_cvt_pk_bf16_f32 v184, v184, v185\n"
"v_cvt_pk_bf16_f32 v185, v186, v187\n"
"v_cvt_pk_bf16_f32 v186, v8, v9\n"
"v_cvt_pk_bf16_f32 v187, v10, v11\n"
"v_cvt_pk_bf16_f32 v188, v20, v21\n"
"v_cvt_pk_bf16_f32 v189, v22, v23\n"
"v_mfma_f32_16x16x32_bf16 v[214:217], v[44:47], v[222:225], v[214:217]\n"
"v_mfma_f32_16x16x32_bf16 v[140:143], v[48:51], v[222:225], v[140:143]\n"
"s_waitcnt lgkmcnt(1)\n"
"v_bfe_u32 v255, v6, 2, 6\n"
"v_mfma_f32_16x16x32_bf16 v[190:193], v[52:55], v[12:15], v[190:193]\n"
"ds_read_b128 v[218:221], v4 offset:59392\n"
"v_mfma_f32_16x16x32_bf16 v[214:217], v[56:59], v[12:15], v[214:217]\n"
"s_add_u32 s68, s50, 0\n"
"ds_read_b32 v255, v255 offset:1216\n"
"v_mfma_f32_16x16x32_bf16 v[140:143], v[60:63], v[12:15], v[140:143]\n"
"s_waitcnt lgkmcnt(2)\n"
"v_mfma_f32_16x16x32_bf16 v[190:193], v[64:67], v[144:147], v[190:193]\n"
"ds_read_b128 v[222:225], v5 offset:59392\n"
"v_mfma_f32_16x16x32_bf16 v[214:217], v[68:71], v[144:147], v[214:217]\n"
"v_mfma_f32_16x16x32_bf16 v[140:143], v[72:75], v[144:147], v[140:143]\n"
"s_add_u32 s69, s50, 1040\n"
"s_waitcnt lgkmcnt(2)\n"
"v_mfma_f32_16x16x32_bf16 v[190:193], v[76:79], v[218:221], v[190:193]\n"
"s_waitcnt lgkmcnt(1)\n"
"v_add_u32_sdwa v254, v255, v7 dst_sel:DWORD dst_unused:UNUSED_PAD src0_sel:WORD_0 src1_sel:DWORD\n"
"v_mfma_f32_16x16x32_bf16 v[214:217], v[80:83], v[218:221], v[214:217]\n"
"v_max_u32 v249, s68, v254\n"
"v_mfma_f32_16x16x32_bf16 v[140:143], v[84:87], v[218:221], v[140:143]\n"
"s_waitcnt lgkmcnt(0)\n"
"ds_read_b128 v[244:247], v249 offset:16\n"
"v_min_u32 v248, s69, v254\n"
"v_mfma_f32_16x16x32_bf16 v[190:193], v[88:91], v[222:225], v[190:193]\n"
"ds_read_b128 v[8:11], v249 offset:32\n"
"v_mfma_f32_16x16x32_bf16 v[214:217], v[92:95], v[222:225], v[214:217]\n"
"ds_read_b128 v[12:15], v248 offset:1344\n"
"v_mfma_f32_16x16x32_bf16 v[140:143], v[96:99], v[222:225], v[140:143]\n"
"s_add_u32 s70, s50, 4294966992\n"
"s_waitcnt lgkmcnt(2)\n"
"v_mfma_f32_16x16x32_bf16 v[244:247], v[202:205], v[250:253], v[244:247]\n"
"ds_read_b128 v[218:221], v248 offset:1360\n"
"v_mfma_f32_16x16x32_bf16 v[244:247], v[204:207], v[24:27], v[244:247]\n"
"v_cvt_pk_bf16_f32 v190, v190, v191\n"
"v_cvt_pk_bf16_f32 v191, v192, v193\n"
"v_cvt_pk_bf16_f32 v192, v214, v215\n"
"v_cvt_pk_bf16_f32 v193, v216, v217\n"
"v_cvt_pk_bf16_f32 v194, v140, v141\n"
"v_cvt_pk_bf16_f32 v195, v142, v143\n"
"s_waitcnt lgkmcnt(2)\n"
"v_mfma_f32_16x16x32_bf16 v[8:11], v[208:211], v[250:253], v[8:11]\n"
"ds_read_b128 v[222:225], v249 offset:0\n"
"v_min_u32 v255, s70, v254\n"
"v_mfma_f32_16x16x32_bf16 v[8:11], v[210:213], v[24:27], v[8:11]\n"
"s_waitcnt lgkmcnt(2)\n"
"v_max3_f32 v214, v244, v245, v246\n"
"v_max_f32 v214, v214, v247\n"
"v_mfma_f32_16x16x32_bf16 v[12:15], v[226:229], v[250:253], v[12:15]\n"
"ds_read_b128 v[20:23], v255 offset:2688\n"
"v_mfma_f32_16x16x32_bf16 v[12:15], v[228:231], v[24:27], v[12:15]\n"
"s_waitcnt lgkmcnt(2)\n"
"s_nop 0\n"
"v_max3_f32 v214, v214, v8, v9\n"
"v_max3_f32 v214, v214, v10, v11\n"
"v_mfma_f32_16x16x32_bf16 v[218:221], v[232:235], v[250:253], v[218:221]\n"
"ds_read_b128 v[244:247], v248 offset:1376\n"
"v_mfma_f32_16x16x32_bf16 v[218:221], v[234:237], v[24:27], v[218:221]\n"
"s_waitcnt lgkmcnt(2)\n"
"v_max3_f32 v214, v214, v12, v13\n"
"v_max3_f32 v214, v214, v14, v15\n"
"v_mfma_f32_16x16x32_bf16 v[222:225], v[196:199], v[250:253], v[222:225]\n"
"ds_read_b128 v[8:11], v255 offset:2704\n"
"v_mfma_f32_16x16x32_bf16 v[222:225], v[198:201], v[24:27], v[222:225]\n"
"s_waitcnt lgkmcnt(2)\n"
"s_nop 0\n"
"v_max3_f32 v214, v214, v218, v219\n"
"v_max3_f32 v214, v214, v220, v221\n"
"v_mfma_f32_16x16x32_bf16 v[20:23], v[178:181], v[250:253], v[20:23]\n"
"ds_read_b128 v[12:15], v255 offset:2720\n"
"v_mfma_f32_16x16x32_bf16 v[20:23], v[180:183], v[24:27], v[20:23]\n"
"s_waitcnt lgkmcnt(2)\n"
"v_max3_f32 v214, v214, v222, v223\n"
"v_max3_f32 v214, v214, v224, v225\n"
"v_mfma_f32_16x16x32_bf16 v[244:247], v[238:241], v[250:253], v[244:247]\n"
"ds_read_b128 v[216:219], v249 offset:16\n"
"v_mfma_f32_16x16x32_bf16 v[244:247], v[240:243], v[24:27], v[244:247]\n"
"s_waitcnt lgkmcnt(2)\n"
"s_nop 0\n"
"v_max3_f32 v214, v214, v20, v21\n"
"v_max3_f32 v214, v214, v22, v23\n"
"v_mfma_f32_16x16x32_bf16 v[8:11], v[184:187], v[250:253], v[8:11]\n"
"ds_read_b128 v[220:223], v249 offset:32\n"
"v_mfma_f32_16x16x32_bf16 v[8:11], v[186:189], v[24:27], v[8:11]\n"
"s_waitcnt lgkmcnt(2)\n"
"v_max3_f32 v214, v214, v244, v245\n"
"v_max3_f32 v214, v214, v246, v247\n"
"v_mfma_f32_16x16x32_bf16 v[12:15], v[190:193], v[250:253], v[12:15]\n"
"ds_read_b128 v[20:23], v248 offset:1344\n"
"v_mfma_f32_16x16x32_bf16 v[12:15], v[192:195], v[24:27], v[12:15]\n"
"s_waitcnt lgkmcnt(2)\n"
"s_nop 0\n"
"v_max3_f32 v214, v214, v8, v9\n"
"v_max3_f32 v214, v214, v10, v11\n"
"v_mfma_f32_16x16x32_bf16 v[216:219], v[202:205], v[250:253], v[216:219]\n"
"ds_read_b128 v[244:247], v248 offset:1360\n"
"v_mfma_f32_16x16x32_bf16 v[216:219], v[204:207], v[24:27], v[216:219]\n"
"s_nop 0\n"
"v_max3_f32 v214, v214, v12, v13\n"
"v_max3_f32 v214, v214, v14, v15\n"
"v_mov_b32 v215, v214\n"
"s_nop 1\n"
"v_permlane16_swap_b32 v214, v215\n"
"v_max_f32 v214, v214, v215\n"
"v_mov_b32 v215, v214\n"
"s_nop 1\n"
"v_permlane32_swap_b32 v214, v215\n"
"v_max_f32 v214, v214, v215\n"
"v_sub_f32 v216, v216, v214\n"
"v_exp_f32 v216, v216\n"
"v_sub_f32 v217, v217, v214\n"
"v_exp_f32 v217, v217\n"
"v_sub_f32 v218, v218, v214\n"
"v_exp_f32 v218, v218\n"
"v_sub_f32 v219, v219, v214\n"
"v_exp_f32 v219, v219\n"
"s_waitcnt lgkmcnt(2)\n"
"v_mfma_f32_16x16x32_bf16 v[220:223], v[208:211], v[250:253], v[220:223]\n"
"ds_read_b128 v[8:11], v249 offset:0\n"
"v_cvt_pk_bf16_f32 v216, v216, v217\n"
"v_mfma_f32_16x16x32_bf16 v[220:223], v[210:213], v[24:27], v[220:223]\n"
"v_cvt_pk_bf16_f32 v217, v218, v219\n"
"ds_write_b64 v6, v[216:217] offset:53248\n"
"s_waitcnt lgkmcnt(3)\n"
"v_mfma_f32_16x16x32_bf16 v[20:23], v[226:229], v[250:253], v[20:23]\n"
"ds_read_b128 v[12:15], v255 offset:2688\n"
"v_mfma_f32_16x16x32_bf16 v[20:23], v[228:231], v[24:27], v[20:23]\n"
"s_nop 1\n"
"v_sub_f32 v220, v220, v214\n"
"v_exp_f32 v220, v220\n"
"v_sub_f32 v221, v221, v214\n"
"v_exp_f32 v221, v221\n"
"s_nop 0\n"
"v_cvt_pk_bf16_f32 v220, v220, v221\n"
"v_mov_b32 v221, 0\n"
"ds_write_b64 v6, v[220:221] offset:53256\n"
"s_waitcnt lgkmcnt(4)\n"
"v_mfma_f32_16x16x32_bf16 v[244:247], v[232:235], v[250:253], v[244:247]\n"
"ds_read_b128 v[216:219], v248 offset:1376\n"
"v_sub_f32 v20, v20, v214\n"
"v_exp_f32 v20, v20\n"
"v_sub_f32 v21, v21, v214\n"
"v_exp_f32 v21, v21\n"
"v_sub_f32 v22, v22, v214\n"
"v_exp_f32 v22, v22\n"
"v_sub_f32 v23, v23, v214\n"
"v_exp_f32 v23, v23\n"
"v_mfma_f32_16x16x32_bf16 v[244:247], v[234:237], v[24:27], v[244:247]\n"
"v_cvt_pk_bf16_f32 v20, v20, v21\n"
"v_cvt_pk_bf16_f32 v21, v22, v23\n"
"ds_write_b64 v6, v[20:21] offset:54272\n"
"s_waitcnt lgkmcnt(5)\n"
"v_mfma_f32_16x16x32_bf16 v[8:11], v[196:199], v[250:253], v[8:11]\n"
"ds_read_b128 v[220:223], v255 offset:2704\n"
"v_mfma_f32_16x16x32_bf16 v[8:11], v[198:201], v[24:27], v[8:11]\n"
"s_nop 0\n"
"v_sub_f32 v244, v244, v214\n"
"v_exp_f32 v244, v244\n"
"v_sub_f32 v245, v245, v214\n"
"v_exp_f32 v245, v245\n"
"v_sub_f32 v246, v246, v214\n"
"v_exp_f32 v246, v246\n"
"v_sub_f32 v247, v247, v214\n"
"v_exp_f32 v247, v247\n"
"v_cvt_pk_bf16_f32 v244, v244, v245\n"
"v_cvt_pk_bf16_f32 v245, v246, v247\n"
"v_sub_f32 v8, v8, v214\n"
"v_exp_f32 v8, v8\n"
"v_sub_f32 v9, v9, v214\n"
"v_exp_f32 v9, v9\n"
"v_sub_f32 v10, v10, v214\n"
"v_exp_f32 v10, v10\n"
"v_sub_f32 v11, v11, v214\n"
"v_exp_f32 v11, v11\n"
"v_cvt_pk_bf16_f32 v8, v8, v9\n"
"v_cvt_pk_bf16_f32 v9, v10, v11\n"
"ds_write_b64 v6, v[244:245] offset:54280\n"
"s_waitcnt lgkmcnt(5)\n"
"v_mfma_f32_16x16x32_bf16 v[12:15], v[178:181], v[250:253], v[12:15]\n"
"ds_read_b128 v[196:199], v255 offset:2720\n"
"v_mfma_f32_16x16x32_bf16 v[12:15], v[180:183], v[24:27], v[12:15]\n"
"s_waitcnt lgkmcnt(1)\n"
"s_barrier\n"
"ds_write_b64 v6, v[8:9] offset:58376\n"
"v_mfma_f32_16x16x32_bf16 v[216:219], v[238:241], v[250:253], v[216:219]\n"
"ds_read_b128 v[244:247], v4 offset:12288\n"
"v_mfma_f32_16x16x32_bf16 v[216:219], v[240:243], v[24:27], v[216:219]\n"
"s_nop 1\n"
"v_sub_f32 v12, v12, v214\n"
"v_exp_f32 v12, v12\n"
"v_sub_f32 v13, v13, v214\n"
"v_exp_f32 v13, v13\n"
"v_sub_f32 v14, v14, v214\n"
"v_exp_f32 v14, v14\n"
"v_sub_f32 v15, v15, v214\n"
"v_exp_f32 v15, v15\n"
"v_cvt_pk_bf16_f32 v12, v12, v13\n"
"v_cvt_pk_bf16_f32 v13, v14, v15\n"
"v_sub_f32 v216, v216, v214\n"
"v_exp_f32 v216, v216\n"
"v_sub_f32 v217, v217, v214\n"
"v_exp_f32 v217, v217\n"
"ds_write_b64 v6, v[12:13] offset:57344\n"
"v_mfma_f32_16x16x32_bf16 v[220:223], v[184:187], v[250:253], v[220:223]\n"
"ds_read_b128 v[8:11], v6 offset:4096\n"
"v_cvt_pk_bf16_f32 v216, v216, v217\n"
"v_mfma_f32_16x16x32_bf16 v[220:223], v[186:189], v[24:27], v[220:223]\n"
"ds_read_b128 v[20:23], v5 offset:12288\n"
"v_mov_b32 v217, 0\n"
"ds_write_b64 v6, v[216:217] offset:33280\n"
"s_waitcnt lgkmcnt(6)\n"
"v_mfma_f32_16x16x32_bf16 v[196:199], v[190:193], v[250:253], v[196:199]\n"
"v_mfma_f32_16x16x32_bf16 v[196:199], v[192:195], v[24:27], v[196:199]\n"
"s_waitcnt lgkmcnt(4)\n"
"ds_read_b128 v[12:15], v6 offset:5120\n"
"v_mfma_f32_16x16x32_bf16 v[140:143], v[116:119], v[244:247], 0\n"
"ds_read_b128 v[144:147], v4 offset:14336\n"
"v_sub_f32 v220, v220, v214\n"
"v_exp_f32 v220, v220\n"
"v_sub_f32 v221, v221, v214\n"
"v_exp_f32 v221, v221\n"
"v_mfma_f32_16x16x32_bf16 v[198:201], v[120:123], v[244:247], 0\n"
"s_waitcnt lgkmcnt(4)\n"
"v_sub_f32 v222, v222, v214\n"
"v_exp_f32 v222, v222\n"
"v_mfma_f32_16x16x32_bf16 v[248:251], v[8:11], v[244:247], 0\n"
"ds_read_b128 v[216:219], v6 offset:6144\n"
"v_cvt_pk_bf16_f32 v220, v220, v221\n"
"v_sub_f32 v223, v223, v214\n"
"v_exp_f32 v223, v223\n"
"s_waitcnt lgkmcnt(4)\n"
"v_mfma_f32_16x16x32_bf16 v[140:143], v[124:127], v[20:23], v[140:143]\n"
"ds_read_b128 v[252:255], v5 offset:14336\n"
"v_cvt_pk_bf16_f32 v221, v222, v223\n"
"v_mfma_f32_16x16x32_bf16 v[198:201], v[128:131], v[20:23], v[198:201]\n"
"ds_write_b64 v6, v[220:221] offset:57352\n"
"s_waitcnt lgkmcnt(4)\n"
"v_mfma_f32_16x16x32_bf16 v[248:251], v[12:15], v[20:23], v[248:251]\n"
"s_waitcnt lgkmcnt(3)\n"
"ds_read_b128 v[244:247], v6 offset:7168\n"
"v_mfma_f32_16x16x32_bf16 v[140:143], v[0:3], v[144:147], v[140:143]\n"
"ds_read_b128 v[8:11], v4 offset:16384\n"
"v_mfma_f32_16x16x32_bf16 v[198:201], v[16:19], v[144:147], v[198:201]\n"
"v_sub_f32 v196, v196, v214\n"
"v_exp_f32 v196, v196\n"
"v_sub_f32 v197, v197, v214\n"
"v_exp_f32 v197, v197\n"
"s_waitcnt lgkmcnt(4)\n"
"v_mfma_f32_16x16x32_bf16 v[248:251], v[216:219], v[144:147], v[248:251]\n"
"s_waitcnt lgkmcnt(3)\n"
"ds_read_b128 v[220:223], v6 offset:8192\n"
"v_cvt_pk_bf16_f32 v196, v196, v197\n"
"v_mov_b32 v197, 0\n"
"v_mfma_f32_16x16x32_bf16 v[140:143], v[132:135], v[252:255], v[140:143]\n"
"ds_write_b64 v6, v[196:197] offset:58368\n"
"v_mfma_f32_16x16x32_bf16 v[198:201], v[136:139], v[252:255], v[198:201]\n"
"s_waitcnt lgkmcnt(3)\n"
"ds_read_b128 v[12:15], v5 offset:16384\n"
"v_mfma_f32_16x16x32_bf16 v[248:251], v[244:247], v[252:255], v[248:251]\n"
"s_waitcnt lgkmcnt(3)\n"
"ds_read_b128 v[20:23], v6 offset:9216\n"
"v_mfma_f32_16x16x32_bf16 v[140:143], v[100:103], v[8:11], v[140:143]\n"
"ds_read_b128 v[144:147], v4 offset:0\n"
"v_mfma_f32_16x16x32_bf16 v[198:201], v[104:107], v[8:11], v[198:201]\n"
"s_waitcnt lgkmcnt(3)\n"
"s_barrier\n"
"v_mfma_f32_16x16x32_bf16 v[248:251], v[220:223], v[8:11], v[248:251]\n"
"s_waitcnt lgkmcnt(2)\n"
"v_mfma_f32_16x16x32_bf16 v[140:143], v[108:111], v[12:15], v[140:143]\n"
"ds_read_b128 v[244:247], v5 offset:0\n"
"v_mfma_f32_16x16x32_bf16 v[198:201], v[112:115], v[12:15], v[198:201]\n"
"s_waitcnt lgkmcnt(2)\n"
"v_mfma_f32_16x16x32_bf16 v[248:251], v[20:23], v[12:15], v[248:251]\n"
"s_waitcnt lgkmcnt(1)\n"
"v_mfma_f32_16x16x32_bf16 v[214:217], v[28:31], v[144:147], 0\n"
"ds_read_b128 v[252:255], v4 offset:2048\n"
"v_mfma_f32_16x16x32_bf16 v[220:223], v[32:35], v[144:147], 0\n"
"v_mfma_f32_16x16x32_bf16 v[8:11], v[36:39], v[144:147], 0\n"
"s_waitcnt lgkmcnt(1)\n"
"v_mfma_f32_16x16x32_bf16 v[214:217], v[40:43], v[244:247], v[214:217]\n"
"ds_read_b128 v[148:151], v5 offset:2048\n"
"v_mul_f32 v140, s60, v140\n"
"v_mfma_f32_16x16x32_bf16 v[220:223], v[44:47], v[244:247], v[220:223]\n"
"v_mul_f32 v141, s60, v141\n"
"v_cvt_pk_bf16_f32 v140, v140, v141\n"
"v_mfma_f32_16x16x32_bf16 v[8:11], v[48:51], v[244:247], v[8:11]\n"
"s_waitcnt lgkmcnt(1)\n"
"v_mul_f32 v142, s60, v142\n"
"v_mul_f32 v143, s60, v143\n"
"v_mfma_f32_16x16x32_bf16 v[214:217], v[52:55], v[252:255], v[214:217]\n"
"ds_read_b128 v[12:15], v4 offset:4096\n"
"v_cvt_pk_bf16_f32 v141, v142, v143\n"
"v_mfma_f32_16x16x32_bf16 v[220:223], v[56:59], v[252:255], v[220:223]\n"
"v_mul_f32 v198, s60, v198\n"
"v_mul_f32 v199, s60, v199\n"
"v_mfma_f32_16x16x32_bf16 v[8:11], v[60:63], v[252:255], v[8:11]\n"
"v_mul_f32 v200, s60, v200\n"
"v_mul_f32 v201, s60, v201\n"
"v_cvt_pk_bf16_f32 v142, v198, v199\n"
"v_cvt_pk_bf16_f32 v143, v200, v201\n"
"v_mul_f32 v248, s60, v248\n"
"v_mul_f32 v249, s60, v249\n"
"v_mul_f32 v250, s60, v250\n"
"v_mul_f32 v251, s60, v251\n"
"s_waitcnt lgkmcnt(1)\n"
"v_mfma_f32_16x16x32_bf16 v[214:217], v[64:67], v[148:151], v[214:217]\n"
"ds_read_b128 v[196:199], v5 offset:4096\n"
"v_cvt_pk_bf16_f32 v26, v248, v249\n"
"v_mfma_f32_16x16x32_bf16 v[220:223], v[68:71], v[148:151], v[220:223]\n"
"v_cvt_pk_bf16_f32 v27, v250, v251\n"
"v_mfma_f32_16x16x32_bf16 v[8:11], v[72:75], v[148:151], v[8:11]\n"
"s_waitcnt lgkmcnt(1)\n"
"v_mfma_f32_16x16x32_bf16 v[214:217], v[76:79], v[12:15], v[214:217]\n"
"ds_read_b128 v[252:255], v4 offset:6144\n"
"v_mfma_f32_16x16x32_bf16 v[220:223], v[80:83], v[12:15], v[220:223]\n"
"v_mfma_f32_16x16x32_bf16 v[8:11], v[84:87], v[12:15], v[8:11]\n"
"s_waitcnt lgkmcnt(1)\n"
"v_mfma_f32_16x16x32_bf16 v[214:217], v[88:91], v[196:199], v[214:217]\n"
"ds_read_b128 v[248:251], v5 offset:6144\n"
"v_mfma_f32_16x16x32_bf16 v[220:223], v[92:95], v[196:199], v[220:223]\n"
"v_mfma_f32_16x16x32_bf16 v[8:11], v[96:99], v[196:199], v[8:11]\n"
"s_waitcnt lgkmcnt(1)\n"
"v_mfma_f32_16x16x32_bf16 v[244:247], v[28:31], v[252:255], 0\n"
"ds_read_b128 v[20:23], v4 offset:8192\n"
"v_mfma_f32_16x16x32_bf16 v[12:15], v[32:35], v[252:255], 0\n"
"v_mfma_f32_16x16x32_bf16 v[144:147], v[36:39], v[252:255], 0\n"
"s_waitcnt lgkmcnt(1)\n"
"v_mfma_f32_16x16x32_bf16 v[244:247], v[40:43], v[248:251], v[244:247]\n"
"ds_read_b128 v[148:151], v5 offset:8192\n"
"v_cvt_pk_bf16_f32 v214, v214, v215\n"
"v_cvt_pk_bf16_f32 v215, v216, v217\n"
"v_cvt_pk_bf16_f32 v216, v220, v221\n"
"v_cvt_pk_bf16_f32 v217, v222, v223\n"
"v_cvt_pk_bf16_f32 v218, v8, v9\n"
"v_cvt_pk_bf16_f32 v219, v10, v11\n"
"v_mfma_f32_16x16x32_bf16 v[12:15], v[44:47], v[248:251], v[12:15]\n"
"v_mfma_f32_16x16x32_bf16 v[144:147], v[48:51], v[248:251], v[144:147]\n"
"s_waitcnt lgkmcnt(1)\n"
"v_bfe_u32 v225, v6, 2, 6\n"
"v_mfma_f32_16x16x32_bf16 v[244:247], v[52:55], v[20:23], v[244:247]\n"
"ds_read_b128 v[196:199], v4 offset:10240\n"
"v_mfma_f32_16x16x32_bf16 v[12:15], v[56:59], v[20:23], v[12:15]\n"
"s_add_u32 s71, s50, 4294967280\n"
"ds_read_b32 v225, v225 offset:1280\n"
"v_mfma_f32_16x16x32_bf16 v[144:147], v[60:63], v[20:23], v[144:147]\n"
"s_waitcnt lgkmcnt(2)\n"
"v_mfma_f32_16x16x32_bf16 v[244:247], v[64:67], v[148:151], v[244:247]\n"
"ds_read_b128 v[220:223], v5 offset:10240\n"
"v_mfma_f32_16x16x32_bf16 v[12:15], v[68:71], v[148:151], v[12:15]\n"
"v_mfma_f32_16x16x32_bf16 v[144:147], v[72:75], v[148:151], v[144:147]\n"
"s_add_u32 s72, s50, 1024\n"
"s_waitcnt lgkmcnt(2)\n"
"v_mfma_f32_16x16x32_bf16 v[244:247], v[76:79], v[196:199], v[244:247]\n"
"s_waitcnt lgkmcnt(1)\n"
"v_add_u32_sdwa v224, v225, v7 dst_sel:DWORD dst_unused:UNUSED_PAD src0_sel:WORD_0 src1_sel:DWORD\n"
"v_mfma_f32_16x16x32_bf16 v[12:15], v[80:83], v[196:199], v[12:15]\n"
"v_max_u32 v201, s71, v224\n"
"v_mfma_f32_16x16x32_bf16 v[144:147], v[84:87], v[196:199], v[144:147]\n"
"s_waitcnt lgkmcnt(0)\n"
"ds_read_b128 v[250:253], v201 offset:16\n"
"v_min_u32 v200, s72, v224\n"
"v_mfma_f32_16x16x32_bf16 v[244:247], v[88:91], v[220:223], v[244:247]\n"
"ds_read_b128 v[8:11], v201 offset:32\n"
"v_mfma_f32_16x16x32_bf16 v[12:15], v[92:95], v[220:223], v[12:15]\n"
"ds_read_b128 v[20:23], v200 offset:1376\n"
"v_mfma_f32_16x16x32_bf16 v[144:147], v[96:99], v[220:223], v[144:147]\n"
"s_waitcnt lgkmcnt(2)\n"
"v_mfma_f32_16x16x32_bf16 v[250:253], v[202:205], v[140:143], v[250:253]\n"
"ds_read_b128 v[196:199], v200 offset:1360\n"
"v_mfma_f32_16x16x32_bf16 v[250:253], v[204:207], v[24:27], v[250:253]\n"
"s_waitcnt lgkmcnt(2)\n"
"v_mfma_f32_16x16x32_bf16 v[8:11], v[208:211], v[140:143], v[8:11]\n"
"ds_read_b128 v[148:151], v201 offset:48\n"
"v_cvt_pk_bf16_f32 v244, v244, v245\n"
"v_cvt_pk_bf16_f32 v245, v246, v247\n"
"v_cvt_pk_bf16_f32 v246, v12, v13\n"
"v_cvt_pk_bf16_f32 v247, v14, v15\n"
"v_cvt_pk_bf16_f32 v248, v144, v145\n"
"v_cvt_pk_bf16_f32 v249, v146, v147\n"
"v_mfma_f32_16x16x32_bf16 v[8:11], v[210:213], v[24:27], v[8:11]\n"
"s_waitcnt lgkmcnt(2)\n"
"v_max3_f32 v224, v250, v251, v252\n"
"v_max_f32 v224, v224, v253\n"
"v_mfma_f32_16x16x32_bf16 v[20:23], v[238:241], v[140:143], v[20:23]\n"
"ds_read_b128 v[220:223], v200 offset:1392\n"
"v_mfma_f32_16x16x32_bf16 v[20:23], v[240:243], v[24:27], v[20:23]\n"
"s_waitcnt lgkmcnt(2)\n"
"s_nop 0\n"
"v_max3_f32 v224, v224, v8, v9\n"
"v_max3_f32 v224, v224, v10, v11\n"
"v_mfma_f32_16x16x32_bf16 v[196:199], v[232:235], v[140:143], v[196:199]\n"
"ds_read_b128 v[250:253], v201 offset:16\n"
"v_mfma_f32_16x16x32_bf16 v[196:199], v[234:237], v[24:27], v[196:199]\n"
"s_waitcnt lgkmcnt(2)\n"
"v_max3_f32 v224, v224, v20, v21\n"
"v_max3_f32 v224, v224, v22, v23\n"
"v_mfma_f32_16x16x32_bf16 v[148:151], v[214:217], v[140:143], v[148:151]\n"
"ds_read_b128 v[8:11], v201 offset:32\n"
"v_mfma_f32_16x16x32_bf16 v[148:151], v[216:219], v[24:27], v[148:151]\n"
"s_waitcnt lgkmcnt(2)\n"
"s_nop 0\n"
"v_max3_f32 v224, v224, v196, v197\n"
"v_max3_f32 v224, v224, v198, v199\n"
"v_mfma_f32_16x16x32_bf16 v[220:223], v[244:247], v[140:143], v[220:223]\n"
"ds_read_b128 v[12:15], v200 offset:1376\n"
"v_mfma_f32_16x16x32_bf16 v[220:223], v[246:249], v[24:27], v[220:223]\n"
"s_waitcnt lgkmcnt(2)\n"
"v_max3_f32 v224, v224, v148, v149\n"
"v_max3_f32 v224, v224, v150, v151\n"
"v_mfma_f32_16x16x32_bf16 v[250:253], v[202:205], v[140:143], v[250:253]\n"
"ds_read_b128 v[196:199], v200 offset:1360\n"
"v_mfma_f32_16x16x32_bf16 v[250:253], v[204:207], v[24:27], v[250:253]\n"
"s_nop 1\n"
"v_max3_f32 v224, v224, v220, v221\n"
"v_max3_f32 v224, v224, v222, v223\n"
"v_mov_b32 v225, v224\n"
"s_nop 1\n"
"v_permlane16_swap_b32 v224, v225\n"
"v_max_f32 v224, v224, v225\n"
"v_mov_b32 v225, v224\n"
"s_nop 1\n"
"v_permlane32_swap_b32 v224, v225\n"
"v_max_f32 v224, v224, v225\n"
"v_sub_f32 v250, v250, v224\n"
"v_exp_f32 v250, v250\n"
"v_sub_f32 v251, v251, v224\n"
"v_exp_f32 v251, v251\n"
"v_sub_f32 v252, v252, v224\n"
"v_exp_f32 v252, v252\n"
"v_sub_f32 v253, v253, v224\n"
"v_exp_f32 v253, v253\n"
"s_waitcnt lgkmcnt(2)\n"
"v_mfma_f32_16x16x32_bf16 v[8:11], v[208:211], v[140:143], v[8:11]\n"
"ds_read_b128 v[220:223], v201 offset:48\n"
"v_cvt_pk_bf16_f32 v250, v250, v251\n"
"v_mfma_f32_16x16x32_bf16 v[8:11], v[210:213], v[24:27], v[8:11]\n"
"v_cvt_pk_bf16_f32 v251, v252, v253\n"
"ds_write_b64 v6, v[250:251] offset:55296\n"
"s_waitcnt lgkmcnt(3)\n"
"v_mfma_f32_16x16x32_bf16 v[12:15], v[238:241], v[140:143], v[12:15]\n"
"ds_read_b128 v[20:23], v200 offset:1392\n"
"v_mfma_f32_16x16x32_bf16 v[12:15], v[240:243], v[24:27], v[12:15]\n"
"s_nop 1\n"
"v_sub_f32 v8, v8, v224\n"
"v_exp_f32 v8, v8\n"
"v_sub_f32 v9, v9, v224\n"
"v_exp_f32 v9, v9\n"
"v_sub_f32 v10, v10, v224\n"
"v_exp_f32 v10, v10\n"
"v_sub_f32 v11, v11, v224\n"
"v_exp_f32 v11, v11\n"
"v_sub_f32 v12, v12, v224\n"
"v_exp_f32 v12, v12\n"
"v_sub_f32 v13, v13, v224\n"
"v_exp_f32 v13, v13\n"
"v_cvt_pk_bf16_f32 v8, v8, v9\n"
"v_sub_f32 v14, v14, v224\n"
"v_exp_f32 v14, v14\n"
"v_cvt_pk_bf16_f32 v9, v10, v11\n"
"v_sub_f32 v15, v15, v224\n"
"v_exp_f32 v15, v15\n"
"v_cvt_pk_bf16_f32 v12, v12, v13\n"
"v_cvt_pk_bf16_f32 v13, v14, v15\n"
"ds_write_b64 v6, v[8:9] offset:55304\n"
"s_waitcnt lgkmcnt(4)\n"
"v_mfma_f32_16x16x32_bf16 v[196:199], v[232:235], v[140:143], v[196:199]\n"
"ds_write_b64 v6, v[12:13] offset:56320\n"
"v_mfma_f32_16x16x32_bf16 v[196:199], v[234:237], v[24:27], v[196:199]\n"
"s_waitcnt lgkmcnt(4)\n"
"ds_read_b128 v[250:253], v4 offset:18432\n"
"v_mfma_f32_16x16x32_bf16 v[220:223], v[214:217], v[140:143], v[220:223]\n"
"ds_read_b128 v[144:147], v6 offset:4096\n"
"v_mfma_f32_16x16x32_bf16 v[220:223], v[216:219], v[24:27], v[220:223]\n"
"s_waitcnt lgkmcnt(2)\n"
"ds_read_b128 v[8:11], v5 offset:18432\n"
"s_barrier\n"
"v_sub_f32 v196, v196, v224\n"
"v_exp_f32 v196, v196\n"
"v_sub_f32 v197, v197, v224\n"
"v_exp_f32 v197, v197\n"
"v_sub_f32 v198, v198, v224\n"
"v_exp_f32 v198, v198\n"
"v_sub_f32 v199, v199, v224\n"
"v_exp_f32 v199, v199\n"
"v_cvt_pk_bf16_f32 v196, v196, v197\n"
"v_cvt_pk_bf16_f32 v197, v198, v199\n"
"v_sub_f32 v220, v220, v224\n"
"v_exp_f32 v220, v220\n"
"v_sub_f32 v221, v221, v224\n"
"v_exp_f32 v221, v221\n"
"v_sub_f32 v222, v222, v224\n"
"v_exp_f32 v222, v222\n"
"v_sub_f32 v223, v223, v224\n"
"v_exp_f32 v223, v223\n"
"v_cvt_pk_bf16_f32 v220, v220, v221\n"
"v_cvt_pk_bf16_f32 v221, v222, v223\n"
"ds_write_b64 v6, v[196:197] offset:57352\n"
"v_mfma_f32_16x16x32_bf16 v[20:23], v[244:247], v[140:143], v[20:23]\n"
"ds_write_b64 v6, v[220:221] offset:57344\n"
"v_mfma_f32_16x16x32_bf16 v[20:23], v[246:249], v[24:27], v[20:23]\n"
"s_waitcnt lgkmcnt(4)\n"
"ds_read_b128 v[198:201], v6 offset:5120\n"
"v_mfma_f32_16x16x32_bf16 v[12:15], v[116:119], v[250:253], 0\n"
"ds_read_b128 v[148:151], v4 offset:20480\n"
"v_mfma_f32_16x16x32_bf16 v[152:155], v[120:123], v[250:253], 0\n"
"s_waitcnt lgkmcnt(5)\n"
"v_mfma_f32_16x16x32_bf16 v[140:143], v[144:147], v[250:253], 0\n"
"s_waitcnt lgkmcnt(4)\n"
"ds_read_b128 v[220:223], v6 offset:6144\n"
"v_mfma_f32_16x16x32_bf16 v[12:15], v[124:127], v[8:11], v[12:15]\n"
"ds_read_b128 v[156:159], v5 offset:20480\n"
"v_mfma_f32_16x16x32_bf16 v[152:155], v[128:131], v[8:11], v[152:155]\n"
"v_sub_f32 v20, v20, v224\n"
"v_exp_f32 v20, v20\n"
"v_sub_f32 v21, v21, v224\n"
"v_exp_f32 v21, v21\n"
"s_waitcnt lgkmcnt(3)\n"
"v_mfma_f32_16x16x32_bf16 v[140:143], v[198:201], v[8:11], v[140:143]\n"
"s_waitcnt lgkmcnt(2)\n"
"ds_read_b128 v[250:253], v6 offset:7168\n"
"v_cvt_pk_bf16_f32 v20, v20, v21\n"
"v_mfma_f32_16x16x32_bf16 v[12:15], v[0:3], v[148:151], v[12:15]\n"
"ds_read_b128 v[144:147], v4 offset:22528\n"
"v_mfma_f32_16x16x32_bf16 v[152:155], v[16:19], v[148:151], v[152:155]\n"
"s_waitcnt lgkmcnt(3)\n"
"v_sub_f32 v22, v22, v224\n"
"v_exp_f32 v22, v22\n"
"v_mfma_f32_16x16x32_bf16 v[140:143], v[220:223], v[148:151], v[140:143]\n"
"s_waitcnt lgkmcnt(2)\n"
"ds_read_b128 v[8:11], v6 offset:8192\n"
"v_sub_f32 v23, v23, v224\n"
"v_exp_f32 v23, v23\n"
"v_mfma_f32_16x16x32_bf16 v[12:15], v[132:135], v[156:159], v[12:15]\n"
"ds_read_b128 v[160:163], v5 offset:22528\n"
"v_cvt_pk_bf16_f32 v21, v22, v23\n"
"v_mfma_f32_16x16x32_bf16 v[152:155], v[136:139], v[156:159], v[152:155]\n"
"ds_write_b64 v6, v[20:21] offset:56328\n"
"s_waitcnt lgkmcnt(4)\n"
"v_mfma_f32_16x16x32_bf16 v[140:143], v[250:253], v[156:159], v[140:143]\n"
"s_waitcnt lgkmcnt(3)\n"
"ds_read_b128 v[220:223], v6 offset:9216\n"
"v_mfma_f32_16x16x32_bf16 v[12:15], v[100:103], v[144:147], v[12:15]\n"
"ds_read_b128 v[148:151], v4 offset:24576\n"
"v_mfma_f32_16x16x32_bf16 v[152:155], v[104:107], v[144:147], v[152:155]\n"
"s_waitcnt lgkmcnt(2)\n"
"s_barrier\n"
"v_mfma_f32_16x16x32_bf16 v[140:143], v[8:11], v[144:147], v[140:143]\n"
"v_mfma_f32_16x16x32_bf16 v[12:15], v[108:111], v[160:163], v[12:15]\n"
"ds_read_b128 v[250:253], v5 offset:24576\n"
"v_mfma_f32_16x16x32_bf16 v[152:155], v[112:115], v[160:163], v[152:155]\n"
"s_waitcnt lgkmcnt(2)\n"
"v_mfma_f32_16x16x32_bf16 v[140:143], v[220:223], v[160:163], v[140:143]\n"
"s_waitcnt lgkmcnt(1)\n"
"v_mfma_f32_16x16x32_bf16 v[196:199], v[28:31], v[148:151], 0\n"
"ds_read_b128 v[20:23], v4 offset:26624\n"
"v_mfma_f32_16x16x32_bf16 v[8:11], v[32:35], v[148:151], 0\n"
"v_mfma_f32_16x16x32_bf16 v[144:147], v[36:39], v[148:151], 0\n"
"s_waitcnt lgkmcnt(1)\n"
"v_mfma_f32_16x16x32_bf16 v[196:199], v[40:43], v[250:253], v[196:199]\n"
"ds_read_b128 v[156:159], v5 offset:26624\n"
"v_mul_f32 v12, s60, v12\n"
"v_mfma_f32_16x16x32_bf16 v[8:11], v[44:47], v[250:253], v[8:11]\n"
"v_mul_f32 v13, s60, v13\n"
"v_cvt_pk_bf16_f32 v12, v12, v13\n"
"v_mfma_f32_16x16x32_bf16 v[144:147], v[48:51], v[250:253], v[144:147]\n"
"v_mul_f32 v14, s60, v14\n"
"v_mul_f32 v15, s60, v15\n"
"v_cvt_pk_bf16_f32 v13, v14, v15\n"
"v_mul_f32 v152, s60, v152\n"
"v_mul_f32 v153, s60, v153\n"
"s_waitcnt lgkmcnt(1)\n"
"v_mfma_f32_16x16x32_bf16 v[196:199], v[52:55], v[20:23], v[196:199]\n"
"ds_read_b128 v[250:253], v4 offset:28672\n"
"v_mul_f32 v154, s60, v154\n"
"v_mfma_f32_16x16x32_bf16 v[8:11], v[56:59], v[20:23], v[8:11]\n"
"v_mul_f32 v155, s60, v155\n"
"v_cvt_pk_bf16_f32 v14, v152, v153\n"
"v_mfma_f32_16x16x32_bf16 v[144:147], v[60:63], v[20:23], v[144:147]\n"
"v_cvt_pk_bf16_f32 v15, v154, v155\n"
"v_mul_f32 v140, s60, v140\n"
"v_mul_f32 v141, s60, v141\n"
"v_mul_f32 v142, s60, v142\n"
"v_mul_f32 v143, s60, v143\n"
"s_waitcnt lgkmcnt(1)\n"
"v_mfma_f32_16x16x32_bf16 v[196:199], v[64:67], v[156:159], v[196:199]\n"
"ds_read_b128 v[20:23], v5 offset:28672\n"
"v_cvt_pk_bf16_f32 v26, v140, v141\n"
"v_mfma_f32_16x16x32_bf16 v[8:11], v[68:71], v[156:159], v[8:11]\n"
"v_cvt_pk_bf16_f32 v27, v142, v143\n"
"v_mfma_f32_16x16x32_bf16 v[144:147], v[72:75], v[156:159], v[144:147]\n"
"s_waitcnt lgkmcnt(1)\n"
"v_mfma_f32_16x16x32_bf16 v[196:199], v[76:79], v[250:253], v[196:199]\n"
"ds_read_b128 v[148:151], v4 offset:30720\n"
"v_mfma_f32_16x16x32_bf16 v[8:11], v[80:83], v[250:253], v[8:11]\n"
"v_mfma_f32_16x16x32_bf16 v[144:147], v[84:87], v[250:253], v[144:147]\n"
"s_waitcnt lgkmcnt(1)\n"
"v_mfma_f32_16x16x32_bf16 v[196:199], v[88:91], v[20:23], v[196:199]\n"
"ds_read_b128 v[140:143], v5 offset:30720\n"
"v_mfma_f32_16x16x32_bf16 v[8:11], v[92:95], v[20:23], v[8:11]\n"
"v_mfma_f32_16x16x32_bf16 v[144:147], v[96:99], v[20:23], v[144:147]\n"
"s_waitcnt lgkmcnt(1)\n"
"v_mfma_f32_16x16x32_bf16 v[220:223], v[28:31], v[148:151], 0\n"
"ds_read_b128 v[152:155], v4 offset:32768\n"
"v_mfma_f32_16x16x32_bf16 v[250:253], v[32:35], v[148:151], 0\n"
"v_mfma_f32_16x16x32_bf16 v[156:159], v[36:39], v[148:151], 0\n"
"s_waitcnt lgkmcnt(1)\n"
"v_mfma_f32_16x16x32_bf16 v[220:223], v[40:43], v[140:143], v[220:223]\n"
"ds_read_b128 v[160:163], v5 offset:32768\n"
"v_cvt_pk_bf16_f32 v196, v196, v197\n"
"v_cvt_pk_bf16_f32 v197, v198, v199\n"
"v_cvt_pk_bf16_f32 v198, v8, v9\n"
"v_cvt_pk_bf16_f32 v199, v10, v11\n"
"v_cvt_pk_bf16_f32 v200, v144, v145\n"
"v_cvt_pk_bf16_f32 v201, v146, v147\n"
"v_mfma_f32_16x16x32_bf16 v[250:253], v[44:47], v[140:143], v[250:253]\n"
"v_mfma_f32_16x16x32_bf16 v[156:159], v[48:51], v[140:143], v[156:159]\n"
"s_waitcnt lgkmcnt(1)\n"
"v_bfe_u32 v255, v6, 2, 6\n"
"v_mfma_f32_16x16x32_bf16 v[220:223], v[52:55], v[152:155], v[220:223]\n"
"ds_read_b128 v[8:11], v4 offset:34816\n"
"v_mfma_f32_16x16x32_bf16 v[250:253], v[56:59], v[152:155], v[250:253]\n"
"s_add_u32 s69, s50, 4294966976\n"
"ds_read_b32 v255, v255 offset:1344\n"
"v_mfma_f32_16x16x32_bf16 v[156:159], v[60:63], v[152:155], v[156:159]\n"
"s_waitcnt lgkmcnt(2)\n"
"v_mfma_f32_16x16x32_bf16 v[220:223], v[64:67], v[160:163], v[220:223]\n"
"ds_read_b128 v[20:23], v5 offset:34816\n"
"v_mfma_f32_16x16x32_bf16 v[250:253], v[68:71], v[160:163], v[250:253]\n"
"v_mfma_f32_16x16x32_bf16 v[156:159], v[72:75], v[160:163], v[156:159]\n"
"s_add_u32 s73, s50, 4294967280\n"
"s_waitcnt lgkmcnt(2)\n"
"v_mfma_f32_16x16x32_bf16 v[220:223], v[76:79], v[8:11], v[220:223]\n"
"s_waitcnt lgkmcnt(1)\n"
"v_add_u32_sdwa v254, v255, v7 dst_sel:DWORD dst_unused:UNUSED_PAD src0_sel:WORD_0 src1_sel:DWORD\n"
"v_mfma_f32_16x16x32_bf16 v[250:253], v[80:83], v[8:11], v[250:253]\n"
"v_min_u32 v177, s69, v254\n"
"v_mfma_f32_16x16x32_bf16 v[156:159], v[84:87], v[8:11], v[156:159]\n"
"s_waitcnt lgkmcnt(0)\n"
"ds_read_b128 v[140:143], v177 offset:2704\n"
"v_max_u32 v176, s73, v254\n"
"v_mfma_f32_16x16x32_bf16 v[220:223], v[88:91], v[20:23], v[220:223]\n"
"s_add_u32 s68, s50, 4294965936\n"
"ds_read_b128 v[144:147], v177 offset:2720\n"
"v_mfma_f32_16x16x32_bf16 v[250:253], v[92:95], v[20:23], v[250:253]\n"
"ds_read_b128 v[8:11], v176 offset:32\n"
"v_mfma_f32_16x16x32_bf16 v[156:159], v[96:99], v[20:23], v[156:159]\n"
"s_waitcnt lgkmcnt(2)\n"
"v_mfma_f32_16x16x32_bf16 v[140:143], v[184:187], v[12:15], v[140:143]\n"
"ds_read_b128 v[148:151], v176 offset:48\n"
"v_max_u32 v255, s68, v254\n"
"v_cvt_pk_bf16_f32 v220, v220, v221\n"
"v_cvt_pk_bf16_f32 v221, v222, v223\n"
"v_mfma_f32_16x16x32_bf16 v[140:143], v[186:189], v[24:27], v[140:143]\n"
"v_cvt_pk_bf16_f32 v222, v250, v251\n"
"v_cvt_pk_bf16_f32 v223, v252, v253\n"
"v_cvt_pk_bf16_f32 v224, v156, v157\n"
"v_cvt_pk_bf16_f32 v225, v158, v159\n"
"s_waitcnt lgkmcnt(2)\n"
"v_mfma_f32_16x16x32_bf16 v[144:147], v[190:193], v[12:15], v[144:147]\n"
"ds_read_b128 v[20:23], v255 offset:1392\n"
"v_mfma_f32_16x16x32_bf16 v[144:147], v[192:195], v[24:27], v[144:147]\n"
"s_waitcnt lgkmcnt(2)\n"
"v_max3_f32 v250, v140, v141, v142\n"
"v_max_f32 v250, v250, v143\n"
"v_mfma_f32_16x16x32_bf16 v[8:11], v[208:211], v[12:15], v[8:11]\n"
"ds_read_b128 v[152:155], v176 offset:16\n"
"v_mfma_f32_16x16x32_bf16 v[8:11], v[210:213], v[24:27], v[8:11]\n"
"s_waitcnt lgkmcnt(2)\n"
"s_nop 0\n"
"v_max3_f32 v250, v250, v144, v145\n"
"v_max3_f32 v250, v250, v146, v147\n"
"v_mfma_f32_16x16x32_bf16 v[148:151], v[214:217], v[12:15], v[148:151]\n"
"ds_read_b128 v[140:143], v255 offset:1360\n"
"v_mfma_f32_16x16x32_bf16 v[148:151], v[216:219], v[24:27], v[148:151]\n"
"s_waitcnt lgkmcnt(2)\n"
"v_max3_f32 v250, v250, v8, v9\n"
"v_max3_f32 v250, v250, v10, v11\n"
"v_mfma_f32_16x16x32_bf16 v[20:23], v[244:247], v[12:15], v[20:23]\n"
"ds_read_b128 v[144:147], v255 offset:1376\n"
"v_mfma_f32_16x16x32_bf16 v[20:23], v[246:249], v[24:27], v[20:23]\n"
"s_waitcnt lgkmcnt(2)\n"
"s_nop 0\n"
"v_max3_f32 v250, v250, v148, v149\n"
"v_max3_f32 v250, v250, v150, v151\n"
"v_mfma_f32_16x16x32_bf16 v[152:155], v[202:205], v[12:15], v[152:155]\n"
"ds_read_b128 v[8:11], v177 offset:2736\n"
"v_mfma_f32_16x16x32_bf16 v[152:155], v[204:207], v[24:27], v[152:155]\n"
"s_waitcnt lgkmcnt(2)\n"
"v_max3_f32 v250, v250, v20, v21\n"
"v_max3_f32 v250, v250, v22, v23\n"
"v_mfma_f32_16x16x32_bf16 v[140:143], v[232:235], v[12:15], v[140:143]\n"
"ds_read_b128 v[148:151], v177 offset:2704\n"
"v_mfma_f32_16x16x32_bf16 v[140:143], v[234:237], v[24:27], v[140:143]\n"
"s_waitcnt lgkmcnt(2)\n"
"s_nop 0\n"
"v_max3_f32 v250, v250, v152, v153\n"
"v_max3_f32 v250, v250, v154, v155\n"
"v_mfma_f32_16x16x32_bf16 v[144:147], v[238:241], v[12:15], v[144:147]\n"
"ds_read_b128 v[20:23], v177 offset:2720\n"
"v_mfma_f32_16x16x32_bf16 v[144:147], v[240:243], v[24:27], v[144:147]\n"
"s_waitcnt lgkmcnt(2)\n"
"v_max3_f32 v250, v250, v140, v141\n"
"v_max3_f32 v250, v250, v142, v143\n"
"v_mfma_f32_16x16x32_bf16 v[8:11], v[196:199], v[12:15], v[8:11]\n"
"ds_read_b128 v[152:155], v176 offset:32\n"
"v_mfma_f32_16x16x32_bf16 v[8:11], v[198:201], v[24:27], v[8:11]\n"
"s_waitcnt lgkmcnt(2)\n"
"s_nop 0\n"
"v_max3_f32 v250, v250, v144, v145\n"
"v_max3_f32 v250, v250, v146, v147\n"
"v_mfma_f32_16x16x32_bf16 v[148:151], v[184:187], v[12:15], v[148:151]\n"
"ds_read_b128 v[140:143], v176 offset:48\n"
"v_mfma_f32_16x16x32_bf16 v[148:151], v[186:189], v[24:27], v[148:151]\n"
"s_nop 0\n"
"v_max3_f32 v250, v250, v8, v9\n"
"v_max3_f32 v250, v250, v10, v11\n"
"v_mov_b32 v251, v250\n"
"s_nop 1\n"
"v_permlane16_swap_b32 v250, v251\n"
"v_max_f32 v250, v250, v251\n"
"v_mov_b32 v251, v250\n"
"s_nop 1\n"
"v_permlane32_swap_b32 v250, v251\n"
"v_max_f32 v250, v250, v251\n"
"v_sub_f32 v148, v148, v250\n"
"v_exp_f32 v148, v148\n"
"v_sub_f32 v149, v149, v250\n"
"v_exp_f32 v149, v149\n"
"v_sub_f32 v150, v150, v250\n"
"v_exp_f32 v150, v150\n"
"v_sub_f32 v151, v151, v250\n"
"v_exp_f32 v151, v151\n"
"s_waitcnt lgkmcnt(2)\n"
"v_mfma_f32_16x16x32_bf16 v[20:23], v[190:193], v[12:15], v[20:23]\n"
"ds_read_b128 v[8:11], v255 offset:1392\n"
"v_cvt_pk_bf16_f32 v148, v148, v149\n"
"v_mfma_f32_16x16x32_bf16 v[20:23], v[192:195], v[24:27], v[20:23]\n"
"v_cvt_pk_bf16_f32 v149, v150, v151\n"
"ds_write_b64 v6, v[148:149] offset:53248\n"
"s_waitcnt lgkmcnt(3)\n"
"v_mfma_f32_16x16x32_bf16 v[152:155], v[208:211], v[12:15], v[152:155]\n"
"ds_read_b128 v[144:147], v176 offset:16\n"
"v_mfma_f32_16x16x32_bf16 v[152:155], v[210:213], v[24:27], v[152:155]\n"
"s_nop 1\n"
"v_sub_f32 v20, v20, v250\n"
"v_exp_f32 v20, v20\n"
"v_sub_f32 v21, v21, v250\n"
"v_exp_f32 v21, v21\n"
"v_sub_f32 v22, v22, v250\n"
"v_exp_f32 v22, v22\n"
"v_sub_f32 v23, v23, v250\n"
"v_exp_f32 v23, v23\n"
"v_sub_f32 v152, v152, v250\n"
"v_exp_f32 v152, v152\n"
"v_sub_f32 v153, v153, v250\n"
"v_exp_f32 v153, v153\n"
"v_sub_f32 v154, v154, v250\n"
"v_exp_f32 v154, v154\n"
"v_sub_f32 v155, v155, v250\n"
"v_exp_f32 v155, v155\n"
"v_cvt_pk_bf16_f32 v20, v20, v21\n"
"v_cvt_pk_bf16_f32 v21, v22, v23\n"
"v_cvt_pk_bf16_f32 v152, v152, v153\n"
"v_cvt_pk_bf16_f32 v153, v154, v155\n"
"ds_write_b64 v6, v[20:21] offset:53256\n"
"s_waitcnt lgkmcnt(4)\n"
"v_mfma_f32_16x16x32_bf16 v[140:143], v[214:217], v[12:15], v[140:143]\n"
"ds_read_b128 v[148:151], v255 offset:1360\n"
"v_mfma_f32_16x16x32_bf16 v[140:143], v[216:219], v[24:27], v[140:143]\n"
"ds_write_b64 v6, v[152:153] offset:54272\n"
"s_waitcnt lgkmcnt(5)\n"
"v_mfma_f32_16x16x32_bf16 v[8:11], v[244:247], v[12:15], v[8:11]\n"
"ds_read_b128 v[156:159], v255 offset:1376\n"
"v_mfma_f32_16x16x32_bf16 v[8:11], v[246:249], v[24:27], v[8:11]\n"
"s_nop 2\n"
"v_sub_f32 v140, v140, v250\n"
"v_exp_f32 v140, v140\n"
"v_sub_f32 v141, v141, v250\n"
"v_exp_f32 v141, v141\n"
"v_sub_f32 v142, v142, v250\n"
"v_exp_f32 v142, v142\n"
"v_sub_f32 v143, v143, v250\n"
"v_exp_f32 v143, v143\n"
"v_sub_f32 v8, v8, v250\n"
"v_exp_f32 v8, v8\n"
"v_sub_f32 v9, v9, v250\n"
"v_exp_f32 v9, v9\n"
"v_cvt_pk_bf16_f32 v140, v140, v141\n"
"v_sub_f32 v10, v10, v250\n"
"v_exp_f32 v10, v10\n"
"v_cvt_pk_bf16_f32 v141, v142, v143\n"
"v_sub_f32 v11, v11, v250\n"
"v_exp_f32 v11, v11\n"
"v_cvt_pk_bf16_f32 v8, v8, v9\n"
"v_cvt_pk_bf16_f32 v9, v10, v11\n"
"ds_write_b64 v6, v[140:141] offset:54280\n"
"s_waitcnt lgkmcnt(5)\n"
"v_mfma_f32_16x16x32_bf16 v[144:147], v[202:205], v[12:15], v[144:147]\n"
"ds_write_b64 v6, v[8:9] offset:33280\n"
"v_mfma_f32_16x16x32_bf16 v[144:147], v[204:207], v[24:27], v[144:147]\n"
"s_waitcnt lgkmcnt(4)\n"
"ds_read_b128 v[252:255], v177 offset:2736\n"
"v_mfma_f32_16x16x32_bf16 v[148:151], v[232:235], v[12:15], v[148:151]\n"
"ds_read_b128 v[20:23], v4 offset:36864\n"
"v_mfma_f32_16x16x32_bf16 v[148:151], v[234:237], v[24:27], v[148:151]\n"
"s_waitcnt lgkmcnt(2)\n"
"s_barrier\n"
"s_nop 0\n"
"v_sub_f32 v144, v144, v250\n"
"v_exp_f32 v144, v144\n"
"v_sub_f32 v145, v145, v250\n"
"v_exp_f32 v145, v145\n"
"v_sub_f32 v146, v146, v250\n"
"v_exp_f32 v146, v146\n"
"v_sub_f32 v147, v147, v250\n"
"v_exp_f32 v147, v147\n"
"v_sub_f32 v148, v148, v250\n"
"v_exp_f32 v148, v148\n"
"v_cvt_pk_bf16_f32 v144, v144, v145\n"
"v_cvt_pk_bf16_f32 v145, v146, v147\n"
"v_sub_f32 v149, v149, v250\n"
"v_exp_f32 v149, v149\n"
"v_sub_f32 v150, v150, v250\n"
"v_exp_f32 v150, v150\n"
"v_sub_f32 v151, v151, v250\n"
"v_exp_f32 v151, v151\n"
"v_cvt_pk_bf16_f32 v148, v148, v149\n"
"v_cvt_pk_bf16_f32 v149, v150, v151\n"
"ds_write_b64 v6, v[144:145] offset:57352\n"
"v_mfma_f32_16x16x32_bf16 v[156:159], v[238:241], v[12:15], v[156:159]\n"
"ds_read_b128 v[202:205], v6 offset:4096\n"
"v_mfma_f32_16x16x32_bf16 v[156:159], v[240:243], v[24:27], v[156:159]\n"
"ds_write_b64 v6, v[148:149] offset:58368\n"
"s_waitcnt lgkmcnt(4)\n"
"v_mfma_f32_16x16x32_bf16 v[252:255], v[196:199], v[12:15], v[252:255]\n"
"ds_read_b128 v[8:11], v5 offset:36864\n"
"v_mfma_f32_16x16x32_bf16 v[252:255], v[198:201], v[24:27], v[252:255]\n"
"s_waitcnt lgkmcnt(4)\n"
"ds_read_b128 v[140:143], v6 offset:5120\n"
"v_mfma_f32_16x16x32_bf16 v[144:147], v[116:119], v[20:23], 0\n"
"ds_read_b128 v[152:155], v4 offset:38912\n"
"v_mfma_f32_16x16x32_bf16 v[148:151], v[120:123], v[20:23], 0\n"
"v_sub_f32 v156, v156, v250\n"
"v_exp_f32 v156, v156\n"
"v_sub_f32 v157, v157, v250\n"
"v_exp_f32 v157, v157\n"
"v_sub_f32 v158, v158, v250\n"
"v_exp_f32 v158, v158\n"
"s_waitcnt lgkmcnt(4)\n"
"v_mfma_f32_16x16x32_bf16 v[12:15], v[202:205], v[20:23], 0\n"
"ds_read_b128 v[160:163], v6 offset:6144\n"
"v_sub_f32 v159, v159, v250\n"
"v_exp_f32 v159, v159\n"
"v_cvt_pk_bf16_f32 v156, v156, v157\n"
"v_cvt_pk_bf16_f32 v157, v158, v159\n"
"s_waitcnt lgkmcnt(3)\n"
"v_mfma_f32_16x16x32_bf16 v[144:147], v[124:127], v[8:11], v[144:147]\n"
"ds_write_b64 v6, v[156:157] offset:58376\n"
"v_mfma_f32_16x16x32_bf16 v[148:151], v[128:131], v[8:11], v[148:151]\n"
"s_waitcnt lgkmcnt(3)\n"
"ds_read_b128 v[202:205], v5 offset:38912\n"
"v_mfma_f32_16x16x32_bf16 v[12:15], v[140:143], v[8:11], v[12:15]\n"
"s_waitcnt lgkmcnt(3)\n"
"ds_read_b128 v[20:23], v6 offset:7168\n"
"v_mfma_f32_16x16x32_bf16 v[144:147], v[0:3], v[152:155], v[144:147]\n"
"ds_read_b128 v[164:167], v4 offset:40960\n"
"v_sub_f32 v252, v252, v250\n"
"v_exp_f32 v252, v252\n"
"v_sub_f32 v253, v253, v250\n"
"v_exp_f32 v253, v253\n"
"v_mfma_f32_16x16x32_bf16 v[148:151], v[16:19], v[152:155], v[148:151]\n"
"s_waitcnt lgkmcnt(4)\n"
"v_sub_f32 v254, v254, v250\n"
"v_exp_f32 v254, v254\n"
"v_mfma_f32_16x16x32_bf16 v[12:15], v[160:163], v[152:155], v[12:15]\n"
"ds_read_b128 v[8:11], v6 offset:8192\n"
"v_sub_f32 v255, v255, v250\n"
"v_exp_f32 v255, v255\n"
"v_cvt_pk_bf16_f32 v252, v252, v253\n"
"v_cvt_pk_bf16_f32 v253, v254, v255\n"
"s_waitcnt lgkmcnt(3)\n"
"v_mfma_f32_16x16x32_bf16 v[144:147], v[132:135], v[202:205], v[144:147]\n"
"ds_write_b64 v6, v[252:253] offset:57344\n"
"v_mfma_f32_16x16x32_bf16 v[148:151], v[136:139], v[202:205], v[148:151]\n"
"s_waitcnt lgkmcnt(3)\n"
"ds_read_b128 v[140:143], v5 offset:40960\n"
"v_mfma_f32_16x16x32_bf16 v[12:15], v[20:23], v[202:205], v[12:15]\n"
"s_waitcnt lgkmcnt(3)\n"
"ds_read_b128 v[152:155], v6 offset:9216\n"
"v_mfma_f32_16x16x32_bf16 v[144:147], v[100:103], v[164:167], v[144:147]\n"
"ds_read_b128 v[156:159], v4 offset:49152\n"
"v_mfma_f32_16x16x32_bf16 v[148:151], v[104:107], v[164:167], v[148:151]\n"
"s_waitcnt lgkmcnt(3)\n"
"s_barrier\n"
"v_mfma_f32_16x16x32_bf16 v[12:15], v[8:11], v[164:167], v[12:15]\n"
"s_waitcnt lgkmcnt(2)\n"
"v_mfma_f32_16x16x32_bf16 v[144:147], v[108:111], v[140:143], v[144:147]\n"
"ds_read_b128 v[202:205], v5 offset:49152\n"
"v_mfma_f32_16x16x32_bf16 v[148:151], v[112:115], v[140:143], v[148:151]\n"
"s_waitcnt lgkmcnt(2)\n"
"v_mfma_f32_16x16x32_bf16 v[12:15], v[152:155], v[140:143], v[12:15]\n"
"s_waitcnt lgkmcnt(1)\n"
"v_mfma_f32_16x16x32_bf16 v[250:253], v[28:31], v[156:159], 0\n"
"ds_read_b128 v[20:23], v4 offset:51200\n"
"v_mfma_f32_16x16x32_bf16 v[8:11], v[32:35], v[156:159], 0\n"
"v_mfma_f32_16x16x32_bf16 v[160:163], v[36:39], v[156:159], 0\n"
"s_waitcnt lgkmcnt(1)\n"
"v_mfma_f32_16x16x32_bf16 v[250:253], v[40:43], v[202:205], v[250:253]\n"
"ds_read_b128 v[164:167], v5 offset:51200\n"
"v_mul_f32 v144, s60, v144\n"
"v_mfma_f32_16x16x32_bf16 v[8:11], v[44:47], v[202:205], v[8:11]\n"
"v_mul_f32 v145, s60, v145\n"
"v_cvt_pk_bf16_f32 v144, v144, v145\n"
"v_mfma_f32_16x16x32_bf16 v[160:163], v[48:51], v[202:205], v[160:163]\n"
"s_waitcnt lgkmcnt(1)\n"
"v_mul_f32 v146, s60, v146\n"
"v_mul_f32 v147, s60, v147\n"
"v_mfma_f32_16x16x32_bf16 v[250:253], v[52:55], v[20:23], v[250:253]\n"
"ds_read_b128 v[140:143], v4 offset:53248\n"
"v_cvt_pk_bf16_f32 v145, v146, v147\n"
"v_mfma_f32_16x16x32_bf16 v[8:11], v[56:59], v[20:23], v[8:11]\n"
"v_mul_f32 v148, s60, v148\n"
"v_mul_f32 v149, s60, v149\n"
"v_mfma_f32_16x16x32_bf16 v[160:163], v[60:63], v[20:23], v[160:163]\n"
"v_mul_f32 v150, s60, v150\n"
"v_mul_f32 v151, s60, v151\n"
"v_cvt_pk_bf16_f32 v146, v148, v149\n"
"v_cvt_pk_bf16_f32 v147, v150, v151\n"
"v_mul_f32 v12, s60, v12\n"
"v_mul_f32 v13, s60, v13\n"
"v_mul_f32 v14, s60, v14\n"
"v_mul_f32 v15, s60, v15\n"
"s_waitcnt lgkmcnt(1)\n"
"v_mfma_f32_16x16x32_bf16 v[250:253], v[64:67], v[164:167], v[250:253]\n"
"ds_read_b128 v[20:23], v5 offset:53248\n"
"v_cvt_pk_bf16_f32 v26, v12, v13\n"
"v_mfma_f32_16x16x32_bf16 v[8:11], v[68:71], v[164:167], v[8:11]\n"
"v_cvt_pk_bf16_f32 v27, v14, v15\n"
"v_mfma_f32_16x16x32_bf16 v[160:163], v[72:75], v[164:167], v[160:163]\n"
"s_waitcnt lgkmcnt(1)\n"
"v_mfma_f32_16x16x32_bf16 v[250:253], v[76:79], v[140:143], v[250:253]\n"
"ds_read_b128 v[148:151], v4 offset:55296\n"
"v_mfma_f32_16x16x32_bf16 v[8:11], v[80:83], v[140:143], v[8:11]\n"
"v_mfma_f32_16x16x32_bf16 v[160:163], v[84:87], v[140:143], v[160:163]\n"
"s_waitcnt lgkmcnt(1)\n"
"v_mfma_f32_16x16x32_bf16 v[250:253], v[88:91], v[20:23], v[250:253]\n"
"ds_read_b128 v[12:15], v5 offset:55296\n"
"v_mfma_f32_16x16x32_bf16 v[8:11], v[92:95], v[20:23], v[8:11]\n"
"v_mfma_f32_16x16x32_bf16 v[160:163], v[96:99], v[20:23], v[160:163]\n"
"s_waitcnt lgkmcnt(1)\n"
"v_mfma_f32_16x16x32_bf16 v[202:205], v[28:31], v[148:151], 0\n"
"ds_read_b128 v[152:155], v4 offset:57344\n"
"v_mfma_f32_16x16x32_bf16 v[140:143], v[32:35], v[148:151], 0\n"
"v_mfma_f32_16x16x32_bf16 v[156:159], v[36:39], v[148:151], 0\n"
"s_waitcnt lgkmcnt(1)\n"
"v_mfma_f32_16x16x32_bf16 v[202:205], v[40:43], v[12:15], v[202:205]\n"
"ds_read_b128 v[164:167], v5 offset:57344\n"
"v_cvt_pk_bf16_f32 v250, v250, v251\n"
"v_cvt_pk_bf16_f32 v251, v252, v253\n"
"v_cvt_pk_bf16_f32 v252, v8, v9\n"
"v_cvt_pk_bf16_f32 v253, v10, v11\n"
"v_cvt_pk_bf16_f32 v254, v160, v161\n"
"v_cvt_pk_bf16_f32 v255, v162, v163\n"
"v_mfma_f32_16x16x32_bf16 v[140:143], v[44:47], v[12:15], v[140:143]\n"
"v_mfma_f32_16x16x32_bf16 v[156:159], v[48:51], v[12:15], v[156:159]\n"
"s_waitcnt lgkmcnt(1)\n"
"v_bfe_u32 v177, v6, 2, 6\n"
"v_mfma_f32_16x16x32_bf16 v[202:205], v[52:55], v[152:155], v[202:205]\n"
"ds_read_b128 v[8:11], v4 offset:59392\n"
"v_mfma_f32_16x16x32_bf16 v[140:143], v[56:59], v[152:155], v[140:143]\n"
"s_add_u32 s70, s50, 4294967264\n"
"ds_read_b32 v177, v177 offset:1408\n"
"v_mfma_f32_16x16x32_bf16 v[156:159], v[60:63], v[152:155], v[156:159]\n"
"s_waitcnt lgkmcnt(2)\n"
"v_mfma_f32_16x16x32_bf16 v[202:205], v[64:67], v[164:167], v[202:205]\n"
"ds_read_b128 v[12:15], v5 offset:59392\n"
"v_mfma_f32_16x16x32_bf16 v[140:143], v[68:71], v[164:167], v[140:143]\n"
"v_mfma_f32_16x16x32_bf16 v[156:159], v[72:75], v[164:167], v[156:159]\n"
"s_add_u32 s72, s50, 4294966960\n"
"s_waitcnt lgkmcnt(2)\n"
"v_mfma_f32_16x16x32_bf16 v[202:205], v[76:79], v[8:11], v[202:205]\n"
"s_waitcnt lgkmcnt(1)\n"
"v_add_u32_sdwa v176, v177, v7 dst_sel:DWORD dst_unused:UNUSED_PAD src0_sel:WORD_0 src1_sel:DWORD\n"
"v_mfma_f32_16x16x32_bf16 v[140:143], v[80:83], v[8:11], v[140:143]\n"
"v_max_u32 v175, s70, v176\n"
"v_add_u32_sdwa v177, sext(v177), v176 dst_sel:DWORD dst_unused:UNUSED_PAD src0_sel:WORD_1 src1_sel:DWORD\n"
"v_mfma_f32_16x16x32_bf16 v[156:159], v[84:87], v[8:11], v[156:159]\n"
"s_waitcnt lgkmcnt(0)\n"
"ds_read_b128 v[20:23], v175 offset:48\n"
"v_max_u32 v174, s70, v177\n"
"v_mfma_f32_16x16x32_bf16 v[202:205], v[88:91], v[12:15], v[202:205]\n"
"ds_read_b128 v[148:151], v174 offset:64\n"
"v_min_u32 v173, s72, v176\n"
"v_mfma_f32_16x16x32_bf16 v[140:143], v[92:95], v[12:15], v[140:143]\n"
"ds_read_b128 v[8:11], v173 offset:2720\n"
"v_mfma_f32_16x16x32_bf16 v[156:159], v[96:99], v[12:15], v[156:159]\n"
"s_add_u32 s71, s50, 1008\n"
"s_waitcnt lgkmcnt(2)\n"
"v_mfma_f32_16x16x32_bf16 v[20:23], v[214:217], v[144:147], v[20:23]\n"
"ds_read_b128 v[152:155], v173 offset:2736\n"
"v_cvt_pk_bf16_f32 v202, v202, v203\n"
"v_cvt_pk_bf16_f32 v203, v204, v205\n"
"v_mfma_f32_16x16x32_bf16 v[20:23], v[216:219], v[24:27], v[20:23]\n"
"v_cvt_pk_bf16_f32 v204, v140, v141\n"
"v_cvt_pk_bf16_f32 v205, v142, v143\n"
"v_cvt_pk_bf16_f32 v206, v156, v157\n"
"v_cvt_pk_bf16_f32 v207, v158, v159\n"
"s_waitcnt lgkmcnt(2)\n"
"v_mfma_f32_16x16x32_bf16 v[148:151], v[220:223], v[144:147], v[148:151]\n"
"ds_read_b128 v[12:15], v175 offset:32\n"
"v_min_u32 v172, s71, v176\n"
"v_mfma_f32_16x16x32_bf16 v[148:151], v[222:225], v[24:27], v[148:151]\n"
"s_waitcnt lgkmcnt(2)\n"
"v_max3_f32 v170, v20, v21, v22\n"
"v_max_f32 v170, v170, v23\n"
"v_mfma_f32_16x16x32_bf16 v[8:11], v[190:193], v[144:147], v[8:11]\n"
"ds_read_b128 v[140:143], v172 offset:1376\n"
"v_mfma_f32_16x16x32_bf16 v[8:11], v[192:195], v[24:27], v[8:11]\n"
"s_waitcnt lgkmcnt(2)\n"
"s_nop 0\n"
"v_max3_f32 v170, v170, v148, v149\n"
"v_max3_f32 v170, v170, v150, v151\n"
"v_mfma_f32_16x16x32_bf16 v[152:155], v[196:199], v[144:147], v[152:155]\n"
"ds_read_b128 v[20:23], v172 offset:1392\n"
"v_min_u32 v176, s71, v177\n"
"v_mfma_f32_16x16x32_bf16 v[152:155], v[198:201], v[24:27], v[152:155]\n"
"s_waitcnt lgkmcnt(2)\n"
"v_max3_f32 v170, v170, v8, v9\n"
"v_max3_f32 v170, v170, v10, v11\n"
"v_mfma_f32_16x16x32_bf16 v[12:15], v[208:211], v[144:147], v[12:15]\n"
"ds_read_b128 v[148:151], v176 offset:1408\n"
"v_min_u32 v169, s72, v177\n"
"v_mfma_f32_16x16x32_bf16 v[12:15], v[210:213], v[24:27], v[12:15]\n"
"s_waitcnt lgkmcnt(2)\n"
"v_max3_f32 v170, v170, v152, v153\n"
"v_max3_f32 v170, v170, v154, v155\n"
"v_mfma_f32_16x16x32_bf16 v[140:143], v[238:241], v[144:147], v[140:143]\n"
"ds_read_b128 v[8:11], v169 offset:2752\n"
"v_mfma_f32_16x16x32_bf16 v[140:143], v[240:243], v[24:27], v[140:143]\n"
"s_waitcnt lgkmcnt(2)\n"
"s_nop 0\n"
"v_max3_f32 v170, v170, v12, v13\n"
"v_max3_f32 v170, v170, v14, v15\n"
"v_mfma_f32_16x16x32_bf16 v[20:23], v[244:247], v[144:147], v[20:23]\n"
"ds_read_b128 v[152:155], v175 offset:48\n"
"v_mfma_f32_16x16x32_bf16 v[20:23], v[246:249], v[24:27], v[20:23]\n"
"s_waitcnt lgkmcnt(2)\n"
"v_max3_f32 v170, v170, v140, v141\n"
"v_max3_f32 v170, v170, v142, v143\n"
"v_mfma_f32_16x16x32_bf16 v[148:151], v[250:253], v[144:147], v[148:151]\n"
"ds_read_b128 v[12:15], v174 offset:64\n"
"v_mfma_f32_16x16x32_bf16 v[148:151], v[252:255], v[24:27], v[148:151]\n"
"s_waitcnt lgkmcnt(2)\n"
"s_nop 0\n"
"v_max3_f32 v170, v170, v20, v21\n"
"v_max3_f32 v170, v170, v22, v23\n"
"v_mfma_f32_16x16x32_bf16 v[8:11], v[202:205], v[144:147], v[8:11]\n"
"ds_read_b128 v[140:143], v173 offset:2720\n"
"v_mfma_f32_16x16x32_bf16 v[8:11], v[204:207], v[24:27], v[8:11]\n"
"s_waitcnt lgkmcnt(2)\n"
"v_max3_f32 v170, v170, v148, v149\n"
"v_max3_f32 v170, v170, v150, v151\n"
"v_mfma_f32_16x16x32_bf16 v[152:155], v[214:217], v[144:147], v[152:155]\n"
"ds_read_b128 v[20:23], v173 offset:2736\n"
"v_mfma_f32_16x16x32_bf16 v[152:155], v[216:219], v[24:27], v[152:155]\n"
"s_nop 1\n"
"v_max3_f32 v170, v170, v8, v9\n"
"v_max3_f32 v170, v170, v10, v11\n"
"v_mov_b32 v171, v170\n"
"s_nop 1\n"
"v_permlane16_swap_b32 v170, v171\n"
"v_max_f32 v170, v170, v171\n"
"v_mov_b32 v171, v170\n"
"s_nop 1\n"
"v_permlane32_swap_b32 v170, v171\n"
"v_max_f32 v170, v170, v171\n"
"v_sub_f32 v152, v152, v170\n"
"v_exp_f32 v152, v152\n"
"v_sub_f32 v153, v153, v170\n"
"v_exp_f32 v153, v153\n"
"v_sub_f32 v154, v154, v170\n"
"v_exp_f32 v154, v154\n"
"v_sub_f32 v155, v155, v170\n"
"v_exp_f32 v155, v155\n"
"s_waitcnt lgkmcnt(2)\n"
"v_mfma_f32_16x16x32_bf16 v[12:15], v[220:223], v[144:147], v[12:15]\n"
"ds_read_b128 v[214:217], v175 offset:32\n"
"v_cvt_pk_bf16_f32 v152, v152, v153\n"
"v_mfma_f32_16x16x32_bf16 v[12:15], v[222:225], v[24:27], v[12:15]\n"
"v_cvt_pk_bf16_f32 v153, v154, v155\n"
"ds_write_b64 v6, v[152:153] offset:55296\n"
"s_waitcnt lgkmcnt(3)\n"
"v_mfma_f32_16x16x32_bf16 v[140:143], v[190:193], v[144:147], v[140:143]\n"
"ds_read_b128 v[218:221], v172 offset:1376\n"
"v_mfma_f32_16x16x32_bf16 v[140:143], v[192:195], v[24:27], v[140:143]\n"
"s_nop 1\n"
"v_sub_f32 v12, v12, v170\n"
"v_exp_f32 v12, v12\n"
"v_sub_f32 v13, v13, v170\n"
"v_exp_f32 v13, v13\n"
"v_sub_f32 v14, v14, v170\n"
"v_exp_f32 v14, v14\n"
"v_sub_f32 v15, v15, v170\n"
"v_exp_f32 v15, v15\n"
"v_sub_f32 v142, v142, v170\n"
"v_exp_f32 v142, v142\n"
"v_cvt_pk_bf16_f32 v12, v12, v13\n"
"v_cvt_pk_bf16_f32 v13, v14, v15\n"
"v_sub_f32 v143, v143, v170\n"
"v_exp_f32 v143, v143\n"
"v_mov_b32 v140, 0\n"
"v_cvt_pk_bf16_f32 v141, v142, v143\n"
"ds_write_b64 v6, v[12:13] offset:55304\n"
"s_waitcnt lgkmcnt(4)\n"
"v_mfma_f32_16x16x32_bf16 v[20:23], v[196:199], v[144:147], v[20:23]\n"
"ds_read_b128 v[222:225], v172 offset:1392\n"
"v_mfma_f32_16x16x32_bf16 v[20:23], v[198:201], v[24:27], v[20:23]\n"
"ds_write_b64 v6, v[140:141] offset:56320\n"
"s_waitcnt lgkmcnt(5)\n"
"v_mfma_f32_16x16x32_bf16 v[214:217], v[208:211], v[144:147], v[214:217]\n"
"ds_read_b128 v[8:11], v176 offset:1408\n"
"v_mfma_f32_16x16x32_bf16 v[214:217], v[210:213], v[24:27], v[214:217]\n"
"s_nop 2\n"
"v_sub_f32 v20, v20, v170\n"
"v_exp_f32 v20, v20\n"
"v_sub_f32 v21, v21, v170\n"
"v_exp_f32 v21, v21\n"
"v_sub_f32 v22, v22, v170\n"
"v_exp_f32 v22, v22\n"
"v_sub_f32 v23, v23, v170\n"
"v_exp_f32 v23, v23\n"
"v_cvt_pk_bf16_f32 v20, v20, v21\n"
"v_cvt_pk_bf16_f32 v21, v22, v23\n"
"v_sub_f32 v216, v216, v170\n"
"v_exp_f32 v216, v216\n"
"v_sub_f32 v217, v217, v170\n"
"v_exp_f32 v217, v217\n"
"v_mov_b32 v214, 0\n"
"v_cvt_pk_bf16_f32 v215, v216, v217\n"
"ds_write_b64 v6, v[20:21] offset:56328\n"
"s_waitcnt lgkmcnt(5)\n"
"v_mfma_f32_16x16x32_bf16 v[218:221], v[238:241], v[144:147], v[218:221]\n"
"ds_read_b128 v[208:211], v169 offset:2752\n"
"v_mfma_f32_16x16x32_bf16 v[218:221], v[240:243], v[24:27], v[218:221]\n"
"s_waitcnt lgkmcnt(1)\n"
"s_barrier\n"
"ds_write_b64 v6, v[214:215] offset:57352\n"
"v_mfma_f32_16x16x32_bf16 v[222:225], v[244:247], v[144:147], v[222:225]\n"
"ds_read_b128 v[12:15], v4 offset:43008\n"
"v_mfma_f32_16x16x32_bf16 v[222:225], v[246:249], v[24:27], v[222:225]\n"
"s_nop 1\n"
"v_sub_f32 v220, v220, v170\n"
"v_exp_f32 v220, v220\n"
"v_sub_f32 v221, v221, v170\n"
"v_exp_f32 v221, v221\n"
"v_mov_b32 v218, 0\n"
"v_cvt_pk_bf16_f32 v219, v220, v221\n"
"ds_write_b64 v6, v[218:219] offset:58368\n"
"v_mfma_f32_16x16x32_bf16 v[8:11], v[250:253], v[144:147], v[8:11]\n"
"ds_read_b128 v[212:215], v6 offset:4096\n"
"v_sub_f32 v222, v222, v170\n"
"v_exp_f32 v222, v222\n"
"v_sub_f32 v223, v223, v170\n"
"v_exp_f32 v223, v223\n"
"v_sub_f32 v224, v224, v170\n"
"v_exp_f32 v224, v224\n"
"v_sub_f32 v225, v225, v170\n"
"v_exp_f32 v225, v225\n"
"v_cvt_pk_bf16_f32 v222, v222, v223\n"
"v_mfma_f32_16x16x32_bf16 v[8:11], v[252:255], v[24:27], v[8:11]\n"
"ds_read_b128 v[216:219], v5 offset:43008\n"
"v_cvt_pk_bf16_f32 v223, v224, v225\n"
"ds_write_b64 v6, v[222:223] offset:58376\n"
"s_waitcnt lgkmcnt(6)\n"
"v_mfma_f32_16x16x32_bf16 v[208:211], v[202:205], v[144:147], v[208:211]\n"
"v_mfma_f32_16x16x32_bf16 v[208:211], v[204:207], v[24:27], v[208:211]\n"
"s_waitcnt lgkmcnt(4)\n"
"ds_read_b128 v[20:23], v6 offset:5120\n"
"v_mfma_f32_16x16x32_bf16 v[140:143], v[116:119], v[12:15], 0\n"
"ds_read_b128 v[148:151], v4 offset:45056\n"
"v_sub_f32 v8, v8, v170\n"
"v_exp_f32 v8, v8\n"
"v_sub_f32 v9, v9, v170\n"
"v_exp_f32 v9, v9\n"
"v_mfma_f32_16x16x32_bf16 v[220:223], v[120:123], v[12:15], 0\n"
"s_waitcnt lgkmcnt(4)\n"
"v_sub_f32 v10, v10, v170\n"
"v_exp_f32 v10, v10\n"
"v_mfma_f32_16x16x32_bf16 v[144:147], v[212:215], v[12:15], 0\n"
"ds_read_b128 v[152:155], v6 offset:6144\n"
"v_cvt_pk_bf16_f32 v8, v8, v9\n"
"v_sub_f32 v11, v11, v170\n"
"v_exp_f32 v11, v11\n"
"s_waitcnt lgkmcnt(4)\n"
"v_mfma_f32_16x16x32_bf16 v[140:143], v[124:127], v[216:219], v[140:143]\n"
"ds_read_b128 v[156:159], v5 offset:45056\n"
"v_cvt_pk_bf16_f32 v9, v10, v11\n"
"v_mfma_f32_16x16x32_bf16 v[220:223], v[128:131], v[216:219], v[220:223]\n"
"ds_write_b64 v6, v[8:9] offset:33288\n"
"s_waitcnt lgkmcnt(4)\n"
"v_mfma_f32_16x16x32_bf16 v[144:147], v[20:23], v[216:219], v[144:147]\n"
"s_waitcnt lgkmcnt(3)\n"
"ds_read_b128 v[212:215], v6 offset:7168\n"
"v_mfma_f32_16x16x32_bf16 v[140:143], v[0:3], v[148:151], v[140:143]\n"
"ds_read_b128 v[12:15], v4 offset:47104\n"
"v_sub_f32 v208, v208, v170\n"
"v_exp_f32 v208, v208\n"
"v_sub_f32 v209, v209, v170\n"
"v_exp_f32 v209, v209\n"
"v_mfma_f32_16x16x32_bf16 v[220:223], v[16:19], v[148:151], v[220:223]\n"
"s_waitcnt lgkmcnt(4)\n"
"v_sub_f32 v210, v210, v170\n"
"v_exp_f32 v210, v210\n"
"v_mfma_f32_16x16x32_bf16 v[144:147], v[152:155], v[148:151], v[144:147]\n"
"ds_read_b128 v[216:219], v6 offset:8192\n"
"v_cvt_pk_bf16_f32 v208, v208, v209\n"
"v_sub_f32 v211, v211, v170\n"
"v_exp_f32 v211, v211\n"
"s_waitcnt lgkmcnt(4)\n"
"v_mfma_f32_16x16x32_bf16 v[140:143], v[132:135], v[156:159], v[140:143]\n"
"ds_read_b128 v[8:11], v5 offset:47104\n"
"v_cvt_pk_bf16_f32 v209, v210, v211\n"
"v_mfma_f32_16x16x32_bf16 v[220:223], v[136:139], v[156:159], v[220:223]\n"
"ds_write_b64 v6, v[208:209] offset:57344\n"
"s_waitcnt lgkmcnt(4)\n"
"v_mfma_f32_16x16x32_bf16 v[144:147], v[212:215], v[156:159], v[144:147]\n"
"s_waitcnt lgkmcnt(3)\n"
"ds_read_b128 v[20:23], v6 offset:9216\n"
"v_mfma_f32_16x16x32_bf16 v[140:143], v[100:103], v[12:15], v[140:143]\n"
"ds_read_b128 v[148:151], v4 offset:0\n"
"v_mfma_f32_16x16x32_bf16 v[220:223], v[104:107], v[12:15], v[220:223]\n"
"s_waitcnt lgkmcnt(2)\n"
"s_barrier\n"
"v_mfma_f32_16x16x32_bf16 v[144:147], v[216:219], v[12:15], v[144:147]\n"
"v_mfma_f32_16x16x32_bf16 v[140:143], v[108:111], v[8:11], v[140:143]\n"
"ds_read_b128 v[212:215], v5 offset:0\n"
"v_mfma_f32_16x16x32_bf16 v[220:223], v[112:115], v[8:11], v[220:223]\n"
"s_waitcnt lgkmcnt(2)\n"
"v_mfma_f32_16x16x32_bf16 v[144:147], v[20:23], v[8:11], v[144:147]\n"
"s_waitcnt lgkmcnt(1)\n"
"v_mfma_f32_16x16x32_bf16 v[208:211], v[28:31], v[148:151], 0\n"
"ds_read_b128 v[152:155], v4 offset:2048\n"
"v_mfma_f32_16x16x32_bf16 v[12:15], v[32:35], v[148:151], 0\n"
"v_mfma_f32_16x16x32_bf16 v[156:159], v[36:39], v[148:151], 0\n"
"s_waitcnt lgkmcnt(1)\n"
"v_mfma_f32_16x16x32_bf16 v[208:211], v[40:43], v[212:215], v[208:211]\n"
"ds_read_b128 v[216:219], v5 offset:2048\n"
"v_mul_f32 v140, s60, v140\n"
"v_mfma_f32_16x16x32_bf16 v[12:15], v[44:47], v[212:215], v[12:15]\n"
"v_mul_f32 v141, s60, v141\n"
"v_cvt_pk_bf16_f32 v140, v140, v141\n"
"v_mfma_f32_16x16x32_bf16 v[156:159], v[48:51], v[212:215], v[156:159]\n"
"s_waitcnt lgkmcnt(1)\n"
"v_mul_f32 v142, s60, v142\n"
"v_mul_f32 v143, s60, v143\n"
"v_cvt_pk_bf16_f32 v141, v142, v143\n"
"v_mfma_f32_16x16x32_bf16 v[208:211], v[52:55], v[152:155], v[208:211]\n"
"ds_read_b128 v[8:11], v4 offset:4096\n"
"v_mul_f32 v220, s60, v220\n"
"v_mfma_f32_16x16x32_bf16 v[12:15], v[56:59], v[152:155], v[12:15]\n"
"v_mul_f32 v221, s60, v221\n"
"v_mul_f32 v222, s60, v222\n"
"v_mfma_f32_16x16x32_bf16 v[156:159], v[60:63], v[152:155], v[156:159]\n"
"v_mul_f32 v223, s60, v223\n"
"v_cvt_pk_bf16_f32 v142, v220, v221\n"
"v_cvt_pk_bf16_f32 v143, v222, v223\n"
"v_mul_f32 v144, s60, v144\n"
"v_mul_f32 v145, s60, v145\n"
"v_mul_f32 v146, s60, v146\n"
"v_mul_f32 v147, s60, v147\n"
"s_waitcnt lgkmcnt(1)\n"
"v_mfma_f32_16x16x32_bf16 v[208:211], v[64:67], v[216:219], v[208:211]\n"
"ds_read_b128 v[20:23], v5 offset:4096\n"
"v_cvt_pk_bf16_f32 v26, v144, v145\n"
"v_mfma_f32_16x16x32_bf16 v[12:15], v[68:71], v[216:219], v[12:15]\n"
"v_cvt_pk_bf16_f32 v27, v146, v147\n"
"v_mfma_f32_16x16x32_bf16 v[156:159], v[72:75], v[216:219], v[156:159]\n"
"s_waitcnt lgkmcnt(1)\n"
"v_mfma_f32_16x16x32_bf16 v[208:211], v[76:79], v[8:11], v[208:211]\n"
"ds_read_b128 v[220:223], v4 offset:6144\n"
"v_mfma_f32_16x16x32_bf16 v[12:15], v[80:83], v[8:11], v[12:15]\n"
"v_mfma_f32_16x16x32_bf16 v[156:159], v[84:87], v[8:11], v[156:159]\n"
"s_waitcnt lgkmcnt(1)\n"
"v_mfma_f32_16x16x32_bf16 v[208:211], v[88:91], v[20:23], v[208:211]\n"
"ds_read_b128 v[144:147], v5 offset:6144\n"
"v_mfma_f32_16x16x32_bf16 v[12:15], v[92:95], v[20:23], v[12:15]\n"
"v_mfma_f32_16x16x32_bf16 v[156:159], v[96:99], v[20:23], v[156:159]\n"
"s_waitcnt lgkmcnt(1)\n"
"v_mfma_f32_16x16x32_bf16 v[214:217], v[28:31], v[220:223], 0\n"
"ds_read_b128 v[148:151], v4 offset:8192\n"
"v_mfma_f32_16x16x32_bf16 v[8:11], v[32:35], v[220:223], 0\n"
"v_mfma_f32_16x16x32_bf16 v[152:155], v[36:39], v[220:223], 0\n"
"s_waitcnt lgkmcnt(1)\n"
"v_mfma_f32_16x16x32_bf16 v[214:217], v[40:43], v[144:147], v[214:217]\n"
"ds_read_b128 v[160:163], v5 offset:8192\n"
"v_cvt_pk_bf16_f32 v208, v208, v209\n"
"v_cvt_pk_bf16_f32 v209, v210, v211\n"
"v_cvt_pk_bf16_f32 v210, v12, v13\n"
"v_cvt_pk_bf16_f32 v211, v14, v15\n"
"v_cvt_pk_bf16_f32 v212, v156, v157\n"
"v_cvt_pk_bf16_f32 v213, v158, v159\n"
"v_mfma_f32_16x16x32_bf16 v[8:11], v[44:47], v[144:147], v[8:11]\n"
"v_mfma_f32_16x16x32_bf16 v[152:155], v[48:51], v[144:147], v[152:155]\n"
"s_waitcnt lgkmcnt(1)\n"
"v_bfe_u32 v225, v6, 2, 6\n"
"v_mfma_f32_16x16x32_bf16 v[214:217], v[52:55], v[148:151], v[214:217]\n"
"ds_read_b128 v[218:221], v4 offset:10240\n"
"v_mfma_f32_16x16x32_bf16 v[8:11], v[56:59], v[148:151], v[8:11]\n"
"s_add_u32 s68, s50, 4294966992\n"
"ds_read_b32 v225, v225 offset:1472\n"
"v_mfma_f32_16x16x32_bf16 v[152:155], v[60:63], v[148:151], v[152:155]\n"
"s_waitcnt lgkmcnt(2)\n"
"v_mfma_f32_16x16x32_bf16 v[214:217], v[64:67], v[160:163], v[214:217]\n"
"ds_read_b128 v[12:15], v5 offset:10240\n"
"v_mfma_f32_16x16x32_bf16 v[8:11], v[68:71], v[160:163], v[8:11]\n"
"v_mfma_f32_16x16x32_bf16 v[152:155], v[72:75], v[160:163], v[152:155]\n"
"s_add_u32 s73, s50, 4294965952\n"
"s_waitcnt lgkmcnt(2)\n"
"v_mfma_f32_16x16x32_bf16 v[214:217], v[76:79], v[218:221], v[214:217]\n"
"s_waitcnt lgkmcnt(1)\n"
"v_add_u32_sdwa v224, v225, v7 dst_sel:DWORD dst_unused:UNUSED_PAD src0_sel:WORD_0 src1_sel:DWORD\n"
"v_mfma_f32_16x16x32_bf16 v[8:11], v[80:83], v[218:221], v[8:11]\n"
"v_min_u32 v177, s68, v224\n"
"v_mfma_f32_16x16x32_bf16 v[152:155], v[84:87], v[218:221], v[152:155]\n"
"s_waitcnt lgkmcnt(0)\n"
"ds_read_b128 v[20:23], v177 offset:2688\n"
"v_max_u32 v176, s73, v224\n"
"v_mfma_f32_16x16x32_bf16 v[214:217], v[88:91], v[12:15], v[214:217]\n"
"ds_read_b128 v[144:147], v177 offset:2704\n"
"v_mfma_f32_16x16x32_bf16 v[8:11], v[92:95], v[12:15], v[8:11]\n"
"ds_read_b128 v[148:151], v176 offset:1344\n"
"v_mfma_f32_16x16x32_bf16 v[152:155], v[96:99], v[12:15], v[152:155]\n"
"s_waitcnt lgkmcnt(2)\n"
"v_mfma_f32_16x16x32_bf16 v[20:23], v[178:181], v[140:143], v[20:23]\n"
"ds_read_b128 v[220:223], v176 offset:1360\n"
"v_mfma_f32_16x16x32_bf16 v[20:23], v[180:183], v[24:27], v[20:23]\n"
"s_waitcnt lgkmcnt(2)\n"
"v_mfma_f32_16x16x32_bf16 v[144:147], v[184:187], v[140:143], v[144:147]\n"
"ds_read_b128 v[156:159], v176 offset:1376\n"
"v_cvt_pk_bf16_f32 v214, v214, v215\n"
"v_cvt_pk_bf16_f32 v215, v216, v217\n"
"v_cvt_pk_bf16_f32 v216, v8, v9\n"
"v_cvt_pk_bf16_f32 v217, v10, v11\n"
"v_cvt_pk_bf16_f32 v218, v152, v153\n"
"v_cvt_pk_bf16_f32 v219, v154, v155\n"
"v_mfma_f32_16x16x32_bf16 v[144:147], v[186:189], v[24:27], v[144:147]\n"
"s_waitcnt lgkmcnt(2)\n"
"v_max3_f32 v224, v20, v21, v22\n"
"v_max_f32 v224, v224, v23\n"
"v_mfma_f32_16x16x32_bf16 v[148:151], v[226:229], v[140:143], v[148:151]\n"
"ds_read_b128 v[8:11], v177 offset:2720\n"
"v_mfma_f32_16x16x32_bf16 v[148:151], v[228:231], v[24:27], v[148:151]\n"
"s_waitcnt lgkmcnt(2)\n"
"s_nop 0\n"
"v_max3_f32 v224, v224, v144, v145\n"
"v_max3_f32 v224, v224, v146, v147\n"
"v_mfma_f32_16x16x32_bf16 v[220:223], v[232:235], v[140:143], v[220:223]\n"
"ds_read_b128 v[12:15], v177 offset:2688\n"
"v_mfma_f32_16x16x32_bf16 v[220:223], v[234:237], v[24:27], v[220:223]\n"
"s_waitcnt lgkmcnt(2)\n"
"v_max3_f32 v224, v224, v148, v149\n"
"v_max3_f32 v224, v224, v150, v151\n"
"v_mfma_f32_16x16x32_bf16 v[156:159], v[238:241], v[140:143], v[156:159]\n"
"ds_read_b128 v[20:23], v177 offset:2704\n"
"v_mfma_f32_16x16x32_bf16 v[156:159], v[240:243], v[24:27], v[156:159]\n"
"s_waitcnt lgkmcnt(2)\n"
"s_nop 0\n"
"v_max3_f32 v224, v224, v220, v221\n"
"v_max3_f32 v224, v224, v222, v223\n"
"v_mfma_f32_16x16x32_bf16 v[8:11], v[190:193], v[140:143], v[8:11]\n"
"ds_read_b128 v[144:147], v176 offset:1344\n"
"v_mfma_f32_16x16x32_bf16 v[8:11], v[192:195], v[24:27], v[8:11]\n"
"s_waitcnt lgkmcnt(2)\n"
"v_max3_f32 v224, v224, v156, v157\n"
"v_max3_f32 v224, v224, v158, v159\n"
"v_mfma_f32_16x16x32_bf16 v[12:15], v[178:181], v[140:143], v[12:15]\n"
"ds_read_b128 v[220:223], v176 offset:1360\n"
"v_mfma_f32_16x16x32_bf16 v[12:15], v[180:183], v[24:27], v[12:15]\n"
"s_nop 1\n"
"v_max3_f32 v224, v224, v8, v9\n"
"v_max3_f32 v224, v224, v10, v11\n"
"v_mov_b32 v225, v224\n"
"s_nop 1\n"
"v_permlane16_swap_b32 v224, v225\n"
"v_max_f32 v224, v224, v225\n"
"v_mov_b32 v225, v224\n"
"s_nop 1\n"
"v_permlane32_swap_b32 v224, v225\n"
"v_max_f32 v224, v224, v225\n"
"v_sub_f32 v12, v12, v224\n"
"v_exp_f32 v12, v12\n"
"v_sub_f32 v13, v13, v224\n"
"v_exp_f32 v13, v13\n"
"v_sub_f32 v14, v14, v224\n"
"v_exp_f32 v14, v14\n"
"v_sub_f32 v15, v15, v224\n"
"v_exp_f32 v15, v15\n"
"s_waitcnt lgkmcnt(2)\n"
"v_mfma_f32_16x16x32_bf16 v[20:23], v[184:187], v[140:143], v[20:23]\n"
"ds_read_b128 v[8:11], v176 offset:1376\n"
"v_cvt_pk_bf16_f32 v12, v12, v13\n"
"v_mfma_f32_16x16x32_bf16 v[20:23], v[186:189], v[24:27], v[20:23]\n"
"v_cvt_pk_bf16_f32 v13, v14, v15\n"
"ds_write_b64 v6, v[12:13] offset:53248\n"
"s_waitcnt lgkmcnt(3)\n"
"v_mfma_f32_16x16x32_bf16 v[144:147], v[226:229], v[140:143], v[144:147]\n"
"ds_read_b128 v[148:151], v177 offset:2720\n"
"v_mfma_f32_16x16x32_bf16 v[144:147], v[228:231], v[24:27], v[144:147]\n"
"s_nop 1\n"
"v_sub_f32 v20, v20, v224\n"
"v_exp_f32 v20, v20\n"
"v_sub_f32 v21, v21, v224\n"
"v_exp_f32 v21, v21\n"
"v_sub_f32 v22, v22, v224\n"
"v_exp_f32 v22, v22\n"
"v_sub_f32 v23, v23, v224\n"
"v_exp_f32 v23, v23\n"
"v_sub_f32 v144, v144, v224\n"
"v_exp_f32 v144, v144\n"
"v_sub_f32 v145, v145, v224\n"
"v_exp_f32 v145, v145\n"
"v_cvt_pk_bf16_f32 v20, v20, v21\n"
"v_sub_f32 v146, v146, v224\n"
"v_exp_f32 v146, v146\n"
"v_cvt_pk_bf16_f32 v21, v22, v23\n"
"v_sub_f32 v147, v147, v224\n"
"v_exp_f32 v147, v147\n"
"v_cvt_pk_bf16_f32 v144, v144, v145\n"
"v_cvt_pk_bf16_f32 v145, v146, v147\n"
"ds_write_b64 v6, v[20:21] offset:53256\n"
"s_waitcnt lgkmcnt(4)\n"
"v_mfma_f32_16x16x32_bf16 v[220:223], v[232:235], v[140:143], v[220:223]\n"
"ds_write_b64 v6, v[144:145] offset:54272\n"
"v_mfma_f32_16x16x32_bf16 v[220:223], v[234:237], v[24:27], v[220:223]\n"
"s_waitcnt lgkmcnt(4)\n"
"ds_read_b128 v[12:15], v4 offset:18432\n"
"v_mfma_f32_16x16x32_bf16 v[8:11], v[238:241], v[140:143], v[8:11]\n"
"ds_read_b128 v[152:155], v6 offset:4096\n"
"v_mfma_f32_16x16x32_bf16 v[8:11], v[240:243], v[24:27], v[8:11]\n"
"s_waitcnt lgkmcnt(2)\n"
"ds_read_b128 v[20:23], v5 offset:18432\n"
"s_barrier\n"
"v_sub_f32 v220, v220, v224\n"
"v_exp_f32 v220, v220\n"
"v_sub_f32 v221, v221, v224\n"
"v_exp_f32 v221, v221\n"
"v_sub_f32 v222, v222, v224\n"
"v_exp_f32 v222, v222\n"
"v_sub_f32 v223, v223, v224\n"
"v_exp_f32 v223, v223\n"
"v_cvt_pk_bf16_f32 v220, v220, v221\n"
"v_cvt_pk_bf16_f32 v221, v222, v223\n"
"v_sub_f32 v8, v8, v224\n"
"v_exp_f32 v8, v8\n"
"v_sub_f32 v9, v9, v224\n"
"v_exp_f32 v9, v9\n"
"v_sub_f32 v10, v10, v224\n"
"v_exp_f32 v10, v10\n"
"v_sub_f32 v11, v11, v224\n"
"v_exp_f32 v11, v11\n"
"v_cvt_pk_bf16_f32 v8, v8, v9\n"
"v_cvt_pk_bf16_f32 v9, v10, v11\n"
"ds_write_b64 v6, v[220:221] offset:54280\n"
"v_mfma_f32_16x16x32_bf16 v[148:151], v[190:193], v[140:143], v[148:151]\n"
"ds_write_b64 v6, v[8:9] offset:57352\n"
"v_mfma_f32_16x16x32_bf16 v[148:151], v[192:195], v[24:27], v[148:151]\n"
"s_waitcnt lgkmcnt(4)\n"
"ds_read_b128 v[144:147], v6 offset:5120\n"
"v_mfma_f32_16x16x32_bf16 v[156:159], v[116:119], v[12:15], 0\n"
"ds_read_b128 v[160:163], v4 offset:20480\n"
"v_mfma_f32_16x16x32_bf16 v[164:167], v[120:123], v[12:15], 0\n"
"s_waitcnt lgkmcnt(5)\n"
"v_mfma_f32_16x16x32_bf16 v[140:143], v[152:155], v[12:15], 0\n"
"s_waitcnt lgkmcnt(4)\n"
"ds_read_b128 v[220:223], v6 offset:6144\n"
"v_mfma_f32_16x16x32_bf16 v[156:159], v[124:127], v[20:23], v[156:159]\n"
"ds_read_b128 v[8:11], v5 offset:20480\n"
"v_mfma_f32_16x16x32_bf16 v[164:167], v[128:131], v[20:23], v[164:167]\n"
"v_sub_f32 v148, v148, v224\n"
"v_exp_f32 v148, v148\n"
"v_sub_f32 v149, v149, v224\n"
"v_exp_f32 v149, v149\n"
"s_waitcnt lgkmcnt(3)\n"
"v_mfma_f32_16x16x32_bf16 v[140:143], v[144:147], v[20:23], v[140:143]\n"
"s_waitcnt lgkmcnt(2)\n"
"ds_read_b128 v[12:15], v6 offset:7168\n"
"v_cvt_pk_bf16_f32 v148, v148, v149\n"
"v_mfma_f32_16x16x32_bf16 v[156:159], v[0:3], v[160:163], v[156:159]\n"
"ds_read_b128 v[152:155], v4 offset:22528\n"
"v_mfma_f32_16x16x32_bf16 v[164:167], v[16:19], v[160:163], v[164:167]\n"
"s_waitcnt lgkmcnt(3)\n"
"v_sub_f32 v150, v150, v224\n"
"v_exp_f32 v150, v150\n"
"v_mfma_f32_16x16x32_bf16 v[140:143], v[220:223], v[160:163], v[140:143]\n"
"s_waitcnt lgkmcnt(2)\n"
"ds_read_b128 v[20:23], v6 offset:8192\n"
"v_sub_f32 v151, v151, v224\n"
"v_exp_f32 v151, v151\n"
"v_mfma_f32_16x16x32_bf16 v[156:159], v[132:135], v[8:11], v[156:159]\n"
"ds_read_b128 v[144:147], v5 offset:22528\n"
"v_cvt_pk_bf16_f32 v149, v150, v151\n"
"v_mfma_f32_16x16x32_bf16 v[164:167], v[136:139], v[8:11], v[164:167]\n"
"ds_write_b64 v6, v[148:149] offset:57344\n"
"s_waitcnt lgkmcnt(4)\n"
"v_mfma_f32_16x16x32_bf16 v[140:143], v[12:15], v[8:11], v[140:143]\n"
"s_waitcnt lgkmcnt(3)\n"
"ds_read_b128 v[160:163], v6 offset:9216\n"
"v_mfma_f32_16x16x32_bf16 v[156:159], v[100:103], v[152:155], v[156:159]\n"
"ds_read_b128 v[168:171], v4 offset:12288\n"
"v_mfma_f32_16x16x32_bf16 v[164:167], v[104:107], v[152:155], v[164:167]\n"
"s_waitcnt lgkmcnt(2)\n"
"s_barrier\n"
"v_mfma_f32_16x16x32_bf16 v[140:143], v[20:23], v[152:155], v[140:143]\n"
"v_mfma_f32_16x16x32_bf16 v[156:159], v[108:111], v[144:147], v[156:159]\n"
"ds_read_b128 v[8:11], v5 offset:12288\n"
"v_mfma_f32_16x16x32_bf16 v[164:167], v[112:115], v[144:147], v[164:167]\n"
"s_waitcnt lgkmcnt(2)\n"
"v_mfma_f32_16x16x32_bf16 v[140:143], v[160:163], v[144:147], v[140:143]\n"
"s_waitcnt lgkmcnt(1)\n"
"v_mfma_f32_16x16x32_bf16 v[220:223], v[28:31], v[168:171], 0\n"
"ds_read_b128 v[12:15], v4 offset:14336\n"
"v_mfma_f32_16x16x32_bf16 v[20:23], v[32:35], v[168:171], 0\n"
"v_mfma_f32_16x16x32_bf16 v[148:151], v[36:39], v[168:171], 0\n"
"s_waitcnt lgkmcnt(1)\n"
"v_mfma_f32_16x16x32_bf16 v[220:223], v[40:43], v[8:11], v[220:223]\n"
"ds_read_b128 v[152:155], v5 offset:14336\n"
"v_mul_f32 v156, s60, v156\n"
"v_mfma_f32_16x16x32_bf16 v[20:23], v[44:47], v[8:11], v[20:23]\n"
"v_mul_f32 v157, s60, v157\n"
"v_cvt_pk_bf16_f32 v156, v156, v157\n"
"v_mfma_f32_16x16x32_bf16 v[148:151], v[48:51], v[8:11], v[148:151]\n"
"v_bfe_u32 v225, v6, 2, 6\n"
"v_mul_f32 v158, s60, v158\n"
"v_mul_f32 v159, s60, v159\n"
"v_cvt_pk_bf16_f32 v157, v158, v159\n"
"v_mul_f32 v164, s60, v164\n"
"s_waitcnt lgkmcnt(1)\n"
"v_mfma_f32_16x16x32_bf16 v[220:223], v[52:55], v[12:15], v[220:223]\n"
"ds_read_b128 v[8:11], v4 offset:16384\n"
"v_mul_f32 v165, s60, v165\n"
"v_mfma_f32_16x16x32_bf16 v[20:23], v[56:59], v[12:15], v[20:23]\n"
"s_add_u32 s70, s50, 4294964608\n"
"ds_read_b32 v225, v225 offset:1536\n"
"v_mul_f32 v166, s60, v166\n"
"v_mfma_f32_16x16x32_bf16 v[148:151], v[60:63], v[12:15], v[148:151]\n"
"v_mul_f32 v167, s60, v167\n"
"v_cvt_pk_bf16_f32 v158, v164, v165\n"
"v_cvt_pk_bf16_f32 v159, v166, v167\n"
"v_mul_f32 v140, s60, v140\n"
"v_mul_f32 v141, s60, v141\n"
"s_waitcnt lgkmcnt(2)\n"
"v_mfma_f32_16x16x32_bf16 v[220:223], v[64:67], v[152:155], v[220:223]\n"
"ds_read_b128 v[12:15], v5 offset:16384\n"
"v_mul_f32 v142, s60, v142\n"
"v_mfma_f32_16x16x32_bf16 v[20:23], v[68:71], v[152:155], v[20:23]\n"
"v_mul_f32 v143, s60, v143\n"
"v_cvt_pk_bf16_f32 v26, v140, v141\n"
"v_mfma_f32_16x16x32_bf16 v[148:151], v[72:75], v[152:155], v[148:151]\n"
"s_add_u32 s71, s50, 4294965648\n"
"v_cvt_pk_bf16_f32 v27, v142, v143\n"
"s_waitcnt lgkmcnt(2)\n"
"v_mfma_f32_16x16x32_bf16 v[220:223], v[76:79], v[8:11], v[220:223]\n"
"s_waitcnt lgkmcnt(1)\n"
"v_add_u32_sdwa v177, v225, v7 dst_sel:DWORD dst_unused:UNUSED_PAD src0_sel:WORD_0 src1_sel:DWORD\n"
"v_mfma_f32_16x16x32_bf16 v[20:23], v[80:83], v[8:11], v[20:23]\n"
"v_max_u32 v176, s70, v177\n"
"v_mfma_f32_16x16x32_bf16 v[148:151], v[84:87], v[8:11], v[148:151]\n"
"s_waitcnt lgkmcnt(0)\n"
"ds_read_b128 v[144:147], v176 offset:2688\n"
"v_min_u32 v175, s71, v177\n"
"v_mfma_f32_16x16x32_bf16 v[220:223], v[88:91], v[12:15], v[220:223]\n"
"ds_read_b128 v[140:143], v176 offset:2704\n"
"v_mfma_f32_16x16x32_bf16 v[20:23], v[92:95], v[12:15], v[20:23]\n"
"ds_read_b128 v[152:155], v175 offset:4032\n"
"v_mfma_f32_16x16x32_bf16 v[148:151], v[96:99], v[12:15], v[148:151]\n"
"s_add_u32 s69, s50, 4294965952\n"
"s_waitcnt lgkmcnt(2)\n"
"v_mfma_f32_16x16x32_bf16 v[144:147], v[178:181], v[156:159], v[144:147]\n"
"ds_read_b128 v[8:11], v175 offset:4048\n"
"v_mfma_f32_16x16x32_bf16 v[144:147], v[180:183], v[24:27], v[144:147]\n"
"v_cvt_pk_bf16_f32 v220, v220, v221\n"
"v_cvt_pk_bf16_f32 v221, v222, v223\n"
"v_cvt_pk_bf16_f32 v222, v20, v21\n"
"v_cvt_pk_bf16_f32 v223, v22, v23\n"
"v_cvt_pk_bf16_f32 v224, v148, v149\n"
"v_cvt_pk_bf16_f32 v225, v150, v151\n"
"s_waitcnt lgkmcnt(2)\n"
"v_mfma_f32_16x16x32_bf16 v[140:143], v[184:187], v[156:159], v[140:143]\n"
"ds_read_b128 v[12:15], v176 offset:2720\n"
"v_max_u32 v174, s69, v177\n"
"v_mfma_f32_16x16x32_bf16 v[140:143], v[186:189], v[24:27], v[140:143]\n"
"s_waitcnt lgkmcnt(2)\n"
"v_max3_f32 v172, v144, v145, v146\n"
"v_max_f32 v172, v172, v147\n"
"v_mfma_f32_16x16x32_bf16 v[152:155], v[208:211], v[156:159], v[152:155]\n"
"ds_read_b128 v[20:23], v174 offset:1344\n"
"v_mfma_f32_16x16x32_bf16 v[152:155], v[210:213], v[24:27], v[152:155]\n"
"s_waitcnt lgkmcnt(2)\n"
"s_nop 0\n"
"v_max3_f32 v172, v172, v140, v141\n"
"v_max3_f32 v172, v172, v142, v143\n"
"v_mfma_f32_16x16x32_bf16 v[8:11], v[214:217], v[156:159], v[8:11]\n"
"ds_read_b128 v[144:147], v174 offset:1360\n"
"v_mfma_f32_16x16x32_bf16 v[8:11], v[216:219], v[24:27], v[8:11]\n"
"s_waitcnt lgkmcnt(2)\n"
"v_max3_f32 v172, v172, v152, v153\n"
"v_max3_f32 v172, v172, v154, v155\n"
"v_mfma_f32_16x16x32_bf16 v[12:15], v[190:193], v[156:159], v[12:15]\n"
"ds_read_b128 v[140:143], v174 offset:1376\n"
"v_mfma_f32_16x16x32_bf16 v[12:15], v[192:195], v[24:27], v[12:15]\n"
"s_waitcnt lgkmcnt(2)\n"
"s_nop 0\n"
"v_max3_f32 v172, v172, v8, v9\n"
"v_max3_f32 v172, v172, v10, v11\n"
"v_mfma_f32_16x16x32_bf16 v[20:23], v[226:229], v[156:159], v[20:23]\n"
"ds_read_b128 v[148:151], v175 offset:4064\n"
"v_mfma_f32_16x16x32_bf16 v[20:23], v[228:231], v[24:27], v[20:23]\n"
"s_waitcnt lgkmcnt(2)\n"
"v_max3_f32 v172, v172, v12, v13\n"
"v_max3_f32 v172, v172, v14, v15\n"
"v_mfma_f32_16x16x32_bf16 v[144:147], v[232:235], v[156:159], v[144:147]\n"
"ds_read_b128 v[8:11], v176 offset:2688\n"
"v_mfma_f32_16x16x32_bf16 v[144:147], v[234:237], v[24:27], v[144:147]\n"
"s_waitcnt lgkmcnt(2)\n"
"s_nop 0\n"
"v_max3_f32 v172, v172, v20, v21\n"
"v_max3_f32 v172, v172, v22, v23\n"
"v_mfma_f32_16x16x32_bf16 v[140:143], v[238:241], v[156:159], v[140:143]\n"
"ds_read_b128 v[12:15], v176 offset:2704\n"
"v_mfma_f32_16x16x32_bf16 v[140:143], v[240:243], v[24:27], v[140:143]\n"
"s_waitcnt lgkmcnt(2)\n"
"v_max3_f32 v172, v172, v144, v145\n"
"v_max3_f32 v172, v172, v146, v147\n"
"v_mfma_f32_16x16x32_bf16 v[148:151], v[220:223], v[156:159], v[148:151]\n"
"ds_read_b128 v[20:23], v175 offset:4032\n"
"v_mfma_f32_16x16x32_bf16 v[148:151], v[222:225], v[24:27], v[148:151]\n"
"s_waitcnt lgkmcnt(2)\n"
"s_nop 0\n"
"v_max3_f32 v172, v172, v140, v141\n"
"v_max3_f32 v172, v172, v142, v143\n"
"v_mfma_f32_16x16x32_bf16 v[8:11], v[178:181], v[156:159], v[8:11]\n"
"ds_read_b128 v[144:147], v175 offset:4048\n"
"v_mfma_f32_16x16x32_bf16 v[8:11], v[180:183], v[24:27], v[8:11]\n"
"s_nop 0\n"
"v_max3_f32 v172, v172, v148, v149\n"
"v_max3_f32 v172, v172, v150, v151\n"
"v_mov_b32 v173, v172\n"
"s_nop 1\n"
"v_permlane16_swap_b32 v172, v173\n"
"v_max_f32 v172, v172, v173\n"
"v_mov_b32 v173, v172\n"
"s_nop 1\n"
"v_permlane32_swap_b32 v172, v173\n"
"v_max_f32 v172, v172, v173\n"
"v_sub_f32 v8, v8, v172\n"
"v_exp_f32 v8, v8\n"
"v_sub_f32 v9, v9, v172\n"
"v_exp_f32 v9, v9\n"
"v_sub_f32 v10, v10, v172\n"
"v_exp_f32 v10, v10\n"
"v_sub_f32 v11, v11, v172\n"
"v_exp_f32 v11, v11\n"
"s_waitcnt lgkmcnt(2)\n"
"v_mfma_f32_16x16x32_bf16 v[12:15], v[184:187], v[156:159], v[12:15]\n"
"ds_read_b128 v[140:143], v176 offset:2720\n"
"v_cvt_pk_bf16_f32 v8, v8, v9\n"
"v_mfma_f32_16x16x32_bf16 v[12:15], v[186:189], v[24:27], v[12:15]\n"
"v_cvt_pk_bf16_f32 v9, v10, v11\n"
"ds_write_b64 v6, v[8:9] offset:55296\n"
"s_waitcnt lgkmcnt(3)\n"
"v_mfma_f32_16x16x32_bf16 v[20:23], v[208:211], v[156:159], v[20:23]\n"
"ds_read_b128 v[148:151], v174 offset:1344\n"
"v_mfma_f32_16x16x32_bf16 v[20:23], v[210:213], v[24:27], v[20:23]\n"
"s_nop 1\n"
"v_sub_f32 v12, v12, v172\n"
"v_exp_f32 v12, v12\n"
"v_sub_f32 v13, v13, v172\n"
"v_exp_f32 v13, v13\n"
"v_sub_f32 v14, v14, v172\n"
"v_exp_f32 v14, v14\n"
"v_sub_f32 v15, v15, v172\n"
"v_exp_f32 v15, v15\n"
"v_sub_f32 v20, v20, v172\n"
"v_exp_f32 v20, v20\n"
"v_sub_f32 v21, v21, v172\n"
"v_exp_f32 v21, v21\n"
"v_cvt_pk_bf16_f32 v12, v12, v13\n"
"v_cvt_pk_bf16_f32 v13, v14, v15\n"
"v_sub_f32 v22, v22, v172\n"
"v_exp_f32 v22, v22\n"
"v_sub_f32 v23, v23, v172\n"
"v_exp_f32 v23, v23\n"
"v_cvt_pk_bf16_f32 v20, v20, v21\n"
"v_cvt_pk_bf16_f32 v21, v22, v23\n"
"ds_write_b64 v6, v[12:13] offset:55304\n"
"s_waitcnt lgkmcnt(4)\n"
"v_mfma_f32_16x16x32_bf16 v[144:147], v[214:217], v[156:159], v[144:147]\n"
"ds_read_b128 v[8:11], v174 offset:1360\n"
"v_mfma_f32_16x16x32_bf16 v[144:147], v[216:219], v[24:27], v[144:147]\n"
"ds_write_b64 v6, v[20:21] offset:56320\n"
"s_waitcnt lgkmcnt(5)\n"
"v_mfma_f32_16x16x32_bf16 v[140:143], v[190:193], v[156:159], v[140:143]\n"
"ds_read_b128 v[152:155], v174 offset:1376\n"
"v_mfma_f32_16x16x32_bf16 v[140:143], v[192:195], v[24:27], v[140:143]\n"
"s_nop 2\n"
"v_sub_f32 v144, v144, v172\n"
"v_exp_f32 v144, v144\n"
"v_sub_f32 v145, v145, v172\n"
"v_exp_f32 v145, v145\n"
"v_sub_f32 v146, v146, v172\n"
"v_exp_f32 v146, v146\n"
"v_sub_f32 v147, v147, v172\n"
"v_exp_f32 v147, v147\n"
"v_sub_f32 v140, v140, v172\n"
"v_exp_f32 v140, v140\n"
"v_cvt_pk_bf16_f32 v144, v144, v145\n"
"v_cvt_pk_bf16_f32 v145, v146, v147\n"
"v_sub_f32 v141, v141, v172\n"
"v_exp_f32 v141, v141\n"
"ds_write_b64 v6, v[144:145] offset:56328\n"
"s_waitcnt lgkmcnt(5)\n"
"v_mfma_f32_16x16x32_bf16 v[148:151], v[226:229], v[156:159], v[148:151]\n"
"ds_read_b128 v[12:15], v175 offset:4064\n"
"v_cvt_pk_bf16_f32 v140, v140, v141\n"
"v_mov_b32 v141, 0\n"
"v_mfma_f32_16x16x32_bf16 v[148:151], v[228:231], v[24:27], v[148:151]\n"
"ds_write_b64 v6, v[140:141] offset:33288\n"
"s_waitcnt lgkmcnt(5)\n"
"v_mfma_f32_16x16x32_bf16 v[8:11], v[232:235], v[156:159], v[8:11]\n"
"ds_read_b128 v[20:23], v4 offset:30720\n"
"v_mfma_f32_16x16x32_bf16 v[8:11], v[234:237], v[24:27], v[8:11]\n"
"s_waitcnt lgkmcnt(1)\n"
"s_barrier\n"
"s_nop 0\n"
"v_sub_f32 v148, v148, v172\n"
"v_exp_f32 v148, v148\n"
"v_sub_f32 v149, v149, v172\n"
"v_exp_f32 v149, v149\n"
"v_sub_f32 v150, v150, v172\n"
"v_exp_f32 v150, v150\n"
"v_sub_f32 v151, v151, v172\n"
"v_exp_f32 v151, v151\n"
"v_sub_f32 v8, v8, v172\n"
"v_exp_f32 v8, v8\n"
"v_cvt_pk_bf16_f32 v148, v148, v149\n"
"v_cvt_pk_bf16_f32 v149, v150, v151\n"
"v_sub_f32 v9, v9, v172\n"
"v_exp_f32 v9, v9\n"
"v_sub_f32 v10, v10, v172\n"
"v_exp_f32 v10, v10\n"
"v_sub_f32 v11, v11, v172\n"
"v_exp_f32 v11, v11\n"
"v_cvt_pk_bf16_f32 v8, v8, v9\n"
"v_cvt_pk_bf16_f32 v9, v10, v11\n"
"ds_write_b64 v6, v[148:149] offset:57352\n"
"v_mfma_f32_16x16x32_bf16 v[152:155], v[238:241], v[156:159], v[152:155]\n"
"ds_read_b128 v[226:229], v6 offset:4096\n"
"v_mfma_f32_16x16x32_bf16 v[152:155], v[240:243], v[24:27], v[152:155]\n"
"ds_write_b64 v6, v[8:9] offset:58368\n"
"v_mfma_f32_16x16x32_bf16 v[12:15], v[220:223], v[156:159], v[12:15]\n"
"ds_read_b128 v[140:143], v5 offset:30720\n"
"v_mfma_f32_16x16x32_bf16 v[12:15], v[222:225], v[24:27], v[12:15]\n"
"s_waitcnt lgkmcnt(4)\n"
"ds_read_b128 v[144:147], v6 offset:5120\n"
"v_mfma_f32_16x16x32_bf16 v[148:151], v[116:119], v[20:23], 0\n"
"ds_read_b128 v[160:163], v4 offset:32768\n"
"v_mfma_f32_16x16x32_bf16 v[8:11], v[120:123], v[20:23], 0\n"
"v_sub_f32 v152, v152, v172\n"
"v_exp_f32 v152, v152\n"
"v_sub_f32 v153, v153, v172\n"
"v_exp_f32 v153, v153\n"
"s_waitcnt lgkmcnt(4)\n"
"v_mfma_f32_16x16x32_bf16 v[156:159], v[226:229], v[20:23], 0\n"
"ds_read_b128 v[164:167], v6 offset:6144\n"
"v_cvt_pk_bf16_f32 v152, v152, v153\n"
"v_mov_b32 v153, 0\n"
"v_sub_f32 v12, v12, v172\n"
"v_exp_f32 v12, v12\n"
"s_waitcnt lgkmcnt(3)\n"
"v_mfma_f32_16x16x32_bf16 v[148:151], v[124:127], v[140:143], v[148:151]\n"
"ds_write_b64 v6, v[152:153] offset:58376\n"
"v_mfma_f32_16x16x32_bf16 v[8:11], v[128:131], v[140:143], v[8:11]\n"
"s_waitcnt lgkmcnt(3)\n"
"ds_read_b128 v[226:229], v5 offset:32768\n"
"v_mfma_f32_16x16x32_bf16 v[156:159], v[144:147], v[140:143], v[156:159]\n"
"s_waitcnt lgkmcnt(3)\n"
"ds_read_b128 v[20:23], v6 offset:7168\n"
"v_mfma_f32_16x16x32_bf16 v[148:151], v[0:3], v[160:163], v[148:151]\n"
"ds_read_b128 v[168:171], v4 offset:34816\n"
"v_mfma_f32_16x16x32_bf16 v[8:11], v[16:19], v[160:163], v[8:11]\n"
"s_waitcnt lgkmcnt(4)\n"
"v_sub_f32 v13, v13, v172\n"
"v_exp_f32 v13, v13\n"
"v_mfma_f32_16x16x32_bf16 v[156:159], v[164:167], v[160:163], v[156:159]\n"
"ds_read_b128 v[140:143], v6 offset:8192\n"
"v_cvt_pk_bf16_f32 v12, v12, v13\n"
"v_mov_b32 v13, 0\n"
"ds_write_b64 v6, v[12:13] offset:57344\n"
"s_waitcnt lgkmcnt(4)\n"
"v_mfma_f32_16x16x32_bf16 v[148:151], v[132:135], v[226:229], v[148:151]\n"
"ds_read_b128 v[144:147], v5 offset:34816\n"
"v_mfma_f32_16x16x32_bf16 v[8:11], v[136:139], v[226:229], v[8:11]\n"
"s_waitcnt lgkmcnt(4)\n"
"v_mfma_f32_16x16x32_bf16 v[156:159], v[20:23], v[226:229], v[156:159]\n"
"s_waitcnt lgkmcnt(1)\n"
"ds_read_b128 v[152:155], v6 offset:9216\n"
"s_barrier\n"
"v_mfma_f32_16x16x32_bf16 v[148:151], v[100:103], v[168:171], v[148:151]\n"
"ds_read_b128 v[12:15], v4 offset:24576\n"
"v_mfma_f32_16x16x32_bf16 v[8:11], v[104:107], v[168:171], v[8:11]\n"
"v_mfma_f32_16x16x32_bf16 v[156:159], v[140:143], v[168:171], v[156:159]\n"
"s_waitcnt lgkmcnt(2)\n"
"v_mfma_f32_16x16x32_bf16 v[148:151], v[108:111], v[144:147], v[148:151]\n"
"ds_read_b128 v[20:23], v5 offset:24576\n"
"v_mfma_f32_16x16x32_bf16 v[8:11], v[112:115], v[144:147], v[8:11]\n"
"s_waitcnt lgkmcnt(2)\n"
"v_mfma_f32_16x16x32_bf16 v[156:159], v[152:155], v[144:147], v[156:159]\n"
"s_waitcnt lgkmcnt(1)\n"
"v_mfma_f32_16x16x32_bf16 v[226:229], v[28:31], v[12:15], 0\n"
"ds_read_b128 v[160:163], v4 offset:26624\n"
"v_mfma_f32_16x16x32_bf16 v[140:143], v[32:35], v[12:15], 0\n"
"v_mfma_f32_16x16x32_bf16 v[164:167], v[36:39], v[12:15], 0\n"
"s_waitcnt lgkmcnt(1)\n"
"v_mfma_f32_16x16x32_bf16 v[226:229], v[40:43], v[20:23], v[226:229]\n"
"ds_read_b128 v[168:171], v5 offset:26624\n"
"v_mul_f32 v148, s60, v148\n"
"v_mfma_f32_16x16x32_bf16 v[140:143], v[44:47], v[20:23], v[140:143]\n"
"v_mul_f32 v149, s60, v149\n"
"v_cvt_pk_bf16_f32 v148, v148, v149\n"
"v_mfma_f32_16x16x32_bf16 v[164:167], v[48:51], v[20:23], v[164:167]\n"
"s_waitcnt lgkmcnt(1)\n"
"v_bfe_u32 v231, v6, 2, 6\n"
"v_mul_f32 v150, s60, v150\n"
"v_mfma_f32_16x16x32_bf16 v[226:229], v[52:55], v[160:163], v[226:229]\n"
"ds_read_b128 v[12:15], v4 offset:28672\n"
"v_mul_f32 v151, s60, v151\n"
"v_mfma_f32_16x16x32_bf16 v[140:143], v[56:59], v[160:163], v[140:143]\n"
"s_add_u32 s73, s50, 4294966976\n"
"ds_read_b32 v231, v231 offset:1600\n"
"v_cvt_pk_bf16_f32 v149, v150, v151\n"
"v_mfma_f32_16x16x32_bf16 v[164:167], v[60:63], v[160:163], v[164:167]\n"
"v_mul_f32 v8, s60, v8\n"
"v_mul_f32 v9, s60, v9\n"
"v_mul_f32 v10, s60, v10\n"
"v_mul_f32 v11, s60, v11\n"
"v_cvt_pk_bf16_f32 v150, v8, v9\n"
"v_cvt_pk_bf16_f32 v151, v10, v11\n"
"v_mul_f32 v156, s60, v156\n"
"v_mul_f32 v157, s60, v157\n"
"s_waitcnt lgkmcnt(2)\n"
"v_mfma_f32_16x16x32_bf16 v[226:229], v[64:67], v[168:171], v[226:229]\n"
"ds_read_b128 v[20:23], v5 offset:28672\n"
"v_mul_f32 v158, s60, v158\n"
"v_mfma_f32_16x16x32_bf16 v[140:143], v[68:71], v[168:171], v[140:143]\n"
"v_mul_f32 v159, s60, v159\n"
"v_cvt_pk_bf16_f32 v26, v156, v157\n"
"v_mfma_f32_16x16x32_bf16 v[164:167], v[72:75], v[168:171], v[164:167]\n"
"s_add_u32 s68, s50, 4294965632\n"
"v_cvt_pk_bf16_f32 v27, v158, v159\n"
"s_waitcnt lgkmcnt(2)\n"
"v_mfma_f32_16x16x32_bf16 v[226:229], v[76:79], v[12:15], v[226:229]\n"
"s_waitcnt lgkmcnt(1)\n"
"v_add_u32_sdwa v177, v231, v7 dst_sel:DWORD dst_unused:UNUSED_PAD src0_sel:WORD_0 src1_sel:DWORD\n"
"v_mfma_f32_16x16x32_bf16 v[140:143], v[80:83], v[12:15], v[140:143]\n"
"v_min_u32 v176, s73, v177\n"
"v_mfma_f32_16x16x32_bf16 v[164:167], v[84:87], v[12:15], v[164:167]\n"
"s_waitcnt lgkmcnt(0)\n"
"ds_read_b128 v[8:11], v176 offset:2704\n"
"v_min_u32 v175, s68, v177\n"
"ds_read_b128 v[144:147], v176 offset:2720\n"
"v_mfma_f32_16x16x32_bf16 v[226:229], v[88:91], v[20:23], v[226:229]\n"
"v_mfma_f32_16x16x32_bf16 v[140:143], v[92:95], v[20:23], v[140:143]\n"
"ds_read_b128 v[152:155], v175 offset:4048\n"
"v_mfma_f32_16x16x32_bf16 v[164:167], v[96:99], v[20:23], v[164:167]\n"
"s_add_u32 s72, s50, 4294965936\n"
"s_waitcnt lgkmcnt(2)\n"
"v_mfma_f32_16x16x32_bf16 v[8:11], v[184:187], v[148:151], v[8:11]\n"
"ds_read_b128 v[12:15], v175 offset:4064\n"
"v_mfma_f32_16x16x32_bf16 v[8:11], v[186:189], v[24:27], v[8:11]\n"
"s_waitcnt lgkmcnt(2)\n"
"v_mfma_f32_16x16x32_bf16 v[144:147], v[190:193], v[148:151], v[144:147]\n"
"ds_read_b128 v[156:159], v176 offset:2736\n"
"v_cvt_pk_bf16_f32 v226, v226, v227\n"
"v_cvt_pk_bf16_f32 v227, v228, v229\n"
"v_cvt_pk_bf16_f32 v228, v140, v141\n"
"v_cvt_pk_bf16_f32 v229, v142, v143\n"
"v_cvt_pk_bf16_f32 v230, v164, v165\n"
"v_cvt_pk_bf16_f32 v231, v166, v167\n"
"v_mfma_f32_16x16x32_bf16 v[144:147], v[192:195], v[24:27], v[144:147]\n"
"s_waitcnt lgkmcnt(2)\n"
"v_max3_f32 v172, v8, v9, v10\n"
"v_max_f32 v172, v172, v11\n"
"v_mfma_f32_16x16x32_bf16 v[152:155], v[214:217], v[148:151], v[152:155]\n"
"v_max_u32 v174, s72, v177\n"
"v_mfma_f32_16x16x32_bf16 v[152:155], v[216:219], v[24:27], v[152:155]\n"
"s_waitcnt lgkmcnt(1)\n"
"ds_read_b128 v[20:23], v174 offset:1360\n"
"v_max3_f32 v172, v172, v144, v145\n"
"v_max3_f32 v172, v172, v146, v147\n"
"v_mfma_f32_16x16x32_bf16 v[12:15], v[220:223], v[148:151], v[12:15]\n"
"ds_read_b128 v[8:11], v174 offset:1376\n"
"v_mfma_f32_16x16x32_bf16 v[12:15], v[222:225], v[24:27], v[12:15]\n"
"s_waitcnt lgkmcnt(2)\n"
"v_max3_f32 v172, v172, v152, v153\n"
"v_max3_f32 v172, v172, v154, v155\n"
"v_mfma_f32_16x16x32_bf16 v[156:159], v[196:199], v[148:151], v[156:159]\n"
"ds_read_b128 v[140:143], v174 offset:1392\n"
"v_mfma_f32_16x16x32_bf16 v[156:159], v[198:201], v[24:27], v[156:159]\n"
"s_waitcnt lgkmcnt(2)\n"
"s_nop 0\n"
"v_max3_f32 v172, v172, v12, v13\n"
"v_max3_f32 v172, v172, v14, v15\n"
"v_mfma_f32_16x16x32_bf16 v[20:23], v[232:235], v[148:151], v[20:23]\n"
"ds_read_b128 v[144:147], v175 offset:4080\n"
"v_mfma_f32_16x16x32_bf16 v[20:23], v[234:237], v[24:27], v[20:23]\n"
"s_waitcnt lgkmcnt(2)\n"
"v_max3_f32 v172, v172, v156, v157\n"
"v_max3_f32 v172, v172, v158, v159\n"
"v_mfma_f32_16x16x32_bf16 v[8:11], v[238:241], v[148:151], v[8:11]\n"
"ds_read_b128 v[12:15], v176 offset:2704\n"
"v_mfma_f32_16x16x32_bf16 v[8:11], v[240:243], v[24:27], v[8:11]\n"
"s_waitcnt lgkmcnt(2)\n"
"s_nop 0\n"
"v_max3_f32 v172, v172, v20, v21\n"
"v_max3_f32 v172, v172, v22, v23\n"
"v_mfma_f32_16x16x32_bf16 v[140:143], v[244:247], v[148:151], v[140:143]\n"
"ds_read_b128 v[152:155], v176 offset:2720\n"
"v_mfma_f32_16x16x32_bf16 v[140:143], v[246:249], v[24:27], v[140:143]\n"
"s_waitcnt lgkmcnt(2)\n"
"v_max3_f32 v172, v172, v8, v9\n"
"v_max3_f32 v172, v172, v10, v11\n"
"v_mfma_f32_16x16x32_bf16 v[144:147], v[226:229], v[148:151], v[144:147]\n"
"ds_read_b128 v[20:23], v175 offset:4048\n"
"v_mfma_f32_16x16x32_bf16 v[144:147], v[228:231], v[24:27], v[144:147]\n"
"s_waitcnt lgkmcnt(2)\n"
"s_nop 0\n"
"v_max3_f32 v172, v172, v140, v141\n"
"v_max3_f32 v172, v172, v142, v143\n"
"v_mfma_f32_16x16x32_bf16 v[12:15], v[184:187], v[148:151], v[12:15]\n"
"ds_read_b128 v[8:11], v175 offset:4064\n"
"v_mfma_f32_16x16x32_bf16 v[12:15], v[186:189], v[24:27], v[12:15]\n"
"s_nop 0\n"
"v_max3_f32 v172, v172, v144, v145\n"
"v_max3_f32 v172, v172, v146, v147\n"
"v_mov_b32 v173, v172\n"
"s_nop 1\n"
"v_permlane16_swap_b32 v172, v173\n"
"v_max_f32 v172, v172, v173\n"
"v_mov_b32 v173, v172\n"
"s_nop 1\n"
"v_permlane32_swap_b32 v172, v173\n"
"v_max_f32 v172, v172, v173\n"
"v_sub_f32 v12, v12, v172\n"
"v_exp_f32 v12, v12\n"
"v_sub_f32 v13, v13, v172\n"
"v_exp_f32 v13, v13\n"
"v_sub_f32 v14, v14, v172\n"
"v_exp_f32 v14, v14\n"
"v_sub_f32 v15, v15, v172\n"
"v_exp_f32 v15, v15\n"
"s_waitcnt lgkmcnt(2)\n"
"v_mfma_f32_16x16x32_bf16 v[152:155], v[190:193], v[148:151], v[152:155]\n"
"ds_read_b128 v[140:143], v176 offset:2736\n"
"v_cvt_pk_bf16_f32 v12, v12, v13\n"
"v_mfma_f32_16x16x32_bf16 v[152:155], v[192:195], v[24:27], v[152:155]\n"
"v_cvt_pk_bf16_f32 v13, v14, v15\n"
"ds_write_b64 v6, v[12:13] offset:53248\n"
"s_waitcnt lgkmcnt(3)\n"
"v_mfma_f32_16x16x32_bf16 v[20:23], v[214:217], v[148:151], v[20:23]\n"
"ds_read_b128 v[144:147], v174 offset:1360\n"
"v_mfma_f32_16x16x32_bf16 v[20:23], v[216:219], v[24:27], v[20:23]\n"
"s_nop 1\n"
"v_sub_f32 v152, v152, v172\n"
"v_exp_f32 v152, v152\n"
"v_sub_f32 v153, v153, v172\n"
"v_exp_f32 v153, v153\n"
"v_sub_f32 v154, v154, v172\n"
"v_exp_f32 v154, v154\n"
"v_sub_f32 v155, v155, v172\n"
"v_exp_f32 v155, v155\n"
"v_sub_f32 v20, v20, v172\n"
"v_exp_f32 v20, v20\n"
"v_sub_f32 v21, v21, v172\n"
"v_exp_f32 v21, v21\n"
"v_sub_f32 v22, v22, v172\n"
"v_exp_f32 v22, v22\n"
"v_sub_f32 v23, v23, v172\n"
"v_exp_f32 v23, v23\n"
"v_cvt_pk_bf16_f32 v152, v152, v153\n"
"v_cvt_pk_bf16_f32 v153, v154, v155\n"
"v_cvt_pk_bf16_f32 v20, v20, v21\n"
"v_cvt_pk_bf16_f32 v21, v22, v23\n"
"ds_write_b64 v6, v[152:153] offset:53256\n"
"s_waitcnt lgkmcnt(4)\n"
"v_mfma_f32_16x16x32_bf16 v[8:11], v[220:223], v[148:151], v[8:11]\n"
"ds_read_b128 v[12:15], v174 offset:1376\n"
"v_mfma_f32_16x16x32_bf16 v[8:11], v[222:225], v[24:27], v[8:11]\n"
"ds_write_b64 v6, v[20:21] offset:54272\n"
"s_waitcnt lgkmcnt(5)\n"
"v_mfma_f32_16x16x32_bf16 v[140:143], v[196:199], v[148:151], v[140:143]\n"
"ds_read_b128 v[156:159], v174 offset:1392\n"
"v_mfma_f32_16x16x32_bf16 v[140:143], v[198:201], v[24:27], v[140:143]\n"
"s_nop 2\n"
"v_sub_f32 v8, v8, v172\n"
"v_exp_f32 v8, v8\n"
"v_sub_f32 v9, v9, v172\n"
"v_exp_f32 v9, v9\n"
"v_sub_f32 v10, v10, v172\n"
"v_exp_f32 v10, v10\n"
"v_sub_f32 v11, v11, v172\n"
"v_exp_f32 v11, v11\n"
"v_sub_f32 v140, v140, v172\n"
"v_exp_f32 v140, v140\n"
"v_sub_f32 v141, v141, v172\n"
"v_exp_f32 v141, v141\n"
"v_cvt_pk_bf16_f32 v8, v8, v9\n"
"v_sub_f32 v142, v142, v172\n"
"v_exp_f32 v142, v142\n"
"v_cvt_pk_bf16_f32 v9, v10, v11\n"
"v_sub_f32 v143, v143, v172\n"
"v_exp_f32 v143, v143\n"
"v_cvt_pk_bf16_f32 v140, v140, v141\n"
"v_cvt_pk_bf16_f32 v141, v142, v143\n"
"ds_write_b64 v6, v[8:9] offset:54280\n"
"s_waitcnt lgkmcnt(5)\n"
"v_mfma_f32_16x16x32_bf16 v[144:147], v[232:235], v[148:151], v[144:147]\n"
"ds_write_b64 v6, v[140:141] offset:33280\n"
"v_mfma_f32_16x16x32_bf16 v[144:147], v[234:237], v[24:27], v[144:147]\n"
"s_waitcnt lgkmcnt(4)\n"
"ds_read_b128 v[20:23], v175 offset:4080\n"
"v_mfma_f32_16x16x32_bf16 v[12:15], v[238:241], v[148:151], v[12:15]\n"
"ds_read_b128 v[152:155], v4 offset:55296\n"
"v_mfma_f32_16x16x32_bf16 v[12:15], v[240:243], v[24:27], v[12:15]\n"
"s_waitcnt lgkmcnt(2)\n"
"s_barrier\n"
"s_nop 0\n"
"v_sub_f32 v144, v144, v172\n"
"v_exp_f32 v144, v144\n"
"v_sub_f32 v145, v145, v172\n"
"v_exp_f32 v145, v145\n"
"v_sub_f32 v146, v146, v172\n"
"v_exp_f32 v146, v146\n"
"v_sub_f32 v147, v147, v172\n"
"v_exp_f32 v147, v147\n"
"v_sub_f32 v12, v12, v172\n"
"v_exp_f32 v12, v12\n"
"v_cvt_pk_bf16_f32 v144, v144, v145\n"
"v_cvt_pk_bf16_f32 v145, v146, v147\n"
"v_sub_f32 v13, v13, v172\n"
"v_exp_f32 v13, v13\n"
"v_sub_f32 v14, v14, v172\n"
"v_exp_f32 v14, v14\n"
"v_sub_f32 v15, v15, v172\n"
"v_exp_f32 v15, v15\n"
"v_cvt_pk_bf16_f32 v12, v12, v13\n"
"v_cvt_pk_bf16_f32 v13, v14, v15\n"
"ds_write_b64 v6, v[144:145] offset:57352\n"
"v_mfma_f32_16x16x32_bf16 v[156:159], v[244:247], v[148:151], v[156:159]\n"
"ds_read_b128 v[232:235], v6 offset:4096\n"
"v_mfma_f32_16x16x32_bf16 v[156:159], v[246:249], v[24:27], v[156:159]\n"
"ds_write_b64 v6, v[12:13] offset:58368\n"
"s_waitcnt lgkmcnt(4)\n"
"v_mfma_f32_16x16x32_bf16 v[20:23], v[226:229], v[148:151], v[20:23]\n"
"ds_read_b128 v[8:11], v5 offset:55296\n"
"v_mfma_f32_16x16x32_bf16 v[20:23], v[228:231], v[24:27], v[20:23]\n"
"s_waitcnt lgkmcnt(4)\n"
"ds_read_b128 v[140:143], v6 offset:5120\n"
"v_mfma_f32_16x16x32_bf16 v[144:147], v[116:119], v[152:155], 0\n"
"ds_read_b128 v[160:163], v4 offset:57344\n"
"v_mfma_f32_16x16x32_bf16 v[12:15], v[120:123], v[152:155], 0\n"
"v_sub_f32 v156, v156, v172\n"
"v_exp_f32 v156, v156\n"
"v_sub_f32 v157, v157, v172\n"
"v_exp_f32 v157, v157\n"
"v_sub_f32 v158, v158, v172\n"
"v_exp_f32 v158, v158\n"
"s_waitcnt lgkmcnt(4)\n"
"v_mfma_f32_16x16x32_bf16 v[148:151], v[232:235], v[152:155], 0\n"
"ds_read_b128 v[164:167], v6 offset:6144\n"
"v_sub_f32 v159, v159, v172\n"
"v_exp_f32 v159, v159\n"
"v_cvt_pk_bf16_f32 v156, v156, v157\n"
"v_cvt_pk_bf16_f32 v157, v158, v159\n"
"s_waitcnt lgkmcnt(3)\n"
"v_mfma_f32_16x16x32_bf16 v[144:147], v[124:127], v[8:11], v[144:147]\n"
"ds_write_b64 v6, v[156:157] offset:58376\n"
"v_mfma_f32_16x16x32_bf16 v[12:15], v[128:131], v[8:11], v[12:15]\n"
"s_waitcnt lgkmcnt(3)\n"
"ds_read_b128 v[232:235], v5 offset:57344\n"
"v_mfma_f32_16x16x32_bf16 v[148:151], v[140:143], v[8:11], v[148:151]\n"
"s_waitcnt lgkmcnt(3)\n"
"ds_read_b128 v[152:155], v6 offset:7168\n"
"v_mfma_f32_16x16x32_bf16 v[144:147], v[0:3], v[160:163], v[144:147]\n"
"ds_read_b128 v[168:171], v4 offset:59392\n"
"v_sub_f32 v20, v20, v172\n"
"v_exp_f32 v20, v20\n"
"v_sub_f32 v21, v21, v172\n"
"v_exp_f32 v21, v21\n"
"v_mfma_f32_16x16x32_bf16 v[12:15], v[16:19], v[160:163], v[12:15]\n"
"s_waitcnt lgkmcnt(4)\n"
"v_sub_f32 v22, v22, v172\n"
"v_exp_f32 v22, v22\n"
"v_mfma_f32_16x16x32_bf16 v[148:151], v[164:167], v[160:163], v[148:151]\n"
"ds_read_b128 v[8:11], v6 offset:8192\n"
"v_sub_f32 v23, v23, v172\n"
"v_exp_f32 v23, v23\n"
"v_cvt_pk_bf16_f32 v20, v20, v21\n"
"v_cvt_pk_bf16_f32 v21, v22, v23\n"
"s_waitcnt lgkmcnt(3)\n"
"v_mfma_f32_16x16x32_bf16 v[144:147], v[132:135], v[232:235], v[144:147]\n"
"ds_write_b64 v6, v[20:21] offset:57344\n"
"v_mfma_f32_16x16x32_bf16 v[12:15], v[136:139], v[232:235], v[12:15]\n"
"s_waitcnt lgkmcnt(3)\n"
"ds_read_b128 v[140:143], v5 offset:59392\n"
"v_mfma_f32_16x16x32_bf16 v[148:151], v[152:155], v[232:235], v[148:151]\n"
"s_waitcnt lgkmcnt(3)\n"
"ds_read_b128 v[156:159], v6 offset:9216\n"
"v_mfma_f32_16x16x32_bf16 v[144:147], v[100:103], v[168:171], v[144:147]\n"
"ds_read_b128 v[160:163], v4 offset:49152\n"
"v_mfma_f32_16x16x32_bf16 v[12:15], v[104:107], v[168:171], v[12:15]\n"
"s_waitcnt lgkmcnt(3)\n"
"s_barrier\n"
"v_mfma_f32_16x16x32_bf16 v[148:151], v[8:11], v[168:171], v[148:151]\n"
"s_waitcnt lgkmcnt(2)\n"
"v_mfma_f32_16x16x32_bf16 v[144:147], v[108:111], v[140:143], v[144:147]\n"
"ds_read_b128 v[20:23], v5 offset:49152\n"
"v_mfma_f32_16x16x32_bf16 v[12:15], v[112:115], v[140:143], v[12:15]\n"
"s_waitcnt lgkmcnt(2)\n"
"v_mfma_f32_16x16x32_bf16 v[148:151], v[156:159], v[140:143], v[148:151]\n"
"s_waitcnt lgkmcnt(1)\n"
"v_mfma_f32_16x16x32_bf16 v[232:235], v[28:31], v[160:163], 0\n"
"ds_read_b128 v[152:155], v4 offset:51200\n"
"v_mfma_f32_16x16x32_bf16 v[8:11], v[32:35], v[160:163], 0\n"
"v_mfma_f32_16x16x32_bf16 v[164:167], v[36:39], v[160:163], 0\n"
"s_waitcnt lgkmcnt(1)\n"
"v_mfma_f32_16x16x32_bf16 v[232:235], v[40:43], v[20:23], v[232:235]\n"
"ds_read_b128 v[168:171], v5 offset:51200\n"
"v_mul_f32 v144, s60, v144\n"
"v_mfma_f32_16x16x32_bf16 v[8:11], v[44:47], v[20:23], v[8:11]\n"
"v_mul_f32 v145, s60, v145\n"
"v_cvt_pk_bf16_f32 v144, v144, v145\n"
"v_mfma_f32_16x16x32_bf16 v[164:167], v[48:51], v[20:23], v[164:167]\n"
"s_waitcnt lgkmcnt(1)\n"
"v_bfe_u32 v177, v6, 2, 6\n"
"v_mul_f32 v146, s60, v146\n"
"v_mfma_f32_16x16x32_bf16 v[232:235], v[52:55], v[152:155], v[232:235]\n"
"ds_read_b128 v[140:143], v4 offset:53248\n"
"v_mul_f32 v147, s60, v147\n"
"v_mfma_f32_16x16x32_bf16 v[8:11], v[56:59], v[152:155], v[8:11]\n"
"s_add_u32 s69, s50, 4294965920\n"
"ds_read_b32 v177, v177 offset:1664\n"
"v_cvt_pk_bf16_f32 v145, v146, v147\n"
"v_mfma_f32_16x16x32_bf16 v[164:167], v[60:63], v[152:155], v[164:167]\n"
"v_mul_f32 v12, s60, v12\n"
"v_mul_f32 v13, s60, v13\n"
"v_mul_f32 v14, s60, v14\n"
"v_mul_f32 v15, s60, v15\n"
"v_cvt_pk_bf16_f32 v146, v12, v13\n"
"v_cvt_pk_bf16_f32 v147, v14, v15\n"
"v_mul_f32 v148, s60, v148\n"
"v_mul_f32 v149, s60, v149\n"
"s_waitcnt lgkmcnt(2)\n"
"v_mfma_f32_16x16x32_bf16 v[232:235], v[64:67], v[168:171], v[232:235]\n"
"ds_read_b128 v[20:23], v5 offset:53248\n"
"v_mul_f32 v150, s60, v150\n"
"v_mfma_f32_16x16x32_bf16 v[8:11], v[68:71], v[168:171], v[8:11]\n"
"v_mul_f32 v151, s60, v151\n"
"v_cvt_pk_bf16_f32 v26, v148, v149\n"
"v_mfma_f32_16x16x32_bf16 v[164:167], v[72:75], v[168:171], v[164:167]\n"
"s_add_u32 s70, s50, 4294966960\n"
"v_cvt_pk_bf16_f32 v27, v150, v151\n"
"s_waitcnt lgkmcnt(2)\n"
"v_mfma_f32_16x16x32_bf16 v[232:235], v[76:79], v[140:143], v[232:235]\n"
"s_waitcnt lgkmcnt(1)\n"
"v_add_u32_sdwa v176, v177, v7 dst_sel:DWORD dst_unused:UNUSED_PAD src0_sel:WORD_0 src1_sel:DWORD\n"
"v_mfma_f32_16x16x32_bf16 v[8:11], v[80:83], v[140:143], v[8:11]\n"
"v_max_u32 v149, s69, v176\n"
"v_add_u32_sdwa v177, sext(v177), v176 dst_sel:DWORD dst_unused:UNUSED_PAD src0_sel:WORD_1 src1_sel:DWORD\n"
"v_mfma_f32_16x16x32_bf16 v[164:167], v[84:87], v[140:143], v[164:167]\n"
"ds_read_b128 v[12:15], v149 offset:1392\n"
"v_max_u32 v148, s69, v177\n"
"v_min_u32 v151, s70, v176\n"
"ds_read_b128 v[152:155], v148 offset:1408\n"
"s_waitcnt lgkmcnt(2)\n"
"v_mfma_f32_16x16x32_bf16 v[232:235], v[88:91], v[20:23], v[232:235]\n"
"v_mfma_f32_16x16x32_bf16 v[8:11], v[92:95], v[20:23], v[8:11]\n"
"ds_read_b128 v[140:143], v151 offset:2720\n"
"v_mfma_f32_16x16x32_bf16 v[164:167], v[96:99], v[20:23], v[164:167]\n"
"s_waitcnt lgkmcnt(2)\n"
"v_mfma_f32_16x16x32_bf16 v[12:15], v[244:247], v[144:147], v[12:15]\n"
"ds_read_b128 v[156:159], v151 offset:2736\n"
"v_min_u32 v150, s70, v177\n"
"v_mfma_f32_16x16x32_bf16 v[12:15], v[246:249], v[24:27], v[12:15]\n"
"s_add_u32 s71, s50, 4294965616\n"
"v_cvt_pk_bf16_f32 v232, v232, v233\n"
"v_cvt_pk_bf16_f32 v233, v234, v235\n"
"v_cvt_pk_bf16_f32 v234, v8, v9\n"
"v_cvt_pk_bf16_f32 v235, v10, v11\n"
"v_cvt_pk_bf16_f32 v236, v164, v165\n"
"v_cvt_pk_bf16_f32 v237, v166, v167\n"
"s_waitcnt lgkmcnt(2)\n"
"v_mfma_f32_16x16x32_bf16 v[152:155], v[250:253], v[144:147], v[152:155]\n"
"ds_read_b128 v[20:23], v150 offset:2752\n"
"v_mfma_f32_16x16x32_bf16 v[152:155], v[252:255], v[24:27], v[152:155]\n"
"s_waitcnt lgkmcnt(2)\n"
"v_max3_f32 v8, v12, v13, v14\n"
"v_max_f32 v8, v8, v15\n"
"v_mfma_f32_16x16x32_bf16 v[140:143], v[190:193], v[144:147], v[140:143]\n"
"ds_read_b128 v[160:163], v149 offset:1376\n"
"v_min_u32 v11, s71, v176\n"
"v_mfma_f32_16x16x32_bf16 v[140:143], v[192:195], v[24:27], v[140:143]\n"
"s_waitcnt lgkmcnt(2)\n"
"v_max3_f32 v8, v8, v152, v153\n"
"v_max3_f32 v8, v8, v154, v155\n"
"v_mfma_f32_16x16x32_bf16 v[156:159], v[196:199], v[144:147], v[156:159]\n"
"ds_read_b128 v[12:15], v11 offset:4064\n"
"v_mfma_f32_16x16x32_bf16 v[156:159], v[198:201], v[24:27], v[156:159]\n"
"s_waitcnt lgkmcnt(2)\n"
"s_nop 0\n"
"v_max3_f32 v8, v8, v140, v141\n"
"v_max3_f32 v8, v8, v142, v143\n"
"v_mfma_f32_16x16x32_bf16 v[20:23], v[202:205], v[144:147], v[20:23]\n"
"ds_read_b128 v[152:155], v11 offset:4080\n"
"v_min_u32 v10, s71, v177\n"
"v_mfma_f32_16x16x32_bf16 v[20:23], v[204:207], v[24:27], v[20:23]\n"
"s_waitcnt lgkmcnt(2)\n"
"v_max3_f32 v8, v8, v156, v157\n"
"v_max3_f32 v8, v8, v158, v159\n"
"v_mfma_f32_16x16x32_bf16 v[160:163], v[238:241], v[144:147], v[160:163]\n"
"ds_read_b128 v[140:143], v10 offset:4096\n"
"v_mfma_f32_16x16x32_bf16 v[160:163], v[240:243], v[24:27], v[160:163]\n"
"s_waitcnt lgkmcnt(2)\n"
"s_nop 0\n"
"v_max3_f32 v8, v8, v20, v21\n"
"v_max3_f32 v8, v8, v22, v23\n"
"v_mfma_f32_16x16x32_bf16 v[12:15], v[220:223], v[144:147], v[12:15]\n"
"ds_read_b128 v[156:159], v149 offset:1392\n"
"v_mfma_f32_16x16x32_bf16 v[12:15], v[222:225], v[24:27], v[12:15]\n"
"s_waitcnt lgkmcnt(2)\n"
"v_max3_f32 v8, v8, v160, v161\n"
"v_max3_f32 v8, v8, v162, v163\n"
"v_mfma_f32_16x16x32_bf16 v[152:155], v[226:229], v[144:147], v[152:155]\n"
"ds_read_b128 v[20:23], v148 offset:1408\n"
"v_mfma_f32_16x16x32_bf16 v[152:155], v[228:231], v[24:27], v[152:155]\n"
"s_waitcnt lgkmcnt(2)\n"
"s_nop 0\n"
"v_max3_f32 v8, v8, v12, v13\n"
"v_max3_f32 v8, v8, v14, v15\n"
"v_mfma_f32_16x16x32_bf16 v[140:143], v[232:235], v[144:147], v[140:143]\n"
"ds_read_b128 v[160:163], v151 offset:2720\n"
"v_mfma_f32_16x16x32_bf16 v[140:143], v[234:237], v[24:27], v[140:143]\n"
"s_waitcnt lgkmcnt(2)\n"
"v_max3_f32 v8, v8, v152, v153\n"
"v_max3_f32 v8, v8, v154, v155\n"
"v_mfma_f32_16x16x32_bf16 v[156:159], v[244:247], v[144:147], v[156:159]\n"
"ds_read_b128 v[12:15], v151 offset:2736\n"
"v_mfma_f32_16x16x32_bf16 v[156:159], v[246:249], v[24:27], v[156:159]\n"
"s_nop 1\n"
"v_max3_f32 v8, v8, v140, v141\n"
"v_max3_f32 v8, v8, v142, v143\n"
"v_mov_b32 v9, v8\n"
"s_nop 1\n"
"v_permlane16_swap_b32 v8, v9\n"
"v_max_f32 v8, v8, v9\n"
"v_mov_b32 v9, v8\n"
"s_nop 1\n"
"v_permlane32_swap_b32 v8, v9\n"
"v_max_f32 v8, v8, v9\n"
"v_sub_f32 v156, v156, v8\n"
"v_exp_f32 v156, v156\n"
"v_sub_f32 v157, v157, v8\n"
"v_exp_f32 v157, v157\n"
"v_sub_f32 v158, v158, v8\n"
"v_exp_f32 v158, v158\n"
"v_sub_f32 v159, v159, v8\n"
"v_exp_f32 v159, v159\n"
"s_waitcnt lgkmcnt(2)\n"
"v_mfma_f32_16x16x32_bf16 v[20:23], v[250:253], v[144:147], v[20:23]\n"
"ds_read_b128 v[244:247], v150 offset:2752\n"
"v_cvt_pk_bf16_f32 v156, v156, v157\n"
"v_mfma_f32_16x16x32_bf16 v[20:23], v[252:255], v[24:27], v[20:23]\n"
"v_cvt_pk_bf16_f32 v157, v158, v159\n"
"ds_write_b64 v6, v[156:157] offset:55296\n"
"s_waitcnt lgkmcnt(3)\n"
"v_mfma_f32_16x16x32_bf16 v[160:163], v[190:193], v[144:147], v[160:163]\n"
"ds_read_b128 v[248:251], v149 offset:1376\n"
"v_mfma_f32_16x16x32_bf16 v[160:163], v[192:195], v[24:27], v[160:163]\n"
"s_nop 1\n"
"v_sub_f32 v20, v20, v8\n"
"v_exp_f32 v20, v20\n"
"v_sub_f32 v21, v21, v8\n"
"v_exp_f32 v21, v21\n"
"v_sub_f32 v22, v22, v8\n"
"v_exp_f32 v22, v22\n"
"v_sub_f32 v23, v23, v8\n"
"v_exp_f32 v23, v23\n"
"v_sub_f32 v162, v162, v8\n"
"v_exp_f32 v162, v162\n"
"v_sub_f32 v163, v163, v8\n"
"v_exp_f32 v163, v163\n"
"v_cvt_pk_bf16_f32 v20, v20, v21\n"
"v_cvt_pk_bf16_f32 v21, v22, v23\n"
"v_mov_b32 v160, 0\n"
"v_cvt_pk_bf16_f32 v161, v162, v163\n"
"ds_write_b64 v6, v[20:21] offset:55304\n"
"s_waitcnt lgkmcnt(4)\n"
"v_mfma_f32_16x16x32_bf16 v[12:15], v[196:199], v[144:147], v[12:15]\n"
"ds_read_b128 v[252:255], v11 offset:4064\n"
"v_mfma_f32_16x16x32_bf16 v[12:15], v[198:201], v[24:27], v[12:15]\n"
"ds_write_b64 v6, v[160:161] offset:56320\n"
"s_waitcnt lgkmcnt(5)\n"
"v_mfma_f32_16x16x32_bf16 v[244:247], v[202:205], v[144:147], v[244:247]\n"
"ds_read_b128 v[140:143], v11 offset:4080\n"
"v_mfma_f32_16x16x32_bf16 v[244:247], v[204:207], v[24:27], v[244:247]\n"
"s_nop 2\n"
"v_sub_f32 v12, v12, v8\n"
"v_exp_f32 v12, v12\n"
"v_sub_f32 v13, v13, v8\n"
"v_exp_f32 v13, v13\n"
"v_sub_f32 v14, v14, v8\n"
"v_exp_f32 v14, v14\n"
"v_sub_f32 v15, v15, v8\n"
"v_exp_f32 v15, v15\n"
"v_sub_f32 v244, v244, v8\n"
"v_exp_f32 v244, v244\n"
"v_sub_f32 v245, v245, v8\n"
"v_exp_f32 v245, v245\n"
"v_cvt_pk_bf16_f32 v12, v12, v13\n"
"v_sub_f32 v246, v246, v8\n"
"v_exp_f32 v246, v246\n"
"v_cvt_pk_bf16_f32 v13, v14, v15\n"
"v_sub_f32 v247, v247, v8\n"
"v_exp_f32 v247, v247\n"
"v_cvt_pk_bf16_f32 v244, v244, v245\n"
"v_cvt_pk_bf16_f32 v245, v246, v247\n"
"ds_write_b64 v6, v[12:13] offset:56328\n"
"s_waitcnt lgkmcnt(5)\n"
"v_mfma_f32_16x16x32_bf16 v[248:251], v[238:241], v[144:147], v[248:251]\n"
"ds_write_b64 v6, v[244:245] offset:33288\n"
"v_mfma_f32_16x16x32_bf16 v[248:251], v[240:243], v[24:27], v[248:251]\n"
"s_waitcnt lgkmcnt(4)\n"
"ds_read_b128 v[20:23], v10 offset:4096\n"
"v_mfma_f32_16x16x32_bf16 v[252:255], v[220:223], v[144:147], v[252:255]\n"
"ds_read_b128 v[148:151], v4 offset:36864\n"
"v_mfma_f32_16x16x32_bf16 v[252:255], v[222:225], v[24:27], v[252:255]\n"
"s_waitcnt lgkmcnt(2)\n"
"s_barrier\n"
"s_nop 0\n"
"v_sub_f32 v250, v250, v8\n"
"v_exp_f32 v250, v250\n"
"v_sub_f32 v251, v251, v8\n"
"v_exp_f32 v251, v251\n"
"v_mov_b32 v248, 0\n"
"v_cvt_pk_bf16_f32 v249, v250, v251\n"
"ds_write_b64 v6, v[248:249] offset:58376\n"
"v_sub_f32 v254, v254, v8\n"
"v_exp_f32 v254, v254\n"
"v_mfma_f32_16x16x32_bf16 v[140:143], v[226:229], v[144:147], v[140:143]\n"
"ds_read_b128 v[238:241], v6 offset:4096\n"
"v_mov_b32 v252, 0\n"
"v_sub_f32 v255, v255, v8\n"
"v_exp_f32 v255, v255\n"
"v_mfma_f32_16x16x32_bf16 v[140:143], v[228:231], v[24:27], v[140:143]\n"
"ds_read_b128 v[242:245], v5 offset:36864\n"
"v_cvt_pk_bf16_f32 v253, v254, v255\n"
"ds_write_b64 v6, v[252:253] offset:57344\n"
"s_waitcnt lgkmcnt(5)\n"
"v_mfma_f32_16x16x32_bf16 v[20:23], v[232:235], v[144:147], v[20:23]\n"
"v_mfma_f32_16x16x32_bf16 v[20:23], v[234:237], v[24:27], v[20:23]\n"
"s_waitcnt lgkmcnt(4)\n"
"ds_read_b128 v[246:249], v6 offset:5120\n"
"v_mfma_f32_16x16x32_bf16 v[12:15], v[116:119], v[148:151], 0\n"
"ds_read_b128 v[152:155], v4 offset:38912\n"
"v_sub_f32 v140, v140, v8\n"
"v_exp_f32 v140, v140\n"
"v_sub_f32 v141, v141, v8\n"
"v_exp_f32 v141, v141\n"
"v_mfma_f32_16x16x32_bf16 v[250:253], v[120:123], v[148:151], 0\n"
"s_waitcnt lgkmcnt(4)\n"
"v_sub_f32 v142, v142, v8\n"
"v_exp_f32 v142, v142\n"
"v_mfma_f32_16x16x32_bf16 v[144:147], v[238:241], v[148:151], 0\n"
"ds_read_b128 v[156:159], v6 offset:6144\n"
"v_cvt_pk_bf16_f32 v140, v140, v141\n"
"v_sub_f32 v143, v143, v8\n"
"v_exp_f32 v143, v143\n"
"s_waitcnt lgkmcnt(4)\n"
"v_mfma_f32_16x16x32_bf16 v[12:15], v[124:127], v[242:245], v[12:15]\n"
"ds_read_b128 v[160:163], v5 offset:38912\n"
"v_cvt_pk_bf16_f32 v141, v142, v143\n"
"v_mfma_f32_16x16x32_bf16 v[250:253], v[128:131], v[242:245], v[250:253]\n"
"ds_write_b64 v6, v[140:141] offset:57352\n"
"s_waitcnt lgkmcnt(4)\n"
"v_mfma_f32_16x16x32_bf16 v[144:147], v[246:249], v[242:245], v[144:147]\n"
"s_waitcnt lgkmcnt(3)\n"
"ds_read_b128 v[238:241], v6 offset:7168\n"
"v_mfma_f32_16x16x32_bf16 v[12:15], v[0:3], v[152:155], v[12:15]\n"
"ds_read_b128 v[148:151], v4 offset:40960\n"
"v_sub_f32 v20, v20, v8\n"
"v_exp_f32 v20, v20\n"
"v_sub_f32 v21, v21, v8\n"
"v_exp_f32 v21, v21\n"
"v_mfma_f32_16x16x32_bf16 v[250:253], v[16:19], v[152:155], v[250:253]\n"
"s_waitcnt lgkmcnt(4)\n"
"v_sub_f32 v22, v22, v8\n"
"v_exp_f32 v22, v22\n"
"v_mfma_f32_16x16x32_bf16 v[144:147], v[156:159], v[152:155], v[144:147]\n"
"ds_read_b128 v[242:245], v6 offset:8192\n"
"v_cvt_pk_bf16_f32 v20, v20, v21\n"
"v_sub_f32 v23, v23, v8\n"
"v_exp_f32 v23, v23\n"
"s_waitcnt lgkmcnt(4)\n"
"v_mfma_f32_16x16x32_bf16 v[12:15], v[132:135], v[160:163], v[12:15]\n"
"ds_read_b128 v[246:249], v5 offset:40960\n"
"v_cvt_pk_bf16_f32 v21, v22, v23\n"
"v_mfma_f32_16x16x32_bf16 v[250:253], v[136:139], v[160:163], v[250:253]\n"
"ds_write_b64 v6, v[20:21] offset:58368\n"
"s_waitcnt lgkmcnt(4)\n"
"v_mfma_f32_16x16x32_bf16 v[144:147], v[238:241], v[160:163], v[144:147]\n"
"s_waitcnt lgkmcnt(3)\n"
"ds_read_b128 v[8:11], v6 offset:9216\n"
"v_mfma_f32_16x16x32_bf16 v[12:15], v[100:103], v[148:151], v[12:15]\n"
"ds_read_b128 v[140:143], v4 offset:0\n"
"v_mfma_f32_16x16x32_bf16 v[250:253], v[104:107], v[148:151], v[250:253]\n"
"s_waitcnt lgkmcnt(2)\n"
"s_barrier\n"
"v_mfma_f32_16x16x32_bf16 v[144:147], v[242:245], v[148:151], v[144:147]\n"
"v_mfma_f32_16x16x32_bf16 v[12:15], v[108:111], v[246:249], v[12:15]\n"
"ds_read_b128 v[20:23], v5 offset:0\n"
"v_mfma_f32_16x16x32_bf16 v[250:253], v[112:115], v[246:249], v[250:253]\n"
"s_waitcnt lgkmcnt(2)\n"
"v_mfma_f32_16x16x32_bf16 v[144:147], v[8:11], v[246:249], v[144:147]\n"
"s_waitcnt lgkmcnt(1)\n"
"v_mfma_f32_16x16x32_bf16 v[238:241], v[28:31], v[140:143], 0\n"
"ds_read_b128 v[152:155], v4 offset:2048\n"
"v_mfma_f32_16x16x32_bf16 v[148:151], v[32:35], v[140:143], 0\n"
"v_mfma_f32_16x16x32_bf16 v[156:159], v[36:39], v[140:143], 0\n"
"s_waitcnt lgkmcnt(1)\n"
"v_mfma_f32_16x16x32_bf16 v[238:241], v[40:43], v[20:23], v[238:241]\n"
"ds_read_b128 v[242:245], v5 offset:2048\n"
"v_mul_f32 v12, s60, v12\n"
"v_mfma_f32_16x16x32_bf16 v[148:151], v[44:47], v[20:23], v[148:151]\n"
"v_mul_f32 v13, s60, v13\n"
"v_cvt_pk_bf16_f32 v12, v12, v13\n"
"v_mfma_f32_16x16x32_bf16 v[156:159], v[48:51], v[20:23], v[156:159]\n"
"s_waitcnt lgkmcnt(1)\n"
"v_mul_f32 v14, s60, v14\n"
"v_mul_f32 v15, s60, v15\n"
"v_cvt_pk_bf16_f32 v13, v14, v15\n"
"v_mfma_f32_16x16x32_bf16 v[238:241], v[52:55], v[152:155], v[238:241]\n"
"ds_read_b128 v[8:11], v4 offset:4096\n"
"v_mul_f32 v250, s60, v250\n"
"v_mfma_f32_16x16x32_bf16 v[148:151], v[56:59], v[152:155], v[148:151]\n"
"v_mul_f32 v251, s60, v251\n"
"v_mul_f32 v252, s60, v252\n"
"v_mfma_f32_16x16x32_bf16 v[156:159], v[60:63], v[152:155], v[156:159]\n"
"v_mul_f32 v253, s60, v253\n"
"v_cvt_pk_bf16_f32 v14, v250, v251\n"
"v_cvt_pk_bf16_f32 v15, v252, v253\n"
"v_mul_f32 v144, s60, v144\n"
"v_mul_f32 v145, s60, v145\n"
"v_mul_f32 v146, s60, v146\n"
"v_mul_f32 v147, s60, v147\n"
"s_waitcnt lgkmcnt(1)\n"
"v_mfma_f32_16x16x32_bf16 v[238:241], v[64:67], v[242:245], v[238:241]\n"
"ds_read_b128 v[248:251], v5 offset:4096\n"
"v_cvt_pk_bf16_f32 v26, v144, v145\n"
"v_mfma_f32_16x16x32_bf16 v[148:151], v[68:71], v[242:245], v[148:151]\n"
"v_cvt_pk_bf16_f32 v27, v146, v147\n"
"v_mfma_f32_16x16x32_bf16 v[156:159], v[72:75], v[242:245], v[156:159]\n"
"s_waitcnt lgkmcnt(1)\n"
"v_mfma_f32_16x16x32_bf16 v[238:241], v[76:79], v[8:11], v[238:241]\n"
"ds_read_b128 v[252:255], v4 offset:6144\n"
"v_mfma_f32_16x16x32_bf16 v[148:151], v[80:83], v[8:11], v[148:151]\n"
"v_mfma_f32_16x16x32_bf16 v[156:159], v[84:87], v[8:11], v[156:159]\n"
"s_waitcnt lgkmcnt(1)\n"
"v_mfma_f32_16x16x32_bf16 v[238:241], v[88:91], v[248:251], v[238:241]\n"
"ds_read_b128 v[20:23], v5 offset:6144\n"
"v_mfma_f32_16x16x32_bf16 v[148:151], v[92:95], v[248:251], v[148:151]\n"
"v_mfma_f32_16x16x32_bf16 v[156:159], v[96:99], v[248:251], v[156:159]\n"
"s_waitcnt lgkmcnt(1)\n"
"v_mfma_f32_16x16x32_bf16 v[244:247], v[28:31], v[252:255], 0\n"
"ds_read_b128 v[140:143], v4 offset:8192\n"
"v_mfma_f32_16x16x32_bf16 v[8:11], v[32:35], v[252:255], 0\n"
"v_mfma_f32_16x16x32_bf16 v[144:147], v[36:39], v[252:255], 0\n"
"s_waitcnt lgkmcnt(1)\n"
"v_mfma_f32_16x16x32_bf16 v[244:247], v[40:43], v[20:23], v[244:247]\n"
"ds_read_b128 v[152:155], v5 offset:8192\n"
"v_cvt_pk_bf16_f32 v238, v238, v239\n"
"v_cvt_pk_bf16_f32 v239, v240, v241\n"
"v_cvt_pk_bf16_f32 v240, v148, v149\n"
"v_cvt_pk_bf16_f32 v241, v150, v151\n"
"v_cvt_pk_bf16_f32 v242, v156, v157\n"
"v_cvt_pk_bf16_f32 v243, v158, v159\n"
"v_mfma_f32_16x16x32_bf16 v[8:11], v[44:47], v[20:23], v[8:11]\n"
"v_mfma_f32_16x16x32_bf16 v[144:147], v[48:51], v[20:23], v[144:147]\n"
"s_waitcnt lgkmcnt(1)\n"
"v_bfe_u32 v177, v6, 2, 6\n"
"v_mfma_f32_16x16x32_bf16 v[244:247], v[52:55], v[140:143], v[244:247]\n"
"ds_read_b128 v[248:251], v4 offset:10240\n"
"v_mfma_f32_16x16x32_bf16 v[8:11], v[56:59], v[140:143], v[8:11]\n"
"s_add_u32 s72, s50, 4294964592\n"
"ds_read_b32 v177, v177 offset:1728\n"
"v_mfma_f32_16x16x32_bf16 v[144:147], v[60:63], v[140:143], v[144:147]\n"
"s_waitcnt lgkmcnt(2)\n"
"v_mfma_f32_16x16x32_bf16 v[244:247], v[64:67], v[152:155], v[244:247]\n"
"ds_read_b128 v[252:255], v5 offset:10240\n"
"v_mfma_f32_16x16x32_bf16 v[8:11], v[68:71], v[152:155], v[8:11]\n"
"v_mfma_f32_16x16x32_bf16 v[144:147], v[72:75], v[152:155], v[144:147]\n"
"s_waitcnt lgkmcnt(2)\n"
"v_mfma_f32_16x16x32_bf16 v[244:247], v[76:79], v[248:251], v[244:247]\n"
"s_waitcnt lgkmcnt(1)\n"
"v_add_u32_sdwa v176, v177, v7 dst_sel:DWORD dst_unused:UNUSED_PAD src0_sel:WORD_0 src1_sel:DWORD\n"
"v_mfma_f32_16x16x32_bf16 v[8:11], v[80:83], v[248:251], v[8:11]\n"
"v_max_u32 v175, s72, v176\n"
"v_add_u32_sdwa v177, sext(v177), v176 dst_sel:DWORD dst_unused:UNUSED_PAD src0_sel:WORD_1 src1_sel:DWORD\n"
"v_mfma_f32_16x16x32_bf16 v[144:147], v[84:87], v[248:251], v[144:147]\n"
"s_waitcnt lgkmcnt(0)\n"
"ds_read_b128 v[20:23], v175 offset:2704\n"
"v_mfma_f32_16x16x32_bf16 v[244:247], v[88:91], v[252:255], v[244:247]\n"
"s_add_u32 s73, s50, 4294965632\n"
"ds_read_b128 v[140:143], v175 offset:2720\n"
"v_mfma_f32_16x16x32_bf16 v[8:11], v[92:95], v[252:255], v[8:11]\n"
"ds_read_b128 v[148:151], v175 offset:2736\n"
"v_max_u32 v174, s72, v177\n"
"v_mfma_f32_16x16x32_bf16 v[144:147], v[96:99], v[252:255], v[144:147]\n"
"s_waitcnt lgkmcnt(2)\n"
"v_mfma_f32_16x16x32_bf16 v[20:23], v[184:187], v[12:15], v[20:23]\n"
"ds_read_b128 v[152:155], v174 offset:2752\n"
"v_cvt_pk_bf16_f32 v244, v244, v245\n"
"v_cvt_pk_bf16_f32 v245, v246, v247\n"
"v_mfma_f32_16x16x32_bf16 v[20:23], v[186:189], v[24:27], v[20:23]\n"
"s_waitcnt lgkmcnt(2)\n"
"v_cvt_pk_bf16_f32 v246, v8, v9\n"
"v_cvt_pk_bf16_f32 v247, v10, v11\n"
"v_mfma_f32_16x16x32_bf16 v[140:143], v[190:193], v[12:15], v[140:143]\n"
"v_cvt_pk_bf16_f32 v248, v144, v145\n"
"v_cvt_pk_bf16_f32 v249, v146, v147\n"
"v_mfma_f32_16x16x32_bf16 v[140:143], v[192:195], v[24:27], v[140:143]\n"
"v_min_u32 v251, s73, v176\n"
"ds_read_b128 v[252:255], v251 offset:4048\n"
"v_max3_f32 v172, v20, v21, v22\n"
"v_max_f32 v172, v172, v23\n"
"s_waitcnt lgkmcnt(2)\n"
"v_mfma_f32_16x16x32_bf16 v[148:151], v[196:199], v[12:15], v[148:151]\n"
"ds_read_b128 v[8:11], v251 offset:4064\n"
"v_mfma_f32_16x16x32_bf16 v[148:151], v[198:201], v[24:27], v[148:151]\n"
"s_waitcnt lgkmcnt(2)\n"
"v_max3_f32 v172, v172, v140, v141\n"
"v_max3_f32 v172, v172, v142, v143\n"
"v_mfma_f32_16x16x32_bf16 v[152:155], v[202:205], v[12:15], v[152:155]\n"
"ds_read_b128 v[20:23], v251 offset:4080\n"
"v_min_u32 v250, s73, v177\n"
"v_mfma_f32_16x16x32_bf16 v[152:155], v[204:207], v[24:27], v[152:155]\n"
"s_waitcnt lgkmcnt(2)\n"
"v_max3_f32 v172, v172, v148, v149\n"
"v_max3_f32 v172, v172, v150, v151\n"
"v_mfma_f32_16x16x32_bf16 v[252:255], v[214:217], v[12:15], v[252:255]\n"
"ds_read_b128 v[140:143], v250 offset:4096\n"
"v_mfma_f32_16x16x32_bf16 v[252:255], v[216:219], v[24:27], v[252:255]\n"
"s_waitcnt lgkmcnt(2)\n"
"s_nop 0\n"
"v_max3_f32 v172, v172, v152, v153\n"
"v_max3_f32 v172, v172, v154, v155\n"
"v_mfma_f32_16x16x32_bf16 v[8:11], v[220:223], v[12:15], v[8:11]\n"
"ds_read_b128 v[144:147], v175 offset:2704\n"
"v_mfma_f32_16x16x32_bf16 v[8:11], v[222:225], v[24:27], v[8:11]\n"
"s_waitcnt lgkmcnt(2)\n"
"v_max3_f32 v172, v172, v252, v253\n"
"v_max3_f32 v172, v172, v254, v255\n"
"v_mfma_f32_16x16x32_bf16 v[20:23], v[226:229], v[12:15], v[20:23]\n"
"ds_read_b128 v[148:151], v175 offset:2720\n"
"v_mfma_f32_16x16x32_bf16 v[20:23], v[228:231], v[24:27], v[20:23]\n"
"s_waitcnt lgkmcnt(2)\n"
"s_nop 0\n"
"v_max3_f32 v172, v172, v8, v9\n"
"v_max3_f32 v172, v172, v10, v11\n"
"v_mfma_f32_16x16x32_bf16 v[140:143], v[232:235], v[12:15], v[140:143]\n"
"ds_read_b128 v[252:255], v175 offset:2736\n"
"v_mfma_f32_16x16x32_bf16 v[140:143], v[234:237], v[24:27], v[140:143]\n"
"s_waitcnt lgkmcnt(2)\n"
"v_max3_f32 v172, v172, v20, v21\n"
"v_max3_f32 v172, v172, v22, v23\n"
"v_mfma_f32_16x16x32_bf16 v[144:147], v[184:187], v[12:15], v[144:147]\n"
"ds_read_b128 v[8:11], v174 offset:2752\n"
"v_mfma_f32_16x16x32_bf16 v[144:147], v[186:189], v[24:27], v[144:147]\n"
"s_nop 1\n"
"v_max3_f32 v172, v172, v140, v141\n"
"v_max3_f32 v172, v172, v142, v143\n"
"v_mov_b32 v173, v172\n"
"s_nop 1\n"
"v_permlane16_swap_b32 v172, v173\n"
"v_max_f32 v172, v172, v173\n"
"v_mov_b32 v173, v172\n"
"s_nop 1\n"
"v_permlane32_swap_b32 v172, v173\n"
"v_max_f32 v172, v172, v173\n"
"v_sub_f32 v146, v146, v172\n"
"v_exp_f32 v146, v146\n"
"v_sub_f32 v147, v147, v172\n"
"v_exp_f32 v147, v147\n"
"s_waitcnt lgkmcnt(2)\n"
"v_mfma_f32_16x16x32_bf16 v[148:151], v[190:193], v[12:15], v[148:151]\n"
"ds_read_b128 v[20:23], v251 offset:4048\n"
"v_mov_b32 v144, 0\n"
"v_mfma_f32_16x16x32_bf16 v[148:151], v[192:195], v[24:27], v[148:151]\n"
"v_cvt_pk_bf16_f32 v145, v146, v147\n"
"ds_write_b64 v6, v[144:145] offset:53248\n"
"s_waitcnt lgkmcnt(3)\n"
"v_mfma_f32_16x16x32_bf16 v[252:255], v[196:199], v[12:15], v[252:255]\n"
"ds_read_b128 v[140:143], v251 offset:4064\n"
"v_mfma_f32_16x16x32_bf16 v[252:255], v[198:201], v[24:27], v[252:255]\n"
"s_nop 1\n"
"v_sub_f32 v148, v148, v172\n"
"v_exp_f32 v148, v148\n"
"v_sub_f32 v149, v149, v172\n"
"v_exp_f32 v149, v149\n"
"v_sub_f32 v150, v150, v172\n"
"v_exp_f32 v150, v150\n"
"v_sub_f32 v151, v151, v172\n"
"v_exp_f32 v151, v151\n"
"v_sub_f32 v252, v252, v172\n"
"v_exp_f32 v252, v252\n"
"v_sub_f32 v253, v253, v172\n"
"v_exp_f32 v253, v253\n"
"v_sub_f32 v254, v254, v172\n"
"v_exp_f32 v254, v254\n"
"v_cvt_pk_bf16_f32 v148, v148, v149\n"
"v_cvt_pk_bf16_f32 v149, v150, v151\n"
"v_sub_f32 v255, v255, v172\n"
"v_exp_f32 v255, v255\n"
"v_cvt_pk_bf16_f32 v252, v252, v253\n"
"v_cvt_pk_bf16_f32 v253, v254, v255\n"
"ds_write_b64 v6, v[148:149] offset:53256\n"
"s_waitcnt lgkmcnt(4)\n"
"v_mfma_f32_16x16x32_bf16 v[8:11], v[202:205], v[12:15], v[8:11]\n"
"ds_read_b128 v[144:147], v251 offset:4080\n"
"v_mfma_f32_16x16x32_bf16 v[8:11], v[204:207], v[24:27], v[8:11]\n"
"ds_write_b64 v6, v[252:253] offset:54272\n"
"s_waitcnt lgkmcnt(5)\n"
"v_mfma_f32_16x16x32_bf16 v[20:23], v[214:217], v[12:15], v[20:23]\n"
"ds_read_b128 v[152:155], v250 offset:4096\n"
"v_mfma_f32_16x16x32_bf16 v[20:23], v[216:219], v[24:27], v[20:23]\n"
"s_nop 2\n"
"v_sub_f32 v8, v8, v172\n"
"v_exp_f32 v8, v8\n"
"v_sub_f32 v9, v9, v172\n"
"v_exp_f32 v9, v9\n"
"v_sub_f32 v10, v10, v172\n"
"v_exp_f32 v10, v10\n"
"v_sub_f32 v11, v11, v172\n"
"v_exp_f32 v11, v11\n"
"v_cvt_pk_bf16_f32 v8, v8, v9\n"
"v_cvt_pk_bf16_f32 v9, v10, v11\n"
"v_sub_f32 v22, v22, v172\n"
"v_exp_f32 v22, v22\n"
"v_sub_f32 v23, v23, v172\n"
"v_exp_f32 v23, v23\n"
"v_mov_b32 v20, 0\n"
"v_cvt_pk_bf16_f32 v21, v22, v23\n"
"ds_write_b64 v6, v[8:9] offset:54280\n"
"s_waitcnt lgkmcnt(5)\n"
"v_mfma_f32_16x16x32_bf16 v[140:143], v[220:223], v[12:15], v[140:143]\n"
"ds_read_b128 v[250:253], v4 offset:12288\n"
"v_mfma_f32_16x16x32_bf16 v[140:143], v[222:225], v[24:27], v[140:143]\n"
"s_waitcnt lgkmcnt(1)\n"
"s_barrier\n"
"ds_write_b64 v6, v[20:21] offset:57344\n"
"v_mfma_f32_16x16x32_bf16 v[144:147], v[226:229], v[12:15], v[144:147]\n"
"ds_read_b128 v[8:11], v6 offset:4096\n"
"v_mfma_f32_16x16x32_bf16 v[144:147], v[228:231], v[24:27], v[144:147]\n"
"ds_read_b128 v[148:151], v5 offset:12288\n"
"s_nop 0\n"
"v_sub_f32 v140, v140, v172\n"
"v_exp_f32 v140, v140\n"
"v_sub_f32 v141, v141, v172\n"
"v_exp_f32 v141, v141\n"
"v_sub_f32 v142, v142, v172\n"
"v_exp_f32 v142, v142\n"
"v_sub_f32 v143, v143, v172\n"
"v_exp_f32 v143, v143\n"
"v_cvt_pk_bf16_f32 v140, v140, v141\n"
"v_cvt_pk_bf16_f32 v141, v142, v143\n"
"v_sub_f32 v144, v144, v172\n"
"v_exp_f32 v144, v144\n"
"v_sub_f32 v145, v145, v172\n"
"v_exp_f32 v145, v145\n"
"v_sub_f32 v146, v146, v172\n"
"v_exp_f32 v146, v146\n"
"v_sub_f32 v147, v147, v172\n"
"v_exp_f32 v147, v147\n"
"v_cvt_pk_bf16_f32 v144, v144, v145\n"
"v_cvt_pk_bf16_f32 v145, v146, v147\n"
"ds_write_b64 v6, v[140:141] offset:57352\n"
"v_mfma_f32_16x16x32_bf16 v[152:155], v[232:235], v[12:15], v[152:155]\n"
"ds_write_b64 v6, v[144:145] offset:58368\n"
"v_mfma_f32_16x16x32_bf16 v[152:155], v[234:237], v[24:27], v[152:155]\n"
"s_waitcnt lgkmcnt(5)\n"
"ds_read_b128 v[20:23], v6 offset:5120\n"
"v_mfma_f32_16x16x32_bf16 v[156:159], v[116:119], v[250:253], 0\n"
"ds_read_b128 v[160:163], v4 offset:14336\n"
"v_mfma_f32_16x16x32_bf16 v[140:143], v[120:123], v[250:253], 0\n"
"s_waitcnt lgkmcnt(5)\n"
"v_mfma_f32_16x16x32_bf16 v[12:15], v[8:11], v[250:253], 0\n"
"s_waitcnt lgkmcnt(4)\n"
"ds_read_b128 v[144:147], v6 offset:6144\n"
"v_mfma_f32_16x16x32_bf16 v[156:159], v[124:127], v[148:151], v[156:159]\n"
"ds_read_b128 v[164:167], v5 offset:14336\n"
"v_mfma_f32_16x16x32_bf16 v[140:143], v[128:131], v[148:151], v[140:143]\n"
"v_sub_f32 v152, v152, v172\n"
"v_exp_f32 v152, v152\n"
"v_sub_f32 v153, v153, v172\n"
"v_exp_f32 v153, v153\n"
"s_waitcnt lgkmcnt(3)\n"
"v_mfma_f32_16x16x32_bf16 v[12:15], v[20:23], v[148:151], v[12:15]\n"
"s_waitcnt lgkmcnt(2)\n"
"ds_read_b128 v[250:253], v6 offset:7168\n"
"v_cvt_pk_bf16_f32 v152, v152, v153\n"
"v_mfma_f32_16x16x32_bf16 v[156:159], v[0:3], v[160:163], v[156:159]\n"
"ds_read_b128 v[8:11], v4 offset:16384\n"
"v_mfma_f32_16x16x32_bf16 v[140:143], v[16:19], v[160:163], v[140:143]\n"
"s_waitcnt lgkmcnt(3)\n"
"v_sub_f32 v154, v154, v172\n"
"v_exp_f32 v154, v154\n"
"v_mfma_f32_16x16x32_bf16 v[12:15], v[144:147], v[160:163], v[12:15]\n"
"s_waitcnt lgkmcnt(2)\n"
"ds_read_b128 v[20:23], v6 offset:8192\n"
"v_sub_f32 v155, v155, v172\n"
"v_exp_f32 v155, v155\n"
"v_mfma_f32_16x16x32_bf16 v[156:159], v[132:135], v[164:167], v[156:159]\n"
"ds_read_b128 v[148:151], v5 offset:16384\n"
"v_cvt_pk_bf16_f32 v153, v154, v155\n"
"v_mfma_f32_16x16x32_bf16 v[140:143], v[136:139], v[164:167], v[140:143]\n"
"ds_write_b64 v6, v[152:153] offset:58376\n"
"s_waitcnt lgkmcnt(4)\n"
"v_mfma_f32_16x16x32_bf16 v[12:15], v[250:253], v[164:167], v[12:15]\n"
"s_waitcnt lgkmcnt(3)\n"
"ds_read_b128 v[144:147], v6 offset:9216\n"
"v_mfma_f32_16x16x32_bf16 v[156:159], v[100:103], v[8:11], v[156:159]\n"
"ds_read_b128 v[160:163], v4 offset:43008\n"
"v_mfma_f32_16x16x32_bf16 v[140:143], v[104:107], v[8:11], v[140:143]\n"
"s_waitcnt lgkmcnt(2)\n"
"s_barrier\n"
"v_mfma_f32_16x16x32_bf16 v[12:15], v[20:23], v[8:11], v[12:15]\n"
"v_mfma_f32_16x16x32_bf16 v[156:159], v[108:111], v[148:151], v[156:159]\n"
"ds_read_b128 v[152:155], v5 offset:43008\n"
"v_mfma_f32_16x16x32_bf16 v[140:143], v[112:115], v[148:151], v[140:143]\n"
"s_waitcnt lgkmcnt(2)\n"
"v_mfma_f32_16x16x32_bf16 v[12:15], v[144:147], v[148:151], v[12:15]\n"
"s_waitcnt lgkmcnt(1)\n"
"v_mfma_f32_16x16x32_bf16 v[250:253], v[28:31], v[160:163], 0\n"
"ds_read_b128 v[164:167], v4 offset:45056\n"
"v_mfma_f32_16x16x32_bf16 v[8:11], v[32:35], v[160:163], 0\n"
"v_mfma_f32_16x16x32_bf16 v[20:23], v[36:39], v[160:163], 0\n"
"s_waitcnt lgkmcnt(1)\n"
"v_mfma_f32_16x16x32_bf16 v[250:253], v[40:43], v[152:155], v[250:253]\n"
"ds_read_b128 v[168:171], v5 offset:45056\n"
"v_mul_f32 v156, s60, v156\n"
"v_mfma_f32_16x16x32_bf16 v[8:11], v[44:47], v[152:155], v[8:11]\n"
"v_mul_f32 v157, s60, v157\n"
"v_cvt_pk_bf16_f32 v156, v156, v157\n"
"v_mfma_f32_16x16x32_bf16 v[20:23], v[48:51], v[152:155], v[20:23]\n"
"v_bfe_u32 v255, v6, 2, 6\n"
"v_mul_f32 v158, s60, v158\n"
"v_mul_f32 v159, s60, v159\n"
"v_cvt_pk_bf16_f32 v157, v158, v159\n"
"v_mul_f32 v140, s60, v140\n"
"s_waitcnt lgkmcnt(1)\n"
"v_mfma_f32_16x16x32_bf16 v[250:253], v[52:55], v[164:167], v[250:253]\n"
"ds_read_b128 v[144:147], v4 offset:47104\n"
"v_mul_f32 v141, s60, v141\n"
"v_mfma_f32_16x16x32_bf16 v[8:11], v[56:59], v[164:167], v[8:11]\n"
"s_add_u32 s68, s50, 4294964608\n"
"ds_read_b32 v255, v255 offset:1792\n"
"v_mul_f32 v142, s60, v142\n"
"v_mfma_f32_16x16x32_bf16 v[20:23], v[60:63], v[164:167], v[20:23]\n"
"v_mul_f32 v143, s60, v143\n"
"v_cvt_pk_bf16_f32 v158, v140, v141\n"
"v_cvt_pk_bf16_f32 v159, v142, v143\n"
"v_mul_f32 v12, s60, v12\n"
"v_mul_f32 v13, s60, v13\n"
"s_waitcnt lgkmcnt(2)\n"
"v_mfma_f32_16x16x32_bf16 v[250:253], v[64:67], v[168:171], v[250:253]\n"
"s_add_u32 s69, s50, 4294963264\n"
"ds_read_b128 v[148:151], v5 offset:47104\n"
"v_mul_f32 v14, s60, v14\n"
"v_mfma_f32_16x16x32_bf16 v[8:11], v[68:71], v[168:171], v[8:11]\n"
"v_mul_f32 v15, s60, v15\n"
"v_cvt_pk_bf16_f32 v26, v12, v13\n"
"v_mfma_f32_16x16x32_bf16 v[20:23], v[72:75], v[168:171], v[20:23]\n"
"s_waitcnt lgkmcnt(2)\n"
"v_cvt_pk_bf16_f32 v27, v14, v15\n"
"v_mfma_f32_16x16x32_bf16 v[250:253], v[76:79], v[144:147], v[250:253]\n"
"s_waitcnt lgkmcnt(1)\n"
"v_add_u32_sdwa v254, v255, v7 dst_sel:DWORD dst_unused:UNUSED_PAD src0_sel:WORD_0 src1_sel:DWORD\n"
"v_mfma_f32_16x16x32_bf16 v[8:11], v[80:83], v[144:147], v[8:11]\n"
"v_max_u32 v177, s68, v254\n"
"v_max_u32 v176, s69, v254\n"
"v_mfma_f32_16x16x32_bf16 v[20:23], v[84:87], v[144:147], v[20:23]\n"
"s_waitcnt lgkmcnt(0)\n"
"ds_read_b128 v[12:15], v177 offset:2688\n"
"ds_read_b128 v[140:143], v176 offset:4032\n"
"v_mfma_f32_16x16x32_bf16 v[250:253], v[88:91], v[148:151], v[250:253]\n"
"s_add_u32 s70, s50, 4294964304\n"
"v_mfma_f32_16x16x32_bf16 v[8:11], v[92:95], v[148:151], v[8:11]\n"
"ds_read_b128 v[152:155], v177 offset:2704\n"
"v_mfma_f32_16x16x32_bf16 v[20:23], v[96:99], v[148:151], v[20:23]\n"
"v_min_u32 v175, s70, v254\n"
"s_mov_b64 exec, s[58:59]\n"
"v_add_u32 v175, 1040, v175\n"
"s_mov_b64 exec, -1\n"
"v_cvt_pk_bf16_f32 v250, v250, v251\n"
"v_cvt_pk_bf16_f32 v251, v252, v253\n"
"v_cvt_pk_bf16_f32 v252, v8, v9\n"
"v_cvt_pk_bf16_f32 v253, v10, v11\n"
"v_cvt_pk_bf16_f32 v254, v20, v21\n"
"v_cvt_pk_bf16_f32 v255, v22, v23\n"
"s_waitcnt lgkmcnt(2)\n"
"v_mfma_f32_16x16x32_bf16 v[12:15], v[178:181], v[156:159], v[12:15]\n"
"ds_read_b128 v[144:147], v176 offset:4048\n"
"v_mfma_f32_16x16x32_bf16 v[12:15], v[180:183], v[24:27], v[12:15]\n"
"s_waitcnt lgkmcnt(2)\n"
"v_mfma_f32_16x16x32_bf16 v[140:143], v[208:211], v[156:159], v[140:143]\n"
"ds_read_b128 v[8:11], v175 offset:5376\n"
"v_mfma_f32_16x16x32_bf16 v[140:143], v[210:213], v[24:27], v[140:143]\n"
"s_waitcnt lgkmcnt(2)\n"
"s_nop 2\n"
"v_max3_f32 v172, v12, v13, v14\n"
"v_max_f32 v172, v172, v15\n"
"v_mfma_f32_16x16x32_bf16 v[152:155], v[184:187], v[156:159], v[152:155]\n"
"ds_read_b128 v[20:23], v177 offset:2720\n"
"v_mfma_f32_16x16x32_bf16 v[152:155], v[186:189], v[24:27], v[152:155]\n"
"s_waitcnt lgkmcnt(2)\n"
"v_max3_f32 v172, v172, v140, v141\n"
"v_max3_f32 v172, v172, v142, v143\n"
"v_mfma_f32_16x16x32_bf16 v[144:147], v[214:217], v[156:159], v[144:147]\n"
"ds_read_b128 v[12:15], v176 offset:4064\n"
"v_mfma_f32_16x16x32_bf16 v[144:147], v[216:219], v[24:27], v[144:147]\n"
"s_waitcnt lgkmcnt(2)\n"
"s_nop 0\n"
"v_max3_f32 v172, v172, v152, v153\n"
"v_max3_f32 v172, v172, v154, v155\n"
"v_mfma_f32_16x16x32_bf16 v[8:11], v[238:241], v[156:159], v[8:11]\n"
"ds_read_b128 v[140:143], v175 offset:5392\n"
"v_mfma_f32_16x16x32_bf16 v[8:11], v[240:243], v[24:27], v[8:11]\n"
"s_waitcnt lgkmcnt(2)\n"
"v_max3_f32 v172, v172, v144, v145\n"
"v_max3_f32 v172, v172, v146, v147\n"
"v_mfma_f32_16x16x32_bf16 v[20:23], v[190:193], v[156:159], v[20:23]\n"
"ds_read_b128 v[148:151], v175 offset:5408\n"
"v_mfma_f32_16x16x32_bf16 v[20:23], v[192:195], v[24:27], v[20:23]\n"
"s_waitcnt lgkmcnt(2)\n"
"s_nop 0\n"
"v_max3_f32 v172, v172, v8, v9\n"
"v_max3_f32 v172, v172, v10, v11\n"
"v_mfma_f32_16x16x32_bf16 v[12:15], v[220:223], v[156:159], v[12:15]\n"
"ds_read_b128 v[144:147], v177 offset:2688\n"
"v_mfma_f32_16x16x32_bf16 v[12:15], v[222:225], v[24:27], v[12:15]\n"
"s_waitcnt lgkmcnt(2)\n"
"v_max3_f32 v172, v172, v20, v21\n"
"v_max3_f32 v172, v172, v22, v23\n"
"v_mfma_f32_16x16x32_bf16 v[140:143], v[244:247], v[156:159], v[140:143]\n"
"ds_read_b128 v[8:11], v176 offset:4032\n"
"v_mfma_f32_16x16x32_bf16 v[140:143], v[246:249], v[24:27], v[140:143]\n"
"s_waitcnt lgkmcnt(2)\n"
"s_nop 0\n"
"v_max3_f32 v172, v172, v12, v13\n"
"v_max3_f32 v172, v172, v14, v15\n"
"v_mfma_f32_16x16x32_bf16 v[148:151], v[250:253], v[156:159], v[148:151]\n"
"ds_read_b128 v[20:23], v177 offset:2704\n"
"v_mfma_f32_16x16x32_bf16 v[148:151], v[252:255], v[24:27], v[148:151]\n"
"s_waitcnt lgkmcnt(2)\n"
"v_max3_f32 v172, v172, v140, v141\n"
"v_max3_f32 v172, v172, v142, v143\n"
"v_mfma_f32_16x16x32_bf16 v[144:147], v[178:181], v[156:159], v[144:147]\n"
"ds_read_b128 v[12:15], v176 offset:4048\n"
"v_mfma_f32_16x16x32_bf16 v[144:147], v[180:183], v[24:27], v[144:147]\n"
"s_nop 1\n"
"v_max3_f32 v172, v172, v148, v149\n"
"v_max3_f32 v172, v172, v150, v151\n"
"v_mov_b32 v173, v172\n"
"s_nop 1\n"
"v_permlane16_swap_b32 v172, v173\n"
"v_max_f32 v172, v172, v173\n"
"v_mov_b32 v173, v172\n"
"s_nop 1\n"
"v_permlane32_swap_b32 v172, v173\n"
"v_max_f32 v172, v172, v173\n"
"v_sub_f32 v144, v144, v172\n"
"v_exp_f32 v144, v144\n"
"v_sub_f32 v145, v145, v172\n"
"v_exp_f32 v145, v145\n"
"v_sub_f32 v146, v146, v172\n"
"v_exp_f32 v146, v146\n"
"v_sub_f32 v147, v147, v172\n"
"v_exp_f32 v147, v147\n"
"s_waitcnt lgkmcnt(2)\n"
"v_mfma_f32_16x16x32_bf16 v[8:11], v[208:211], v[156:159], v[8:11]\n"
"ds_read_b128 v[178:181], v175 offset:5376\n"
"v_cvt_pk_bf16_f32 v144, v144, v145\n"
"v_mfma_f32_16x16x32_bf16 v[8:11], v[210:213], v[24:27], v[8:11]\n"
"v_cvt_pk_bf16_f32 v145, v146, v147\n"
"ds_write_b64 v6, v[144:145] offset:55296\n"
"s_waitcnt lgkmcnt(3)\n"
"v_mfma_f32_16x16x32_bf16 v[20:23], v[184:187], v[156:159], v[20:23]\n"
"ds_read_b128 v[140:143], v177 offset:2720\n"
"v_mfma_f32_16x16x32_bf16 v[20:23], v[186:189], v[24:27], v[20:23]\n"
"s_nop 1\n"
"v_sub_f32 v8, v8, v172\n"
"v_exp_f32 v8, v8\n"
"v_sub_f32 v9, v9, v172\n"
"v_exp_f32 v9, v9\n"
"v_sub_f32 v10, v10, v172\n"
"v_exp_f32 v10, v10\n"
"v_sub_f32 v11, v11, v172\n"
"v_exp_f32 v11, v11\n"
"v_sub_f32 v20, v20, v172\n"
"v_exp_f32 v20, v20\n"
"v_sub_f32 v21, v21, v172\n"
"v_exp_f32 v21, v21\n"
"v_sub_f32 v22, v22, v172\n"
"v_exp_f32 v22, v22\n"
"v_cvt_pk_bf16_f32 v8, v8, v9\n"
"v_cvt_pk_bf16_f32 v9, v10, v11\n"
"v_sub_f32 v23, v23, v172\n"
"v_exp_f32 v23, v23\n"
"v_cvt_pk_bf16_f32 v20, v20, v21\n"
"v_cvt_pk_bf16_f32 v21, v22, v23\n"
"ds_write_b64 v6, v[8:9] offset:56320\n"
"s_waitcnt lgkmcnt(4)\n"
"v_mfma_f32_16x16x32_bf16 v[12:15], v[214:217], v[156:159], v[12:15]\n"
"ds_read_b128 v[208:211], v176 offset:4064\n"
"v_mfma_f32_16x16x32_bf16 v[12:15], v[216:219], v[24:27], v[12:15]\n"
"ds_write_b64 v6, v[20:21] offset:55304\n"
"s_waitcnt lgkmcnt(5)\n"
"v_mfma_f32_16x16x32_bf16 v[178:181], v[238:241], v[156:159], v[178:181]\n"
"ds_read_b128 v[144:147], v175 offset:5392\n"
"v_mfma_f32_16x16x32_bf16 v[178:181], v[240:243], v[24:27], v[178:181]\n"
"s_nop 2\n"
"v_sub_f32 v12, v12, v172\n"
"v_exp_f32 v12, v12\n"
"v_sub_f32 v13, v13, v172\n"
"v_exp_f32 v13, v13\n"
"v_sub_f32 v14, v14, v172\n"
"v_exp_f32 v14, v14\n"
"v_sub_f32 v15, v15, v172\n"
"v_exp_f32 v15, v15\n"
"v_cvt_pk_bf16_f32 v12, v12, v13\n"
"v_cvt_pk_bf16_f32 v13, v14, v15\n"
"v_sub_f32 v178, v178, v172\n"
"v_exp_f32 v178, v178\n"
"v_sub_f32 v179, v179, v172\n"
"v_exp_f32 v179, v179\n"
"v_sub_f32 v180, v180, v172\n"
"v_exp_f32 v180, v180\n"
"v_sub_f32 v181, v181, v172\n"
"v_exp_f32 v181, v181\n"
"v_cvt_pk_bf16_f32 v178, v178, v179\n"
"v_cvt_pk_bf16_f32 v179, v180, v181\n"
"ds_write_b64 v6, v[12:13] offset:56328\n"
"s_waitcnt lgkmcnt(5)\n"
"v_mfma_f32_16x16x32_bf16 v[140:143], v[190:193], v[156:159], v[140:143]\n"
"ds_read_b128 v[238:241], v175 offset:5408\n"
"v_mfma_f32_16x16x32_bf16 v[140:143], v[192:195], v[24:27], v[140:143]\n"
"s_waitcnt lgkmcnt(1)\n"
"s_barrier\n"
"ds_write_b64 v6, v[178:179] offset:57344\n"
"v_mfma_f32_16x16x32_bf16 v[208:211], v[220:223], v[156:159], v[208:211]\n"
"ds_read_b128 v[180:183], v4 offset:24576\n"
"v_mfma_f32_16x16x32_bf16 v[208:211], v[222:225], v[24:27], v[208:211]\n"
"s_nop 1\n"
"v_sub_f32 v140, v140, v172\n"
"v_exp_f32 v140, v140\n"
"v_sub_f32 v141, v141, v172\n"
"v_exp_f32 v141, v141\n"
"s_nop 0\n"
"v_cvt_pk_bf16_f32 v140, v140, v141\n"
"v_mov_b32 v141, 0\n"
"ds_write_b64 v6, v[140:141] offset:58368\n"
"v_mfma_f32_16x16x32_bf16 v[144:147], v[244:247], v[156:159], v[144:147]\n"
"ds_read_b128 v[210:213], v6 offset:4096\n"
"v_sub_f32 v208, v208, v172\n"
"v_exp_f32 v208, v208\n"
"v_sub_f32 v209, v209, v172\n"
"v_exp_f32 v209, v209\n"
"v_mfma_f32_16x16x32_bf16 v[144:147], v[246:249], v[24:27], v[144:147]\n"
"ds_read_b128 v[8:11], v5 offset:24576\n"
"v_cvt_pk_bf16_f32 v208, v208, v209\n"
"v_mov_b32 v209, 0\n"
"ds_write_b64 v6, v[208:209] offset:58376\n"
"s_waitcnt lgkmcnt(6)\n"
"v_mfma_f32_16x16x32_bf16 v[238:241], v[250:253], v[156:159], v[238:241]\n"
"v_mfma_f32_16x16x32_bf16 v[238:241], v[252:255], v[24:27], v[238:241]\n"
"s_waitcnt lgkmcnt(4)\n"
"ds_read_b128 v[12:15], v6 offset:5120\n"
"v_mfma_f32_16x16x32_bf16 v[20:23], v[116:119], v[180:183], 0\n"
"ds_read_b128 v[140:143], v4 offset:26624\n"
"v_sub_f32 v144, v144, v172\n"
"v_exp_f32 v144, v144\n"
"v_sub_f32 v145, v145, v172\n"
"v_exp_f32 v145, v145\n"
"v_mfma_f32_16x16x32_bf16 v[240:243], v[120:123], v[180:183], 0\n"
"s_waitcnt lgkmcnt(4)\n"
"v_sub_f32 v146, v146, v172\n"
"v_exp_f32 v146, v146\n"
"v_mfma_f32_16x16x32_bf16 v[148:151], v[210:213], v[180:183], 0\n"
"ds_read_b128 v[152:155], v6 offset:6144\n"
"v_cvt_pk_bf16_f32 v144, v144, v145\n"
"v_sub_f32 v147, v147, v172\n"
"v_exp_f32 v147, v147\n"
"s_waitcnt lgkmcnt(4)\n"
"v_mfma_f32_16x16x32_bf16 v[20:23], v[124:127], v[8:11], v[20:23]\n"
"ds_read_b128 v[156:159], v5 offset:26624\n"
"v_cvt_pk_bf16_f32 v145, v146, v147\n"
"v_mfma_f32_16x16x32_bf16 v[240:243], v[128:131], v[8:11], v[240:243]\n"
"ds_write_b64 v6, v[144:145] offset:57352\n"
"s_waitcnt lgkmcnt(4)\n"
"v_mfma_f32_16x16x32_bf16 v[148:151], v[12:15], v[8:11], v[148:151]\n"
"s_waitcnt lgkmcnt(3)\n"
"ds_read_b128 v[178:181], v6 offset:7168\n"
"v_mfma_f32_16x16x32_bf16 v[20:23], v[0:3], v[140:143], v[20:23]\n"
"ds_read_b128 v[208:211], v4 offset:28672\n"
"v_mfma_f32_16x16x32_bf16 v[240:243], v[16:19], v[140:143], v[240:243]\n"
"v_sub_f32 v238, v238, v172\n"
"v_exp_f32 v238, v238\n"
"v_sub_f32 v239, v239, v172\n"
"v_exp_f32 v239, v239\n"
"s_waitcnt lgkmcnt(4)\n"
"v_mfma_f32_16x16x32_bf16 v[148:151], v[152:155], v[140:143], v[148:151]\n"
"s_waitcnt lgkmcnt(3)\n"
"ds_read_b128 v[8:11], v6 offset:8192\n"
"v_cvt_pk_bf16_f32 v238, v238, v239\n"
"v_mov_b32 v239, 0\n"
"v_mfma_f32_16x16x32_bf16 v[20:23], v[132:135], v[156:159], v[20:23]\n"
"ds_write_b64 v6, v[238:239] offset:33288\n"
"v_mfma_f32_16x16x32_bf16 v[240:243], v[136:139], v[156:159], v[240:243]\n"
"s_waitcnt lgkmcnt(3)\n"
"ds_read_b128 v[12:15], v5 offset:28672\n"
"v_mfma_f32_16x16x32_bf16 v[148:151], v[178:181], v[156:159], v[148:151]\n"
"s_waitcnt lgkmcnt(3)\n"
"ds_read_b128 v[140:143], v6 offset:9216\n"
"v_mfma_f32_16x16x32_bf16 v[20:23], v[100:103], v[208:211], v[20:23]\n"
"ds_read_b128 v[144:147], v4 offset:18432\n"
"v_mfma_f32_16x16x32_bf16 v[240:243], v[104:107], v[208:211], v[240:243]\n"
"s_waitcnt lgkmcnt(3)\n"
"s_barrier\n"
"v_mfma_f32_16x16x32_bf16 v[148:151], v[8:11], v[208:211], v[148:151]\n"
"s_waitcnt lgkmcnt(2)\n"
"v_mfma_f32_16x16x32_bf16 v[20:23], v[108:111], v[12:15], v[20:23]\n"
"ds_read_b128 v[152:155], v5 offset:18432\n"
"v_mfma_f32_16x16x32_bf16 v[240:243], v[112:115], v[12:15], v[240:243]\n"
"s_waitcnt lgkmcnt(2)\n"
"v_mfma_f32_16x16x32_bf16 v[148:151], v[140:143], v[12:15], v[148:151]\n"
"s_waitcnt lgkmcnt(1)\n"
"v_mfma_f32_16x16x32_bf16 v[178:181], v[28:31], v[144:147], 0\n"
"ds_read_b128 v[156:159], v4 offset:20480\n"
"v_mfma_f32_16x16x32_bf16 v[208:211], v[32:35], v[144:147], 0\n"
"v_mfma_f32_16x16x32_bf16 v[8:11], v[36:39], v[144:147], 0\n"
"s_waitcnt lgkmcnt(1)\n"
"v_mfma_f32_16x16x32_bf16 v[178:181], v[40:43], v[152:155], v[178:181]\n"
"ds_read_b128 v[160:163], v5 offset:20480\n"
"v_mul_f32 v20, s60, v20\n"
"v_mfma_f32_16x16x32_bf16 v[208:211], v[44:47], v[152:155], v[208:211]\n"
"v_mul_f32 v21, s60, v21\n"
"v_cvt_pk_bf16_f32 v20, v20, v21\n"
"v_mfma_f32_16x16x32_bf16 v[8:11], v[48:51], v[152:155], v[8:11]\n"
"s_waitcnt lgkmcnt(1)\n"
"v_bfe_u32 v239, v6, 2, 6\n"
"v_mul_f32 v22, s60, v22\n"
"v_mfma_f32_16x16x32_bf16 v[178:181], v[52:55], v[156:159], v[178:181]\n"
"ds_read_b128 v[12:15], v4 offset:22528\n"
"v_mul_f32 v23, s60, v23\n"
"v_mfma_f32_16x16x32_bf16 v[208:211], v[56:59], v[156:159], v[208:211]\n"
"s_add_u32 s71, s50, 4294964592\n"
"ds_read_b32 v239, v239 offset:1856\n"
"v_cvt_pk_bf16_f32 v21, v22, v23\n"
"v_mfma_f32_16x16x32_bf16 v[8:11], v[60:63], v[156:159], v[8:11]\n"
"v_mul_f32 v240, s60, v240\n"
"v_mul_f32 v241, s60, v241\n"
"v_mul_f32 v242, s60, v242\n"
"v_mul_f32 v243, s60, v243\n"
"v_cvt_pk_bf16_f32 v22, v240, v241\n"
"v_cvt_pk_bf16_f32 v23, v242, v243\n"
"v_mul_f32 v148, s60, v148\n"
"v_mul_f32 v149, s60, v149\n"
"s_waitcnt lgkmcnt(2)\n"
"v_mfma_f32_16x16x32_bf16 v[178:181], v[64:67], v[160:163], v[178:181]\n"
"ds_read_b128 v[140:143], v5 offset:22528\n"
"v_mul_f32 v150, s60, v150\n"
"v_mfma_f32_16x16x32_bf16 v[208:211], v[68:71], v[160:163], v[208:211]\n"
"v_mul_f32 v151, s60, v151\n"
"v_cvt_pk_bf16_f32 v26, v148, v149\n"
"v_mfma_f32_16x16x32_bf16 v[8:11], v[72:75], v[160:163], v[8:11]\n"
"s_add_u32 s72, s50, 4294965632\n"
"v_cvt_pk_bf16_f32 v27, v150, v151\n"
"s_waitcnt lgkmcnt(2)\n"
"v_mfma_f32_16x16x32_bf16 v[178:181], v[76:79], v[12:15], v[178:181]\n"
"s_waitcnt lgkmcnt(1)\n"
"v_add_u32_sdwa v238, v239, v7 dst_sel:DWORD dst_unused:UNUSED_PAD src0_sel:WORD_0 src1_sel:DWORD\n"
"v_mfma_f32_16x16x32_bf16 v[208:211], v[80:83], v[12:15], v[208:211]\n"
"v_max_u32 v213, s71, v238\n"
"v_mfma_f32_16x16x32_bf16 v[8:11], v[84:87], v[12:15], v[8:11]\n"
"s_waitcnt lgkmcnt(0)\n"
"ds_read_b128 v[240:243], v213 offset:2720\n"
"v_min_u32 v212, s72, v238\n"
"ds_read_b128 v[144:147], v213 offset:2736\n"
"v_mfma_f32_16x16x32_bf16 v[178:181], v[88:91], v[140:143], v[178:181]\n"
"v_mfma_f32_16x16x32_bf16 v[208:211], v[92:95], v[140:143], v[208:211]\n"
"ds_read_b128 v[148:151], v212 offset:4048\n"
"v_mfma_f32_16x16x32_bf16 v[8:11], v[96:99], v[140:143], v[8:11]\n"
"s_waitcnt lgkmcnt(2)\n"
"v_mfma_f32_16x16x32_bf16 v[240:243], v[190:193], v[20:23], v[240:243]\n"
"ds_read_b128 v[12:15], v212 offset:4064\n"
"v_mfma_f32_16x16x32_bf16 v[240:243], v[192:195], v[24:27], v[240:243]\n"
"s_add_u32 s73, s50, 4294964288\n"
"s_waitcnt lgkmcnt(2)\n"
"v_mfma_f32_16x16x32_bf16 v[144:147], v[196:199], v[20:23], v[144:147]\n"
"ds_read_b128 v[152:155], v212 offset:4080\n"
"v_cvt_pk_bf16_f32 v178, v178, v179\n"
"v_cvt_pk_bf16_f32 v179, v180, v181\n"
"v_cvt_pk_bf16_f32 v180, v208, v209\n"
"v_cvt_pk_bf16_f32 v181, v210, v211\n"
"v_cvt_pk_bf16_f32 v182, v8, v9\n"
"v_cvt_pk_bf16_f32 v183, v10, v11\n"
"v_mfma_f32_16x16x32_bf16 v[144:147], v[198:201], v[24:27], v[144:147]\n"
"s_waitcnt lgkmcnt(2)\n"
"v_max3_f32 v176, v240, v241, v242\n"
"v_max_f32 v176, v176, v243\n"
"v_mfma_f32_16x16x32_bf16 v[148:151], v[214:217], v[20:23], v[148:151]\n"
"ds_read_b128 v[208:211], v213 offset:2704\n"
"v_min_u32 v239, s73, v238\n"
"v_mfma_f32_16x16x32_bf16 v[148:151], v[216:219], v[24:27], v[148:151]\n"
"s_mov_b64 exec, s[58:59]\n"
"v_add_u32 v239, 1040, v239\n"
"s_mov_b64 exec, -1\n"
"v_max3_f32 v176, v176, v144, v145\n"
"v_max3_f32 v176, v176, v146, v147\n"
"s_waitcnt lgkmcnt(2)\n"
"v_mfma_f32_16x16x32_bf16 v[12:15], v[220:223], v[20:23], v[12:15]\n"
"ds_read_b128 v[240:243], v239 offset:5392\n"
"v_mfma_f32_16x16x32_bf16 v[12:15], v[222:225], v[24:27], v[12:15]\n"
"s_waitcnt lgkmcnt(2)\n"
"v_max3_f32 v176, v176, v148, v149\n"
"v_max3_f32 v176, v176, v150, v151\n"
"v_mfma_f32_16x16x32_bf16 v[152:155], v[226:229], v[20:23], v[152:155]\n"
"ds_read_b128 v[8:11], v239 offset:5408\n"
"v_mfma_f32_16x16x32_bf16 v[152:155], v[228:231], v[24:27], v[152:155]\n"
"s_waitcnt lgkmcnt(2)\n"
"s_nop 0\n"
"v_max3_f32 v176, v176, v12, v13\n"
"v_max3_f32 v176, v176, v14, v15\n"
"v_mfma_f32_16x16x32_bf16 v[208:211], v[184:187], v[20:23], v[208:211]\n"
"ds_read_b128 v[140:143], v239 offset:5424\n"
"v_mfma_f32_16x16x32_bf16 v[208:211], v[186:189], v[24:27], v[208:211]\n"
"s_waitcnt lgkmcnt(2)\n"
"v_max3_f32 v176, v176, v152, v153\n"
"v_max3_f32 v176, v176, v154, v155\n"
"v_mfma_f32_16x16x32_bf16 v[240:243], v[244:247], v[20:23], v[240:243]\n"
"ds_read_b128 v[12:15], v213 offset:2720\n"
"v_mfma_f32_16x16x32_bf16 v[240:243], v[246:249], v[24:27], v[240:243]\n"
"s_waitcnt lgkmcnt(2)\n"
"s_nop 0\n"
"v_max3_f32 v176, v176, v208, v209\n"
"v_max3_f32 v176, v176, v210, v211\n"
"v_mfma_f32_16x16x32_bf16 v[8:11], v[250:253], v[20:23], v[8:11]\n"
"ds_read_b128 v[144:147], v213 offset:2736\n"
"v_mfma_f32_16x16x32_bf16 v[8:11], v[252:255], v[24:27], v[8:11]\n"
"s_waitcnt lgkmcnt(2)\n"
"v_max3_f32 v176, v176, v240, v241\n"
"v_max3_f32 v176, v176, v242, v243\n"
"v_mfma_f32_16x16x32_bf16 v[140:143], v[178:181], v[20:23], v[140:143]\n"
"ds_read_b128 v[208:211], v212 offset:4048\n"
"v_mfma_f32_16x16x32_bf16 v[140:143], v[180:183], v[24:27], v[140:143]\n"
"s_waitcnt lgkmcnt(2)\n"
"s_nop 0\n"
"v_max3_f32 v176, v176, v8, v9\n"
"v_max3_f32 v176, v176, v10, v11\n"
"v_mfma_f32_16x16x32_bf16 v[12:15], v[190:193], v[20:23], v[12:15]\n"
"ds_read_b128 v[240:243], v212 offset:4064\n"
"v_mfma_f32_16x16x32_bf16 v[12:15], v[192:195], v[24:27], v[12:15]\n"
"s_nop 0\n"
"v_max3_f32 v176, v176, v140, v141\n"
"v_max3_f32 v176, v176, v142, v143\n"
"v_mov_b32 v177, v176\n"
"s_nop 1\n"
"v_permlane16_swap_b32 v176, v177\n"
"v_max_f32 v176, v176, v177\n"
"v_mov_b32 v177, v176\n"
"s_nop 1\n"
"v_permlane32_swap_b32 v176, v177\n"
"v_max_f32 v176, v176, v177\n"
"v_sub_f32 v12, v12, v176\n"
"v_exp_f32 v12, v12\n"
"v_sub_f32 v13, v13, v176\n"
"v_exp_f32 v13, v13\n"
"v_sub_f32 v14, v14, v176\n"
"v_exp_f32 v14, v14\n"
"v_sub_f32 v15, v15, v176\n"
"v_exp_f32 v15, v15\n"
"s_waitcnt lgkmcnt(2)\n"
"v_mfma_f32_16x16x32_bf16 v[144:147], v[196:199], v[20:23], v[144:147]\n"
"ds_read_b128 v[8:11], v212 offset:4080\n"
"v_cvt_pk_bf16_f32 v12, v12, v13\n"
"v_mfma_f32_16x16x32_bf16 v[144:147], v[198:201], v[24:27], v[144:147]\n"
"v_cvt_pk_bf16_f32 v13, v14, v15\n"
"ds_write_b64 v6, v[12:13] offset:53248\n"
"s_waitcnt lgkmcnt(3)\n"
"v_mfma_f32_16x16x32_bf16 v[208:211], v[214:217], v[20:23], v[208:211]\n"
"ds_read_b128 v[140:143], v213 offset:2704\n"
"v_mfma_f32_16x16x32_bf16 v[208:211], v[216:219], v[24:27], v[208:211]\n"
"s_nop 1\n"
"v_sub_f32 v144, v144, v176\n"
"v_exp_f32 v144, v144\n"
"v_sub_f32 v145, v145, v176\n"
"v_exp_f32 v145, v145\n"
"v_sub_f32 v146, v146, v176\n"
"v_exp_f32 v146, v146\n"
"v_sub_f32 v147, v147, v176\n"
"v_exp_f32 v147, v147\n"
"v_sub_f32 v208, v208, v176\n"
"v_exp_f32 v208, v208\n"
"v_sub_f32 v209, v209, v176\n"
"v_exp_f32 v209, v209\n"
"v_sub_f32 v210, v210, v176\n"
"v_exp_f32 v210, v210\n"
"v_sub_f32 v211, v211, v176\n"
"v_exp_f32 v211, v211\n"
"v_cvt_pk_bf16_f32 v144, v144, v145\n"
"v_cvt_pk_bf16_f32 v145, v146, v147\n"
"v_cvt_pk_bf16_f32 v208, v208, v209\n"
"v_cvt_pk_bf16_f32 v209, v210, v211\n"
"ds_write_b64 v6, v[144:145] offset:53256\n"
"s_waitcnt lgkmcnt(4)\n"
"v_mfma_f32_16x16x32_bf16 v[240:243], v[220:223], v[20:23], v[240:243]\n"
"ds_read_b128 v[12:15], v239 offset:5392\n"
"v_mfma_f32_16x16x32_bf16 v[240:243], v[222:225], v[24:27], v[240:243]\n"
"ds_write_b64 v6, v[208:209] offset:54272\n"
"s_waitcnt lgkmcnt(5)\n"
"v_mfma_f32_16x16x32_bf16 v[8:11], v[226:229], v[20:23], v[8:11]\n"
"ds_read_b128 v[210:213], v239 offset:5408\n"
"v_mfma_f32_16x16x32_bf16 v[8:11], v[228:231], v[24:27], v[8:11]\n"
"s_nop 2\n"
"v_sub_f32 v240, v240, v176\n"
"v_exp_f32 v240, v240\n"
"v_sub_f32 v241, v241, v176\n"
"v_exp_f32 v241, v241\n"
"v_sub_f32 v242, v242, v176\n"
"v_exp_f32 v242, v242\n"
"v_sub_f32 v243, v243, v176\n"
"v_exp_f32 v243, v243\n"
"v_sub_f32 v8, v8, v176\n"
"v_exp_f32 v8, v8\n"
"v_sub_f32 v9, v9, v176\n"
"v_exp_f32 v9, v9\n"
"v_cvt_pk_bf16_f32 v240, v240, v241\n"
"v_sub_f32 v10, v10, v176\n"
"v_exp_f32 v10, v10\n"
"v_cvt_pk_bf16_f32 v241, v242, v243\n"
"v_sub_f32 v11, v11, v176\n"
"v_exp_f32 v11, v11\n"
"v_cvt_pk_bf16_f32 v8, v8, v9\n"
"v_cvt_pk_bf16_f32 v9, v10, v11\n"
"ds_write_b64 v6, v[240:241] offset:54280\n"
"s_waitcnt lgkmcnt(5)\n"
"v_mfma_f32_16x16x32_bf16 v[140:143], v[184:187], v[20:23], v[140:143]\n"
"ds_write_b64 v6, v[8:9] offset:33280\n"
"v_mfma_f32_16x16x32_bf16 v[140:143], v[186:189], v[24:27], v[140:143]\n"
"s_waitcnt lgkmcnt(4)\n"
"ds_read_b128 v[144:147], v239 offset:5424\n"
"v_mfma_f32_16x16x32_bf16 v[12:15], v[244:247], v[20:23], v[12:15]\n"
"ds_read_b128 v[148:151], v4 offset:49152\n"
"v_mfma_f32_16x16x32_bf16 v[12:15], v[246:249], v[24:27], v[12:15]\n"
"s_waitcnt lgkmcnt(2)\n"
"s_barrier\n"
"s_nop 0\n"
"v_sub_f32 v140, v140, v176\n"
"v_exp_f32 v140, v140\n"
"v_sub_f32 v141, v141, v176\n"
"v_exp_f32 v141, v141\n"
"v_sub_f32 v142, v142, v176\n"
"v_exp_f32 v142, v142\n"
"v_sub_f32 v143, v143, v176\n"
"v_exp_f32 v143, v143\n"
"v_sub_f32 v12, v12, v176\n"
"v_exp_f32 v12, v12\n"
"v_cvt_pk_bf16_f32 v140, v140, v141\n"
"v_cvt_pk_bf16_f32 v141, v142, v143\n"
"v_sub_f32 v13, v13, v176\n"
"v_exp_f32 v13, v13\n"
"v_sub_f32 v14, v14, v176\n"
"v_exp_f32 v14, v14\n"
"v_sub_f32 v15, v15, v176\n"
"v_exp_f32 v15, v15\n"
"v_cvt_pk_bf16_f32 v12, v12, v13\n"
"v_cvt_pk_bf16_f32 v13, v14, v15\n"
"ds_write_b64 v6, v[140:141] offset:57352\n"
"v_mfma_f32_16x16x32_bf16 v[210:213], v[250:253], v[20:23], v[210:213]\n"
"ds_read_b128 v[184:187], v6 offset:4096\n"
"v_mfma_f32_16x16x32_bf16 v[210:213], v[252:255], v[24:27], v[210:213]\n"
"ds_write_b64 v6, v[12:13] offset:58368\n"
"s_waitcnt lgkmcnt(4)\n"
"v_mfma_f32_16x16x32_bf16 v[144:147], v[178:181], v[20:23], v[144:147]\n"
"ds_read_b128 v[238:241], v5 offset:49152\n"
"v_mfma_f32_16x16x32_bf16 v[144:147], v[180:183], v[24:27], v[144:147]\n"
"s_waitcnt lgkmcnt(4)\n"
"ds_read_b128 v[8:11], v6 offset:5120\n"
"v_mfma_f32_16x16x32_bf16 v[140:143], v[116:119], v[148:151], 0\n"
"ds_read_b128 v[152:155], v4 offset:51200\n"
"v_mfma_f32_16x16x32_bf16 v[12:15], v[120:123], v[148:151], 0\n"
"v_sub_f32 v210, v210, v176\n"
"v_exp_f32 v210, v210\n"
"v_sub_f32 v211, v211, v176\n"
"v_exp_f32 v211, v211\n"
"v_sub_f32 v212, v212, v176\n"
"v_exp_f32 v212, v212\n"
"s_waitcnt lgkmcnt(4)\n"
"v_mfma_f32_16x16x32_bf16 v[20:23], v[184:187], v[148:151], 0\n"
"ds_read_b128 v[156:159], v6 offset:6144\n"
"v_sub_f32 v213, v213, v176\n"
"v_exp_f32 v213, v213\n"
"v_cvt_pk_bf16_f32 v210, v210, v211\n"
"v_cvt_pk_bf16_f32 v211, v212, v213\n"
"s_waitcnt lgkmcnt(3)\n"
"v_mfma_f32_16x16x32_bf16 v[140:143], v[124:127], v[238:241], v[140:143]\n"
"ds_write_b64 v6, v[210:211] offset:58376\n"
"v_mfma_f32_16x16x32_bf16 v[12:15], v[128:131], v[238:241], v[12:15]\n"
"s_waitcnt lgkmcnt(3)\n"
"ds_read_b128 v[184:187], v5 offset:51200\n"
"v_mfma_f32_16x16x32_bf16 v[20:23], v[8:11], v[238:241], v[20:23]\n"
"s_waitcnt lgkmcnt(3)\n"
"ds_read_b128 v[148:151], v6 offset:7168\n"
"v_mfma_f32_16x16x32_bf16 v[140:143], v[0:3], v[152:155], v[140:143]\n"
"ds_read_b128 v[160:163], v4 offset:53248\n"
"v_sub_f32 v144, v144, v176\n"
"v_exp_f32 v144, v144\n"
"v_sub_f32 v145, v145, v176\n"
"v_exp_f32 v145, v145\n"
"v_mfma_f32_16x16x32_bf16 v[12:15], v[16:19], v[152:155], v[12:15]\n"
"s_waitcnt lgkmcnt(4)\n"
"v_sub_f32 v146, v146, v176\n"
"v_exp_f32 v146, v146\n"
"v_mfma_f32_16x16x32_bf16 v[20:23], v[156:159], v[152:155], v[20:23]\n"
"ds_read_b128 v[208:211], v6 offset:8192\n"
"v_sub_f32 v147, v147, v176\n"
"v_exp_f32 v147, v147\n"
"v_cvt_pk_bf16_f32 v144, v144, v145\n"
"v_cvt_pk_bf16_f32 v145, v146, v147\n"
"s_waitcnt lgkmcnt(3)\n"
"v_mfma_f32_16x16x32_bf16 v[140:143], v[132:135], v[184:187], v[140:143]\n"
"ds_write_b64 v6, v[144:145] offset:57344\n"
"v_mfma_f32_16x16x32_bf16 v[12:15], v[136:139], v[184:187], v[12:15]\n"
"s_waitcnt lgkmcnt(3)\n"
"ds_read_b128 v[238:241], v5 offset:53248\n"
"v_mfma_f32_16x16x32_bf16 v[20:23], v[148:151], v[184:187], v[20:23]\n"
"s_waitcnt lgkmcnt(3)\n"
"ds_read_b128 v[8:11], v6 offset:9216\n"
"v_mfma_f32_16x16x32_bf16 v[140:143], v[100:103], v[160:163], v[140:143]\n"
"ds_read_b128 v[152:155], v4 offset:30720\n"
"v_mfma_f32_16x16x32_bf16 v[12:15], v[104:107], v[160:163], v[12:15]\n"
"s_waitcnt lgkmcnt(3)\n"
"s_barrier\n"
"v_mfma_f32_16x16x32_bf16 v[20:23], v[208:211], v[160:163], v[20:23]\n"
"s_waitcnt lgkmcnt(2)\n"
"v_mfma_f32_16x16x32_bf16 v[140:143], v[108:111], v[238:241], v[140:143]\n"
"ds_read_b128 v[144:147], v5 offset:30720\n"
"v_mfma_f32_16x16x32_bf16 v[12:15], v[112:115], v[238:241], v[12:15]\n"
"s_waitcnt lgkmcnt(2)\n"
"v_mfma_f32_16x16x32_bf16 v[20:23], v[8:11], v[238:241], v[20:23]\n"
"s_waitcnt lgkmcnt(1)\n"
"v_mfma_f32_16x16x32_bf16 v[184:187], v[28:31], v[152:155], 0\n"
"ds_read_b128 v[148:151], v4 offset:32768\n"
"v_mfma_f32_16x16x32_bf16 v[208:211], v[32:35], v[152:155], 0\n"
"v_mfma_f32_16x16x32_bf16 v[156:159], v[36:39], v[152:155], 0\n"
"s_waitcnt lgkmcnt(1)\n"
"v_mfma_f32_16x16x32_bf16 v[184:187], v[40:43], v[144:147], v[184:187]\n"
"ds_read_b128 v[160:163], v5 offset:32768\n"
"v_mul_f32 v140, s60, v140\n"
"v_mfma_f32_16x16x32_bf16 v[208:211], v[44:47], v[144:147], v[208:211]\n"
"v_mul_f32 v141, s60, v141\n"
"v_cvt_pk_bf16_f32 v140, v140, v141\n"
"v_mfma_f32_16x16x32_bf16 v[156:159], v[48:51], v[144:147], v[156:159]\n"
"s_waitcnt lgkmcnt(1)\n"
"v_bfe_u32 v239, v6, 2, 6\n"
"v_mul_f32 v142, s60, v142\n"
"v_mfma_f32_16x16x32_bf16 v[184:187], v[52:55], v[148:151], v[184:187]\n"
"ds_read_b128 v[240:243], v4 offset:34816\n"
"v_mul_f32 v143, s60, v143\n"
"v_mfma_f32_16x16x32_bf16 v[208:211], v[56:59], v[148:151], v[208:211]\n"
"s_add_u32 s68, s50, 4294964576\n"
"ds_read_b32 v239, v239 offset:1920\n"
"v_cvt_pk_bf16_f32 v141, v142, v143\n"
"v_mfma_f32_16x16x32_bf16 v[156:159], v[60:63], v[148:151], v[156:159]\n"
"v_mul_f32 v12, s60, v12\n"
"v_mul_f32 v13, s60, v13\n"
"v_mul_f32 v14, s60, v14\n"
"v_mul_f32 v15, s60, v15\n"
"v_cvt_pk_bf16_f32 v142, v12, v13\n"
"v_cvt_pk_bf16_f32 v143, v14, v15\n"
"v_mul_f32 v20, s60, v20\n"
"v_mul_f32 v21, s60, v21\n"
"s_waitcnt lgkmcnt(2)\n"
"v_mfma_f32_16x16x32_bf16 v[184:187], v[64:67], v[160:163], v[184:187]\n"
"ds_read_b128 v[8:11], v5 offset:34816\n"
"v_mul_f32 v22, s60, v22\n"
"v_mfma_f32_16x16x32_bf16 v[208:211], v[68:71], v[160:163], v[208:211]\n"
"v_mul_f32 v23, s60, v23\n"
"v_cvt_pk_bf16_f32 v26, v20, v21\n"
"v_mfma_f32_16x16x32_bf16 v[156:159], v[72:75], v[160:163], v[156:159]\n"
"s_waitcnt lgkmcnt(2)\n"
"v_cvt_pk_bf16_f32 v27, v22, v23\n"
"v_mfma_f32_16x16x32_bf16 v[184:187], v[76:79], v[240:243], v[184:187]\n"
"s_waitcnt lgkmcnt(1)\n"
"v_add_u32_sdwa v238, v239, v7 dst_sel:DWORD dst_unused:UNUSED_PAD src0_sel:WORD_0 src1_sel:DWORD\n"
"v_mfma_f32_16x16x32_bf16 v[208:211], v[80:83], v[240:243], v[208:211]\n"
"s_add_u32 s69, s50, 4294963232\n"
"v_max_u32 v213, s68, v238\n"
"v_add_u32_sdwa v239, sext(v239), v238 dst_sel:DWORD dst_unused:UNUSED_PAD src0_sel:WORD_1 src1_sel:DWORD\n"
"v_mfma_f32_16x16x32_bf16 v[156:159], v[84:87], v[240:243], v[156:159]\n"
"s_waitcnt lgkmcnt(0)\n"
"ds_read_b128 v[12:15], v213 offset:2720\n"
"v_max_u32 v212, s68, v239\n"
"ds_read_b128 v[20:23], v213 offset:2736\n"
"v_mfma_f32_16x16x32_bf16 v[184:187], v[88:91], v[8:11], v[184:187]\n"
"s_add_u32 s70, s50, 4294964272\n"
"v_mfma_f32_16x16x32_bf16 v[208:211], v[92:95], v[8:11], v[208:211]\n"
"ds_read_b128 v[240:243], v212 offset:2752\n"
"v_max_u32 v177, s69, v239\n"
"v_mfma_f32_16x16x32_bf16 v[156:159], v[96:99], v[8:11], v[156:159]\n"
"v_min_u32 v176, s70, v238\n"
"v_min_u32 v175, s70, v239\n"
"s_mov_b64 exec, s[58:59]\n"
"v_add_u32 v176, 1040, v176\n"
"v_add_u32 v175, 1040, v175\n"
"s_mov_b64 exec, -1\n"
"v_cvt_pk_bf16_f32 v184, v184, v185\n"
"v_cvt_pk_bf16_f32 v185, v186, v187\n"
"v_cvt_pk_bf16_f32 v186, v208, v209\n"
"v_cvt_pk_bf16_f32 v187, v210, v211\n"
"v_cvt_pk_bf16_f32 v188, v156, v157\n"
"s_waitcnt lgkmcnt(2)\n"
"v_mfma_f32_16x16x32_bf16 v[12:15], v[190:193], v[140:143], v[12:15]\n"
"ds_read_b128 v[8:11], v177 offset:4096\n"
"v_cvt_pk_bf16_f32 v189, v158, v159\n"
"v_mfma_f32_16x16x32_bf16 v[12:15], v[192:195], v[24:27], v[12:15]\n"
"s_waitcnt lgkmcnt(2)\n"
"v_mfma_f32_16x16x32_bf16 v[20:23], v[196:199], v[140:143], v[20:23]\n"
"ds_read_b128 v[208:211], v176 offset:5408\n"
"v_max_u32 v174, s69, v238\n"
"v_mfma_f32_16x16x32_bf16 v[20:23], v[198:201], v[24:27], v[20:23]\n"
"s_waitcnt lgkmcnt(2)\n"
"s_nop 1\n"
"v_max3_f32 v172, v12, v13, v14\n"
"v_max_f32 v172, v172, v15\n"
"v_mfma_f32_16x16x32_bf16 v[240:243], v[202:205], v[140:143], v[240:243]\n"
"ds_read_b128 v[28:31], v174 offset:4064\n"
"v_mfma_f32_16x16x32_bf16 v[240:243], v[204:207], v[24:27], v[240:243]\n"
"s_waitcnt lgkmcnt(2)\n"
"v_max3_f32 v172, v172, v20, v21\n"
"v_max3_f32 v172, v172, v22, v23\n"
"v_mfma_f32_16x16x32_bf16 v[8:11], v[232:235], v[140:143], v[8:11]\n"
"ds_read_b128 v[12:15], v174 offset:4080\n"
"v_mfma_f32_16x16x32_bf16 v[8:11], v[234:237], v[24:27], v[8:11]\n"
"s_waitcnt lgkmcnt(2)\n"
"s_nop 0\n"
"v_max3_f32 v172, v172, v240, v241\n"
"v_max3_f32 v172, v172, v242, v243\n"
"v_mfma_f32_16x16x32_bf16 v[208:211], v[250:253], v[140:143], v[208:211]\n"
"ds_read_b128 v[20:23], v176 offset:5424\n"
"v_mfma_f32_16x16x32_bf16 v[208:211], v[252:255], v[24:27], v[208:211]\n"
"s_waitcnt lgkmcnt(2)\n"
"v_max3_f32 v172, v172, v8, v9\n"
"v_max3_f32 v172, v172, v10, v11\n"
"v_mfma_f32_16x16x32_bf16 v[28:31], v[220:223], v[140:143], v[28:31]\n"
"ds_read_b128 v[238:241], v175 offset:5440\n"
"v_mfma_f32_16x16x32_bf16 v[28:31], v[222:225], v[24:27], v[28:31]\n"
"s_waitcnt lgkmcnt(2)\n"
"s_nop 0\n"
"v_max3_f32 v172, v172, v208, v209\n"
"v_max3_f32 v172, v172, v210, v211\n"
"v_mfma_f32_16x16x32_bf16 v[12:15], v[226:229], v[140:143], v[12:15]\n"
"ds_read_b128 v[8:11], v213 offset:2720\n"
"v_mfma_f32_16x16x32_bf16 v[12:15], v[228:231], v[24:27], v[12:15]\n"
"s_waitcnt lgkmcnt(2)\n"
"v_max3_f32 v172, v172, v28, v29\n"
"v_max3_f32 v172, v172, v30, v31\n"
"v_mfma_f32_16x16x32_bf16 v[20:23], v[178:181], v[140:143], v[20:23]\n"
"ds_read_b128 v[208:211], v213 offset:2736\n"
"v_mfma_f32_16x16x32_bf16 v[20:23], v[180:183], v[24:27], v[20:23]\n"
"s_waitcnt lgkmcnt(2)\n"
"s_nop 0\n"
"v_max3_f32 v172, v172, v12, v13\n"
"v_max3_f32 v172, v172, v14, v15\n"
"v_mfma_f32_16x16x32_bf16 v[238:241], v[184:187], v[140:143], v[238:241]\n"
"ds_read_b128 v[28:31], v212 offset:2752\n"
"v_mfma_f32_16x16x32_bf16 v[238:241], v[186:189], v[24:27], v[238:241]\n"
"s_waitcnt lgkmcnt(2)\n"
"v_max3_f32 v172, v172, v20, v21\n"
"v_max3_f32 v172, v172, v22, v23\n"
"v_mfma_f32_16x16x32_bf16 v[8:11], v[190:193], v[140:143], v[8:11]\n"
"ds_read_b128 v[12:15], v177 offset:4096\n"
"v_mfma_f32_16x16x32_bf16 v[8:11], v[192:195], v[24:27], v[8:11]\n"
"s_nop 1\n"
"v_max3_f32 v172, v172, v238, v239\n"
"v_max3_f32 v172, v172, v240, v241\n"
"v_mov_b32 v173, v172\n"
"s_nop 1\n"
"v_permlane16_swap_b32 v172, v173\n"
"v_max_f32 v172, v172, v173\n"
"v_mov_b32 v173, v172\n"
"s_nop 1\n"
"v_permlane32_swap_b32 v172, v173\n"
"v_max_f32 v172, v172, v173\n"
"v_sub_f32 v10, v10, v172\n"
"v_exp_f32 v10, v10\n"
"v_sub_f32 v11, v11, v172\n"
"v_exp_f32 v11, v11\n"
"s_waitcnt lgkmcnt(2)\n"
"v_mfma_f32_16x16x32_bf16 v[208:211], v[196:199], v[140:143], v[208:211]\n"
"ds_read_b128 v[190:193], v176 offset:5408\n"
"v_mov_b32 v8, 0\n"
"v_mfma_f32_16x16x32_bf16 v[208:211], v[198:201], v[24:27], v[208:211]\n"
"v_cvt_pk_bf16_f32 v9, v10, v11\n"
"ds_write_b64 v6, v[8:9] offset:55296\n"
"s_waitcnt lgkmcnt(3)\n"
"v_mfma_f32_16x16x32_bf16 v[28:31], v[202:205], v[140:143], v[28:31]\n"
"ds_read_b128 v[194:197], v174 offset:4064\n"
"v_mfma_f32_16x16x32_bf16 v[28:31], v[204:207], v[24:27], v[28:31]\n"
"s_nop 1\n"
"v_sub_f32 v208, v208, v172\n"
"v_exp_f32 v208, v208\n"
"v_sub_f32 v209, v209, v172\n"
"v_exp_f32 v209, v209\n"
"v_sub_f32 v210, v210, v172\n"
"v_exp_f32 v210, v210\n"
"v_sub_f32 v211, v211, v172\n"
"v_exp_f32 v211, v211\n"
"v_sub_f32 v28, v28, v172\n"
"v_exp_f32 v28, v28\n"
"v_sub_f32 v29, v29, v172\n"
"v_exp_f32 v29, v29\n"
"v_cvt_pk_bf16_f32 v208, v208, v209\n"
"v_cvt_pk_bf16_f32 v209, v210, v211\n"
"v_sub_f32 v30, v30, v172\n"
"v_exp_f32 v30, v30\n"
"v_sub_f32 v31, v31, v172\n"
"v_exp_f32 v31, v31\n"
"v_cvt_pk_bf16_f32 v28, v28, v29\n"
"v_cvt_pk_bf16_f32 v29, v30, v31\n"
"ds_write_b64 v6, v[208:209] offset:55304\n"
"s_waitcnt lgkmcnt(4)\n"
"v_mfma_f32_16x16x32_bf16 v[12:15], v[232:235], v[140:143], v[12:15]\n"
"ds_read_b128 v[198:201], v174 offset:4080\n"
"v_mfma_f32_16x16x32_bf16 v[12:15], v[234:237], v[24:27], v[12:15]\n"
"ds_write_b64 v6, v[28:29] offset:56320\n"
"s_waitcnt lgkmcnt(5)\n"
"v_mfma_f32_16x16x32_bf16 v[190:193], v[250:253], v[140:143], v[190:193]\n"
"ds_read_b128 v[202:205], v176 offset:5424\n"
"v_mfma_f32_16x16x32_bf16 v[190:193], v[252:255], v[24:27], v[190:193]\n"
"s_nop 2\n"
"v_sub_f32 v12, v12, v172\n"
"v_exp_f32 v12, v12\n"
"v_sub_f32 v13, v13, v172\n"
"v_exp_f32 v13, v13\n"
"v_sub_f32 v14, v14, v172\n"
"v_exp_f32 v14, v14\n"
"v_sub_f32 v15, v15, v172\n"
"v_exp_f32 v15, v15\n"
"v_cvt_pk_bf16_f32 v12, v12, v13\n"
"v_sub_f32 v192, v192, v172\n"
"v_exp_f32 v192, v192\n"
"v_cvt_pk_bf16_f32 v13, v14, v15\n"
"v_sub_f32 v193, v193, v172\n"
"v_exp_f32 v193, v193\n"
"v_mov_b32 v190, 0\n"
"v_cvt_pk_bf16_f32 v191, v192, v193\n"
"ds_write_b64 v6, v[12:13] offset:56328\n"
"s_waitcnt lgkmcnt(5)\n"
"v_mfma_f32_16x16x32_bf16 v[194:197], v[220:223], v[140:143], v[194:197]\n"
"ds_write_b64 v6, v[190:191] offset:33288\n"
"v_mfma_f32_16x16x32_bf16 v[194:197], v[222:225], v[24:27], v[194:197]\n"
"s_waitcnt lgkmcnt(4)\n"
"ds_read_b128 v[206:209], v175 offset:5440\n"
"v_mfma_f32_16x16x32_bf16 v[198:201], v[226:229], v[140:143], v[198:201]\n"
"ds_read_b128 v[210:213], v4 offset:55296\n"
"v_mfma_f32_16x16x32_bf16 v[198:201], v[228:231], v[24:27], v[198:201]\n"
"s_waitcnt lgkmcnt(2)\n"
"s_barrier\n"
"s_nop 0\n"
"v_sub_f32 v196, v196, v172\n"
"v_exp_f32 v196, v196\n"
"v_sub_f32 v197, v197, v172\n"
"v_exp_f32 v197, v197\n"
"v_mov_b32 v194, 0\n"
"v_cvt_pk_bf16_f32 v195, v196, v197\n"
"ds_write_b64 v6, v[194:195] offset:58368\n"
"v_sub_f32 v198, v198, v172\n"
"v_exp_f32 v198, v198\n"
"v_sub_f32 v199, v199, v172\n"
"v_exp_f32 v199, v199\n"
"v_sub_f32 v200, v200, v172\n"
"v_exp_f32 v200, v200\n"
"v_mfma_f32_16x16x32_bf16 v[202:205], v[178:181], v[140:143], v[202:205]\n"
"ds_read_b128 v[190:193], v6 offset:4096\n"
"v_cvt_pk_bf16_f32 v198, v198, v199\n"
"v_sub_f32 v201, v201, v172\n"
"v_exp_f32 v201, v201\n"
"v_mfma_f32_16x16x32_bf16 v[202:205], v[180:183], v[24:27], v[202:205]\n"
"ds_read_b128 v[194:197], v5 offset:55296\n"
"v_cvt_pk_bf16_f32 v199, v200, v201\n"
"ds_write_b64 v6, v[198:199] offset:58376\n"
"s_waitcnt lgkmcnt(5)\n"
"v_mfma_f32_16x16x32_bf16 v[206:209], v[184:187], v[140:143], v[206:209]\n"
"v_mfma_f32_16x16x32_bf16 v[206:209], v[186:189], v[24:27], v[206:209]\n"
"s_waitcnt lgkmcnt(4)\n"
"ds_read_b128 v[232:235], v6 offset:5120\n"
"v_mfma_f32_16x16x32_bf16 v[236:239], v[116:119], v[210:213], 0\n"
"ds_read_b128 v[240:243], v4 offset:57344\n"
"v_sub_f32 v202, v202, v172\n"
"v_exp_f32 v202, v202\n"
"v_sub_f32 v203, v203, v172\n"
"v_exp_f32 v203, v203\n"
"v_mfma_f32_16x16x32_bf16 v[184:187], v[120:123], v[210:213], 0\n"
"s_waitcnt lgkmcnt(4)\n"
"v_sub_f32 v204, v204, v172\n"
"v_exp_f32 v204, v204\n"
"v_mfma_f32_16x16x32_bf16 v[198:201], v[190:193], v[210:213], 0\n"
"ds_read_b128 v[8:11], v6 offset:6144\n"
"v_cvt_pk_bf16_f32 v202, v202, v203\n"
"v_sub_f32 v205, v205, v172\n"
"v_exp_f32 v205, v205\n"
"s_waitcnt lgkmcnt(4)\n"
"v_mfma_f32_16x16x32_bf16 v[236:239], v[124:127], v[194:197], v[236:239]\n"
"ds_read_b128 v[12:15], v5 offset:57344\n"
"v_cvt_pk_bf16_f32 v203, v204, v205\n"
"v_mfma_f32_16x16x32_bf16 v[184:187], v[128:131], v[194:197], v[184:187]\n"
"ds_write_b64 v6, v[202:203] offset:57344\n"
"s_waitcnt lgkmcnt(4)\n"
"v_mfma_f32_16x16x32_bf16 v[198:201], v[232:235], v[194:197], v[198:201]\n"
"s_waitcnt lgkmcnt(3)\n"
"ds_read_b128 v[188:191], v6 offset:7168\n"
"v_bfe_u32 v213, v6, 2, 6\n"
"v_mfma_f32_16x16x32_bf16 v[236:239], v[0:3], v[240:243], v[236:239]\n"
"ds_read_b128 v[20:23], v4 offset:59392\n"
"v_sub_f32 v206, v206, v172\n"
"v_exp_f32 v206, v206\n"
"v_sub_f32 v207, v207, v172\n"
"v_exp_f32 v207, v207\n"
"v_mfma_f32_16x16x32_bf16 v[184:187], v[16:19], v[240:243], v[184:187]\n"
"s_add_u32 s71, s50, 4294963248\n"
"ds_read_b32 v213, v213 offset:1984\n"
"v_cvt_pk_bf16_f32 v206, v206, v207\n"
"v_sub_f32 v208, v208, v172\n"
"v_exp_f32 v208, v208\n"
"s_waitcnt lgkmcnt(5)\n"
"v_mfma_f32_16x16x32_bf16 v[198:201], v[8:11], v[240:243], v[198:201]\n"
"s_waitcnt lgkmcnt(4)\n"
"ds_read_b128 v[192:195], v6 offset:8192\n"
"v_sub_f32 v209, v209, v172\n"
"v_exp_f32 v209, v209\n"
"v_mfma_f32_16x16x32_bf16 v[236:239], v[132:135], v[12:15], v[236:239]\n"
"ds_read_b128 v[202:205], v5 offset:59392\n"
"v_cvt_pk_bf16_f32 v207, v208, v209\n"
"v_mfma_f32_16x16x32_bf16 v[184:187], v[136:139], v[12:15], v[184:187]\n"
"ds_write_b64 v6, v[206:207] offset:57352\n"
"s_waitcnt lgkmcnt(5)\n"
"v_mfma_f32_16x16x32_bf16 v[198:201], v[188:191], v[12:15], v[198:201]\n"
"s_add_u32 s72, s50, 4294964288\n"
"ds_read_b128 v[232:235], v6 offset:9216\n"
"s_waitcnt lgkmcnt(5)\n"
"v_mfma_f32_16x16x32_bf16 v[236:239], v[100:103], v[20:23], v[236:239]\n"
"s_waitcnt lgkmcnt(4)\n"
"v_add_u32_sdwa v212, v213, v7 dst_sel:DWORD dst_unused:UNUSED_PAD src0_sel:WORD_0 src1_sel:DWORD\n"
"v_mfma_f32_16x16x32_bf16 v[184:187], v[104:107], v[20:23], v[184:187]\n"
"s_waitcnt lgkmcnt(1)\n"
"v_max_u32 v7, s71, v212\n"
"v_min_u32 v177, s72, v212\n"
"s_barrier\n"
"v_mfma_f32_16x16x32_bf16 v[198:201], v[192:195], v[20:23], v[198:201]\n"
"s_mov_b64 exec, s[58:59]\n"
"v_add_u32 v177, 1040, v177\n"
"s_mov_b64 exec, -1\n"
"ds_read_b128 v[188:191], v7 offset:4048\n"
"v_mfma_f32_16x16x32_bf16 v[236:239], v[108:111], v[202:205], v[236:239]\n"
"ds_read_b128 v[206:209], v7 offset:4064\n"
"v_mfma_f32_16x16x32_bf16 v[184:187], v[112:115], v[202:205], v[184:187]\n"
"s_waitcnt lgkmcnt(2)\n"
"ds_read_b128 v[192:195], v177 offset:5392\n"
"v_mfma_f32_16x16x32_bf16 v[198:201], v[232:235], v[202:205], v[198:201]\n"
"s_nop 2\n"
"v_mul_f32 v236, s60, v236\n"
"v_mul_f32 v237, s60, v237\n"
"v_cvt_pk_bf16_f32 v236, v236, v237\n"
"v_mul_f32 v238, s60, v238\n"
"v_mul_f32 v239, s60, v239\n"
"v_cvt_pk_bf16_f32 v237, v238, v239\n"
"v_mul_f32 v184, s60, v184\n"
"v_mul_f32 v185, s60, v185\n"
"v_mul_f32 v186, s60, v186\n"
"v_mul_f32 v187, s60, v187\n"
"v_cvt_pk_bf16_f32 v238, v184, v185\n"
"v_cvt_pk_bf16_f32 v239, v186, v187\n"
"v_mul_f32 v198, s60, v198\n"
"v_mul_f32 v199, s60, v199\n"
"s_waitcnt lgkmcnt(2)\n"
"v_mfma_f32_16x16x32_bf16 v[188:191], v[214:217], v[236:239], v[188:191]\n"
"ds_read_b128 v[202:205], v7 offset:4080\n"
"v_mul_f32 v200, s60, v200\n"
"v_mul_f32 v201, s60, v201\n"
"v_cvt_pk_bf16_f32 v26, v198, v199\n"
"v_cvt_pk_bf16_f32 v27, v200, v201\n"
"s_nop 1\n"
"v_mfma_f32_16x16x32_bf16 v[188:191], v[216:219], v[24:27], v[188:191]\n"
"s_waitcnt lgkmcnt(2)\n"
"v_mfma_f32_16x16x32_bf16 v[206:209], v[220:223], v[236:239], v[206:209]\n"
"ds_read_b128 v[184:187], v177 offset:5408\n"
"v_mfma_f32_16x16x32_bf16 v[206:209], v[222:225], v[24:27], v[206:209]\n"
"s_waitcnt lgkmcnt(2)\n"
"s_nop 2\n"
"v_max3_f32 v4, v188, v189, v190\n"
"v_max_f32 v4, v4, v191\n"
"v_mfma_f32_16x16x32_bf16 v[192:195], v[244:247], v[236:239], v[192:195]\n"
"ds_read_b128 v[196:199], v177 offset:5424\n"
"v_mfma_f32_16x16x32_bf16 v[192:195], v[246:249], v[24:27], v[192:195]\n"
"s_waitcnt lgkmcnt(2)\n"
"v_max3_f32 v4, v4, v206, v207\n"
"v_max3_f32 v4, v4, v208, v209\n"
"v_mfma_f32_16x16x32_bf16 v[202:205], v[226:229], v[236:239], v[202:205]\n"
"ds_read_b128 v[188:191], v7 offset:4048\n"
"v_mfma_f32_16x16x32_bf16 v[202:205], v[228:231], v[24:27], v[202:205]\n"
"s_waitcnt lgkmcnt(2)\n"
"s_nop 0\n"
"v_max3_f32 v4, v4, v192, v193\n"
"v_max3_f32 v4, v4, v194, v195\n"
"v_mfma_f32_16x16x32_bf16 v[184:187], v[250:253], v[236:239], v[184:187]\n"
"ds_read_b128 v[206:209], v7 offset:4064\n"
"v_mfma_f32_16x16x32_bf16 v[184:187], v[252:255], v[24:27], v[184:187]\n"
"s_waitcnt lgkmcnt(2)\n"
"v_max3_f32 v4, v4, v202, v203\n"
"v_max3_f32 v4, v4, v204, v205\n"
"v_mfma_f32_16x16x32_bf16 v[196:199], v[178:181], v[236:239], v[196:199]\n"
"ds_read_b128 v[192:195], v177 offset:5392\n"
"v_mfma_f32_16x16x32_bf16 v[196:199], v[180:183], v[24:27], v[196:199]\n"
"s_waitcnt lgkmcnt(2)\n"
"s_nop 0\n"
"v_max3_f32 v4, v4, v184, v185\n"
"v_max3_f32 v4, v4, v186, v187\n"
"v_mfma_f32_16x16x32_bf16 v[188:191], v[214:217], v[236:239], v[188:191]\n"
"ds_read_b128 v[200:203], v7 offset:4080\n"
"v_mfma_f32_16x16x32_bf16 v[188:191], v[216:219], v[24:27], v[188:191]\n"
"s_nop 0\n"
"v_max3_f32 v4, v4, v196, v197\n"
"v_max3_f32 v4, v4, v198, v199\n"
"v_mov_b32 v5, v4\n"
"s_nop 1\n"
"v_permlane16_swap_b32 v4, v5\n"
"v_max_f32 v4, v4, v5\n"
"v_mov_b32 v5, v4\n"
"s_nop 1\n"
"v_permlane32_swap_b32 v4, v5\n"
"v_max_f32 v4, v4, v5\n"
"v_sub_f32 v188, v188, v4\n"
"v_exp_f32 v188, v188\n"
"v_sub_f32 v189, v189, v4\n"
"v_exp_f32 v189, v189\n"
"v_sub_f32 v190, v190, v4\n"
"v_exp_f32 v190, v190\n"
"v_sub_f32 v191, v191, v4\n"
"v_exp_f32 v191, v191\n"
"s_waitcnt lgkmcnt(2)\n"
"v_mfma_f32_16x16x32_bf16 v[206:209], v[220:223], v[236:239], v[206:209]\n"
"ds_read_b128 v[184:187], v177 offset:5408\n"
"v_cvt_pk_bf16_f32 v188, v188, v189\n"
"v_mfma_f32_16x16x32_bf16 v[206:209], v[222:225], v[24:27], v[206:209]\n"
"v_cvt_pk_bf16_f32 v189, v190, v191\n"
"ds_write_b64 v6, v[188:189] offset:53248\n"
"s_waitcnt lgkmcnt(3)\n"
"v_mfma_f32_16x16x32_bf16 v[192:195], v[244:247], v[236:239], v[192:195]\n"
"ds_read_b128 v[196:199], v177 offset:5424\n"
"v_mfma_f32_16x16x32_bf16 v[192:195], v[246:249], v[24:27], v[192:195]\n"
"s_nop 1\n"
"v_sub_f32 v206, v206, v4\n"
"v_exp_f32 v206, v206\n"
"v_sub_f32 v207, v207, v4\n"
"v_exp_f32 v207, v207\n"
"v_sub_f32 v208, v208, v4\n"
"v_exp_f32 v208, v208\n"
"v_sub_f32 v209, v209, v4\n"
"v_exp_f32 v209, v209\n"
"v_sub_f32 v192, v192, v4\n"
"v_exp_f32 v192, v192\n"
"v_sub_f32 v193, v193, v4\n"
"v_exp_f32 v193, v193\n"
"v_cvt_pk_bf16_f32 v206, v206, v207\n"
"v_sub_f32 v194, v194, v4\n"
"v_exp_f32 v194, v194\n"
"v_cvt_pk_bf16_f32 v207, v208, v209\n"
"v_sub_f32 v195, v195, v4\n"
"v_exp_f32 v195, v195\n"
"v_cvt_pk_bf16_f32 v192, v192, v193\n"
"v_cvt_pk_bf16_f32 v193, v194, v195\n"
"ds_write_b64 v6, v[206:207] offset:53256\n"
"s_waitcnt lgkmcnt(4)\n"
"v_mfma_f32_16x16x32_bf16 v[200:203], v[226:229], v[236:239], v[200:203]\n"
"ds_write_b64 v6, v[192:193] offset:54272\n"
"v_mfma_f32_16x16x32_bf16 v[200:203], v[228:231], v[24:27], v[200:203]\n"
"s_waitcnt lgkmcnt(4)\n"
"v_mfma_f32_16x16x32_bf16 v[184:187], v[250:253], v[236:239], v[184:187]\n"
"v_mfma_f32_16x16x32_bf16 v[184:187], v[252:255], v[24:27], v[184:187]\n"
"s_waitcnt lgkmcnt(0)\n"
"s_barrier\n"
"s_nop 2\n"
"v_sub_f32 v200, v200, v4\n"
"v_exp_f32 v200, v200\n"
"v_sub_f32 v201, v201, v4\n"
"v_exp_f32 v201, v201\n"
"v_sub_f32 v202, v202, v4\n"
"v_exp_f32 v202, v202\n"
"v_sub_f32 v203, v203, v4\n"
"v_exp_f32 v203, v203\n"
"v_cvt_pk_bf16_f32 v200, v200, v201\n"
"v_cvt_pk_bf16_f32 v201, v202, v203\n"
"v_sub_f32 v184, v184, v4\n"
"v_exp_f32 v184, v184\n"
"v_sub_f32 v185, v185, v4\n"
"v_exp_f32 v185, v185\n"
"v_sub_f32 v186, v186, v4\n"
"v_exp_f32 v186, v186\n"
"v_sub_f32 v187, v187, v4\n"
"v_exp_f32 v187, v187\n"
"v_cvt_pk_bf16_f32 v184, v184, v185\n"
"v_cvt_pk_bf16_f32 v185, v186, v187\n"
"ds_write_b64 v6, v[200:201] offset:57344\n"
"v_mfma_f32_16x16x32_bf16 v[196:199], v[178:181], v[236:239], v[196:199]\n"
"ds_write_b64 v6, v[184:185] offset:54280\n"
"v_mfma_f32_16x16x32_bf16 v[196:199], v[180:183], v[24:27], v[196:199]\n"
"s_nop 7\n"
"v_sub_f32 v196, v196, v4\n"
"v_exp_f32 v196, v196\n"
"v_sub_f32 v197, v197, v4\n"
"v_exp_f32 v197, v197\n"
"s_nop 0\n"
"v_cvt_pk_bf16_f32 v196, v196, v197\n"
"v_sub_f32 v198, v198, v4\n"
"v_exp_f32 v198, v198\n"
"v_sub_f32 v199, v199, v4\n"
"v_exp_f32 v199, v199\n"
"s_nop 0\n"
"v_cvt_pk_bf16_f32 v197, v198, v199\n"
"ds_write_b64 v6, v[196:197] offset:57352\n"
"s_waitcnt lgkmcnt(0)\n"
"s_barrier\n"
"s_barrier\n"
"s_barrier\n"
"s_barrier\n"
  );
}
DI void waveB_safe(const uint16_t* x, const uint16_t* wqkv, const uint16_t* wo, uint16_t* out, const Geo& G, int h) {
  B_HEAD;
  XDIR(0, 0, 0, 1, 102400); SB;
  XDIR(1, 0, 1, 1, 114688); SB;
  XDIR(0, 2, 2, 0, 126976); SB;
  RAWB(); B_SETUP(); SB;
  ASM_B(
"s_setprio 0\n"
"s_movk_i32 s40, 768\n"
"s_mov_b64 s[62:63], 0\n"
"s_movk_i32 s64, 0x207\n"
"s_add_u32 s58, s56, 128\n"
"s_addc_u32 s59, s57, 0\n"
"s_add_u32 s60, s56, 256\n"
"s_addc_u32 s61, s57, 0\n"
"s_waitcnt vmcnt(0)\n"
"s_barrier\n"
"s_add_u32 s42, s46, 768\n"
"s_addc_u32 s43, s47, 0\n"
"s_add_u32 m0, s50, 0\n"
"s_nop 0\n"
"global_load_lds_dwordx4 v0, s[42:43]\n"
"s_add_u32 m0, s50, 1024\n"
"s_nop 0\n"
"global_load_lds_dwordx4 v1, s[42:43]\n"
"s_add_u32 m0, s50, 2048\n"
"s_nop 0\n"
"global_load_lds_dwordx4 v2, s[42:43]\n"
"s_add_u32 s42, s46, 37632\n"
"s_addc_u32 s43, s47, 0\n"
"s_add_u32 m0, s50, 3072\n"
"s_nop 0\n"
"global_load_lds_dwordx4 v0, s[42:43]\n"
"s_add_u32 m0, s50, 4096\n"
"s_nop 0\n"
"global_load_lds_dwordx4 v1, s[42:43]\n"
"s_add_u32 m0, s50, 5120\n"
"s_nop 0\n"
"global_load_lds_dwordx4 v2, s[42:43]\n"
"s_add_u32 s42, s46, 74496\n"
"s_addc_u32 s43, s47, 0\n"
"s_add_u32 m0, s50, 24576\n"
"s_nop 0\n"
"global_load_lds_dwordx4 v0, s[42:43]\n"
"s_add_u32 m0, s50, 25600\n"
"s_nop 0\n"
"global_load_lds_dwordx4 v1, s[42:43]\n"
"s_add_u32 m0, s50, 26624\n"
"s_nop 0\n"
"global_load_lds_dwordx4 v2, s[42:43]\n"
"s_add_u32 s42, s46, 111360\n"
"s_addc_u32 s43, s47, 0\n"
"s_add_u32 m0, s50, 27648\n"
"s_nop 0\n"
"global_load_lds_dwordx4 v0, s[42:43]\n"
"s_add_u32 m0, s50, 28672\n"
"s_nop 0\n"
"global_load_lds_dwordx4 v1, s[42:43]\n"
"s_add_u32 m0, s50, 29696\n"
"s_nop 0\n"
"global_load_lds_dwordx4 v2, s[42:43]\n"
"s_barrier\n"
"s_add_u32 s42, s46, 148224\n"
"s_addc_u32 s43, s47, 0\n"
"s_add_u32 m0, s50, 4294942720\n"
"s_nop 0\n"
"global_load_lds_dwordx4 v0, s[42:43]\n"
"s_add_u32 m0, s50, 4294943744\n"
"s_nop 0\n"
"global_load_lds_dwordx4 v1, s[42:43]\n"
"s_add_u32 m0, s50, 4294944768\n"
"s_nop 0\n"
"global_load_lds_dwordx4 v2, s[42:43]\n"
"s_add_u32 s42, s46, 185088\n"
"s_addc_u32 s43, s47, 0\n"
"s_add_u32 m0, s50, 4294945792\n"
"s_nop 0\n"
"global_load_lds_dwordx4 v0, s[42:43]\n"
"s_add_u32 m0, s50, 4294946816\n"
"s_nop 0\n"
"global_load_lds_dwordx4 v1, s[42:43]\n"
"s_add_u32 m0, s50, 4294947840\n"
"s_nop 0\n"
"global_load_lds_dwordx4 v2, s[42:43]\n"
"s_waitcnt vmcnt(15)\n"
"ds_read_b64_tr_b16 v[20:21], v10 offset:28736\n"
"ds_read_b64_tr_b16 v[22:23], v10 offset:29504\n"
"s_andn2_b32 s41, 576, s55\n"
"v_add_u32 v9, s41, v15\n"
"ds_read_u16 v9, v9 offset:224\n"
"ds_read_b64_tr_b16 v[24:25], v10 offset:28672\n"
"ds_read_b64_tr_b16 v[26:27], v10 offset:29440\n"
"ds_read_b128 v[178:181], v12 offset:0\n"
"ds_read_b64_tr_b16 v[28:29], v10 offset:28704\n"
"ds_read_b64_tr_b16 v[30:31], v10 offset:29472\n"
"ds_read_b128 v[182:185], v12 offset:6144\n"
"ds_read_b128 v[186:189], v12 offset:12288\n"
"ds_read_b128 v[190:193], v13 offset:0\n"
"s_waitcnt lgkmcnt(8)\n"
"v_mad_u32_u24 v9, v9, s40, v16\n"
"s_add_u32 m0, s51, 139264\n"
"s_nop 0\n"
"global_load_lds_dwordx4 v9, s[56:57]\n"
"s_add_u32 m0, s51, 141312\n"
"s_nop 0\n"
"global_load_lds_dwordx4 v9, s[58:59]\n"
"s_add_u32 m0, s51, 143360\n"
"s_nop 0\n"
"global_load_lds_dwordx4 v9, s[60:61]\n"
"s_waitcnt vmcnt(15)\n"
"ds_read_b64_tr_b16 v[32:33], v10 offset:31808\n"
"ds_read_b64_tr_b16 v[34:35], v10 offset:32576\n"
"s_and_b32 s41, 32, s55\n"
"v_add_u32 v9, s41, v15\n"
"ds_read_u16 v9, v9 offset:352\n"
"ds_read_b64_tr_b16 v[36:37], v10 offset:31744\n"
"ds_read_b64_tr_b16 v[38:39], v10 offset:32512\n"
"ds_read_b64_tr_b16 v[40:41], v10 offset:31776\n"
"ds_read_b64_tr_b16 v[42:43], v10 offset:32544\n"
"s_waitcnt lgkmcnt(4)\n"
"v_mad_u32_u24 v9, v9, s40, v16\n"
"s_add_u32 m0, s51, 151552\n"
"s_nop 0\n"
"global_load_lds_dwordx4 v9, s[56:57]\n"
"s_add_u32 m0, s51, 153600\n"
"s_nop 0\n"
"global_load_lds_dwordx4 v9, s[58:59]\n"
"s_add_u32 m0, s51, 155648\n"
"s_nop 0\n"
"global_load_lds_dwordx4 v9, s[60:61]\n"
"v_mfma_f32_16x16x32_bf16 v[194:197], v[178:181], v[24:27], 0\n"
"s_waitcnt vmcnt(15)\n"
"ds_read_b64_tr_b16 v[44:45], v10 offset:53248\n"
"ds_read_b64_tr_b16 v[46:47], v10 offset:54016\n"
"v_mfma_f32_16x16x32_bf16 v[198:201], v[178:181], v[28:31], 0\n"
"ds_read_b128 v[202:205], v13 offset:6144\n"
"v_mfma_f32_16x16x32_bf16 v[206:209], v[178:181], v[20:23], 0\n"
"ds_read_b64_tr_b16 v[48:49], v10 offset:53280\n"
"ds_read_b64_tr_b16 v[50:51], v10 offset:54048\n"
"v_mfma_f32_16x16x32_bf16 v[178:181], v[182:185], v[24:27], 0\n"
"ds_read_b64_tr_b16 v[52:53], v10 offset:53312\n"
"ds_read_b64_tr_b16 v[54:55], v10 offset:54080\n"
"v_mfma_f32_16x16x32_bf16 v[214:217], v[182:185], v[28:31], 0\n"
"ds_read_b128 v[210:213], v13 offset:12288\n"
"v_mfma_f32_16x16x32_bf16 v[218:221], v[182:185], v[20:23], 0\n"
"v_mfma_f32_16x16x32_bf16 v[222:225], v[186:189], v[24:27], 0\n"
"v_mfma_f32_16x16x32_bf16 v[226:229], v[186:189], v[28:31], 0\n"
"ds_read_b128 v[182:185], v12 offset:2048\n"
"v_mfma_f32_16x16x32_bf16 v[230:233], v[186:189], v[20:23], 0\n"
"s_waitcnt lgkmcnt(11)\n"
"v_mfma_f32_16x16x32_bf16 v[194:197], v[190:193], v[36:39], v[194:197]\n"
"s_waitcnt lgkmcnt(9)\n"
"v_mfma_f32_16x16x32_bf16 v[198:201], v[190:193], v[40:43], v[198:201]\n"
"ds_read_b128 v[186:189], v12 offset:8192\n"
"v_mfma_f32_16x16x32_bf16 v[206:209], v[190:193], v[32:35], v[206:209]\n"
"s_waitcnt lgkmcnt(7)\n"
"v_mfma_f32_16x16x32_bf16 v[178:181], v[202:205], v[36:39], v[178:181]\n"
"v_mfma_f32_16x16x32_bf16 v[214:217], v[202:205], v[40:43], v[214:217]\n"
"ds_read_b128 v[190:193], v12 offset:14336\n"
"v_mfma_f32_16x16x32_bf16 v[218:221], v[202:205], v[32:35], v[218:221]\n"
"s_waitcnt lgkmcnt(3)\n"
"v_mfma_f32_16x16x32_bf16 v[222:225], v[210:213], v[36:39], v[222:225]\n"
"s_waitcnt vmcnt(12)\n"
"ds_read_b64_tr_b16 v[56:57], v10 offset:56320\n"
"ds_read_b64_tr_b16 v[58:59], v10 offset:57088\n"
"v_mfma_f32_16x16x32_bf16 v[226:229], v[210:213], v[40:43], v[226:229]\n"
"ds_read_b128 v[202:205], v13 offset:2048\n"
"v_mfma_f32_16x16x32_bf16 v[230:233], v[210:213], v[32:35], v[230:233]\n"
"s_waitcnt lgkmcnt(5)\n"
"ds_read_b64_tr_b16 v[60:61], v10 offset:56352\n"
"ds_read_b64_tr_b16 v[62:63], v10 offset:57120\n"
"v_mfma_f32_16x16x32_bf16 v[194:197], v[182:185], v[44:47], v[194:197]\n"
"ds_read_b64_tr_b16 v[64:65], v10 offset:56384\n"
"ds_read_b64_tr_b16 v[66:67], v10 offset:57152\n"
"v_mfma_f32_16x16x32_bf16 v[198:201], v[182:185], v[48:51], v[198:201]\n"
"ds_read_b128 v[210:213], v13 offset:8192\n"
"v_mfma_f32_16x16x32_bf16 v[206:209], v[182:185], v[52:55], v[206:209]\n"
"s_waitcnt lgkmcnt(9)\n"
"v_mfma_f32_16x16x32_bf16 v[178:181], v[186:189], v[44:47], v[178:181]\n"
"v_mfma_f32_16x16x32_bf16 v[214:217], v[186:189], v[48:51], v[214:217]\n"
"ds_read_b128 v[182:185], v13 offset:14336\n"
"v_mfma_f32_16x16x32_bf16 v[218:221], v[186:189], v[52:55], v[218:221]\n"
"s_waitcnt lgkmcnt(9)\n"
"v_mfma_f32_16x16x32_bf16 v[222:225], v[190:193], v[44:47], v[222:225]\n"
"s_waitcnt vmcnt(9)\n"
"ds_read_b64_tr_b16 v[68:69], v10 offset:4096\n"
"ds_read_b64_tr_b16 v[70:71], v10 offset:4864\n"
"v_mfma_f32_16x16x32_bf16 v[226:229], v[190:193], v[48:51], v[226:229]\n"
"s_waitcnt lgkmcnt(4)\n"
"ds_read_b128 v[186:189], v12 offset:4096\n"
"s_barrier\n"
"v_mfma_f32_16x16x32_bf16 v[230:233], v[190:193], v[52:55], v[230:233]\n"
"ds_read_b64_tr_b16 v[72:73], v10 offset:4128\n"
"ds_read_b64_tr_b16 v[74:75], v10 offset:4896\n"
"v_mfma_f32_16x16x32_bf16 v[194:197], v[202:205], v[56:59], v[194:197]\n"
"ds_read_b64_tr_b16 v[76:77], v10 offset:4160\n"
"ds_read_b64_tr_b16 v[78:79], v10 offset:4928\n"
"v_mfma_f32_16x16x32_bf16 v[198:201], v[202:205], v[60:63], v[198:201]\n"
"ds_read_b128 v[190:193], v12 offset:10240\n"
"v_mfma_f32_16x16x32_bf16 v[206:209], v[202:205], v[64:67], v[206:209]\n"
"s_waitcnt lgkmcnt(9)\n"
"v_mfma_f32_16x16x32_bf16 v[178:181], v[210:213], v[56:59], v[178:181]\n"
"v_mfma_f32_16x16x32_bf16 v[214:217], v[210:213], v[60:63], v[214:217]\n"
"ds_read_b128 v[202:205], v12 offset:16384\n"
"v_mfma_f32_16x16x32_bf16 v[218:221], v[210:213], v[64:67], v[218:221]\n"
"s_waitcnt lgkmcnt(9)\n"
"v_mfma_f32_16x16x32_bf16 v[222:225], v[182:185], v[56:59], v[222:225]\n"
"s_waitcnt vmcnt(6)\n"
"ds_read_b64_tr_b16 v[80:81], v10 offset:7168\n"
"ds_read_b64_tr_b16 v[82:83], v10 offset:7936\n"
"v_mfma_f32_16x16x32_bf16 v[226:229], v[182:185], v[60:63], v[226:229]\n"
"ds_read_b128 v[210:213], v13 offset:4096\n"
"v_mfma_f32_16x16x32_bf16 v[230:233], v[182:185], v[64:67], v[230:233]\n"
"s_waitcnt lgkmcnt(9)\n"
"ds_read_b64_tr_b16 v[84:85], v10 offset:7200\n"
"ds_read_b64_tr_b16 v[86:87], v10 offset:7968\n"
"v_mfma_f32_16x16x32_bf16 v[194:197], v[186:189], v[68:71], v[194:197]\n"
"s_waitcnt lgkmcnt(9)\n"
"ds_read_b64_tr_b16 v[88:89], v10 offset:7232\n"
"ds_read_b64_tr_b16 v[90:91], v10 offset:8000\n"
"v_mfma_f32_16x16x32_bf16 v[198:201], v[186:189], v[72:75], v[198:201]\n"
"s_waitcnt lgkmcnt(9)\n"
"ds_read_b128 v[182:185], v13 offset:10240\n"
"v_mfma_f32_16x16x32_bf16 v[206:209], v[186:189], v[76:79], v[206:209]\n"
"s_waitcnt lgkmcnt(9)\n"
"v_mfma_f32_16x16x32_bf16 v[178:181], v[190:193], v[68:71], v[178:181]\n"
"v_mfma_f32_16x16x32_bf16 v[214:217], v[190:193], v[72:75], v[214:217]\n"
"ds_read_b128 v[186:189], v13 offset:16384\n"
"v_mfma_f32_16x16x32_bf16 v[218:221], v[190:193], v[76:79], v[218:221]\n"
"s_waitcnt lgkmcnt(9)\n"
"v_mfma_f32_16x16x32_bf16 v[222:225], v[202:205], v[68:71], v[222:225]\n"
"v_mfma_f32_16x16x32_bf16 v[226:229], v[202:205], v[72:75], v[226:229]\n"
"ds_read_b128 v[190:193], v12 offset:18432\n"
"v_mfma_f32_16x16x32_bf16 v[230:233], v[202:205], v[76:79], v[230:233]\n"
"s_waitcnt lgkmcnt(7)\n"
"v_mfma_f32_16x16x32_bf16 v[194:197], v[210:213], v[80:83], v[194:197]\n"
"s_waitcnt lgkmcnt(5)\n"
"v_mfma_f32_16x16x32_bf16 v[198:201], v[210:213], v[84:87], v[198:201]\n"
"s_waitcnt lgkmcnt(3)\n"
"ds_read_b128 v[202:205], v13 offset:18432\n"
"v_mfma_f32_16x16x32_bf16 v[206:209], v[210:213], v[88:91], v[206:209]\n"
"s_waitcnt lgkmcnt(3)\n"
"v_mfma_f32_16x16x32_bf16 v[178:181], v[182:185], v[80:83], v[178:181]\n"
"v_mfma_f32_16x16x32_bf16 v[214:217], v[182:185], v[84:87], v[214:217]\n"
"ds_read_b128 v[240:243], v12 offset:20480\n"
"v_mfma_f32_16x16x32_bf16 v[218:221], v[182:185], v[88:91], v[218:221]\n"
"s_waitcnt lgkmcnt(3)\n"
"v_mfma_f32_16x16x32_bf16 v[222:225], v[186:189], v[80:83], v[222:225]\n"
"v_cvt_pk_bf16_f32 v184, v194, v195\n"
"v_cvt_pk_bf16_f32 v185, v196, v197\n"
"v_cvt_pk_bf16_f32 v210, v198, v199\n"
"v_cvt_pk_bf16_f32 v211, v200, v201\n"
"v_mfma_f32_16x16x32_bf16 v[226:229], v[186:189], v[84:87], v[226:229]\n"
"ds_read_b128 v[196:199], v13 offset:20480\n"
"v_cvt_pk_bf16_f32 v236, v206, v207\n"
"v_cvt_pk_bf16_f32 v237, v208, v209\n"
"v_mfma_f32_16x16x32_bf16 v[230:233], v[186:189], v[88:91], v[230:233]\n"
"s_waitcnt lgkmcnt(3)\n"
"v_cvt_pk_bf16_f32 v186, v178, v179\n"
"v_cvt_pk_bf16_f32 v187, v180, v181\n"
"v_mfma_f32_16x16x32_bf16 v[178:181], v[190:193], v[24:27], 0\n"
"v_cvt_pk_bf16_f32 v212, v214, v215\n"
"v_cvt_pk_bf16_f32 v213, v216, v217\n"
"v_mfma_f32_16x16x32_bf16 v[206:209], v[190:193], v[28:31], 0\n"
"ds_read_b128 v[214:217], v12 offset:22528\n"
"v_cvt_pk_bf16_f32 v238, v218, v219\n"
"v_cvt_pk_bf16_f32 v239, v220, v221\n"
"v_mfma_f32_16x16x32_bf16 v[250:253], v[190:193], v[20:23], 0\n"
"s_waitcnt lgkmcnt(3)\n"
"v_cvt_pk_bf16_f32 v194, v222, v223\n"
"v_cvt_pk_bf16_f32 v195, v224, v225\n"
"v_mfma_f32_16x16x32_bf16 v[178:181], v[202:205], v[36:39], v[178:181]\n"
"v_cvt_pk_bf16_f32 v220, v226, v227\n"
"v_cvt_pk_bf16_f32 v221, v228, v229\n"
"v_mfma_f32_16x16x32_bf16 v[206:209], v[202:205], v[40:43], v[206:209]\n"
"ds_read_b128 v[188:191], v13 offset:22528\n"
"v_cvt_pk_bf16_f32 v246, v230, v231\n"
"v_cvt_pk_bf16_f32 v247, v232, v233\n"
"v_mfma_f32_16x16x32_bf16 v[250:253], v[202:205], v[32:35], v[250:253]\n"
"s_waitcnt lgkmcnt(3)\n"
"v_mfma_f32_16x16x32_bf16 v[178:181], v[240:243], v[44:47], v[178:181]\n"
"v_mfma_f32_16x16x32_bf16 v[206:209], v[240:243], v[48:51], v[206:209]\n"
"ds_read_b128 v[200:203], v12 offset:24576\n"
"v_mfma_f32_16x16x32_bf16 v[250:253], v[240:243], v[52:55], v[250:253]\n"
"s_waitcnt lgkmcnt(3)\n"
"v_mfma_f32_16x16x32_bf16 v[178:181], v[196:199], v[56:59], v[178:181]\n"
"v_mfma_f32_16x16x32_bf16 v[206:209], v[196:199], v[60:63], v[206:209]\n"
"ds_read_b128 v[224:227], v13 offset:24576\n"
"v_mfma_f32_16x16x32_bf16 v[250:253], v[196:199], v[64:67], v[250:253]\n"
"s_waitcnt lgkmcnt(3)\n"
"v_mfma_f32_16x16x32_bf16 v[178:181], v[214:217], v[68:71], v[178:181]\n"
"v_mfma_f32_16x16x32_bf16 v[206:209], v[214:217], v[72:75], v[206:209]\n"
"ds_read_b128 v[228:231], v12 offset:26624\n"
"v_mfma_f32_16x16x32_bf16 v[250:253], v[214:217], v[76:79], v[250:253]\n"
"s_waitcnt lgkmcnt(3)\n"
"v_mfma_f32_16x16x32_bf16 v[178:181], v[188:191], v[80:83], v[178:181]\n"
"v_mfma_f32_16x16x32_bf16 v[206:209], v[188:191], v[84:87], v[206:209]\n"
"ds_read_b128 v[214:217], v13 offset:26624\n"
"v_mfma_f32_16x16x32_bf16 v[250:253], v[188:191], v[88:91], v[250:253]\n"
"s_waitcnt lgkmcnt(3)\n"
"v_mfma_f32_16x16x32_bf16 v[190:193], v[200:203], v[24:27], 0\n"
"v_mfma_f32_16x16x32_bf16 v[232:235], v[200:203], v[28:31], 0\n"
"ds_read_b128 v[240:243], v12 offset:28672\n"
"v_mfma_f32_16x16x32_bf16 v[92:95], v[200:203], v[20:23], 0\n"
"s_waitcnt lgkmcnt(3)\n"
"v_mfma_f32_16x16x32_bf16 v[190:193], v[224:227], v[36:39], v[190:193]\n"
"v_cvt_pk_bf16_f32 v196, v178, v179\n"
"v_cvt_pk_bf16_f32 v197, v180, v181\n"
"v_cvt_pk_bf16_f32 v222, v206, v207\n"
"v_cvt_pk_bf16_f32 v223, v208, v209\n"
"v_mfma_f32_16x16x32_bf16 v[232:235], v[224:227], v[40:43], v[232:235]\n"
"ds_read_b128 v[178:181], v13 offset:28672\n"
"v_cvt_pk_bf16_f32 v248, v250, v251\n"
"v_cvt_pk_bf16_f32 v249, v252, v253\n"
"v_mfma_f32_16x16x32_bf16 v[92:95], v[224:227], v[32:35], v[92:95]\n"
"s_waitcnt lgkmcnt(3)\n"
"v_mfma_f32_16x16x32_bf16 v[190:193], v[228:231], v[44:47], v[190:193]\n"
"v_mfma_f32_16x16x32_bf16 v[232:235], v[228:231], v[48:51], v[232:235]\n"
"ds_read_b128 v[198:201], v12 offset:30720\n"
"v_mfma_f32_16x16x32_bf16 v[92:95], v[228:231], v[52:55], v[92:95]\n"
"s_waitcnt lgkmcnt(3)\n"
"v_mfma_f32_16x16x32_bf16 v[190:193], v[214:217], v[56:59], v[190:193]\n"
"v_mfma_f32_16x16x32_bf16 v[232:235], v[214:217], v[60:63], v[232:235]\n"
"ds_read_b128 v[202:205], v13 offset:30720\n"
"v_mfma_f32_16x16x32_bf16 v[92:95], v[214:217], v[64:67], v[92:95]\n"
"s_waitcnt lgkmcnt(3)\n"
"v_mfma_f32_16x16x32_bf16 v[190:193], v[240:243], v[68:71], v[190:193]\n"
"v_mfma_f32_16x16x32_bf16 v[232:235], v[240:243], v[72:75], v[232:235]\n"
"ds_read_b128 v[206:209], v12 offset:32768\n"
"v_mfma_f32_16x16x32_bf16 v[92:95], v[240:243], v[76:79], v[92:95]\n"
"s_waitcnt lgkmcnt(3)\n"
"v_mfma_f32_16x16x32_bf16 v[190:193], v[178:181], v[80:83], v[190:193]\n"
"v_mfma_f32_16x16x32_bf16 v[232:235], v[178:181], v[84:87], v[232:235]\n"
"ds_read_b128 v[216:219], v13 offset:32768\n"
"v_mfma_f32_16x16x32_bf16 v[92:95], v[178:181], v[88:91], v[92:95]\n"
"s_waitcnt lgkmcnt(3)\n"
"v_mfma_f32_16x16x32_bf16 v[180:183], v[198:201], v[24:27], 0\n"
"v_mfma_f32_16x16x32_bf16 v[224:227], v[198:201], v[28:31], 0\n"
"ds_read_b128 v[228:231], v12 offset:34816\n"
"v_mfma_f32_16x16x32_bf16 v[242:245], v[198:201], v[20:23], 0\n"
"s_waitcnt lgkmcnt(3)\n"
"v_mfma_f32_16x16x32_bf16 v[180:183], v[202:205], v[36:39], v[180:183]\n"
"v_cvt_pk_bf16_f32 v188, v190, v191\n"
"v_cvt_pk_bf16_f32 v189, v192, v193\n"
"v_cvt_pk_bf16_f32 v214, v232, v233\n"
"v_cvt_pk_bf16_f32 v215, v234, v235\n"
"v_mfma_f32_16x16x32_bf16 v[224:227], v[202:205], v[40:43], v[224:227]\n"
"ds_read_b128 v[190:193], v13 offset:34816\n"
"v_cvt_pk_bf16_f32 v240, v92, v93\n"
"v_cvt_pk_bf16_f32 v241, v94, v95\n"
"v_mfma_f32_16x16x32_bf16 v[242:245], v[202:205], v[32:35], v[242:245]\n"
"s_waitcnt lgkmcnt(3)\n"
"v_mfma_f32_16x16x32_bf16 v[180:183], v[206:209], v[44:47], v[180:183]\n"
"v_mfma_f32_16x16x32_bf16 v[224:227], v[206:209], v[48:51], v[224:227]\n"
"v_mfma_f32_16x16x32_bf16 v[242:245], v[206:209], v[52:55], v[242:245]\n"
"s_waitcnt lgkmcnt(2)\n"
"v_mfma_f32_16x16x32_bf16 v[180:183], v[216:219], v[56:59], v[180:183]\n"
"v_mfma_f32_16x16x32_bf16 v[224:227], v[216:219], v[60:63], v[224:227]\n"
"v_mfma_f32_16x16x32_bf16 v[242:245], v[216:219], v[64:67], v[242:245]\n"
"s_and_b32 s41, 160, s55\n"
"v_add_u32 v9, s41, v15\n"
"s_waitcnt lgkmcnt(1)\n"
"v_mfma_f32_16x16x32_bf16 v[180:183], v[228:231], v[68:71], v[180:183]\n"
"ds_read_u16 v9, v9 offset:96\n"
"v_mfma_f32_16x16x32_bf16 v[224:227], v[228:231], v[72:75], v[224:227]\n"
"v_mfma_f32_16x16x32_bf16 v[242:245], v[228:231], v[76:79], v[242:245]\n"
"s_waitcnt vmcnt(0)\n"
"s_barrier\n"
"ds_read_b128 v[198:201], v12 offset:43008\n"
"s_waitcnt lgkmcnt(2)\n"
"v_mfma_f32_16x16x32_bf16 v[180:183], v[190:193], v[80:83], v[180:183]\n"
"ds_read_b128 v[206:209], v13 offset:43008\n"
"v_mfma_f32_16x16x32_bf16 v[224:227], v[190:193], v[84:87], v[224:227]\n"
"v_mfma_f32_16x16x32_bf16 v[242:245], v[190:193], v[88:91], v[242:245]\n"
"s_waitcnt lgkmcnt(2)\n"
"ds_read_b128 v[190:193], v12 offset:45056\n"
"v_mad_u32_u24 v9, v9, s40, v16\n"
"s_waitcnt lgkmcnt(2)\n"
"v_mfma_f32_16x16x32_bf16 v[216:219], v[198:201], v[24:27], 0\n"
"v_mfma_f32_16x16x32_bf16 v[232:235], v[198:201], v[28:31], 0\n"
"v_mfma_f32_16x16x32_bf16 v[252:255], v[198:201], v[20:23], 0\n"
"s_waitcnt lgkmcnt(1)\n"
"ds_read_b128 v[198:201], v13 offset:45056\n"
"v_cvt_pk_bf16_f32 v178, v180, v181\n"
"v_cvt_pk_bf16_f32 v179, v182, v183\n"
"v_mfma_f32_16x16x32_bf16 v[216:219], v[206:209], v[36:39], v[216:219]\n"
"v_cvt_pk_bf16_f32 v204, v224, v225\n"
"v_cvt_pk_bf16_f32 v205, v226, v227\n"
"v_mfma_f32_16x16x32_bf16 v[232:235], v[206:209], v[40:43], v[232:235]\n"
"v_cvt_pk_bf16_f32 v230, v242, v243\n"
"v_cvt_pk_bf16_f32 v231, v244, v245\n"
"v_mfma_f32_16x16x32_bf16 v[252:255], v[206:209], v[32:35], v[252:255]\n"
"s_waitcnt lgkmcnt(1)\n"
"ds_read_b128 v[180:183], v12 offset:47104\n"
"v_mfma_f32_16x16x32_bf16 v[216:219], v[190:193], v[44:47], v[216:219]\n"
"v_mfma_f32_16x16x32_bf16 v[232:235], v[190:193], v[48:51], v[232:235]\n"
"s_add_u32 m0, s51, 102400\n"
"s_nop 0\n"
"global_load_lds_dwordx4 v9, s[56:57]\n"
"v_mfma_f32_16x16x32_bf16 v[252:255], v[190:193], v[52:55], v[252:255]\n"
"s_waitcnt lgkmcnt(1)\n"
"ds_read_b128 v[190:193], v13 offset:47104\n"
"v_mfma_f32_16x16x32_bf16 v[216:219], v[198:201], v[56:59], v[216:219]\n"
"v_mfma_f32_16x16x32_bf16 v[232:235], v[198:201], v[60:63], v[232:235]\n"
"s_add_u32 m0, s51, 104448\n"
"s_nop 0\n"
"global_load_lds_dwordx4 v9, s[58:59]\n"
"v_mfma_f32_16x16x32_bf16 v[252:255], v[198:201], v[64:67], v[252:255]\n"
"s_waitcnt lgkmcnt(1)\n"
"ds_read_b128 v[198:201], v12 offset:49152\n"
"v_mfma_f32_16x16x32_bf16 v[216:219], v[180:183], v[68:71], v[216:219]\n"
"v_mfma_f32_16x16x32_bf16 v[232:235], v[180:183], v[72:75], v[232:235]\n"
"s_add_u32 m0, s51, 106496\n"
"s_nop 0\n"
"global_load_lds_dwordx4 v9, s[60:61]\n"
"s_and_b32 s41, 32, s55\n"
"v_add_u32 v9, s41, v15\n"
"v_mfma_f32_16x16x32_bf16 v[252:255], v[180:183], v[76:79], v[252:255]\n"
"s_waitcnt lgkmcnt(1)\n"
"ds_read_u16 v9, v9 offset:832\n"
"v_mfma_f32_16x16x32_bf16 v[216:219], v[190:193], v[80:83], v[216:219]\n"
"ds_read_b128 v[180:183], v13 offset:49152\n"
"v_mfma_f32_16x16x32_bf16 v[232:235], v[190:193], v[84:87], v[232:235]\n"
"v_mfma_f32_16x16x32_bf16 v[252:255], v[190:193], v[88:91], v[252:255]\n"
"s_waitcnt lgkmcnt(2)\n"
"ds_read_b128 v[190:193], v12 offset:51200\n"
"v_mfma_f32_16x16x32_bf16 v[206:209], v[198:201], v[24:27], 0\n"
"v_mfma_f32_16x16x32_bf16 v[226:229], v[198:201], v[28:31], 0\n"
"v_mfma_f32_16x16x32_bf16 v[242:245], v[198:201], v[20:23], 0\n"
"s_barrier\n"
"s_add_u32 s42, s48, 0\n"
"s_addc_u32 s43, s49, 0\n"
"s_add_u32 m0, s50, 24576\n"
"s_nop 0\n"
"global_load_lds_dwordx4 v3, s[42:43]\n"
"s_waitcnt lgkmcnt(2)\n"
"v_mad_u32_u24 v9, v9, s40, v16\n"
"v_cvt_pk_bf16_f32 v198, v216, v217\n"
"v_cvt_pk_bf16_f32 v199, v218, v219\n"
"s_waitcnt lgkmcnt(1)\n"
"v_mfma_f32_16x16x32_bf16 v[206:209], v[180:183], v[36:39], v[206:209]\n"
"s_add_u32 m0, s51, 114688\n"
"s_nop 0\n"
"global_load_lds_dwordx4 v9, s[56:57]\n"
"s_add_u32 m0, s50, 25600\n"
"s_nop 0\n"
"global_load_lds_dwordx4 v4, s[42:43]\n"
"ds_read_b128 v[200:203], v13 offset:51200\n"
"v_cvt_pk_bf16_f32 v224, v232, v233\n"
"v_cvt_pk_bf16_f32 v225, v234, v235\n"
"v_mfma_f32_16x16x32_bf16 v[226:229], v[180:183], v[40:43], v[226:229]\n"
"v_cvt_pk_bf16_f32 v250, v252, v253\n"
"v_cvt_pk_bf16_f32 v251, v254, v255\n"
"v_mfma_f32_16x16x32_bf16 v[242:245], v[180:183], v[32:35], v[242:245]\n"
"s_add_u32 m0, s50, 26624\n"
"s_nop 0\n"
"global_load_lds_dwordx4 v5, s[42:43]\n"
"s_add_u32 m0, s51, 116736\n"
"s_nop 0\n"
"global_load_lds_dwordx4 v9, s[58:59]\n"
"s_waitcnt lgkmcnt(1)\n"
"v_mfma_f32_16x16x32_bf16 v[206:209], v[190:193], v[44:47], v[206:209]\n"
"ds_read_b128 v[180:183], v12 offset:53248\n"
"s_add_u32 s42, s48, 18432\n"
"s_addc_u32 s43, s49, 0\n"
"s_add_u32 m0, s50, 27648\n"
"s_nop 0\n"
"global_load_lds_dwordx4 v3, s[42:43]\n"
"v_mfma_f32_16x16x32_bf16 v[226:229], v[190:193], v[48:51], v[226:229]\n"
"s_add_u32 m0, s51, 118784\n"
"s_nop 0\n"
"global_load_lds_dwordx4 v9, s[60:61]\n"
"s_add_u32 m0, s50, 28672\n"
"s_nop 0\n"
"global_load_lds_dwordx4 v4, s[42:43]\n"
"v_mfma_f32_16x16x32_bf16 v[242:245], v[190:193], v[52:55], v[242:245]\n"
"s_waitcnt lgkmcnt(1)\n"
"ds_read_b128 v[190:193], v13 offset:53248\n"
"v_mfma_f32_16x16x32_bf16 v[206:209], v[200:203], v[56:59], v[206:209]\n"
"s_add_u32 m0, s50, 29696\n"
"s_nop 0\n"
"global_load_lds_dwordx4 v5, s[42:43]\n"
"s_andn2_b32 s41, 288, s55\n"
"v_add_u32 v9, s41, v15\n"
"v_mfma_f32_16x16x32_bf16 v[226:229], v[200:203], v[60:63], v[226:229]\n"
"ds_read_u16 v9, v9 offset:128\n"
"v_mfma_f32_16x16x32_bf16 v[242:245], v[200:203], v[64:67], v[242:245]\n"
"s_waitcnt lgkmcnt(2)\n"
"ds_read_b128 v[200:203], v12 offset:55296\n"
"v_mfma_f32_16x16x32_bf16 v[206:209], v[180:183], v[68:71], v[206:209]\n"
"v_mfma_f32_16x16x32_bf16 v[226:229], v[180:183], v[72:75], v[226:229]\n"
"v_mfma_f32_16x16x32_bf16 v[242:245], v[180:183], v[76:79], v[242:245]\n"
"s_waitcnt lgkmcnt(2)\n"
"ds_read_b128 v[216:219], v13 offset:55296\n"
"v_mfma_f32_16x16x32_bf16 v[206:209], v[190:193], v[80:83], v[206:209]\n"
"v_mfma_f32_16x16x32_bf16 v[226:229], v[190:193], v[84:87], v[226:229]\n"
"v_mfma_f32_16x16x32_bf16 v[242:245], v[190:193], v[88:91], v[242:245]\n"
"s_waitcnt lgkmcnt(2)\n"
"ds_read_b128 v[190:193], v12 offset:57344\n"
"v_mad_u32_u24 v9, v9, s40, v16\n"
"s_add_u32 m0, s51, 126976\n"
"s_nop 0\n"
"global_load_lds_dwordx4 v9, s[56:57]\n"
"s_waitcnt lgkmcnt(2)\n"
"v_mfma_f32_16x16x32_bf16 v[252:255], v[200:203], v[24:27], 0\n"
"v_mfma_f32_16x16x32_bf16 v[108:111], v[200:203], v[28:31], 0\n"
"v_cvt_pk_bf16_f32 v180, v206, v207\n"
"v_cvt_pk_bf16_f32 v181, v208, v209\n"
"v_mfma_f32_16x16x32_bf16 v[112:115], v[200:203], v[20:23], 0\n"
"s_waitcnt lgkmcnt(1)\n"
"ds_read_b128 v[200:203], v13 offset:57344\n"
"v_cvt_pk_bf16_f32 v206, v226, v227\n"
"v_mfma_f32_16x16x32_bf16 v[252:255], v[216:219], v[36:39], v[252:255]\n"
"s_add_u32 m0, s51, 129024\n"
"s_nop 0\n"
"global_load_lds_dwordx4 v9, s[58:59]\n"
"v_cvt_pk_bf16_f32 v207, v228, v229\n"
"v_mfma_f32_16x16x32_bf16 v[108:111], v[216:219], v[40:43], v[108:111]\n"
"s_waitcnt vmcnt(4)\n"
"s_barrier\n"
"s_waitcnt vmcnt(7)\n"
"v_cvt_pk_bf16_f32 v232, v242, v243\n"
"v_cvt_pk_bf16_f32 v233, v244, v245\n"
"v_mfma_f32_16x16x32_bf16 v[112:115], v[216:219], v[32:35], v[112:115]\n"
"s_waitcnt lgkmcnt(1)\n"
"ds_read_b64_tr_b16 v[92:93], v11 offset:53248\n"
"ds_read_b64_tr_b16 v[94:95], v11 offset:54016\n"
"v_mfma_f32_16x16x32_bf16 v[252:255], v[190:193], v[44:47], v[252:255]\n"
"s_add_u32 m0, s51, 131072\n"
"s_nop 0\n"
"global_load_lds_dwordx4 v9, s[60:61]\n"
"ds_read_b64_tr_b16 v[96:97], v11 offset:53256\n"
"ds_read_b64_tr_b16 v[98:99], v11 offset:54024\n"
"v_mfma_f32_16x16x32_bf16 v[108:111], v[190:193], v[48:51], v[108:111]\n"
"ds_read_b64_tr_b16 v[226:227], v10 offset:53312\n"
"ds_read_b64_tr_b16 v[228:229], v10 offset:54080\n"
"s_waitcnt vmcnt(3)\n"
"ds_read_b64_tr_b16 v[100:101], v11 offset:56320\n"
"ds_read_b64_tr_b16 v[102:103], v11 offset:57088\n"
"v_mfma_f32_16x16x32_bf16 v[112:115], v[190:193], v[52:55], v[112:115]\n"
"s_waitcnt lgkmcnt(8)\n"
"ds_read_b64_tr_b16 v[104:105], v11 offset:56328\n"
"ds_read_b64_tr_b16 v[106:107], v11 offset:57096\n"
"v_mfma_f32_16x16x32_bf16 v[252:255], v[200:203], v[56:59], v[252:255]\n"
"ds_read_b64_tr_b16 v[132:133], v10 offset:56384\n"
"ds_read_b64_tr_b16 v[134:135], v10 offset:57152\n"
"v_mfma_f32_16x16x32_bf16 v[108:111], v[200:203], v[60:63], v[108:111]\n"
"ds_read_b128 v[190:193], v12 offset:59392\n"
"ds_read_b128 v[216:219], v13 offset:59392\n"
"v_mfma_f32_16x16x32_bf16 v[112:115], v[200:203], v[64:67], v[112:115]\n"
"s_waitcnt lgkmcnt(8)\n"
"s_add_u32 s42, s48, 36864\n"
"s_addc_u32 s43, s49, 0\n"
"s_add_u32 m0, s50, 24576\n"
"s_nop 0\n"
"global_load_lds_dwordx4 v3, s[42:43]\n"
"s_add_u32 m0, s50, 25600\n"
"s_nop 0\n"
"global_load_lds_dwordx4 v4, s[42:43]\n"
"s_waitcnt lgkmcnt(1)\n"
"v_mfma_f32_16x16x32_bf16 v[252:255], v[190:193], v[68:71], v[252:255]\n"
"v_mfma_f32_16x16x32_bf16 v[108:111], v[190:193], v[72:75], v[108:111]\n"
"s_add_u32 m0, s50, 26624\n"
"s_nop 0\n"
"global_load_lds_dwordx4 v5, s[42:43]\n"
"v_mfma_f32_16x16x32_bf16 v[112:115], v[190:193], v[76:79], v[112:115]\n"
"s_waitcnt lgkmcnt(0)\n"
"v_mov_b32 v190, 0x3f803f80\n"
"v_mov_b32 v191, 0x3f803f80\n"
"v_mfma_f32_16x16x32_bf16 v[252:255], v[216:219], v[80:83], v[252:255]\n"
"v_mov_b32 v192, 0x3f803f80\n"
"v_mov_b32 v193, 0x3f803f80\n"
"s_add_u32 s42, s48, 55296\n"
"s_addc_u32 s43, s49, 0\n"
"s_add_u32 m0, s50, 27648\n"
"s_nop 0\n"
"global_load_lds_dwordx4 v3, s[42:43]\n"
"v_mfma_f32_16x16x32_bf16 v[108:111], v[216:219], v[84:87], v[108:111]\n"
"v_mfma_f32_16x16x32_bf16 v[112:115], v[216:219], v[88:91], v[112:115]\n"
"s_add_u32 m0, s50, 28672\n"
"s_nop 0\n"
"global_load_lds_dwordx4 v4, s[42:43]\n"
"v_cvt_pk_bf16_f32 v182, v252, v253\n"
"v_cvt_pk_bf16_f32 v183, v254, v255\n"
"s_add_u32 m0, s50, 29696\n"
"s_nop 0\n"
"global_load_lds_dwordx4 v5, s[42:43]\n"
"s_and_b32 s41, 32, s55\n"
"v_add_u32 v9, s41, v15\n"
"s_barrier\n"
"s_waitcnt vmcnt(3)\n"
"ds_read_b64_tr_b16 v[0:1], v11 offset:53248\n"
"ds_read_b64_tr_b16 v[2:3], v11 offset:54016\n"
"v_cvt_pk_bf16_f32 v208, v108, v109\n"
"v_cvt_pk_bf16_f32 v209, v110, v111\n"
"v_cvt_pk_bf16_f32 v234, v112, v113\n"
"v_cvt_pk_bf16_f32 v235, v114, v115\n"
"ds_read_b64_tr_b16 v[108:109], v11 offset:53256\n"
"ds_read_b64_tr_b16 v[110:111], v11 offset:54024\n"
"ds_read_b64_tr_b16 v[200:201], v10 offset:53312\n"
"ds_read_b64_tr_b16 v[202:203], v10 offset:54080\n"
"s_waitcnt vmcnt(0)\n"
"ds_read_b64_tr_b16 v[112:113], v11 offset:56320\n"
"ds_read_b64_tr_b16 v[114:115], v11 offset:57088\n"
"ds_read_b64_tr_b16 v[116:117], v11 offset:56328\n"
"ds_read_b64_tr_b16 v[118:119], v11 offset:57096\n"
"ds_read_b64_tr_b16 v[252:253], v10 offset:56384\n"
"ds_read_b64_tr_b16 v[254:255], v10 offset:57152\n"
"ds_read_u16 v9, v9 offset:896\n"
"ds_read_b128 v[216:219], v17 offset:28672\n"
"ds_read_b128 v[242:245], v17 offset:29696\n"
"s_waitcnt lgkmcnt(9)\n"
"s_add_u32 s42, s48, 12288\n"
"s_addc_u32 s43, s49, 0\n"
"s_add_u32 m0, s50, 24576\n"
"s_nop 0\n"
"global_load_lds_dwordx4 v6, s[42:43]\n"
"s_waitcnt lgkmcnt(2)\n"
"v_mad_u32_u24 v9, v9, s40, v16\n"
"s_add_u32 m0, s50, 25600\n"
"s_nop 0\n"
"global_load_lds_dwordx4 v7, s[42:43]\n"
"s_add_u32 m0, s51, 139264\n"
"s_nop 0\n"
"global_load_lds_dwordx4 v9, s[56:57]\n"
"s_waitcnt lgkmcnt(1)\n"
"v_mfma_f32_16x16x32_bf16 v[120:123], v[190:193], v[216:219], 0\n"
"s_add_u32 m0, s50, 26624\n"
"s_nop 0\n"
"global_load_lds_dwordx4 v8, s[42:43]\n"
"v_mfma_f32_16x16x32_bf16 v[136:139], v[186:189], v[216:219], 0\n"
"ds_read_b128 v[124:127], v17 offset:32768\n"
"s_add_u32 m0, s51, 141312\n"
"s_nop 0\n"
"global_load_lds_dwordx4 v9, s[58:59]\n"
"s_add_u32 s42, s48, 49152\n"
"s_addc_u32 s43, s49, 0\n"
"s_add_u32 m0, s50, 27648\n"
"s_nop 0\n"
"global_load_lds_dwordx4 v6, s[42:43]\n"
"v_mfma_f32_16x16x32_bf16 v[128:131], v[212:215], v[216:219], 0\n"
"s_add_u32 m0, s51, 143360\n"
"s_nop 0\n"
"global_load_lds_dwordx4 v9, s[60:61]\n"
"s_add_u32 m0, s50, 28672\n"
"s_nop 0\n"
"global_load_lds_dwordx4 v7, s[42:43]\n"
"v_mfma_f32_16x16x32_bf16 v[4:7], v[238:241], v[216:219], 0\n"
"s_waitcnt lgkmcnt(1)\n"
"v_mfma_f32_16x16x32_bf16 v[120:123], v[190:193], v[242:245], v[120:123]\n"
"s_add_u32 m0, s50, 29696\n"
"s_nop 0\n"
"global_load_lds_dwordx4 v8, s[42:43]\n"
"v_mfma_f32_16x16x32_bf16 v[136:139], v[194:197], v[242:245], v[136:139]\n"
"s_and_b32 s41, 160, s55\n"
"v_add_u32 v177, s41, v15\n"
"v_mfma_f32_16x16x32_bf16 v[128:131], v[220:223], v[242:245], v[128:131]\n"
"ds_read_b128 v[216:219], v17 offset:33792\n"
"v_mfma_f32_16x16x32_bf16 v[4:7], v[246:249], v[242:245], v[4:7]\n"
"s_waitcnt lgkmcnt(1)\n"
"ds_read_u16 v177, v177 offset:288\n"
"v_mfma_f32_16x16x32_bf16 v[120:123], v[190:193], v[124:127], v[120:123]\n"
"ds_read_b64 v[244:245], v17 offset:8704\n"
"v_mfma_f32_16x16x32_bf16 v[136:139], v[178:181], v[124:127], v[136:139]\n"
"v_mfma_f32_16x16x32_bf16 v[128:131], v[204:207], v[124:127], v[128:131]\n"
"v_mov_b32 v242, 0\n"
"v_mov_b32 v243, 0\n"
"v_mfma_f32_16x16x32_bf16 v[4:7], v[230:233], v[124:127], v[4:7]\n"
"s_waitcnt lgkmcnt(2)\n"
"v_mfma_f32_16x16x32_bf16 v[120:123], v[190:193], v[216:219], v[120:123]\n"
"s_waitcnt lgkmcnt(1)\n"
"v_mad_u32_u24 v177, v177, s40, v16\n"
"s_waitcnt lgkmcnt(0)\n"
"v_mfma_f32_16x16x32_bf16 v[120:123], v[190:193], v[242:245], v[120:123]\n"
"s_add_u32 m0, s51, 151552\n"
"s_nop 0\n"
"global_load_lds_dwordx4 v177, s[56:57]\n"
"v_mfma_f32_16x16x32_bf16 v[4:7], v[234:237], v[216:219], v[4:7]\n"
"ds_read_b128 v[190:193], v12 offset:0\n"
"v_mfma_f32_16x16x32_bf16 v[4:7], v[248:251], v[242:245], v[4:7]\n"
"v_mfma_f32_16x16x32_bf16 v[136:139], v[182:185], v[216:219], v[136:139]\n"
"ds_read_b128 v[124:127], v13 offset:0\n"
"s_add_u32 m0, s51, 153600\n"
"s_nop 0\n"
"global_load_lds_dwordx4 v177, s[58:59]\n"
"v_rcp_f32 v176, v120\n"
"v_mfma_f32_16x16x32_bf16 v[136:139], v[196:199], v[242:245], v[136:139]\n"
"v_mfma_f32_16x16x32_bf16 v[128:131], v[208:211], v[216:219], v[128:131]\n"
"v_mfma_f32_16x16x32_bf16 v[128:131], v[222:225], v[242:245], v[128:131]\n"
"s_add_u32 m0, s51, 155648\n"
"s_nop 0\n"
"global_load_lds_dwordx4 v177, s[60:61]\n"
"v_mul_f32 v4, v4, v176\n"
"v_mul_f32 v5, v5, v176\n"
"v_cvt_pk_bf16_f32 v244, v4, v5\n"
"s_waitcnt lgkmcnt(1)\n"
"v_mfma_f32_16x16x32_bf16 v[216:219], v[190:193], v[24:27], 0\n"
"ds_read_b128 v[120:123], v12 offset:2048\n"
"v_mul_f32 v6, v6, v176\n"
"v_mfma_f32_16x16x32_bf16 v[140:143], v[190:193], v[28:31], 0\n"
"v_mul_f32 v7, v7, v176\n"
"v_cvt_pk_bf16_f32 v245, v6, v7\n"
"v_mfma_f32_16x16x32_bf16 v[144:147], v[190:193], v[20:23], 0\n"
"s_waitcnt lgkmcnt(1)\n"
"ds_read_b128 v[190:193], v13 offset:2048\n"
"v_mul_f32 v136, v136, v176\n"
"v_mfma_f32_16x16x32_bf16 v[216:219], v[124:127], v[36:39], v[216:219]\n"
"v_mul_f32 v137, v137, v176\n"
"v_cvt_pk_bf16_f32 v136, v136, v137\n"
"v_mfma_f32_16x16x32_bf16 v[140:143], v[124:127], v[40:43], v[140:143]\n"
"v_mul_f32 v138, v138, v176\n"
"v_mul_f32 v139, v139, v176\n"
"v_mfma_f32_16x16x32_bf16 v[144:147], v[124:127], v[32:35], v[144:147]\n"
"ds_read_b128 v[4:7], v12 offset:4096\n"
"v_cvt_pk_bf16_f32 v137, v138, v139\n"
"v_mul_f32 v128, v128, v176\n"
"v_mul_f32 v129, v129, v176\n"
"v_cvt_pk_bf16_f32 v138, v128, v129\n"
"v_mul_f32 v130, v130, v176\n"
"v_mul_f32 v131, v131, v176\n"
"v_cvt_pk_bf16_f32 v139, v130, v131\n"
"s_waitcnt lgkmcnt(2)\n"
"v_mfma_f32_16x16x32_bf16 v[216:219], v[120:123], v[44:47], v[216:219]\n"
"v_mfma_f32_16x16x32_bf16 v[140:143], v[120:123], v[48:51], v[140:143]\n"
"v_mfma_f32_16x16x32_bf16 v[144:147], v[120:123], v[52:55], v[144:147]\n"
"s_waitcnt lgkmcnt(1)\n"
"ds_read_b128 v[128:131], v13 offset:4096\n"
"v_mfma_f32_16x16x32_bf16 v[216:219], v[190:193], v[56:59], v[216:219]\n"
"v_mfma_f32_16x16x32_bf16 v[140:143], v[190:193], v[60:63], v[140:143]\n"
"v_mfma_f32_16x16x32_bf16 v[144:147], v[190:193], v[64:67], v[144:147]\n"
"s_waitcnt lgkmcnt(1)\n"
"ds_read_b128 v[148:151], v12 offset:6144\n"
"v_mfma_f32_16x16x32_bf16 v[216:219], v[4:7], v[68:71], v[216:219]\n"
"v_mfma_f32_16x16x32_bf16 v[140:143], v[4:7], v[72:75], v[140:143]\n"
"v_mfma_f32_16x16x32_bf16 v[144:147], v[4:7], v[76:79], v[144:147]\n"
"s_waitcnt vmcnt(18)\n"
"s_barrier\n"
"ds_read_b128 v[152:155], v13 offset:6144\n"
"s_waitcnt vmcnt(8)\n"
"ds_read_b64_tr_b16 v[4:5], v11 offset:53248\n"
"ds_read_b64_tr_b16 v[6:7], v11 offset:54016\n"
"s_waitcnt lgkmcnt(4)\n"
"v_mfma_f32_16x16x32_bf16 v[216:219], v[128:131], v[80:83], v[216:219]\n"
"ds_read_b64_tr_b16 v[120:121], v11 offset:53256\n"
"ds_read_b64_tr_b16 v[122:123], v11 offset:54024\n"
"v_mfma_f32_16x16x32_bf16 v[140:143], v[128:131], v[84:87], v[140:143]\n"
"ds_read_b64_tr_b16 v[156:157], v10 offset:53312\n"
"ds_read_b64_tr_b16 v[158:159], v10 offset:54080\n"
"s_waitcnt vmcnt(3)\n"
"ds_read_b64_tr_b16 v[124:125], v11 offset:56320\n"
"ds_read_b64_tr_b16 v[126:127], v11 offset:57088\n"
"v_mfma_f32_16x16x32_bf16 v[144:147], v[128:131], v[88:91], v[144:147]\n"
"s_waitcnt lgkmcnt(9)\n"
"ds_read_b64_tr_b16 v[128:129], v11 offset:56328\n"
"ds_read_b64_tr_b16 v[130:131], v11 offset:57096\n"
"v_mfma_f32_16x16x32_bf16 v[160:163], v[148:151], v[24:27], 0\n"
"ds_read_b64_tr_b16 v[164:165], v10 offset:56384\n"
"ds_read_b64_tr_b16 v[166:167], v10 offset:57152\n"
"v_mfma_f32_16x16x32_bf16 v[8:11], v[148:151], v[28:31], 0\n"
"ds_read_b128 v[168:171], v12 offset:8192\n"
"v_cvt_pk_bf16_f32 v190, v216, v217\n"
"v_mfma_f32_16x16x32_bf16 v[172:175], v[148:151], v[20:23], 0\n"
"s_waitcnt lgkmcnt(13)\n"
"ds_read_b128 v[148:151], v13 offset:8192\n"
"v_cvt_pk_bf16_f32 v191, v218, v219\n"
"v_mfma_f32_16x16x32_bf16 v[160:163], v[152:155], v[36:39], v[160:163]\n"
"ds_write_b128 v17, v[136:139] offset:7680\n"
"v_cvt_pk_bf16_f32 v216, v140, v141\n"
"v_cvt_pk_bf16_f32 v217, v142, v143\n"
"v_mfma_f32_16x16x32_bf16 v[8:11], v[152:155], v[40:43], v[8:11]\n"
"v_cvt_pk_bf16_f32 v242, v144, v145\n"
"v_cvt_pk_bf16_f32 v243, v146, v147\n"
"v_mfma_f32_16x16x32_bf16 v[172:175], v[152:155], v[32:35], v[172:175]\n"
"ds_read_b128 v[136:139], v12 offset:10240\n"
"v_add_u32 v237, s54, v14\n"
"ds_write_b64 v237, v[244:245] offset:0\n"
"s_waitcnt lgkmcnt(4)\n"
"v_mfma_f32_16x16x32_bf16 v[160:163], v[168:171], v[44:47], v[160:163]\n"
"v_mfma_f32_16x16x32_bf16 v[8:11], v[168:171], v[48:51], v[8:11]\n"
"ds_write_b128 v17, v[226:229] offset:53248\n"
"v_mfma_f32_16x16x32_bf16 v[172:175], v[168:171], v[52:55], v[172:175]\n"
"s_waitcnt lgkmcnt(4)\n"
"ds_read_b128 v[226:229], v13 offset:10240\n"
"v_mfma_f32_16x16x32_bf16 v[160:163], v[148:151], v[56:59], v[160:163]\n"
"ds_write_b128 v17, v[132:135] offset:54272\n"
"v_mfma_f32_16x16x32_bf16 v[8:11], v[148:151], v[60:63], v[8:11]\n"
"ds_write_b128 v17, v[200:203] offset:55296\n"
"v_mfma_f32_16x16x32_bf16 v[172:175], v[148:151], v[64:67], v[172:175]\n"
"ds_write_b128 v17, v[252:255] offset:56320\n"
"ds_write_b128 v17, v[156:159] offset:57344\n"
"ds_write_b128 v17, v[164:167] offset:58368\n"
"s_waitcnt lgkmcnt(8)\n"
"v_mfma_f32_16x16x32_bf16 v[160:163], v[136:139], v[68:71], v[160:163]\n"
"v_mfma_f32_16x16x32_bf16 v[8:11], v[136:139], v[72:75], v[8:11]\n"
"v_mfma_f32_16x16x32_bf16 v[172:175], v[136:139], v[76:79], v[172:175]\n"
"s_waitcnt lgkmcnt(5)\n"
"v_mov_b32 v132, 0x3f803f80\n"
"v_mov_b32 v133, 0x3f803f80\n"
"v_mfma_f32_16x16x32_bf16 v[160:163], v[226:229], v[80:83], v[160:163]\n"
"s_waitcnt vmcnt(5)\n"
"s_waitcnt lgkmcnt(0)\n"
"s_barrier\n"
"ds_read_b128 v[136:139], v17 offset:30720\n"
"v_mov_b32 v134, 0x3f803f80\n"
"v_mfma_f32_16x16x32_bf16 v[8:11], v[226:229], v[84:87], v[8:11]\n"
"s_and_b32 s41, 32, s55\n"
"v_add_u32 v245, s41, v15\n"
"v_mov_b32 v135, 0x3f803f80\n"
"v_mfma_f32_16x16x32_bf16 v[172:175], v[226:229], v[88:91], v[172:175]\n"
"ds_read_b128 v[140:143], v17 offset:31744\n"
"v_cvt_pk_bf16_f32 v200, v160, v161\n"
"v_cvt_pk_bf16_f32 v201, v162, v163\n"
"ds_read_b128 v[146:149], v17 offset:32768\n"
"v_cvt_pk_bf16_f32 v226, v8, v9\n"
"v_cvt_pk_bf16_f32 v227, v10, v11\n"
"ds_read_u16 v245, v245 offset:480\n"
"s_waitcnt lgkmcnt(3)\n"
"v_mfma_f32_16x16x32_bf16 v[8:11], v[132:135], v[136:139], 0\n"
"v_cvt_pk_bf16_f32 v252, v172, v173\n"
"v_cvt_pk_bf16_f32 v253, v174, v175\n"
"v_mfma_f32_16x16x32_bf16 v[152:155], v[186:189], v[136:139], 0\n"
"v_mfma_f32_16x16x32_bf16 v[156:159], v[212:215], v[136:139], 0\n"
"v_mfma_f32_16x16x32_bf16 v[160:163], v[238:241], v[136:139], 0\n"
"s_waitcnt lgkmcnt(2)\n"
"v_mfma_f32_16x16x32_bf16 v[8:11], v[132:135], v[140:143], v[8:11]\n"
"s_waitcnt lgkmcnt(0)\n"
"v_mad_u32_u24 v245, v245, s40, v16\n"
"v_mov_b32 v144, 0\n"
"v_mfma_f32_16x16x32_bf16 v[8:11], v[132:135], v[146:149], v[8:11]\n"
"s_add_u32 m0, s51, 102400\n"
"s_nop 0\n"
"global_load_lds_dwordx4 v245, s[56:57]\n"
"v_mfma_f32_16x16x32_bf16 v[160:163], v[250:253], v[140:143], v[160:163]\n"
"v_mov_b32 v145, 0\n"
"v_mov_b32 v150, 0\n"
"v_mov_b32 v151, 0\n"
"v_mfma_f32_16x16x32_bf16 v[160:163], v[240:243], v[144:147], v[160:163]\n"
"s_nop 0\n"
"v_mfma_f32_16x16x32_bf16 v[160:163], v[248:251], v[148:151], v[160:163]\n"
"ds_read_b128 v[132:135], v14 offset:32256\n"
"s_add_u32 m0, s51, 104448\n"
"s_nop 0\n"
"global_load_lds_dwordx4 v245, s[58:59]\n"
"v_mfma_f32_16x16x32_bf16 v[152:155], v[198:201], v[140:143], v[152:155]\n"
"v_rcp_f32 v244, v8\n"
"v_mfma_f32_16x16x32_bf16 v[152:155], v[188:191], v[144:147], v[152:155]\n"
"ds_read_b128 v[8:11], v17 offset:53248\n"
"v_mfma_f32_16x16x32_bf16 v[152:155], v[196:199], v[148:151], v[152:155]\n"
"ds_read_b128 v[136:139], v14 offset:38400\n"
"v_mul_f32 v160, v160, v244\n"
"v_mfma_f32_16x16x32_bf16 v[156:159], v[224:227], v[140:143], v[156:159]\n"
"s_add_u32 m0, s51, 106496\n"
"s_nop 0\n"
"global_load_lds_dwordx4 v245, s[60:61]\n"
"v_mfma_f32_16x16x32_bf16 v[156:159], v[214:217], v[144:147], v[156:159]\n"
"ds_read_b128 v[140:143], v17 offset:54272\n"
"v_mul_f32 v161, v161, v244\n"
"v_mfma_f32_16x16x32_bf16 v[156:159], v[222:225], v[148:151], v[156:159]\n"
"s_waitcnt lgkmcnt(3)\n"
"ds_read_b128 v[144:147], v14 offset:44544\n"
"v_cvt_pk_bf16_f32 v254, v160, v161\n"
"v_mfma_f32_16x16x32_bf16 v[148:151], v[92:95], v[132:135], 0\n"
"v_mul_f32 v162, v162, v244\n"
"v_mul_f32 v163, v163, v244\n"
"v_mfma_f32_16x16x32_bf16 v[164:167], v[96:99], v[132:135], 0\n"
"s_waitcnt lgkmcnt(3)\n"
"ds_read_b128 v[168:171], v17 offset:55296\n"
"v_cvt_pk_bf16_f32 v255, v162, v163\n"
"v_mfma_f32_16x16x32_bf16 v[160:163], v[8:11], v[132:135], 0\n"
"s_waitcnt lgkmcnt(3)\n"
"ds_read_b128 v[8:11], v14 offset:50688\n"
"v_mul_f32 v152, v152, v244\n"
"v_mfma_f32_16x16x32_bf16 v[148:151], v[100:103], v[136:139], v[148:151]\n"
"v_mul_f32 v153, v153, v244\n"
"v_cvt_pk_bf16_f32 v152, v152, v153\n"
"v_mfma_f32_16x16x32_bf16 v[164:167], v[104:107], v[136:139], v[164:167]\n"
"ds_read_b128 v[132:135], v17 offset:56320\n"
"v_mul_f32 v154, v154, v244\n"
"v_mul_f32 v155, v155, v244\n"
"v_cvt_pk_bf16_f32 v153, v154, v155\n"
"s_waitcnt lgkmcnt(4)\n"
"v_mfma_f32_16x16x32_bf16 v[160:163], v[140:143], v[136:139], v[160:163]\n"
"s_waitcnt lgkmcnt(3)\n"
"ds_read_b128 v[136:139], v14 offset:2048\n"
"v_mul_f32 v156, v156, v244\n"
"v_mfma_f32_16x16x32_bf16 v[148:151], v[0:3], v[144:147], v[148:151]\n"
"v_mul_f32 v157, v157, v244\n"
"v_cvt_pk_bf16_f32 v154, v156, v157\n"
"v_mfma_f32_16x16x32_bf16 v[164:167], v[108:111], v[144:147], v[164:167]\n"
"s_waitcnt lgkmcnt(3)\n"
"ds_read_b128 v[140:143], v17 offset:57344\n"
"v_mul_f32 v158, v158, v244\n"
"v_mfma_f32_16x16x32_bf16 v[160:163], v[168:171], v[144:147], v[160:163]\n"
"s_waitcnt lgkmcnt(3)\n"
"ds_read_b128 v[144:147], v14 offset:3072\n"
"v_mul_f32 v159, v159, v244\n"
"v_mfma_f32_16x16x32_bf16 v[148:151], v[112:115], v[8:11], v[148:151]\n"
"v_cvt_pk_bf16_f32 v155, v158, v159\n"
"v_mfma_f32_16x16x32_bf16 v[164:167], v[116:119], v[8:11], v[164:167]\n"
"s_waitcnt lgkmcnt(3)\n"
"ds_read_b128 v[156:159], v17 offset:58368\n"
"v_mfma_f32_16x16x32_bf16 v[160:163], v[132:135], v[8:11], v[160:163]\n"
"s_waitcnt lgkmcnt(3)\n"
"ds_read_b128 v[8:11], v12 offset:24576\n"
"v_mfma_f32_16x16x32_bf16 v[148:151], v[4:7], v[136:139], v[148:151]\n"
"v_mfma_f32_16x16x32_bf16 v[164:167], v[120:123], v[136:139], v[164:167]\n"
"s_waitcnt lgkmcnt(3)\n"
"v_mfma_f32_16x16x32_bf16 v[160:163], v[140:143], v[136:139], v[160:163]\n"
"s_waitcnt lgkmcnt(2)\n"
"ds_read_b128 v[132:135], v13 offset:24576\n"
"v_bfe_u32 v245, v14, 3, 5\n"
"v_mfma_f32_16x16x32_bf16 v[148:151], v[124:127], v[144:147], v[148:151]\n"
"ds_read_u16 v245, v245 offset:800\n"
"v_mfma_f32_16x16x32_bf16 v[164:167], v[128:131], v[144:147], v[164:167]\n"
"s_waitcnt vmcnt(3)\n"
"s_barrier\n"
"s_waitcnt lgkmcnt(3)\n"
"v_mfma_f32_16x16x32_bf16 v[160:163], v[156:159], v[144:147], v[160:163]\n"
"s_and_b32 s41, 416, s55\n"
"v_add_u32 v244, s41, v15\n"
"s_waitcnt lgkmcnt(2)\n"
"v_mfma_f32_16x16x32_bf16 v[136:139], v[8:11], v[24:27], 0\n"
"ds_read_b128 v[140:143], v12 offset:26624\n"
"v_cvt_pk_bf16_f32 v148, v148, v149\n"
"v_mfma_f32_16x16x32_bf16 v[144:147], v[8:11], v[28:31], 0\n"
"ds_read_u16 v244, v244 offset:544\n"
"v_cvt_pk_bf16_f32 v149, v150, v151\n"
"v_mfma_f32_16x16x32_bf16 v[156:159], v[8:11], v[20:23], 0\n"
"ds_read_b128 v[8:11], v13 offset:26624\n"
"v_cvt_pk_bf16_f32 v150, v164, v165\n"
"v_cvt_pk_bf16_f32 v151, v166, v167\n"
"v_cvt_pk_bf16_f32 v160, v160, v161\n"
"s_waitcnt lgkmcnt(4)\n"
"v_mfma_f32_16x16x32_bf16 v[136:139], v[132:135], v[36:39], v[136:139]\n"
"ds_write_b128 v17, v[152:155] offset:7680\n"
"v_mfma_f32_16x16x32_bf16 v[144:147], v[132:135], v[40:43], v[144:147]\n"
"s_waitcnt lgkmcnt(4)\n"
"v_mad_u32_u24 v161, v245, s40, v18\n"
"v_mad_u32_u24 v245, v245, s40, v19\n"
"v_mfma_f32_16x16x32_bf16 v[156:159], v[132:135], v[32:35], v[156:159]\n"
"global_store_dwordx4 v161, v[148:151], s[68:69] nt\n"
"s_waitcnt lgkmcnt(3)\n"
"v_mfma_f32_16x16x32_bf16 v[136:139], v[140:143], v[44:47], v[136:139]\n"
"s_waitcnt lgkmcnt(2)\n"
"ds_read_b128 v[132:135], v12 offset:28672\n"
"v_mad_u32_u24 v244, v244, s40, v16\n"
"v_add_u32 v161, s54, v14\n"
"v_mfma_f32_16x16x32_bf16 v[144:147], v[140:143], v[48:51], v[144:147]\n"
"s_add_u32 m0, s51, 114688\n"
"s_nop 0\n"
"global_load_lds_dwordx4 v244, s[56:57]\n"
"ds_write_b64 v161, v[254:255] offset:0\n"
"v_mfma_f32_16x16x32_bf16 v[156:159], v[140:143], v[52:55], v[156:159]\n"
"s_waitcnt lgkmcnt(3)\n"
"ds_read_b128 v[140:143], v13 offset:28672\n"
"v_cvt_pk_bf16_f32 v161, v162, v163\n"
"v_mfma_f32_16x16x32_bf16 v[136:139], v[8:11], v[56:59], v[136:139]\n"
"global_store_dwordx2 v245, v[160:161], s[70:71] nt\n"
"s_add_u32 m0, s51, 116736\n"
"s_nop 0\n"
"global_load_lds_dwordx4 v244, s[58:59]\n"
"v_mfma_f32_16x16x32_bf16 v[144:147], v[8:11], v[60:63], v[144:147]\n"
"v_mfma_f32_16x16x32_bf16 v[156:159], v[8:11], v[64:67], v[156:159]\n"
"s_waitcnt lgkmcnt(2)\n"
"ds_read_b128 v[8:11], v12 offset:30720\n"
"v_mfma_f32_16x16x32_bf16 v[136:139], v[132:135], v[68:71], v[136:139]\n"
"v_mfma_f32_16x16x32_bf16 v[144:147], v[132:135], v[72:75], v[144:147]\n"
"s_add_u32 m0, s51, 118784\n"
"s_nop 0\n"
"global_load_lds_dwordx4 v244, s[60:61]\n"
"v_mfma_f32_16x16x32_bf16 v[156:159], v[132:135], v[76:79], v[156:159]\n"
"s_waitcnt lgkmcnt(1)\n"
"ds_read_b128 v[132:135], v13 offset:30720\n"
"v_mfma_f32_16x16x32_bf16 v[136:139], v[140:143], v[80:83], v[136:139]\n"
"v_mfma_f32_16x16x32_bf16 v[144:147], v[140:143], v[84:87], v[144:147]\n"
"v_mfma_f32_16x16x32_bf16 v[156:159], v[140:143], v[88:91], v[156:159]\n"
"s_waitcnt lgkmcnt(1)\n"
"ds_read_b128 v[140:143], v12 offset:32768\n"
"v_mfma_f32_16x16x32_bf16 v[148:151], v[8:11], v[24:27], 0\n"
"v_mfma_f32_16x16x32_bf16 v[152:155], v[8:11], v[28:31], 0\n"
"v_mfma_f32_16x16x32_bf16 v[160:163], v[8:11], v[20:23], 0\n"
"s_waitcnt lgkmcnt(1)\n"
"ds_read_b128 v[8:11], v13 offset:32768\n"
"v_mfma_f32_16x16x32_bf16 v[148:151], v[132:135], v[36:39], v[148:151]\n"
"v_cvt_pk_bf16_f32 v184, v136, v137\n"
"v_cvt_pk_bf16_f32 v185, v138, v139\n"
"v_cvt_pk_bf16_f32 v210, v144, v145\n"
"v_cvt_pk_bf16_f32 v211, v146, v147\n"
"v_mfma_f32_16x16x32_bf16 v[152:155], v[132:135], v[40:43], v[152:155]\n"
"v_cvt_pk_bf16_f32 v236, v156, v157\n"
"v_cvt_pk_bf16_f32 v237, v158, v159\n"
"v_mfma_f32_16x16x32_bf16 v[160:163], v[132:135], v[32:35], v[160:163]\n"
"s_waitcnt lgkmcnt(1)\n"
"ds_read_b128 v[132:135], v12 offset:34816\n"
"v_mfma_f32_16x16x32_bf16 v[148:151], v[140:143], v[44:47], v[148:151]\n"
"v_mfma_f32_16x16x32_bf16 v[152:155], v[140:143], v[48:51], v[152:155]\n"
"v_mfma_f32_16x16x32_bf16 v[160:163], v[140:143], v[52:55], v[160:163]\n"
"s_waitcnt lgkmcnt(1)\n"
"ds_read_b128 v[136:139], v13 offset:34816\n"
"v_mfma_f32_16x16x32_bf16 v[148:151], v[8:11], v[56:59], v[148:151]\n"
"v_mfma_f32_16x16x32_bf16 v[152:155], v[8:11], v[60:63], v[152:155]\n"
"v_mfma_f32_16x16x32_bf16 v[160:163], v[8:11], v[64:67], v[160:163]\n"
"s_waitcnt lgkmcnt(1)\n"
"v_mfma_f32_16x16x32_bf16 v[148:151], v[132:135], v[68:71], v[148:151]\n"
"v_mfma_f32_16x16x32_bf16 v[152:155], v[132:135], v[72:75], v[152:155]\n"
"v_mfma_f32_16x16x32_bf16 v[160:163], v[132:135], v[76:79], v[160:163]\n"
"s_waitcnt lgkmcnt(0)\n"
"v_mov_b32 v8, 0x3f803f80\n"
"v_mov_b32 v9, 0x3f803f80\n"
"v_mfma_f32_16x16x32_bf16 v[148:151], v[136:139], v[80:83], v[148:151]\n"
"s_barrier\n"
"ds_read_b128 v[132:135], v17 offset:28672\n"
"v_mov_b32 v10, 0x3f803f80\n"
"v_mfma_f32_16x16x32_bf16 v[152:155], v[136:139], v[84:87], v[152:155]\n"
"s_and_b32 s41, 416, s55\n"
"v_add_u32 v255, s41, v15\n"
"v_mov_b32 v11, 0x3f803f80\n"
"v_mfma_f32_16x16x32_bf16 v[160:163], v[136:139], v[88:91], v[160:163]\n"
"ds_read_b128 v[136:139], v17 offset:29696\n"
"v_cvt_pk_bf16_f32 v192, v148, v149\n"
"v_cvt_pk_bf16_f32 v193, v150, v151\n"
"ds_read_u16 v255, v255 offset:576\n"
"v_cvt_pk_bf16_f32 v218, v152, v153\n"
"v_cvt_pk_bf16_f32 v219, v154, v155\n"
"s_waitcnt lgkmcnt(2)\n"
"v_mfma_f32_16x16x32_bf16 v[140:143], v[8:11], v[132:135], 0\n"
"v_mfma_f32_16x16x32_bf16 v[144:147], v[180:183], v[132:135], 0\n"
"ds_read_b128 v[148:151], v17 offset:32768\n"
"v_cvt_pk_bf16_f32 v244, v160, v161\n"
"v_mfma_f32_16x16x32_bf16 v[152:155], v[206:209], v[132:135], 0\n"
"v_cvt_pk_bf16_f32 v245, v162, v163\n"
"v_mfma_f32_16x16x32_bf16 v[156:159], v[232:235], v[132:135], 0\n"
"s_waitcnt lgkmcnt(1)\n"
"v_mad_u32_u24 v255, v255, s40, v16\n"
"v_mfma_f32_16x16x32_bf16 v[140:143], v[8:11], v[136:139], v[140:143]\n"
"s_add_u32 m0, s51, 126976\n"
"s_nop 0\n"
"global_load_lds_dwordx4 v255, s[56:57]\n"
"v_mfma_f32_16x16x32_bf16 v[144:147], v[188:191], v[136:139], v[144:147]\n"
"ds_read_b128 v[132:135], v17 offset:33792\n"
"v_mfma_f32_16x16x32_bf16 v[152:155], v[214:217], v[136:139], v[152:155]\n"
"ds_read_b64 v[162:163], v17 offset:8704\n"
"v_mfma_f32_16x16x32_bf16 v[156:159], v[240:243], v[136:139], v[156:159]\n"
"s_waitcnt lgkmcnt(2)\n"
"v_mfma_f32_16x16x32_bf16 v[140:143], v[8:11], v[148:151], v[140:143]\n"
"s_add_u32 m0, s51, 129024\n"
"s_nop 0\n"
"global_load_lds_dwordx4 v255, s[58:59]\n"
"v_mfma_f32_16x16x32_bf16 v[144:147], v[184:187], v[148:151], v[144:147]\n"
"v_mfma_f32_16x16x32_bf16 v[152:155], v[210:213], v[148:151], v[152:155]\n"
"v_mov_b32 v160, 0\n"
"v_mov_b32 v161, 0\n"
"v_mfma_f32_16x16x32_bf16 v[156:159], v[236:239], v[148:151], v[156:159]\n"
"s_waitcnt lgkmcnt(1)\n"
"v_mfma_f32_16x16x32_bf16 v[140:143], v[8:11], v[132:135], v[140:143]\n"
"s_add_u32 m0, s51, 131072\n"
"s_nop 0\n"
"global_load_lds_dwordx4 v255, s[60:61]\n"
"s_waitcnt lgkmcnt(0)\n"
"v_mfma_f32_16x16x32_bf16 v[140:143], v[8:11], v[160:163], v[140:143]\n"
"ds_read_b128 v[8:11], v14 offset:32256\n"
"v_mfma_f32_16x16x32_bf16 v[156:159], v[248:251], v[132:135], v[156:159]\n"
"v_mfma_f32_16x16x32_bf16 v[156:159], v[250:253], v[160:163], v[156:159]\n"
"ds_read_b128 v[136:139], v17 offset:53248\n"
"v_mfma_f32_16x16x32_bf16 v[144:147], v[196:199], v[132:135], v[144:147]\n"
"ds_read_b128 v[148:151], v14 offset:38400\n"
"v_mfma_f32_16x16x32_bf16 v[144:147], v[198:201], v[160:163], v[144:147]\n"
"v_mfma_f32_16x16x32_bf16 v[152:155], v[222:225], v[132:135], v[152:155]\n"
"ds_read_b128 v[132:135], v17 offset:54272\n"
"v_rcp_f32 v255, v140\n"
"v_mfma_f32_16x16x32_bf16 v[152:155], v[224:227], v[160:163], v[152:155]\n"
"ds_read_b128 v[140:143], v14 offset:44544\n"
"v_mul_f32 v156, v156, v255\n"
"v_mul_f32 v157, v157, v255\n"
"v_cvt_pk_bf16_f32 v238, v156, v157\n"
"s_waitcnt lgkmcnt(4)\n"
"v_mfma_f32_16x16x32_bf16 v[160:163], v[92:95], v[8:11], 0\n"
"v_mul_f32 v158, v158, v255\n"
"v_mul_f32 v159, v159, v255\n"
"v_mfma_f32_16x16x32_bf16 v[164:167], v[96:99], v[8:11], 0\n"
"s_waitcnt lgkmcnt(3)\n"
"ds_read_b128 v[168:171], v17 offset:55296\n"
"v_cvt_pk_bf16_f32 v239, v158, v159\n"
"v_mfma_f32_16x16x32_bf16 v[156:159], v[136:139], v[8:11], 0\n"
"s_waitcnt lgkmcnt(3)\n"
"ds_read_b128 v[8:11], v14 offset:50688\n"
"v_mul_f32 v144, v144, v255\n"
"v_mfma_f32_16x16x32_bf16 v[160:163], v[100:103], v[148:151], v[160:163]\n"
"v_mul_f32 v145, v145, v255\n"
"v_cvt_pk_bf16_f32 v144, v144, v145\n"
"v_mfma_f32_16x16x32_bf16 v[164:167], v[104:107], v[148:151], v[164:167]\n"
"s_waitcnt lgkmcnt(3)\n"
"ds_read_b128 v[136:139], v17 offset:56320\n"
"v_mul_f32 v146, v146, v255\n"
"v_mfma_f32_16x16x32_bf16 v[156:159], v[132:135], v[148:151], v[156:159]\n"
"s_waitcnt lgkmcnt(3)\n"
"ds_read_b128 v[132:135], v14 offset:2048\n"
"v_mul_f32 v147, v147, v255\n"
"v_cvt_pk_bf16_f32 v145, v146, v147\n"
"v_mfma_f32_16x16x32_bf16 v[160:163], v[0:3], v[140:143], v[160:163]\n"
"v_mul_f32 v152, v152, v255\n"
"v_mul_f32 v153, v153, v255\n"
"v_mfma_f32_16x16x32_bf16 v[164:167], v[108:111], v[140:143], v[164:167]\n"
"s_waitcnt lgkmcnt(3)\n"
"ds_read_b128 v[148:151], v17 offset:57344\n"
"v_cvt_pk_bf16_f32 v146, v152, v153\n"
"v_mul_f32 v154, v154, v255\n"
"v_mfma_f32_16x16x32_bf16 v[156:159], v[168:171], v[140:143], v[156:159]\n"
"s_waitcnt lgkmcnt(3)\n"
"ds_read_b128 v[140:143], v14 offset:3072\n"
"v_mul_f32 v155, v155, v255\n"
"v_mfma_f32_16x16x32_bf16 v[160:163], v[112:115], v[8:11], v[160:163]\n"
"v_cvt_pk_bf16_f32 v147, v154, v155\n"
"v_mfma_f32_16x16x32_bf16 v[164:167], v[116:119], v[8:11], v[164:167]\n"
"s_waitcnt lgkmcnt(3)\n"
"ds_read_b128 v[152:155], v17 offset:58368\n"
"v_mfma_f32_16x16x32_bf16 v[156:159], v[136:139], v[8:11], v[156:159]\n"
"s_waitcnt lgkmcnt(3)\n"
"ds_read_b128 v[8:11], v12 offset:49152\n"
"v_mfma_f32_16x16x32_bf16 v[160:163], v[4:7], v[132:135], v[160:163]\n"
"v_mfma_f32_16x16x32_bf16 v[164:167], v[120:123], v[132:135], v[164:167]\n"
"s_waitcnt lgkmcnt(3)\n"
"v_mfma_f32_16x16x32_bf16 v[156:159], v[148:151], v[132:135], v[156:159]\n"
"s_waitcnt lgkmcnt(2)\n"
"ds_read_b128 v[132:135], v13 offset:49152\n"
"v_bfe_u32 v255, v14, 3, 5\n"
"v_mfma_f32_16x16x32_bf16 v[160:163], v[124:127], v[140:143], v[160:163]\n"
"ds_read_u16 v255, v255 offset:832\n"
"v_mfma_f32_16x16x32_bf16 v[164:167], v[128:131], v[140:143], v[164:167]\n"
"s_waitcnt vmcnt(8)\n"
"s_barrier\n"
"s_waitcnt lgkmcnt(3)\n"
"v_mfma_f32_16x16x32_bf16 v[156:159], v[152:155], v[140:143], v[156:159]\n"
"s_waitcnt lgkmcnt(2)\n"
"ds_read_b128 v[136:139], v12 offset:51200\n"
"v_mfma_f32_16x16x32_bf16 v[140:143], v[8:11], v[24:27], 0\n"
"ds_write_b128 v17, v[144:147] offset:7680\n"
"v_mfma_f32_16x16x32_bf16 v[144:147], v[8:11], v[28:31], 0\n"
"v_cvt_pk_bf16_f32 v160, v160, v161\n"
"v_cvt_pk_bf16_f32 v161, v162, v163\n"
"v_mfma_f32_16x16x32_bf16 v[148:151], v[8:11], v[20:23], 0\n"
"s_waitcnt lgkmcnt(3)\n"
"ds_read_b128 v[8:11], v13 offset:51200\n"
"v_cvt_pk_bf16_f32 v162, v164, v165\n"
"v_mfma_f32_16x16x32_bf16 v[140:143], v[132:135], v[36:39], v[140:143]\n"
"s_waitcnt lgkmcnt(3)\n"
"v_cvt_pk_bf16_f32 v163, v166, v167\n"
"v_mad_u32_u24 v254, v255, s40, v18\n"
"v_mfma_f32_16x16x32_bf16 v[144:147], v[132:135], v[40:43], v[144:147]\n"
"global_store_dwordx4 v254, v[160:163], s[68:69] nt\n"
"v_mfma_f32_16x16x32_bf16 v[148:151], v[132:135], v[32:35], v[148:151]\n"
"s_waitcnt lgkmcnt(2)\n"
"ds_read_b128 v[132:135], v12 offset:53248\n"
"v_add_u32 v254, s54, v14\n"
"v_mfma_f32_16x16x32_bf16 v[140:143], v[136:139], v[44:47], v[140:143]\n"
"ds_write_b64 v254, v[238:239] offset:0\n"
"v_mfma_f32_16x16x32_bf16 v[144:147], v[136:139], v[48:51], v[144:147]\n"
"v_cvt_pk_bf16_f32 v156, v156, v157\n"
"v_mad_u32_u24 v255, v255, s40, v19\n"
"v_mfma_f32_16x16x32_bf16 v[148:151], v[136:139], v[52:55], v[148:151]\n"
"s_waitcnt lgkmcnt(2)\n"
"ds_read_b128 v[136:139], v13 offset:53248\n"
"v_cvt_pk_bf16_f32 v157, v158, v159\n"
"v_mfma_f32_16x16x32_bf16 v[140:143], v[8:11], v[56:59], v[140:143]\n"
"global_store_dwordx2 v255, v[156:157], s[70:71] nt\n"
"v_mfma_f32_16x16x32_bf16 v[144:147], v[8:11], v[60:63], v[144:147]\n"
"v_mfma_f32_16x16x32_bf16 v[148:151], v[8:11], v[64:67], v[148:151]\n"
"s_waitcnt lgkmcnt(2)\n"
"ds_read_b128 v[8:11], v12 offset:55296\n"
"v_mfma_f32_16x16x32_bf16 v[140:143], v[132:135], v[68:71], v[140:143]\n"
"v_mfma_f32_16x16x32_bf16 v[144:147], v[132:135], v[72:75], v[144:147]\n"
"v_mfma_f32_16x16x32_bf16 v[148:151], v[132:135], v[76:79], v[148:151]\n"
"s_waitcnt lgkmcnt(1)\n"
"ds_read_b128 v[132:135], v13 offset:55296\n"
"v_mfma_f32_16x16x32_bf16 v[140:143], v[136:139], v[80:83], v[140:143]\n"
"v_mfma_f32_16x16x32_bf16 v[144:147], v[136:139], v[84:87], v[144:147]\n"
"v_mfma_f32_16x16x32_bf16 v[148:151], v[136:139], v[88:91], v[148:151]\n"
"s_waitcnt lgkmcnt(1)\n"
"ds_read_b128 v[136:139], v12 offset:57344\n"
"v_mfma_f32_16x16x32_bf16 v[152:155], v[8:11], v[24:27], 0\n"
"v_mfma_f32_16x16x32_bf16 v[156:159], v[8:11], v[28:31], 0\n"
"v_mfma_f32_16x16x32_bf16 v[160:163], v[8:11], v[20:23], 0\n"
"s_waitcnt lgkmcnt(1)\n"
"ds_read_b128 v[8:11], v13 offset:57344\n"
"v_mfma_f32_16x16x32_bf16 v[152:155], v[132:135], v[36:39], v[152:155]\n"
"v_cvt_pk_bf16_f32 v202, v140, v141\n"
"v_cvt_pk_bf16_f32 v203, v142, v143\n"
"v_cvt_pk_bf16_f32 v228, v144, v145\n"
"v_cvt_pk_bf16_f32 v229, v146, v147\n"
"v_mfma_f32_16x16x32_bf16 v[156:159], v[132:135], v[40:43], v[156:159]\n"
"v_cvt_pk_bf16_f32 v254, v148, v149\n"
"v_cvt_pk_bf16_f32 v255, v150, v151\n"
"v_mfma_f32_16x16x32_bf16 v[160:163], v[132:135], v[32:35], v[160:163]\n"
"s_waitcnt lgkmcnt(1)\n"
"ds_read_b128 v[132:135], v12 offset:59392\n"
"v_mfma_f32_16x16x32_bf16 v[152:155], v[136:139], v[44:47], v[152:155]\n"
"v_mfma_f32_16x16x32_bf16 v[156:159], v[136:139], v[48:51], v[156:159]\n"
"v_mfma_f32_16x16x32_bf16 v[160:163], v[136:139], v[52:55], v[160:163]\n"
"s_waitcnt lgkmcnt(1)\n"
"ds_read_b128 v[136:139], v13 offset:59392\n"
"v_mfma_f32_16x16x32_bf16 v[152:155], v[8:11], v[56:59], v[152:155]\n"
"v_mfma_f32_16x16x32_bf16 v[156:159], v[8:11], v[60:63], v[156:159]\n"
"v_mfma_f32_16x16x32_bf16 v[160:163], v[8:11], v[64:67], v[160:163]\n"
"s_waitcnt lgkmcnt(1)\n"
"v_mfma_f32_16x16x32_bf16 v[152:155], v[132:135], v[68:71], v[152:155]\n"
"v_mfma_f32_16x16x32_bf16 v[156:159], v[132:135], v[72:75], v[156:159]\n"
"v_mfma_f32_16x16x32_bf16 v[160:163], v[132:135], v[76:79], v[160:163]\n"
"s_waitcnt lgkmcnt(0)\n"
"v_mov_b32 v8, 0x3f803f80\n"
"v_mov_b32 v9, 0x3f803f80\n"
"v_mfma_f32_16x16x32_bf16 v[152:155], v[136:139], v[80:83], v[152:155]\n"
"s_waitcnt vmcnt(5)\n"
"s_barrier\n"
"ds_read_b128 v[132:135], v17 offset:30720\n"
"v_mov_b32 v10, 0x3f803f80\n"
"v_mfma_f32_16x16x32_bf16 v[156:159], v[136:139], v[84:87], v[156:159]\n"
"s_and_b32 s41, 416, s55\n"
"v_add_u32 v177, s41, v15\n"
"v_mov_b32 v11, 0x3f803f80\n"
"v_mfma_f32_16x16x32_bf16 v[160:163], v[136:139], v[88:91], v[160:163]\n"
"ds_read_b128 v[136:139], v17 offset:31744\n"
"v_cvt_pk_bf16_f32 v186, v152, v153\n"
"v_cvt_pk_bf16_f32 v187, v154, v155\n"
"ds_read_u16 v177, v177 offset:608\n"
"v_cvt_pk_bf16_f32 v212, v156, v157\n"
"v_cvt_pk_bf16_f32 v213, v158, v159\n"
"s_waitcnt lgkmcnt(2)\n"
"v_mfma_f32_16x16x32_bf16 v[140:143], v[8:11], v[132:135], 0\n"
"v_mfma_f32_16x16x32_bf16 v[144:147], v[190:193], v[132:135], 0\n"
"ds_read_b128 v[190:193], v17 offset:32768\n"
"v_cvt_pk_bf16_f32 v238, v160, v161\n"
"v_mfma_f32_16x16x32_bf16 v[148:151], v[216:219], v[132:135], 0\n"
"v_cvt_pk_bf16_f32 v239, v162, v163\n"
"v_mfma_f32_16x16x32_bf16 v[216:219], v[242:245], v[132:135], 0\n"
"s_waitcnt lgkmcnt(1)\n"
"v_mad_u32_u24 v177, v177, s40, v16\n"
"v_mfma_f32_16x16x32_bf16 v[140:143], v[8:11], v[136:139], v[140:143]\n"
"s_add_u32 m0, s51, 151552\n"
"s_nop 0\n"
"global_load_lds_dwordx4 v177, s[56:57]\n"
"v_mfma_f32_16x16x32_bf16 v[144:147], v[182:185], v[136:139], v[144:147]\n"
"ds_read_b128 v[242:245], v17 offset:33792\n"
"v_mfma_f32_16x16x32_bf16 v[148:151], v[208:211], v[136:139], v[148:151]\n"
"ds_read_b64 v[134:135], v17 offset:8712\n"
"v_mfma_f32_16x16x32_bf16 v[216:219], v[234:237], v[136:139], v[216:219]\n"
"s_waitcnt lgkmcnt(2)\n"
"v_mfma_f32_16x16x32_bf16 v[140:143], v[8:11], v[190:193], v[140:143]\n"
"s_add_u32 m0, s51, 153600\n"
"s_nop 0\n"
"global_load_lds_dwordx4 v177, s[58:59]\n"
"v_mfma_f32_16x16x32_bf16 v[144:147], v[186:189], v[190:193], v[144:147]\n"
"v_mfma_f32_16x16x32_bf16 v[148:151], v[212:215], v[190:193], v[148:151]\n"
"v_mov_b32 v132, 0\n"
"v_mov_b32 v133, 0\n"
"v_mfma_f32_16x16x32_bf16 v[216:219], v[238:241], v[190:193], v[216:219]\n"
"s_waitcnt lgkmcnt(1)\n"
"v_mfma_f32_16x16x32_bf16 v[140:143], v[8:11], v[242:245], v[140:143]\n"
"s_add_u32 m0, s51, 155648\n"
"s_nop 0\n"
"global_load_lds_dwordx4 v177, s[60:61]\n"
"s_waitcnt lgkmcnt(0)\n"
"v_mfma_f32_16x16x32_bf16 v[140:143], v[8:11], v[132:135], v[140:143]\n"
"ds_read_b128 v[188:191], v14 offset:32256\n"
"v_mfma_f32_16x16x32_bf16 v[216:219], v[250:253], v[242:245], v[216:219]\n"
"v_mfma_f32_16x16x32_bf16 v[216:219], v[252:255], v[132:135], v[216:219]\n"
"ds_read_b128 v[8:11], v17 offset:53248\n"
"v_mfma_f32_16x16x32_bf16 v[144:147], v[198:201], v[242:245], v[144:147]\n"
"ds_read_b128 v[136:139], v14 offset:38400\n"
"v_mfma_f32_16x16x32_bf16 v[144:147], v[200:203], v[132:135], v[144:147]\n"
"v_mfma_f32_16x16x32_bf16 v[148:151], v[224:227], v[242:245], v[148:151]\n"
"ds_read_b128 v[240:243], v17 offset:54272\n"
"v_rcp_f32 v245, v140\n"
"v_mfma_f32_16x16x32_bf16 v[148:151], v[226:229], v[132:135], v[148:151]\n"
"ds_read_b128 v[132:135], v14 offset:44544\n"
"v_mul_f32 v216, v216, v245\n"
"v_mul_f32 v217, v217, v245\n"
"v_cvt_pk_bf16_f32 v214, v216, v217\n"
"s_waitcnt lgkmcnt(4)\n"
"v_mfma_f32_16x16x32_bf16 v[140:143], v[92:95], v[188:191], 0\n"
"v_mul_f32 v218, v218, v245\n"
"v_mul_f32 v219, v219, v245\n"
"v_mfma_f32_16x16x32_bf16 v[152:155], v[96:99], v[188:191], 0\n"
"s_waitcnt lgkmcnt(3)\n"
"ds_read_b128 v[156:159], v17 offset:55296\n"
"v_cvt_pk_bf16_f32 v215, v218, v219\n"
"v_mfma_f32_16x16x32_bf16 v[216:219], v[8:11], v[188:191], 0\n"
"s_waitcnt lgkmcnt(3)\n"
"ds_read_b128 v[188:191], v14 offset:50688\n"
"v_mul_f32 v144, v144, v245\n"
"v_mfma_f32_16x16x32_bf16 v[140:143], v[100:103], v[136:139], v[140:143]\n"
"v_mul_f32 v145, v145, v245\n"
"v_cvt_pk_bf16_f32 v144, v144, v145\n"
"v_mfma_f32_16x16x32_bf16 v[152:155], v[104:107], v[136:139], v[152:155]\n"
"s_waitcnt lgkmcnt(3)\n"
"ds_read_b128 v[8:11], v17 offset:56320\n"
"v_mul_f32 v146, v146, v245\n"
"v_mfma_f32_16x16x32_bf16 v[216:219], v[240:243], v[136:139], v[216:219]\n"
"s_waitcnt lgkmcnt(3)\n"
"ds_read_b128 v[240:243], v14 offset:2048\n"
"v_mul_f32 v147, v147, v245\n"
"v_cvt_pk_bf16_f32 v145, v146, v147\n"
"v_mfma_f32_16x16x32_bf16 v[140:143], v[0:3], v[132:135], v[140:143]\n"
"v_mul_f32 v148, v148, v245\n"
"v_mul_f32 v149, v149, v245\n"
"v_mfma_f32_16x16x32_bf16 v[152:155], v[108:111], v[132:135], v[152:155]\n"
"s_waitcnt lgkmcnt(3)\n"
"ds_read_b128 v[136:139], v17 offset:57344\n"
"v_cvt_pk_bf16_f32 v146, v148, v149\n"
"v_mul_f32 v150, v150, v245\n"
"v_mfma_f32_16x16x32_bf16 v[216:219], v[156:159], v[132:135], v[216:219]\n"
"s_waitcnt lgkmcnt(3)\n"
"ds_read_b128 v[132:135], v14 offset:3072\n"
"v_mul_f32 v151, v151, v245\n"
"v_mfma_f32_16x16x32_bf16 v[140:143], v[112:115], v[188:191], v[140:143]\n"
"v_cvt_pk_bf16_f32 v147, v150, v151\n"
"v_mfma_f32_16x16x32_bf16 v[152:155], v[116:119], v[188:191], v[152:155]\n"
"s_waitcnt lgkmcnt(3)\n"
"ds_read_b128 v[148:151], v17 offset:58368\n"
"v_mfma_f32_16x16x32_bf16 v[216:219], v[8:11], v[188:191], v[216:219]\n"
"s_waitcnt lgkmcnt(3)\n"
"ds_read_b128 v[188:191], v12 offset:0\n"
"v_mfma_f32_16x16x32_bf16 v[140:143], v[4:7], v[240:243], v[140:143]\n"
"v_mfma_f32_16x16x32_bf16 v[152:155], v[120:123], v[240:243], v[152:155]\n"
"s_waitcnt lgkmcnt(3)\n"
"v_mfma_f32_16x16x32_bf16 v[216:219], v[136:139], v[240:243], v[216:219]\n"
"s_waitcnt lgkmcnt(2)\n"
"ds_read_b128 v[240:243], v13 offset:0\n"
"v_bfe_u32 v245, v14, 3, 5\n"
"v_mfma_f32_16x16x32_bf16 v[140:143], v[124:127], v[132:135], v[140:143]\n"
"ds_read_u16 v245, v245 offset:864\n"
"v_mfma_f32_16x16x32_bf16 v[152:155], v[128:131], v[132:135], v[152:155]\n"
"s_barrier\n"
"s_waitcnt lgkmcnt(3)\n"
"v_mfma_f32_16x16x32_bf16 v[216:219], v[148:151], v[132:135], v[216:219]\n"
"s_andn2_b32 s41, 352, s55\n"
"v_add_u32 v244, s41, v15\n"
"s_waitcnt lgkmcnt(2)\n"
"v_mfma_f32_16x16x32_bf16 v[8:11], v[188:191], v[24:27], 0\n"
"ds_read_b128 v[132:135], v12 offset:2048\n"
"v_cvt_pk_bf16_f32 v140, v140, v141\n"
"v_mfma_f32_16x16x32_bf16 v[136:139], v[188:191], v[28:31], 0\n"
"ds_read_u16 v244, v244 offset:704\n"
"v_cvt_pk_bf16_f32 v141, v142, v143\n"
"v_mfma_f32_16x16x32_bf16 v[148:151], v[188:191], v[20:23], 0\n"
"s_waitcnt lgkmcnt(3)\n"
"ds_read_b128 v[188:191], v13 offset:2048\n"
"v_cvt_pk_bf16_f32 v142, v152, v153\n"
"v_cvt_pk_bf16_f32 v143, v154, v155\n"
"v_mfma_f32_16x16x32_bf16 v[8:11], v[240:243], v[36:39], v[8:11]\n"
"ds_write_b128 v17, v[144:147] offset:7680\n"
"v_mfma_f32_16x16x32_bf16 v[136:139], v[240:243], v[40:43], v[136:139]\n"
"s_waitcnt lgkmcnt(4)\n"
"v_mad_u32_u24 v193, v245, s40, v18\n"
"v_cvt_pk_bf16_f32 v216, v216, v217\n"
"v_mfma_f32_16x16x32_bf16 v[148:151], v[240:243], v[32:35], v[148:151]\n"
"global_store_dwordx4 v193, v[140:143], s[68:69] nt\n"
"s_waitcnt lgkmcnt(3)\n"
"v_mfma_f32_16x16x32_bf16 v[8:11], v[132:135], v[44:47], v[8:11]\n"
"s_waitcnt lgkmcnt(2)\n"
"ds_read_b128 v[240:243], v12 offset:4096\n"
"v_mad_u32_u24 v244, v244, s40, v16\n"
"v_add_u32 v217, s54, v14\n"
"v_mfma_f32_16x16x32_bf16 v[136:139], v[132:135], v[48:51], v[136:139]\n"
"s_add_u32 m0, s51, 139264\n"
"s_nop 0\n"
"global_load_lds_dwordx4 v244, s[56:57]\n"
"ds_write_b64 v217, v[214:215] offset:0\n"
"v_mfma_f32_16x16x32_bf16 v[148:151], v[132:135], v[52:55], v[148:151]\n"
"s_waitcnt lgkmcnt(3)\n"
"ds_read_b128 v[132:135], v13 offset:4096\n"
"v_mad_u32_u24 v245, v245, s40, v19\n"
"v_mfma_f32_16x16x32_bf16 v[8:11], v[188:191], v[56:59], v[8:11]\n"
"v_cvt_pk_bf16_f32 v217, v218, v219\n"
"v_mfma_f32_16x16x32_bf16 v[136:139], v[188:191], v[60:63], v[136:139]\n"
"s_add_u32 m0, s51, 141312\n"
"s_nop 0\n"
"global_load_lds_dwordx4 v244, s[58:59]\n"
"global_store_dwordx2 v245, v[216:217], s[70:71] nt\n"
"v_mfma_f32_16x16x32_bf16 v[148:151], v[188:191], v[64:67], v[148:151]\n"
"s_waitcnt lgkmcnt(2)\n"
"ds_read_b128 v[188:191], v12 offset:6144\n"
"v_mfma_f32_16x16x32_bf16 v[8:11], v[240:243], v[68:71], v[8:11]\n"
"v_mfma_f32_16x16x32_bf16 v[136:139], v[240:243], v[72:75], v[136:139]\n"
"s_add_u32 m0, s51, 143360\n"
"s_nop 0\n"
"global_load_lds_dwordx4 v244, s[60:61]\n"
"v_mfma_f32_16x16x32_bf16 v[148:151], v[240:243], v[76:79], v[148:151]\n"
"s_waitcnt lgkmcnt(1)\n"
"ds_read_b128 v[216:219], v13 offset:6144\n"
"v_mfma_f32_16x16x32_bf16 v[8:11], v[132:135], v[80:83], v[8:11]\n"
"v_mfma_f32_16x16x32_bf16 v[136:139], v[132:135], v[84:87], v[136:139]\n"
"v_mfma_f32_16x16x32_bf16 v[148:151], v[132:135], v[88:91], v[148:151]\n"
"s_waitcnt lgkmcnt(1)\n"
"ds_read_b128 v[242:245], v12 offset:8192\n"
"v_mfma_f32_16x16x32_bf16 v[132:135], v[188:191], v[24:27], 0\n"
"v_mfma_f32_16x16x32_bf16 v[140:143], v[188:191], v[28:31], 0\n"
"v_mfma_f32_16x16x32_bf16 v[144:147], v[188:191], v[20:23], 0\n"
"s_waitcnt lgkmcnt(1)\n"
"ds_read_b128 v[190:193], v13 offset:8192\n"
"v_mfma_f32_16x16x32_bf16 v[132:135], v[216:219], v[36:39], v[132:135]\n"
"v_cvt_pk_bf16_f32 v188, v8, v9\n"
"v_cvt_pk_bf16_f32 v189, v10, v11\n"
"v_cvt_pk_bf16_f32 v214, v136, v137\n"
"v_cvt_pk_bf16_f32 v215, v138, v139\n"
"v_mfma_f32_16x16x32_bf16 v[140:143], v[216:219], v[40:43], v[140:143]\n"
"v_cvt_pk_bf16_f32 v240, v148, v149\n"
"v_cvt_pk_bf16_f32 v241, v150, v151\n"
"v_mfma_f32_16x16x32_bf16 v[144:147], v[216:219], v[32:35], v[144:147]\n"
"s_waitcnt lgkmcnt(1)\n"
"ds_read_b128 v[216:219], v12 offset:10240\n"
"v_mfma_f32_16x16x32_bf16 v[132:135], v[242:245], v[44:47], v[132:135]\n"
"v_mfma_f32_16x16x32_bf16 v[140:143], v[242:245], v[48:51], v[140:143]\n"
"v_mfma_f32_16x16x32_bf16 v[144:147], v[242:245], v[52:55], v[144:147]\n"
"s_waitcnt lgkmcnt(1)\n"
"ds_read_b128 v[242:245], v13 offset:10240\n"
"v_mfma_f32_16x16x32_bf16 v[132:135], v[190:193], v[56:59], v[132:135]\n"
"v_mfma_f32_16x16x32_bf16 v[140:143], v[190:193], v[60:63], v[140:143]\n"
"v_mfma_f32_16x16x32_bf16 v[144:147], v[190:193], v[64:67], v[144:147]\n"
"s_waitcnt lgkmcnt(1)\n"
"v_mfma_f32_16x16x32_bf16 v[132:135], v[216:219], v[68:71], v[132:135]\n"
"v_mfma_f32_16x16x32_bf16 v[140:143], v[216:219], v[72:75], v[140:143]\n"
"v_mfma_f32_16x16x32_bf16 v[144:147], v[216:219], v[76:79], v[144:147]\n"
"s_waitcnt lgkmcnt(0)\n"
"v_mov_b32 v8, 0x3f803f80\n"
"v_mov_b32 v9, 0x3f803f80\n"
"v_mfma_f32_16x16x32_bf16 v[132:135], v[242:245], v[80:83], v[132:135]\n"
"s_waitcnt vmcnt(10)\n"
"s_barrier\n"
"ds_read_b128 v[136:139], v17 offset:28672\n"
"v_mov_b32 v10, 0x3f803f80\n"
"v_mfma_f32_16x16x32_bf16 v[140:143], v[242:245], v[84:87], v[140:143]\n"
"s_and_b32 s41, 32, s55\n"
"v_add_u32 v193, s41, v15\n"
"v_mov_b32 v11, 0x3f803f80\n"
"v_mfma_f32_16x16x32_bf16 v[144:147], v[242:245], v[88:91], v[144:147]\n"
"ds_read_b128 v[148:151], v17 offset:29696\n"
"v_cvt_pk_bf16_f32 v190, v132, v133\n"
"v_cvt_pk_bf16_f32 v191, v134, v135\n"
"ds_read_b128 v[154:157], v17 offset:32768\n"
"v_cvt_pk_bf16_f32 v216, v140, v141\n"
"v_cvt_pk_bf16_f32 v217, v142, v143\n"
"ds_read_u16 v193, v193 offset:640\n"
"s_waitcnt lgkmcnt(3)\n"
"v_mfma_f32_16x16x32_bf16 v[132:135], v[8:11], v[136:139], 0\n"
"v_cvt_pk_bf16_f32 v242, v144, v145\n"
"v_cvt_pk_bf16_f32 v243, v146, v147\n"
"v_mfma_f32_16x16x32_bf16 v[140:143], v[178:181], v[136:139], 0\n"
"v_mfma_f32_16x16x32_bf16 v[144:147], v[204:207], v[136:139], 0\n"
"v_mfma_f32_16x16x32_bf16 v[160:163], v[230:233], v[136:139], 0\n"
"s_waitcnt lgkmcnt(2)\n"
"v_mfma_f32_16x16x32_bf16 v[132:135], v[8:11], v[148:151], v[132:135]\n"
"s_waitcnt lgkmcnt(0)\n"
"v_mad_u32_u24 v193, v193, s40, v16\n"
"v_mov_b32 v152, 0\n"
"v_mfma_f32_16x16x32_bf16 v[132:135], v[8:11], v[154:157], v[132:135]\n"
"s_add_u32 m0, s51, 102400\n"
"s_nop 0\n"
"global_load_lds_dwordx4 v193, s[56:57]\n"
"v_mfma_f32_16x16x32_bf16 v[160:163], v[246:249], v[148:151], v[160:163]\n"
"v_mov_b32 v153, 0\n"
"v_mov_b32 v158, 0\n"
"v_mov_b32 v159, 0\n"
"v_mfma_f32_16x16x32_bf16 v[160:163], v[232:235], v[152:155], v[160:163]\n"
"s_nop 0\n"
"v_mfma_f32_16x16x32_bf16 v[160:163], v[250:253], v[156:159], v[160:163]\n"
"ds_read_b128 v[8:11], v14 offset:32256\n"
"s_add_u32 m0, s51, 104448\n"
"s_nop 0\n"
"global_load_lds_dwordx4 v193, s[58:59]\n"
"v_mfma_f32_16x16x32_bf16 v[140:143], v[194:197], v[148:151], v[140:143]\n"
"v_rcp_f32 v192, v132\n"
"v_mfma_f32_16x16x32_bf16 v[140:143], v[180:183], v[152:155], v[140:143]\n"
"ds_read_b128 v[132:135], v17 offset:53248\n"
"v_mfma_f32_16x16x32_bf16 v[140:143], v[198:201], v[156:159], v[140:143]\n"
"ds_read_b128 v[136:139], v14 offset:38400\n"
"v_mul_f32 v160, v160, v192\n"
"v_mfma_f32_16x16x32_bf16 v[144:147], v[220:223], v[148:151], v[144:147]\n"
"s_add_u32 m0, s51, 106496\n"
"s_nop 0\n"
"global_load_lds_dwordx4 v193, s[60:61]\n"
"v_mfma_f32_16x16x32_bf16 v[144:147], v[206:209], v[152:155], v[144:147]\n"
"ds_read_b128 v[148:151], v17 offset:54272\n"
"v_mul_f32 v161, v161, v192\n"
"v_mfma_f32_16x16x32_bf16 v[144:147], v[224:227], v[156:159], v[144:147]\n"
"s_waitcnt lgkmcnt(3)\n"
"ds_read_b128 v[152:155], v14 offset:44544\n"
"v_cvt_pk_bf16_f32 v244, v160, v161\n"
"v_mfma_f32_16x16x32_bf16 v[156:159], v[92:95], v[8:11], 0\n"
"v_mul_f32 v162, v162, v192\n"
"v_mul_f32 v163, v163, v192\n"
"v_mfma_f32_16x16x32_bf16 v[164:167], v[96:99], v[8:11], 0\n"
"s_waitcnt lgkmcnt(3)\n"
"ds_read_b128 v[168:171], v17 offset:55296\n"
"v_cvt_pk_bf16_f32 v245, v162, v163\n"
"v_mfma_f32_16x16x32_bf16 v[160:163], v[132:135], v[8:11], 0\n"
"s_waitcnt lgkmcnt(3)\n"
"ds_read_b128 v[8:11], v14 offset:50688\n"
"v_mul_f32 v140, v140, v192\n"
"v_mfma_f32_16x16x32_bf16 v[156:159], v[100:103], v[136:139], v[156:159]\n"
"v_mul_f32 v141, v141, v192\n"
"v_cvt_pk_bf16_f32 v140, v140, v141\n"
"v_mfma_f32_16x16x32_bf16 v[164:167], v[104:107], v[136:139], v[164:167]\n"
"ds_read_b128 v[132:135], v17 offset:56320\n"
"v_mul_f32 v142, v142, v192\n"
"v_mul_f32 v143, v143, v192\n"
"v_cvt_pk_bf16_f32 v141, v142, v143\n"
"s_waitcnt lgkmcnt(4)\n"
"v_mfma_f32_16x16x32_bf16 v[160:163], v[148:151], v[136:139], v[160:163]\n"
"s_waitcnt lgkmcnt(3)\n"
"ds_read_b128 v[136:139], v14 offset:2048\n"
"v_mul_f32 v144, v144, v192\n"
"v_mfma_f32_16x16x32_bf16 v[156:159], v[0:3], v[152:155], v[156:159]\n"
"v_mul_f32 v145, v145, v192\n"
"v_cvt_pk_bf16_f32 v142, v144, v145\n"
"v_mfma_f32_16x16x32_bf16 v[164:167], v[108:111], v[152:155], v[164:167]\n"
"s_waitcnt lgkmcnt(3)\n"
"ds_read_b128 v[148:151], v17 offset:57344\n"
"v_mul_f32 v146, v146, v192\n"
"v_mfma_f32_16x16x32_bf16 v[160:163], v[168:171], v[152:155], v[160:163]\n"
"s_waitcnt lgkmcnt(3)\n"
"ds_read_b128 v[152:155], v14 offset:3072\n"
"v_mul_f32 v147, v147, v192\n"
"v_mfma_f32_16x16x32_bf16 v[156:159], v[112:115], v[8:11], v[156:159]\n"
"v_cvt_pk_bf16_f32 v143, v146, v147\n"
"v_mfma_f32_16x16x32_bf16 v[164:167], v[116:119], v[8:11], v[164:167]\n"
"s_waitcnt lgkmcnt(3)\n"
"ds_read_b128 v[144:147], v17 offset:58368\n"
"v_mfma_f32_16x16x32_bf16 v[160:163], v[132:135], v[8:11], v[160:163]\n"
"s_waitcnt lgkmcnt(3)\n"
"ds_read_b128 v[8:11], v12 offset:12288\n"
"v_mfma_f32_16x16x32_bf16 v[156:159], v[4:7], v[136:139], v[156:159]\n"
"v_mfma_f32_16x16x32_bf16 v[164:167], v[120:123], v[136:139], v[164:167]\n"
"s_waitcnt lgkmcnt(3)\n"
"v_mfma_f32_16x16x32_bf16 v[160:163], v[148:151], v[136:139], v[160:163]\n"
"s_waitcnt lgkmcnt(2)\n"
"ds_read_b128 v[132:135], v13 offset:12288\n"
"v_bfe_u32 v219, v14, 3, 5\n"
"v_mfma_f32_16x16x32_bf16 v[156:159], v[124:127], v[152:155], v[156:159]\n"
"ds_read_u16 v219, v219 offset:896\n"
"v_mfma_f32_16x16x32_bf16 v[164:167], v[128:131], v[152:155], v[164:167]\n"
"s_barrier\n"
"s_waitcnt lgkmcnt(3)\n"
"v_mfma_f32_16x16x32_bf16 v[160:163], v[144:147], v[152:155], v[160:163]\n"
"s_waitcnt lgkmcnt(2)\n"
"ds_read_b128 v[136:139], v12 offset:14336\n"
"v_mfma_f32_16x16x32_bf16 v[144:147], v[8:11], v[24:27], 0\n"
"ds_write_b128 v17, v[140:143] offset:7680\n"
"v_mfma_f32_16x16x32_bf16 v[140:143], v[8:11], v[28:31], 0\n"
"v_cvt_pk_bf16_f32 v156, v156, v157\n"
"v_cvt_pk_bf16_f32 v157, v158, v159\n"
"v_mfma_f32_16x16x32_bf16 v[148:151], v[8:11], v[20:23], 0\n"
"s_waitcnt lgkmcnt(3)\n"
"ds_read_b128 v[8:11], v13 offset:14336\n"
"v_cvt_pk_bf16_f32 v158, v164, v165\n"
"v_mfma_f32_16x16x32_bf16 v[144:147], v[132:135], v[36:39], v[144:147]\n"
"s_waitcnt lgkmcnt(3)\n"
"v_cvt_pk_bf16_f32 v159, v166, v167\n"
"v_mad_u32_u24 v218, v219, s40, v18\n"
"v_mfma_f32_16x16x32_bf16 v[140:143], v[132:135], v[40:43], v[140:143]\n"
"global_store_dwordx4 v218, v[156:159], s[68:69] nt\n"
"v_mfma_f32_16x16x32_bf16 v[148:151], v[132:135], v[32:35], v[148:151]\n"
"s_waitcnt lgkmcnt(2)\n"
"ds_read_b128 v[132:135], v12 offset:16384\n"
"v_add_u32 v218, s54, v14\n"
"v_mfma_f32_16x16x32_bf16 v[144:147], v[136:139], v[44:47], v[144:147]\n"
"ds_write_b64 v218, v[244:245] offset:0\n"
"v_mfma_f32_16x16x32_bf16 v[140:143], v[136:139], v[48:51], v[140:143]\n"
"v_cvt_pk_bf16_f32 v160, v160, v161\n"
"v_mad_u32_u24 v219, v219, s40, v19\n"
"v_mfma_f32_16x16x32_bf16 v[148:151], v[136:139], v[52:55], v[148:151]\n"
"s_waitcnt lgkmcnt(2)\n"
"ds_read_b128 v[136:139], v13 offset:16384\n"
"v_cvt_pk_bf16_f32 v161, v162, v163\n"
"v_mfma_f32_16x16x32_bf16 v[144:147], v[8:11], v[56:59], v[144:147]\n"
"global_store_dwordx2 v219, v[160:161], s[70:71] nt\n"
"v_mfma_f32_16x16x32_bf16 v[140:143], v[8:11], v[60:63], v[140:143]\n"
"v_mfma_f32_16x16x32_bf16 v[148:151], v[8:11], v[64:67], v[148:151]\n"
"s_waitcnt lgkmcnt(2)\n"
"v_mfma_f32_16x16x32_bf16 v[144:147], v[132:135], v[68:71], v[144:147]\n"
"v_mfma_f32_16x16x32_bf16 v[140:143], v[132:135], v[72:75], v[140:143]\n"
"v_mfma_f32_16x16x32_bf16 v[148:151], v[132:135], v[76:79], v[148:151]\n"
"s_waitcnt lgkmcnt(0)\n"
"v_mov_b32 v8, 0x3f803f80\n"
"v_mov_b32 v9, 0x3f803f80\n"
"v_mov_b32 v10, 0x3f803f80\n"
"v_mfma_f32_16x16x32_bf16 v[144:147], v[136:139], v[80:83], v[144:147]\n"
"s_waitcnt vmcnt(10)\n"
"s_barrier\n"
"ds_read_b128 v[132:135], v17 offset:30720\n"
"v_mov_b32 v11, 0x3f803f80\n"
"v_mfma_f32_16x16x32_bf16 v[140:143], v[136:139], v[84:87], v[140:143]\n"
"s_andn2_b32 s41, 352, s55\n"
"v_add_u32 v177, s41, v15\n"
"v_mfma_f32_16x16x32_bf16 v[148:151], v[136:139], v[88:91], v[148:151]\n"
"ds_read_b128 v[136:139], v17 offset:31744\n"
"v_cvt_pk_bf16_f32 v192, v144, v145\n"
"v_cvt_pk_bf16_f32 v193, v146, v147\n"
"ds_read_u16 v177, v177 offset:736\n"
"s_waitcnt lgkmcnt(2)\n"
"v_mfma_f32_16x16x32_bf16 v[144:147], v[8:11], v[132:135], 0\n"
"v_cvt_pk_bf16_f32 v218, v140, v141\n"
"v_cvt_pk_bf16_f32 v219, v142, v143\n"
"v_mfma_f32_16x16x32_bf16 v[140:143], v[178:181], v[132:135], 0\n"
"ds_read_b128 v[152:155], v17 offset:32768\n"
"v_cvt_pk_bf16_f32 v244, v148, v149\n"
"v_mfma_f32_16x16x32_bf16 v[156:159], v[204:207], v[132:135], 0\n"
"v_cvt_pk_bf16_f32 v245, v150, v151\n"
"v_mfma_f32_16x16x32_bf16 v[148:151], v[230:233], v[132:135], 0\n"
"s_waitcnt lgkmcnt(1)\n"
"v_mad_u32_u24 v177, v177, s40, v16\n"
"v_mfma_f32_16x16x32_bf16 v[144:147], v[8:11], v[136:139], v[144:147]\n"
"s_add_u32 m0, s51, 114688\n"
"s_nop 0\n"
"global_load_lds_dwordx4 v177, s[56:57]\n"
"v_mfma_f32_16x16x32_bf16 v[140:143], v[188:191], v[136:139], v[140:143]\n"
"ds_read_b128 v[132:135], v17 offset:33792\n"
"v_mfma_f32_16x16x32_bf16 v[156:159], v[214:217], v[136:139], v[156:159]\n"
"ds_read_b64 v[162:163], v17 offset:8712\n"
"v_mfma_f32_16x16x32_bf16 v[148:151], v[240:243], v[136:139], v[148:151]\n"
"s_waitcnt lgkmcnt(2)\n"
"v_mfma_f32_16x16x32_bf16 v[144:147], v[8:11], v[152:155], v[144:147]\n"
"s_add_u32 m0, s51, 116736\n"
"s_nop 0\n"
"global_load_lds_dwordx4 v177, s[58:59]\n"
"v_mfma_f32_16x16x32_bf16 v[140:143], v[192:195], v[152:155], v[140:143]\n"
"v_mfma_f32_16x16x32_bf16 v[156:159], v[218:221], v[152:155], v[156:159]\n"
"v_mov_b32 v160, 0\n"
"v_mov_b32 v161, 0\n"
"v_mfma_f32_16x16x32_bf16 v[148:151], v[244:247], v[152:155], v[148:151]\n"
"s_waitcnt lgkmcnt(1)\n"
"v_mfma_f32_16x16x32_bf16 v[144:147], v[8:11], v[132:135], v[144:147]\n"
"s_add_u32 m0, s51, 118784\n"
"s_nop 0\n"
"global_load_lds_dwordx4 v177, s[60:61]\n"
"s_waitcnt lgkmcnt(0)\n"
"v_mfma_f32_16x16x32_bf16 v[144:147], v[8:11], v[160:163], v[144:147]\n"
"ds_read_b128 v[8:11], v14 offset:32256\n"
"v_mfma_f32_16x16x32_bf16 v[148:151], v[248:251], v[132:135], v[148:151]\n"
"v_mfma_f32_16x16x32_bf16 v[148:151], v[232:235], v[160:163], v[148:151]\n"
"ds_read_b128 v[136:139], v17 offset:53248\n"
"v_mfma_f32_16x16x32_bf16 v[140:143], v[196:199], v[132:135], v[140:143]\n"
"ds_read_b128 v[152:155], v14 offset:38400\n"
"v_mfma_f32_16x16x32_bf16 v[140:143], v[180:183], v[160:163], v[140:143]\n"
"v_mfma_f32_16x16x32_bf16 v[156:159], v[222:225], v[132:135], v[156:159]\n"
"ds_read_b128 v[132:135], v17 offset:54272\n"
"v_rcp_f32 v247, v144\n"
"v_mfma_f32_16x16x32_bf16 v[156:159], v[206:209], v[160:163], v[156:159]\n"
"ds_read_b128 v[144:147], v14 offset:44544\n"
"v_mul_f32 v148, v148, v247\n"
"v_mul_f32 v149, v149, v247\n"
"v_cvt_pk_bf16_f32 v220, v148, v149\n"
"s_waitcnt lgkmcnt(4)\n"
"v_mfma_f32_16x16x32_bf16 v[160:163], v[92:95], v[8:11], 0\n"
"v_mul_f32 v150, v150, v247\n"
"v_mul_f32 v151, v151, v247\n"
"v_mfma_f32_16x16x32_bf16 v[164:167], v[96:99], v[8:11], 0\n"
"s_waitcnt lgkmcnt(3)\n"
"ds_read_b128 v[168:171], v17 offset:55296\n"
"v_cvt_pk_bf16_f32 v221, v150, v151\n"
"v_mfma_f32_16x16x32_bf16 v[148:151], v[136:139], v[8:11], 0\n"
"s_waitcnt lgkmcnt(3)\n"
"ds_read_b128 v[8:11], v14 offset:50688\n"
"v_mul_f32 v140, v140, v247\n"
"v_mfma_f32_16x16x32_bf16 v[160:163], v[100:103], v[152:155], v[160:163]\n"
"v_mul_f32 v141, v141, v247\n"
"v_cvt_pk_bf16_f32 v140, v140, v141\n"
"v_mfma_f32_16x16x32_bf16 v[164:167], v[104:107], v[152:155], v[164:167]\n"
"s_waitcnt lgkmcnt(3)\n"
"ds_read_b128 v[136:139], v17 offset:56320\n"
"v_mul_f32 v142, v142, v247\n"
"v_mfma_f32_16x16x32_bf16 v[148:151], v[132:135], v[152:155], v[148:151]\n"
"s_waitcnt lgkmcnt(3)\n"
"ds_read_b128 v[132:135], v14 offset:2048\n"
"v_mul_f32 v143, v143, v247\n"
"v_cvt_pk_bf16_f32 v141, v142, v143\n"
"v_mfma_f32_16x16x32_bf16 v[160:163], v[0:3], v[144:147], v[160:163]\n"
"v_mul_f32 v156, v156, v247\n"
"v_mul_f32 v157, v157, v247\n"
"v_mfma_f32_16x16x32_bf16 v[164:167], v[108:111], v[144:147], v[164:167]\n"
"s_waitcnt lgkmcnt(3)\n"
"ds_read_b128 v[152:155], v17 offset:57344\n"
"v_cvt_pk_bf16_f32 v142, v156, v157\n"
"v_mul_f32 v158, v158, v247\n"
"v_mfma_f32_16x16x32_bf16 v[148:151], v[168:171], v[144:147], v[148:151]\n"
"s_waitcnt lgkmcnt(3)\n"
"ds_read_b128 v[144:147], v14 offset:3072\n"
"v_mul_f32 v159, v159, v247\n"
"v_mfma_f32_16x16x32_bf16 v[160:163], v[112:115], v[8:11], v[160:163]\n"
"v_cvt_pk_bf16_f32 v143, v158, v159\n"
"v_mfma_f32_16x16x32_bf16 v[164:167], v[116:119], v[8:11], v[164:167]\n"
"s_waitcnt lgkmcnt(3)\n"
"ds_read_b128 v[156:159], v17 offset:58368\n"
"v_mfma_f32_16x16x32_bf16 v[148:151], v[136:139], v[8:11], v[148:151]\n"
"s_waitcnt lgkmcnt(3)\n"
"ds_read_b128 v[8:11], v12 offset:24576\n"
"v_mfma_f32_16x16x32_bf16 v[160:163], v[4:7], v[132:135], v[160:163]\n"
"v_mfma_f32_16x16x32_bf16 v[164:167], v[120:123], v[132:135], v[164:167]\n"
"s_waitcnt lgkmcnt(3)\n"
"v_mfma_f32_16x16x32_bf16 v[148:151], v[152:155], v[132:135], v[148:151]\n"
"s_waitcnt lgkmcnt(2)\n"
"ds_read_b128 v[132:135], v13 offset:24576\n"
"v_bfe_u32 v247, v14, 3, 5\n"
"v_mfma_f32_16x16x32_bf16 v[160:163], v[124:127], v[144:147], v[160:163]\n"
"ds_read_u16 v247, v247 offset:928\n"
"v_mfma_f32_16x16x32_bf16 v[164:167], v[128:131], v[144:147], v[164:167]\n"
"s_barrier\n"
"s_waitcnt lgkmcnt(3)\n"
"v_mfma_f32_16x16x32_bf16 v[148:151], v[156:159], v[144:147], v[148:151]\n"
"s_waitcnt lgkmcnt(2)\n"
"ds_read_b128 v[136:139], v12 offset:26624\n"
"v_mfma_f32_16x16x32_bf16 v[144:147], v[8:11], v[24:27], 0\n"
"ds_write_b128 v17, v[140:143] offset:7680\n"
"v_mfma_f32_16x16x32_bf16 v[140:143], v[8:11], v[28:31], 0\n"
"v_cvt_pk_bf16_f32 v160, v160, v161\n"
"v_cvt_pk_bf16_f32 v161, v162, v163\n"
"v_mfma_f32_16x16x32_bf16 v[152:155], v[8:11], v[20:23], 0\n"
"s_waitcnt lgkmcnt(3)\n"
"ds_read_b128 v[8:11], v13 offset:26624\n"
"v_cvt_pk_bf16_f32 v162, v164, v165\n"
"v_mfma_f32_16x16x32_bf16 v[144:147], v[132:135], v[36:39], v[144:147]\n"
"s_waitcnt lgkmcnt(3)\n"
"v_cvt_pk_bf16_f32 v163, v166, v167\n"
"v_mad_u32_u24 v246, v247, s40, v18\n"
"v_mfma_f32_16x16x32_bf16 v[140:143], v[132:135], v[40:43], v[140:143]\n"
"global_store_dwordx4 v246, v[160:163], s[68:69] nt\n"
"v_mfma_f32_16x16x32_bf16 v[152:155], v[132:135], v[32:35], v[152:155]\n"
"s_waitcnt lgkmcnt(2)\n"
"ds_read_b128 v[132:135], v12 offset:28672\n"
"v_add_u32 v246, s54, v14\n"
"v_mfma_f32_16x16x32_bf16 v[144:147], v[136:139], v[44:47], v[144:147]\n"
"ds_write_b64 v246, v[220:221] offset:0\n"
"v_mfma_f32_16x16x32_bf16 v[140:143], v[136:139], v[48:51], v[140:143]\n"
"v_cvt_pk_bf16_f32 v148, v148, v149\n"
"v_mad_u32_u24 v247, v247, s40, v19\n"
"v_mfma_f32_16x16x32_bf16 v[152:155], v[136:139], v[52:55], v[152:155]\n"
"s_waitcnt lgkmcnt(2)\n"
"ds_read_b128 v[136:139], v13 offset:28672\n"
"v_cvt_pk_bf16_f32 v149, v150, v151\n"
"v_mfma_f32_16x16x32_bf16 v[144:147], v[8:11], v[56:59], v[144:147]\n"
"global_store_dwordx2 v247, v[148:149], s[70:71] nt\n"
"v_mfma_f32_16x16x32_bf16 v[140:143], v[8:11], v[60:63], v[140:143]\n"
"v_mfma_f32_16x16x32_bf16 v[152:155], v[8:11], v[64:67], v[152:155]\n"
"s_waitcnt lgkmcnt(2)\n"
"v_mfma_f32_16x16x32_bf16 v[144:147], v[132:135], v[68:71], v[144:147]\n"
"v_mfma_f32_16x16x32_bf16 v[140:143], v[132:135], v[72:75], v[140:143]\n"
"v_mfma_f32_16x16x32_bf16 v[152:155], v[132:135], v[76:79], v[152:155]\n"
"s_waitcnt lgkmcnt(0)\n"
"v_mov_b32 v8, 0x3f803f80\n"
"v_mov_b32 v9, 0x3f803f80\n"
"v_mov_b32 v10, 0x3f803f80\n"
"v_mfma_f32_16x16x32_bf16 v[144:147], v[136:139], v[80:83], v[144:147]\n"
"s_waitcnt vmcnt(10)\n"
"s_barrier\n"
"ds_read_b128 v[132:135], v17 offset:28672\n"
"v_mov_b32 v11, 0x3f803f80\n"
"v_mfma_f32_16x16x32_bf16 v[140:143], v[136:139], v[84:87], v[140:143]\n"
"s_andn2_b32 s41, 352, s55\n"
"v_add_u32 v177, s41, v15\n"
"v_mfma_f32_16x16x32_bf16 v[152:155], v[136:139], v[88:91], v[152:155]\n"
"ds_read_b128 v[136:139], v17 offset:29696\n"
"v_cvt_pk_bf16_f32 v194, v144, v145\n"
"v_cvt_pk_bf16_f32 v195, v146, v147\n"
"ds_read_u16 v177, v177 offset:768\n"
"s_waitcnt lgkmcnt(2)\n"
"v_mfma_f32_16x16x32_bf16 v[144:147], v[8:11], v[132:135], 0\n"
"v_cvt_pk_bf16_f32 v220, v140, v141\n"
"v_cvt_pk_bf16_f32 v221, v142, v143\n"
"v_mfma_f32_16x16x32_bf16 v[140:143], v[180:183], v[132:135], 0\n"
"ds_read_b128 v[148:151], v17 offset:32768\n"
"v_cvt_pk_bf16_f32 v246, v152, v153\n"
"v_mfma_f32_16x16x32_bf16 v[156:159], v[206:209], v[132:135], 0\n"
"v_cvt_pk_bf16_f32 v247, v154, v155\n"
"v_mfma_f32_16x16x32_bf16 v[152:155], v[232:235], v[132:135], 0\n"
"s_waitcnt lgkmcnt(1)\n"
"v_mad_u32_u24 v177, v177, s40, v16\n"
"v_mfma_f32_16x16x32_bf16 v[144:147], v[8:11], v[136:139], v[144:147]\n"
"s_add_u32 m0, s51, 126976\n"
"s_nop 0\n"
"global_load_lds_dwordx4 v177, s[56:57]\n"
"v_mfma_f32_16x16x32_bf16 v[140:143], v[190:193], v[136:139], v[140:143]\n"
"ds_read_b128 v[132:135], v17 offset:33792\n"
"v_mfma_f32_16x16x32_bf16 v[156:159], v[216:219], v[136:139], v[156:159]\n"
"ds_read_b64 v[162:163], v17 offset:8704\n"
"v_mfma_f32_16x16x32_bf16 v[152:155], v[242:245], v[136:139], v[152:155]\n"
"s_waitcnt lgkmcnt(2)\n"
"v_mfma_f32_16x16x32_bf16 v[144:147], v[8:11], v[148:151], v[144:147]\n"
"s_add_u32 m0, s51, 129024\n"
"s_nop 0\n"
"global_load_lds_dwordx4 v177, s[58:59]\n"
"v_mfma_f32_16x16x32_bf16 v[140:143], v[194:197], v[148:151], v[140:143]\n"
"v_mfma_f32_16x16x32_bf16 v[156:159], v[220:223], v[148:151], v[156:159]\n"
"v_mov_b32 v160, 0\n"
"v_mov_b32 v161, 0\n"
"v_mfma_f32_16x16x32_bf16 v[152:155], v[246:249], v[148:151], v[152:155]\n"
"s_waitcnt lgkmcnt(1)\n"
"v_mfma_f32_16x16x32_bf16 v[144:147], v[8:11], v[132:135], v[144:147]\n"
"s_add_u32 m0, s51, 131072\n"
"s_nop 0\n"
"global_load_lds_dwordx4 v177, s[60:61]\n"
"s_waitcnt lgkmcnt(0)\n"
"v_mfma_f32_16x16x32_bf16 v[144:147], v[8:11], v[160:163], v[144:147]\n"
"ds_read_b128 v[8:11], v14 offset:32256\n"
"v_mfma_f32_16x16x32_bf16 v[152:155], v[250:253], v[132:135], v[152:155]\n"
"v_mfma_f32_16x16x32_bf16 v[152:155], v[234:237], v[160:163], v[152:155]\n"
"ds_read_b128 v[136:139], v17 offset:53248\n"
"v_mfma_f32_16x16x32_bf16 v[140:143], v[198:201], v[132:135], v[140:143]\n"
"ds_read_b128 v[148:151], v14 offset:38400\n"
"v_mfma_f32_16x16x32_bf16 v[140:143], v[182:185], v[160:163], v[140:143]\n"
"v_mfma_f32_16x16x32_bf16 v[156:159], v[224:227], v[132:135], v[156:159]\n"
"ds_read_b128 v[132:135], v17 offset:54272\n"
"v_rcp_f32 v249, v144\n"
"v_mfma_f32_16x16x32_bf16 v[156:159], v[208:211], v[160:163], v[156:159]\n"
"ds_read_b128 v[144:147], v14 offset:44544\n"
"v_mul_f32 v152, v152, v249\n"
"v_mul_f32 v153, v153, v249\n"
"v_cvt_pk_bf16_f32 v222, v152, v153\n"
"s_waitcnt lgkmcnt(4)\n"
"v_mfma_f32_16x16x32_bf16 v[160:163], v[92:95], v[8:11], 0\n"
"v_mul_f32 v154, v154, v249\n"
"v_mul_f32 v155, v155, v249\n"
"v_mfma_f32_16x16x32_bf16 v[164:167], v[96:99], v[8:11], 0\n"
"s_waitcnt lgkmcnt(3)\n"
"ds_read_b128 v[168:171], v17 offset:55296\n"
"v_cvt_pk_bf16_f32 v223, v154, v155\n"
"v_mfma_f32_16x16x32_bf16 v[152:155], v[136:139], v[8:11], 0\n"
"s_waitcnt lgkmcnt(3)\n"
"ds_read_b128 v[8:11], v14 offset:50688\n"
"v_mul_f32 v140, v140, v249\n"
"v_mfma_f32_16x16x32_bf16 v[160:163], v[100:103], v[148:151], v[160:163]\n"
"v_mul_f32 v141, v141, v249\n"
"v_cvt_pk_bf16_f32 v140, v140, v141\n"
"v_mfma_f32_16x16x32_bf16 v[164:167], v[104:107], v[148:151], v[164:167]\n"
"s_waitcnt lgkmcnt(3)\n"
"ds_read_b128 v[136:139], v17 offset:56320\n"
"v_mul_f32 v142, v142, v249\n"
"v_mfma_f32_16x16x32_bf16 v[152:155], v[132:135], v[148:151], v[152:155]\n"
"s_waitcnt lgkmcnt(3)\n"
"ds_read_b128 v[132:135], v14 offset:2048\n"
"v_mul_f32 v143, v143, v249\n"
"v_cvt_pk_bf16_f32 v141, v142, v143\n"
"v_mfma_f32_16x16x32_bf16 v[160:163], v[0:3], v[144:147], v[160:163]\n"
"v_mul_f32 v156, v156, v249\n"
"v_mul_f32 v157, v157, v249\n"
"v_mfma_f32_16x16x32_bf16 v[164:167], v[108:111], v[144:147], v[164:167]\n"
"s_waitcnt lgkmcnt(3)\n"
"ds_read_b128 v[148:151], v17 offset:57344\n"
"v_cvt_pk_bf16_f32 v142, v156, v157\n"
"v_mul_f32 v158, v158, v249\n"
"v_mfma_f32_16x16x32_bf16 v[152:155], v[168:171], v[144:147], v[152:155]\n"
"s_waitcnt lgkmcnt(3)\n"
"ds_read_b128 v[144:147], v14 offset:3072\n"
"v_mul_f32 v159, v159, v249\n"
"v_mfma_f32_16x16x32_bf16 v[160:163], v[112:115], v[8:11], v[160:163]\n"
"v_cvt_pk_bf16_f32 v143, v158, v159\n"
"v_mfma_f32_16x16x32_bf16 v[164:167], v[116:119], v[8:11], v[164:167]\n"
"s_waitcnt lgkmcnt(3)\n"
"ds_read_b128 v[156:159], v17 offset:58368\n"
"v_mfma_f32_16x16x32_bf16 v[152:155], v[136:139], v[8:11], v[152:155]\n"
"s_waitcnt lgkmcnt(3)\n"
"ds_read_b128 v[8:11], v12 offset:49152\n"
"v_mfma_f32_16x16x32_bf16 v[160:163], v[4:7], v[132:135], v[160:163]\n"
"v_mfma_f32_16x16x32_bf16 v[164:167], v[120:123], v[132:135], v[164:167]\n"
"s_waitcnt lgkmcnt(3)\n"
"v_mfma_f32_16x16x32_bf16 v[152:155], v[148:151], v[132:135], v[152:155]\n"
"s_waitcnt lgkmcnt(2)\n"
"ds_read_b128 v[132:135], v13 offset:49152\n"
"v_bfe_u32 v249, v14, 3, 5\n"
"v_mfma_f32_16x16x32_bf16 v[160:163], v[124:127], v[144:147], v[160:163]\n"
"ds_read_u16 v249, v249 offset:960\n"
"v_mfma_f32_16x16x32_bf16 v[164:167], v[128:131], v[144:147], v[164:167]\n"
"s_waitcnt vmcnt(10)\n"
"s_barrier\n"
"s_waitcnt lgkmcnt(3)\n"
"v_mfma_f32_16x16x32_bf16 v[152:155], v[156:159], v[144:147], v[152:155]\n"
"s_waitcnt lgkmcnt(2)\n"
"ds_read_b128 v[136:139], v12 offset:51200\n"
"v_mfma_f32_16x16x32_bf16 v[144:147], v[8:11], v[24:27], 0\n"
"ds_write_b128 v17, v[140:143] offset:7680\n"
"v_mfma_f32_16x16x32_bf16 v[140:143], v[8:11], v[28:31], 0\n"
"v_cvt_pk_bf16_f32 v160, v160, v161\n"
"v_cvt_pk_bf16_f32 v161, v162, v163\n"
"v_mfma_f32_16x16x32_bf16 v[148:151], v[8:11], v[20:23], 0\n"
"s_waitcnt lgkmcnt(3)\n"
"ds_read_b128 v[8:11], v13 offset:51200\n"
"v_cvt_pk_bf16_f32 v162, v164, v165\n"
"v_mfma_f32_16x16x32_bf16 v[144:147], v[132:135], v[36:39], v[144:147]\n"
"s_waitcnt lgkmcnt(3)\n"
"v_cvt_pk_bf16_f32 v163, v166, v167\n"
"v_mad_u32_u24 v248, v249, s40, v18\n"
"v_mfma_f32_16x16x32_bf16 v[140:143], v[132:135], v[40:43], v[140:143]\n"
"global_store_dwordx4 v248, v[160:163], s[68:69] nt\n"
"v_mfma_f32_16x16x32_bf16 v[148:151], v[132:135], v[32:35], v[148:151]\n"
"s_waitcnt lgkmcnt(2)\n"
"ds_read_b128 v[132:135], v12 offset:53248\n"
"v_add_u32 v248, s54, v14\n"
"v_mfma_f32_16x16x32_bf16 v[144:147], v[136:139], v[44:47], v[144:147]\n"
"ds_write_b64 v248, v[222:223] offset:0\n"
"v_mfma_f32_16x16x32_bf16 v[140:143], v[136:139], v[48:51], v[140:143]\n"
"v_cvt_pk_bf16_f32 v152, v152, v153\n"
"v_mad_u32_u24 v249, v249, s40, v19\n"
"v_mfma_f32_16x16x32_bf16 v[148:151], v[136:139], v[52:55], v[148:151]\n"
"s_waitcnt lgkmcnt(2)\n"
"ds_read_b128 v[136:139], v13 offset:53248\n"
"v_cvt_pk_bf16_f32 v153, v154, v155\n"
"v_mfma_f32_16x16x32_bf16 v[144:147], v[8:11], v[56:59], v[144:147]\n"
"global_store_dwordx2 v249, v[152:153], s[70:71] nt\n"
"v_mfma_f32_16x16x32_bf16 v[140:143], v[8:11], v[60:63], v[140:143]\n"
"v_mfma_f32_16x16x32_bf16 v[148:151], v[8:11], v[64:67], v[148:151]\n"
"s_waitcnt lgkmcnt(2)\n"
"v_mfma_f32_16x16x32_bf16 v[144:147], v[132:135], v[68:71], v[144:147]\n"
"v_mfma_f32_16x16x32_bf16 v[140:143], v[132:135], v[72:75], v[140:143]\n"
"v_mfma_f32_16x16x32_bf16 v[148:151], v[132:135], v[76:79], v[148:151]\n"
"s_waitcnt lgkmcnt(0)\n"
"v_mov_b32 v8, 0x3f803f80\n"
"v_mov_b32 v9, 0x3f803f80\n"
"v_mov_b32 v10, 0x3f803f80\n"
"v_mfma_f32_16x16x32_bf16 v[144:147], v[136:139], v[80:83], v[144:147]\n"
"s_waitcnt vmcnt(7)\n"
"s_barrier\n"
"ds_read_b128 v[132:135], v17 offset:30720\n"
"v_mov_b32 v11, 0x3f803f80\n"
"v_mfma_f32_16x16x32_bf16 v[140:143], v[136:139], v[84:87], v[140:143]\n"
"s_and_b32 s41, 32, s55\n"
"v_add_u32 v177, s41, v15\n"
"v_mfma_f32_16x16x32_bf16 v[148:151], v[136:139], v[88:91], v[148:151]\n"
"ds_read_b128 v[136:139], v17 offset:31744\n"
"v_cvt_pk_bf16_f32 v196, v144, v145\n"
"v_cvt_pk_bf16_f32 v197, v146, v147\n"
"ds_read_u16 v177, v177 offset:1152\n"
"s_waitcnt lgkmcnt(2)\n"
"v_mfma_f32_16x16x32_bf16 v[144:147], v[8:11], v[132:135], 0\n"
"v_cvt_pk_bf16_f32 v222, v140, v141\n"
"v_cvt_pk_bf16_f32 v223, v142, v143\n"
"v_mfma_f32_16x16x32_bf16 v[140:143], v[200:203], v[132:135], 0\n"
"ds_read_b128 v[200:203], v17 offset:32768\n"
"v_cvt_pk_bf16_f32 v248, v148, v149\n"
"v_mfma_f32_16x16x32_bf16 v[152:155], v[226:229], v[132:135], 0\n"
"v_cvt_pk_bf16_f32 v249, v150, v151\n"
"v_mfma_f32_16x16x32_bf16 v[226:229], v[252:255], v[132:135], 0\n"
"s_waitcnt lgkmcnt(1)\n"
"v_mad_u32_u24 v177, v177, s40, v16\n"
"v_mfma_f32_16x16x32_bf16 v[144:147], v[8:11], v[136:139], v[144:147]\n"
"s_add_u32 m0, s51, 151552\n"
"s_nop 0\n"
"global_load_lds_dwordx4 v177, s[56:57]\n"
"v_mfma_f32_16x16x32_bf16 v[140:143], v[182:185], v[136:139], v[140:143]\n"
"ds_read_b128 v[252:255], v17 offset:33792\n"
"v_mfma_f32_16x16x32_bf16 v[152:155], v[208:211], v[136:139], v[152:155]\n"
"ds_read_b64 v[134:135], v17 offset:8712\n"
"v_mfma_f32_16x16x32_bf16 v[226:229], v[234:237], v[136:139], v[226:229]\n"
"s_waitcnt lgkmcnt(2)\n"
"v_mfma_f32_16x16x32_bf16 v[144:147], v[8:11], v[200:203], v[144:147]\n"
"s_add_u32 m0, s51, 153600\n"
"s_nop 0\n"
"global_load_lds_dwordx4 v177, s[58:59]\n"
"v_mfma_f32_16x16x32_bf16 v[140:143], v[192:195], v[200:203], v[140:143]\n"
"v_mfma_f32_16x16x32_bf16 v[152:155], v[218:221], v[200:203], v[152:155]\n"
"v_mov_b32 v132, 0\n"
"v_mov_b32 v133, 0\n"
"v_mfma_f32_16x16x32_bf16 v[226:229], v[244:247], v[200:203], v[226:229]\n"
"s_waitcnt lgkmcnt(1)\n"
"v_mfma_f32_16x16x32_bf16 v[144:147], v[8:11], v[252:255], v[144:147]\n"
"s_add_u32 m0, s51, 155648\n"
"s_nop 0\n"
"global_load_lds_dwordx4 v177, s[60:61]\n"
"s_waitcnt lgkmcnt(0)\n"
"v_mfma_f32_16x16x32_bf16 v[144:147], v[8:11], v[132:135], v[144:147]\n"
"ds_read_b128 v[200:203], v14 offset:32256\n"
"v_mfma_f32_16x16x32_bf16 v[226:229], v[248:251], v[252:255], v[226:229]\n"
"v_mfma_f32_16x16x32_bf16 v[226:229], v[236:239], v[132:135], v[226:229]\n"
"ds_read_b128 v[8:11], v17 offset:53248\n"
"v_mfma_f32_16x16x32_bf16 v[140:143], v[196:199], v[252:255], v[140:143]\n"
"ds_read_b128 v[136:139], v14 offset:38400\n"
"v_mfma_f32_16x16x32_bf16 v[140:143], v[184:187], v[132:135], v[140:143]\n"
"v_mfma_f32_16x16x32_bf16 v[152:155], v[222:225], v[252:255], v[152:155]\n"
"ds_read_b128 v[250:253], v17 offset:54272\n"
"v_rcp_f32 v16, v144\n"
"v_mfma_f32_16x16x32_bf16 v[152:155], v[210:213], v[132:135], v[152:155]\n"
"ds_read_b128 v[132:135], v14 offset:44544\n"
"v_mul_f32 v226, v226, v16\n"
"v_mul_f32 v227, v227, v16\n"
"v_cvt_pk_bf16_f32 v198, v226, v227\n"
"s_waitcnt lgkmcnt(4)\n"
"v_mfma_f32_16x16x32_bf16 v[224:227], v[92:95], v[200:203], 0\n"
"v_mul_f32 v228, v228, v16\n"
"v_mul_f32 v229, v229, v16\n"
"v_mfma_f32_16x16x32_bf16 v[144:147], v[96:99], v[200:203], 0\n"
"s_waitcnt lgkmcnt(3)\n"
"ds_read_b128 v[148:151], v17 offset:55296\n"
"v_cvt_pk_bf16_f32 v199, v228, v229\n"
"v_mfma_f32_16x16x32_bf16 v[156:159], v[8:11], v[200:203], 0\n"
"s_waitcnt lgkmcnt(3)\n"
"ds_read_b128 v[200:203], v14 offset:50688\n"
"v_mul_f32 v140, v140, v16\n"
"v_mfma_f32_16x16x32_bf16 v[224:227], v[100:103], v[136:139], v[224:227]\n"
"v_mul_f32 v141, v141, v16\n"
"v_cvt_pk_bf16_f32 v140, v140, v141\n"
"v_mfma_f32_16x16x32_bf16 v[144:147], v[104:107], v[136:139], v[144:147]\n"
"s_waitcnt lgkmcnt(3)\n"
"ds_read_b128 v[8:11], v17 offset:56320\n"
"v_mul_f32 v142, v142, v16\n"
"v_mfma_f32_16x16x32_bf16 v[156:159], v[250:253], v[136:139], v[156:159]\n"
"s_waitcnt lgkmcnt(3)\n"
"ds_read_b128 v[250:253], v14 offset:2048\n"
"v_mul_f32 v143, v143, v16\n"
"v_cvt_pk_bf16_f32 v141, v142, v143\n"
"v_mfma_f32_16x16x32_bf16 v[224:227], v[0:3], v[132:135], v[224:227]\n"
"v_mul_f32 v152, v152, v16\n"
"v_mul_f32 v153, v153, v16\n"
"v_mfma_f32_16x16x32_bf16 v[144:147], v[108:111], v[132:135], v[144:147]\n"
"s_waitcnt lgkmcnt(3)\n"
"ds_read_b128 v[136:139], v17 offset:57344\n"
"v_cvt_pk_bf16_f32 v142, v152, v153\n"
"v_mul_f32 v154, v154, v16\n"
"v_mfma_f32_16x16x32_bf16 v[156:159], v[148:151], v[132:135], v[156:159]\n"
"s_waitcnt lgkmcnt(3)\n"
"ds_read_b128 v[132:135], v14 offset:3072\n"
"v_mul_f32 v155, v155, v16\n"
"v_mfma_f32_16x16x32_bf16 v[224:227], v[112:115], v[200:203], v[224:227]\n"
"v_cvt_pk_bf16_f32 v143, v154, v155\n"
"v_mfma_f32_16x16x32_bf16 v[144:147], v[116:119], v[200:203], v[144:147]\n"
"s_waitcnt lgkmcnt(3)\n"
"ds_read_b128 v[148:151], v17 offset:58368\n"
"v_mfma_f32_16x16x32_bf16 v[156:159], v[8:11], v[200:203], v[156:159]\n"
"s_waitcnt lgkmcnt(3)\n"
"ds_read_b128 v[200:203], v12 offset:0\n"
"v_mfma_f32_16x16x32_bf16 v[224:227], v[4:7], v[250:253], v[224:227]\n"
"v_mfma_f32_16x16x32_bf16 v[144:147], v[120:123], v[250:253], v[144:147]\n"
"s_waitcnt lgkmcnt(3)\n"
"v_mfma_f32_16x16x32_bf16 v[156:159], v[136:139], v[250:253], v[156:159]\n"
"s_waitcnt lgkmcnt(2)\n"
"ds_read_b128 v[250:253], v13 offset:0\n"
"v_bfe_u32 v16, v14, 3, 5\n"
"v_mfma_f32_16x16x32_bf16 v[224:227], v[124:127], v[132:135], v[224:227]\n"
"ds_read_u16 v16, v16 offset:992\n"
"v_mfma_f32_16x16x32_bf16 v[144:147], v[128:131], v[132:135], v[144:147]\n"
"s_barrier\n"
"s_waitcnt lgkmcnt(3)\n"
"v_mfma_f32_16x16x32_bf16 v[156:159], v[148:151], v[132:135], v[156:159]\n"
"s_waitcnt lgkmcnt(2)\n"
"ds_read_b128 v[8:11], v12 offset:2048\n"
"v_mfma_f32_16x16x32_bf16 v[132:135], v[200:203], v[24:27], 0\n"
"ds_write_b128 v17, v[140:143] offset:7680\n"
"v_mfma_f32_16x16x32_bf16 v[136:139], v[200:203], v[28:31], 0\n"
"v_cvt_pk_bf16_f32 v224, v224, v225\n"
"v_cvt_pk_bf16_f32 v225, v226, v227\n"
"v_mfma_f32_16x16x32_bf16 v[140:143], v[200:203], v[20:23], 0\n"
"s_waitcnt lgkmcnt(3)\n"
"ds_read_b128 v[200:203], v13 offset:2048\n"
"v_cvt_pk_bf16_f32 v226, v144, v145\n"
"v_mfma_f32_16x16x32_bf16 v[132:135], v[250:253], v[36:39], v[132:135]\n"
"s_waitcnt lgkmcnt(3)\n"
"v_cvt_pk_bf16_f32 v227, v146, v147\n"
"v_mad_u32_u24 v15, v16, s40, v18\n"
"v_mfma_f32_16x16x32_bf16 v[136:139], v[250:253], v[40:43], v[136:139]\n"
"global_store_dwordx4 v15, v[224:227], s[68:69] nt\n"
"v_mfma_f32_16x16x32_bf16 v[140:143], v[250:253], v[32:35], v[140:143]\n"
"s_waitcnt lgkmcnt(2)\n"
"ds_read_b128 v[224:227], v12 offset:4096\n"
"v_add_u32 v15, s54, v14\n"
"v_mfma_f32_16x16x32_bf16 v[132:135], v[8:11], v[44:47], v[132:135]\n"
"ds_write_b64 v15, v[198:199] offset:0\n"
"v_mfma_f32_16x16x32_bf16 v[136:139], v[8:11], v[48:51], v[136:139]\n"
"v_cvt_pk_bf16_f32 v156, v156, v157\n"
"v_mad_u32_u24 v16, v16, s40, v19\n"
"v_mfma_f32_16x16x32_bf16 v[140:143], v[8:11], v[52:55], v[140:143]\n"
"s_waitcnt lgkmcnt(2)\n"
"ds_read_b128 v[250:253], v13 offset:4096\n"
"v_cvt_pk_bf16_f32 v157, v158, v159\n"
"v_mfma_f32_16x16x32_bf16 v[132:135], v[200:203], v[56:59], v[132:135]\n"
"global_store_dwordx2 v16, v[156:157], s[70:71] nt\n"
"v_mfma_f32_16x16x32_bf16 v[136:139], v[200:203], v[60:63], v[136:139]\n"
"v_mfma_f32_16x16x32_bf16 v[140:143], v[200:203], v[64:67], v[140:143]\n"
"s_waitcnt lgkmcnt(2)\n"
"ds_read_b128 v[198:201], v12 offset:6144\n"
"v_mfma_f32_16x16x32_bf16 v[132:135], v[224:227], v[68:71], v[132:135]\n"
"v_mfma_f32_16x16x32_bf16 v[136:139], v[224:227], v[72:75], v[136:139]\n"
"v_mfma_f32_16x16x32_bf16 v[140:143], v[224:227], v[76:79], v[140:143]\n"
"s_waitcnt lgkmcnt(1)\n"
"ds_read_b128 v[226:229], v13 offset:6144\n"
"v_mfma_f32_16x16x32_bf16 v[132:135], v[250:253], v[80:83], v[132:135]\n"
"v_mfma_f32_16x16x32_bf16 v[136:139], v[250:253], v[84:87], v[136:139]\n"
"v_mfma_f32_16x16x32_bf16 v[140:143], v[250:253], v[88:91], v[140:143]\n"
"s_waitcnt lgkmcnt(1)\n"
"ds_read_b128 v[252:255], v12 offset:8192\n"
"v_mfma_f32_16x16x32_bf16 v[8:11], v[198:201], v[24:27], 0\n"
"v_mfma_f32_16x16x32_bf16 v[144:147], v[198:201], v[28:31], 0\n"
"v_mfma_f32_16x16x32_bf16 v[148:151], v[198:201], v[20:23], 0\n"
"s_waitcnt lgkmcnt(1)\n"
"ds_read_b128 v[200:203], v13 offset:8192\n"
"v_mfma_f32_16x16x32_bf16 v[8:11], v[226:229], v[36:39], v[8:11]\n"
"v_cvt_pk_bf16_f32 v198, v132, v133\n"
"v_cvt_pk_bf16_f32 v199, v134, v135\n"
"v_cvt_pk_bf16_f32 v224, v136, v137\n"
"v_cvt_pk_bf16_f32 v225, v138, v139\n"
"v_mfma_f32_16x16x32_bf16 v[144:147], v[226:229], v[40:43], v[144:147]\n"
"v_cvt_pk_bf16_f32 v250, v140, v141\n"
"v_cvt_pk_bf16_f32 v251, v142, v143\n"
"v_mfma_f32_16x16x32_bf16 v[148:151], v[226:229], v[32:35], v[148:151]\n"
"s_waitcnt lgkmcnt(1)\n"
"ds_read_b128 v[226:229], v12 offset:10240\n"
"v_mfma_f32_16x16x32_bf16 v[8:11], v[252:255], v[44:47], v[8:11]\n"
"v_mfma_f32_16x16x32_bf16 v[144:147], v[252:255], v[48:51], v[144:147]\n"
"v_mfma_f32_16x16x32_bf16 v[148:151], v[252:255], v[52:55], v[148:151]\n"
"s_waitcnt lgkmcnt(1)\n"
"ds_read_b128 v[252:255], v13 offset:10240\n"
"v_mfma_f32_16x16x32_bf16 v[8:11], v[200:203], v[56:59], v[8:11]\n"
"v_mfma_f32_16x16x32_bf16 v[144:147], v[200:203], v[60:63], v[144:147]\n"
"v_mfma_f32_16x16x32_bf16 v[148:151], v[200:203], v[64:67], v[148:151]\n"
"s_waitcnt lgkmcnt(1)\n"
"v_mfma_f32_16x16x32_bf16 v[8:11], v[226:229], v[68:71], v[8:11]\n"
"v_mfma_f32_16x16x32_bf16 v[144:147], v[226:229], v[72:75], v[144:147]\n"
"v_mfma_f32_16x16x32_bf16 v[148:151], v[226:229], v[76:79], v[148:151]\n"
"s_waitcnt lgkmcnt(0)\n"
"v_mov_b32 v132, 0x3f803f80\n"
"v_mov_b32 v133, 0x3f803f80\n"
"v_mfma_f32_16x16x32_bf16 v[8:11], v[252:255], v[80:83], v[8:11]\n"
"s_barrier\n"
"ds_read_b128 v[136:139], v17 offset:28672\n"
"v_mov_b32 v134, 0x3f803f80\n"
"v_mfma_f32_16x16x32_bf16 v[144:147], v[252:255], v[84:87], v[144:147]\n"
"ds_read_b128 v[140:143], v17 offset:29696\n"
"v_mov_b32 v135, 0x3f803f80\n"
"v_mfma_f32_16x16x32_bf16 v[148:151], v[252:255], v[88:91], v[148:151]\n"
"s_waitcnt lgkmcnt(1)\n"
"v_mfma_f32_16x16x32_bf16 v[152:155], v[132:135], v[136:139], 0\n"
"v_cvt_pk_bf16_f32 v200, v8, v9\n"
"v_cvt_pk_bf16_f32 v201, v10, v11\n"
"v_mfma_f32_16x16x32_bf16 v[8:11], v[180:183], v[136:139], 0\n"
"ds_read_b128 v[156:159], v17 offset:32768\n"
"v_cvt_pk_bf16_f32 v226, v144, v145\n"
"v_mfma_f32_16x16x32_bf16 v[160:163], v[206:209], v[136:139], 0\n"
"ds_read_b128 v[164:167], v17 offset:33792\n"
"v_cvt_pk_bf16_f32 v227, v146, v147\n"
"v_mfma_f32_16x16x32_bf16 v[144:147], v[232:235], v[136:139], 0\n"
"s_waitcnt lgkmcnt(2)\n"
"v_cvt_pk_bf16_f32 v252, v148, v149\n"
"v_cvt_pk_bf16_f32 v253, v150, v151\n"
"v_mfma_f32_16x16x32_bf16 v[152:155], v[132:135], v[140:143], v[152:155]\n"
"v_mfma_f32_16x16x32_bf16 v[8:11], v[184:187], v[140:143], v[8:11]\n"
"v_mfma_f32_16x16x32_bf16 v[160:163], v[210:213], v[140:143], v[160:163]\n"
"v_mfma_f32_16x16x32_bf16 v[144:147], v[236:239], v[140:143], v[144:147]\n"
"s_waitcnt lgkmcnt(1)\n"
"v_mfma_f32_16x16x32_bf16 v[152:155], v[132:135], v[156:159], v[152:155]\n"
"s_waitcnt lgkmcnt(0)\n"
"v_mfma_f32_16x16x32_bf16 v[152:155], v[132:135], v[164:167], v[152:155]\n"
"ds_read_b128 v[132:135], v14 offset:32256\n"
"v_mfma_f32_16x16x32_bf16 v[144:147], v[242:245], v[156:159], v[144:147]\n"
"v_mfma_f32_16x16x32_bf16 v[144:147], v[246:249], v[164:167], v[144:147]\n"
"ds_read_b128 v[136:139], v17 offset:53248\n"
"v_mfma_f32_16x16x32_bf16 v[8:11], v[190:193], v[156:159], v[8:11]\n"
"ds_read_b128 v[140:143], v14 offset:38400\n"
"v_mfma_f32_16x16x32_bf16 v[8:11], v[194:197], v[164:167], v[8:11]\n"
"v_mfma_f32_16x16x32_bf16 v[160:163], v[216:219], v[156:159], v[160:163]\n"
"ds_read_b128 v[148:151], v17 offset:54272\n"
"v_rcp_f32 v16, v152\n"
"v_mfma_f32_16x16x32_bf16 v[160:163], v[220:223], v[164:167], v[160:163]\n"
"ds_read_b128 v[152:155], v14 offset:44544\n"
"v_mul_f32 v144, v144, v16\n"
"v_mul_f32 v145, v145, v16\n"
"v_cvt_pk_bf16_f32 v254, v144, v145\n"
"s_waitcnt lgkmcnt(4)\n"
"v_mfma_f32_16x16x32_bf16 v[156:159], v[92:95], v[132:135], 0\n"
"v_mul_f32 v146, v146, v16\n"
"v_mul_f32 v147, v147, v16\n"
"v_mfma_f32_16x16x32_bf16 v[164:167], v[96:99], v[132:135], 0\n"
"s_waitcnt lgkmcnt(3)\n"
"ds_read_b128 v[168:171], v17 offset:55296\n"
"v_cvt_pk_bf16_f32 v255, v146, v147\n"
"v_mfma_f32_16x16x32_bf16 v[144:147], v[136:139], v[132:135], 0\n"
"s_waitcnt lgkmcnt(3)\n"
"ds_read_b128 v[132:135], v14 offset:50688\n"
"v_mul_f32 v8, v8, v16\n"
"v_mfma_f32_16x16x32_bf16 v[156:159], v[100:103], v[140:143], v[156:159]\n"
"v_mul_f32 v9, v9, v16\n"
"v_cvt_pk_bf16_f32 v8, v8, v9\n"
"v_mfma_f32_16x16x32_bf16 v[164:167], v[104:107], v[140:143], v[164:167]\n"
"s_waitcnt lgkmcnt(3)\n"
"ds_read_b128 v[136:139], v17 offset:56320\n"
"v_mul_f32 v10, v10, v16\n"
"v_mfma_f32_16x16x32_bf16 v[144:147], v[148:151], v[140:143], v[144:147]\n"
"s_waitcnt lgkmcnt(3)\n"
"ds_read_b128 v[140:143], v14 offset:2048\n"
"v_mul_f32 v11, v11, v16\n"
"v_cvt_pk_bf16_f32 v9, v10, v11\n"
"v_mfma_f32_16x16x32_bf16 v[156:159], v[0:3], v[152:155], v[156:159]\n"
"v_mul_f32 v160, v160, v16\n"
"v_mul_f32 v161, v161, v16\n"
"v_mfma_f32_16x16x32_bf16 v[164:167], v[108:111], v[152:155], v[164:167]\n"
"s_waitcnt lgkmcnt(3)\n"
"ds_read_b128 v[148:151], v17 offset:57344\n"
"v_cvt_pk_bf16_f32 v10, v160, v161\n"
"v_mul_f32 v162, v162, v16\n"
"v_mfma_f32_16x16x32_bf16 v[144:147], v[168:171], v[152:155], v[144:147]\n"
"s_waitcnt lgkmcnt(3)\n"
"ds_read_b128 v[152:155], v14 offset:3072\n"
"v_mul_f32 v163, v163, v16\n"
"v_mfma_f32_16x16x32_bf16 v[156:159], v[112:115], v[132:135], v[156:159]\n"
"v_cvt_pk_bf16_f32 v11, v162, v163\n"
"v_mfma_f32_16x16x32_bf16 v[164:167], v[116:119], v[132:135], v[164:167]\n"
"s_waitcnt lgkmcnt(3)\n"
"ds_read_b128 v[160:163], v17 offset:58368\n"
"v_mfma_f32_16x16x32_bf16 v[144:147], v[136:139], v[132:135], v[144:147]\n"
"s_waitcnt lgkmcnt(3)\n"
"ds_read_b128 v[132:135], v12 offset:43008\n"
"v_mfma_f32_16x16x32_bf16 v[156:159], v[4:7], v[140:143], v[156:159]\n"
"v_mfma_f32_16x16x32_bf16 v[164:167], v[120:123], v[140:143], v[164:167]\n"
"s_waitcnt lgkmcnt(3)\n"
"v_mfma_f32_16x16x32_bf16 v[144:147], v[148:151], v[140:143], v[144:147]\n"
"s_waitcnt lgkmcnt(2)\n"
"ds_read_b128 v[136:139], v13 offset:43008\n"
"v_bfe_u32 v16, v14, 3, 5\n"
"v_mfma_f32_16x16x32_bf16 v[156:159], v[124:127], v[152:155], v[156:159]\n"
"ds_read_u16 v16, v16 offset:1024\n"
"v_mfma_f32_16x16x32_bf16 v[164:167], v[128:131], v[152:155], v[164:167]\n"
"s_waitcnt vmcnt(7)\n"
"s_barrier\n"
"s_waitcnt lgkmcnt(3)\n"
"v_mfma_f32_16x16x32_bf16 v[144:147], v[160:163], v[152:155], v[144:147]\n"
"s_waitcnt lgkmcnt(2)\n"
"ds_read_b128 v[140:143], v12 offset:45056\n"
"v_mfma_f32_16x16x32_bf16 v[148:151], v[132:135], v[24:27], 0\n"
"ds_write_b128 v17, v[8:11] offset:7680\n"
"v_mfma_f32_16x16x32_bf16 v[8:11], v[132:135], v[28:31], 0\n"
"v_cvt_pk_bf16_f32 v156, v156, v157\n"
"v_cvt_pk_bf16_f32 v157, v158, v159\n"
"v_mfma_f32_16x16x32_bf16 v[152:155], v[132:135], v[20:23], 0\n"
"s_waitcnt lgkmcnt(3)\n"
"ds_read_b128 v[132:135], v13 offset:45056\n"
"v_cvt_pk_bf16_f32 v158, v164, v165\n"
"v_mfma_f32_16x16x32_bf16 v[148:151], v[136:139], v[36:39], v[148:151]\n"
"s_waitcnt lgkmcnt(3)\n"
"v_cvt_pk_bf16_f32 v159, v166, v167\n"
"v_mad_u32_u24 v15, v16, s40, v18\n"
"v_mfma_f32_16x16x32_bf16 v[8:11], v[136:139], v[40:43], v[8:11]\n"
"global_store_dwordx4 v15, v[156:159], s[68:69] nt\n"
"v_mfma_f32_16x16x32_bf16 v[152:155], v[136:139], v[32:35], v[152:155]\n"
"s_waitcnt lgkmcnt(2)\n"
"ds_read_b128 v[136:139], v12 offset:47104\n"
"v_add_u32 v15, s54, v14\n"
"v_mfma_f32_16x16x32_bf16 v[148:151], v[140:143], v[44:47], v[148:151]\n"
"ds_write_b64 v15, v[254:255] offset:0\n"
"v_mfma_f32_16x16x32_bf16 v[8:11], v[140:143], v[48:51], v[8:11]\n"
"v_cvt_pk_bf16_f32 v144, v144, v145\n"
"v_mad_u32_u24 v16, v16, s40, v19\n"
"v_mfma_f32_16x16x32_bf16 v[152:155], v[140:143], v[52:55], v[152:155]\n"
"s_waitcnt lgkmcnt(2)\n"
"ds_read_b128 v[140:143], v13 offset:47104\n"
"v_cvt_pk_bf16_f32 v145, v146, v147\n"
"v_mfma_f32_16x16x32_bf16 v[148:151], v[132:135], v[56:59], v[148:151]\n"
"global_store_dwordx2 v16, v[144:145], s[70:71] nt\n"
"v_mfma_f32_16x16x32_bf16 v[8:11], v[132:135], v[60:63], v[8:11]\n"
"v_mfma_f32_16x16x32_bf16 v[152:155], v[132:135], v[64:67], v[152:155]\n"
"s_waitcnt lgkmcnt(2)\n"
"v_mfma_f32_16x16x32_bf16 v[148:151], v[136:139], v[68:71], v[148:151]\n"
"v_mfma_f32_16x16x32_bf16 v[8:11], v[136:139], v[72:75], v[8:11]\n"
"v_mfma_f32_16x16x32_bf16 v[152:155], v[136:139], v[76:79], v[152:155]\n"
"s_waitcnt lgkmcnt(0)\n"
"v_mov_b32 v132, 0x3f803f80\n"
"v_mov_b32 v133, 0x3f803f80\n"
"v_mov_b32 v134, 0x3f803f80\n"
"v_mfma_f32_16x16x32_bf16 v[148:151], v[140:143], v[80:83], v[148:151]\n"
"s_waitcnt vmcnt(4)\n"
"s_barrier\n"
"ds_read_b128 v[136:139], v17 offset:30720\n"
"v_mov_b32 v135, 0x3f803f80\n"
"v_mfma_f32_16x16x32_bf16 v[8:11], v[140:143], v[84:87], v[8:11]\n"
"ds_read_b128 v[144:147], v17 offset:31744\n"
"v_mfma_f32_16x16x32_bf16 v[152:155], v[140:143], v[88:91], v[152:155]\n"
"s_waitcnt lgkmcnt(1)\n"
"v_mfma_f32_16x16x32_bf16 v[140:143], v[132:135], v[136:139], 0\n"
"v_mfma_f32_16x16x32_bf16 v[156:159], v[178:181], v[136:139], 0\n"
"ds_read_b128 v[160:163], v17 offset:32768\n"
"v_cvt_pk_bf16_f32 v202, v148, v149\n"
"v_mfma_f32_16x16x32_bf16 v[164:167], v[204:207], v[136:139], 0\n"
"v_cvt_pk_bf16_f32 v203, v150, v151\n"
"v_cvt_pk_bf16_f32 v228, v8, v9\n"
"v_mfma_f32_16x16x32_bf16 v[148:151], v[230:233], v[136:139], 0\n"
"s_waitcnt lgkmcnt(1)\n"
"v_cvt_pk_bf16_f32 v229, v10, v11\n"
"v_cvt_pk_bf16_f32 v254, v152, v153\n"
"v_mfma_f32_16x16x32_bf16 v[140:143], v[132:135], v[144:147], v[140:143]\n"
"v_cvt_pk_bf16_f32 v255, v154, v155\n"
"v_mfma_f32_16x16x32_bf16 v[156:159], v[188:191], v[144:147], v[156:159]\n"
"ds_read_b128 v[170:173], v17 offset:33792\n"
"v_mfma_f32_16x16x32_bf16 v[164:167], v[214:217], v[144:147], v[164:167]\n"
"ds_read_b64 v[10:11], v17 offset:8712\n"
"v_mfma_f32_16x16x32_bf16 v[148:151], v[240:243], v[144:147], v[148:151]\n"
"s_waitcnt lgkmcnt(2)\n"
"v_mfma_f32_16x16x32_bf16 v[140:143], v[132:135], v[160:163], v[140:143]\n"
"v_mfma_f32_16x16x32_bf16 v[156:159], v[198:201], v[160:163], v[156:159]\n"
"v_mfma_f32_16x16x32_bf16 v[164:167], v[224:227], v[160:163], v[164:167]\n"
"v_mov_b32 v8, 0\n"
"v_mov_b32 v9, 0\n"
"v_mfma_f32_16x16x32_bf16 v[148:151], v[250:253], v[160:163], v[148:151]\n"
"s_waitcnt lgkmcnt(1)\n"
"v_mov_b32 v168, 0\n"
"v_mov_b32 v169, 0\n"
"v_mfma_f32_16x16x32_bf16 v[140:143], v[132:135], v[170:173], v[140:143]\n"
"s_waitcnt lgkmcnt(0)\n"
"v_mov_b32 v174, 0\n"
"v_mov_b32 v175, 0\n"
"v_mfma_f32_16x16x32_bf16 v[140:143], v[132:135], v[8:11], v[140:143]\n"
"v_mfma_f32_16x16x32_bf16 v[148:151], v[232:235], v[168:171], v[148:151]\n"
"v_mfma_f32_16x16x32_bf16 v[148:151], v[244:247], v[172:175], v[148:151]\n"
"v_mfma_f32_16x16x32_bf16 v[148:151], v[252:255], v[8:11], v[148:151]\n"
"ds_read_b128 v[132:135], v14 offset:32256\n"
"v_mfma_f32_16x16x32_bf16 v[156:159], v[180:183], v[168:171], v[156:159]\n"
"v_mfma_f32_16x16x32_bf16 v[156:159], v[192:195], v[172:175], v[156:159]\n"
"ds_read_b128 v[136:139], v17 offset:53248\n"
"v_mfma_f32_16x16x32_bf16 v[156:159], v[200:203], v[8:11], v[156:159]\n"
"ds_read_b128 v[144:147], v14 offset:38400\n"
"v_rcp_f32 v16, v140\n"
"v_mfma_f32_16x16x32_bf16 v[164:167], v[206:209], v[168:171], v[164:167]\n"
"v_mul_f32 v148, v148, v16\n"
"v_mul_f32 v149, v149, v16\n"
"v_mfma_f32_16x16x32_bf16 v[164:167], v[218:221], v[172:175], v[164:167]\n"
"ds_read_b128 v[140:143], v17 offset:54272\n"
"v_cvt_pk_bf16_f32 v240, v148, v149\n"
"v_mfma_f32_16x16x32_bf16 v[164:167], v[226:229], v[8:11], v[164:167]\n"
"s_waitcnt lgkmcnt(3)\n"
"ds_read_b128 v[8:11], v14 offset:44544\n"
"v_mul_f32 v150, v150, v16\n"
"v_mul_f32 v151, v151, v16\n"
"v_mfma_f32_16x16x32_bf16 v[152:155], v[92:95], v[132:135], 0\n"
"v_cvt_pk_bf16_f32 v241, v150, v151\n"
"v_mul_f32 v156, v156, v16\n"
"v_mfma_f32_16x16x32_bf16 v[148:151], v[96:99], v[132:135], 0\n"
"s_waitcnt lgkmcnt(3)\n"
"ds_read_b128 v[160:163], v17 offset:55296\n"
"v_mul_f32 v157, v157, v16\n"
"v_mfma_f32_16x16x32_bf16 v[168:171], v[136:139], v[132:135], 0\n"
"s_waitcnt lgkmcnt(3)\n"
"ds_read_b128 v[132:135], v14 offset:50688\n"
"v_cvt_pk_bf16_f32 v156, v156, v157\n"
"v_mfma_f32_16x16x32_bf16 v[152:155], v[100:103], v[144:147], v[152:155]\n"
"v_mul_f32 v158, v158, v16\n"
"v_mul_f32 v159, v159, v16\n"
"v_mfma_f32_16x16x32_bf16 v[148:151], v[104:107], v[144:147], v[148:151]\n"
"s_waitcnt lgkmcnt(3)\n"
"ds_read_b128 v[136:139], v17 offset:56320\n"
"v_cvt_pk_bf16_f32 v157, v158, v159\n"
"v_mul_f32 v164, v164, v16\n"
"v_mfma_f32_16x16x32_bf16 v[168:171], v[140:143], v[144:147], v[168:171]\n"
"s_waitcnt lgkmcnt(3)\n"
"ds_read_b128 v[140:143], v14 offset:2048\n"
"v_mul_f32 v165, v165, v16\n"
"v_mfma_f32_16x16x32_bf16 v[152:155], v[0:3], v[8:11], v[152:155]\n"
"v_cvt_pk_bf16_f32 v158, v164, v165\n"
"v_mul_f32 v166, v166, v16\n"
"v_mfma_f32_16x16x32_bf16 v[148:151], v[108:111], v[8:11], v[148:151]\n"
"s_waitcnt lgkmcnt(3)\n"
"ds_read_b128 v[144:147], v17 offset:57344\n"
"v_mul_f32 v167, v167, v16\n"
"v_cvt_pk_bf16_f32 v159, v166, v167\n"
"v_mfma_f32_16x16x32_bf16 v[168:171], v[160:163], v[8:11], v[168:171]\n"
"s_waitcnt lgkmcnt(3)\n"
"ds_read_b128 v[8:11], v14 offset:3072\n"
"v_mfma_f32_16x16x32_bf16 v[152:155], v[112:115], v[132:135], v[152:155]\n"
"v_mfma_f32_16x16x32_bf16 v[148:151], v[116:119], v[132:135], v[148:151]\n"
"s_waitcnt lgkmcnt(3)\n"
"ds_read_b128 v[160:163], v17 offset:58368\n"
"v_mfma_f32_16x16x32_bf16 v[168:171], v[136:139], v[132:135], v[168:171]\n"
"s_waitcnt lgkmcnt(3)\n"
"ds_read_b128 v[132:135], v12 offset:18432\n"
"v_mfma_f32_16x16x32_bf16 v[152:155], v[4:7], v[140:143], v[152:155]\n"
"v_mfma_f32_16x16x32_bf16 v[148:151], v[120:123], v[140:143], v[148:151]\n"
"s_waitcnt lgkmcnt(3)\n"
"v_mfma_f32_16x16x32_bf16 v[168:171], v[144:147], v[140:143], v[168:171]\n"
"s_waitcnt lgkmcnt(2)\n"
"ds_read_b128 v[136:139], v13 offset:18432\n"
"v_bfe_u32 v16, v14, 3, 5\n"
"v_mfma_f32_16x16x32_bf16 v[152:155], v[124:127], v[8:11], v[152:155]\n"
"ds_read_u16 v16, v16 offset:1056\n"
"v_mfma_f32_16x16x32_bf16 v[148:151], v[128:131], v[8:11], v[148:151]\n"
"s_barrier\n"
"s_waitcnt lgkmcnt(3)\n"
"v_mfma_f32_16x16x32_bf16 v[168:171], v[160:163], v[8:11], v[168:171]\n"
"s_waitcnt lgkmcnt(2)\n"
"ds_read_b128 v[8:11], v12 offset:20480\n"
"v_mfma_f32_16x16x32_bf16 v[140:143], v[132:135], v[24:27], 0\n"
"ds_write_b128 v17, v[156:159] offset:7680\n"
"v_mfma_f32_16x16x32_bf16 v[144:147], v[132:135], v[28:31], 0\n"
"v_cvt_pk_bf16_f32 v152, v152, v153\n"
"v_cvt_pk_bf16_f32 v153, v154, v155\n"
"v_mfma_f32_16x16x32_bf16 v[156:159], v[132:135], v[20:23], 0\n"
"s_waitcnt lgkmcnt(3)\n"
"ds_read_b128 v[132:135], v13 offset:20480\n"
"v_cvt_pk_bf16_f32 v154, v148, v149\n"
"v_mfma_f32_16x16x32_bf16 v[140:143], v[136:139], v[36:39], v[140:143]\n"
"s_waitcnt lgkmcnt(3)\n"
"v_cvt_pk_bf16_f32 v155, v150, v151\n"
"v_mad_u32_u24 v15, v16, s40, v18\n"
"v_mfma_f32_16x16x32_bf16 v[144:147], v[136:139], v[40:43], v[144:147]\n"
"global_store_dwordx4 v15, v[152:155], s[68:69] nt\n"
"v_mfma_f32_16x16x32_bf16 v[156:159], v[136:139], v[32:35], v[156:159]\n"
"s_waitcnt lgkmcnt(2)\n"
"ds_read_b128 v[136:139], v12 offset:22528\n"
"v_add_u32 v15, s54, v14\n"
"v_mfma_f32_16x16x32_bf16 v[140:143], v[8:11], v[44:47], v[140:143]\n"
"ds_write_b64 v15, v[240:241] offset:0\n"
"v_mfma_f32_16x16x32_bf16 v[144:147], v[8:11], v[48:51], v[144:147]\n"
"v_cvt_pk_bf16_f32 v168, v168, v169\n"
"v_mad_u32_u24 v16, v16, s40, v19\n"
"v_mfma_f32_16x16x32_bf16 v[156:159], v[8:11], v[52:55], v[156:159]\n"
"s_waitcnt lgkmcnt(2)\n"
"ds_read_b128 v[8:11], v13 offset:22528\n"
"v_cvt_pk_bf16_f32 v169, v170, v171\n"
"v_mfma_f32_16x16x32_bf16 v[140:143], v[132:135], v[56:59], v[140:143]\n"
"global_store_dwordx2 v16, v[168:169], s[70:71] nt\n"
"v_mfma_f32_16x16x32_bf16 v[144:147], v[132:135], v[60:63], v[144:147]\n"
"v_mfma_f32_16x16x32_bf16 v[156:159], v[132:135], v[64:67], v[156:159]\n"
"s_waitcnt lgkmcnt(2)\n"
"v_mfma_f32_16x16x32_bf16 v[140:143], v[136:139], v[68:71], v[140:143]\n"
"v_mfma_f32_16x16x32_bf16 v[144:147], v[136:139], v[72:75], v[144:147]\n"
"v_mfma_f32_16x16x32_bf16 v[156:159], v[136:139], v[76:79], v[156:159]\n"
"s_waitcnt lgkmcnt(0)\n"
"v_mov_b32 v132, 0x3f803f80\n"
"v_mov_b32 v133, 0x3f803f80\n"
"v_mov_b32 v134, 0x3f803f80\n"
"v_mfma_f32_16x16x32_bf16 v[140:143], v[8:11], v[80:83], v[140:143]\n"
"s_barrier\n"
"ds_read_b128 v[136:139], v17 offset:28672\n"
"v_mov_b32 v135, 0x3f803f80\n"
"v_mfma_f32_16x16x32_bf16 v[144:147], v[8:11], v[84:87], v[144:147]\n"
"ds_read_b128 v[148:151], v17 offset:29696\n"
"v_mfma_f32_16x16x32_bf16 v[156:159], v[8:11], v[88:91], v[156:159]\n"
"s_waitcnt lgkmcnt(1)\n"
"v_mfma_f32_16x16x32_bf16 v[8:11], v[132:135], v[136:139], 0\n"
"v_mfma_f32_16x16x32_bf16 v[152:155], v[182:185], v[136:139], 0\n"
"ds_read_b128 v[160:163], v17 offset:32768\n"
"v_cvt_pk_bf16_f32 v178, v140, v141\n"
"v_mfma_f32_16x16x32_bf16 v[164:167], v[208:211], v[136:139], 0\n"
"v_cvt_pk_bf16_f32 v179, v142, v143\n"
"v_cvt_pk_bf16_f32 v204, v144, v145\n"
"v_mfma_f32_16x16x32_bf16 v[140:143], v[234:237], v[136:139], 0\n"
"s_waitcnt lgkmcnt(1)\n"
"v_cvt_pk_bf16_f32 v205, v146, v147\n"
"v_cvt_pk_bf16_f32 v230, v156, v157\n"
"v_mfma_f32_16x16x32_bf16 v[8:11], v[132:135], v[148:151], v[8:11]\n"
"v_cvt_pk_bf16_f32 v231, v158, v159\n"
"v_mfma_f32_16x16x32_bf16 v[152:155], v[190:193], v[148:151], v[152:155]\n"
"ds_read_b128 v[136:139], v17 offset:33792\n"
"v_mfma_f32_16x16x32_bf16 v[164:167], v[216:219], v[148:151], v[164:167]\n"
"ds_read_b64 v[146:147], v17 offset:8704\n"
"v_mfma_f32_16x16x32_bf16 v[140:143], v[242:245], v[148:151], v[140:143]\n"
"s_waitcnt lgkmcnt(2)\n"
"v_mfma_f32_16x16x32_bf16 v[8:11], v[132:135], v[160:163], v[8:11]\n"
"v_mfma_f32_16x16x32_bf16 v[152:155], v[178:181], v[160:163], v[152:155]\n"
"v_mfma_f32_16x16x32_bf16 v[164:167], v[204:207], v[160:163], v[164:167]\n"
"v_mov_b32 v144, 0\n"
"v_mov_b32 v145, 0\n"
"v_mfma_f32_16x16x32_bf16 v[140:143], v[230:233], v[160:163], v[140:143]\n"
"s_waitcnt lgkmcnt(1)\n"
"v_mfma_f32_16x16x32_bf16 v[8:11], v[132:135], v[136:139], v[8:11]\n"
"s_waitcnt lgkmcnt(0)\n"
"v_mfma_f32_16x16x32_bf16 v[8:11], v[132:135], v[144:147], v[8:11]\n"
"ds_read_b128 v[132:135], v14 offset:32256\n"
"v_mfma_f32_16x16x32_bf16 v[140:143], v[252:255], v[136:139], v[140:143]\n"
"v_mfma_f32_16x16x32_bf16 v[140:143], v[244:247], v[144:147], v[140:143]\n"
"ds_read_b128 v[148:151], v17 offset:53248\n"
"v_mfma_f32_16x16x32_bf16 v[152:155], v[200:203], v[136:139], v[152:155]\n"
"ds_read_b128 v[156:159], v14 offset:38400\n"
"v_mfma_f32_16x16x32_bf16 v[152:155], v[192:195], v[144:147], v[152:155]\n"
"v_mfma_f32_16x16x32_bf16 v[164:167], v[226:229], v[136:139], v[164:167]\n"
"ds_read_b128 v[136:139], v17 offset:54272\n"
"v_rcp_f32 v16, v8\n"
"v_mfma_f32_16x16x32_bf16 v[164:167], v[218:221], v[144:147], v[164:167]\n"
"ds_read_b128 v[8:11], v14 offset:44544\n"
"v_mul_f32 v140, v140, v16\n"
"v_mul_f32 v141, v141, v16\n"
"v_cvt_pk_bf16_f32 v240, v140, v141\n"
"s_waitcnt lgkmcnt(4)\n"
"v_mfma_f32_16x16x32_bf16 v[144:147], v[92:95], v[132:135], 0\n"
"v_mul_f32 v142, v142, v16\n"
"v_mul_f32 v143, v143, v16\n"
"v_mfma_f32_16x16x32_bf16 v[160:163], v[96:99], v[132:135], 0\n"
"s_waitcnt lgkmcnt(3)\n"
"ds_read_b128 v[168:171], v17 offset:55296\n"
"v_cvt_pk_bf16_f32 v241, v142, v143\n"
"v_mfma_f32_16x16x32_bf16 v[140:143], v[148:151], v[132:135], 0\n"
"s_waitcnt lgkmcnt(3)\n"
"ds_read_b128 v[132:135], v14 offset:50688\n"
"v_mul_f32 v152, v152, v16\n"
"v_mfma_f32_16x16x32_bf16 v[144:147], v[100:103], v[156:159], v[144:147]\n"
"v_mul_f32 v153, v153, v16\n"
"v_cvt_pk_bf16_f32 v152, v152, v153\n"
"v_mfma_f32_16x16x32_bf16 v[160:163], v[104:107], v[156:159], v[160:163]\n"
"s_waitcnt lgkmcnt(3)\n"
"ds_read_b128 v[148:151], v17 offset:56320\n"
"v_mul_f32 v154, v154, v16\n"
"v_mfma_f32_16x16x32_bf16 v[140:143], v[136:139], v[156:159], v[140:143]\n"
"s_waitcnt lgkmcnt(3)\n"
"ds_read_b128 v[136:139], v14 offset:2048\n"
"v_mul_f32 v155, v155, v16\n"
"v_cvt_pk_bf16_f32 v153, v154, v155\n"
"v_mfma_f32_16x16x32_bf16 v[144:147], v[0:3], v[8:11], v[144:147]\n"
"v_mul_f32 v164, v164, v16\n"
"v_mul_f32 v165, v165, v16\n"
"v_mfma_f32_16x16x32_bf16 v[160:163], v[108:111], v[8:11], v[160:163]\n"
"s_waitcnt lgkmcnt(3)\n"
"ds_read_b128 v[156:159], v17 offset:57344\n"
"v_cvt_pk_bf16_f32 v154, v164, v165\n"
"v_mul_f32 v166, v166, v16\n"
"v_mfma_f32_16x16x32_bf16 v[140:143], v[168:171], v[8:11], v[140:143]\n"
"s_waitcnt lgkmcnt(3)\n"
"ds_read_b128 v[8:11], v14 offset:3072\n"
"v_mul_f32 v167, v167, v16\n"
"v_mfma_f32_16x16x32_bf16 v[144:147], v[112:115], v[132:135], v[144:147]\n"
"v_cvt_pk_bf16_f32 v155, v166, v167\n"
"v_mfma_f32_16x16x32_bf16 v[160:163], v[116:119], v[132:135], v[160:163]\n"
"s_waitcnt lgkmcnt(3)\n"
"ds_read_b128 v[164:167], v17 offset:58368\n"
"v_mfma_f32_16x16x32_bf16 v[140:143], v[148:151], v[132:135], v[140:143]\n"
"s_waitcnt lgkmcnt(3)\n"
"ds_read_b128 v[132:135], v12 offset:30720\n"
"v_mfma_f32_16x16x32_bf16 v[144:147], v[4:7], v[136:139], v[144:147]\n"
"v_mfma_f32_16x16x32_bf16 v[160:163], v[120:123], v[136:139], v[160:163]\n"
"s_waitcnt lgkmcnt(3)\n"
"v_mfma_f32_16x16x32_bf16 v[140:143], v[156:159], v[136:139], v[140:143]\n"
"s_waitcnt lgkmcnt(2)\n"
"ds_read_b128 v[136:139], v13 offset:30720\n"
"v_bfe_u32 v16, v14, 3, 5\n"
"v_mfma_f32_16x16x32_bf16 v[144:147], v[124:127], v[8:11], v[144:147]\n"
"ds_read_u16 v16, v16 offset:1088\n"
"v_mfma_f32_16x16x32_bf16 v[160:163], v[128:131], v[8:11], v[160:163]\n"
"s_barrier\n"
"s_waitcnt lgkmcnt(3)\n"
"v_mfma_f32_16x16x32_bf16 v[140:143], v[164:167], v[8:11], v[140:143]\n"
"s_waitcnt lgkmcnt(2)\n"
"ds_read_b128 v[8:11], v12 offset:32768\n"
"v_mfma_f32_16x16x32_bf16 v[148:151], v[132:135], v[24:27], 0\n"
"ds_write_b128 v17, v[152:155] offset:7680\n"
"v_mfma_f32_16x16x32_bf16 v[24:27], v[132:135], v[28:31], 0\n"
"v_cvt_pk_bf16_f32 v144, v144, v145\n"
"v_cvt_pk_bf16_f32 v145, v146, v147\n"
"v_mfma_f32_16x16x32_bf16 v[28:31], v[132:135], v[20:23], 0\n"
"s_waitcnt lgkmcnt(3)\n"
"ds_read_b128 v[20:23], v13 offset:32768\n"
"v_cvt_pk_bf16_f32 v146, v160, v161\n"
"v_mfma_f32_16x16x32_bf16 v[148:151], v[136:139], v[36:39], v[148:151]\n"
"s_waitcnt lgkmcnt(3)\n"
"v_cvt_pk_bf16_f32 v147, v162, v163\n"
"v_mad_u32_u24 v15, v16, s40, v18\n"
"v_mfma_f32_16x16x32_bf16 v[24:27], v[136:139], v[40:43], v[24:27]\n"
"global_store_dwordx4 v15, v[144:147], s[68:69] nt\n"
"v_mfma_f32_16x16x32_bf16 v[28:31], v[136:139], v[32:35], v[28:31]\n"
"s_waitcnt lgkmcnt(2)\n"
"ds_read_b128 v[32:35], v12 offset:34816\n"
"v_add_u32 v15, s54, v14\n"
"v_mfma_f32_16x16x32_bf16 v[148:151], v[8:11], v[44:47], v[148:151]\n"
"ds_write_b64 v15, v[240:241] offset:0\n"
"v_mfma_f32_16x16x32_bf16 v[24:27], v[8:11], v[48:51], v[24:27]\n"
"v_cvt_pk_bf16_f32 v140, v140, v141\n"
"v_mad_u32_u24 v16, v16, s40, v19\n"
"v_mfma_f32_16x16x32_bf16 v[28:31], v[8:11], v[52:55], v[28:31]\n"
"s_waitcnt lgkmcnt(2)\n"
"ds_read_b128 v[8:11], v13 offset:34816\n"
"v_cvt_pk_bf16_f32 v141, v142, v143\n"
"v_mfma_f32_16x16x32_bf16 v[148:151], v[20:23], v[56:59], v[148:151]\n"
"global_store_dwordx2 v16, v[140:141], s[70:71] nt\n"
"v_mfma_f32_16x16x32_bf16 v[24:27], v[20:23], v[60:63], v[24:27]\n"
"v_mfma_f32_16x16x32_bf16 v[28:31], v[20:23], v[64:67], v[28:31]\n"
"s_waitcnt lgkmcnt(2)\n"
"v_mfma_f32_16x16x32_bf16 v[148:151], v[32:35], v[68:71], v[148:151]\n"
"v_mfma_f32_16x16x32_bf16 v[24:27], v[32:35], v[72:75], v[24:27]\n"
"v_mfma_f32_16x16x32_bf16 v[28:31], v[32:35], v[76:79], v[28:31]\n"
"s_waitcnt lgkmcnt(0)\n"
"v_mov_b32 v20, 0x3f803f80\n"
"v_mov_b32 v21, 0x3f803f80\n"
"v_mov_b32 v22, 0x3f803f80\n"
"v_mfma_f32_16x16x32_bf16 v[148:151], v[8:11], v[80:83], v[148:151]\n"
"s_barrier\n"
"ds_read_b128 v[32:35], v17 offset:30720\n"
"v_mov_b32 v23, 0x3f803f80\n"
"v_mfma_f32_16x16x32_bf16 v[24:27], v[8:11], v[84:87], v[24:27]\n"
"ds_read_b128 v[36:39], v17 offset:32768\n"
"v_mfma_f32_16x16x32_bf16 v[28:31], v[8:11], v[88:91], v[28:31]\n"
"s_waitcnt lgkmcnt(1)\n"
"v_mfma_f32_16x16x32_bf16 v[8:11], v[20:23], v[32:35], 0\n"
"v_mfma_f32_16x16x32_bf16 v[40:43], v[182:185], v[32:35], 0\n"
"ds_read_b128 v[44:47], v17 offset:33792\n"
"v_cvt_pk_bf16_f32 v180, v148, v149\n"
"v_mfma_f32_16x16x32_bf16 v[48:51], v[208:211], v[32:35], 0\n"
"v_cvt_pk_bf16_f32 v181, v150, v151\n"
"v_cvt_pk_bf16_f32 v206, v24, v25\n"
"v_mfma_f32_16x16x32_bf16 v[52:55], v[234:237], v[32:35], 0\n"
"s_waitcnt lgkmcnt(1)\n"
"v_cvt_pk_bf16_f32 v207, v26, v27\n"
"v_cvt_pk_bf16_f32 v232, v28, v29\n"
"v_mfma_f32_16x16x32_bf16 v[8:11], v[20:23], v[36:39], v[8:11]\n"
"v_cvt_pk_bf16_f32 v233, v30, v31\n"
"v_mfma_f32_16x16x32_bf16 v[40:43], v[178:181], v[36:39], v[40:43]\n"
"ds_read_b128 v[26:29], v17 offset:31744\n"
"v_mfma_f32_16x16x32_bf16 v[48:51], v[204:207], v[36:39], v[48:51]\n"
"ds_read_b64 v[34:35], v17 offset:8712\n"
"v_mfma_f32_16x16x32_bf16 v[52:55], v[230:233], v[36:39], v[52:55]\n"
"s_waitcnt lgkmcnt(2)\n"
"v_mfma_f32_16x16x32_bf16 v[8:11], v[20:23], v[44:47], v[8:11]\n"
"v_mfma_f32_16x16x32_bf16 v[40:43], v[192:195], v[44:47], v[40:43]\n"
"v_mfma_f32_16x16x32_bf16 v[48:51], v[218:221], v[44:47], v[48:51]\n"
"v_mov_b32 v32, 0\n"
"v_mov_b32 v33, 0\n"
"v_mfma_f32_16x16x32_bf16 v[52:55], v[244:247], v[44:47], v[52:55]\n"
"s_waitcnt lgkmcnt(1)\n"
"v_mov_b32 v24, 0\n"
"v_mov_b32 v25, 0\n"
"v_mfma_f32_16x16x32_bf16 v[8:11], v[20:23], v[26:29], v[8:11]\n"
"s_waitcnt lgkmcnt(0)\n"
"v_mov_b32 v30, 0\n"
"v_mov_b32 v31, 0\n"
"v_mfma_f32_16x16x32_bf16 v[8:11], v[20:23], v[32:35], v[8:11]\n"
"v_mfma_f32_16x16x32_bf16 v[52:55], v[236:239], v[24:27], v[52:55]\n"
"v_mfma_f32_16x16x32_bf16 v[52:55], v[248:251], v[28:31], v[52:55]\n"
"v_mfma_f32_16x16x32_bf16 v[52:55], v[252:255], v[32:35], v[52:55]\n"
"ds_read_b128 v[234:237], v14 offset:32256\n"
"v_mfma_f32_16x16x32_bf16 v[40:43], v[184:187], v[24:27], v[40:43]\n"
"v_mfma_f32_16x16x32_bf16 v[40:43], v[196:199], v[28:31], v[40:43]\n"
"ds_read_b128 v[182:185], v17 offset:53248\n"
"v_mfma_f32_16x16x32_bf16 v[40:43], v[200:203], v[32:35], v[40:43]\n"
"ds_read_b128 v[186:189], v14 offset:38400\n"
"v_rcp_f32 v16, v8\n"
"v_mfma_f32_16x16x32_bf16 v[48:51], v[210:213], v[24:27], v[48:51]\n"
"v_mul_f32 v52, v52, v16\n"
"v_mul_f32 v53, v53, v16\n"
"v_mfma_f32_16x16x32_bf16 v[48:51], v[222:225], v[28:31], v[48:51]\n"
"ds_read_b128 v[196:199], v17 offset:54272\n"
"v_cvt_pk_bf16_f32 v240, v52, v53\n"
"v_mfma_f32_16x16x32_bf16 v[48:51], v[226:229], v[32:35], v[48:51]\n"
"s_waitcnt lgkmcnt(3)\n"
"ds_read_b128 v[208:211], v14 offset:44544\n"
"v_mul_f32 v54, v54, v16\n"
"v_mul_f32 v55, v55, v16\n"
"v_mfma_f32_16x16x32_bf16 v[212:215], v[92:95], v[234:237], 0\n"
"v_cvt_pk_bf16_f32 v241, v54, v55\n"
"v_mul_f32 v40, v40, v16\n"
"v_mfma_f32_16x16x32_bf16 v[222:225], v[96:99], v[234:237], 0\n"
"s_waitcnt lgkmcnt(3)\n"
"ds_read_b128 v[248:251], v17 offset:55296\n"
"v_mul_f32 v41, v41, v16\n"
"v_mfma_f32_16x16x32_bf16 v[8:11], v[182:185], v[234:237], 0\n"
"s_waitcnt lgkmcnt(3)\n"
"ds_read_b128 v[182:185], v14 offset:50688\n"
"v_cvt_pk_bf16_f32 v40, v40, v41\n"
"v_mfma_f32_16x16x32_bf16 v[212:215], v[100:103], v[186:189], v[212:215]\n"
"v_mul_f32 v42, v42, v16\n"
"v_mul_f32 v43, v43, v16\n"
"v_mfma_f32_16x16x32_bf16 v[222:225], v[104:107], v[186:189], v[222:225]\n"
"s_waitcnt lgkmcnt(3)\n"
"ds_read_b128 v[234:237], v17 offset:56320\n"
"v_cvt_pk_bf16_f32 v41, v42, v43\n"
"v_mul_f32 v48, v48, v16\n"
"v_mfma_f32_16x16x32_bf16 v[8:11], v[196:199], v[186:189], v[8:11]\n"
"s_waitcnt lgkmcnt(3)\n"
"ds_read_b128 v[186:189], v14 offset:2048\n"
"v_mul_f32 v49, v49, v16\n"
"v_mfma_f32_16x16x32_bf16 v[212:215], v[0:3], v[208:211], v[212:215]\n"
"v_cvt_pk_bf16_f32 v42, v48, v49\n"
"v_mul_f32 v50, v50, v16\n"
"v_mfma_f32_16x16x32_bf16 v[222:225], v[108:111], v[208:211], v[222:225]\n"
"s_waitcnt lgkmcnt(3)\n"
"ds_read_b128 v[196:199], v17 offset:57344\n"
"v_mul_f32 v51, v51, v16\n"
"v_cvt_pk_bf16_f32 v43, v50, v51\n"
"v_mfma_f32_16x16x32_bf16 v[8:11], v[248:251], v[208:211], v[8:11]\n"
"s_waitcnt lgkmcnt(3)\n"
"ds_read_b128 v[208:211], v14 offset:3072\n"
"v_mfma_f32_16x16x32_bf16 v[212:215], v[112:115], v[182:185], v[212:215]\n"
"v_mfma_f32_16x16x32_bf16 v[222:225], v[116:119], v[182:185], v[222:225]\n"
"s_waitcnt lgkmcnt(3)\n"
"ds_read_b128 v[248:251], v17 offset:58368\n"
"v_add_u32 v16, s54, v14\n"
"v_mfma_f32_16x16x32_bf16 v[8:11], v[234:237], v[182:185], v[8:11]\n"
"s_waitcnt lgkmcnt(3)\n"
"v_mfma_f32_16x16x32_bf16 v[212:215], v[4:7], v[186:189], v[212:215]\n"
"v_mfma_f32_16x16x32_bf16 v[222:225], v[120:123], v[186:189], v[222:225]\n"
"s_waitcnt lgkmcnt(2)\n"
"v_mfma_f32_16x16x32_bf16 v[8:11], v[196:199], v[186:189], v[8:11]\n"
"s_waitcnt lgkmcnt(1)\n"
"v_bfe_u32 v15, v14, 3, 5\n"
"v_mov_b32 v182, 0x3f803f80\n"
"v_mov_b32 v183, 0x3f803f80\n"
"v_mfma_f32_16x16x32_bf16 v[212:215], v[124:127], v[208:211], v[212:215]\n"
"ds_read_u16 v15, v15 offset:1120\n"
"v_mov_b32 v184, 0x3f803f80\n"
"v_mfma_f32_16x16x32_bf16 v[222:225], v[128:131], v[208:211], v[222:225]\n"
"s_barrier\n"
"ds_write_b64 v16, v[240:241] offset:0\n"
"ds_write_b128 v17, v[40:43] offset:7680\n"
"s_waitcnt lgkmcnt(3)\n"
"v_mfma_f32_16x16x32_bf16 v[8:11], v[248:251], v[208:211], v[8:11]\n"
"s_waitcnt lgkmcnt(0)\n"
"v_mov_b32 v185, 0x3f803f80\n"
"v_cvt_pk_bf16_f32 v212, v212, v213\n"
"v_cvt_pk_bf16_f32 v213, v214, v215\n"
"s_barrier\n"
"ds_read_b128 v[186:189], v17 offset:28672\n"
"v_cvt_pk_bf16_f32 v214, v222, v223\n"
"v_cvt_pk_bf16_f32 v215, v224, v225\n"
"v_mad_u32_u24 v16, v15, s40, v18\n"
"v_cvt_pk_bf16_f32 v8, v8, v9\n"
"v_mad_u32_u24 v15, v15, s40, v19\n"
"v_cvt_pk_bf16_f32 v9, v10, v11\n"
"ds_read_b128 v[196:199], v17 offset:29696\n"
"ds_read_b128 v[236:239], v17 offset:32768\n"
"global_store_dwordx4 v16, v[212:215], s[68:69] nt\n"
"s_waitcnt lgkmcnt(2)\n"
"v_mfma_f32_16x16x32_bf16 v[208:211], v[182:185], v[186:189], 0\n"
"v_mfma_f32_16x16x32_bf16 v[212:215], v[190:193], v[186:189], 0\n"
"v_mfma_f32_16x16x32_bf16 v[222:225], v[216:219], v[186:189], 0\n"
"v_mfma_f32_16x16x32_bf16 v[248:251], v[242:245], v[186:189], 0\n"
"s_waitcnt lgkmcnt(1)\n"
"v_mfma_f32_16x16x32_bf16 v[208:211], v[182:185], v[196:199], v[208:211]\n"
"s_waitcnt lgkmcnt(0)\n"
"v_mov_b32 v234, 0\n"
"v_mov_b32 v235, 0\n"
"v_mfma_f32_16x16x32_bf16 v[208:211], v[182:185], v[236:239], v[208:211]\n"
"v_mov_b32 v240, 0\n"
"v_mov_b32 v241, 0\n"
"v_mfma_f32_16x16x32_bf16 v[248:251], v[252:255], v[196:199], v[248:251]\n"
"global_store_dwordx2 v15, v[8:9], s[70:71] nt\n"
"v_mfma_f32_16x16x32_bf16 v[248:251], v[244:247], v[234:237], v[248:251]\n"
"v_mfma_f32_16x16x32_bf16 v[248:251], v[230:233], v[238:241], v[248:251]\n"
"ds_read_b128 v[182:185], v14 offset:32256\n"
"v_mfma_f32_16x16x32_bf16 v[212:215], v[200:203], v[196:199], v[212:215]\n"
"v_rcp_f32 v16, v208\n"
"v_mfma_f32_16x16x32_bf16 v[212:215], v[192:195], v[234:237], v[212:215]\n"
"ds_read_b128 v[186:189], v17 offset:53248\n"
"v_mfma_f32_16x16x32_bf16 v[212:215], v[178:181], v[238:241], v[212:215]\n"
"ds_read_b128 v[178:181], v14 offset:38400\n"
"v_mfma_f32_16x16x32_bf16 v[222:225], v[226:229], v[196:199], v[222:225]\n"
"v_mul_f32 v248, v248, v16\n"
"v_mul_f32 v249, v249, v16\n"
"v_mfma_f32_16x16x32_bf16 v[222:225], v[218:221], v[234:237], v[222:225]\n"
"ds_read_b128 v[190:193], v17 offset:54272\n"
"v_cvt_pk_bf16_f32 v12, v248, v249\n"
"v_mfma_f32_16x16x32_bf16 v[222:225], v[204:207], v[238:241], v[222:225]\n"
"ds_read_b128 v[194:197], v14 offset:44544\n"
"v_mul_f32 v250, v250, v16\n"
"v_mul_f32 v251, v251, v16\n"
"v_cvt_pk_bf16_f32 v13, v250, v251\n"
"s_waitcnt lgkmcnt(4)\n"
"v_mfma_f32_16x16x32_bf16 v[198:201], v[92:95], v[182:185], 0\n"
"v_mul_f32 v212, v212, v16\n"
"v_mul_f32 v213, v213, v16\n"
"v_mfma_f32_16x16x32_bf16 v[202:205], v[96:99], v[182:185], 0\n"
"s_waitcnt lgkmcnt(3)\n"
"ds_read_b128 v[206:209], v17 offset:55296\n"
"v_cvt_pk_bf16_f32 v212, v212, v213\n"
"v_mfma_f32_16x16x32_bf16 v[216:219], v[186:189], v[182:185], 0\n"
"s_waitcnt lgkmcnt(3)\n"
"ds_read_b128 v[182:185], v14 offset:50688\n"
"v_mul_f32 v214, v214, v16\n"
"v_mfma_f32_16x16x32_bf16 v[198:201], v[100:103], v[178:181], v[198:201]\n"
"v_mul_f32 v215, v215, v16\n"
"v_cvt_pk_bf16_f32 v213, v214, v215\n"
"v_mfma_f32_16x16x32_bf16 v[202:205], v[104:107], v[178:181], v[202:205]\n"
"s_waitcnt lgkmcnt(3)\n"
"ds_read_b128 v[186:189], v17 offset:56320\n"
"v_mul_f32 v222, v222, v16\n"
"v_mfma_f32_16x16x32_bf16 v[216:219], v[190:193], v[178:181], v[216:219]\n"
"s_waitcnt lgkmcnt(3)\n"
"ds_read_b128 v[178:181], v14 offset:2048\n"
"v_mul_f32 v223, v223, v16\n"
"v_mfma_f32_16x16x32_bf16 v[198:201], v[0:3], v[194:197], v[198:201]\n"
"v_cvt_pk_bf16_f32 v214, v222, v223\n"
"v_mul_f32 v224, v224, v16\n"
"v_mfma_f32_16x16x32_bf16 v[202:205], v[108:111], v[194:197], v[202:205]\n"
"ds_read_b128 v[190:193], v17 offset:57344\n"
"v_mul_f32 v225, v225, v16\n"
"v_add_u32 v16, s54, v14\n"
"v_cvt_pk_bf16_f32 v215, v224, v225\n"
"s_waitcnt lgkmcnt(4)\n"
"v_mfma_f32_16x16x32_bf16 v[216:219], v[206:209], v[194:197], v[216:219]\n"
"s_waitcnt lgkmcnt(3)\n"
"ds_read_b128 v[194:197], v14 offset:3072\n"
"v_mfma_f32_16x16x32_bf16 v[198:201], v[112:115], v[182:185], v[198:201]\n"
"v_mfma_f32_16x16x32_bf16 v[202:205], v[116:119], v[182:185], v[202:205]\n"
"s_waitcnt lgkmcnt(3)\n"
"ds_read_b128 v[206:209], v17 offset:58368\n"
"v_mfma_f32_16x16x32_bf16 v[216:219], v[186:189], v[182:185], v[216:219]\n"
"s_waitcnt lgkmcnt(3)\n"
"v_mfma_f32_16x16x32_bf16 v[198:201], v[4:7], v[178:181], v[198:201]\n"
"v_mfma_f32_16x16x32_bf16 v[202:205], v[120:123], v[178:181], v[202:205]\n"
"s_waitcnt lgkmcnt(2)\n"
"ds_read_b128 v[182:185], v17 offset:53248\n"
"v_mfma_f32_16x16x32_bf16 v[216:219], v[190:193], v[178:181], v[216:219]\n"
"s_waitcnt lgkmcnt(2)\n"
"v_bfe_u32 v15, v14, 3, 5\n"
"ds_read_u16 v15, v15 offset:1152\n"
"v_mfma_f32_16x16x32_bf16 v[198:201], v[124:127], v[194:197], v[198:201]\n"
"v_mfma_f32_16x16x32_bf16 v[202:205], v[128:131], v[194:197], v[202:205]\n"
"s_barrier\n"
"ds_write_b64 v16, v[12:13] offset:0\n"
"ds_write_b128 v17, v[212:215] offset:7680\n"
"s_waitcnt lgkmcnt(4)\n"
"v_mfma_f32_16x16x32_bf16 v[216:219], v[206:209], v[194:197], v[216:219]\n"
"s_waitcnt lgkmcnt(0)\n"
"ds_read_b128 v[178:181], v17 offset:54272\n"
"s_barrier\n"
"ds_read_b128 v[186:189], v14 offset:32256\n"
"v_cvt_pk_bf16_f32 v198, v198, v199\n"
"v_cvt_pk_bf16_f32 v199, v200, v201\n"
"v_mad_u32_u24 v16, v15, s40, v18\n"
"v_cvt_pk_bf16_f32 v200, v202, v203\n"
"v_cvt_pk_bf16_f32 v201, v204, v205\n"
"v_cvt_pk_bf16_f32 v216, v216, v217\n"
"v_mad_u32_u24 v15, v15, s40, v19\n"
"v_cvt_pk_bf16_f32 v217, v218, v219\n"
"ds_read_b128 v[190:193], v14 offset:38400\n"
"ds_read_b128 v[194:197], v14 offset:44544\n"
"global_store_dwordx4 v16, v[198:201], s[68:69] nt\n"
"s_waitcnt lgkmcnt(2)\n"
"s_nop 0\n"
"v_mfma_f32_16x16x32_bf16 v[198:201], v[92:95], v[186:189], 0\n"
"v_mfma_f32_16x16x32_bf16 v[202:205], v[96:99], v[186:189], 0\n"
"ds_read_b128 v[206:209], v17 offset:55296\n"
"v_mfma_f32_16x16x32_bf16 v[210:213], v[182:185], v[186:189], 0\n"
"s_waitcnt lgkmcnt(2)\n"
"ds_read_b128 v[182:185], v14 offset:50688\n"
"v_mfma_f32_16x16x32_bf16 v[198:201], v[100:103], v[190:193], v[198:201]\n"
"v_mfma_f32_16x16x32_bf16 v[202:205], v[104:107], v[190:193], v[202:205]\n"
"ds_read_b128 v[186:189], v17 offset:56320\n"
"v_mfma_f32_16x16x32_bf16 v[210:213], v[178:181], v[190:193], v[210:213]\n"
"s_waitcnt lgkmcnt(3)\n"
"ds_read_b128 v[178:181], v14 offset:2048\n"
"v_mfma_f32_16x16x32_bf16 v[198:201], v[0:3], v[194:197], v[198:201]\n"
"global_store_dwordx2 v15, v[216:217], s[70:71] nt\n"
"v_mfma_f32_16x16x32_bf16 v[202:205], v[108:111], v[194:197], v[202:205]\n"
"s_waitcnt lgkmcnt(3)\n"
"ds_read_b128 v[190:193], v17 offset:57344\n"
"v_mfma_f32_16x16x32_bf16 v[210:213], v[206:209], v[194:197], v[210:213]\n"
"s_waitcnt lgkmcnt(3)\n"
"ds_read_b128 v[194:197], v14 offset:3072\n"
"v_mfma_f32_16x16x32_bf16 v[198:201], v[112:115], v[182:185], v[198:201]\n"
"v_mfma_f32_16x16x32_bf16 v[202:205], v[116:119], v[182:185], v[202:205]\n"
"s_waitcnt lgkmcnt(3)\n"
"ds_read_b128 v[206:209], v17 offset:58368\n"
"v_mfma_f32_16x16x32_bf16 v[210:213], v[186:189], v[182:185], v[210:213]\n"
"s_waitcnt lgkmcnt(3)\n"
"v_mfma_f32_16x16x32_bf16 v[198:201], v[4:7], v[178:181], v[198:201]\n"
"v_mfma_f32_16x16x32_bf16 v[202:205], v[120:123], v[178:181], v[202:205]\n"
"s_waitcnt lgkmcnt(2)\n"
"v_mfma_f32_16x16x32_bf16 v[210:213], v[190:193], v[178:181], v[210:213]\n"
"s_waitcnt lgkmcnt(1)\n"
"v_bfe_u32 v215, v14, 3, 5\n"
"v_mfma_f32_16x16x32_bf16 v[198:201], v[124:127], v[194:197], v[198:201]\n"
"s_barrier\n"
"ds_read_u16 v215, v215 offset:1184\n"
"v_mfma_f32_16x16x32_bf16 v[202:205], v[128:131], v[194:197], v[202:205]\n"
"s_waitcnt lgkmcnt(1)\n"
"v_mfma_f32_16x16x32_bf16 v[210:213], v[206:209], v[194:197], v[210:213]\n"
"s_waitcnt lgkmcnt(0)\n"
"v_mad_u32_u24 v214, v215, s40, v18\n"
"v_mad_u32_u24 v215, v215, s40, v19\n"
"v_cvt_pk_bf16_f32 v198, v198, v199\n"
"v_cvt_pk_bf16_f32 v199, v200, v201\n"
"s_nop 0\n"
"v_cvt_pk_bf16_f32 v200, v202, v203\n"
"v_cvt_pk_bf16_f32 v201, v204, v205\n"
"global_store_dwordx4 v214, v[198:201], s[68:69] nt\n"
"v_cvt_pk_bf16_f32 v210, v210, v211\n"
"v_cvt_pk_bf16_f32 v211, v212, v213\n"
"global_store_dwordx2 v215, v[210:211], s[70:71] nt\n"
  );
}

__global__ __launch_bounds__(512, 2) void dinat_fused(const uint16_t* __restrict__ x, const uint16_t* __restrict__ wqkv,
                                                      const uint16_t* __restrict__ wo, const uint16_t* __restrict__ rpb,
                                                      uint16_t* __restrict__ out PPARM) {
  __shared__ __attribute__((aligned(256))) char smem[LDS_BYTES];
  PSTART;
  asm volatile("" :: "s"((unsigned)(size_t)(LDS_AS char*)smem));
  const int tid = threadIdx.x;
  const int w = UNI(tid >> 6);
  const int bidx = blockIdx.x;
  const int xcd = bidx & 7, idx = bidx >> 3;
  const int ta = 2 * (xcd >> 1) + (idx >> 4), tb = (idx >> 1) & 7;
  Geo G;
  asm volatile("s_getpc_b64 %0" : "=s"(G.pc));
  G.sr = (ta & 1) ? 224 : -224; G.sc = (tb & 1) ? 1 : -1;
  G.t0 = 224 * (14 * ta + ((ta & 1) ? -3 : 16)) + 14 * tb + ((tb & 1) ? -3 : 16);
  G.er = (ta == 0 || ta == 7) ? 7 : 0; G.ec = (tb == 0 || tb == 7) ? 7 : 0;
  G.so = ((xcd & 1) * 224 + (idx & 1)) * 384;
  if (w < 4) waveA_fast(wqkv, wo, rpb, G, w SARG);
  else waveB_fast(x, wqkv, wo, out, G, w - 4 SARG);
  PEND;
  const u32x4 fl = *LP(const u32x4, FLAG);
  if (__builtin_expect(UNI(fl[0] | fl[1] | fl[2] | fl[3]) != 0, 0)) {
    asm volatile("s_waitcnt vmcnt(0) lgkmcnt(0)\n\ts_barrier" ::: "memory");
    __builtin_amdgcn_s_setprio(0);
    if (w < 4) waveA_safe(wqkv, wo, rpb, G, w);
    else waveB_safe(x, wqkv, wo, out, G, w - 4);
  }
}

extern "C" void kernel_launch(void* const* d_in, const int* in_sizes, int n_in,
                              void* d_out, int out_size, void* d_ws, size_t ws_size,
                              hipStream_t stream) {
  dinat_fused<<<256, 512, 0, stream>>>((const uint16_t*)d_in[0], (const uint16_t*)d_in[1], (const uint16_t*)d_in[2],
                                       (const uint16_t*)d_in[3], (uint16_t*)d_out
  );
}
```

```cpp
#include <hip/hip_runtime.h>
#include <hip/hip_bf16.h>
#include <stdint.h>

typedef __attribute__((ext_vector_type(4))) float f32x4;
typedef __attribute__((ext_vector_type(4))) unsigned u32x4;
typedef __attribute__((ext_vector_type(8))) unsigned u32x8;

#define LDS_AS __attribute__((address_space(3)))
#define DI __device__ __forceinline__
#define SB __builtin_amdgcn_sched_barrier(0)
#define OPQ(v) asm volatile("" : "+v"(v))
#define UNI(v) __builtin_amdgcn_readfirstlane(v)

#define TOFF 0
#define TE 1216
#define RAW 2048
#define PFD 3584
#define O45 2048
#define FLAG 32192
#define TMPA 28672
#define BB 24576
#define CPF 1
#define RBF 1
#define HTA 0
#define SS0 8
#define OID 1
#define PFC 430
#define XR 102400
#define TMPB 53248
#define RESA 4096
#define RESB 77824
#define NREG 36
#define UD0 0x1312030201001110ULL
#define UD1 0x0409050605040807ULL
#define UD2 0x2928171615141918ULL
#define UD3 0x1D1C0D0C0B0A1B1AULL
#define UD4 0x2025212221202423ULL
#define UD5 0x4342333231304140ULL
#define UD6 0x2726373635343938ULL
#define UD7 0x3D3C2D2C2B2A3B3AULL
#define UD8 0x4B4A4D4C47464948ULL
#define UD9 0x6362535251506160ULL
#define UD10 0x4559445755545856ULL
#define UD11 0x6D6C5D5C5B5A6B6AULL
#define UD12 0x6469656665646867ULL
#define UD13 0x6469656665646867ULL
#define UD14 0x6469656665646867ULL
#define UD15 0x6469656665646867ULL
#define CLOB "v24", "v25", "v26", "v27", "v28", "v29", "v30", "v31", "v32", "v33", "v34", "v35", "v36", "v37", "v38", "v39", "v40", "v41", "v42", "v43", "v44", "v45", "v46", "v47", "v48", "v49", "v50", "v51", "v52", "v53", "v54", "v55", "v56", "v57", "v58", "v59", "v60", "v61", "v62", "v63", "v64", "v65", "v66", "v67", "v68", "v69", "v70", "v71", "v72", "v73", "v74", "v75", "v76", "v77", "v78", "v79", "v80", "v81", "v82", "v83", "v84", "v85", "v86", "v87", "v88", "v89", "v90", "v91", "v92", "v93", "v94", "v95", "v96", "v97", "v98", "v99", "v100", "v101", "v102", "v103", "v104", "v105", "v106", "v107", "v108", "v109", "v110", "v111", "v112", "v113", "v114", "v115", "v116", "v117", "v118", "v119", "v120", "v121", "v122", "v123", "v124", "v125", "v126", "v127", "v128", "v129", "v130", "v131", "v132", "v133", "v134", "v135", "v136", "v137", "v138", "v139", "v140", "v141", "v142", "v143", "v144", "v145", "v146", "v147", "v148", "v149", "v150", "v151", "v152", "v153", "v154", "v155", "v156", "v157", "v158", "v159", "v160", "v161", "v162", "v163", "v164", "v165", "v166", "v167", "v168", "v169", "v170", "v171", "v172", "v173", "v174", "v175", "v176", "v177", "v178", "v179", "v180", "v181", "v182", "v183", "v184", "v185", "v186", "v187", "v188", "v189", "v190", "v191", "v192", "v193", "v194", "v195", "v196", "v197", "v198", "v199", "v200", "v201", "v202", "v203", "v204", "v205", "v206", "v207", "v208", "v209", "v210", "v211", "v212", "v213", "v214", "v215", "v216", "v217", "v218", "v219", "v220", "v221", "v222", "v223", "v224", "v225", "v226", "v227", "v228", "v229", "v230", "v231", "v232", "v233", "v234", "v235", "v236", "v237", "v238", "v239", "v240", "v241", "v242", "v243", "v244", "v245", "v246", "v247", "v248", "v249", "v250", "v251", "v252", "v253", "v254", "v255"
#define PRE "s_and_b32 s40, s44, 3\n" "s_mul_i32 s41, s40, 96\n" "s_add_u32 s46, s46, s41\n" "s_addc_u32 s47, s47, 0\n" "s_mul_i32 s54, s40, 6144\n"
#define LDS_BYTES 163840
#define LOG2E 1.4426950408889634f
#define LP(T, o) ((LDS_AS T*)(size_t)(unsigned)(o))

DI const char* uni(const void* p) {
  unsigned long long sv = (unsigned long long)p;
  unsigned lo = UNI((unsigned)sv), hi = UNI((unsigned)(sv >> 32));
  return (const char*)(((unsigned long long)hi << 32) | lo);
}
#define DMA(src, voff, dst) asm volatile("s_mov_b32 m0, %2\n\ts_nop 0\n\tglobal_load_lds_dwordx4 %0, %1" :: "v"(voff), "s"(src), "s"(dst) : "memory", "m0")
#define DMA4(src, voff, dst) asm volatile("s_mov_b32 m0, %2\n\ts_nop 0\n\tglobal_load_lds_dword %0, %1" :: "v"(voff), "s"(src), "s"(dst) : "memory", "m0")
#define RAWB() asm volatile("s_barrier" ::: "memory")
#define WVM(n) asm volatile("s_waitcnt vmcnt(%0)" :: "n"(n) : "memory")
#define PSTART
#define PEND
#define PPARM
#define SPARM
#define SARG
#define PCLOB

struct Geo { int t0, sr, sc, er, ec, so; unsigned long long pc; };
DI int tok_idx(const Geo& G, int hr, int hc) {
  return G.t0 + G.sr * (hr - (hr >= 17 ? G.er : 0)) + G.sc * (hc - (hc >= 17 ? G.ec : 0));
}
template <int STR, int HB>
DI void mk_lo(int l, unsigned (&lo)[3], int h = 0) {
#pragma unroll
  for (int i = 0; i < 3; ++i) {
    const int idx = 64 * i + l, t = (idx * 171) >> 10, c = idx - 6 * t;
    const int r = HB ? ((t >> 3) & 1) * HB + ((t >> 1) & 8) + (t & 7) : ((t & ~12) | ((t & 4) << 1) | ((t & 8) >> 1));
    lo[i] = r * STR + c * 16 + (HB ? (c < 4 ? 64 * h : 192 + 32 * h) : 0);
    OPQ(lo[i]);
  }
}

DI int lane_id() {
  int l;
  asm volatile("v_mbcnt_lo_u32_b32 %0, -1, 0\n\tv_mbcnt_hi_u32_b32 %0, -1, %0" : "=v"(l));
  return l;
}
struct Regs { unsigned r[12]; u32x8 p; u32x4 q; };
#define XDIR(a0, b0, a1, b1, dst) do { \
  const unsigned xv = xl + UNI(3072 * (G.sr * (h >= 2 ? a1 : a0) + G.sc * (h >= 2 ? b1 : b0))); \
  DMA(xs, xv, sXB + (dst)); DMA(xs + 128, xv, sXB + (dst) + 2048); DMA(xs + 256, xv, sXB + (dst) + 4096); \
} while (0)

DI void prologue(const uint16_t* x, const uint16_t* wqkv, const uint16_t* wo, const uint16_t* rpb, const Geo& G, int w, Regs& R) {
  const int l = lane_id() & 63, h = w & 3;
  const int u = l & 15, g = l >> 4;
  const int tk = 8 * (h & 1) + (l >> 3);
  const unsigned ck16 = 16 * ((l & 7) ^ (tk >> 1));
  if (w < 4) {
    const int rb = (338 * h) & ~3;
    unsigned r0 = min(rb + 4 * l, 1348), r1 = min(rb + 128 + 4 * l, 1348);
    const int cu = (blockIdx.x >> 3) & 31, line = cu * 72 + 18 * h + l;
    unsigned po = 128 * (l >= 18 ? min(16 * cu + 4 * h + min(l - 18, 3), PFC) : (line < 1728 ? line : line - 1728));
    OPQ(po); OPQ(r0); OPQ(r1);
    SB;
    const char* rp = uni(rpb);
    DMA4(rp, r0, UNI(RAW + 384 * h));
    DMA4(rp, r1, UNI(RAW + 384 * h + 128));
    SB;
    const unsigned long long pb = l >= 18 ? G.pc : (line < 1728 ? (unsigned long long)wqkv : (unsigned long long)wo);
    asm volatile("s_mov_b32 m0, %1\n\ts_nop 0\n\tglobal_load_lds_dword %0, off" :: "v"(pb + po), "s"(PFD) : "memory", "m0");
  } else {
    unsigned xl = (G.t0 + G.sr * (tk >> 2) + G.sc * (tk & 3)) * 768 + ck16;
    OPQ(xl);
    const unsigned sXB = UNI(6144 * (h >> 1) + 1024 * (h & 1));
    SB;
    const char* xs = uni((const char*)x + G.so);
  XDIR(0, 0, 0, 1, 126976); SB;
  XDIR(1, 0, 1, 1, 139264); SB;
  XDIR(0, 2, 2, 0, 151552); SB;
  }
  SB;
  unsigned lo3[3];
  mk_lo<1152, 0>(l, lo3);
  R.r[0] = lo3[0]; R.r[1] = lo3[1]; R.r[2] = lo3[2];
  unsigned wrow = 6144 * h + (16 * (g >> 1) + 4 * (g & 1) + (u >> 2)) * 96;
  unsigned xa = XR + u * 128 + 16 * (g ^ (u >> 1));
  {
    const int e = min(64 * w + l, 399), blk = e >> 4, a = (blk * 205) >> 10, b = blk - 5 * a;
    *LP(uint16_t, TOFF + 2 * e) = tok_idx(G, 4 * a + ((e >> 2) & 3), 4 * b + (e & 3));
  }
  OPQ(wrow); OPQ(xa);
  SB;
  int w2 = w;
  asm volatile("" : "+s"(w2));
  if (w2 < 4) {
    const char* wq_ = uni((const char*)wqkv + 96 * h);
    const unsigned sW = UNI(TMPA + 6144 * h);
   SB;
   SB;
  DMA(wq_ + 384, lo3[0], sW + (-24576)); SB;
  DMA(wq_ + 384, lo3[1], sW + (-23552)); SB;
  DMA(wq_ + 384, lo3[2], sW + (-22528)); SB;
    R.r[3] = wrow + 8 * (u & 3); R.r[4] = xa; R.r[5] = xa ^ 64; R.r[6] = 16 * l + 6144 * h; R.r[7] = 6144 * h + 336 * g;
    *LP(uint16_t, PFD + 256) = 0xF180;
    const int row = l >> 2, qd = l & 3;
    const int rho = row >= 7 ? row - 7 : row + 3;
    const int src = RAW + 384 * h + ((338 * h) & 2) + 26 * (G.sr < 0 ? 12 - rho : rho) + (G.sc < 0 ? 24 : 0);
    R.r[8] = sW + 64 + 336 * row + (row >= 7 ? 32 : 0) + 16 * qd;
    R.r[9] = sW + (l < 10 ? 384 + 336 * l + (l >= 7 ? 32 : 0) : (l < 14 ? 16 * (l - 10) : (l < 16 ? 2416 + 16 * (l - 14) : 3456 + 16 * (l & 1))));
    R.r[10] = R.r[11] = 0;
#pragma unroll
    for (int e = 0; e < 7; ++e) R.p[e] = (unsigned)(4 * qd + e - 3) <= 6u ? src + 2 * G.sc * (4 * qd + e) : PFD + 256;
    R.p[7] = 0;
#pragma unroll
    for (int i = 0; i < 4; ++i) R.q[i] = (qd < 3 && i <= qd + 1) ? src + 2 * G.sc * (8 - qd + i - (i >= 1 ? 7 : 0)) : PFD + 256;
  } else {
    unsigned lo4[3], lo5[3];
    mk_lo<384, 16>(l, lo4, h); mk_lo<384, 48>(l, lo5, h);
#pragma unroll
    for (int i = 0; i < 3; ++i) { R.r[3 + i] = lo4[i]; R.r[6 + i] = lo5[i]; }
    R.r[9] = 4 * l + BB + 6144 * h; R.r[10] = BB + wrow + 8 * (u & 3); R.r[11] = BB + wrow + 16 * (u & 3);
    R.p = (u32x8){xa, xa ^ 64, (OID ? 4u : 16u) * l, 2u * tk, ck16, 16u * l + BB + 6144 * h, 16u * u, 8u * u};
    R.q = (u32x4){XR + 1552u * (u >> 2) + 64u * (u & 3) + 16u * h + 4u * g, 2u * (4 * h + g), 0, 0};
    const unsigned long long d0 = h == 0 ? UD0 : (h == 1 ? UD4 : (h == 2 ? UD8 : UD12)), d1 = h == 0 ? UD1 : (h == 1 ? UD5 : (h == 2 ? UD9 : UD13));
    const unsigned long long d2 = h == 0 ? UD2 : (h == 1 ? UD6 : (h == 2 ? UD10 : UD14)), d3 = h == 0 ? UD3 : (h == 1 ? UD7 : (h == 2 ? UD11 : UD15));
    const unsigned long long d = g < 2 ? (g == 0 ? d0 : d1) : (g == 2 ? d2 : d3);
    const int by = (int)(d >> (8 * (u >> 1))) & 255, c = by & 15, r = 2 * (by >> 4) + (u & 1);
    const int e = 16 * min(4 * h + g, 12) + u;
    *LP(uint16_t, TOFF + 800 + 2 * e) = G.t0 + G.sr * (r + 3) + G.sc * (c + 3);
    const int cp = 64 * (c & 3) - 16 * (c >> 2);
    const int dl = (G.ec && c >= 11) ? 16 * c + 16 - cp : 0;
    *LP(unsigned, TE + 4 * e) = (unsigned)(TMPA + 64 + cp - 336 * r) | ((unsigned)dl << 16);
  }
}
#define BODY(ta, tb) do { \
  const char *pq = uni(wqkv), *px = uni(x), *po = uni(out); \
  asm volatile(PRE "s_waitcnt lgkmcnt(0)\ns_cmp_lt_u32 s44, 4\ns_cbranch_scc0 1f\n" ta "s_branch 2f\n1:\n" tb "2:\n" \
  : "+{v0}"(R.r[0]), "+{v1}"(R.r[1]), "+{v2}"(R.r[2]), "+{v3}"(R.r[3]), "+{v4}"(R.r[4]), "+{v5}"(R.r[5]), "+{v6}"(R.r[6]), "+{v7}"(R.r[7]), \
    "+{v8}"(R.r[8]), "+{v9}"(R.r[9]), "+{v10}"(R.r[10]), "+{v11}"(R.r[11]), "+{v[12:19]}"(R.p), "+{v[20:23]}"(R.q), \
    "+{s[46:47]}"(pq), "+{s[56:57]}"(px), "+{s[66:67]}"(po) \
  : "{s44}"(w), "{s45}"(G.so | G.er), "{s[48:49]}"(uni(wo)) \
  : CLOB, "s40", "s41", "s42", "s43", "s50", "s51", "s52", "s53", "s54", "s55", "s58", "s59", "s60", "s61", "s62", "s63", "s64", "s65", \
    "s68", "s69", "s70", "s71", "s72", "s73", "s74", "s75", "vcc", "scc", "m0", "memory" PCLOB); } while (0)

__global__ __launch_bounds__(512, 2) void dinat_fused(const uint16_t* __restrict__ x, const uint16_t* __restrict__ wqkv,
                                                      const uint16_t* __restrict__ wo, const uint16_t* __restrict__ rpb,
                                                      uint16_t* __restrict__ out PPARM) {
  __shared__ __attribute__((aligned(256))) char smem[LDS_BYTES];
  PSTART;
  asm volatile("" :: "s"((unsigned)(size_t)(LDS_AS char*)smem));
  const int tid = threadIdx.x;
  const int w = UNI(tid >> 6);
  Geo G;
  asm volatile("s_getpc_b64 %0" : "=s"(G.pc));
  if (CPF) DMA4(G.pc, 128 * ((tid >> 4) + 2), UNI(PFD));
  SB;
  const int bidx = blockIdx.x;
  const int xcd = bidx & 7, idx = bidx >> 3;
  const int ta = 2 * (xcd >> 1) + (idx >> 4), tb = (idx >> 1) & 7;
  G.sr = (ta & 1) ? 224 : -224; G.sc = (tb & 1) ? 1 : -1;
  G.t0 = 224 * (14 * ta + ((ta & 1) ? -3 : 16)) + 14 * tb + ((tb & 1) ? -3 : 16);
  G.er = (ta == 0 || ta == 7) ? 7 : 0; G.ec = (tb == 0 || tb == 7) ? 7 : 0;
  G.so = ((xcd & 1) * 224 + (idx & 1)) * 384;
  {
    Regs R;
    prologue(x, wqkv, wo, rpb, G, w, R);
    BODY(
"s_add_u32 s50, s54, 28672\n"
"s_bitcmp1_b32 s45, 0\n"
"s_cselect_b32 s53, -1, 0\n"
"s_lshl_b32 s52, s53, 16\n"
"s_setprio 1\n"
"s_mov_b32 s60, 0x3e553b95\n"
"s_mov_b32 s61, 0x3fb8aa3b\n"
"v_add_u32 v27, 77824, v3\n"
"v_mov_b32 v24, 0\n"
"v_mov_b32 v25, 0\n"
"v_mov_b32 v178, 0xf1800000\n"
"v_mov_b32 v179, 0xf1800000\n"
"v_mov_b32 v180, 0xf1800000\n"
"v_mov_b32 v181, 0xf1800000\n"
"s_bfm_b64 exec, 18, 0\n"
"ds_write_b128 v9, v[178:181] offset:0\n"
"s_mov_b64 exec, -1\n"
"s_waitcnt vmcnt(4)\n"
"ds_read_u16 v13, v13 offset:0\n"
"ds_read_u16 v14, v14 offset:0\n"
"ds_read_u16 v15, v15 offset:0\n"
"ds_read_u16 v16, v16 offset:0\n"
"ds_read_u16 v17, v17 offset:0\n"
"ds_read_u16 v18, v18 offset:0\n"
"ds_read_u16 v12, v12 offset:0\n"
"ds_read_u16 v20, v20 offset:0\n"
"ds_read_u16 v21, v21 offset:0\n"
"ds_read_u16 v22, v22 offset:0\n"
"ds_read_u16 v23, v23 offset:0\n"
"s_waitcnt lgkmcnt(10)\n"
"v_lshlrev_b32 v13, 16, v13\n"
"s_waitcnt lgkmcnt(9)\n"
"v_lshlrev_b32 v14, 16, v14\n"
"v_mul_f32 v13, s61, v13\n"
"s_waitcnt lgkmcnt(8)\n"
"v_lshlrev_b32 v15, 16, v15\n"
"v_mul_f32 v14, s61, v14\n"
"s_waitcnt lgkmcnt(7)\n"
"v_lshlrev_b32 v16, 16, v16\n"
"v_mul_f32 v15, s61, v15\n"
"s_waitcnt lgkmcnt(6)\n"
"v_lshlrev_b32 v17, 16, v17\n"
"v_mul_f32 v16, s61, v16\n"
"s_waitcnt lgkmcnt(5)\n"
"v_lshlrev_b32 v18, 16, v18\n"
"v_mul_f32 v17, s61, v17\n"
"s_waitcnt lgkmcnt(4)\n"
"v_lshlrev_b32 v12, 16, v12\n"
"v_mul_f32 v18, s61, v18\n"
"s_waitcnt lgkmcnt(3)\n"
"v_lshlrev_b32 v20, 16, v20\n"
"v_mul_f32 v12, s61, v12\n"
"s_waitcnt lgkmcnt(2)\n"
"v_lshlrev_b32 v21, 16, v21\n"
"v_mul_f32 v20, s61, v20\n"
"s_waitcnt lgkmcnt(1)\n"
"v_lshlrev_b32 v22, 16, v22\n"
"v_mul_f32 v21, s61, v21\n"
"s_waitcnt lgkmcnt(0)\n"
"v_lshlrev_b32 v23, 16, v23\n"
"v_mul_f32 v22, s61, v22\n"
"v_mul_f32 v23, s61, v23\n"
"v_mov_b32 v178, v13\n"
"v_mov_b32 v179, v14\n"
"v_mov_b32 v180, v15\n"
"v_mov_b32 v181, v16\n"
"v_mov_b32 v182, v17\n"
"v_mov_b32 v183, v18\n"
"s_bfm_b64 exec, 40, 0\n"
"ds_write_b128 v8, v[180:183] offset:0\n"
"ds_write_b128 v8, v[14:17] offset:64\n"
"ds_write_b128 v8, v[178:181] offset:128\n"
"ds_write_b128 v8, v[12:15] offset:192\n"
"ds_write_b128 v8, v[20:23] offset:256\n"
"s_mov_b64 exec, -1\n"
"s_add_u32 s42, s46, 37248\n"
"s_addc_u32 s43, s47, 0\n"
"s_add_u32 m0, s50, 4294945792\n"
"s_nop 0\n"
"global_load_lds_dwordx4 v0, s[42:43]\n"
"s_add_u32 m0, s50, 4294946816\n"
"s_nop 0\n"
"global_load_lds_dwordx4 v1, s[42:43]\n"
"s_add_u32 m0, s50, 4294947840\n"
"s_nop 0\n"
"global_load_lds_dwordx4 v2, s[42:43]\n"
"s_add_u32 s42, s46, 74112\n"
"s_addc_u32 s43, s47, 0\n"
"s_add_u32 m0, s50, 49152\n"
"s_nop 0\n"
"global_load_lds_dwordx4 v0, s[42:43]\n"
"s_add_u32 m0, s50, 50176\n"
"s_nop 0\n"
"global_load_lds_dwordx4 v1, s[42:43]\n"
"s_add_u32 m0, s50, 51200\n"
"s_nop 0\n"
"global_load_lds_dwordx4 v2, s[42:43]\n"
"s_barrier\n"
"ds_read_b128 v[178:181], v4 offset:24576\n"
"s_waitcnt vmcnt(6)\n"
"ds_read_b64_tr_b16 v[10:11], v3 offset:4864\n"
"ds_read_b64_tr_b16 v[8:9], v3 offset:4096\n"
"ds_read_b64_tr_b16 v[12:13], v3 offset:4128\n"
"ds_read_b64_tr_b16 v[14:15], v3 offset:4896\n"
"ds_read_b64_tr_b16 v[16:17], v3 offset:4160\n"
"ds_read_b64_tr_b16 v[18:19], v3 offset:4928\n"
"ds_read_b128 v[182:185], v4 offset:30720\n"
"ds_read_b128 v[186:189], v4 offset:36864\n"
"ds_read_b128 v[190:193], v4 offset:43008\n"
"s_waitcnt lgkmcnt(7)\n"
"v_mfma_f32_16x16x32_bf16 v[196:199], v[8:11], v[178:181], 0\n"
"s_waitcnt lgkmcnt(5)\n"
"v_mfma_f32_16x16x32_bf16 v[212:215], v[12:15], v[178:181], 0\n"
"s_waitcnt lgkmcnt(3)\n"
"v_mfma_f32_16x16x32_bf16 v[216:219], v[16:19], v[178:181], 0\n"
"s_waitcnt lgkmcnt(2)\n"
"v_mfma_f32_16x16x32_bf16 v[202:205], v[8:11], v[182:185], 0\n"
"v_mfma_f32_16x16x32_bf16 v[220:223], v[12:15], v[182:185], 0\n"
"ds_read_b128 v[178:181], v4 offset:49152\n"
"v_mfma_f32_16x16x32_bf16 v[236:239], v[16:19], v[182:185], 0\n"
"s_waitcnt lgkmcnt(2)\n"
"v_mfma_f32_16x16x32_bf16 v[226:229], v[8:11], v[186:189], 0\n"
"s_waitcnt vmcnt(3)\n"
"ds_read_b64_tr_b16 v[22:23], v3 offset:7936\n"
"v_mfma_f32_16x16x32_bf16 v[240:243], v[12:15], v[186:189], 0\n"
"ds_read_b64_tr_b16 v[20:21], v3 offset:7168\n"
"v_mfma_f32_16x16x32_bf16 v[244:247], v[16:19], v[186:189], 0\n"
"s_waitcnt lgkmcnt(3)\n"
"ds_read_b128 v[182:185], v4 offset:55296\n"
"v_mfma_f32_16x16x32_bf16 v[232:235], v[8:11], v[190:193], 0\n"
"ds_read_b128 v[36:39], v5 offset:36864\n"
"v_mfma_f32_16x16x32_bf16 v[248:251], v[12:15], v[190:193], 0\n"
"v_mfma_f32_16x16x32_bf16 v[252:255], v[16:19], v[190:193], 0\n"
"s_waitcnt lgkmcnt(4)\n"
"ds_read_b128 v[186:189], v5 offset:24576\n"
"v_mfma_f32_16x16x32_bf16 v[208:211], v[8:11], v[178:181], 0\n"
"ds_read_b64_tr_b16 v[30:31], v3 offset:7968\n"
"v_mfma_f32_16x16x32_bf16 v[190:193], v[12:15], v[178:181], 0\n"
"ds_read_b64_tr_b16 v[28:29], v3 offset:7200\n"
"v_mfma_f32_16x16x32_bf16 v[84:87], v[16:19], v[178:181], 0\n"
"s_waitcnt lgkmcnt(4)\n"
"ds_read_b64_tr_b16 v[34:35], v3 offset:8000\n"
"v_mfma_f32_16x16x32_bf16 v[178:181], v[8:11], v[182:185], 0\n"
"ds_read_b64_tr_b16 v[32:33], v3 offset:7232\n"
"v_mfma_f32_16x16x32_bf16 v[88:91], v[12:15], v[182:185], 0\n"
"v_mfma_f32_16x16x32_bf16 v[92:95], v[16:19], v[182:185], 0\n"
"s_waitcnt lgkmcnt(4)\n"
"ds_read_b128 v[182:185], v5 offset:30720\n"
"v_mfma_f32_16x16x32_bf16 v[196:199], v[20:23], v[186:189], v[196:199]\n"
"s_waitcnt lgkmcnt(3)\n"
"v_mfma_f32_16x16x32_bf16 v[212:215], v[28:31], v[186:189], v[212:215]\n"
"s_waitcnt lgkmcnt(1)\n"
"ds_read_b128 v[40:43], v5 offset:43008\n"
"v_mfma_f32_16x16x32_bf16 v[216:219], v[32:35], v[186:189], v[216:219]\n"
"s_waitcnt lgkmcnt(1)\n"
"v_mfma_f32_16x16x32_bf16 v[202:205], v[20:23], v[182:185], v[202:205]\n"
"ds_read_b128 v[186:189], v5 offset:49152\n"
"v_mfma_f32_16x16x32_bf16 v[220:223], v[28:31], v[182:185], v[220:223]\n"
"v_mfma_f32_16x16x32_bf16 v[236:239], v[32:35], v[182:185], v[236:239]\n"
"ds_read_b128 v[182:185], v5 offset:55296\n"
"s_waitcnt vmcnt(0)\n"
"v_mfma_f32_16x16x32_bf16 v[226:229], v[20:23], v[36:39], v[226:229]\n"
"v_mfma_f32_16x16x32_bf16 v[240:243], v[28:31], v[36:39], v[240:243]\n"
"ds_read_b128 v[48:51], v4 offset:26624\n"
"v_mfma_f32_16x16x32_bf16 v[244:247], v[32:35], v[36:39], v[244:247]\n"
"s_waitcnt lgkmcnt(3)\n"
"ds_read_b64_tr_b16 v[36:37], v27 offset:0\n"
"v_mfma_f32_16x16x32_bf16 v[232:235], v[20:23], v[40:43], v[232:235]\n"
"ds_read_b64_tr_b16 v[44:45], v27 offset:64\n"
"v_mfma_f32_16x16x32_bf16 v[248:251], v[28:31], v[40:43], v[248:251]\n"
"v_mfma_f32_16x16x32_bf16 v[252:255], v[32:35], v[40:43], v[252:255]\n"
"s_waitcnt lgkmcnt(4)\n"
"ds_read_b64_tr_b16 v[40:41], v27 offset:32\n"
"v_mfma_f32_16x16x32_bf16 v[208:211], v[20:23], v[186:189], v[208:211]\n"
"ds_read_b64_tr_b16 v[38:39], v27 offset:768\n"
"v_mfma_f32_16x16x32_bf16 v[190:193], v[28:31], v[186:189], v[190:193]\n"
"ds_read_b64_tr_b16 v[46:47], v27 offset:832\n"
"v_mfma_f32_16x16x32_bf16 v[84:87], v[32:35], v[186:189], v[84:87]\n"
"s_waitcnt lgkmcnt(6)\n"
"ds_read_b64_tr_b16 v[42:43], v27 offset:800\n"
"v_mfma_f32_16x16x32_bf16 v[178:181], v[20:23], v[182:185], v[178:181]\n"
"ds_read_b128 v[52:55], v4 offset:45056\n"
"v_mfma_f32_16x16x32_bf16 v[88:91], v[28:31], v[182:185], v[88:91]\n"
"v_mfma_f32_16x16x32_bf16 v[92:95], v[32:35], v[182:185], v[92:95]\n"
"s_waitcnt lgkmcnt(3)\n"
"ds_read_b128 v[182:185], v4 offset:32768\n"
"v_mfma_f32_16x16x32_bf16 v[196:199], v[36:39], v[48:51], v[196:199]\n"
"s_waitcnt lgkmcnt(2)\n"
"ds_read_b128 v[186:189], v4 offset:38912\n"
"v_mfma_f32_16x16x32_bf16 v[212:215], v[40:43], v[48:51], v[212:215]\n"
"v_mfma_f32_16x16x32_bf16 v[216:219], v[44:47], v[48:51], v[216:219]\n"
"s_barrier\n"
"s_waitcnt lgkmcnt(1)\n"
"v_mfma_f32_16x16x32_bf16 v[202:205], v[36:39], v[182:185], v[202:205]\n"
"v_mfma_f32_16x16x32_bf16 v[220:223], v[40:43], v[182:185], v[220:223]\n"
"v_mfma_f32_16x16x32_bf16 v[236:239], v[44:47], v[182:185], v[236:239]\n"
"s_waitcnt lgkmcnt(0)\n"
"ds_read_b64_tr_b16 v[48:49], v27 offset:3072\n"
"v_mfma_f32_16x16x32_bf16 v[226:229], v[36:39], v[186:189], v[226:229]\n"
"ds_read_b128 v[56:59], v4 offset:51200\n"
"v_mfma_f32_16x16x32_bf16 v[240:243], v[40:43], v[186:189], v[240:243]\n"
"ds_read_b128 v[182:185], v4 offset:57344\n"
"v_mfma_f32_16x16x32_bf16 v[244:247], v[44:47], v[186:189], v[244:247]\n"
"v_mfma_f32_16x16x32_bf16 v[232:235], v[36:39], v[52:55], v[232:235]\n"
"ds_read_b128 v[64:67], v5 offset:45056\n"
"v_mfma_f32_16x16x32_bf16 v[248:251], v[40:43], v[52:55], v[248:251]\n"
"ds_read_b128 v[186:189], v5 offset:26624\n"
"v_mfma_f32_16x16x32_bf16 v[252:255], v[44:47], v[52:55], v[252:255]\n"
"s_waitcnt lgkmcnt(3)\n"
"ds_read_b64_tr_b16 v[52:53], v27 offset:3104\n"
"v_mfma_f32_16x16x32_bf16 v[208:211], v[36:39], v[56:59], v[208:211]\n"
"v_mfma_f32_16x16x32_bf16 v[190:193], v[40:43], v[56:59], v[190:193]\n"
"ds_read_b64_tr_b16 v[50:51], v27 offset:3840\n"
"v_mfma_f32_16x16x32_bf16 v[84:87], v[44:47], v[56:59], v[84:87]\n"
"s_waitcnt lgkmcnt(4)\n"
"ds_read_b64_tr_b16 v[58:59], v27 offset:3904\n"
"v_mfma_f32_16x16x32_bf16 v[178:181], v[36:39], v[182:185], v[178:181]\n"
"ds_read_b64_tr_b16 v[54:55], v27 offset:3872\n"
"v_mfma_f32_16x16x32_bf16 v[88:91], v[40:43], v[182:185], v[88:91]\n"
"ds_read_b64_tr_b16 v[56:57], v27 offset:3136\n"
"v_mfma_f32_16x16x32_bf16 v[92:95], v[44:47], v[182:185], v[92:95]\n"
"s_waitcnt lgkmcnt(3)\n"
"ds_read_b128 v[182:185], v5 offset:32768\n"
"v_mfma_f32_16x16x32_bf16 v[196:199], v[48:51], v[186:189], v[196:199]\n"
"s_waitcnt lgkmcnt(2)\n"
"ds_read_b128 v[80:83], v4 offset:28672\n"
"v_mfma_f32_16x16x32_bf16 v[212:215], v[52:55], v[186:189], v[212:215]\n"
"s_waitcnt lgkmcnt(2)\n"
"ds_read_b128 v[60:63], v5 offset:38912\n"
"v_mfma_f32_16x16x32_bf16 v[216:219], v[56:59], v[186:189], v[216:219]\n"
"s_waitcnt lgkmcnt(2)\n"
"v_mfma_f32_16x16x32_bf16 v[202:205], v[48:51], v[182:185], v[202:205]\n"
"v_mfma_f32_16x16x32_bf16 v[220:223], v[52:55], v[182:185], v[220:223]\n"
"v_mfma_f32_16x16x32_bf16 v[236:239], v[56:59], v[182:185], v[236:239]\n"
"s_waitcnt lgkmcnt(0)\n"
"v_mfma_f32_16x16x32_bf16 v[226:229], v[48:51], v[60:63], v[226:229]\n"
"ds_read_b128 v[182:185], v5 offset:57344\n"
"v_mfma_f32_16x16x32_bf16 v[240:243], v[52:55], v[60:63], v[240:243]\n"
"ds_read_b128 v[186:189], v5 offset:51200\n"
"v_mfma_f32_16x16x32_bf16 v[244:247], v[56:59], v[60:63], v[244:247]\n"
"s_barrier\n"
"v_mfma_f32_16x16x32_bf16 v[232:235], v[48:51], v[64:67], v[232:235]\n"
"ds_read_b64_tr_b16 v[70:71], v27 offset:25408\n"
"v_mfma_f32_16x16x32_bf16 v[248:251], v[52:55], v[64:67], v[248:251]\n"
"ds_read_b64_tr_b16 v[60:61], v27 offset:24576\n"
"v_mfma_f32_16x16x32_bf16 v[252:255], v[56:59], v[64:67], v[252:255]\n"
"s_waitcnt lgkmcnt(2)\n"
"v_mfma_f32_16x16x32_bf16 v[208:211], v[48:51], v[186:189], v[208:211]\n"
"ds_read_b64_tr_b16 v[66:67], v27 offset:25376\n"
"v_mfma_f32_16x16x32_bf16 v[190:193], v[52:55], v[186:189], v[190:193]\n"
"ds_read_b64_tr_b16 v[62:63], v27 offset:25344\n"
"v_mfma_f32_16x16x32_bf16 v[84:87], v[56:59], v[186:189], v[84:87]\n"
"ds_read_b64_tr_b16 v[64:65], v27 offset:24608\n"
"ds_read_b64_tr_b16 v[68:69], v27 offset:24640\n"
"v_mfma_f32_16x16x32_bf16 v[178:181], v[48:51], v[182:185], v[178:181]\n"
"ds_read_b64_tr_b16 v[72:73], v27 offset:27648\n"
"v_mfma_f32_16x16x32_bf16 v[88:91], v[52:55], v[182:185], v[88:91]\n"
"ds_read_b64_tr_b16 v[74:75], v27 offset:28416\n"
"v_mfma_f32_16x16x32_bf16 v[92:95], v[56:59], v[182:185], v[92:95]\n"
"s_waitcnt lgkmcnt(4)\n"
"ds_read_b128 v[182:185], v4 offset:34816\n"
"ds_read_b64_tr_b16 v[76:77], v27 offset:27680\n"
"v_mfma_f32_16x16x32_bf16 v[196:199], v[60:63], v[80:83], v[196:199]\n"
"s_waitcnt lgkmcnt(5)\n"
"ds_read_b64_tr_b16 v[78:79], v27 offset:28448\n"
"v_mfma_f32_16x16x32_bf16 v[212:215], v[64:67], v[80:83], v[212:215]\n"
"s_waitcnt lgkmcnt(5)\n"
"v_mfma_f32_16x16x32_bf16 v[216:219], v[68:71], v[80:83], v[216:219]\n"
"s_waitcnt lgkmcnt(2)\n"
"ds_read_b64_tr_b16 v[80:81], v27 offset:27712\n"
"ds_read_b64_tr_b16 v[82:83], v27 offset:28480\n"
"v_mfma_f32_16x16x32_bf16 v[202:205], v[60:63], v[182:185], v[202:205]\n"
"v_mfma_f32_16x16x32_bf16 v[220:223], v[64:67], v[182:185], v[220:223]\n"
"ds_read_b128 v[186:189], v4 offset:40960\n"
"ds_read_b128 v[96:99], v4 offset:47104\n"
"v_mfma_f32_16x16x32_bf16 v[236:239], v[68:71], v[182:185], v[236:239]\n"
"s_waitcnt lgkmcnt(2)\n"
"s_barrier\n"
"s_waitcnt lgkmcnt(1)\n"
"v_mfma_f32_16x16x32_bf16 v[226:229], v[60:63], v[186:189], v[226:229]\n"
"ds_read_b128 v[100:103], v5 offset:34816\n"
"ds_read_b128 v[108:111], v4 offset:53248\n"
"v_mfma_f32_16x16x32_bf16 v[240:243], v[64:67], v[186:189], v[240:243]\n"
"v_mfma_f32_16x16x32_bf16 v[244:247], v[68:71], v[186:189], v[244:247]\n"
"s_waitcnt lgkmcnt(2)\n"
"ds_read_b128 v[182:185], v5 offset:28672\n"
"v_mfma_f32_16x16x32_bf16 v[232:235], v[60:63], v[96:99], v[232:235]\n"
"v_mfma_f32_16x16x32_bf16 v[248:251], v[64:67], v[96:99], v[248:251]\n"
"v_mfma_f32_16x16x32_bf16 v[252:255], v[68:71], v[96:99], v[252:255]\n"
"s_waitcnt lgkmcnt(1)\n"
"ds_read_b128 v[116:119], v4 offset:59392\n"
"ds_read_b128 v[104:107], v5 offset:40960\n"
"ds_read_b128 v[186:189], v5 offset:47104\n"
"v_mfma_f32_16x16x32_bf16 v[208:211], v[60:63], v[108:111], v[208:211]\n"
"v_mfma_f32_16x16x32_bf16 v[190:193], v[64:67], v[108:111], v[190:193]\n"
"ds_read_b128 v[112:115], v5 offset:53248\n"
"v_mfma_f32_16x16x32_bf16 v[84:87], v[68:71], v[108:111], v[84:87]\n"
"s_waitcnt lgkmcnt(3)\n"
"v_mfma_f32_16x16x32_bf16 v[178:181], v[60:63], v[116:119], v[178:181]\n"
"v_mfma_f32_16x16x32_bf16 v[88:91], v[64:67], v[116:119], v[88:91]\n"
"ds_read_b128 v[120:123], v5 offset:59392\n"
"v_mfma_f32_16x16x32_bf16 v[92:95], v[68:71], v[116:119], v[92:95]\n"
"v_mfma_f32_16x16x32_bf16 v[196:199], v[72:75], v[182:185], v[196:199]\n"
"v_mfma_f32_16x16x32_bf16 v[212:215], v[76:79], v[182:185], v[212:215]\n"
"v_mfma_f32_16x16x32_bf16 v[216:219], v[80:83], v[182:185], v[216:219]\n"
"v_mfma_f32_16x16x32_bf16 v[202:205], v[72:75], v[100:103], v[202:205]\n"
"v_mfma_f32_16x16x32_bf16 v[220:223], v[76:79], v[100:103], v[220:223]\n"
"v_mfma_f32_16x16x32_bf16 v[236:239], v[80:83], v[100:103], v[236:239]\n"
"s_waitcnt lgkmcnt(3)\n"
"v_mfma_f32_16x16x32_bf16 v[226:229], v[72:75], v[104:107], v[226:229]\n"
"v_mfma_f32_16x16x32_bf16 v[240:243], v[76:79], v[104:107], v[240:243]\n"
"v_mfma_f32_16x16x32_bf16 v[244:247], v[80:83], v[104:107], v[244:247]\n"
"s_waitcnt lgkmcnt(2)\n"
"v_mfma_f32_16x16x32_bf16 v[232:235], v[72:75], v[186:189], v[232:235]\n"
"v_mfma_f32_16x16x32_bf16 v[248:251], v[76:79], v[186:189], v[248:251]\n"
"v_mfma_f32_16x16x32_bf16 v[252:255], v[80:83], v[186:189], v[252:255]\n"
"s_waitcnt lgkmcnt(1)\n"
"v_mfma_f32_16x16x32_bf16 v[208:211], v[72:75], v[112:115], v[208:211]\n"
"v_mfma_f32_16x16x32_bf16 v[190:193], v[76:79], v[112:115], v[190:193]\n"
"v_mfma_f32_16x16x32_bf16 v[84:87], v[80:83], v[112:115], v[84:87]\n"
"s_waitcnt lgkmcnt(0)\n"
"v_cvt_pk_bf16_f32 v206, v236, v237\n"
"v_cvt_pk_bf16_f32 v196, v196, v197\n"
"v_mfma_f32_16x16x32_bf16 v[178:181], v[72:75], v[120:123], v[178:181]\n"
"v_mfma_f32_16x16x32_bf16 v[88:91], v[76:79], v[120:123], v[88:91]\n"
"v_cvt_pk_bf16_f32 v231, v246, v247\n"
"v_mfma_f32_16x16x32_bf16 v[92:95], v[80:83], v[120:123], v[92:95]\n"
"s_barrier\n"
"v_cvt_pk_bf16_f32 v202, v202, v203\n"
"v_cvt_pk_bf16_f32 v197, v198, v199\n"
"v_cvt_pk_bf16_f32 v232, v232, v233\n"
"s_nop 0\n"
"v_cvt_pk_bf16_f32 v178, v178, v179\n"
"v_cvt_pk_bf16_f32 v201, v218, v219\n"
"s_add_u32 s42, s46, 0\n"
"s_addc_u32 s43, s47, 0\n"
"s_add_u32 m0, s50, 24576\n"
"s_nop 0\n"
"global_load_lds_dwordx4 v0, s[42:43]\n"
"v_cvt_pk_bf16_f32 v208, v208, v209\n"
"v_cvt_pk_bf16_f32 v203, v204, v205\n"
"v_cvt_pk_bf16_f32 v233, v234, v235\n"
"v_cvt_pk_bf16_f32 v230, v244, v245\n"
"v_cvt_pk_bf16_f32 v226, v226, v227\n"
"s_add_u32 m0, s50, 25600\n"
"s_nop 0\n"
"global_load_lds_dwordx4 v1, s[42:43]\n"
"v_cvt_pk_bf16_f32 v198, v212, v213\n"
"v_cvt_pk_bf16_f32 v209, v210, v211\n"
"v_cvt_pk_bf16_f32 v205, v222, v223\n"
"s_add_u32 m0, s50, 26624\n"
"s_nop 0\n"
"global_load_lds_dwordx4 v2, s[42:43]\n"
"s_barrier\n"
"s_add_u32 s42, s46, 36864\n"
"s_addc_u32 s43, s47, 0\n"
"s_add_u32 m0, s50, 27648\n"
"s_nop 0\n"
"global_load_lds_dwordx4 v0, s[42:43]\n"
"s_add_u32 m0, s50, 28672\n"
"s_nop 0\n"
"global_load_lds_dwordx4 v1, s[42:43]\n"
"v_cvt_pk_bf16_f32 v227, v228, v229\n"
"s_add_u32 m0, s50, 29696\n"
"s_nop 0\n"
"global_load_lds_dwordx4 v2, s[42:43]\n"
"v_cvt_pk_bf16_f32 v235, v250, v251\n"
"v_cvt_pk_bf16_f32 v179, v180, v181\n"
"v_cvt_pk_bf16_f32 v183, v94, v95\n"
"s_add_u32 s42, s46, 73728\n"
"s_addc_u32 s43, s47, 0\n"
"s_add_u32 m0, s50, 4294942720\n"
"s_nop 0\n"
"global_load_lds_dwordx4 v0, s[42:43]\n"
"s_add_u32 m0, s50, 4294943744\n"
"s_nop 0\n"
"global_load_lds_dwordx4 v1, s[42:43]\n"
"v_cvt_pk_bf16_f32 v237, v254, v255\n"
"v_cvt_pk_bf16_f32 v180, v88, v89\n"
"v_cvt_pk_bf16_f32 v213, v86, v87\n"
"v_cvt_pk_bf16_f32 v210, v190, v191\n"
"v_cvt_pk_bf16_f32 v200, v216, v217\n"
"s_add_u32 m0, s50, 4294944768\n"
"s_nop 0\n"
"global_load_lds_dwordx4 v2, s[42:43]\n"
"s_add_u32 s42, s46, 110592\n"
"s_addc_u32 s43, s47, 0\n"
"s_add_u32 m0, s50, 4294945792\n"
"s_nop 0\n"
"global_load_lds_dwordx4 v0, s[42:43]\n"
"s_add_u32 m0, s50, 4294946816\n"
"s_nop 0\n"
"global_load_lds_dwordx4 v1, s[42:43]\n"
"v_cvt_pk_bf16_f32 v212, v84, v85\n"
"s_add_u32 m0, s50, 4294947840\n"
"s_nop 0\n"
"global_load_lds_dwordx4 v2, s[42:43]\n"
"v_cvt_pk_bf16_f32 v228, v240, v241\n"
"v_cvt_pk_bf16_f32 v207, v238, v239\n"
"v_cvt_pk_bf16_f32 v199, v214, v215\n"
"v_cvt_pk_bf16_f32 v181, v90, v91\n"
"s_add_u32 s42, s46, 147456\n"
"s_addc_u32 s43, s47, 0\n"
"s_add_u32 m0, s50, 49152\n"
"s_nop 0\n"
"global_load_lds_dwordx4 v0, s[42:43]\n"
"v_cvt_pk_bf16_f32 v234, v248, v249\n"
"s_add_u32 m0, s50, 50176\n"
"s_nop 0\n"
"global_load_lds_dwordx4 v1, s[42:43]\n"
"s_add_u32 m0, s50, 51200\n"
"s_nop 0\n"
"global_load_lds_dwordx4 v2, s[42:43]\n"
"v_cvt_pk_bf16_f32 v236, v252, v253\n"
"s_add_u32 s42, s46, 184320\n"
"s_addc_u32 s43, s47, 0\n"
"s_add_u32 m0, s50, 52224\n"
"s_nop 0\n"
"global_load_lds_dwordx4 v0, s[42:43]\n"
"s_add_u32 m0, s50, 53248\n"
"s_nop 0\n"
"global_load_lds_dwordx4 v1, s[42:43]\n"
"v_cvt_pk_bf16_f32 v204, v220, v221\n"
"v_cvt_pk_bf16_f32 v211, v192, v193\n"
"s_add_u32 m0, s50, 54272\n"
"s_nop 0\n"
"global_load_lds_dwordx4 v2, s[42:43]\n"
"s_barrier\n"
"s_waitcnt vmcnt(15)\n"
"ds_read_b128 v[184:187], v4 offset:6144\n"
"ds_read_b64_tr_b16 v[86:87], v3 offset:54016\n"
"s_waitcnt vmcnt(12)\n"
"ds_read_b128 v[188:191], v5 offset:6144\n"
"s_waitcnt vmcnt(9)\n"
"v_cvt_pk_bf16_f32 v229, v242, v243\n"
"s_waitcnt lgkmcnt(2)\n"
"v_mfma_f32_16x16x32_bf16 v[238:241], v[8:11], v[184:187], 0\n"
"ds_read_b64_tr_b16 v[96:97], v3 offset:56320\n"
"v_cvt_pk_bf16_f32 v182, v92, v93\n"
"v_mfma_f32_16x16x32_bf16 v[214:217], v[12:15], v[184:187], 0\n"
"ds_read_b64_tr_b16 v[104:105], v3 offset:4096\n"
"v_mfma_f32_16x16x32_bf16 v[218:221], v[16:19], v[184:187], 0\n"
"s_waitcnt lgkmcnt(2)\n"
"v_mfma_f32_16x16x32_bf16 v[238:241], v[20:23], v[188:191], v[238:241]\n"
"v_mfma_f32_16x16x32_bf16 v[214:217], v[28:31], v[188:191], v[214:217]\n"
"s_waitcnt vmcnt(6)\n"
"ds_read_b128 v[192:195], v4 offset:8192\n"
"s_sub_u32 m0, s50, 28672\n"
"ds_read_b64_tr_b16 v[88:89], v3 offset:53280\n"
"ds_read_b64_tr_b16 v[222:223], v3 offset:56384\n"
"ds_read_b64_tr_b16 v[94:95], v3 offset:54080\n"
"ds_read_b64_tr_b16 v[102:103], v3 offset:57120\n"
"v_mfma_f32_16x16x32_bf16 v[218:221], v[32:35], v[188:191], v[218:221]\n"
"s_waitcnt lgkmcnt(4)\n"
"ds_read_b64_tr_b16 v[224:225], v3 offset:57152\n"
"ds_read_b64_tr_b16 v[106:107], v3 offset:4864\n"
"v_mfma_f32_16x16x32_bf16 v[238:241], v[36:39], v[192:195], v[238:241]\n"
"ds_read_b64_tr_b16 v[114:115], v3 offset:7936\n"
"ds_read_b64_tr_b16 v[92:93], v3 offset:53312\n"
"ds_read_b128 v[184:187], v5 offset:8192\n"
"s_waitcnt vmcnt(3)\n"
"v_mfma_f32_16x16x32_bf16 v[214:217], v[40:43], v[192:195], v[214:217]\n"
"ds_read_b64_tr_b16 v[98:99], v3 offset:57088\n"
"ds_read_b64_tr_b16 v[100:101], v3 offset:56352\n"
"ds_read_b64_tr_b16 v[84:85], v3 offset:53248\n"
"ds_read_b64_tr_b16 v[244:245], v3 offset:4160\n"
"v_mfma_f32_16x16x32_bf16 v[218:221], v[44:47], v[192:195], v[218:221]\n"
"s_waitcnt lgkmcnt(4)\n"
"ds_read_b64_tr_b16 v[90:91], v3 offset:54048\n"
"ds_read_b128 v[188:191], v4 offset:10240\n"
"v_mfma_f32_16x16x32_bf16 v[238:241], v[48:51], v[184:187], v[238:241]\n"
"ds_read_b64_tr_b16 v[108:109], v3 offset:4128\n"
"ds_read_b64_tr_b16 v[110:111], v3 offset:4896\n"
"v_mfma_f32_16x16x32_bf16 v[214:217], v[52:55], v[184:187], v[214:217]\n"
"ds_read_b64_tr_b16 v[116:117], v3 offset:7200\n"
"ds_read_b128 v[192:195], v5 offset:10240\n"
"v_mfma_f32_16x16x32_bf16 v[218:221], v[56:59], v[184:187], v[218:221]\n"
"s_waitcnt lgkmcnt(4)\n"
"ds_read_b64_tr_b16 v[112:113], v3 offset:7168\n"
"ds_read_b64_tr_b16 v[246:247], v3 offset:4928\n"
"v_mfma_f32_16x16x32_bf16 v[238:241], v[60:63], v[188:191], v[238:241]\n"
"s_waitcnt vmcnt(0)\n"
"ds_read_b64_tr_b16 v[248:249], v3 offset:7232\n"
"ds_read_b64_tr_b16 v[250:251], v3 offset:8000\n"
"v_mfma_f32_16x16x32_bf16 v[214:217], v[64:67], v[188:191], v[214:217]\n"
"ds_read_b64_tr_b16 v[118:119], v3 offset:7968\n"
"ds_read_b128 v[184:187], v4 offset:0\n"
"v_mfma_f32_16x16x32_bf16 v[218:221], v[68:71], v[188:191], v[218:221]\n"
"s_waitcnt lgkmcnt(6)\n"
"ds_read_b128 v[188:191], v5 offset:0\n"
"v_mfma_f32_16x16x32_bf16 v[238:241], v[72:75], v[192:195], v[238:241]\n"
"ds_read_b64_tr_b16 v[0:1], v27 offset:0\n"
"ds_read_b64_tr_b16 v[122:123], v27 offset:800\n"
"ds_read_b64_tr_b16 v[130:131], v27 offset:3872\n"
"ds_read_b64_tr_b16 v[2:3], v27 offset:768\n"
"ds_read_b64_tr_b16 v[120:121], v27 offset:32\n"
"v_mfma_f32_16x16x32_bf16 v[214:217], v[76:79], v[192:195], v[214:217]\n"
"ds_read_b64_tr_b16 v[254:255], v27 offset:832\n"
"ds_read_b64_tr_b16 v[128:129], v27 offset:3104\n"
"v_mfma_f32_16x16x32_bf16 v[218:221], v[80:83], v[192:195], v[218:221]\n"
"s_waitcnt lgkmcnt(8)\n"
"ds_read_b64_tr_b16 v[252:253], v27 offset:64\n"
"v_cvt_pk_bf16_f32 v238, v238, v239\n"
"v_mfma_f32_16x16x32_bf16 v[132:135], v[84:87], v[184:187], 0\n"
"ds_read_b64_tr_b16 v[126:127], v27 offset:3840\n"
"v_cvt_pk_bf16_f32 v239, v240, v241\n"
"v_cvt_pk_bf16_f32 v240, v214, v215\n"
"v_mfma_f32_16x16x32_bf16 v[192:195], v[88:91], v[184:187], 0\n"
"ds_read_b64_tr_b16 v[214:215], v27 offset:3136\n"
"v_cvt_pk_bf16_f32 v242, v218, v219\n"
"v_cvt_pk_bf16_f32 v243, v220, v221\n"
"v_mfma_f32_16x16x32_bf16 v[218:221], v[92:95], v[184:187], 0\n"
"s_waitcnt lgkmcnt(10)\n"
"ds_read_b128 v[184:187], v4 offset:2048\n"
"v_cvt_pk_bf16_f32 v241, v216, v217\n"
"v_mfma_f32_16x16x32_bf16 v[132:135], v[96:99], v[188:191], v[132:135]\n"
"ds_read_b64_tr_b16 v[216:217], v27 offset:3904\n"
"v_mfma_f32_16x16x32_bf16 v[192:195], v[100:103], v[188:191], v[192:195]\n"
"ds_read_b64_tr_b16 v[124:125], v27 offset:3072\n"
"ds_read_b128 v[136:139], v5 offset:2048\n"
"v_mfma_f32_16x16x32_bf16 v[218:221], v[222:225], v[188:191], v[218:221]\n"
"s_waitcnt lgkmcnt(3)\n"
"v_mfma_f32_16x16x32_bf16 v[132:135], v[104:107], v[184:187], v[132:135]\n"
"v_mfma_f32_16x16x32_bf16 v[192:195], v[108:111], v[184:187], v[192:195]\n"
"v_mfma_f32_16x16x32_bf16 v[218:221], v[244:247], v[184:187], v[218:221]\n"
"s_waitcnt lgkmcnt(0)\n"
"v_mfma_f32_16x16x32_bf16 v[132:135], v[112:115], v[136:139], v[132:135]\n"
"ds_read_b128 v[188:191], v4 offset:4096\n"
"v_mfma_f32_16x16x32_bf16 v[192:195], v[116:119], v[136:139], v[192:195]\n"
"s_barrier\n"
"v_bfe_u32 v177, v6, 2, 6\n"
"ds_read_b128 v[184:187], v5 offset:4096\n"
"v_mfma_f32_16x16x32_bf16 v[218:221], v[248:251], v[136:139], v[218:221]\n"
"s_waitcnt lgkmcnt(1)\n"
"ds_read_b32 v177, v177 offset:1216\n"
"v_mfma_f32_16x16x32_bf16 v[132:135], v[0:3], v[188:191], v[132:135]\n"
"ds_read_b128 v[136:139], v4 offset:12288\n"
"v_mfma_f32_16x16x32_bf16 v[192:195], v[120:123], v[188:191], v[192:195]\n"
"v_mfma_f32_16x16x32_bf16 v[218:221], v[252:255], v[188:191], v[218:221]\n"
"s_waitcnt lgkmcnt(2)\n"
"v_mfma_f32_16x16x32_bf16 v[132:135], v[124:127], v[184:187], v[132:135]\n"
"v_mfma_f32_16x16x32_bf16 v[192:195], v[128:131], v[184:187], v[192:195]\n"
"s_waitcnt lgkmcnt(1)\n"
"v_add_u32_sdwa v176, v177, v7 dst_sel:DWORD dst_unused:UNUSED_PAD src0_sel:WORD_0 src1_sel:DWORD\n"
"ds_read_b128 v[188:191], v5 offset:12288\n"
"v_mfma_f32_16x16x32_bf16 v[218:221], v[214:217], v[184:187], v[218:221]\n"
"s_waitcnt lgkmcnt(1)\n"
"ds_read_b128 v[140:143], v4 offset:14336\n"
"v_mfma_f32_16x16x32_bf16 v[184:187], v[8:11], v[136:139], 0\n"
"s_add_u32 s68, s50, 0\n"
"v_mul_f32 v133, s60, v133\n"
"v_mfma_f32_16x16x32_bf16 v[144:147], v[12:15], v[136:139], 0\n"
"v_mul_f32 v134, s60, v134\n"
"v_max_u32 v177, s68, v176\n"
"v_mfma_f32_16x16x32_bf16 v[152:155], v[16:19], v[136:139], 0\n"
"s_waitcnt lgkmcnt(1)\n"
"ds_read_b128 v[136:139], v5 offset:14336\n"
"v_mul_f32 v135, s60, v135\n"
"v_mfma_f32_16x16x32_bf16 v[184:187], v[20:23], v[188:191], v[184:187]\n"
"v_mul_f32 v132, s60, v132\n"
"v_mfma_f32_16x16x32_bf16 v[144:147], v[28:31], v[188:191], v[144:147]\n"
"ds_read_b128 v[148:151], v177 offset:16\n"
"v_mul_f32 v194, s60, v194\n"
"v_mul_f32 v192, s60, v192\n"
"v_mul_f32 v193, s60, v193\n"
"v_mfma_f32_16x16x32_bf16 v[152:155], v[32:35], v[188:191], v[152:155]\n"
"s_waitcnt lgkmcnt(2)\n"
"ds_read_b128 v[188:191], v4 offset:16384\n"
"v_mfma_f32_16x16x32_bf16 v[184:187], v[36:39], v[140:143], v[184:187]\n"
"v_cvt_pk_bf16_f32 v132, v132, v133\n"
"v_cvt_pk_bf16_f32 v133, v134, v135\n"
"v_mfma_f32_16x16x32_bf16 v[144:147], v[40:43], v[140:143], v[144:147]\n"
"v_mul_f32 v195, s60, v195\n"
"v_mul_f32 v219, s60, v219\n"
"v_cvt_pk_bf16_f32 v134, v192, v193\n"
"v_mul_f32 v218, s60, v218\n"
"v_cvt_pk_bf16_f32 v26, v218, v219\n"
"v_mul_f32 v221, s60, v221\n"
"v_mul_f32 v220, s60, v220\n"
"v_cvt_pk_bf16_f32 v135, v194, v195\n"
"s_waitcnt lgkmcnt(1)\n"
"s_nop 0\n"
"v_mfma_f32_16x16x32_bf16 v[148:151], v[202:205], v[132:135], v[148:151]\n"
"v_cvt_pk_bf16_f32 v27, v220, v221\n"
"ds_read_b128 v[218:221], v177 offset:32\n"
"s_nop 0\n"
"v_mfma_f32_16x16x32_bf16 v[148:151], v[204:207], v[24:27], v[148:151]\n"
"ds_write_b128 v6, v[222:225] offset:5120\n"
"v_mfma_f32_16x16x32_bf16 v[152:155], v[44:47], v[140:143], v[152:155]\n"
"v_mfma_f32_16x16x32_bf16 v[184:187], v[48:51], v[136:139], v[184:187]\n"
"s_add_u32 s69, s50, 1040\n"
"v_min_u32 v175, s69, v176\n"
"v_mfma_f32_16x16x32_bf16 v[144:147], v[52:55], v[136:139], v[144:147]\n"
"v_mfma_f32_16x16x32_bf16 v[152:155], v[56:59], v[136:139], v[152:155]\n"
"s_waitcnt lgkmcnt(2)\n"
"ds_read_b128 v[192:195], v5 offset:16384\n"
"v_mfma_f32_16x16x32_bf16 v[184:187], v[60:63], v[188:191], v[184:187]\n"
"ds_read_b128 v[140:143], v175 offset:1344\n"
"ds_read_b128 v[136:139], v4 offset:18432\n"
"v_mfma_f32_16x16x32_bf16 v[144:147], v[64:67], v[188:191], v[144:147]\n"
"s_waitcnt lgkmcnt(4)\n"
"v_mfma_f32_16x16x32_bf16 v[218:221], v[208:211], v[132:135], v[218:221]\n"
"ds_read_b128 v[156:159], v5 offset:18432\n"
"v_mfma_f32_16x16x32_bf16 v[218:221], v[210:213], v[24:27], v[218:221]\n"
"v_exp_f32 v148, v148\n"
"v_exp_f32 v149, v149\n"
"s_nop 0\n"
"v_cvt_pk_bf16_f32 v148, v148, v149\n"
"v_exp_f32 v150, v150\n"
"v_mfma_f32_16x16x32_bf16 v[152:155], v[68:71], v[188:191], v[152:155]\n"
"s_waitcnt lgkmcnt(3)\n"
"v_exp_f32 v151, v151\n"
"v_mfma_f32_16x16x32_bf16 v[184:187], v[72:75], v[192:195], v[184:187]\n"
"v_cvt_pk_bf16_f32 v149, v150, v151\n"
"ds_write_addtid_b32 v148 offset:53248\n"
"v_mfma_f32_16x16x32_bf16 v[144:147], v[76:79], v[192:195], v[144:147]\n"
"ds_write_addtid_b32 v149 offset:53504\n"
"v_mfma_f32_16x16x32_bf16 v[152:155], v[80:83], v[192:195], v[152:155]\n"
"ds_write_b128 v6, v[244:247] offset:6144\n"
"s_waitcnt lgkmcnt(4)\n"
"v_mfma_f32_16x16x32_bf16 v[190:193], v[8:11], v[136:139], 0\n"
"v_cvt_pk_bf16_f32 v184, v184, v185\n"
"s_nop 3\n"
"v_cvt_pk_bf16_f32 v188, v152, v153\n"
"v_mfma_f32_16x16x32_bf16 v[140:143], v[226:229], v[132:135], v[140:143]\n"
"v_cvt_pk_bf16_f32 v185, v186, v187\n"
"v_cvt_pk_bf16_f32 v187, v146, v147\n"
"v_mfma_f32_16x16x32_bf16 v[140:143], v[228:231], v[24:27], v[140:143]\n"
"v_exp_f32 v218, v218\n"
"v_cvt_pk_bf16_f32 v186, v144, v145\n"
"v_cvt_pk_bf16_f32 v189, v154, v155\n"
"ds_read_b128 v[144:147], v175 offset:1360\n"
"ds_read_b128 v[152:155], v4 offset:20480\n"
"v_exp_f32 v219, v219\n"
"v_mfma_f32_16x16x32_bf16 v[148:151], v[12:15], v[136:139], 0\n"
"v_mfma_f32_16x16x32_bf16 v[160:163], v[16:19], v[136:139], 0\n"
"s_waitcnt lgkmcnt(5)\n"
"v_cvt_pk_bf16_f32 v218, v218, v219\n"
"v_mfma_f32_16x16x32_bf16 v[190:193], v[20:23], v[156:159], v[190:193]\n"
"v_exp_f32 v140, v140\n"
"v_mfma_f32_16x16x32_bf16 v[148:151], v[28:31], v[156:159], v[148:151]\n"
"ds_write_addtid_b32 v218 offset:53760\n"
"ds_read_b128 v[218:221], v5 offset:20480\n"
"v_mfma_f32_16x16x32_bf16 v[160:163], v[32:35], v[156:159], v[160:163]\n"
"s_waitcnt lgkmcnt(3)\n"
"v_exp_f32 v141, v141\n"
"v_mfma_f32_16x16x32_bf16 v[144:147], v[232:235], v[132:135], v[144:147]\n"
"ds_read_b128 v[156:159], v4 offset:22528\n"
"v_exp_f32 v142, v142\n"
"v_mfma_f32_16x16x32_bf16 v[144:147], v[234:237], v[24:27], v[144:147]\n"
"s_waitcnt lgkmcnt(3)\n"
"ds_read_b128 v[136:139], v177 offset:0\n"
"v_mfma_f32_16x16x32_bf16 v[190:193], v[36:39], v[152:155], v[190:193]\n"
"v_exp_f32 v143, v143\n"
"v_mfma_f32_16x16x32_bf16 v[148:151], v[40:43], v[152:155], v[148:151]\n"
"s_add_u32 s70, s50, 4294966992\n"
"ds_write_b128 v6, v[248:251] offset:7168\n"
"v_cvt_pk_bf16_f32 v140, v140, v141\n"
"v_cvt_pk_bf16_f32 v141, v142, v143\n"
"v_mfma_f32_16x16x32_bf16 v[160:163], v[44:47], v[152:155], v[160:163]\n"
"s_waitcnt lgkmcnt(3)\n"
"v_min_u32 v177, s70, v176\n"
"v_exp_f32 v144, v144\n"
"v_exp_f32 v145, v145\n"
"v_mfma_f32_16x16x32_bf16 v[190:193], v[48:51], v[218:221], v[190:193]\n"
"ds_write_addtid_b32 v140 offset:54272\n"
"ds_write_addtid_b32 v141 offset:54528\n"
"v_mfma_f32_16x16x32_bf16 v[148:151], v[52:55], v[218:221], v[148:151]\n"
"s_waitcnt lgkmcnt(3)\n"
"ds_read_b128 v[140:143], v5 offset:22528\n"
"v_exp_f32 v146, v146\n"
"v_mfma_f32_16x16x32_bf16 v[136:139], v[196:199], v[132:135], v[136:139]\n"
"v_exp_f32 v147, v147\n"
"v_mfma_f32_16x16x32_bf16 v[136:139], v[198:201], v[24:27], v[136:139]\n"
"ds_read_b128 v[152:155], v177 offset:2688\n"
"v_cvt_pk_bf16_f32 v144, v144, v145\n"
"v_cvt_pk_bf16_f32 v145, v146, v147\n"
"v_mfma_f32_16x16x32_bf16 v[160:163], v[56:59], v[218:221], v[160:163]\n"
"ds_write_addtid_b32 v144 offset:54784\n"
"ds_write_addtid_b32 v145 offset:55040\n"
"v_mfma_f32_16x16x32_bf16 v[190:193], v[60:63], v[156:159], v[190:193]\n"
"s_nop 0\n"
"v_exp_f32 v136, v136\n"
"v_mfma_f32_16x16x32_bf16 v[148:151], v[64:67], v[156:159], v[148:151]\n"
"v_exp_f32 v137, v137\n"
"v_mfma_f32_16x16x32_bf16 v[160:163], v[68:71], v[156:159], v[160:163]\n"
"s_waitcnt lgkmcnt(3)\n"
"ds_read_b128 v[218:221], v175 offset:1376\n"
"v_mfma_f32_16x16x32_bf16 v[190:193], v[72:75], v[140:143], v[190:193]\n"
"s_waitcnt lgkmcnt(3)\n"
"v_cvt_pk_bf16_f32 v136, v136, v137\n"
"v_mfma_f32_16x16x32_bf16 v[152:155], v[178:181], v[132:135], v[152:155]\n"
"v_exp_f32 v138, v138\n"
"v_mfma_f32_16x16x32_bf16 v[152:155], v[180:183], v[24:27], v[152:155]\n"
"s_waitcnt lgkmcnt(1)\n"
"s_barrier\n"
"ds_read_b128 v[196:199], v4 offset:36864\n"
"v_mfma_f32_16x16x32_bf16 v[148:151], v[76:79], v[140:143], v[148:151]\n"
"ds_write_addtid_b32 v136 offset:58880\n"
"ds_read_b128 v[164:167], v4 offset:38912\n"
"v_mfma_f32_16x16x32_bf16 v[160:163], v[80:83], v[140:143], v[160:163]\n"
"ds_read_b128 v[144:147], v5 offset:36864\n"
"v_cvt_pk_bf16_f32 v190, v190, v191\n"
"v_cvt_pk_bf16_f32 v191, v192, v193\n"
"s_nop 1\n"
"v_cvt_pk_bf16_f32 v192, v148, v149\n"
"v_exp_f32 v139, v139\n"
"s_nop 0\n"
"v_cvt_pk_bf16_f32 v137, v138, v139\n"
"v_cvt_pk_bf16_f32 v194, v160, v161\n"
"s_waitcnt lgkmcnt(3)\n"
"v_mfma_f32_16x16x32_bf16 v[140:143], v[84:87], v[196:199], 0\n"
"ds_write_b128 v6, v[252:255] offset:8192\n"
"v_mfma_f32_16x16x32_bf16 v[156:159], v[88:91], v[196:199], 0\n"
"ds_write_addtid_b32 v137 offset:59136\n"
"v_mfma_f32_16x16x32_bf16 v[136:139], v[92:95], v[196:199], 0\n"
"v_exp_f32 v152, v152\n"
"v_mfma_f32_16x16x32_bf16 v[218:221], v[238:241], v[132:135], v[218:221]\n"
"ds_read_b128 v[196:199], v177 offset:2704\n"
"v_cvt_pk_bf16_f32 v195, v162, v163\n"
"v_mfma_f32_16x16x32_bf16 v[218:221], v[240:243], v[24:27], v[218:221]\n"
"s_waitcnt lgkmcnt(3)\n"
"v_exp_f32 v153, v153\n"
"v_exp_f32 v154, v154\n"
"v_exp_f32 v155, v155\n"
"v_mfma_f32_16x16x32_bf16 v[140:143], v[96:99], v[144:147], v[140:143]\n"
"v_cvt_pk_bf16_f32 v152, v152, v153\n"
"v_cvt_pk_bf16_f32 v193, v150, v151\n"
"v_mfma_f32_16x16x32_bf16 v[156:159], v[100:103], v[144:147], v[156:159]\n"
"ds_read_b128 v[148:151], v5 offset:38912\n"
"v_mfma_f32_16x16x32_bf16 v[136:139], v[222:225], v[144:147], v[136:139]\n"
"ds_read_b128 v[144:147], v177 offset:2720\n"
"v_mfma_f32_16x16x32_bf16 v[140:143], v[104:107], v[164:167], v[140:143]\n"
"v_exp_f32 v218, v218\n"
"v_mfma_f32_16x16x32_bf16 v[156:159], v[108:111], v[164:167], v[156:159]\n"
"ds_write_addtid_b32 v152 offset:57344\n"
"s_waitcnt lgkmcnt(3)\n"
"v_mfma_f32_16x16x32_bf16 v[196:199], v[184:187], v[132:135], v[196:199]\n"
"ds_read_b128 v[220:223], v4 offset:40960\n"
"v_cvt_pk_bf16_f32 v153, v154, v155\n"
"v_mfma_f32_16x16x32_bf16 v[196:199], v[186:189], v[24:27], v[196:199]\n"
"v_exp_f32 v219, v219\n"
"v_mfma_f32_16x16x32_bf16 v[136:139], v[244:247], v[164:167], v[136:139]\n"
"ds_read_b128 v[244:247], v5 offset:40960\n"
"ds_write_addtid_b32 v153 offset:57600\n"
"s_waitcnt lgkmcnt(5)\n"
"v_mfma_f32_16x16x32_bf16 v[140:143], v[112:115], v[148:151], v[140:143]\n"
"ds_write_b128 v6, v[214:217] offset:9216\n"
"v_mfma_f32_16x16x32_bf16 v[156:159], v[116:119], v[148:151], v[156:159]\n"
"v_cvt_pk_bf16_f32 v218, v218, v219\n"
"v_bfe_u32 v225, v6, 2, 6\n"
"v_mfma_f32_16x16x32_bf16 v[136:139], v[248:251], v[148:151], v[136:139]\n"
"s_waitcnt lgkmcnt(3)\n"
"ds_read_b128 v[248:251], v4 offset:24576\n"
"v_mfma_f32_16x16x32_bf16 v[140:143], v[0:3], v[220:223], v[140:143]\n"
"ds_read_b32 v225, v225 offset:1280\n"
"v_mfma_f32_16x16x32_bf16 v[144:147], v[190:193], v[132:135], v[144:147]\n"
"s_add_u32 s71, s50, 4294967280\n"
"v_mfma_f32_16x16x32_bf16 v[144:147], v[192:195], v[24:27], v[144:147]\n"
"v_exp_f32 v196, v196\n"
"v_mfma_f32_16x16x32_bf16 v[156:159], v[120:123], v[220:223], v[156:159]\n"
"ds_write_addtid_b32 v218 offset:33792\n"
"v_exp_f32 v197, v197\n"
"v_exp_f32 v198, v198\n"
"v_mfma_f32_16x16x32_bf16 v[136:139], v[252:255], v[220:223], v[136:139]\n"
"s_waitcnt lgkmcnt(5)\n"
"ds_read_b128 v[218:221], v5 offset:24576\n"
"v_cvt_pk_bf16_f32 v196, v196, v197\n"
"v_mfma_f32_16x16x32_bf16 v[140:143], v[124:127], v[244:247], v[140:143]\n"
"ds_write_addtid_b32 v196 offset:57856\n"
"v_exp_f32 v199, v199\n"
"v_mfma_f32_16x16x32_bf16 v[156:159], v[128:131], v[244:247], v[156:159]\n"
"s_waitcnt lgkmcnt(3)\n"
"v_add_u32_sdwa v224, v225, v7 dst_sel:DWORD dst_unused:UNUSED_PAD src0_sel:WORD_0 src1_sel:DWORD\n"
"v_cvt_pk_bf16_f32 v197, v198, v199\n"
"v_mfma_f32_16x16x32_bf16 v[136:139], v[214:217], v[244:247], v[136:139]\n"
"ds_write_addtid_b32 v197 offset:58112\n"
"v_mul_f32 v140, s60, v140\n"
"v_exp_f32 v144, v144\n"
"v_mfma_f32_16x16x32_bf16 v[214:217], v[8:11], v[248:251], 0\n"
"ds_read_b128 v[196:199], v4 offset:26624\n"
"v_exp_f32 v145, v145\n"
"v_mul_f32 v142, s60, v142\n"
"v_mfma_f32_16x16x32_bf16 v[252:255], v[12:15], v[248:251], 0\n"
"v_max_u32 v225, s71, v224\n"
"v_mul_f32 v158, s60, v158\n"
"v_mfma_f32_16x16x32_bf16 v[132:135], v[16:19], v[248:251], 0\n"
"s_waitcnt lgkmcnt(3)\n"
"ds_read_b128 v[248:251], v225 offset:16\n"
"v_cvt_pk_bf16_f32 v144, v144, v145\n"
"v_mfma_f32_16x16x32_bf16 v[214:217], v[20:23], v[218:221], v[214:217]\n"
"ds_write_addtid_b32 v144 offset:58368\n"
"v_mul_f32 v141, s60, v141\n"
"v_cvt_pk_bf16_f32 v140, v140, v141\n"
"v_mul_f32 v157, s60, v157\n"
"v_mfma_f32_16x16x32_bf16 v[252:255], v[28:31], v[218:221], v[252:255]\n"
"v_mul_f32 v139, s60, v139\n"
"v_mul_f32 v137, s60, v137\n"
"v_mul_f32 v143, s60, v143\n"
"v_cvt_pk_bf16_f32 v141, v142, v143\n"
"v_mul_f32 v138, s60, v138\n"
"v_mfma_f32_16x16x32_bf16 v[132:135], v[32:35], v[218:221], v[132:135]\n"
"s_waitcnt lgkmcnt(2)\n"
"ds_read_b128 v[244:247], v4 offset:28672\n"
"v_mul_f32 v156, s60, v156\n"
"v_mul_f32 v159, s60, v159\n"
"v_mfma_f32_16x16x32_bf16 v[214:217], v[36:39], v[196:199], v[214:217]\n"
"ds_read_b128 v[218:221], v5 offset:26624\n"
"v_cvt_pk_bf16_f32 v143, v158, v159\n"
"v_cvt_pk_bf16_f32 v27, v138, v139\n"
"v_cvt_pk_bf16_f32 v142, v156, v157\n"
"v_mfma_f32_16x16x32_bf16 v[252:255], v[40:43], v[196:199], v[252:255]\n"
"s_waitcnt lgkmcnt(3)\n"
"v_mul_f32 v136, s60, v136\n"
"v_cvt_pk_bf16_f32 v26, v136, v137\n"
"v_mfma_f32_16x16x32_bf16 v[248:251], v[202:205], v[140:143], v[248:251]\n"
"s_waitcnt lgkmcnt(2)\n"
"s_barrier\n"
"v_mfma_f32_16x16x32_bf16 v[248:251], v[204:207], v[24:27], v[248:251]\n"
"ds_read_b128 v[136:139], v225 offset:32\n"
"v_mfma_f32_16x16x32_bf16 v[132:135], v[44:47], v[196:199], v[132:135]\n"
"s_waitcnt lgkmcnt(1)\n"
"ds_read_b128 v[196:199], v5 offset:28672\n"
"v_mfma_f32_16x16x32_bf16 v[214:217], v[48:51], v[218:221], v[214:217]\n"
"v_mfma_f32_16x16x32_bf16 v[252:255], v[52:55], v[218:221], v[252:255]\n"
"v_mfma_f32_16x16x32_bf16 v[132:135], v[56:59], v[218:221], v[132:135]\n"
"ds_read_b128 v[220:223], v4 offset:30720\n"
"v_mfma_f32_16x16x32_bf16 v[214:217], v[60:63], v[244:247], v[214:217]\n"
"v_mfma_f32_16x16x32_bf16 v[252:255], v[64:67], v[244:247], v[252:255]\n"
"s_add_u32 s72, s50, 1024\n"
"v_exp_f32 v248, v248\n"
"v_mfma_f32_16x16x32_bf16 v[132:135], v[68:71], v[244:247], v[132:135]\n"
"s_waitcnt lgkmcnt(1)\n"
"ds_read_b128 v[144:147], v5 offset:30720\n"
"v_mfma_f32_16x16x32_bf16 v[214:217], v[72:75], v[196:199], v[214:217]\n"
"v_mfma_f32_16x16x32_bf16 v[136:139], v[208:211], v[140:143], v[136:139]\n"
"v_exp_f32 v249, v249\n"
"s_nop 0\n"
"v_cvt_pk_bf16_f32 v248, v248, v249\n"
"v_mfma_f32_16x16x32_bf16 v[136:139], v[210:213], v[24:27], v[136:139]\n"
"ds_write_addtid_b32 v248 offset:55296\n"
"v_min_u32 v201, s72, v224\n"
"v_mfma_f32_16x16x32_bf16 v[252:255], v[76:79], v[196:199], v[252:255]\n"
"v_exp_f32 v250, v250\n"
"v_cvt_pk_bf16_f32 v214, v214, v215\n"
"v_cvt_pk_bf16_f32 v215, v216, v217\n"
"v_mfma_f32_16x16x32_bf16 v[132:135], v[80:83], v[196:199], v[132:135]\n"
"s_waitcnt lgkmcnt(2)\n"
"v_mfma_f32_16x16x32_bf16 v[244:247], v[8:11], v[220:223], 0\n"
"ds_read_b128 v[196:199], v201 offset:1376\n"
"v_mfma_f32_16x16x32_bf16 v[148:151], v[12:15], v[220:223], 0\n"
"ds_read_b128 v[152:155], v4 offset:32768\n"
"v_cvt_pk_bf16_f32 v217, v254, v255\n"
"v_exp_f32 v251, v251\n"
"s_nop 0\n"
"v_cvt_pk_bf16_f32 v249, v250, v251\n"
"ds_write_addtid_b32 v249 offset:55552\n"
"v_cvt_pk_bf16_f32 v218, v132, v133\n"
"v_cvt_pk_bf16_f32 v216, v252, v253\n"
"v_mfma_f32_16x16x32_bf16 v[250:253], v[16:19], v[220:223], 0\n"
"s_waitcnt lgkmcnt(4)\n"
"v_cvt_pk_bf16_f32 v219, v134, v135\n"
"v_mfma_f32_16x16x32_bf16 v[244:247], v[20:23], v[144:147], v[244:247]\n"
"v_exp_f32 v136, v136\n"
"v_exp_f32 v137, v137\n"
"v_mfma_f32_16x16x32_bf16 v[148:151], v[28:31], v[144:147], v[148:151]\n"
"v_cvt_pk_bf16_f32 v136, v136, v137\n"
"ds_write_addtid_b32 v136 offset:55808\n"
"v_mfma_f32_16x16x32_bf16 v[250:253], v[32:35], v[144:147], v[250:253]\n"
"s_waitcnt lgkmcnt(3)\n"
"ds_read_b128 v[220:223], v5 offset:32768\n"
"v_mfma_f32_16x16x32_bf16 v[196:199], v[238:241], v[140:143], v[196:199]\n"
"v_exp_f32 v138, v138\n"
"v_mfma_f32_16x16x32_bf16 v[196:199], v[240:243], v[24:27], v[196:199]\n"
"s_waitcnt lgkmcnt(3)\n"
"ds_read_b128 v[132:135], v4 offset:34816\n"
"v_mfma_f32_16x16x32_bf16 v[244:247], v[36:39], v[152:155], v[244:247]\n"
"v_exp_f32 v139, v139\n"
"ds_read_b128 v[144:147], v201 offset:1360\n"
"v_mfma_f32_16x16x32_bf16 v[148:151], v[40:43], v[152:155], v[148:151]\n"
"v_mfma_f32_16x16x32_bf16 v[250:253], v[44:47], v[152:155], v[250:253]\n"
"s_nop 0\n"
"v_exp_f32 v196, v196\n"
"v_cvt_pk_bf16_f32 v137, v138, v139\n"
"ds_write_addtid_b32 v137 offset:56064\n"
"s_waitcnt lgkmcnt(3)\n"
"v_mfma_f32_16x16x32_bf16 v[244:247], v[48:51], v[220:223], v[244:247]\n"
"v_mfma_f32_16x16x32_bf16 v[148:151], v[52:55], v[220:223], v[148:151]\n"
"v_exp_f32 v197, v197\n"
"v_mfma_f32_16x16x32_bf16 v[250:253], v[56:59], v[220:223], v[250:253]\n"
"s_waitcnt lgkmcnt(2)\n"
"v_mfma_f32_16x16x32_bf16 v[244:247], v[60:63], v[132:135], v[244:247]\n"
"s_waitcnt lgkmcnt(1)\n"
"v_mfma_f32_16x16x32_bf16 v[144:147], v[232:235], v[140:143], v[144:147]\n"
"ds_read_b128 v[136:139], v5 offset:34816\n"
"ds_read_b128 v[220:223], v4 offset:43008\n"
"v_cvt_pk_bf16_f32 v196, v196, v197\n"
"v_mfma_f32_16x16x32_bf16 v[144:147], v[234:237], v[24:27], v[144:147]\n"
"v_mfma_f32_16x16x32_bf16 v[148:151], v[64:67], v[132:135], v[148:151]\n"
"ds_write_addtid_b32 v196 offset:56320\n"
"v_mfma_f32_16x16x32_bf16 v[250:253], v[68:71], v[132:135], v[250:253]\n"
"s_waitcnt lgkmcnt(2)\n"
"v_mfma_f32_16x16x32_bf16 v[244:247], v[72:75], v[136:139], v[244:247]\n"
"v_exp_f32 v198, v198\n"
"v_mfma_f32_16x16x32_bf16 v[148:151], v[76:79], v[136:139], v[148:151]\n"
"ds_read_b128 v[132:135], v225 offset:48\n"
"v_mfma_f32_16x16x32_bf16 v[250:253], v[80:83], v[136:139], v[250:253]\n"
"s_waitcnt lgkmcnt(2)\n"
"v_mfma_f32_16x16x32_bf16 v[136:139], v[84:87], v[220:223], 0\n"
"v_exp_f32 v199, v199\n"
"s_nop 0\n"
"v_cvt_pk_bf16_f32 v244, v244, v245\n"
"v_cvt_pk_bf16_f32 v197, v198, v199\n"
"ds_read_b128 v[152:155], v5 offset:43008\n"
"v_cvt_pk_bf16_f32 v245, v246, v247\n"
"ds_write_addtid_b32 v197 offset:56576\n"
"v_cvt_pk_bf16_f32 v247, v150, v151\n"
"v_cvt_pk_bf16_f32 v246, v148, v149\n"
"v_mfma_f32_16x16x32_bf16 v[148:151], v[88:91], v[220:223], 0\n"
"ds_read_b128 v[156:159], v4 offset:45056\n"
"v_cvt_pk_bf16_f32 v248, v250, v251\n"
"ds_read_b128 v[196:199], v6 offset:5120\n"
"v_cvt_pk_bf16_f32 v249, v252, v253\n"
"s_waitcnt lgkmcnt(2)\n"
"s_barrier\n"
"v_mfma_f32_16x16x32_bf16 v[250:253], v[92:95], v[220:223], 0\n"
"v_exp_f32 v144, v144\n"
"v_mfma_f32_16x16x32_bf16 v[132:135], v[214:217], v[140:143], v[132:135]\n"
"ds_read_b128 v[220:223], v6 offset:6144\n"
"v_mfma_f32_16x16x32_bf16 v[132:135], v[216:219], v[24:27], v[132:135]\n"
"v_exp_f32 v145, v145\n"
"ds_read_b128 v[164:167], v201 offset:1392\n"
"v_mfma_f32_16x16x32_bf16 v[136:139], v[96:99], v[152:155], v[136:139]\n"
"v_exp_f32 v146, v146\n"
"v_mfma_f32_16x16x32_bf16 v[148:151], v[100:103], v[152:155], v[148:151]\n"
"ds_read_b128 v[160:163], v5 offset:45056\n"
"v_cvt_pk_bf16_f32 v144, v144, v145\n"
"ds_write_addtid_b32 v144 offset:57856\n"
"s_waitcnt lgkmcnt(4)\n"
"v_mfma_f32_16x16x32_bf16 v[250:253], v[196:199], v[152:155], v[250:253]\n"
"v_exp_f32 v147, v147\n"
"ds_read_b128 v[196:199], v6 offset:7168\n"
"v_mfma_f32_16x16x32_bf16 v[136:139], v[104:107], v[156:159], v[136:139]\n"
"v_cvt_pk_bf16_f32 v145, v146, v147\n"
"v_mfma_f32_16x16x32_bf16 v[148:151], v[108:111], v[156:159], v[148:151]\n"
"ds_write_addtid_b32 v145 offset:58112\n"
"s_waitcnt lgkmcnt(5)\n"
"v_mfma_f32_16x16x32_bf16 v[250:253], v[220:223], v[156:159], v[250:253]\n"
"s_waitcnt lgkmcnt(3)\n"
"v_exp_f32 v132, v132\n"
"ds_read_b128 v[220:223], v4 offset:47104\n"
"v_mfma_f32_16x16x32_bf16 v[136:139], v[112:115], v[160:163], v[136:139]\n"
"ds_read_b128 v[144:147], v6 offset:8192\n"
"v_mfma_f32_16x16x32_bf16 v[148:151], v[116:119], v[160:163], v[148:151]\n"
"ds_read_b128 v[152:155], v5 offset:47104\n"
"v_exp_f32 v133, v133\n"
"v_mfma_f32_16x16x32_bf16 v[164:167], v[244:247], v[140:143], v[164:167]\n"
"v_mfma_f32_16x16x32_bf16 v[164:167], v[246:249], v[24:27], v[164:167]\n"
"s_waitcnt lgkmcnt(4)\n"
"ds_read_b128 v[156:159], v4 offset:49152\n"
"ds_read_b128 v[140:143], v6 offset:9216\n"
"v_bfe_u32 v255, v6, 2, 6\n"
"v_mfma_f32_16x16x32_bf16 v[250:253], v[196:199], v[160:163], v[250:253]\n"
"s_add_u32 s69, s50, 4294966976\n"
"s_waitcnt lgkmcnt(4)\n"
"v_mfma_f32_16x16x32_bf16 v[136:139], v[0:3], v[220:223], v[136:139]\n"
"v_cvt_pk_bf16_f32 v132, v132, v133\n"
"v_exp_f32 v134, v134\n"
"ds_write_addtid_b32 v132 offset:57344\n"
"v_mfma_f32_16x16x32_bf16 v[148:151], v[120:123], v[220:223], v[148:151]\n"
"s_waitcnt lgkmcnt(4)\n"
"v_exp_f32 v135, v135\n"
"ds_read_b32 v255, v255 offset:1344\n"
"v_mfma_f32_16x16x32_bf16 v[250:253], v[144:147], v[220:223], v[250:253]\n"
"s_waitcnt lgkmcnt(4)\n"
"ds_read_b128 v[220:223], v5 offset:49152\n"
"v_mfma_f32_16x16x32_bf16 v[136:139], v[124:127], v[152:155], v[136:139]\n"
"v_cvt_pk_bf16_f32 v133, v134, v135\n"
"ds_write_addtid_b32 v133 offset:57600\n"
"ds_read_b128 v[132:135], v4 offset:51200\n"
"v_mfma_f32_16x16x32_bf16 v[148:151], v[128:131], v[152:155], v[148:151]\n"
"s_waitcnt lgkmcnt(5)\n"
"v_exp_f32 v164, v164\n"
"v_mfma_f32_16x16x32_bf16 v[250:253], v[140:143], v[152:155], v[250:253]\n"
"s_waitcnt lgkmcnt(3)\n"
"v_add_u32_sdwa v225, v255, v7 dst_sel:DWORD dst_unused:UNUSED_PAD src0_sel:WORD_0 src1_sel:DWORD\n"
"v_exp_f32 v165, v165\n"
"v_min_u32 v177, s69, v225\n"
"v_mfma_f32_16x16x32_bf16 v[196:199], v[8:11], v[156:159], 0\n"
"ds_read_b128 v[152:155], v177 offset:2704\n"
"v_mul_f32 v136, s60, v136\n"
"v_mfma_f32_16x16x32_bf16 v[140:143], v[12:15], v[156:159], 0\n"
"v_mul_f32 v150, s60, v150\n"
"v_cvt_pk_bf16_f32 v164, v164, v165\n"
"v_mfma_f32_16x16x32_bf16 v[144:147], v[16:19], v[156:159], 0\n"
"s_waitcnt lgkmcnt(3)\n"
"v_exp_f32 v166, v166\n"
"v_mfma_f32_16x16x32_bf16 v[196:199], v[20:23], v[220:223], v[196:199]\n"
"v_mul_f32 v137, s60, v137\n"
"v_mul_f32 v138, s60, v138\n"
"v_exp_f32 v167, v167\n"
"v_cvt_pk_bf16_f32 v136, v136, v137\n"
"v_cvt_pk_bf16_f32 v165, v166, v167\n"
"v_mfma_f32_16x16x32_bf16 v[140:143], v[28:31], v[220:223], v[140:143]\n"
"ds_write_addtid_b32 v164 offset:56832\n"
"ds_write_addtid_b32 v165 offset:57088\n"
"v_mul_f32 v139, s60, v139\n"
"v_mul_f32 v149, s60, v149\n"
"v_mfma_f32_16x16x32_bf16 v[144:147], v[32:35], v[220:223], v[144:147]\n"
"s_waitcnt lgkmcnt(3)\n"
"v_mul_f32 v253, s60, v253\n"
"v_mul_f32 v151, s60, v151\n"
"v_mul_f32 v250, s60, v250\n"
"v_mfma_f32_16x16x32_bf16 v[196:199], v[36:39], v[132:135], v[196:199]\n"
"v_cvt_pk_bf16_f32 v137, v138, v139\n"
"v_mul_f32 v148, s60, v148\n"
"v_mfma_f32_16x16x32_bf16 v[140:143], v[40:43], v[132:135], v[140:143]\n"
"ds_read_b128 v[220:223], v5 offset:51200\n"
"v_cvt_pk_bf16_f32 v139, v150, v151\n"
"v_cvt_pk_bf16_f32 v138, v148, v149\n"
"v_mul_f32 v252, s60, v252\n"
"v_cvt_pk_bf16_f32 v27, v252, v253\n"
"s_waitcnt lgkmcnt(3)\n"
"v_mfma_f32_16x16x32_bf16 v[152:155], v[184:187], v[136:139], v[152:155]\n"
"s_waitcnt lgkmcnt(1)\n"
"s_barrier\n"
"ds_read_b128 v[148:151], v4 offset:53248\n"
"v_mul_f32 v251, s60, v251\n"
"v_cvt_pk_bf16_f32 v26, v250, v251\n"
"s_nop 1\n"
"v_mfma_f32_16x16x32_bf16 v[152:155], v[186:189], v[24:27], v[152:155]\n"
"v_mfma_f32_16x16x32_bf16 v[144:147], v[44:47], v[132:135], v[144:147]\n"
"s_add_u32 s73, s50, 4294967280\n"
"ds_read_b128 v[250:253], v177 offset:2720\n"
"v_max_u32 v176, s73, v225\n"
"s_waitcnt lgkmcnt(2)\n"
"v_mfma_f32_16x16x32_bf16 v[196:199], v[48:51], v[220:223], v[196:199]\n"
"v_mfma_f32_16x16x32_bf16 v[140:143], v[52:55], v[220:223], v[140:143]\n"
"v_mfma_f32_16x16x32_bf16 v[144:147], v[56:59], v[220:223], v[144:147]\n"
"s_waitcnt lgkmcnt(1)\n"
"ds_read_b128 v[220:223], v5 offset:53248\n"
"v_exp_f32 v152, v152\n"
"v_mfma_f32_16x16x32_bf16 v[196:199], v[60:63], v[148:151], v[196:199]\n"
"ds_read_b128 v[132:135], v4 offset:55296\n"
"ds_read_b128 v[156:159], v176 offset:32\n"
"v_mfma_f32_16x16x32_bf16 v[140:143], v[64:67], v[148:151], v[140:143]\n"
"s_waitcnt lgkmcnt(3)\n"
"v_exp_f32 v153, v153\n"
"v_mfma_f32_16x16x32_bf16 v[250:253], v[190:193], v[136:139], v[250:253]\n"
"v_exp_f32 v154, v154\n"
"v_mfma_f32_16x16x32_bf16 v[250:253], v[192:195], v[24:27], v[250:253]\n"
"v_mfma_f32_16x16x32_bf16 v[144:147], v[68:71], v[148:151], v[144:147]\n"
"s_waitcnt lgkmcnt(2)\n"
"ds_read_b128 v[148:151], v5 offset:55296\n"
"v_mfma_f32_16x16x32_bf16 v[196:199], v[72:75], v[220:223], v[196:199]\n"
"v_exp_f32 v155, v155\n"
"v_mfma_f32_16x16x32_bf16 v[140:143], v[76:79], v[220:223], v[140:143]\n"
"v_cvt_pk_bf16_f32 v152, v152, v153\n"
"v_cvt_pk_bf16_f32 v153, v154, v155\n"
"v_mfma_f32_16x16x32_bf16 v[144:147], v[80:83], v[220:223], v[144:147]\n"
"ds_write_addtid_b32 v152 offset:53248\n"
"ds_write_addtid_b32 v153 offset:53504\n"
"v_exp_f32 v250, v250\n"
"s_waitcnt lgkmcnt(4)\n"
"v_mfma_f32_16x16x32_bf16 v[220:223], v[8:11], v[132:135], 0\n"
"s_waitcnt lgkmcnt(3)\n"
"v_cvt_pk_bf16_f32 v196, v196, v197\n"
"v_cvt_pk_bf16_f32 v197, v198, v199\n"
"v_cvt_pk_bf16_f32 v199, v142, v143\n"
"v_mfma_f32_16x16x32_bf16 v[156:159], v[208:211], v[136:139], v[156:159]\n"
"v_cvt_pk_bf16_f32 v198, v140, v141\n"
"v_mfma_f32_16x16x32_bf16 v[156:159], v[210:213], v[24:27], v[156:159]\n"
"v_cvt_pk_bf16_f32 v201, v146, v147\n"
"v_cvt_pk_bf16_f32 v200, v144, v145\n"
"v_mfma_f32_16x16x32_bf16 v[144:147], v[12:15], v[132:135], 0\n"
"ds_read_b128 v[140:143], v176 offset:48\n"
"v_mfma_f32_16x16x32_bf16 v[152:155], v[16:19], v[132:135], 0\n"
"s_waitcnt lgkmcnt(3)\n"
"ds_read_b128 v[132:135], v4 offset:57344\n"
"v_exp_f32 v251, v251\n"
"v_mfma_f32_16x16x32_bf16 v[220:223], v[20:23], v[148:151], v[220:223]\n"
"v_exp_f32 v252, v252\n"
"v_mfma_f32_16x16x32_bf16 v[144:147], v[28:31], v[148:151], v[144:147]\n"
"s_add_u32 s68, s50, 4294965936\n"
"v_cvt_pk_bf16_f32 v250, v250, v251\n"
"v_mfma_f32_16x16x32_bf16 v[152:155], v[32:35], v[148:151], v[152:155]\n"
"v_exp_f32 v253, v253\n"
"ds_write_addtid_b32 v250 offset:53760\n"
"v_cvt_pk_bf16_f32 v251, v252, v253\n"
"ds_write_addtid_b32 v251 offset:54016\n"
"s_waitcnt lgkmcnt(3)\n"
"v_mfma_f32_16x16x32_bf16 v[140:143], v[214:217], v[136:139], v[140:143]\n"
"v_exp_f32 v156, v156\n"
"v_mfma_f32_16x16x32_bf16 v[140:143], v[216:219], v[24:27], v[140:143]\n"
"s_waitcnt lgkmcnt(2)\n"
"ds_read_b128 v[252:255], v5 offset:57344\n"
"v_mfma_f32_16x16x32_bf16 v[220:223], v[36:39], v[132:135], v[220:223]\n"
"v_exp_f32 v157, v157\n"
"v_exp_f32 v158, v158\n"
"ds_read_b128 v[160:163], v176 offset:16\n"
"v_max_u32 v250, s68, v225\n"
"ds_read_b128 v[148:151], v250 offset:1392\n"
"v_mfma_f32_16x16x32_bf16 v[144:147], v[40:43], v[132:135], v[144:147]\n"
"v_mfma_f32_16x16x32_bf16 v[152:155], v[44:47], v[132:135], v[152:155]\n"
"s_waitcnt lgkmcnt(2)\n"
"v_cvt_pk_bf16_f32 v156, v156, v157\n"
"v_mfma_f32_16x16x32_bf16 v[220:223], v[48:51], v[252:255], v[220:223]\n"
"v_exp_f32 v159, v159\n"
"v_exp_f32 v140, v140\n"
"v_cvt_pk_bf16_f32 v157, v158, v159\n"
"v_mfma_f32_16x16x32_bf16 v[144:147], v[52:55], v[252:255], v[144:147]\n"
"s_waitcnt lgkmcnt(0)\n"
"ds_read_b128 v[132:135], v4 offset:59392\n"
"v_exp_f32 v141, v141\n"
"v_mfma_f32_16x16x32_bf16 v[148:151], v[244:247], v[136:139], v[148:151]\n"
"ds_write_addtid_b32 v156 offset:54272\n"
"ds_write_addtid_b32 v157 offset:54528\n"
"ds_read_b128 v[156:159], v5 offset:59392\n"
"v_mfma_f32_16x16x32_bf16 v[148:151], v[246:249], v[24:27], v[148:151]\n"
"v_mfma_f32_16x16x32_bf16 v[152:155], v[56:59], v[252:255], v[152:155]\n"
"s_waitcnt lgkmcnt(3)\n"
"v_exp_f32 v142, v142\n"
"v_mfma_f32_16x16x32_bf16 v[220:223], v[60:63], v[132:135], v[220:223]\n"
"ds_read_b128 v[252:255], v4 offset:12288\n"
"v_cvt_pk_bf16_f32 v140, v140, v141\n"
"v_mfma_f32_16x16x32_bf16 v[144:147], v[64:67], v[132:135], v[144:147]\n"
"v_exp_f32 v143, v143\n"
"v_mfma_f32_16x16x32_bf16 v[152:155], v[68:71], v[132:135], v[152:155]\n"
"s_waitcnt lgkmcnt(1)\n"
"v_cvt_pk_bf16_f32 v141, v142, v143\n"
"v_mfma_f32_16x16x32_bf16 v[220:223], v[72:75], v[156:159], v[220:223]\n"
"ds_write_addtid_b32 v140 offset:54784\n"
"ds_write_addtid_b32 v141 offset:55040\n"
"v_mfma_f32_16x16x32_bf16 v[160:163], v[202:205], v[136:139], v[160:163]\n"
"v_mfma_f32_16x16x32_bf16 v[160:163], v[204:207], v[24:27], v[160:163]\n"
"ds_read_b128 v[202:205], v250 offset:1360\n"
"v_mfma_f32_16x16x32_bf16 v[144:147], v[76:79], v[156:159], v[144:147]\n"
"v_mfma_f32_16x16x32_bf16 v[152:155], v[80:83], v[156:159], v[152:155]\n"
"v_exp_f32 v148, v148\n"
"v_exp_f32 v149, v149\n"
"v_exp_f32 v150, v150\n"
"v_exp_f32 v151, v151\n"
"v_cvt_pk_bf16_f32 v220, v220, v221\n"
"s_waitcnt lgkmcnt(3)\n"
"v_mfma_f32_16x16x32_bf16 v[140:143], v[84:87], v[252:255], 0\n"
"ds_read_b128 v[132:135], v5 offset:12288\n"
"v_cvt_pk_bf16_f32 v148, v148, v149\n"
"ds_write_addtid_b32 v148 offset:33792\n"
"v_cvt_pk_bf16_f32 v149, v150, v151\n"
"ds_write_addtid_b32 v149 offset:34048\n"
"v_cvt_pk_bf16_f32 v221, v222, v223\n"
"v_cvt_pk_bf16_f32 v222, v144, v145\n"
"v_cvt_pk_bf16_f32 v223, v146, v147\n"
"v_mfma_f32_16x16x32_bf16 v[144:147], v[88:91], v[252:255], 0\n"
"v_cvt_pk_bf16_f32 v225, v154, v155\n"
"v_cvt_pk_bf16_f32 v224, v152, v153\n"
"v_mfma_f32_16x16x32_bf16 v[152:155], v[92:95], v[252:255], 0\n"
"s_waitcnt lgkmcnt(3)\n"
"v_exp_f32 v160, v160\n"
"v_mfma_f32_16x16x32_bf16 v[202:205], v[232:235], v[136:139], v[202:205]\n"
"ds_read_b128 v[148:151], v6 offset:5120\n"
"ds_read_b128 v[156:159], v250 offset:1376\n"
"ds_read_b128 v[252:255], v4 offset:14336\n"
"v_mfma_f32_16x16x32_bf16 v[202:205], v[234:237], v[24:27], v[202:205]\n"
"s_waitcnt lgkmcnt(3)\n"
"s_barrier\n"
"v_mfma_f32_16x16x32_bf16 v[140:143], v[96:99], v[132:135], v[140:143]\n"
"v_exp_f32 v161, v161\n"
"v_exp_f32 v162, v162\n"
"v_mfma_f32_16x16x32_bf16 v[144:147], v[100:103], v[132:135], v[144:147]\n"
"s_waitcnt lgkmcnt(2)\n"
"ds_read_b128 v[164:167], v6 offset:6144\n"
"v_cvt_pk_bf16_f32 v160, v160, v161\n"
"v_mfma_f32_16x16x32_bf16 v[152:155], v[148:151], v[132:135], v[152:155]\n"
"s_waitcnt lgkmcnt(1)\n"
"v_exp_f32 v163, v163\n"
"v_mfma_f32_16x16x32_bf16 v[140:143], v[104:107], v[252:255], v[140:143]\n"
"ds_read_b128 v[132:135], v5 offset:14336\n"
"v_mfma_f32_16x16x32_bf16 v[144:147], v[108:111], v[252:255], v[144:147]\n"
"ds_write_addtid_b32 v160 offset:57856\n"
"v_cvt_pk_bf16_f32 v161, v162, v163\n"
"ds_write_addtid_b32 v161 offset:58112\n"
"v_mfma_f32_16x16x32_bf16 v[156:159], v[238:241], v[136:139], v[156:159]\n"
"ds_read_b128 v[160:163], v4 offset:16384\n"
"v_mfma_f32_16x16x32_bf16 v[156:159], v[240:243], v[24:27], v[156:159]\n"
"s_waitcnt lgkmcnt(4)\n"
"v_exp_f32 v202, v202\n"
"ds_read_b128 v[148:151], v6 offset:7168\n"
"v_mfma_f32_16x16x32_bf16 v[152:155], v[164:167], v[252:255], v[152:155]\n"
"s_waitcnt lgkmcnt(4)\n"
"ds_read_b128 v[164:167], v177 offset:2736\n"
"v_mfma_f32_16x16x32_bf16 v[140:143], v[112:115], v[132:135], v[140:143]\n"
"v_exp_f32 v203, v203\n"
"v_mfma_f32_16x16x32_bf16 v[144:147], v[116:119], v[132:135], v[144:147]\n"
"v_exp_f32 v204, v204\n"
"v_exp_f32 v205, v205\n"
"v_cvt_pk_bf16_f32 v202, v202, v203\n"
"ds_write_addtid_b32 v202 offset:58368\n"
"v_cvt_pk_bf16_f32 v203, v204, v205\n"
"ds_read_b128 v[250:253], v6 offset:8192\n"
"s_waitcnt lgkmcnt(3)\n"
"v_mfma_f32_16x16x32_bf16 v[152:155], v[148:151], v[132:135], v[152:155]\n"
"ds_read_b128 v[132:135], v5 offset:16384\n"
"v_mfma_f32_16x16x32_bf16 v[140:143], v[0:3], v[160:163], v[140:143]\n"
"s_waitcnt lgkmcnt(3)\n"
"v_mfma_f32_16x16x32_bf16 v[164:167], v[196:199], v[136:139], v[164:167]\n"
"ds_write_addtid_b32 v203 offset:58624\n"
"ds_read_b128 v[202:205], v6 offset:9216\n"
"v_mfma_f32_16x16x32_bf16 v[164:167], v[198:201], v[24:27], v[164:167]\n"
"v_exp_f32 v156, v156\n"
"ds_read_b128 v[136:139], v4 offset:18432\n"
"v_mfma_f32_16x16x32_bf16 v[144:147], v[120:123], v[160:163], v[144:147]\n"
"s_waitcnt lgkmcnt(4)\n"
"v_exp_f32 v157, v157\n"
"v_bfe_u32 v177, v6, 2, 6\n"
"v_mfma_f32_16x16x32_bf16 v[152:155], v[250:253], v[160:163], v[152:155]\n"
"s_waitcnt lgkmcnt(3)\n"
"v_exp_f32 v158, v158\n"
"v_mfma_f32_16x16x32_bf16 v[140:143], v[124:127], v[132:135], v[140:143]\n"
"ds_read_b32 v177, v177 offset:1408\n"
"ds_read_b128 v[148:151], v5 offset:18432\n"
"v_cvt_pk_bf16_f32 v156, v156, v157\n"
"v_mfma_f32_16x16x32_bf16 v[144:147], v[128:131], v[132:135], v[144:147]\n"
"s_nop 3\n"
"v_mul_f32 v140, s60, v140\n"
"v_mul_f32 v141, s60, v141\n"
"s_waitcnt lgkmcnt(3)\n"
"v_mfma_f32_16x16x32_bf16 v[152:155], v[202:205], v[132:135], v[152:155]\n"
"s_waitcnt lgkmcnt(2)\n"
"v_exp_f32 v159, v159\n"
"v_mfma_f32_16x16x32_bf16 v[250:253], v[8:11], v[136:139], 0\n"
"ds_read_b128 v[202:205], v4 offset:20480\n"
"v_mfma_f32_16x16x32_bf16 v[132:135], v[12:15], v[136:139], 0\n"
"ds_write_addtid_b32 v156 offset:58880\n"
"v_cvt_pk_bf16_f32 v157, v158, v159\n"
"ds_write_addtid_b32 v157 offset:59136\n"
"v_mfma_f32_16x16x32_bf16 v[156:159], v[16:19], v[136:139], 0\n"
"s_add_u32 s70, s50, 4294967264\n"
"s_waitcnt lgkmcnt(4)\n"
"v_add_u32_sdwa v176, v177, v7 dst_sel:DWORD dst_unused:UNUSED_PAD src0_sel:WORD_0 src1_sel:DWORD\n"
"v_mul_f32 v155, s60, v155\n"
"v_mul_f32 v152, s60, v152\n"
"s_waitcnt lgkmcnt(3)\n"
"v_mfma_f32_16x16x32_bf16 v[250:253], v[20:23], v[148:151], v[250:253]\n"
"v_exp_f32 v164, v164\n"
"v_exp_f32 v165, v165\n"
"v_exp_f32 v166, v166\n"
"v_cvt_pk_bf16_f32 v164, v164, v165\n"
"v_mfma_f32_16x16x32_bf16 v[132:135], v[28:31], v[148:151], v[132:135]\n"
"v_exp_f32 v167, v167\n"
"v_mfma_f32_16x16x32_bf16 v[156:159], v[32:35], v[148:151], v[156:159]\n"
"ds_read_b128 v[148:151], v5 offset:20480\n"
"ds_write_addtid_b32 v164 offset:57344\n"
"s_waitcnt lgkmcnt(4)\n"
"v_mfma_f32_16x16x32_bf16 v[250:253], v[36:39], v[202:205], v[250:253]\n"
"v_mul_f32 v144, s60, v144\n"
"v_mul_f32 v143, s60, v143\n"
"v_mul_f32 v145, s60, v145\n"
"v_mul_f32 v147, s60, v147\n"
"v_max_u32 v207, s70, v176\n"
"v_cvt_pk_bf16_f32 v165, v166, v167\n"
"ds_write_addtid_b32 v165 offset:57600\n"
"v_mul_f32 v153, s60, v153\n"
"v_cvt_pk_bf16_f32 v140, v140, v141\n"
"v_cvt_pk_bf16_f32 v26, v152, v153\n"
"v_mul_f32 v142, s60, v142\n"
"v_cvt_pk_bf16_f32 v141, v142, v143\n"
"ds_read_b128 v[136:139], v207 offset:48\n"
"v_mul_f32 v146, s60, v146\n"
"v_cvt_pk_bf16_f32 v143, v146, v147\n"
"v_cvt_pk_bf16_f32 v142, v144, v145\n"
"v_mfma_f32_16x16x32_bf16 v[132:135], v[40:43], v[202:205], v[132:135]\n"
"s_waitcnt lgkmcnt(1)\n"
"ds_read_b128 v[144:147], v4 offset:22528\n"
"v_mul_f32 v154, s60, v154\n"
"s_barrier\n"
"s_add_u32 s72, s50, 4294966960\n"
"v_add_u32_sdwa v177, sext(v177), v176 dst_sel:DWORD dst_unused:UNUSED_PAD src0_sel:WORD_1 src1_sel:DWORD\n"
"v_cvt_pk_bf16_f32 v27, v154, v155\n"
"s_waitcnt lgkmcnt(1)\n"
"v_mfma_f32_16x16x32_bf16 v[136:139], v[214:217], v[140:143], v[136:139]\n"
"v_max_u32 v206, s70, v177\n"
"v_mfma_f32_16x16x32_bf16 v[136:139], v[216:219], v[24:27], v[136:139]\n"
"ds_read_b128 v[214:217], v206 offset:64\n"
"v_min_u32 v206, s72, v176\n"
"v_mfma_f32_16x16x32_bf16 v[156:159], v[44:47], v[202:205], v[156:159]\n"
"v_mfma_f32_16x16x32_bf16 v[250:253], v[48:51], v[148:151], v[250:253]\n"
"v_mfma_f32_16x16x32_bf16 v[132:135], v[52:55], v[148:151], v[132:135]\n"
"ds_read_b128 v[202:205], v5 offset:22528\n"
"v_mfma_f32_16x16x32_bf16 v[156:159], v[56:59], v[148:151], v[156:159]\n"
"s_waitcnt lgkmcnt(2)\n"
"v_mfma_f32_16x16x32_bf16 v[250:253], v[60:63], v[144:147], v[250:253]\n"
"v_mfma_f32_16x16x32_bf16 v[132:135], v[64:67], v[144:147], v[132:135]\n"
"s_waitcnt lgkmcnt(1)\n"
"v_exp_f32 v136, v136\n"
"v_mfma_f32_16x16x32_bf16 v[214:217], v[220:223], v[140:143], v[214:217]\n"
"ds_read_b128 v[218:221], v4 offset:24576\n"
"v_mfma_f32_16x16x32_bf16 v[214:217], v[222:225], v[24:27], v[214:217]\n"
"v_exp_f32 v137, v137\n"
"v_exp_f32 v138, v138\n"
"v_mfma_f32_16x16x32_bf16 v[156:159], v[68:71], v[144:147], v[156:159]\n"
"s_waitcnt lgkmcnt(1)\n"
"ds_read_b128 v[222:225], v206 offset:2720\n"
"v_mfma_f32_16x16x32_bf16 v[250:253], v[72:75], v[202:205], v[250:253]\n"
"v_mfma_f32_16x16x32_bf16 v[132:135], v[76:79], v[202:205], v[132:135]\n"
"v_exp_f32 v139, v139\n"
"v_cvt_pk_bf16_f32 v136, v136, v137\n"
"ds_write_addtid_b32 v136 offset:55296\n"
"v_cvt_pk_bf16_f32 v137, v138, v139\n"
"v_mfma_f32_16x16x32_bf16 v[156:159], v[80:83], v[202:205], v[156:159]\n"
"ds_write_addtid_b32 v137 offset:55552\n"
"s_waitcnt lgkmcnt(3)\n"
"v_mfma_f32_16x16x32_bf16 v[202:205], v[8:11], v[218:221], 0\n"
"s_waitcnt lgkmcnt(2)\n"
"ds_read_b128 v[136:139], v5 offset:24576\n"
"v_cvt_pk_bf16_f32 v250, v250, v251\n"
"v_cvt_pk_bf16_f32 v251, v252, v253\n"
"v_mfma_f32_16x16x32_bf16 v[222:225], v[190:193], v[140:143], v[222:225]\n"
"v_cvt_pk_bf16_f32 v254, v156, v157\n"
"v_cvt_pk_bf16_f32 v252, v132, v133\n"
"v_mfma_f32_16x16x32_bf16 v[222:225], v[192:195], v[24:27], v[222:225]\n"
"v_cvt_pk_bf16_f32 v255, v158, v159\n"
"v_cvt_pk_bf16_f32 v253, v134, v135\n"
"v_exp_f32 v214, v214\n"
"v_exp_f32 v215, v215\n"
"v_mfma_f32_16x16x32_bf16 v[144:147], v[12:15], v[218:221], 0\n"
"ds_read_b128 v[132:135], v206 offset:2736\n"
"v_cvt_pk_bf16_f32 v214, v214, v215\n"
"v_mfma_f32_16x16x32_bf16 v[148:151], v[16:19], v[218:221], 0\n"
"s_waitcnt lgkmcnt(1)\n"
"v_exp_f32 v216, v216\n"
"v_exp_f32 v217, v217\n"
"v_mfma_f32_16x16x32_bf16 v[202:205], v[20:23], v[136:139], v[202:205]\n"
"ds_read_b128 v[218:221], v4 offset:26624\n"
"v_mfma_f32_16x16x32_bf16 v[144:147], v[28:31], v[136:139], v[144:147]\n"
"ds_write_addtid_b32 v214 offset:55808\n"
"v_mfma_f32_16x16x32_bf16 v[148:151], v[32:35], v[136:139], v[148:151]\n"
"s_waitcnt lgkmcnt(2)\n"
"ds_read_b128 v[136:139], v207 offset:32\n"
"v_mfma_f32_16x16x32_bf16 v[132:135], v[196:199], v[140:143], v[132:135]\n"
"s_add_u32 s71, s50, 1008\n"
"v_cvt_pk_bf16_f32 v215, v216, v217\n"
"ds_write_addtid_b32 v215 offset:56064\n"
"v_mfma_f32_16x16x32_bf16 v[132:135], v[198:201], v[24:27], v[132:135]\n"
"s_waitcnt lgkmcnt(3)\n"
"v_exp_f32 v224, v224\n"
"ds_read_b128 v[214:217], v5 offset:26624\n"
"v_mfma_f32_16x16x32_bf16 v[202:205], v[36:39], v[218:221], v[202:205]\n"
"v_exp_f32 v225, v225\n"
"v_mfma_f32_16x16x32_bf16 v[144:147], v[40:43], v[218:221], v[144:147]\n"
"v_cvt_pk_bf16_f32 v223, v224, v225\n"
"v_min_u32 v225, s71, v176\n"
"v_mfma_f32_16x16x32_bf16 v[148:151], v[44:47], v[218:221], v[148:151]\n"
"s_waitcnt lgkmcnt(0)\n"
"v_exp_f32 v132, v132\n"
"v_mfma_f32_16x16x32_bf16 v[202:205], v[48:51], v[214:217], v[202:205]\n"
"v_mfma_f32_16x16x32_bf16 v[144:147], v[52:55], v[214:217], v[144:147]\n"
"v_exp_f32 v133, v133\n"
"v_mfma_f32_16x16x32_bf16 v[136:139], v[208:211], v[140:143], v[136:139]\n"
"ds_read_b128 v[206:209], v5 offset:28672\n"
"ds_read_b128 v[218:221], v4 offset:28672\n"
"v_mfma_f32_16x16x32_bf16 v[136:139], v[210:213], v[24:27], v[136:139]\n"
"v_cvt_pk_bf16_f32 v132, v132, v133\n"
"ds_read_b128 v[210:213], v225 offset:1376\n"
"v_mfma_f32_16x16x32_bf16 v[148:151], v[56:59], v[214:217], v[148:151]\n"
"s_waitcnt lgkmcnt(1)\n"
"v_mfma_f32_16x16x32_bf16 v[202:205], v[60:63], v[218:221], v[202:205]\n"
"v_mfma_f32_16x16x32_bf16 v[144:147], v[64:67], v[218:221], v[144:147]\n"
"v_exp_f32 v134, v134\n"
"v_mfma_f32_16x16x32_bf16 v[148:151], v[68:71], v[218:221], v[148:151]\n"
"v_mfma_f32_16x16x32_bf16 v[202:205], v[72:75], v[206:209], v[202:205]\n"
"ds_write_addtid_b32 v223 offset:56576\n"
"s_waitcnt lgkmcnt(1)\n"
"v_mfma_f32_16x16x32_bf16 v[210:213], v[238:241], v[140:143], v[210:213]\n"
"v_exp_f32 v135, v135\n"
"ds_write_addtid_b32 v132 offset:56832\n"
"v_cvt_pk_bf16_f32 v133, v134, v135\n"
"v_exp_f32 v138, v138\n"
"v_mfma_f32_16x16x32_bf16 v[210:213], v[240:243], v[24:27], v[210:213]\n"
"ds_read_b128 v[218:221], v225 offset:1392\n"
"ds_write_addtid_b32 v133 offset:57088\n"
"v_mfma_f32_16x16x32_bf16 v[144:147], v[76:79], v[206:209], v[144:147]\n"
"v_mfma_f32_16x16x32_bf16 v[148:151], v[80:83], v[206:209], v[148:151]\n"
"ds_read_b128 v[214:217], v4 offset:30720\n"
"v_cvt_pk_bf16_f32 v202, v202, v203\n"
"v_cvt_pk_bf16_f32 v203, v204, v205\n"
"s_nop 3\n"
"v_cvt_pk_bf16_f32 v205, v146, v147\n"
"s_waitcnt lgkmcnt(1)\n"
"s_barrier\n"
"v_exp_f32 v139, v139\n"
"s_waitcnt lgkmcnt(0)\n"
"v_mfma_f32_16x16x32_bf16 v[132:135], v[84:87], v[214:217], 0\n"
"ds_read_b128 v[222:225], v5 offset:30720\n"
"v_cvt_pk_bf16_f32 v207, v150, v151\n"
"v_cvt_pk_bf16_f32 v204, v144, v145\n"
"v_min_u32 v176, s71, v177\n"
"v_cvt_pk_bf16_f32 v137, v138, v139\n"
"ds_write_addtid_b32 v137 offset:58112\n"
"v_mfma_f32_16x16x32_bf16 v[136:139], v[88:91], v[214:217], 0\n"
"ds_read_b128 v[144:147], v6 offset:5120\n"
"v_cvt_pk_bf16_f32 v206, v148, v149\n"
"v_mfma_f32_16x16x32_bf16 v[148:151], v[92:95], v[214:217], 0\n"
"ds_read_b128 v[214:217], v4 offset:32768\n"
"ds_read_b128 v[152:155], v176 offset:1408\n"
"v_mfma_f32_16x16x32_bf16 v[218:221], v[244:247], v[140:143], v[218:221]\n"
"v_exp_f32 v212, v212\n"
"v_mfma_f32_16x16x32_bf16 v[218:221], v[246:249], v[24:27], v[218:221]\n"
"s_waitcnt lgkmcnt(4)\n"
"v_mfma_f32_16x16x32_bf16 v[132:135], v[96:99], v[222:225], v[132:135]\n"
"v_exp_f32 v213, v213\n"
"s_nop 0\n"
"v_cvt_pk_bf16_f32 v211, v212, v213\n"
"v_mfma_f32_16x16x32_bf16 v[136:139], v[100:103], v[222:225], v[136:139]\n"
"s_waitcnt lgkmcnt(2)\n"
"ds_read_b128 v[156:159], v6 offset:6144\n"
"v_mfma_f32_16x16x32_bf16 v[148:151], v[144:147], v[222:225], v[148:151]\n"
"ds_read_b128 v[222:225], v6 offset:7168\n"
"ds_write_addtid_b32 v211 offset:58624\n"
"ds_read_b128 v[208:211], v5 offset:32768\n"
"s_waitcnt lgkmcnt(5)\n"
"v_mfma_f32_16x16x32_bf16 v[132:135], v[104:107], v[214:217], v[132:135]\n"
"v_min_u32 v176, s72, v177\n"
"v_mfma_f32_16x16x32_bf16 v[136:139], v[108:111], v[214:217], v[136:139]\n"
"s_waitcnt lgkmcnt(4)\n"
"v_exp_f32 v218, v218\n"
"v_mfma_f32_16x16x32_bf16 v[152:155], v[250:253], v[140:143], v[152:155]\n"
"ds_read_b128 v[144:147], v4 offset:34816\n"
"v_mfma_f32_16x16x32_bf16 v[152:155], v[252:255], v[24:27], v[152:155]\n"
"s_waitcnt lgkmcnt(4)\n"
"v_exp_f32 v219, v219\n"
"ds_read_b128 v[160:163], v176 offset:2752\n"
"v_mfma_f32_16x16x32_bf16 v[148:151], v[156:159], v[214:217], v[148:151]\n"
"s_waitcnt lgkmcnt(2)\n"
"ds_read_b128 v[212:215], v6 offset:8192\n"
"v_mfma_f32_16x16x32_bf16 v[132:135], v[112:115], v[208:211], v[132:135]\n"
"v_mfma_f32_16x16x32_bf16 v[136:139], v[116:119], v[208:211], v[136:139]\n"
"v_exp_f32 v220, v220\n"
"v_exp_f32 v221, v221\n"
"v_mfma_f32_16x16x32_bf16 v[148:151], v[222:225], v[208:211], v[148:151]\n"
"s_waitcnt lgkmcnt(2)\n"
"ds_read_b128 v[208:211], v5 offset:34816\n"
"v_cvt_pk_bf16_f32 v218, v218, v219\n"
"v_mfma_f32_16x16x32_bf16 v[132:135], v[0:3], v[144:147], v[132:135]\n"
"s_waitcnt lgkmcnt(2)\n"
"v_exp_f32 v152, v152\n"
"v_exp_f32 v153, v153\n"
"v_exp_f32 v154, v154\n"
"v_mfma_f32_16x16x32_bf16 v[160:163], v[202:205], v[140:143], v[160:163]\n"
"ds_write_addtid_b32 v218 offset:58880\n"
"v_cvt_pk_bf16_f32 v219, v220, v221\n"
"ds_write_addtid_b32 v219 offset:59136\n"
"v_cvt_pk_bf16_f32 v152, v152, v153\n"
"v_mfma_f32_16x16x32_bf16 v[160:163], v[204:207], v[24:27], v[160:163]\n"
"ds_read_b128 v[216:219], v6 offset:9216\n"
"v_mfma_f32_16x16x32_bf16 v[136:139], v[120:123], v[144:147], v[136:139]\n"
"s_waitcnt lgkmcnt(4)\n"
"v_exp_f32 v155, v155\n"
"v_mfma_f32_16x16x32_bf16 v[148:151], v[212:215], v[144:147], v[148:151]\n"
"s_waitcnt lgkmcnt(3)\n"
"v_bfe_u32 v225, v6, 2, 6\n"
"v_mfma_f32_16x16x32_bf16 v[132:135], v[124:127], v[208:211], v[132:135]\n"
"ds_read_b128 v[220:223], v4 offset:36864\n"
"v_mfma_f32_16x16x32_bf16 v[136:139], v[128:131], v[208:211], v[136:139]\n"
"s_waitcnt lgkmcnt(1)\n"
"v_mfma_f32_16x16x32_bf16 v[148:151], v[216:219], v[208:211], v[148:151]\n"
"ds_read_b128 v[212:215], v5 offset:36864\n"
"v_cvt_pk_bf16_f32 v153, v154, v155\n"
"v_exp_f32 v160, v160\n"
"v_exp_f32 v161, v161\n"
"v_exp_f32 v162, v162\n"
"s_waitcnt lgkmcnt(1)\n"
"v_mfma_f32_16x16x32_bf16 v[208:211], v[8:11], v[220:223], 0\n"
"ds_write_addtid_b32 v152 offset:34304\n"
"v_mul_f32 v132, s60, v132\n"
"v_mul_f32 v133, s60, v133\n"
"v_mul_f32 v136, s60, v136\n"
"v_mul_f32 v148, s60, v148\n"
"v_mfma_f32_16x16x32_bf16 v[140:143], v[12:15], v[220:223], 0\n"
"ds_read_b32 v225, v225 offset:1472\n"
"v_mul_f32 v135, s60, v135\n"
"v_mul_f32 v137, s60, v137\n"
"v_mfma_f32_16x16x32_bf16 v[144:147], v[16:19], v[220:223], 0\n"
"s_waitcnt lgkmcnt(2)\n"
"v_exp_f32 v163, v163\n"
"ds_read_b128 v[216:219], v4 offset:38912\n"
"v_mul_f32 v139, s60, v139\n"
"v_mfma_f32_16x16x32_bf16 v[208:211], v[20:23], v[212:215], v[208:211]\n"
"v_mul_f32 v134, s60, v134\n"
"v_cvt_pk_bf16_f32 v160, v160, v161\n"
"v_cvt_pk_bf16_f32 v161, v162, v163\n"
"ds_write_addtid_b32 v160 offset:57344\n"
"v_mul_f32 v149, s60, v149\n"
"v_mfma_f32_16x16x32_bf16 v[140:143], v[28:31], v[212:215], v[140:143]\n"
"v_cvt_pk_bf16_f32 v132, v132, v133\n"
"v_mul_f32 v138, s60, v138\n"
"ds_write_addtid_b32 v153 offset:34560\n"
"v_mul_f32 v150, s60, v150\n"
"v_mfma_f32_16x16x32_bf16 v[144:147], v[32:35], v[212:215], v[144:147]\n"
"s_waitcnt lgkmcnt(3)\n"
"v_cvt_pk_bf16_f32 v133, v134, v135\n"
"v_add_u32_sdwa v224, v225, v7 dst_sel:DWORD dst_unused:UNUSED_PAD src0_sel:WORD_0 src1_sel:DWORD\n"
"v_cvt_pk_bf16_f32 v134, v136, v137\n"
"s_add_u32 s68, s50, 4294966992\n"
"s_waitcnt lgkmcnt(2)\n"
"v_mfma_f32_16x16x32_bf16 v[208:211], v[36:39], v[216:219], v[208:211]\n"
"ds_read_b128 v[212:215], v5 offset:38912\n"
"ds_write_addtid_b32 v161 offset:57600\n"
"v_min_u32 v225, s68, v224\n"
"ds_read_b128 v[220:223], v225 offset:2688\n"
"v_cvt_pk_bf16_f32 v26, v148, v149\n"
"s_waitcnt lgkmcnt(1)\n"
"s_barrier\n"
"v_mfma_f32_16x16x32_bf16 v[140:143], v[40:43], v[216:219], v[140:143]\n"
"v_mul_f32 v151, s60, v151\n"
"v_cvt_pk_bf16_f32 v27, v150, v151\n"
"v_cvt_pk_bf16_f32 v135, v138, v139\n"
"ds_read_b128 v[136:139], v4 offset:40960\n"
"s_waitcnt lgkmcnt(1)\n"
"v_mfma_f32_16x16x32_bf16 v[220:223], v[178:181], v[132:135], v[220:223]\n"
"v_mfma_f32_16x16x32_bf16 v[220:223], v[180:183], v[24:27], v[220:223]\n"
"v_mfma_f32_16x16x32_bf16 v[144:147], v[44:47], v[216:219], v[144:147]\n"
"ds_read_b128 v[216:219], v5 offset:40960\n"
"v_mfma_f32_16x16x32_bf16 v[208:211], v[48:51], v[212:215], v[208:211]\n"
"ds_read_b128 v[148:151], v225 offset:2704\n"
"v_mfma_f32_16x16x32_bf16 v[140:143], v[52:55], v[212:215], v[140:143]\n"
"v_mfma_f32_16x16x32_bf16 v[144:147], v[56:59], v[212:215], v[144:147]\n"
"s_waitcnt lgkmcnt(2)\n"
"v_mfma_f32_16x16x32_bf16 v[208:211], v[60:63], v[136:139], v[208:211]\n"
"v_mfma_f32_16x16x32_bf16 v[140:143], v[64:67], v[136:139], v[140:143]\n"
"v_exp_f32 v220, v220\n"
"v_exp_f32 v221, v221\n"
"s_nop 0\n"
"v_cvt_pk_bf16_f32 v220, v220, v221\n"
"v_mfma_f32_16x16x32_bf16 v[144:147], v[68:71], v[136:139], v[144:147]\n"
"s_add_u32 s73, s50, 4294965952\n"
"v_exp_f32 v222, v222\n"
"s_waitcnt lgkmcnt(1)\n"
"v_mfma_f32_16x16x32_bf16 v[208:211], v[72:75], v[216:219], v[208:211]\n"
"s_waitcnt lgkmcnt(0)\n"
"v_mfma_f32_16x16x32_bf16 v[148:151], v[184:187], v[132:135], v[148:151]\n"
"ds_read_b128 v[136:139], v4 offset:43008\n"
"ds_write_addtid_b32 v220 offset:53248\n"
"ds_read_b128 v[152:155], v5 offset:43008\n"
"v_mfma_f32_16x16x32_bf16 v[148:151], v[186:189], v[24:27], v[148:151]\n"
"v_mfma_f32_16x16x32_bf16 v[140:143], v[76:79], v[216:219], v[140:143]\n"
"v_max_u32 v177, s73, v224\n"
"ds_read_b128 v[156:159], v177 offset:1344\n"
"v_mfma_f32_16x16x32_bf16 v[144:147], v[80:83], v[216:219], v[144:147]\n"
"s_waitcnt lgkmcnt(3)\n"
"v_exp_f32 v223, v223\n"
"v_mfma_f32_16x16x32_bf16 v[214:217], v[8:11], v[136:139], 0\n"
"v_cvt_pk_bf16_f32 v208, v208, v209\n"
"v_cvt_pk_bf16_f32 v221, v222, v223\n"
"ds_write_addtid_b32 v221 offset:53504\n"
"v_cvt_pk_bf16_f32 v209, v210, v211\n"
"v_mfma_f32_16x16x32_bf16 v[218:221], v[12:15], v[136:139], 0\n"
"v_cvt_pk_bf16_f32 v210, v140, v141\n"
"v_exp_f32 v148, v148\n"
"v_cvt_pk_bf16_f32 v211, v142, v143\n"
"v_mfma_f32_16x16x32_bf16 v[140:143], v[16:19], v[136:139], 0\n"
"s_waitcnt lgkmcnt(2)\n"
"ds_read_b128 v[136:139], v4 offset:45056\n"
"v_cvt_pk_bf16_f32 v213, v146, v147\n"
"v_cvt_pk_bf16_f32 v212, v144, v145\n"
"v_mfma_f32_16x16x32_bf16 v[214:217], v[20:23], v[152:155], v[214:217]\n"
"v_mfma_f32_16x16x32_bf16 v[218:221], v[28:31], v[152:155], v[218:221]\n"
"v_exp_f32 v149, v149\n"
"v_mfma_f32_16x16x32_bf16 v[140:143], v[32:35], v[152:155], v[140:143]\n"
"s_waitcnt lgkmcnt(2)\n"
"v_mfma_f32_16x16x32_bf16 v[156:159], v[226:229], v[132:135], v[156:159]\n"
"v_exp_f32 v150, v150\n"
"ds_read_b128 v[152:155], v4 offset:47104\n"
"v_exp_f32 v151, v151\n"
"v_cvt_pk_bf16_f32 v148, v148, v149\n"
"v_mfma_f32_16x16x32_bf16 v[156:159], v[228:231], v[24:27], v[156:159]\n"
"ds_read_b128 v[160:163], v177 offset:1360\n"
"ds_write_addtid_b32 v148 offset:53760\n"
"ds_read_b128 v[144:147], v5 offset:45056\n"
"s_waitcnt lgkmcnt(4)\n"
"v_mfma_f32_16x16x32_bf16 v[214:217], v[36:39], v[136:139], v[214:217]\n"
"s_nop 2\n"
"v_exp_f32 v156, v156\n"
"v_mfma_f32_16x16x32_bf16 v[218:221], v[40:43], v[136:139], v[218:221]\n"
"v_mfma_f32_16x16x32_bf16 v[140:143], v[44:47], v[136:139], v[140:143]\n"
"s_waitcnt lgkmcnt(0)\n"
"ds_read_b128 v[136:139], v5 offset:47104\n"
"v_mfma_f32_16x16x32_bf16 v[214:217], v[48:51], v[144:147], v[214:217]\n"
"v_cvt_pk_bf16_f32 v149, v150, v151\n"
"v_mfma_f32_16x16x32_bf16 v[218:221], v[52:55], v[144:147], v[218:221]\n"
"ds_write_addtid_b32 v149 offset:54016\n"
"v_mfma_f32_16x16x32_bf16 v[140:143], v[56:59], v[144:147], v[140:143]\n"
"v_exp_f32 v157, v157\n"
"v_mfma_f32_16x16x32_bf16 v[214:217], v[60:63], v[152:155], v[214:217]\n"
"v_cvt_pk_bf16_f32 v156, v156, v157\n"
"v_mfma_f32_16x16x32_bf16 v[160:163], v[232:235], v[132:135], v[160:163]\n"
"ds_read_b128 v[148:151], v177 offset:1376\n"
"v_mfma_f32_16x16x32_bf16 v[160:163], v[234:237], v[24:27], v[160:163]\n"
"v_mfma_f32_16x16x32_bf16 v[218:221], v[64:67], v[152:155], v[218:221]\n"
"v_mfma_f32_16x16x32_bf16 v[140:143], v[68:71], v[152:155], v[140:143]\n"
"ds_write_addtid_b32 v156 offset:54272\n"
"s_waitcnt lgkmcnt(3)\n"
"v_mfma_f32_16x16x32_bf16 v[214:217], v[72:75], v[136:139], v[214:217]\n"
"v_exp_f32 v158, v158\n"
"v_mfma_f32_16x16x32_bf16 v[218:221], v[76:79], v[136:139], v[218:221]\n"
"v_exp_f32 v159, v159\n"
"ds_read_b128 v[144:147], v4 offset:55296\n"
"v_cvt_pk_bf16_f32 v157, v158, v159\n"
"ds_write_addtid_b32 v157 offset:54528\n"
"s_nop 1\n"
"v_cvt_pk_bf16_f32 v214, v214, v215\n"
"v_cvt_pk_bf16_f32 v215, v216, v217\n"
"v_cvt_pk_bf16_f32 v217, v220, v221\n"
"v_mfma_f32_16x16x32_bf16 v[140:143], v[80:83], v[136:139], v[140:143]\n"
"s_waitcnt lgkmcnt(1)\n"
"v_mfma_f32_16x16x32_bf16 v[136:139], v[84:87], v[144:147], 0\n"
"v_cvt_pk_bf16_f32 v216, v218, v219\n"
"s_nop 4\n"
"v_cvt_pk_bf16_f32 v219, v142, v143\n"
"v_cvt_pk_bf16_f32 v218, v140, v141\n"
"v_mfma_f32_16x16x32_bf16 v[156:159], v[88:91], v[144:147], 0\n"
"ds_read_b128 v[152:155], v5 offset:55296\n"
"v_mfma_f32_16x16x32_bf16 v[140:143], v[92:95], v[144:147], 0\n"
"s_waitcnt lgkmcnt(1)\n"
"s_barrier\n"
"ds_read_b128 v[164:167], v6 offset:5120\n"
"v_mfma_f32_16x16x32_bf16 v[148:151], v[238:241], v[132:135], v[148:151]\n"
"ds_read_b128 v[220:223], v4 offset:57344\n"
"v_mfma_f32_16x16x32_bf16 v[148:151], v[240:243], v[24:27], v[148:151]\n"
"s_waitcnt lgkmcnt(2)\n"
"ds_read_b128 v[144:147], v6 offset:6144\n"
"v_mfma_f32_16x16x32_bf16 v[136:139], v[96:99], v[152:155], v[136:139]\n"
"v_exp_f32 v160, v160\n"
"v_exp_f32 v161, v161\n"
"v_exp_f32 v162, v162\n"
"v_mfma_f32_16x16x32_bf16 v[156:159], v[100:103], v[152:155], v[156:159]\n"
"s_waitcnt lgkmcnt(2)\n"
"ds_read_b128 v[168:171], v5 offset:57344\n"
"v_mfma_f32_16x16x32_bf16 v[140:143], v[164:167], v[152:155], v[140:143]\n"
"s_waitcnt lgkmcnt(2)\n"
"v_cvt_pk_bf16_f32 v160, v160, v161\n"
"v_mfma_f32_16x16x32_bf16 v[136:139], v[104:107], v[220:223], v[136:139]\n"
"ds_read_b128 v[152:155], v225 offset:2720\n"
"v_mfma_f32_16x16x32_bf16 v[156:159], v[108:111], v[220:223], v[156:159]\n"
"s_waitcnt lgkmcnt(2)\n"
"v_exp_f32 v163, v163\n"
"v_mfma_f32_16x16x32_bf16 v[140:143], v[144:147], v[220:223], v[140:143]\n"
"s_waitcnt lgkmcnt(1)\n"
"v_cvt_pk_bf16_f32 v161, v162, v163\n"
"v_mfma_f32_16x16x32_bf16 v[136:139], v[112:115], v[168:171], v[136:139]\n"
"ds_read_b128 v[220:223], v6 offset:7168\n"
"v_mfma_f32_16x16x32_bf16 v[156:159], v[116:119], v[168:171], v[156:159]\n"
"ds_read_b128 v[144:147], v4 offset:59392\n"
"ds_write_addtid_b32 v160 offset:54784\n"
"ds_write_addtid_b32 v161 offset:55040\n"
"s_waitcnt lgkmcnt(4)\n"
"v_mfma_f32_16x16x32_bf16 v[152:155], v[190:193], v[132:135], v[152:155]\n"
"ds_read_b128 v[160:163], v6 offset:8192\n"
"v_mfma_f32_16x16x32_bf16 v[152:155], v[192:195], v[24:27], v[152:155]\n"
"s_waitcnt lgkmcnt(4)\n"
"v_exp_f32 v148, v148\n"
"v_mfma_f32_16x16x32_bf16 v[140:143], v[220:223], v[168:171], v[140:143]\n"
"v_exp_f32 v149, v149\n"
"v_exp_f32 v150, v150\n"
"ds_read_b128 v[220:223], v6 offset:9216\n"
"v_cvt_pk_bf16_f32 v148, v148, v149\n"
"ds_write_addtid_b32 v148 offset:57856\n"
"s_waitcnt lgkmcnt(5)\n"
"v_mfma_f32_16x16x32_bf16 v[136:139], v[0:3], v[144:147], v[136:139]\n"
"v_bfe_u32 v225, v6, 2, 6\n"
"v_mfma_f32_16x16x32_bf16 v[156:159], v[120:123], v[144:147], v[156:159]\n"
"s_waitcnt lgkmcnt(2)\n"
"ds_read_b128 v[132:135], v5 offset:59392\n"
"ds_read_b128 v[164:167], v4 offset:49152\n"
"v_mfma_f32_16x16x32_bf16 v[140:143], v[160:163], v[144:147], v[140:143]\n"
"s_waitcnt lgkmcnt(1)\n"
"v_mfma_f32_16x16x32_bf16 v[136:139], v[124:127], v[132:135], v[136:139]\n"
"v_exp_f32 v151, v151\n"
"v_exp_f32 v152, v152\n"
"v_mfma_f32_16x16x32_bf16 v[156:159], v[128:131], v[132:135], v[156:159]\n"
"s_add_u32 s70, s50, 4294964608\n"
"v_mfma_f32_16x16x32_bf16 v[140:143], v[220:223], v[132:135], v[140:143]\n"
"ds_read_b32 v225, v225 offset:1536\n"
"v_cvt_pk_bf16_f32 v149, v150, v151\n"
"s_nop 0\n"
"v_mul_f32 v136, s60, v136\n"
"s_waitcnt lgkmcnt(1)\n"
"v_mfma_f32_16x16x32_bf16 v[220:223], v[8:11], v[164:167], 0\n"
"v_mul_f32 v138, s60, v138\n"
"ds_read_b128 v[144:147], v5 offset:49152\n"
"v_mul_f32 v156, s60, v156\n"
"v_mfma_f32_16x16x32_bf16 v[132:135], v[12:15], v[164:167], 0\n"
"v_exp_f32 v153, v153\n"
"v_exp_f32 v154, v154\n"
"v_mul_f32 v142, s60, v142\n"
"v_cvt_pk_bf16_f32 v152, v152, v153\n"
"ds_write_addtid_b32 v152 offset:57344\n"
"v_mfma_f32_16x16x32_bf16 v[160:163], v[16:19], v[164:167], 0\n"
"s_waitcnt lgkmcnt(1)\n"
"v_mfma_f32_16x16x32_bf16 v[220:223], v[20:23], v[144:147], v[220:223]\n"
"ds_write_addtid_b32 v149 offset:58112\n"
"ds_read_b128 v[148:151], v4 offset:51200\n"
"v_mfma_f32_16x16x32_bf16 v[132:135], v[28:31], v[144:147], v[132:135]\n"
"v_exp_f32 v155, v155\n"
"v_mul_f32 v139, s60, v139\n"
"v_mul_f32 v159, s60, v159\n"
"v_mul_f32 v137, s60, v137\n"
"v_cvt_pk_bf16_f32 v136, v136, v137\n"
"v_mul_f32 v141, s60, v141\n"
"v_mul_f32 v158, s60, v158\n"
"v_mfma_f32_16x16x32_bf16 v[160:163], v[32:35], v[144:147], v[160:163]\n"
"s_waitcnt lgkmcnt(0)\n"
"ds_read_b128 v[144:147], v5 offset:51200\n"
"v_cvt_pk_bf16_f32 v137, v138, v139\n"
"v_mfma_f32_16x16x32_bf16 v[220:223], v[36:39], v[148:151], v[220:223]\n"
"v_mul_f32 v157, s60, v157\n"
"v_cvt_pk_bf16_f32 v138, v156, v157\n"
"v_mfma_f32_16x16x32_bf16 v[132:135], v[40:43], v[148:151], v[132:135]\n"
"v_add_u32_sdwa v177, v225, v7 dst_sel:DWORD dst_unused:UNUSED_PAD src0_sel:WORD_0 src1_sel:DWORD\n"
"v_cvt_pk_bf16_f32 v153, v154, v155\n"
"v_mul_f32 v143, s60, v143\n"
"v_mul_f32 v140, s60, v140\n"
"s_add_u32 s71, s50, 4294965648\n"
"v_max_u32 v176, s70, v177\n"
"ds_write_addtid_b32 v153 offset:57600\n"
"ds_read_b128 v[152:155], v176 offset:2688\n"
"v_cvt_pk_bf16_f32 v139, v158, v159\n"
"s_waitcnt lgkmcnt(1)\n"
"s_barrier\n"
"s_waitcnt lgkmcnt(0)\n"
"v_mfma_f32_16x16x32_bf16 v[152:155], v[178:181], v[136:139], v[152:155]\n"
"v_cvt_pk_bf16_f32 v27, v142, v143\n"
"v_cvt_pk_bf16_f32 v26, v140, v141\n"
"s_nop 1\n"
"v_mfma_f32_16x16x32_bf16 v[152:155], v[180:183], v[24:27], v[152:155]\n"
"ds_read_b128 v[140:143], v176 offset:2704\n"
"v_mfma_f32_16x16x32_bf16 v[160:163], v[44:47], v[148:151], v[160:163]\n"
"ds_read_b128 v[148:151], v4 offset:53248\n"
"v_mfma_f32_16x16x32_bf16 v[220:223], v[48:51], v[144:147], v[220:223]\n"
"v_mfma_f32_16x16x32_bf16 v[132:135], v[52:55], v[144:147], v[132:135]\n"
"v_mfma_f32_16x16x32_bf16 v[160:163], v[56:59], v[144:147], v[160:163]\n"
"s_waitcnt lgkmcnt(1)\n"
"v_min_u32 v175, s71, v177\n"
"ds_read_b128 v[144:147], v175 offset:4032\n"
"v_mfma_f32_16x16x32_bf16 v[140:143], v[184:187], v[136:139], v[140:143]\n"
"v_mfma_f32_16x16x32_bf16 v[140:143], v[186:189], v[24:27], v[140:143]\n"
"s_waitcnt lgkmcnt(1)\n"
"v_exp_f32 v152, v152\n"
"v_exp_f32 v153, v153\n"
"v_mfma_f32_16x16x32_bf16 v[220:223], v[60:63], v[148:151], v[220:223]\n"
"ds_read_b128 v[156:159], v5 offset:53248\n"
"v_mfma_f32_16x16x32_bf16 v[132:135], v[64:67], v[148:151], v[132:135]\n"
"v_exp_f32 v154, v154\n"
"v_exp_f32 v155, v155\n"
"v_mfma_f32_16x16x32_bf16 v[160:163], v[68:71], v[148:151], v[160:163]\n"
"s_waitcnt lgkmcnt(1)\n"
"ds_read_b128 v[148:151], v175 offset:4048\n"
"v_mfma_f32_16x16x32_bf16 v[144:147], v[208:211], v[136:139], v[144:147]\n"
"v_cvt_pk_bf16_f32 v152, v152, v153\n"
"v_cvt_pk_bf16_f32 v153, v154, v155\n"
"ds_write_addtid_b32 v152 offset:55296\n"
"ds_write_addtid_b32 v153 offset:55552\n"
"v_mfma_f32_16x16x32_bf16 v[144:147], v[210:213], v[24:27], v[144:147]\n"
"s_waitcnt lgkmcnt(3)\n"
"v_mfma_f32_16x16x32_bf16 v[220:223], v[72:75], v[156:159], v[220:223]\n"
"v_exp_f32 v140, v140\n"
"v_mfma_f32_16x16x32_bf16 v[132:135], v[76:79], v[156:159], v[132:135]\n"
"v_exp_f32 v141, v141\n"
"v_exp_f32 v142, v142\n"
"ds_read_b128 v[152:155], v4 offset:30720\n"
"v_mfma_f32_16x16x32_bf16 v[160:163], v[80:83], v[156:159], v[160:163]\n"
"v_cvt_pk_bf16_f32 v140, v140, v141\n"
"v_exp_f32 v143, v143\n"
"ds_write_addtid_b32 v140 offset:55808\n"
"v_cvt_pk_bf16_f32 v141, v142, v143\n"
"ds_write_addtid_b32 v141 offset:56064\n"
"s_add_u32 s69, s50, 4294965952\n"
"v_cvt_pk_bf16_f32 v220, v220, v221\n"
"v_cvt_pk_bf16_f32 v221, v222, v223\n"
"s_waitcnt lgkmcnt(5)\n"
"v_mfma_f32_16x16x32_bf16 v[148:151], v[214:217], v[136:139], v[148:151]\n"
"v_cvt_pk_bf16_f32 v225, v162, v163\n"
"v_mfma_f32_16x16x32_bf16 v[148:151], v[216:219], v[24:27], v[148:151]\n"
"s_waitcnt lgkmcnt(2)\n"
"ds_read_b128 v[140:143], v176 offset:2720\n"
"v_cvt_pk_bf16_f32 v223, v134, v135\n"
"v_cvt_pk_bf16_f32 v222, v132, v133\n"
"v_mfma_f32_16x16x32_bf16 v[132:135], v[84:87], v[152:155], 0\n"
"ds_read_b128 v[156:159], v5 offset:30720\n"
"v_cvt_pk_bf16_f32 v224, v160, v161\n"
"v_mfma_f32_16x16x32_bf16 v[160:163], v[88:91], v[152:155], 0\n"
"v_exp_f32 v144, v144\n"
"v_exp_f32 v145, v145\n"
"v_exp_f32 v146, v146\n"
"v_exp_f32 v147, v147\n"
"v_mfma_f32_16x16x32_bf16 v[164:167], v[92:95], v[152:155], 0\n"
"s_waitcnt lgkmcnt(1)\n"
"v_cvt_pk_bf16_f32 v144, v144, v145\n"
"v_mfma_f32_16x16x32_bf16 v[140:143], v[190:193], v[136:139], v[140:143]\n"
"v_exp_f32 v148, v148\n"
"v_max_u32 v174, s69, v177\n"
"ds_read_b128 v[168:171], v174 offset:1344\n"
"ds_write_addtid_b32 v144 offset:56320\n"
"ds_read_b128 v[152:155], v6 offset:5120\n"
"v_mfma_f32_16x16x32_bf16 v[140:143], v[192:195], v[24:27], v[140:143]\n"
"s_waitcnt lgkmcnt(3)\n"
"v_exp_f32 v149, v149\n"
"v_mfma_f32_16x16x32_bf16 v[132:135], v[96:99], v[156:159], v[132:135]\n"
"v_exp_f32 v150, v150\n"
"v_cvt_pk_bf16_f32 v145, v146, v147\n"
"v_mfma_f32_16x16x32_bf16 v[160:163], v[100:103], v[156:159], v[160:163]\n"
"ds_write_addtid_b32 v145 offset:56576\n"
"ds_read_b128 v[144:147], v4 offset:32768\n"
"v_exp_f32 v151, v151\n"
"s_waitcnt lgkmcnt(2)\n"
"v_mfma_f32_16x16x32_bf16 v[164:167], v[152:155], v[156:159], v[164:167]\n"
"ds_read_b128 v[156:159], v174 offset:1360\n"
"v_mfma_f32_16x16x32_bf16 v[168:171], v[226:229], v[136:139], v[168:171]\n"
"ds_read_b128 v[152:155], v6 offset:6144\n"
"v_mfma_f32_16x16x32_bf16 v[168:171], v[228:231], v[24:27], v[168:171]\n"
"s_waitcnt lgkmcnt(2)\n"
"v_cvt_pk_bf16_f32 v148, v148, v149\n"
"v_cvt_pk_bf16_f32 v149, v150, v151\n"
"v_mfma_f32_16x16x32_bf16 v[132:135], v[104:107], v[144:147], v[132:135]\n"
"v_mfma_f32_16x16x32_bf16 v[160:163], v[108:111], v[144:147], v[160:163]\n"
"v_exp_f32 v140, v140\n"
"ds_write_addtid_b32 v148 offset:56832\n"
"s_waitcnt lgkmcnt(1)\n"
"v_mfma_f32_16x16x32_bf16 v[164:167], v[152:155], v[144:147], v[164:167]\n"
"ds_read_b128 v[226:229], v5 offset:32768\n"
"v_mfma_f32_16x16x32_bf16 v[156:159], v[232:235], v[136:139], v[156:159]\n"
"v_exp_f32 v141, v141\n"
"s_nop 0\n"
"v_cvt_pk_bf16_f32 v140, v140, v141\n"
"ds_write_addtid_b32 v140 offset:34304\n"
"ds_write_addtid_b32 v149 offset:57088\n"
"v_mfma_f32_16x16x32_bf16 v[156:159], v[234:237], v[24:27], v[156:159]\n"
"s_waitcnt lgkmcnt(2)\n"
"v_mfma_f32_16x16x32_bf16 v[132:135], v[112:115], v[226:229], v[132:135]\n"
"v_exp_f32 v168, v168\n"
"ds_read_b128 v[144:147], v6 offset:7168\n"
"v_mfma_f32_16x16x32_bf16 v[160:163], v[116:119], v[226:229], v[160:163]\n"
"s_waitcnt lgkmcnt(0)\n"
"ds_read_b128 v[148:151], v174 offset:1376\n"
"v_mfma_f32_16x16x32_bf16 v[164:167], v[144:147], v[226:229], v[164:167]\n"
"v_exp_f32 v169, v169\n"
"v_exp_f32 v170, v170\n"
"v_exp_f32 v171, v171\n"
"s_barrier\n"
"ds_read_b128 v[140:143], v4 offset:34816\n"
"ds_read_b128 v[226:229], v6 offset:8192\n"
"v_cvt_pk_bf16_f32 v168, v168, v169\n"
"v_exp_f32 v156, v156\n"
"ds_write_addtid_b32 v168 offset:57856\n"
"s_waitcnt lgkmcnt(3)\n"
"v_mfma_f32_16x16x32_bf16 v[148:151], v[238:241], v[136:139], v[148:151]\n"
"ds_read_b128 v[144:147], v175 offset:4064\n"
"v_mfma_f32_16x16x32_bf16 v[148:151], v[240:243], v[24:27], v[148:151]\n"
"s_waitcnt lgkmcnt(3)\n"
"v_exp_f32 v157, v157\n"
"v_mfma_f32_16x16x32_bf16 v[132:135], v[0:3], v[140:143], v[132:135]\n"
"ds_read_b128 v[152:155], v5 offset:34816\n"
"v_cvt_pk_bf16_f32 v169, v170, v171\n"
"v_exp_f32 v158, v158\n"
"v_exp_f32 v159, v159\n"
"ds_write_addtid_b32 v169 offset:58112\n"
"v_mfma_f32_16x16x32_bf16 v[160:163], v[120:123], v[140:143], v[160:163]\n"
"s_add_u32 s73, s50, 4294966976\n"
"v_cvt_pk_bf16_f32 v156, v156, v157\n"
"s_waitcnt lgkmcnt(4)\n"
"v_mfma_f32_16x16x32_bf16 v[164:167], v[226:229], v[140:143], v[164:167]\n"
"s_waitcnt lgkmcnt(2)\n"
"ds_read_b128 v[226:229], v6 offset:9216\n"
"v_mfma_f32_16x16x32_bf16 v[144:147], v[220:223], v[136:139], v[144:147]\n"
"ds_read_b128 v[136:139], v4 offset:24576\n"
"v_mfma_f32_16x16x32_bf16 v[144:147], v[222:225], v[24:27], v[144:147]\n"
"ds_write_addtid_b32 v156 offset:58368\n"
"v_cvt_pk_bf16_f32 v157, v158, v159\n"
"ds_read_b128 v[140:143], v5 offset:24576\n"
"s_waitcnt lgkmcnt(5)\n"
"v_mfma_f32_16x16x32_bf16 v[132:135], v[124:127], v[152:155], v[132:135]\n"
"v_mfma_f32_16x16x32_bf16 v[160:163], v[128:131], v[152:155], v[160:163]\n"
"v_exp_f32 v148, v148\n"
"v_exp_f32 v149, v149\n"
"s_nop 0\n"
"v_cvt_pk_bf16_f32 v148, v148, v149\n"
"v_bfe_u32 v177, v6, 2, 6\n"
"ds_write_addtid_b32 v157 offset:58624\n"
"s_waitcnt lgkmcnt(4)\n"
"v_mfma_f32_16x16x32_bf16 v[164:167], v[226:229], v[152:155], v[164:167]\n"
"s_waitcnt lgkmcnt(3)\n"
"v_mul_f32 v132, s60, v132\n"
"v_mfma_f32_16x16x32_bf16 v[226:229], v[8:11], v[136:139], 0\n"
"ds_read_b32 v177, v177 offset:1600\n"
"v_mul_f32 v163, s60, v163\n"
"v_mfma_f32_16x16x32_bf16 v[152:155], v[12:15], v[136:139], 0\n"
"ds_write_addtid_b32 v148 offset:58880\n"
"v_mul_f32 v162, s60, v162\n"
"v_mul_f32 v133, s60, v133\n"
"v_mfma_f32_16x16x32_bf16 v[148:151], v[16:19], v[136:139], 0\n"
"s_waitcnt lgkmcnt(3)\n"
"v_exp_f32 v144, v144\n"
"v_exp_f32 v145, v145\n"
"v_mfma_f32_16x16x32_bf16 v[226:229], v[20:23], v[140:143], v[226:229]\n"
"ds_read_b128 v[136:139], v4 offset:26624\n"
"v_mul_f32 v160, s60, v160\n"
"v_mfma_f32_16x16x32_bf16 v[152:155], v[28:31], v[140:143], v[152:155]\n"
"v_cvt_pk_bf16_f32 v132, v132, v133\n"
"v_mul_f32 v161, s60, v161\n"
"v_cvt_pk_bf16_f32 v144, v144, v145\n"
"v_mul_f32 v164, s60, v164\n"
"s_waitcnt lgkmcnt(2)\n"
"v_add_u32_sdwa v176, v177, v7 dst_sel:DWORD dst_unused:UNUSED_PAD src0_sel:WORD_0 src1_sel:DWORD\n"
"v_mul_f32 v135, s60, v135\n"
"v_mfma_f32_16x16x32_bf16 v[148:151], v[32:35], v[140:143], v[148:151]\n"
"ds_write_addtid_b32 v144 offset:57344\n"
"v_mul_f32 v134, s60, v134\n"
"v_cvt_pk_bf16_f32 v133, v134, v135\n"
"v_cvt_pk_bf16_f32 v134, v160, v161\n"
"v_cvt_pk_bf16_f32 v135, v162, v163\n"
"s_waitcnt lgkmcnt(1)\n"
"v_mfma_f32_16x16x32_bf16 v[226:229], v[36:39], v[136:139], v[226:229]\n"
"v_min_u32 v177, s73, v176\n"
"ds_read_b128 v[156:159], v177 offset:2704\n"
"ds_read_b128 v[140:143], v5 offset:26624\n"
"v_mul_f32 v167, s60, v167\n"
"v_mul_f32 v166, s60, v166\n"
"v_mfma_f32_16x16x32_bf16 v[152:155], v[40:43], v[136:139], v[152:155]\n"
"s_waitcnt lgkmcnt(2)\n"
"s_barrier\n"
"v_mul_f32 v165, s60, v165\n"
"v_cvt_pk_bf16_f32 v26, v164, v165\n"
"v_cvt_pk_bf16_f32 v27, v166, v167\n"
"s_waitcnt lgkmcnt(1)\n"
"v_mfma_f32_16x16x32_bf16 v[156:159], v[184:187], v[132:135], v[156:159]\n"
"v_mfma_f32_16x16x32_bf16 v[156:159], v[186:189], v[24:27], v[156:159]\n"
"ds_read_b128 v[144:147], v177 offset:2720\n"
"v_mfma_f32_16x16x32_bf16 v[148:151], v[44:47], v[136:139], v[148:151]\n"
"s_waitcnt lgkmcnt(1)\n"
"ds_read_b128 v[136:139], v4 offset:28672\n"
"v_mfma_f32_16x16x32_bf16 v[226:229], v[48:51], v[140:143], v[226:229]\n"
"v_mfma_f32_16x16x32_bf16 v[152:155], v[52:55], v[140:143], v[152:155]\n"
"v_mfma_f32_16x16x32_bf16 v[148:151], v[56:59], v[140:143], v[148:151]\n"
"s_waitcnt lgkmcnt(1)\n"
"v_mfma_f32_16x16x32_bf16 v[144:147], v[190:193], v[132:135], v[144:147]\n"
"s_add_u32 s68, s50, 4294965632\n"
"v_exp_f32 v156, v156\n"
"v_min_u32 v175, s68, v176\n"
"ds_read_b128 v[140:143], v175 offset:4048\n"
"v_mfma_f32_16x16x32_bf16 v[144:147], v[192:195], v[24:27], v[144:147]\n"
"s_waitcnt lgkmcnt(1)\n"
"v_exp_f32 v157, v157\n"
"v_exp_f32 v158, v158\n"
"v_mfma_f32_16x16x32_bf16 v[226:229], v[60:63], v[136:139], v[226:229]\n"
"ds_read_b128 v[160:163], v5 offset:28672\n"
"v_mfma_f32_16x16x32_bf16 v[152:155], v[64:67], v[136:139], v[152:155]\n"
"v_cvt_pk_bf16_f32 v156, v156, v157\n"
"ds_write_addtid_b32 v156 offset:53248\n"
"v_mfma_f32_16x16x32_bf16 v[148:151], v[68:71], v[136:139], v[148:151]\n"
"s_waitcnt lgkmcnt(2)\n"
"v_exp_f32 v159, v159\n"
"v_mfma_f32_16x16x32_bf16 v[140:143], v[214:217], v[132:135], v[140:143]\n"
"ds_read_b128 v[136:139], v175 offset:4064\n"
"v_cvt_pk_bf16_f32 v157, v158, v159\n"
"v_mfma_f32_16x16x32_bf16 v[140:143], v[216:219], v[24:27], v[140:143]\n"
"s_waitcnt lgkmcnt(2)\n"
"v_exp_f32 v144, v144\n"
"v_mfma_f32_16x16x32_bf16 v[226:229], v[72:75], v[160:163], v[226:229]\n"
"ds_write_addtid_b32 v157 offset:53504\n"
"ds_read_b128 v[156:159], v4 offset:18432\n"
"v_mfma_f32_16x16x32_bf16 v[152:155], v[76:79], v[160:163], v[152:155]\n"
"s_add_u32 s72, s50, 4294965936\n"
"v_exp_f32 v145, v145\n"
"v_exp_f32 v146, v146\n"
"v_exp_f32 v147, v147\n"
"v_mfma_f32_16x16x32_bf16 v[148:151], v[80:83], v[160:163], v[148:151]\n"
"v_cvt_pk_bf16_f32 v226, v226, v227\n"
"v_cvt_pk_bf16_f32 v144, v144, v145\n"
"ds_write_addtid_b32 v144 offset:53760\n"
"v_cvt_pk_bf16_f32 v145, v146, v147\n"
"ds_write_addtid_b32 v145 offset:54016\n"
"v_cvt_pk_bf16_f32 v227, v228, v229\n"
"s_waitcnt lgkmcnt(4)\n"
"v_mfma_f32_16x16x32_bf16 v[136:139], v[220:223], v[132:135], v[136:139]\n"
"v_cvt_pk_bf16_f32 v229, v154, v155\n"
"v_cvt_pk_bf16_f32 v228, v152, v153\n"
"v_cvt_pk_bf16_f32 v230, v148, v149\n"
"v_mfma_f32_16x16x32_bf16 v[136:139], v[222:225], v[24:27], v[136:139]\n"
"s_waitcnt lgkmcnt(2)\n"
"ds_read_b128 v[144:147], v177 offset:2736\n"
"v_cvt_pk_bf16_f32 v231, v150, v151\n"
"v_mfma_f32_16x16x32_bf16 v[148:151], v[84:87], v[156:159], 0\n"
"v_exp_f32 v140, v140\n"
"v_mfma_f32_16x16x32_bf16 v[160:163], v[88:91], v[156:159], 0\n"
"v_exp_f32 v141, v141\n"
"ds_read_b128 v[152:155], v5 offset:18432\n"
"v_mfma_f32_16x16x32_bf16 v[164:167], v[92:95], v[156:159], 0\n"
"v_cvt_pk_bf16_f32 v140, v140, v141\n"
"v_exp_f32 v142, v142\n"
"ds_write_addtid_b32 v140 offset:54272\n"
"v_max_u32 v174, s72, v176\n"
"s_waitcnt lgkmcnt(2)\n"
"v_mfma_f32_16x16x32_bf16 v[144:147], v[196:199], v[132:135], v[144:147]\n"
"ds_read_b128 v[156:159], v6 offset:5120\n"
"v_mfma_f32_16x16x32_bf16 v[144:147], v[198:201], v[24:27], v[144:147]\n"
"v_exp_f32 v143, v143\n"
"s_nop 0\n"
"v_cvt_pk_bf16_f32 v141, v142, v143\n"
"ds_write_addtid_b32 v141 offset:54528\n"
"ds_read_b128 v[168:171], v174 offset:1360\n"
"ds_read_b128 v[140:143], v4 offset:20480\n"
"s_waitcnt lgkmcnt(5)\n"
"v_mfma_f32_16x16x32_bf16 v[148:151], v[96:99], v[152:155], v[148:151]\n"
"v_exp_f32 v136, v136\n"
"v_mfma_f32_16x16x32_bf16 v[160:163], v[100:103], v[152:155], v[160:163]\n"
"s_waitcnt lgkmcnt(3)\n"
"v_exp_f32 v137, v137\n"
"v_exp_f32 v138, v138\n"
"v_mfma_f32_16x16x32_bf16 v[164:167], v[156:159], v[152:155], v[164:167]\n"
"v_exp_f32 v139, v139\n"
"v_cvt_pk_bf16_f32 v136, v136, v137\n"
"ds_write_addtid_b32 v136 offset:54784\n"
"ds_read_b128 v[152:155], v6 offset:6144\n"
"s_waitcnt lgkmcnt(3)\n"
"v_mfma_f32_16x16x32_bf16 v[168:171], v[232:235], v[132:135], v[168:171]\n"
"v_cvt_pk_bf16_f32 v137, v138, v139\n"
"ds_read_b128 v[156:159], v174 offset:1376\n"
"v_mfma_f32_16x16x32_bf16 v[168:171], v[234:237], v[24:27], v[168:171]\n"
"s_waitcnt lgkmcnt(3)\n"
"v_exp_f32 v144, v144\n"
"v_exp_f32 v145, v145\n"
"v_mfma_f32_16x16x32_bf16 v[148:151], v[104:107], v[140:143], v[148:151]\n"
"ds_write_addtid_b32 v137 offset:55040\n"
"v_exp_f32 v146, v146\n"
"v_mfma_f32_16x16x32_bf16 v[160:163], v[108:111], v[140:143], v[160:163]\n"
"s_waitcnt lgkmcnt(2)\n"
"ds_read_b128 v[232:235], v5 offset:20480\n"
"v_mfma_f32_16x16x32_bf16 v[164:167], v[152:155], v[140:143], v[164:167]\n"
"s_waitcnt lgkmcnt(2)\n"
"ds_read_b128 v[136:139], v6 offset:7168\n"
"v_cvt_pk_bf16_f32 v144, v144, v145\n"
"v_mfma_f32_16x16x32_bf16 v[156:159], v[238:241], v[132:135], v[156:159]\n"
"ds_write_addtid_b32 v144 offset:33792\n"
"v_exp_f32 v147, v147\n"
"s_nop 0\n"
"v_cvt_pk_bf16_f32 v145, v146, v147\n"
"v_mfma_f32_16x16x32_bf16 v[156:159], v[240:243], v[24:27], v[156:159]\n"
"ds_write_addtid_b32 v145 offset:34048\n"
"s_waitcnt lgkmcnt(3)\n"
"v_mfma_f32_16x16x32_bf16 v[148:151], v[112:115], v[232:235], v[148:151]\n"
"v_exp_f32 v168, v168\n"
"ds_read_b128 v[144:147], v4 offset:22528\n"
"ds_read_b128 v[152:155], v5 offset:22528\n"
"v_mfma_f32_16x16x32_bf16 v[160:163], v[116:119], v[232:235], v[160:163]\n"
"s_waitcnt lgkmcnt(4)\n"
"v_exp_f32 v169, v169\n"
"v_exp_f32 v170, v170\n"
"v_mfma_f32_16x16x32_bf16 v[164:167], v[136:139], v[232:235], v[164:167]\n"
"s_waitcnt lgkmcnt(2)\n"
"ds_read_b128 v[140:143], v174 offset:1392\n"
"ds_read_b128 v[232:235], v6 offset:8192\n"
"s_barrier\n"
"ds_read_b128 v[136:139], v175 offset:4080\n"
"v_cvt_pk_bf16_f32 v168, v168, v169\n"
"v_exp_f32 v171, v171\n"
"s_nop 0\n"
"v_cvt_pk_bf16_f32 v169, v170, v171\n"
"s_waitcnt lgkmcnt(2)\n"
"v_mfma_f32_16x16x32_bf16 v[140:143], v[244:247], v[132:135], v[140:143]\n"
"v_mfma_f32_16x16x32_bf16 v[140:143], v[246:249], v[24:27], v[140:143]\n"
"ds_write_addtid_b32 v168 offset:57856\n"
"ds_write_addtid_b32 v169 offset:58112\n"
"v_mfma_f32_16x16x32_bf16 v[148:151], v[0:3], v[144:147], v[148:151]\n"
"v_exp_f32 v156, v156\n"
"v_exp_f32 v157, v157\n"
"v_mfma_f32_16x16x32_bf16 v[160:163], v[120:123], v[144:147], v[160:163]\n"
"s_waitcnt lgkmcnt(3)\n"
"v_exp_f32 v158, v158\n"
"v_exp_f32 v159, v159\n"
"v_mfma_f32_16x16x32_bf16 v[164:167], v[232:235], v[144:147], v[164:167]\n"
"s_waitcnt lgkmcnt(2)\n"
"ds_read_b128 v[232:235], v6 offset:9216\n"
"v_cvt_pk_bf16_f32 v156, v156, v157\n"
"v_mfma_f32_16x16x32_bf16 v[136:139], v[226:229], v[132:135], v[136:139]\n"
"ds_write_addtid_b32 v156 offset:58368\n"
"ds_read_b128 v[132:135], v4 offset:12288\n"
"v_mfma_f32_16x16x32_bf16 v[136:139], v[228:231], v[24:27], v[136:139]\n"
"v_cvt_pk_bf16_f32 v157, v158, v159\n"
"v_bfe_u32 v177, v6, 2, 6\n"
"ds_read_b32 v177, v177 offset:1664\n"
"v_mfma_f32_16x16x32_bf16 v[148:151], v[124:127], v[152:155], v[148:151]\n"
"ds_read_b128 v[144:147], v5 offset:12288\n"
"ds_write_addtid_b32 v157 offset:58624\n"
"v_exp_f32 v140, v140\n"
"v_exp_f32 v141, v141\n"
"v_exp_f32 v142, v142\n"
"v_cvt_pk_bf16_f32 v140, v140, v141\n"
"v_mfma_f32_16x16x32_bf16 v[160:163], v[128:131], v[152:155], v[160:163]\n"
"s_waitcnt lgkmcnt(5)\n"
"v_exp_f32 v143, v143\n"
"v_mfma_f32_16x16x32_bf16 v[164:167], v[232:235], v[152:155], v[164:167]\n"
"s_waitcnt lgkmcnt(2)\n"
"v_add_u32_sdwa v176, v177, v7 dst_sel:DWORD dst_unused:UNUSED_PAD src0_sel:WORD_0 src1_sel:DWORD\n"
"v_mfma_f32_16x16x32_bf16 v[232:235], v[8:11], v[132:135], 0\n"
"ds_write_addtid_b32 v140 offset:58880\n"
"v_cvt_pk_bf16_f32 v141, v142, v143\n"
"v_mfma_f32_16x16x32_bf16 v[152:155], v[12:15], v[132:135], 0\n"
"s_add_u32 s69, s50, 4294965920\n"
"v_mul_f32 v148, s60, v148\n"
"v_max_u32 v175, s69, v176\n"
"ds_write_addtid_b32 v141 offset:59136\n"
"v_mfma_f32_16x16x32_bf16 v[140:143], v[16:19], v[132:135], 0\n"
"s_waitcnt lgkmcnt(3)\n"
"ds_read_b128 v[156:159], v175 offset:1392\n"
"ds_read_b128 v[132:135], v4 offset:14336\n"
"v_mfma_f32_16x16x32_bf16 v[232:235], v[20:23], v[144:147], v[232:235]\n"
"v_exp_f32 v136, v136\n"
"v_exp_f32 v137, v137\n"
"v_mul_f32 v149, s60, v149\n"
"v_mul_f32 v151, s60, v151\n"
"v_cvt_pk_bf16_f32 v136, v136, v137\n"
"v_cvt_pk_bf16_f32 v148, v148, v149\n"
"v_exp_f32 v138, v138\n"
"v_mfma_f32_16x16x32_bf16 v[152:155], v[28:31], v[144:147], v[152:155]\n"
"ds_write_addtid_b32 v136 offset:57344\n"
"v_mul_f32 v162, s60, v162\n"
"v_exp_f32 v139, v139\n"
"v_mfma_f32_16x16x32_bf16 v[140:143], v[32:35], v[144:147], v[140:143]\n"
"s_waitcnt lgkmcnt(1)\n"
"v_mul_f32 v166, s60, v166\n"
"v_cvt_pk_bf16_f32 v137, v138, v139\n"
"v_mfma_f32_16x16x32_bf16 v[232:235], v[36:39], v[132:135], v[232:235]\n"
"ds_write_addtid_b32 v137 offset:57600\n"
"v_mul_f32 v161, s60, v161\n"
"v_mul_f32 v150, s60, v150\n"
"v_mul_f32 v163, s60, v163\n"
"v_mfma_f32_16x16x32_bf16 v[152:155], v[40:43], v[132:135], v[152:155]\n"
"ds_read_b128 v[136:139], v5 offset:14336\n"
"v_cvt_pk_bf16_f32 v149, v150, v151\n"
"v_mul_f32 v164, s60, v164\n"
"v_mul_f32 v160, s60, v160\n"
"v_cvt_pk_bf16_f32 v151, v162, v163\n"
"v_add_u32_sdwa v177, sext(v177), v176 dst_sel:DWORD dst_unused:UNUSED_PAD src0_sel:WORD_1 src1_sel:DWORD\n"
"v_cvt_pk_bf16_f32 v150, v160, v161\n"
"v_mul_f32 v167, s60, v167\n"
"v_mul_f32 v165, s60, v165\n"
"v_cvt_pk_bf16_f32 v27, v166, v167\n"
"v_mfma_f32_16x16x32_bf16 v[156:159], v[244:247], v[148:151], v[156:159]\n"
"s_waitcnt lgkmcnt(1)\n"
"s_barrier\n"
"v_max_u32 v174, s69, v177\n"
"v_cvt_pk_bf16_f32 v26, v164, v165\n"
"s_nop 1\n"
"v_mfma_f32_16x16x32_bf16 v[156:159], v[246:249], v[24:27], v[156:159]\n"
"ds_read_b128 v[244:247], v174 offset:1408\n"
"v_mfma_f32_16x16x32_bf16 v[140:143], v[44:47], v[132:135], v[140:143]\n"
"s_waitcnt lgkmcnt(1)\n"
"ds_read_b128 v[132:135], v4 offset:16384\n"
"v_mfma_f32_16x16x32_bf16 v[232:235], v[48:51], v[136:139], v[232:235]\n"
"s_add_u32 s70, s50, 4294966960\n"
"v_min_u32 v174, s70, v176\n"
"v_mfma_f32_16x16x32_bf16 v[152:155], v[52:55], v[136:139], v[152:155]\n"
"v_mfma_f32_16x16x32_bf16 v[140:143], v[56:59], v[136:139], v[140:143]\n"
"s_waitcnt lgkmcnt(1)\n"
"v_mfma_f32_16x16x32_bf16 v[244:247], v[250:253], v[148:151], v[244:247]\n"
"ds_read_b128 v[248:251], v174 offset:2720\n"
"v_mfma_f32_16x16x32_bf16 v[244:247], v[252:255], v[24:27], v[244:247]\n"
"s_waitcnt lgkmcnt(1)\n"
"v_exp_f32 v156, v156\n"
"v_exp_f32 v157, v157\n"
"v_mfma_f32_16x16x32_bf16 v[232:235], v[60:63], v[132:135], v[232:235]\n"
"ds_read_b128 v[252:255], v5 offset:16384\n"
"ds_read_b128 v[136:139], v4 offset:36864\n"
"v_mfma_f32_16x16x32_bf16 v[152:155], v[64:67], v[132:135], v[152:155]\n"
"v_cvt_pk_bf16_f32 v156, v156, v157\n"
"ds_write_addtid_b32 v156 offset:55296\n"
"v_mfma_f32_16x16x32_bf16 v[140:143], v[68:71], v[132:135], v[140:143]\n"
"s_waitcnt lgkmcnt(3)\n"
"v_exp_f32 v158, v158\n"
"v_mfma_f32_16x16x32_bf16 v[248:251], v[190:193], v[148:151], v[248:251]\n"
"ds_read_b128 v[132:135], v174 offset:2736\n"
"v_mfma_f32_16x16x32_bf16 v[248:251], v[192:195], v[24:27], v[248:251]\n"
"s_waitcnt lgkmcnt(3)\n"
"v_exp_f32 v159, v159\n"
"v_exp_f32 v244, v244\n"
"v_mfma_f32_16x16x32_bf16 v[232:235], v[72:75], v[252:255], v[232:235]\n"
"v_cvt_pk_bf16_f32 v157, v158, v159\n"
"ds_write_addtid_b32 v157 offset:55552\n"
"v_mfma_f32_16x16x32_bf16 v[152:155], v[76:79], v[252:255], v[152:155]\n"
"v_exp_f32 v245, v245\n"
"v_exp_f32 v246, v246\n"
"v_exp_f32 v247, v247\n"
"v_mfma_f32_16x16x32_bf16 v[140:143], v[80:83], v[252:255], v[140:143]\n"
"v_cvt_pk_bf16_f32 v244, v244, v245\n"
"v_cvt_pk_bf16_f32 v245, v246, v247\n"
"v_min_u32 v174, s70, v177\n"
"ds_write_addtid_b32 v244 offset:55808\n"
"s_waitcnt lgkmcnt(2)\n"
"v_mfma_f32_16x16x32_bf16 v[132:135], v[196:199], v[148:151], v[132:135]\n"
"ds_write_addtid_b32 v245 offset:56064\n"
"v_cvt_pk_bf16_f32 v232, v232, v233\n"
"v_cvt_pk_bf16_f32 v233, v234, v235\n"
"v_mfma_f32_16x16x32_bf16 v[132:135], v[198:201], v[24:27], v[132:135]\n"
"ds_read_b128 v[244:247], v174 offset:2752\n"
"v_cvt_pk_bf16_f32 v237, v142, v143\n"
"v_cvt_pk_bf16_f32 v236, v140, v141\n"
"v_cvt_pk_bf16_f32 v234, v152, v153\n"
"v_mfma_f32_16x16x32_bf16 v[252:255], v[84:87], v[136:139], 0\n"
"v_exp_f32 v250, v250\n"
"v_cvt_pk_bf16_f32 v235, v154, v155\n"
"v_mfma_f32_16x16x32_bf16 v[144:147], v[88:91], v[136:139], 0\n"
"v_exp_f32 v251, v251\n"
"v_exp_f32 v132, v132\n"
"ds_read_b128 v[140:143], v5 offset:36864\n"
"v_mfma_f32_16x16x32_bf16 v[152:155], v[92:95], v[136:139], 0\n"
"s_waitcnt lgkmcnt(1)\n"
"ds_read_b128 v[136:139], v6 offset:5120\n"
"v_mfma_f32_16x16x32_bf16 v[244:247], v[202:205], v[148:151], v[244:247]\n"
"v_cvt_pk_bf16_f32 v249, v250, v251\n"
"ds_write_addtid_b32 v249 offset:56576\n"
"ds_read_b128 v[248:251], v175 offset:1376\n"
"v_mfma_f32_16x16x32_bf16 v[244:247], v[204:207], v[24:27], v[244:247]\n"
"s_waitcnt lgkmcnt(3)\n"
"v_mfma_f32_16x16x32_bf16 v[252:255], v[96:99], v[140:143], v[252:255]\n"
"v_exp_f32 v133, v133\n"
"ds_read_b128 v[156:159], v4 offset:38912\n"
"v_mfma_f32_16x16x32_bf16 v[144:147], v[100:103], v[140:143], v[144:147]\n"
"s_add_u32 s71, s50, 4294965616\n"
"v_cvt_pk_bf16_f32 v132, v132, v133\n"
"ds_write_addtid_b32 v132 offset:56832\n"
"s_waitcnt lgkmcnt(4)\n"
"v_mfma_f32_16x16x32_bf16 v[152:155], v[136:139], v[140:143], v[152:155]\n"
"s_waitcnt lgkmcnt(2)\n"
"v_min_u32 v132, s71, v176\n"
"ds_read_b128 v[140:143], v132 offset:4064\n"
"ds_read_b128 v[136:139], v6 offset:6144\n"
"v_mfma_f32_16x16x32_bf16 v[248:251], v[238:241], v[148:151], v[248:251]\n"
"v_exp_f32 v134, v134\n"
"v_exp_f32 v135, v135\n"
"v_exp_f32 v244, v244\n"
"v_exp_f32 v245, v245\n"
"v_cvt_pk_bf16_f32 v133, v134, v135\n"
"ds_write_addtid_b32 v133 offset:57088\n"
"v_mfma_f32_16x16x32_bf16 v[248:251], v[240:243], v[24:27], v[248:251]\n"
"s_waitcnt lgkmcnt(4)\n"
"v_mfma_f32_16x16x32_bf16 v[252:255], v[104:107], v[156:159], v[252:255]\n"
"ds_read_b128 v[238:241], v5 offset:38912\n"
"v_mfma_f32_16x16x32_bf16 v[144:147], v[108:111], v[156:159], v[144:147]\n"
"s_waitcnt lgkmcnt(2)\n"
"v_cvt_pk_bf16_f32 v244, v244, v245\n"
"v_mfma_f32_16x16x32_bf16 v[152:155], v[136:139], v[156:159], v[152:155]\n"
"ds_read_b128 v[156:159], v132 offset:4080\n"
"v_mfma_f32_16x16x32_bf16 v[140:143], v[220:223], v[148:151], v[140:143]\n"
"v_exp_f32 v246, v246\n"
"v_exp_f32 v247, v247\n"
"v_mfma_f32_16x16x32_bf16 v[140:143], v[222:225], v[24:27], v[140:143]\n"
"s_waitcnt lgkmcnt(1)\n"
"ds_read_b128 v[136:139], v6 offset:7168\n"
"v_cvt_pk_bf16_f32 v245, v246, v247\n"
"v_mfma_f32_16x16x32_bf16 v[252:255], v[112:115], v[238:241], v[252:255]\n"
"ds_write_addtid_b32 v244 offset:34304\n"
"ds_write_addtid_b32 v245 offset:34560\n"
"v_mfma_f32_16x16x32_bf16 v[144:147], v[116:119], v[238:241], v[144:147]\n"
"s_waitcnt lgkmcnt(2)\n"
"v_min_u32 v176, s71, v177\n"
"v_mfma_f32_16x16x32_bf16 v[152:155], v[136:139], v[238:241], v[152:155]\n"
"v_exp_f32 v250, v250\n"
"v_exp_f32 v251, v251\n"
"ds_read_b128 v[242:245], v4 offset:40960\n"
"ds_read_b128 v[238:241], v6 offset:8192\n"
"s_add_u32 s72, s50, 4294964592\n"
"v_cvt_pk_bf16_f32 v249, v250, v251\n"
"v_bfe_u32 v177, v6, 2, 6\n"
"s_waitcnt lgkmcnt(2)\n"
"s_barrier\n"
"ds_write_addtid_b32 v249 offset:59136\n"
"v_mfma_f32_16x16x32_bf16 v[156:159], v[226:229], v[148:151], v[156:159]\n"
"ds_read_b128 v[246:249], v176 offset:4096\n"
"v_mfma_f32_16x16x32_bf16 v[156:159], v[228:231], v[24:27], v[156:159]\n"
"s_waitcnt lgkmcnt(3)\n"
"ds_read_b128 v[132:135], v5 offset:40960\n"
"v_mfma_f32_16x16x32_bf16 v[252:255], v[0:3], v[242:245], v[252:255]\n"
"v_mfma_f32_16x16x32_bf16 v[144:147], v[120:123], v[242:245], v[144:147]\n"
"s_waitcnt lgkmcnt(3)\n"
"v_exp_f32 v142, v142\n"
"v_exp_f32 v143, v143\n"
"v_mfma_f32_16x16x32_bf16 v[152:155], v[238:241], v[242:245], v[152:155]\n"
"s_waitcnt lgkmcnt(1)\n"
"ds_read_b128 v[238:241], v6 offset:9216\n"
"v_mfma_f32_16x16x32_bf16 v[246:249], v[232:235], v[148:151], v[246:249]\n"
"ds_read_b32 v177, v177 offset:1728\n"
"v_cvt_pk_bf16_f32 v141, v142, v143\n"
"v_exp_f32 v156, v156\n"
"ds_read_b128 v[242:245], v4 offset:43008\n"
"v_mfma_f32_16x16x32_bf16 v[246:249], v[234:237], v[24:27], v[246:249]\n"
"s_waitcnt lgkmcnt(3)\n"
"v_exp_f32 v157, v157\n"
"v_exp_f32 v158, v158\n"
"v_mfma_f32_16x16x32_bf16 v[252:255], v[124:127], v[132:135], v[252:255]\n"
"v_cvt_pk_bf16_f32 v156, v156, v157\n"
"v_mfma_f32_16x16x32_bf16 v[144:147], v[128:131], v[132:135], v[144:147]\n"
"v_exp_f32 v159, v159\n"
"ds_write_addtid_b32 v141 offset:57600\n"
"s_waitcnt lgkmcnt(3)\n"
"v_mfma_f32_16x16x32_bf16 v[152:155], v[238:241], v[132:135], v[152:155]\n"
"s_waitcnt lgkmcnt(2)\n"
"ds_read_b128 v[136:139], v5 offset:43008\n"
"v_add_u32_sdwa v176, v177, v7 dst_sel:DWORD dst_unused:UNUSED_PAD src0_sel:WORD_0 src1_sel:DWORD\n"
"v_mul_f32 v254, s60, v254\n"
"s_waitcnt lgkmcnt(2)\n"
"v_mfma_f32_16x16x32_bf16 v[238:241], v[8:11], v[242:245], 0\n"
"ds_write_addtid_b32 v156 offset:57856\n"
"v_cvt_pk_bf16_f32 v157, v158, v159\n"
"v_mul_f32 v253, s60, v253\n"
"v_mfma_f32_16x16x32_bf16 v[132:135], v[12:15], v[242:245], 0\n"
"v_mfma_f32_16x16x32_bf16 v[140:143], v[16:19], v[242:245], 0\n"
"ds_write_addtid_b32 v157 offset:58112\n"
"v_max_u32 v251, s72, v176\n"
"ds_read_b128 v[148:151], v251 offset:2704\n"
"v_mul_f32 v147, s60, v147\n"
"s_waitcnt lgkmcnt(3)\n"
"v_mfma_f32_16x16x32_bf16 v[238:241], v[20:23], v[136:139], v[238:241]\n"
"v_exp_f32 v246, v246\n"
"v_exp_f32 v247, v247\n"
"ds_read_b128 v[242:245], v4 offset:45056\n"
"v_mul_f32 v146, s60, v146\n"
"v_mfma_f32_16x16x32_bf16 v[132:135], v[28:31], v[136:139], v[132:135]\n"
"v_exp_f32 v248, v248\n"
"v_mul_f32 v252, s60, v252\n"
"v_mul_f32 v153, s60, v153\n"
"v_exp_f32 v249, v249\n"
"v_mfma_f32_16x16x32_bf16 v[140:143], v[32:35], v[136:139], v[140:143]\n"
"ds_read_b128 v[136:139], v5 offset:45056\n"
"v_mul_f32 v144, s60, v144\n"
"v_cvt_pk_bf16_f32 v252, v252, v253\n"
"v_cvt_pk_bf16_f32 v246, v246, v247\n"
"v_cvt_pk_bf16_f32 v247, v248, v249\n"
"v_mul_f32 v255, s60, v255\n"
"v_mul_f32 v154, s60, v154\n"
"v_mul_f32 v155, s60, v155\n"
"s_waitcnt lgkmcnt(1)\n"
"v_mfma_f32_16x16x32_bf16 v[238:241], v[36:39], v[242:245], v[238:241]\n"
"ds_write_addtid_b32 v246 offset:58368\n"
"v_mul_f32 v145, s60, v145\n"
"ds_write_addtid_b32 v247 offset:58624\n"
"v_cvt_pk_bf16_f32 v253, v254, v255\n"
"v_mfma_f32_16x16x32_bf16 v[132:135], v[40:43], v[242:245], v[132:135]\n"
"v_cvt_pk_bf16_f32 v254, v144, v145\n"
"v_cvt_pk_bf16_f32 v27, v154, v155\n"
"v_cvt_pk_bf16_f32 v255, v146, v147\n"
"v_mul_f32 v152, s60, v152\n"
"v_cvt_pk_bf16_f32 v26, v152, v153\n"
"v_mfma_f32_16x16x32_bf16 v[148:151], v[184:187], v[252:255], v[148:151]\n"
"ds_read_b128 v[246:249], v4 offset:47104\n"
"v_mfma_f32_16x16x32_bf16 v[148:151], v[186:189], v[24:27], v[148:151]\n"
"s_waitcnt lgkmcnt(1)\n"
"s_barrier\n"
"v_mfma_f32_16x16x32_bf16 v[140:143], v[44:47], v[242:245], v[140:143]\n"
"ds_read_b128 v[144:147], v251 offset:2720\n"
"v_add_u32_sdwa v177, sext(v177), v176 dst_sel:DWORD dst_unused:UNUSED_PAD src0_sel:WORD_1 src1_sel:DWORD\n"
"v_mfma_f32_16x16x32_bf16 v[238:241], v[48:51], v[136:139], v[238:241]\n"
"v_mfma_f32_16x16x32_bf16 v[132:135], v[52:55], v[136:139], v[132:135]\n"
"v_mfma_f32_16x16x32_bf16 v[140:143], v[56:59], v[136:139], v[140:143]\n"
"s_waitcnt lgkmcnt(1)\n"
"v_mfma_f32_16x16x32_bf16 v[238:241], v[60:63], v[246:249], v[238:241]\n"
"ds_read_b128 v[242:245], v5 offset:47104\n"
"v_mfma_f32_16x16x32_bf16 v[132:135], v[64:67], v[246:249], v[132:135]\n"
"s_waitcnt lgkmcnt(1)\n"
"ds_read_b128 v[136:139], v4 offset:49152\n"
"v_mfma_f32_16x16x32_bf16 v[144:147], v[190:193], v[252:255], v[144:147]\n"
"v_mfma_f32_16x16x32_bf16 v[144:147], v[192:195], v[24:27], v[144:147]\n"
"v_exp_f32 v150, v150\n"
"v_exp_f32 v151, v151\n"
"v_mfma_f32_16x16x32_bf16 v[140:143], v[68:71], v[246:249], v[140:143]\n"
"s_waitcnt lgkmcnt(1)\n"
"v_cvt_pk_bf16_f32 v149, v150, v151\n"
"ds_read_b128 v[152:155], v251 offset:2736\n"
"v_mfma_f32_16x16x32_bf16 v[238:241], v[72:75], v[242:245], v[238:241]\n"
"ds_write_addtid_b32 v149 offset:53504\n"
"v_mfma_f32_16x16x32_bf16 v[132:135], v[76:79], v[242:245], v[132:135]\n"
"v_exp_f32 v144, v144\n"
"v_exp_f32 v145, v145\n"
"v_mfma_f32_16x16x32_bf16 v[140:143], v[80:83], v[242:245], v[140:143]\n"
"s_waitcnt lgkmcnt(2)\n"
"ds_read_b128 v[248:251], v5 offset:49152\n"
"v_mfma_f32_16x16x32_bf16 v[244:247], v[8:11], v[136:139], 0\n"
"v_cvt_pk_bf16_f32 v238, v238, v239\n"
"v_cvt_pk_bf16_f32 v239, v240, v241\n"
"v_cvt_pk_bf16_f32 v241, v134, v135\n"
"v_cvt_pk_bf16_f32 v240, v132, v133\n"
"v_mfma_f32_16x16x32_bf16 v[148:151], v[12:15], v[136:139], 0\n"
"s_waitcnt lgkmcnt(2)\n"
"ds_read_b128 v[132:135], v4 offset:51200\n"
"v_mfma_f32_16x16x32_bf16 v[152:155], v[196:199], v[252:255], v[152:155]\n"
"v_cvt_pk_bf16_f32 v243, v142, v143\n"
"v_cvt_pk_bf16_f32 v242, v140, v141\n"
"v_mfma_f32_16x16x32_bf16 v[152:155], v[198:201], v[24:27], v[152:155]\n"
"v_exp_f32 v146, v146\n"
"v_mfma_f32_16x16x32_bf16 v[140:143], v[16:19], v[136:139], 0\n"
"s_waitcnt lgkmcnt(1)\n"
"v_max_u32 v175, s72, v177\n"
"v_mfma_f32_16x16x32_bf16 v[244:247], v[20:23], v[248:251], v[244:247]\n"
"v_exp_f32 v147, v147\n"
"v_cvt_pk_bf16_f32 v144, v144, v145\n"
"v_mfma_f32_16x16x32_bf16 v[148:151], v[28:31], v[248:251], v[148:151]\n"
"v_exp_f32 v152, v152\n"
"v_cvt_pk_bf16_f32 v145, v146, v147\n"
"v_mfma_f32_16x16x32_bf16 v[140:143], v[32:35], v[248:251], v[140:143]\n"
"s_add_u32 s73, s50, 4294965632\n"
"ds_read_b128 v[136:139], v175 offset:2752\n"
"s_waitcnt lgkmcnt(1)\n"
"v_mfma_f32_16x16x32_bf16 v[244:247], v[36:39], v[132:135], v[244:247]\n"
"ds_write_addtid_b32 v144 offset:53760\n"
"ds_write_addtid_b32 v145 offset:54016\n"
"v_mfma_f32_16x16x32_bf16 v[148:151], v[40:43], v[132:135], v[148:151]\n"
"s_waitcnt lgkmcnt(2)\n"
"ds_read_b128 v[144:147], v4 offset:53248\n"
"ds_read_b128 v[248:251], v5 offset:51200\n"
"v_mfma_f32_16x16x32_bf16 v[136:139], v[202:205], v[252:255], v[136:139]\n"
"v_exp_f32 v153, v153\n"
"v_mfma_f32_16x16x32_bf16 v[136:139], v[204:207], v[24:27], v[136:139]\n"
"v_exp_f32 v154, v154\n"
"v_cvt_pk_bf16_f32 v152, v152, v153\n"
"v_mfma_f32_16x16x32_bf16 v[140:143], v[44:47], v[132:135], v[140:143]\n"
"s_waitcnt lgkmcnt(0)\n"
"v_min_u32 v175, s73, v176\n"
"v_mfma_f32_16x16x32_bf16 v[244:247], v[48:51], v[248:251], v[244:247]\n"
"ds_read_b128 v[156:159], v175 offset:4048\n"
"v_mfma_f32_16x16x32_bf16 v[148:151], v[52:55], v[248:251], v[148:151]\n"
"ds_write_addtid_b32 v152 offset:54272\n"
"v_mfma_f32_16x16x32_bf16 v[140:143], v[56:59], v[248:251], v[140:143]\n"
"v_exp_f32 v155, v155\n"
"v_mfma_f32_16x16x32_bf16 v[244:247], v[60:63], v[144:147], v[244:247]\n"
"s_waitcnt lgkmcnt(1)\n"
"ds_read_b128 v[248:251], v5 offset:53248\n"
"v_cvt_pk_bf16_f32 v153, v154, v155\n"
"v_mfma_f32_16x16x32_bf16 v[156:159], v[214:217], v[252:255], v[156:159]\n"
"v_exp_f32 v136, v136\n"
"v_mfma_f32_16x16x32_bf16 v[156:159], v[216:219], v[24:27], v[156:159]\n"
"ds_write_addtid_b32 v153 offset:54528\n"
"ds_read_b128 v[132:135], v4 offset:55296\n"
"v_mfma_f32_16x16x32_bf16 v[148:151], v[64:67], v[144:147], v[148:151]\n"
"v_exp_f32 v137, v137\n"
"v_mfma_f32_16x16x32_bf16 v[140:143], v[68:71], v[144:147], v[140:143]\n"
"s_waitcnt lgkmcnt(2)\n"
"ds_read_b128 v[144:147], v5 offset:55296\n"
"ds_read_b128 v[152:155], v175 offset:4064\n"
"v_cvt_pk_bf16_f32 v136, v136, v137\n"
"v_mfma_f32_16x16x32_bf16 v[244:247], v[72:75], v[248:251], v[244:247]\n"
"v_exp_f32 v138, v138\n"
"v_exp_f32 v139, v139\n"
"ds_write_addtid_b32 v136 offset:54784\n"
"v_cvt_pk_bf16_f32 v137, v138, v139\n"
"v_mfma_f32_16x16x32_bf16 v[148:151], v[76:79], v[248:251], v[148:151]\n"
"ds_write_addtid_b32 v137 offset:55040\n"
"v_mfma_f32_16x16x32_bf16 v[140:143], v[80:83], v[248:251], v[140:143]\n"
"s_waitcnt lgkmcnt(4)\n"
"v_exp_f32 v158, v158\n"
"v_cvt_pk_bf16_f32 v244, v244, v245\n"
"ds_read_b128 v[160:163], v6 offset:5120\n"
"v_mfma_f32_16x16x32_bf16 v[136:139], v[84:87], v[132:135], 0\n"
"s_waitcnt lgkmcnt(1)\n"
"v_cvt_pk_bf16_f32 v245, v246, v247\n"
"s_barrier\n"
"v_mfma_f32_16x16x32_bf16 v[152:155], v[220:223], v[252:255], v[152:155]\n"
"v_cvt_pk_bf16_f32 v247, v150, v151\n"
"v_mfma_f32_16x16x32_bf16 v[152:155], v[222:225], v[24:27], v[152:155]\n"
"v_cvt_pk_bf16_f32 v246, v148, v149\n"
"v_cvt_pk_bf16_f32 v248, v140, v141\n"
"ds_read_b128 v[164:167], v175 offset:4080\n"
"v_mfma_f32_16x16x32_bf16 v[148:151], v[88:91], v[132:135], 0\n"
"v_mfma_f32_16x16x32_bf16 v[168:171], v[92:95], v[132:135], 0\n"
"ds_read_b128 v[132:135], v6 offset:6144\n"
"v_mfma_f32_16x16x32_bf16 v[136:139], v[96:99], v[144:147], v[136:139]\n"
"v_mfma_f32_16x16x32_bf16 v[148:151], v[100:103], v[144:147], v[148:151]\n"
"s_waitcnt lgkmcnt(2)\n"
"v_cvt_pk_bf16_f32 v249, v142, v143\n"
"ds_read_b128 v[140:143], v4 offset:57344\n"
"v_mfma_f32_16x16x32_bf16 v[168:171], v[160:163], v[144:147], v[168:171]\n"
"ds_read_b128 v[144:147], v5 offset:57344\n"
"v_min_u32 v176, s73, v177\n"
"v_exp_f32 v159, v159\n"
"v_exp_f32 v152, v152\n"
"v_exp_f32 v153, v153\n"
"v_cvt_pk_bf16_f32 v157, v158, v159\n"
"ds_write_addtid_b32 v157 offset:57600\n"
"s_waitcnt lgkmcnt(2)\n"
"v_mfma_f32_16x16x32_bf16 v[136:139], v[104:107], v[140:143], v[136:139]\n"
"ds_read_b128 v[160:163], v4 offset:59392\n"
"v_mfma_f32_16x16x32_bf16 v[164:167], v[226:229], v[252:255], v[164:167]\n"
"v_mfma_f32_16x16x32_bf16 v[164:167], v[228:231], v[24:27], v[164:167]\n"
"ds_read_b128 v[156:159], v6 offset:7168\n"
"v_mfma_f32_16x16x32_bf16 v[148:151], v[108:111], v[140:143], v[148:151]\n"
"ds_read_b128 v[172:175], v176 offset:4096\n"
"v_mfma_f32_16x16x32_bf16 v[168:171], v[132:135], v[140:143], v[168:171]\n"
"s_waitcnt lgkmcnt(4)\n"
"v_exp_f32 v154, v154\n"
"v_mfma_f32_16x16x32_bf16 v[136:139], v[112:115], v[144:147], v[136:139]\n"
"v_exp_f32 v155, v155\n"
"ds_read_b128 v[132:135], v6 offset:8192\n"
"v_mfma_f32_16x16x32_bf16 v[148:151], v[116:119], v[144:147], v[148:151]\n"
"s_waitcnt lgkmcnt(2)\n"
"v_mfma_f32_16x16x32_bf16 v[168:171], v[156:159], v[144:147], v[168:171]\n"
"s_add_u32 s68, s50, 4294964608\n"
"v_exp_f32 v164, v164\n"
"ds_read_b128 v[140:143], v6 offset:9216\n"
"v_mfma_f32_16x16x32_bf16 v[136:139], v[0:3], v[160:163], v[136:139]\n"
"s_waitcnt lgkmcnt(2)\n"
"v_mfma_f32_16x16x32_bf16 v[172:175], v[232:235], v[252:255], v[172:175]\n"
"ds_read_b128 v[250:253], v5 offset:59392\n"
"ds_read_b128 v[144:147], v4 offset:24576\n"
"v_cvt_pk_bf16_f32 v152, v152, v153\n"
"v_mfma_f32_16x16x32_bf16 v[172:175], v[234:237], v[24:27], v[172:175]\n"
"v_exp_f32 v165, v165\n"
"ds_write_addtid_b32 v152 offset:57856\n"
"v_mfma_f32_16x16x32_bf16 v[148:151], v[120:123], v[160:163], v[148:151]\n"
"v_cvt_pk_bf16_f32 v153, v154, v155\n"
"ds_write_addtid_b32 v153 offset:58112\n"
"s_waitcnt lgkmcnt(5)\n"
"v_mfma_f32_16x16x32_bf16 v[168:171], v[132:135], v[160:163], v[168:171]\n"
"s_waitcnt lgkmcnt(3)\n"
"v_bfe_u32 v177, v6, 2, 6\n"
"v_mfma_f32_16x16x32_bf16 v[136:139], v[124:127], v[250:253], v[136:139]\n"
"v_cvt_pk_bf16_f32 v164, v164, v165\n"
"ds_write_addtid_b32 v164 offset:58368\n"
"v_mfma_f32_16x16x32_bf16 v[148:151], v[128:131], v[250:253], v[148:151]\n"
"v_exp_f32 v166, v166\n"
"v_exp_f32 v167, v167\n"
"v_mfma_f32_16x16x32_bf16 v[168:171], v[140:143], v[250:253], v[168:171]\n"
"s_waitcnt lgkmcnt(3)\n"
"v_mfma_f32_16x16x32_bf16 v[250:253], v[8:11], v[144:147], 0\n"
"ds_read_b128 v[132:135], v5 offset:24576\n"
"v_mul_f32 v139, s60, v139\n"
"v_mul_f32 v136, s60, v136\n"
"ds_read_b32 v177, v177 offset:1792\n"
"v_mfma_f32_16x16x32_bf16 v[140:143], v[12:15], v[144:147], 0\n"
"v_exp_f32 v172, v172\n"
"v_cvt_pk_bf16_f32 v165, v166, v167\n"
"v_mfma_f32_16x16x32_bf16 v[152:155], v[16:19], v[144:147], 0\n"
"v_exp_f32 v173, v173\n"
"v_exp_f32 v174, v174\n"
"v_exp_f32 v175, v175\n"
"v_mul_f32 v137, s60, v137\n"
"v_mul_f32 v170, s60, v170\n"
"v_mul_f32 v151, s60, v151\n"
"v_cvt_pk_bf16_f32 v136, v136, v137\n"
"ds_read_b128 v[156:159], v5 offset:26624\n"
"v_mul_f32 v168, s60, v168\n"
"s_waitcnt lgkmcnt(1)\n"
"v_add_u32_sdwa v255, v177, v7 dst_sel:DWORD dst_unused:UNUSED_PAD src0_sel:WORD_0 src1_sel:DWORD\n"
"ds_write_addtid_b32 v165 offset:58624\n"
"v_cvt_pk_bf16_f32 v172, v172, v173\n"
"v_cvt_pk_bf16_f32 v173, v174, v175\n"
"ds_write_addtid_b32 v172 offset:58880\n"
"v_mfma_f32_16x16x32_bf16 v[250:253], v[20:23], v[132:135], v[250:253]\n"
"ds_read_b128 v[144:147], v4 offset:26624\n"
"v_mul_f32 v150, s60, v150\n"
"v_mul_f32 v138, s60, v138\n"
"v_mfma_f32_16x16x32_bf16 v[140:143], v[28:31], v[132:135], v[140:143]\n"
"v_max_u32 v177, s68, v255\n"
"v_mfma_f32_16x16x32_bf16 v[152:155], v[32:35], v[132:135], v[152:155]\n"
"ds_write_addtid_b32 v173 offset:59136\n"
"s_waitcnt lgkmcnt(1)\n"
"v_mfma_f32_16x16x32_bf16 v[250:253], v[36:39], v[144:147], v[250:253]\n"
"v_cvt_pk_bf16_f32 v137, v138, v139\n"
"v_mul_f32 v148, s60, v148\n"
"ds_read_b128 v[132:135], v177 offset:2688\n"
"v_mfma_f32_16x16x32_bf16 v[140:143], v[40:43], v[144:147], v[140:143]\n"
"s_waitcnt lgkmcnt(1)\n"
"s_barrier\n"
"v_cvt_pk_bf16_f32 v139, v150, v151\n"
"v_mul_f32 v171, s60, v171\n"
"v_mul_f32 v149, s60, v149\n"
"v_cvt_pk_bf16_f32 v138, v148, v149\n"
"v_cvt_pk_bf16_f32 v27, v170, v171\n"
"s_waitcnt lgkmcnt(0)\n"
"v_mfma_f32_16x16x32_bf16 v[132:135], v[178:181], v[136:139], v[132:135]\n"
"v_mul_f32 v169, s60, v169\n"
"v_cvt_pk_bf16_f32 v26, v168, v169\n"
"s_nop 1\n"
"v_mfma_f32_16x16x32_bf16 v[132:135], v[180:183], v[24:27], v[132:135]\n"
"s_add_u32 s69, s50, 4294963264\n"
"v_max_u32 v176, s69, v255\n"
"ds_read_b128 v[178:181], v176 offset:4032\n"
"v_mfma_f32_16x16x32_bf16 v[152:155], v[44:47], v[144:147], v[152:155]\n"
"ds_read_b128 v[144:147], v4 offset:28672\n"
"v_mfma_f32_16x16x32_bf16 v[250:253], v[48:51], v[156:159], v[250:253]\n"
"ds_read_b128 v[148:151], v177 offset:2704\n"
"v_mfma_f32_16x16x32_bf16 v[140:143], v[52:55], v[156:159], v[140:143]\n"
"v_mfma_f32_16x16x32_bf16 v[152:155], v[56:59], v[156:159], v[152:155]\n"
"s_waitcnt lgkmcnt(2)\n"
"v_mfma_f32_16x16x32_bf16 v[178:181], v[208:211], v[136:139], v[178:181]\n"
"v_exp_f32 v132, v132\n"
"v_mfma_f32_16x16x32_bf16 v[178:181], v[210:213], v[24:27], v[178:181]\n"
"s_waitcnt lgkmcnt(1)\n"
"ds_read_b128 v[208:211], v5 offset:28672\n"
"v_mfma_f32_16x16x32_bf16 v[250:253], v[60:63], v[144:147], v[250:253]\n"
"v_mfma_f32_16x16x32_bf16 v[140:143], v[64:67], v[144:147], v[140:143]\n"
"v_exp_f32 v133, v133\n"
"v_exp_f32 v134, v134\n"
"v_exp_f32 v135, v135\n"
"v_mfma_f32_16x16x32_bf16 v[152:155], v[68:71], v[144:147], v[152:155]\n"
"s_waitcnt lgkmcnt(1)\n"
"v_cvt_pk_bf16_f32 v132, v132, v133\n"
"v_cvt_pk_bf16_f32 v133, v134, v135\n"
"v_mfma_f32_16x16x32_bf16 v[148:151], v[184:187], v[136:139], v[148:151]\n"
"ds_read_b128 v[144:147], v4 offset:30720\n"
"ds_write_addtid_b32 v132 offset:55296\n"
"ds_write_addtid_b32 v133 offset:55552\n"
"v_mfma_f32_16x16x32_bf16 v[148:151], v[186:189], v[24:27], v[148:151]\n"
"s_waitcnt lgkmcnt(3)\n"
"ds_read_b128 v[132:135], v176 offset:4048\n"
"v_mfma_f32_16x16x32_bf16 v[250:253], v[72:75], v[208:211], v[250:253]\n"
"s_add_u32 s70, s50, 4294964304\n"
"v_min_u32 v213, s70, v255\n"
"v_mfma_f32_16x16x32_bf16 v[140:143], v[76:79], v[208:211], v[140:143]\n"
"v_exp_f32 v178, v178\n"
"v_exp_f32 v179, v179\n"
"v_mfma_f32_16x16x32_bf16 v[152:155], v[80:83], v[208:211], v[152:155]\n"
"v_exp_f32 v180, v180\n"
"v_cvt_pk_bf16_f32 v178, v178, v179\n"
"ds_write_addtid_b32 v178 offset:56320\n"
"v_cvt_pk_bf16_f32 v250, v250, v251\n"
"s_mov_b64 exec, s[52:53]\n"
"v_add_u32 v213, 1040, v213\n"
"s_mov_b64 exec, -1\n"
"v_exp_f32 v181, v181\n"
"v_cvt_pk_bf16_f32 v254, v152, v153\n"
"v_cvt_pk_bf16_f32 v255, v154, v155\n"
"v_cvt_pk_bf16_f32 v179, v180, v181\n"
"s_waitcnt lgkmcnt(1)\n"
"v_mfma_f32_16x16x32_bf16 v[132:135], v[214:217], v[136:139], v[132:135]\n"
"ds_write_addtid_b32 v179 offset:56576\n"
"v_cvt_pk_bf16_f32 v251, v252, v253\n"
"v_cvt_pk_bf16_f32 v252, v140, v141\n"
"v_cvt_pk_bf16_f32 v253, v142, v143\n"
"v_mfma_f32_16x16x32_bf16 v[132:135], v[216:219], v[24:27], v[132:135]\n"
"ds_read_b128 v[178:181], v213 offset:5376\n"
"ds_read_b128 v[140:143], v5 offset:30720\n"
"v_mfma_f32_16x16x32_bf16 v[208:211], v[84:87], v[144:147], 0\n"
"v_exp_f32 v148, v148\n"
"v_mfma_f32_16x16x32_bf16 v[152:155], v[88:91], v[144:147], 0\n"
"v_exp_f32 v149, v149\n"
"s_nop 0\n"
"v_cvt_pk_bf16_f32 v148, v148, v149\n"
"v_mfma_f32_16x16x32_bf16 v[156:159], v[92:95], v[144:147], 0\n"
"v_exp_f32 v150, v150\n"
"ds_read_b128 v[144:147], v6 offset:5120\n"
"ds_write_addtid_b32 v148 offset:55808\n"
"s_waitcnt lgkmcnt(3)\n"
"v_mfma_f32_16x16x32_bf16 v[178:181], v[238:241], v[136:139], v[178:181]\n"
"v_mfma_f32_16x16x32_bf16 v[178:181], v[240:243], v[24:27], v[178:181]\n"
"v_exp_f32 v151, v151\n"
"s_nop 0\n"
"v_cvt_pk_bf16_f32 v149, v150, v151\n"
"v_exp_f32 v132, v132\n"
"s_waitcnt lgkmcnt(2)\n"
"v_mfma_f32_16x16x32_bf16 v[208:211], v[96:99], v[140:143], v[208:211]\n"
"ds_read_b128 v[238:241], v4 offset:32768\n"
"ds_read_b128 v[160:163], v177 offset:2720\n"
"ds_write_addtid_b32 v149 offset:56064\n"
"v_mfma_f32_16x16x32_bf16 v[152:155], v[100:103], v[140:143], v[152:155]\n"
"s_waitcnt lgkmcnt(4)\n"
"v_exp_f32 v133, v133\n"
"v_mfma_f32_16x16x32_bf16 v[156:159], v[144:147], v[140:143], v[156:159]\n"
"s_waitcnt lgkmcnt(1)\n"
"ds_read_b128 v[144:147], v176 offset:4064\n"
"v_mfma_f32_16x16x32_bf16 v[160:163], v[190:193], v[136:139], v[160:163]\n"
"ds_read_b128 v[140:143], v6 offset:6144\n"
"v_mfma_f32_16x16x32_bf16 v[160:163], v[192:195], v[24:27], v[160:163]\n"
"v_exp_f32 v134, v134\n"
"v_exp_f32 v135, v135\n"
"v_cvt_pk_bf16_f32 v132, v132, v133\n"
"v_cvt_pk_bf16_f32 v133, v134, v135\n"
"v_mfma_f32_16x16x32_bf16 v[208:211], v[104:107], v[238:241], v[208:211]\n"
"ds_write_addtid_b32 v132 offset:56832\n"
"ds_write_addtid_b32 v133 offset:57088\n"
"v_mfma_f32_16x16x32_bf16 v[152:155], v[108:111], v[238:241], v[152:155]\n"
"s_waitcnt lgkmcnt(2)\n"
"v_mfma_f32_16x16x32_bf16 v[156:159], v[140:143], v[238:241], v[156:159]\n"
"v_exp_f32 v178, v178\n"
"v_exp_f32 v179, v179\n"
"ds_read_b128 v[238:241], v6 offset:7168\n"
"v_mfma_f32_16x16x32_bf16 v[144:147], v[220:223], v[136:139], v[144:147]\n"
"v_exp_f32 v180, v180\n"
"v_exp_f32 v181, v181\n"
"v_cvt_pk_bf16_f32 v178, v178, v179\n"
"v_cvt_pk_bf16_f32 v179, v180, v181\n"
"ds_read_b128 v[132:135], v5 offset:32768\n"
"s_waitcnt lgkmcnt(2)\n"
"s_barrier\n"
"v_mfma_f32_16x16x32_bf16 v[144:147], v[222:225], v[24:27], v[144:147]\n"
"s_waitcnt lgkmcnt(0)\n"
"ds_read_b128 v[140:143], v213 offset:5392\n"
"v_mfma_f32_16x16x32_bf16 v[208:211], v[112:115], v[132:135], v[208:211]\n"
"ds_write_addtid_b32 v178 offset:57344\n"
"ds_write_addtid_b32 v179 offset:57600\n"
"v_mfma_f32_16x16x32_bf16 v[152:155], v[116:119], v[132:135], v[152:155]\n"
"v_exp_f32 v160, v160\n"
"ds_read_b128 v[178:181], v4 offset:34816\n"
"v_mfma_f32_16x16x32_bf16 v[156:159], v[238:241], v[132:135], v[156:159]\n"
"s_waitcnt lgkmcnt(3)\n"
"v_exp_f32 v161, v161\n"
"v_mfma_f32_16x16x32_bf16 v[140:143], v[244:247], v[136:139], v[140:143]\n"
"ds_read_b128 v[238:241], v6 offset:8192\n"
"v_cvt_pk_bf16_f32 v160, v160, v161\n"
"ds_write_addtid_b32 v160 offset:58368\n"
"ds_read_b128 v[132:135], v213 offset:5408\n"
"v_mfma_f32_16x16x32_bf16 v[140:143], v[246:249], v[24:27], v[140:143]\n"
"s_waitcnt lgkmcnt(3)\n"
"v_mfma_f32_16x16x32_bf16 v[208:211], v[0:3], v[178:181], v[208:211]\n"
"ds_read_b128 v[148:151], v5 offset:34816\n"
"v_bfe_u32 v243, v6, 2, 6\n"
"v_mfma_f32_16x16x32_bf16 v[152:155], v[120:123], v[178:181], v[152:155]\n"
"s_waitcnt lgkmcnt(3)\n"
"v_exp_f32 v144, v144\n"
"v_exp_f32 v145, v145\n"
"v_mfma_f32_16x16x32_bf16 v[156:159], v[238:241], v[178:181], v[156:159]\n"
"s_waitcnt lgkmcnt(1)\n"
"ds_read_b128 v[178:181], v6 offset:9216\n"
"v_cvt_pk_bf16_f32 v144, v144, v145\n"
"v_mfma_f32_16x16x32_bf16 v[132:135], v[250:253], v[136:139], v[132:135]\n"
"v_exp_f32 v140, v140\n"
"ds_read_b128 v[238:241], v4 offset:12288\n"
"v_mfma_f32_16x16x32_bf16 v[132:135], v[252:255], v[24:27], v[132:135]\n"
"s_waitcnt lgkmcnt(2)\n"
"ds_read_b32 v243, v243 offset:1856\n"
"v_mfma_f32_16x16x32_bf16 v[208:211], v[124:127], v[148:151], v[208:211]\n"
"v_exp_f32 v141, v141\n"
"v_mfma_f32_16x16x32_bf16 v[152:155], v[128:131], v[148:151], v[152:155]\n"
"s_waitcnt lgkmcnt(2)\n"
"ds_read_b128 v[136:139], v5 offset:12288\n"
"v_cvt_pk_bf16_f32 v140, v140, v141\n"
"v_exp_f32 v142, v142\n"
"v_mfma_f32_16x16x32_bf16 v[156:159], v[178:181], v[148:151], v[156:159]\n"
"ds_write_addtid_b32 v144 offset:58880\n"
"s_waitcnt lgkmcnt(3)\n"
"v_mfma_f32_16x16x32_bf16 v[178:181], v[8:11], v[238:241], 0\n"
"v_exp_f32 v143, v143\n"
"ds_write_addtid_b32 v140 offset:57856\n"
"v_cvt_pk_bf16_f32 v141, v142, v143\n"
"v_mfma_f32_16x16x32_bf16 v[144:147], v[12:15], v[238:241], 0\n"
"ds_write_addtid_b32 v141 offset:58112\n"
"v_exp_f32 v132, v132\n"
"v_mfma_f32_16x16x32_bf16 v[140:143], v[16:19], v[238:241], 0\n"
"s_waitcnt lgkmcnt(3)\n"
"ds_read_b128 v[238:241], v4 offset:14336\n"
"v_mfma_f32_16x16x32_bf16 v[178:181], v[20:23], v[136:139], v[178:181]\n"
"v_add_u32_sdwa v213, v243, v7 dst_sel:DWORD dst_unused:UNUSED_PAD src0_sel:WORD_0 src1_sel:DWORD\n"
"v_mul_f32 v208, s60, v208\n"
"v_mul_f32 v209, s60, v209\n"
"v_cvt_pk_bf16_f32 v208, v208, v209\n"
"v_mfma_f32_16x16x32_bf16 v[144:147], v[28:31], v[136:139], v[144:147]\n"
"v_exp_f32 v133, v133\n"
"s_add_u32 s71, s50, 4294964592\n"
"v_max_u32 v212, s71, v213\n"
"v_mul_f32 v159, s60, v159\n"
"v_mfma_f32_16x16x32_bf16 v[140:143], v[32:35], v[136:139], v[140:143]\n"
"ds_read_b128 v[148:151], v212 offset:2720\n"
"v_mul_f32 v211, s60, v211\n"
"v_cvt_pk_bf16_f32 v132, v132, v133\n"
"v_mul_f32 v210, s60, v210\n"
"v_mul_f32 v154, s60, v154\n"
"v_mul_f32 v155, s60, v155\n"
"v_mul_f32 v158, s60, v158\n"
"v_cvt_pk_bf16_f32 v27, v158, v159\n"
"v_cvt_pk_bf16_f32 v209, v210, v211\n"
"ds_write_addtid_b32 v132 offset:34304\n"
"s_waitcnt lgkmcnt(2)\n"
"v_mfma_f32_16x16x32_bf16 v[178:181], v[36:39], v[238:241], v[178:181]\n"
"v_mul_f32 v157, s60, v157\n"
"v_cvt_pk_bf16_f32 v211, v154, v155\n"
"s_add_u32 s72, s50, 4294965632\n"
"ds_read_b128 v[132:135], v5 offset:14336\n"
"v_mul_f32 v152, s60, v152\n"
"v_mul_f32 v156, s60, v156\n"
"v_mul_f32 v153, s60, v153\n"
"v_mfma_f32_16x16x32_bf16 v[144:147], v[40:43], v[238:241], v[144:147]\n"
"s_waitcnt lgkmcnt(1)\n"
"v_cvt_pk_bf16_f32 v210, v152, v153\n"
"s_barrier\n"
"v_min_u32 v177, s72, v213\n"
"v_cvt_pk_bf16_f32 v26, v156, v157\n"
"v_mfma_f32_16x16x32_bf16 v[148:151], v[190:193], v[208:211], v[148:151]\n"
"s_nop 0\n"
"v_mfma_f32_16x16x32_bf16 v[148:151], v[192:195], v[24:27], v[148:151]\n"
"ds_read_b128 v[136:139], v212 offset:2736\n"
"v_mfma_f32_16x16x32_bf16 v[140:143], v[44:47], v[238:241], v[140:143]\n"
"s_waitcnt lgkmcnt(1)\n"
"ds_read_b128 v[238:241], v4 offset:16384\n"
"v_mfma_f32_16x16x32_bf16 v[178:181], v[48:51], v[132:135], v[178:181]\n"
"v_mfma_f32_16x16x32_bf16 v[144:147], v[52:55], v[132:135], v[144:147]\n"
"v_mfma_f32_16x16x32_bf16 v[140:143], v[56:59], v[132:135], v[140:143]\n"
"s_nop 0\n"
"v_exp_f32 v148, v148\n"
"s_waitcnt lgkmcnt(1)\n"
"v_mfma_f32_16x16x32_bf16 v[136:139], v[196:199], v[208:211], v[136:139]\n"
"v_exp_f32 v149, v149\n"
"ds_read_b128 v[132:135], v177 offset:4048\n"
"v_cvt_pk_bf16_f32 v148, v148, v149\n"
"v_mfma_f32_16x16x32_bf16 v[136:139], v[198:201], v[24:27], v[136:139]\n"
"s_waitcnt lgkmcnt(1)\n"
"v_mfma_f32_16x16x32_bf16 v[178:181], v[60:63], v[238:241], v[178:181]\n"
"v_exp_f32 v150, v150\n"
"v_mfma_f32_16x16x32_bf16 v[144:147], v[64:67], v[238:241], v[144:147]\n"
"ds_write_addtid_b32 v148 offset:53248\n"
"ds_read_b128 v[152:155], v5 offset:16384\n"
"v_mfma_f32_16x16x32_bf16 v[140:143], v[68:71], v[238:241], v[140:143]\n"
"s_waitcnt lgkmcnt(2)\n"
"v_exp_f32 v151, v151\n"
"v_mfma_f32_16x16x32_bf16 v[132:135], v[214:217], v[208:211], v[132:135]\n"
"ds_read_b128 v[238:241], v177 offset:4064\n"
"v_cvt_pk_bf16_f32 v149, v150, v151\n"
"v_mfma_f32_16x16x32_bf16 v[132:135], v[216:219], v[24:27], v[132:135]\n"
"s_waitcnt lgkmcnt(1)\n"
"v_mfma_f32_16x16x32_bf16 v[178:181], v[72:75], v[152:155], v[178:181]\n"
"v_exp_f32 v136, v136\n"
"v_exp_f32 v137, v137\n"
"v_mfma_f32_16x16x32_bf16 v[144:147], v[76:79], v[152:155], v[144:147]\n"
"ds_write_addtid_b32 v149 offset:53504\n"
"v_cvt_pk_bf16_f32 v136, v136, v137\n"
"v_exp_f32 v138, v138\n"
"v_exp_f32 v139, v139\n"
"v_mfma_f32_16x16x32_bf16 v[140:143], v[80:83], v[152:155], v[140:143]\n"
"ds_write_addtid_b32 v136 offset:53760\n"
"ds_read_b128 v[148:151], v4 offset:18432\n"
"v_cvt_pk_bf16_f32 v137, v138, v139\n"
"ds_write_addtid_b32 v137 offset:54016\n"
"v_cvt_pk_bf16_f32 v178, v178, v179\n"
"s_nop 2\n"
"v_cvt_pk_bf16_f32 v183, v142, v143\n"
"ds_read_b128 v[136:139], v177 offset:4080\n"
"v_cvt_pk_bf16_f32 v182, v140, v141\n"
"s_waitcnt lgkmcnt(5)\n"
"v_mfma_f32_16x16x32_bf16 v[238:241], v[220:223], v[208:211], v[238:241]\n"
"v_cvt_pk_bf16_f32 v179, v180, v181\n"
"v_cvt_pk_bf16_f32 v180, v144, v145\n"
"v_cvt_pk_bf16_f32 v181, v146, v147\n"
"v_mfma_f32_16x16x32_bf16 v[238:241], v[222:225], v[24:27], v[238:241]\n"
"s_waitcnt lgkmcnt(2)\n"
"v_exp_f32 v132, v132\n"
"v_mfma_f32_16x16x32_bf16 v[140:143], v[84:87], v[148:151], 0\n"
"v_exp_f32 v133, v133\n"
"v_mfma_f32_16x16x32_bf16 v[152:155], v[88:91], v[148:151], 0\n"
"s_add_u32 s73, s50, 4294964288\n"
"ds_read_b128 v[144:147], v5 offset:18432\n"
"v_cvt_pk_bf16_f32 v132, v132, v133\n"
"v_mfma_f32_16x16x32_bf16 v[156:159], v[92:95], v[148:151], 0\n"
"s_waitcnt lgkmcnt(1)\n"
"v_exp_f32 v134, v134\n"
"v_exp_f32 v135, v135\n"
"v_mfma_f32_16x16x32_bf16 v[136:139], v[226:229], v[208:211], v[136:139]\n"
"ds_write_addtid_b32 v132 offset:54272\n"
"ds_read_b128 v[148:151], v6 offset:5120\n"
"ds_read_b128 v[160:163], v212 offset:2704\n"
"v_cvt_pk_bf16_f32 v133, v134, v135\n"
"v_mfma_f32_16x16x32_bf16 v[136:139], v[228:231], v[24:27], v[136:139]\n"
"s_waitcnt lgkmcnt(3)\n"
"v_exp_f32 v238, v238\n"
"v_mfma_f32_16x16x32_bf16 v[140:143], v[96:99], v[144:147], v[140:143]\n"
"ds_write_addtid_b32 v133 offset:54528\n"
"v_min_u32 v212, s73, v213\n"
"ds_read_b128 v[132:135], v4 offset:20480\n"
"v_mfma_f32_16x16x32_bf16 v[152:155], v[100:103], v[144:147], v[152:155]\n"
"s_waitcnt lgkmcnt(3)\n"
"v_exp_f32 v239, v239\n"
"v_exp_f32 v240, v240\n"
"v_mfma_f32_16x16x32_bf16 v[156:159], v[148:151], v[144:147], v[156:159]\n"
"s_mov_b64 exec, s[52:53]\n"
"v_add_u32 v212, 1040, v212\n"
"s_mov_b64 exec, -1\n"
"ds_read_b128 v[144:147], v6 offset:6144\n"
"s_waitcnt lgkmcnt(3)\n"
"v_mfma_f32_16x16x32_bf16 v[160:163], v[184:187], v[208:211], v[160:163]\n"
"v_exp_f32 v241, v241\n"
"ds_read_b128 v[148:151], v212 offset:5392\n"
"v_mfma_f32_16x16x32_bf16 v[160:163], v[186:189], v[24:27], v[160:163]\n"
"s_waitcnt lgkmcnt(2)\n"
"ds_read_b128 v[184:187], v5 offset:20480\n"
"v_cvt_pk_bf16_f32 v238, v238, v239\n"
"v_exp_f32 v136, v136\n"
"v_mfma_f32_16x16x32_bf16 v[140:143], v[104:107], v[132:135], v[140:143]\n"
"ds_write_addtid_b32 v238 offset:54784\n"
"v_mfma_f32_16x16x32_bf16 v[152:155], v[108:111], v[132:135], v[152:155]\n"
"s_waitcnt lgkmcnt(3)\n"
"v_exp_f32 v137, v137\n"
"v_mfma_f32_16x16x32_bf16 v[156:159], v[144:147], v[132:135], v[156:159]\n"
"s_waitcnt lgkmcnt(2)\n"
"v_exp_f32 v138, v138\n"
"v_cvt_pk_bf16_f32 v239, v240, v241\n"
"v_cvt_pk_bf16_f32 v136, v136, v137\n"
"v_mfma_f32_16x16x32_bf16 v[148:151], v[244:247], v[208:211], v[148:151]\n"
"ds_write_addtid_b32 v136 offset:33792\n"
"ds_write_addtid_b32 v239 offset:55040\n"
"v_mfma_f32_16x16x32_bf16 v[148:151], v[246:249], v[24:27], v[148:151]\n"
"v_exp_f32 v139, v139\n"
"v_exp_f32 v160, v160\n"
"ds_read_b128 v[238:241], v6 offset:7168\n"
"v_cvt_pk_bf16_f32 v137, v138, v139\n"
"ds_write_addtid_b32 v137 offset:34048\n"
"s_waitcnt lgkmcnt(5)\n"
"v_mfma_f32_16x16x32_bf16 v[140:143], v[112:115], v[184:187], v[140:143]\n"
"v_exp_f32 v161, v161\n"
"s_nop 0\n"
"v_cvt_pk_bf16_f32 v160, v160, v161\n"
"ds_read_b128 v[132:135], v212 offset:5408\n"
"v_mfma_f32_16x16x32_bf16 v[152:155], v[116:119], v[184:187], v[152:155]\n"
"s_waitcnt lgkmcnt(2)\n"
"v_exp_f32 v162, v162\n"
"v_mfma_f32_16x16x32_bf16 v[156:159], v[238:241], v[184:187], v[156:159]\n"
"s_waitcnt lgkmcnt(1)\n"
"s_barrier\n"
"ds_read_b128 v[184:187], v6 offset:8192\n"
"ds_read_b128 v[136:139], v4 offset:22528\n"
"s_waitcnt lgkmcnt(2)\n"
"v_mfma_f32_16x16x32_bf16 v[132:135], v[250:253], v[208:211], v[132:135]\n"
"v_exp_f32 v163, v163\n"
"ds_read_b128 v[238:241], v212 offset:5424\n"
"v_cvt_pk_bf16_f32 v161, v162, v163\n"
"v_exp_f32 v148, v148\n"
"v_exp_f32 v149, v149\n"
"v_mfma_f32_16x16x32_bf16 v[132:135], v[252:255], v[24:27], v[132:135]\n"
"ds_write_addtid_b32 v160 offset:57856\n"
"s_waitcnt lgkmcnt(2)\n"
"v_mfma_f32_16x16x32_bf16 v[140:143], v[0:3], v[136:139], v[140:143]\n"
"ds_read_b128 v[144:147], v5 offset:22528\n"
"v_cvt_pk_bf16_f32 v148, v148, v149\n"
"v_mfma_f32_16x16x32_bf16 v[152:155], v[120:123], v[136:139], v[152:155]\n"
"v_exp_f32 v150, v150\n"
"ds_write_addtid_b32 v161 offset:58112\n"
"v_exp_f32 v151, v151\n"
"v_mfma_f32_16x16x32_bf16 v[156:159], v[184:187], v[136:139], v[156:159]\n"
"s_waitcnt lgkmcnt(3)\n"
"ds_read_b128 v[184:187], v6 offset:9216\n"
"v_cvt_pk_bf16_f32 v149, v150, v151\n"
"v_mfma_f32_16x16x32_bf16 v[238:241], v[178:181], v[208:211], v[238:241]\n"
"v_exp_f32 v132, v132\n"
"ds_read_b128 v[136:139], v5 offset:36864\n"
"ds_read_b128 v[208:211], v4 offset:36864\n"
"v_exp_f32 v133, v133\n"
"v_mfma_f32_16x16x32_bf16 v[238:241], v[180:183], v[24:27], v[238:241]\n"
"ds_write_addtid_b32 v148 offset:58368\n"
"s_waitcnt lgkmcnt(5)\n"
"v_mfma_f32_16x16x32_bf16 v[140:143], v[124:127], v[144:147], v[140:143]\n"
"v_exp_f32 v134, v134\n"
"v_mfma_f32_16x16x32_bf16 v[152:155], v[128:131], v[144:147], v[152:155]\n"
"ds_write_addtid_b32 v149 offset:58624\n"
"v_bfe_u32 v213, v6, 2, 6\n"
"ds_read_b32 v213, v213 offset:1920\n"
"s_waitcnt lgkmcnt(5)\n"
"v_mfma_f32_16x16x32_bf16 v[156:159], v[184:187], v[144:147], v[156:159]\n"
"s_waitcnt lgkmcnt(3)\n"
"v_exp_f32 v135, v135\n"
"v_mfma_f32_16x16x32_bf16 v[184:187], v[8:11], v[208:211], 0\n"
"v_cvt_pk_bf16_f32 v132, v132, v133\n"
"v_cvt_pk_bf16_f32 v133, v134, v135\n"
"ds_write_addtid_b32 v132 offset:58880\n"
"s_add_u32 s68, s50, 4294964576\n"
"v_mfma_f32_16x16x32_bf16 v[8:11], v[12:15], v[208:211], 0\n"
"ds_write_addtid_b32 v133 offset:59136\n"
"v_mul_f32 v140, s60, v140\n"
"s_waitcnt lgkmcnt(2)\n"
"v_add_u32_sdwa v212, v213, v7 dst_sel:DWORD dst_unused:UNUSED_PAD src0_sel:WORD_0 src1_sel:DWORD\n"
"v_exp_f32 v238, v238\n"
"v_mfma_f32_16x16x32_bf16 v[12:15], v[16:19], v[208:211], 0\n"
"ds_read_b128 v[208:211], v4 offset:38912\n"
"v_max_u32 v177, s68, v212\n"
"v_mfma_f32_16x16x32_bf16 v[184:187], v[20:23], v[136:139], v[184:187]\n"
"v_exp_f32 v239, v239\n"
"ds_read_b128 v[16:19], v177 offset:2720\n"
"v_mul_f32 v141, s60, v141\n"
"v_mfma_f32_16x16x32_bf16 v[8:11], v[28:31], v[136:139], v[8:11]\n"
"v_exp_f32 v240, v240\n"
"v_exp_f32 v241, v241\n"
"v_mfma_f32_16x16x32_bf16 v[12:15], v[32:35], v[136:139], v[12:15]\n"
"s_waitcnt lgkmcnt(1)\n"
"v_cvt_pk_bf16_f32 v238, v238, v239\n"
"v_cvt_pk_bf16_f32 v239, v240, v241\n"
"v_mfma_f32_16x16x32_bf16 v[184:187], v[36:39], v[208:211], v[184:187]\n"
"ds_write_addtid_b32 v238 offset:57344\n"
"v_mul_f32 v143, s60, v143\n"
"v_mfma_f32_16x16x32_bf16 v[8:11], v[40:43], v[208:211], v[8:11]\n"
"ds_write_addtid_b32 v239 offset:57600\n"
"v_mul_f32 v157, s60, v157\n"
"ds_read_b128 v[238:241], v5 offset:38912\n"
"v_mul_f32 v155, s60, v155\n"
"v_mul_f32 v156, s60, v156\n"
"v_cvt_pk_bf16_f32 v26, v156, v157\n"
"v_mul_f32 v152, s60, v152\n"
"v_cvt_pk_bf16_f32 v140, v140, v141\n"
"v_mul_f32 v142, s60, v142\n"
"v_mul_f32 v153, s60, v153\n"
"v_cvt_pk_bf16_f32 v141, v142, v143\n"
"v_mul_f32 v154, s60, v154\n"
"v_cvt_pk_bf16_f32 v143, v154, v155\n"
"v_cvt_pk_bf16_f32 v142, v152, v153\n"
"s_waitcnt lgkmcnt(1)\n"
"s_barrier\n"
"v_mfma_f32_16x16x32_bf16 v[16:19], v[190:193], v[140:143], v[16:19]\n"
"s_add_u32 s69, s50, 4294963232\n"
"v_mul_f32 v159, s60, v159\n"
"v_mul_f32 v158, s60, v158\n"
"v_cvt_pk_bf16_f32 v27, v158, v159\n"
"v_add_u32_sdwa v213, sext(v213), v212 dst_sel:DWORD dst_unused:UNUSED_PAD src0_sel:WORD_1 src1_sel:DWORD\n"
"s_nop 0\n"
"v_mfma_f32_16x16x32_bf16 v[16:19], v[192:195], v[24:27], v[16:19]\n"
"ds_read_b128 v[188:191], v177 offset:2736\n"
"v_max_u32 v177, s68, v213\n"
"v_mfma_f32_16x16x32_bf16 v[12:15], v[44:47], v[208:211], v[12:15]\n"
"s_waitcnt lgkmcnt(1)\n"
"v_mfma_f32_16x16x32_bf16 v[184:187], v[48:51], v[238:241], v[184:187]\n"
"ds_read_b128 v[192:195], v4 offset:40960\n"
"v_mfma_f32_16x16x32_bf16 v[8:11], v[52:55], v[238:241], v[8:11]\n"
"v_mfma_f32_16x16x32_bf16 v[12:15], v[56:59], v[238:241], v[12:15]\n"
"s_waitcnt lgkmcnt(1)\n"
"v_mfma_f32_16x16x32_bf16 v[188:191], v[196:199], v[140:143], v[188:191]\n"
"s_add_u32 s70, s50, 4294964272\n"
"ds_read_b128 v[208:211], v177 offset:2752\n"
"v_exp_f32 v18, v18\n"
"v_mfma_f32_16x16x32_bf16 v[188:191], v[198:201], v[24:27], v[188:191]\n"
"s_waitcnt lgkmcnt(1)\n"
"ds_read_b128 v[196:199], v5 offset:40960\n"
"v_mfma_f32_16x16x32_bf16 v[184:187], v[60:63], v[192:195], v[184:187]\n"
"v_exp_f32 v19, v19\n"
"v_mfma_f32_16x16x32_bf16 v[8:11], v[64:67], v[192:195], v[8:11]\n"
"v_cvt_pk_bf16_f32 v17, v18, v19\n"
"ds_write_addtid_b32 v17 offset:55552\n"
"v_mfma_f32_16x16x32_bf16 v[12:15], v[68:71], v[192:195], v[12:15]\n"
"s_waitcnt lgkmcnt(2)\n"
"v_max_u32 v239, s69, v213\n"
"v_mfma_f32_16x16x32_bf16 v[208:211], v[202:205], v[140:143], v[208:211]\n"
"v_exp_f32 v188, v188\n"
"v_mfma_f32_16x16x32_bf16 v[208:211], v[204:207], v[24:27], v[208:211]\n"
"s_waitcnt lgkmcnt(1)\n"
"v_min_u32 v177, s70, v212\n"
"ds_read_b128 v[192:195], v239 offset:4096\n"
"v_mfma_f32_16x16x32_bf16 v[184:187], v[72:75], v[196:199], v[184:187]\n"
"v_exp_f32 v189, v189\n"
"v_exp_f32 v190, v190\n"
"v_mfma_f32_16x16x32_bf16 v[8:11], v[76:79], v[196:199], v[8:11]\n"
"ds_read_b128 v[200:203], v4 offset:43008\n"
"v_cvt_pk_bf16_f32 v188, v188, v189\n"
"ds_write_addtid_b32 v188 offset:55808\n"
"v_exp_f32 v191, v191\n"
"v_mfma_f32_16x16x32_bf16 v[12:15], v[80:83], v[196:199], v[12:15]\n"
"v_cvt_pk_bf16_f32 v184, v184, v185\n"
"v_cvt_pk_bf16_f32 v185, v186, v187\n"
"v_cvt_pk_bf16_f32 v189, v190, v191\n"
"ds_write_addtid_b32 v189 offset:56064\n"
"v_cvt_pk_bf16_f32 v187, v10, v11\n"
"v_min_u32 v176, s70, v213\n"
"s_waitcnt lgkmcnt(3)\n"
"v_mfma_f32_16x16x32_bf16 v[192:195], v[232:235], v[140:143], v[192:195]\n"
"s_mov_b64 exec, s[52:53]\n"
"v_add_u32 v177, 1040, v177\n"
"v_add_u32 v176, 1040, v176\n"
"s_mov_b64 exec, -1\n"
"ds_read_b128 v[196:199], v177 offset:5408\n"
"v_mfma_f32_16x16x32_bf16 v[192:195], v[234:237], v[24:27], v[192:195]\n"
"s_waitcnt lgkmcnt(3)\n"
"ds_read_b128 v[232:235], v5 offset:43008\n"
"v_cvt_pk_bf16_f32 v189, v14, v15\n"
"v_cvt_pk_bf16_f32 v188, v12, v13\n"
"v_mfma_f32_16x16x32_bf16 v[204:207], v[84:87], v[200:203], 0\n"
"v_exp_f32 v208, v208\n"
"v_mfma_f32_16x16x32_bf16 v[236:239], v[88:91], v[200:203], 0\n"
"v_exp_f32 v209, v209\n"
"v_mfma_f32_16x16x32_bf16 v[240:243], v[92:95], v[200:203], 0\n"
"s_waitcnt lgkmcnt(1)\n"
"v_exp_f32 v210, v210\n"
"v_exp_f32 v211, v211\n"
"v_cvt_pk_bf16_f32 v208, v208, v209\n"
"v_mfma_f32_16x16x32_bf16 v[196:199], v[250:253], v[140:143], v[196:199]\n"
"v_exp_f32 v192, v192\n"
"ds_read_b128 v[200:203], v6 offset:5120\n"
"v_cvt_pk_bf16_f32 v209, v210, v211\n"
"v_mfma_f32_16x16x32_bf16 v[196:199], v[252:255], v[24:27], v[196:199]\n"
"s_waitcnt lgkmcnt(1)\n"
"v_exp_f32 v193, v193\n"
"v_cvt_pk_bf16_f32 v186, v8, v9\n"
"v_mfma_f32_16x16x32_bf16 v[204:207], v[96:99], v[232:235], v[204:207]\n"
"v_cvt_pk_bf16_f32 v192, v192, v193\n"
"ds_write_addtid_b32 v208 offset:56320\n"
"ds_write_addtid_b32 v209 offset:56576\n"
"ds_write_addtid_b32 v192 offset:56832\n"
"v_mfma_f32_16x16x32_bf16 v[236:239], v[100:103], v[232:235], v[236:239]\n"
"v_exp_f32 v194, v194\n"
"v_exp_f32 v195, v195\n"
"s_nop 0\n"
"v_cvt_pk_bf16_f32 v193, v194, v195\n"
"v_max_u32 v213, s69, v212\n"
"ds_read_b128 v[8:11], v213 offset:4064\n"
"v_exp_f32 v198, v198\n"
"ds_read_b128 v[208:211], v4 offset:45056\n"
"s_waitcnt lgkmcnt(5)\n"
"v_mfma_f32_16x16x32_bf16 v[240:243], v[200:203], v[232:235], v[240:243]\n"
"ds_write_addtid_b32 v193 offset:57088\n"
"s_waitcnt lgkmcnt(2)\n"
"v_mfma_f32_16x16x32_bf16 v[8:11], v[220:223], v[140:143], v[8:11]\n"
"ds_read_b128 v[200:203], v6 offset:6144\n"
"v_mfma_f32_16x16x32_bf16 v[8:11], v[222:225], v[24:27], v[8:11]\n"
"s_waitcnt lgkmcnt(2)\n"
"ds_read_b128 v[232:235], v213 offset:4080\n"
"v_mfma_f32_16x16x32_bf16 v[204:207], v[104:107], v[208:211], v[204:207]\n"
"s_add_u32 s71, s50, 4294963248\n"
"ds_read_b128 v[190:193], v5 offset:45056\n"
"v_mfma_f32_16x16x32_bf16 v[236:239], v[108:111], v[208:211], v[236:239]\n"
"s_waitcnt lgkmcnt(2)\n"
"v_exp_f32 v199, v199\n"
"v_mfma_f32_16x16x32_bf16 v[240:243], v[200:203], v[208:211], v[240:243]\n"
"s_waitcnt lgkmcnt(1)\n"
"v_exp_f32 v10, v10\n"
"v_mfma_f32_16x16x32_bf16 v[232:235], v[226:229], v[140:143], v[232:235]\n"
"ds_read_b128 v[208:211], v177 offset:5424\n"
"v_mfma_f32_16x16x32_bf16 v[232:235], v[228:231], v[24:27], v[232:235]\n"
"v_cvt_pk_bf16_f32 v197, v198, v199\n"
"s_add_u32 s72, s50, 4294964288\n"
"ds_write_addtid_b32 v197 offset:34560\n"
"s_waitcnt lgkmcnt(2)\n"
"v_mfma_f32_16x16x32_bf16 v[204:207], v[112:115], v[190:193], v[204:207]\n"
"ds_read_b128 v[200:203], v6 offset:7168\n"
"ds_read_b128 v[194:197], v4 offset:47104\n"
"v_bfe_u32 v4, v6, 2, 6\n"
"v_mfma_f32_16x16x32_bf16 v[236:239], v[116:119], v[190:193], v[236:239]\n"
"s_waitcnt lgkmcnt(1)\n"
"v_mfma_f32_16x16x32_bf16 v[240:243], v[200:203], v[190:193], v[240:243]\n"
"v_exp_f32 v11, v11\n"
"v_exp_f32 v232, v232\n"
"v_mfma_f32_16x16x32_bf16 v[208:211], v[178:181], v[140:143], v[208:211]\n"
"ds_read_b128 v[190:193], v6 offset:8192\n"
"ds_read_b32 v4, v4 offset:1984\n"
"v_exp_f32 v233, v233\n"
"s_barrier\n"
"v_mfma_f32_16x16x32_bf16 v[208:211], v[180:183], v[24:27], v[208:211]\n"
"s_waitcnt lgkmcnt(0)\n"
"v_add_u32_sdwa v213, v4, v7 dst_sel:DWORD dst_unused:UNUSED_PAD src0_sel:WORD_0 src1_sel:DWORD\n"
"ds_read_b128 v[198:201], v176 offset:5440\n"
"v_mfma_f32_16x16x32_bf16 v[204:207], v[0:3], v[194:197], v[204:207]\n"
"ds_read_b128 v[0:3], v5 offset:47104\n"
"v_cvt_pk_bf16_f32 v232, v232, v233\n"
"v_mfma_f32_16x16x32_bf16 v[236:239], v[120:123], v[194:197], v[236:239]\n"
"ds_write_addtid_b32 v232 offset:58880\n"
"v_exp_f32 v234, v234\n"
"v_mfma_f32_16x16x32_bf16 v[240:243], v[190:193], v[194:197], v[240:243]\n"
"ds_read_b128 v[190:193], v6 offset:9216\n"
"v_cvt_pk_bf16_f32 v9, v10, v11\n"
"ds_write_addtid_b32 v9 offset:58624\n"
"v_max_u32 v203, s71, v213\n"
"v_exp_f32 v235, v235\n"
"v_exp_f32 v208, v208\n"
"v_cvt_pk_bf16_f32 v233, v234, v235\n"
"s_waitcnt lgkmcnt(4)\n"
"v_mfma_f32_16x16x32_bf16 v[198:201], v[184:187], v[140:143], v[198:201]\n"
"ds_write_addtid_b32 v233 offset:59136\n"
"v_exp_f32 v209, v209\n"
"v_mfma_f32_16x16x32_bf16 v[198:201], v[186:189], v[24:27], v[198:201]\n"
"s_waitcnt lgkmcnt(4)\n"
"ds_read_b128 v[184:187], v203 offset:4048\n"
"v_mfma_f32_16x16x32_bf16 v[204:207], v[124:127], v[0:3], v[204:207]\n"
"v_cvt_pk_bf16_f32 v208, v208, v209\n"
"v_min_u32 v177, s72, v213\n"
"s_mov_b64 exec, s[52:53]\n"
"v_add_u32 v177, 1040, v177\n"
"s_mov_b64 exec, -1\n"
"v_mfma_f32_16x16x32_bf16 v[236:239], v[128:131], v[0:3], v[236:239]\n"
"s_waitcnt lgkmcnt(3)\n"
"v_exp_f32 v210, v210\n"
"v_exp_f32 v211, v211\n"
"v_mul_f32 v205, s60, v205\n"
"v_mfma_f32_16x16x32_bf16 v[240:243], v[190:193], v[0:3], v[240:243]\n"
"ds_read_b128 v[194:197], v203 offset:4064\n"
"v_mul_f32 v204, s60, v204\n"
"v_cvt_pk_bf16_f32 v204, v204, v205\n"
"v_mul_f32 v207, s60, v207\n"
"ds_read_b128 v[188:191], v177 offset:5392\n"
"v_mul_f32 v206, s60, v206\n"
"s_nop 1\n"
"v_mul_f32 v243, s60, v243\n"
"v_cvt_pk_bf16_f32 v205, v206, v207\n"
"v_mul_f32 v236, s60, v236\n"
"v_mul_f32 v240, s60, v240\n"
"v_mul_f32 v238, s60, v238\n"
"v_mul_f32 v241, s60, v241\n"
"v_mul_f32 v237, s60, v237\n"
"v_mul_f32 v242, s60, v242\n"
"v_cvt_pk_bf16_f32 v27, v242, v243\n"
"v_cvt_pk_bf16_f32 v26, v240, v241\n"
"v_mul_f32 v239, s60, v239\n"
"v_cvt_pk_bf16_f32 v206, v236, v237\n"
"v_cvt_pk_bf16_f32 v207, v238, v239\n"
"v_exp_f32 v198, v198\n"
"s_waitcnt lgkmcnt(2)\n"
"v_mfma_f32_16x16x32_bf16 v[184:187], v[214:217], v[204:207], v[184:187]\n"
"ds_write_addtid_b32 v208 offset:57344\n"
"v_cvt_pk_bf16_f32 v209, v210, v211\n"
"ds_write_addtid_b32 v209 offset:57600\n"
"v_exp_f32 v199, v199\n"
"ds_read_b128 v[212:215], v177 offset:5408\n"
"v_exp_f32 v200, v200\n"
"v_mfma_f32_16x16x32_bf16 v[184:187], v[216:219], v[24:27], v[184:187]\n"
"s_waitcnt lgkmcnt(4)\n"
"ds_read_b128 v[208:211], v203 offset:4080\n"
"v_cvt_pk_bf16_f32 v198, v198, v199\n"
"v_mfma_f32_16x16x32_bf16 v[194:197], v[220:223], v[204:207], v[194:197]\n"
"v_exp_f32 v201, v201\n"
"v_mfma_f32_16x16x32_bf16 v[194:197], v[222:225], v[24:27], v[194:197]\n"
"ds_write_addtid_b32 v198 offset:57856\n"
"v_cvt_pk_bf16_f32 v199, v200, v201\n"
"ds_write_addtid_b32 v199 offset:58112\n"
"s_waitcnt lgkmcnt(6)\n"
"v_mfma_f32_16x16x32_bf16 v[188:191], v[244:247], v[204:207], v[188:191]\n"
"v_exp_f32 v184, v184\n"
"v_exp_f32 v185, v185\n"
"v_mfma_f32_16x16x32_bf16 v[188:191], v[246:249], v[24:27], v[188:191]\n"
"s_waitcnt lgkmcnt(0)\n"
"ds_read_b128 v[198:201], v177 offset:5424\n"
"s_barrier\n"
"v_mfma_f32_16x16x32_bf16 v[208:211], v[226:229], v[204:207], v[208:211]\n"
"v_exp_f32 v186, v186\n"
"v_exp_f32 v187, v187\n"
"v_cvt_pk_bf16_f32 v184, v184, v185\n"
"v_mfma_f32_16x16x32_bf16 v[208:211], v[228:231], v[24:27], v[208:211]\n"
"ds_write_addtid_b32 v184 offset:53248\n"
"v_exp_f32 v194, v194\n"
"v_exp_f32 v195, v195\n"
"s_nop 0\n"
"v_cvt_pk_bf16_f32 v194, v194, v195\n"
"v_cvt_pk_bf16_f32 v185, v186, v187\n"
"ds_write_addtid_b32 v185 offset:53504\n"
"v_exp_f32 v196, v196\n"
"v_exp_f32 v197, v197\n"
"s_nop 0\n"
"v_cvt_pk_bf16_f32 v195, v196, v197\n"
"v_mfma_f32_16x16x32_bf16 v[212:215], v[250:253], v[204:207], v[212:215]\n"
"v_exp_f32 v188, v188\n"
"ds_write_addtid_b32 v194 offset:53760\n"
"ds_write_addtid_b32 v195 offset:54016\n"
"v_mfma_f32_16x16x32_bf16 v[212:215], v[252:255], v[24:27], v[212:215]\n"
"s_waitcnt lgkmcnt(4)\n"
"v_exp_f32 v189, v189\n"
"v_mfma_f32_16x16x32_bf16 v[198:201], v[178:181], v[204:207], v[198:201]\n"
"v_exp_f32 v190, v190\n"
"v_exp_f32 v191, v191\n"
"v_exp_f32 v208, v208\n"
"v_exp_f32 v209, v209\n"
"v_exp_f32 v210, v210\n"
"v_cvt_pk_bf16_f32 v208, v208, v209\n"
"v_mfma_f32_16x16x32_bf16 v[198:201], v[180:183], v[24:27], v[198:201]\n"
"ds_write_addtid_b32 v208 offset:4096\n"
"v_cvt_pk_bf16_f32 v188, v188, v189\n"
"v_exp_f32 v211, v211\n"
"v_exp_f32 v212, v212\n"
"ds_write_addtid_b32 v188 offset:54272\n"
"v_exp_f32 v213, v213\n"
"v_exp_f32 v214, v214\n"
"v_exp_f32 v215, v215\n"
"v_exp_f32 v198, v198\n"
"v_cvt_pk_bf16_f32 v189, v190, v191\n"
"v_cvt_pk_bf16_f32 v212, v212, v213\n"
"v_cvt_pk_bf16_f32 v213, v214, v215\n"
"ds_write_addtid_b32 v212 offset:54784\n"
"ds_write_addtid_b32 v213 offset:55040\n"
"v_exp_f32 v199, v199\n"
"ds_write_addtid_b32 v189 offset:54528\n"
"v_exp_f32 v200, v200\n"
"v_exp_f32 v201, v201\n"
"v_cvt_pk_bf16_f32 v198, v198, v199\n"
"v_cvt_pk_bf16_f32 v199, v200, v201\n"
"ds_write_addtid_b32 v198 offset:4608\n"
"ds_write_addtid_b32 v199 offset:4864\n"
"v_cvt_pk_bf16_f32 v209, v210, v211\n"
"ds_write_addtid_b32 v209 offset:4352\n"
"s_waitcnt lgkmcnt(0)\n"
"s_barrier\n"
"s_barrier\n"
"s_barrier\n"
    ,
"s_add_u32 s50, s54, 53248\n"
"s_lshr_b32 s41, s40, 1\n"
"s_sub_u32 s55, 0, s41\n"
"s_mul_i32 s51, s41, 4096\n"
"s_lshl_b32 s41, s40, 10\n"
"s_add_u32 s51, s51, s41\n"
"s_lshl_b32 s65, s40, 2\n"
"s_add_u32 s65, s65, 32192\n"
"s_andn2_b32 s41, s45, 7\n"
"s_add_u32 s56, s56, s41\n"
"s_addc_u32 s57, s57, 0\n"
"s_add_u32 s66, s66, s41\n"
"s_addc_u32 s67, s67, 0\n"
"s_mul_i32 s68, s40, 1552\n"
"s_add_u32 s68, s68, 57344\n"
"s_setprio 0\n"
"s_movk_i32 s40, 768\n"
"s_mov_b64 s[62:63], 0\n"
"s_movk_i32 s64, 0x207\n"
"s_add_u32 s58, s56, 128\n"
"s_addc_u32 s59, s57, 0\n"
"s_add_u32 s60, s56, 256\n"
"s_addc_u32 s61, s57, 0\n"
"v_subrev_u32 v179, 24576, v10\n"
"s_waitcnt vmcnt(0)\n"
"s_barrier\n"
"s_add_u32 s42, s46, 110976\n"
"s_addc_u32 s43, s47, 0\n"
"s_add_u32 m0, s50, 27648\n"
"s_nop 0\n"
"global_load_lds_dwordx4 v0, s[42:43]\n"
"s_add_u32 m0, s50, 28672\n"
"s_nop 0\n"
"global_load_lds_dwordx4 v1, s[42:43]\n"
"s_add_u32 m0, s50, 29696\n"
"s_nop 0\n"
"global_load_lds_dwordx4 v2, s[42:43]\n"
"s_add_u32 s42, s46, 147840\n"
"s_addc_u32 s43, s47, 0\n"
"s_add_u32 m0, s50, 49152\n"
"s_nop 0\n"
"global_load_lds_dwordx4 v0, s[42:43]\n"
"s_add_u32 m0, s50, 50176\n"
"s_nop 0\n"
"global_load_lds_dwordx4 v1, s[42:43]\n"
"s_add_u32 m0, s50, 51200\n"
"s_nop 0\n"
"global_load_lds_dwordx4 v2, s[42:43]\n"
"s_add_u32 s42, s46, 184704\n"
"s_addc_u32 s43, s47, 0\n"
"s_add_u32 m0, s50, 52224\n"
"s_nop 0\n"
"global_load_lds_dwordx4 v0, s[42:43]\n"
"s_add_u32 m0, s50, 53248\n"
"s_nop 0\n"
"global_load_lds_dwordx4 v1, s[42:43]\n"
"s_add_u32 m0, s50, 54272\n"
"s_nop 0\n"
"global_load_lds_dwordx4 v2, s[42:43]\n"
"s_add_u32 s42, s46, 768\n"
"s_addc_u32 s43, s47, 0\n"
"s_add_u32 m0, s50, 0\n"
"s_nop 0\n"
"global_load_lds_dwordx4 v0, s[42:43]\n"
"s_add_u32 m0, s50, 1024\n"
"s_nop 0\n"
"global_load_lds_dwordx4 v1, s[42:43]\n"
"s_add_u32 m0, s50, 2048\n"
"s_nop 0\n"
"global_load_lds_dwordx4 v2, s[42:43]\n"
"s_waitcnt vmcnt(9)\n"
"s_barrier\n"
"s_add_u32 s42, s46, 37632\n"
"s_addc_u32 s43, s47, 0\n"
"s_add_u32 m0, s50, 3072\n"
"s_nop 0\n"
"global_load_lds_dwordx4 v0, s[42:43]\n"
"s_add_u32 m0, s50, 4096\n"
"s_nop 0\n"
"global_load_lds_dwordx4 v1, s[42:43]\n"
"s_add_u32 m0, s50, 5120\n"
"s_nop 0\n"
"global_load_lds_dwordx4 v2, s[42:43]\n"
"s_add_u32 s42, s46, 74496\n"
"s_addc_u32 s43, s47, 0\n"
"s_add_u32 m0, s50, 4294918144\n"
"s_nop 0\n"
"global_load_lds_dwordx4 v0, s[42:43]\n"
"s_add_u32 m0, s50, 4294919168\n"
"s_nop 0\n"
"global_load_lds_dwordx4 v1, s[42:43]\n"
"s_add_u32 m0, s50, 4294920192\n"
"s_nop 0\n"
"global_load_lds_dwordx4 v2, s[42:43]\n"
"s_waitcnt vmcnt(9)\n"
"s_barrier\n"
"s_setprio 2\n"
"s_add_u32 s42, s46, 111360\n"
"s_addc_u32 s43, s47, 0\n"
"s_add_u32 m0, s50, 4294921216\n"
"s_nop 0\n"
"global_load_lds_dwordx4 v0, s[42:43]\n"
"s_setprio 0\n"
"s_add_u32 m0, s50, 4294922240\n"
"s_nop 0\n"
"global_load_lds_dwordx4 v1, s[42:43]\n"
"s_add_u32 m0, s50, 4294923264\n"
"s_nop 0\n"
"global_load_lds_dwordx4 v2, s[42:43]\n"
"s_barrier\n"
"s_add_u32 s42, s46, 148224\n"
"s_addc_u32 s43, s47, 0\n"
"s_add_u32 m0, s50, 24576\n"
"s_nop 0\n"
"global_load_lds_dwordx4 v0, s[42:43]\n"
"s_add_u32 m0, s50, 25600\n"
"s_nop 0\n"
"global_load_lds_dwordx4 v1, s[42:43]\n"
"s_add_u32 m0, s50, 26624\n"
"s_nop 0\n"
"global_load_lds_dwordx4 v2, s[42:43]\n"
"ds_read_b128 v[180:183], v12 offset:24576\n"
"s_add_u32 s42, s46, 185088\n"
"s_addc_u32 s43, s47, 0\n"
"s_add_u32 m0, s50, 27648\n"
"s_nop 0\n"
"global_load_lds_dwordx4 v0, s[42:43]\n"
"s_add_u32 m0, s50, 28672\n"
"s_nop 0\n"
"global_load_lds_dwordx4 v1, s[42:43]\n"
"s_add_u32 m0, s50, 29696\n"
"s_nop 0\n"
"global_load_lds_dwordx4 v2, s[42:43]\n"
"s_waitcnt vmcnt(15)\n"
"ds_read_b64_tr_b16 v[26:27], v10 offset:29504\n"
"ds_read_b128 v[188:191], v12 offset:36864\n"
"s_andn2_b32 s41, 576, s55\n"
"v_add_u32 v178, s41, v15\n"
"ds_read_b128 v[184:187], v12 offset:30720\n"
"ds_read_u16 v178, v178 offset:224\n"
"ds_read_b64_tr_b16 v[24:25], v10 offset:28736\n"
"ds_read_u16 v23, v21 offset:1152\n"
"ds_read_b64_tr_b16 v[34:35], v10 offset:29472\n"
"ds_read_b64_tr_b16 v[28:29], v10 offset:28672\n"
"s_waitcnt lgkmcnt(4)\n"
"v_mad_u32_u24 v178, v178, s40, v16\n"
"s_add_u32 m0, s51, 102400\n"
"s_nop 0\n"
"global_load_lds_dwordx4 v178, s[56:57]\n"
"s_add_u32 m0, s51, 104448\n"
"s_nop 0\n"
"global_load_lds_dwordx4 v178, s[58:59]\n"
"ds_read_b64_tr_b16 v[32:33], v10 offset:28704\n"
"s_add_u32 m0, s51, 106496\n"
"s_nop 0\n"
"global_load_lds_dwordx4 v178, s[60:61]\n"
"ds_read_u16 v22, v21 offset:1184\n"
"s_waitcnt vmcnt(15)\n"
"ds_read_b64_tr_b16 v[30:31], v10 offset:29440\n"
"ds_read_b64_tr_b16 v[38:39], v10 offset:32576\n"
"s_and_b32 s41, 32, s55\n"
"v_add_u32 v178, s41, v15\n"
"ds_read_u16 v178, v178 offset:352\n"
"ds_read_b64_tr_b16 v[46:47], v10 offset:32544\n"
"s_waitcnt lgkmcnt(4)\n"
"v_mad_u32_u24 v176, v22, s40, v18\n"
"ds_read_b64_tr_b16 v[42:43], v10 offset:32512\n"
"ds_read_b128 v[192:195], v13 offset:24576\n"
"s_waitcnt lgkmcnt(3)\n"
"v_mad_u32_u24 v178, v178, s40, v16\n"
"s_add_u32 m0, s51, 114688\n"
"s_nop 0\n"
"global_load_lds_dwordx4 v178, s[56:57]\n"
"s_add_u32 m0, s51, 116736\n"
"s_nop 0\n"
"global_load_lds_dwordx4 v178, s[58:59]\n"
"s_add_u32 m0, s51, 118784\n"
"s_nop 0\n"
"global_load_lds_dwordx4 v178, s[60:61]\n"
"v_mfma_f32_16x16x32_bf16 v[196:199], v[180:183], v[28:31], 0\n"
"ds_read_b64_tr_b16 v[44:45], v10 offset:31776\n"
"v_mad_u32_u24 v22, v22, s40, v19\n"
"v_mfma_f32_16x16x32_bf16 v[200:203], v[180:183], v[32:35], 0\n"
"s_waitcnt vmcnt(15)\n"
"ds_read_b64_tr_b16 v[40:41], v10 offset:31744\n"
"v_mfma_f32_16x16x32_bf16 v[204:207], v[180:183], v[24:27], 0\n"
"ds_read_b128 v[180:183], v13 offset:30720\n"
"v_mfma_f32_16x16x32_bf16 v[212:215], v[184:187], v[28:31], 0\n"
"v_mad_u32_u24 v177, v23, s40, v18\n"
"v_mfma_f32_16x16x32_bf16 v[216:219], v[184:187], v[32:35], 0\n"
"ds_read_b64_tr_b16 v[58:59], v179 offset:4928\n"
"v_mfma_f32_16x16x32_bf16 v[220:223], v[184:187], v[24:27], 0\n"
"v_mfma_f32_16x16x32_bf16 v[224:227], v[188:191], v[28:31], 0\n"
"ds_read_b64_tr_b16 v[36:37], v10 offset:31808\n"
"v_mfma_f32_16x16x32_bf16 v[228:231], v[188:191], v[32:35], 0\n"
"ds_read_b64_tr_b16 v[50:51], v179 offset:4864\n"
"v_mfma_f32_16x16x32_bf16 v[232:235], v[188:191], v[24:27], 0\n"
"s_waitcnt lgkmcnt(4)\n"
"ds_read_b128 v[188:191], v12 offset:26624\n"
"ds_read_b128 v[184:187], v13 offset:36864\n"
"v_mfma_f32_16x16x32_bf16 v[196:199], v[192:195], v[40:43], v[196:199]\n"
"ds_read_b64_tr_b16 v[48:49], v179 offset:4096\n"
"v_mad_u32_u24 v23, v23, s40, v19\n"
"v_mfma_f32_16x16x32_bf16 v[200:203], v[192:195], v[44:47], v[200:203]\n"
"s_waitcnt vmcnt(12)\n"
"s_waitcnt lgkmcnt(4)\n"
"v_mfma_f32_16x16x32_bf16 v[204:207], v[192:195], v[36:39], v[204:207]\n"
"ds_read_b64_tr_b16 v[68:69], v179 offset:7232\n"
"ds_read_b64_tr_b16 v[60:61], v179 offset:7168\n"
"v_mfma_f32_16x16x32_bf16 v[212:215], v[180:183], v[40:43], v[212:215]\n"
"ds_read_b64_tr_b16 v[52:53], v179 offset:4128\n"
"v_mfma_f32_16x16x32_bf16 v[216:219], v[180:183], v[44:47], v[216:219]\n"
"ds_read_b64_tr_b16 v[54:55], v179 offset:4896\n"
"v_mfma_f32_16x16x32_bf16 v[220:223], v[180:183], v[36:39], v[220:223]\n"
"s_waitcnt lgkmcnt(5)\n"
"ds_read_b128 v[192:195], v12 offset:32768\n"
"v_mfma_f32_16x16x32_bf16 v[224:227], v[184:187], v[40:43], v[224:227]\n"
"ds_read_b64_tr_b16 v[64:65], v179 offset:7200\n"
"ds_read_b64_tr_b16 v[56:57], v179 offset:4160\n"
"v_mfma_f32_16x16x32_bf16 v[228:231], v[184:187], v[44:47], v[228:231]\n"
"v_mfma_f32_16x16x32_bf16 v[232:235], v[184:187], v[36:39], v[232:235]\n"
"s_waitcnt lgkmcnt(7)\n"
"ds_read_b128 v[180:183], v12 offset:38912\n"
"v_mfma_f32_16x16x32_bf16 v[196:199], v[188:191], v[48:51], v[196:199]\n"
"s_waitcnt lgkmcnt(4)\n"
"ds_read_b64_tr_b16 v[70:71], v179 offset:8000\n"
"v_mfma_f32_16x16x32_bf16 v[200:203], v[188:191], v[52:55], v[200:203]\n"
"s_waitcnt lgkmcnt(2)\n"
"ds_read_b128 v[184:187], v13 offset:26624\n"
"ds_read_b64_tr_b16 v[62:63], v179 offset:7936\n"
"v_mfma_f32_16x16x32_bf16 v[204:207], v[188:191], v[56:59], v[204:207]\n"
"ds_read_b128 v[188:191], v13 offset:32768\n"
"v_mfma_f32_16x16x32_bf16 v[212:215], v[192:195], v[48:51], v[212:215]\n"
"s_waitcnt vmcnt(9)\n"
"ds_read_b64_tr_b16 v[66:67], v179 offset:7968\n"
"v_mfma_f32_16x16x32_bf16 v[216:219], v[192:195], v[52:55], v[216:219]\n"
"ds_read_b64_tr_b16 v[82:83], v10 offset:54080\n"
"v_mfma_f32_16x16x32_bf16 v[220:223], v[192:195], v[56:59], v[220:223]\n"
"s_waitcnt lgkmcnt(6)\n"
"v_mfma_f32_16x16x32_bf16 v[224:227], v[180:183], v[48:51], v[224:227]\n"
"ds_read_b64_tr_b16 v[80:81], v10 offset:53312\n"
"v_mfma_f32_16x16x32_bf16 v[228:231], v[180:183], v[52:55], v[228:231]\n"
"ds_read_b128 v[192:195], v13 offset:38912\n"
"v_mfma_f32_16x16x32_bf16 v[232:235], v[180:183], v[56:59], v[232:235]\n"
"s_waitcnt lgkmcnt(5)\n"
"ds_read_b64_tr_b16 v[74:75], v10 offset:54016\n"
"ds_read_b128 v[178:181], v12 offset:28672\n"
"v_mfma_f32_16x16x32_bf16 v[196:199], v[184:187], v[60:63], v[196:199]\n"
"s_waitcnt lgkmcnt(5)\n"
"ds_read_b64_tr_b16 v[76:77], v10 offset:53280\n"
"v_mfma_f32_16x16x32_bf16 v[200:203], v[184:187], v[64:67], v[200:203]\n"
"ds_read_b64_tr_b16 v[78:79], v10 offset:54048\n"
"v_mfma_f32_16x16x32_bf16 v[204:207], v[184:187], v[68:71], v[204:207]\n"
"ds_read_b128 v[182:185], v12 offset:34816\n"
"v_mfma_f32_16x16x32_bf16 v[212:215], v[188:191], v[60:63], v[212:215]\n"
"s_barrier\n"
"v_mfma_f32_16x16x32_bf16 v[216:219], v[188:191], v[64:67], v[216:219]\n"
"ds_read_b64_tr_b16 v[72:73], v10 offset:53248\n"
"v_mfma_f32_16x16x32_bf16 v[220:223], v[188:191], v[68:71], v[220:223]\n"
"s_waitcnt lgkmcnt(6)\n"
"ds_read_b128 v[186:189], v12 offset:40960\n"
"v_mfma_f32_16x16x32_bf16 v[224:227], v[192:195], v[60:63], v[224:227]\n"
"s_waitcnt vmcnt(6)\n"
"ds_read_b64_tr_b16 v[92:93], v10 offset:56384\n"
"v_mfma_f32_16x16x32_bf16 v[228:231], v[192:195], v[64:67], v[228:231]\n"
"ds_read_b64_tr_b16 v[84:85], v10 offset:56320\n"
"v_mfma_f32_16x16x32_bf16 v[232:235], v[192:195], v[68:71], v[232:235]\n"
"s_waitcnt lgkmcnt(3)\n"
"ds_read_b64_tr_b16 v[94:95], v10 offset:57152\n"
"v_mfma_f32_16x16x32_bf16 v[196:199], v[178:181], v[72:75], v[196:199]\n"
"ds_read_b64_tr_b16 v[86:87], v10 offset:57088\n"
"v_mfma_f32_16x16x32_bf16 v[200:203], v[178:181], v[76:79], v[200:203]\n"
"ds_read_b64_tr_b16 v[88:89], v10 offset:56352\n"
"v_mfma_f32_16x16x32_bf16 v[204:207], v[178:181], v[80:83], v[204:207]\n"
"ds_read_b64_tr_b16 v[90:91], v10 offset:57120\n"
"v_mfma_f32_16x16x32_bf16 v[212:215], v[182:185], v[72:75], v[212:215]\n"
"v_mfma_f32_16x16x32_bf16 v[216:219], v[182:185], v[76:79], v[216:219]\n"
"ds_read_b128 v[190:193], v13 offset:28672\n"
"v_mfma_f32_16x16x32_bf16 v[220:223], v[182:185], v[80:83], v[220:223]\n"
"s_waitcnt lgkmcnt(7)\n"
"ds_read_b128 v[238:241], v13 offset:40960\n"
"v_mfma_f32_16x16x32_bf16 v[224:227], v[186:189], v[72:75], v[224:227]\n"
"v_mfma_f32_16x16x32_bf16 v[228:231], v[186:189], v[76:79], v[228:231]\n"
"ds_read_b128 v[178:181], v13 offset:34816\n"
"v_mfma_f32_16x16x32_bf16 v[232:235], v[186:189], v[80:83], v[232:235]\n"
"s_waitcnt lgkmcnt(2)\n"
"v_mfma_f32_16x16x32_bf16 v[196:199], v[190:193], v[84:87], v[196:199]\n"
"v_mfma_f32_16x16x32_bf16 v[200:203], v[190:193], v[88:91], v[200:203]\n"
"s_barrier\n"
"v_mfma_f32_16x16x32_bf16 v[204:207], v[190:193], v[92:95], v[204:207]\n"
"s_waitcnt lgkmcnt(0)\n"
"s_setprio 2\n"
"v_mfma_f32_16x16x32_bf16 v[212:215], v[178:181], v[84:87], v[212:215]\n"
"ds_read_b128 v[242:245], v12 offset:43008\n"
"v_mfma_f32_16x16x32_bf16 v[216:219], v[178:181], v[88:91], v[216:219]\n"
"s_nop 0\n"
"v_cvt_pk_bf16_f32 v210, v200, v201\n"
"v_mfma_f32_16x16x32_bf16 v[220:223], v[178:181], v[92:95], v[220:223]\n"
"v_cvt_pk_bf16_f32 v237, v206, v207\n"
"v_mfma_f32_16x16x32_bf16 v[224:227], v[238:241], v[84:87], v[224:227]\n"
"s_nop 0\n"
"v_cvt_pk_bf16_f32 v187, v214, v215\n"
"v_cvt_pk_bf16_f32 v211, v202, v203\n"
"v_mfma_f32_16x16x32_bf16 v[228:231], v[238:241], v[88:91], v[228:231]\n"
"ds_read_b128 v[188:191], v13 offset:43008\n"
"v_cvt_pk_bf16_f32 v185, v198, v199\n"
"v_mfma_f32_16x16x32_bf16 v[232:235], v[238:241], v[92:95], v[232:235]\n"
"s_waitcnt lgkmcnt(1)\n"
"v_cvt_pk_bf16_f32 v184, v196, v197\n"
"v_cvt_pk_bf16_f32 v236, v204, v205\n"
"v_mfma_f32_16x16x32_bf16 v[200:203], v[242:245], v[28:31], 0\n"
"v_cvt_pk_bf16_f32 v186, v212, v213\n"
"v_cvt_pk_bf16_f32 v212, v216, v217\n"
"v_mfma_f32_16x16x32_bf16 v[204:207], v[242:245], v[32:35], 0\n"
"ds_read_b128 v[178:181], v12 offset:45056\n"
"v_cvt_pk_bf16_f32 v195, v226, v227\n"
"v_mfma_f32_16x16x32_bf16 v[214:217], v[242:245], v[24:27], 0\n"
"s_waitcnt lgkmcnt(1)\n"
"ds_read_b128 v[196:199], v13 offset:45056\n"
"v_mfma_f32_16x16x32_bf16 v[200:203], v[188:191], v[40:43], v[200:203]\n"
"v_cvt_pk_bf16_f32 v194, v224, v225\n"
"v_mfma_f32_16x16x32_bf16 v[204:207], v[188:191], v[44:47], v[204:207]\n"
"v_cvt_pk_bf16_f32 v238, v220, v221\n"
"v_cvt_pk_bf16_f32 v220, v228, v229\n"
"v_mfma_f32_16x16x32_bf16 v[214:217], v[188:191], v[36:39], v[214:217]\n"
"s_waitcnt lgkmcnt(1)\n"
"ds_read_b128 v[188:191], v13 offset:47104\n"
"v_mfma_f32_16x16x32_bf16 v[200:203], v[178:181], v[48:51], v[200:203]\n"
"v_cvt_pk_bf16_f32 v239, v222, v223\n"
"v_mfma_f32_16x16x32_bf16 v[204:207], v[178:181], v[52:55], v[204:207]\n"
"ds_read_b128 v[240:243], v12 offset:47104\n"
"v_cvt_pk_bf16_f32 v247, v234, v235\n"
"v_mfma_f32_16x16x32_bf16 v[214:217], v[178:181], v[56:59], v[214:217]\n"
"s_waitcnt lgkmcnt(2)\n"
"v_cvt_pk_bf16_f32 v213, v218, v219\n"
"v_mfma_f32_16x16x32_bf16 v[200:203], v[196:199], v[60:63], v[200:203]\n"
"s_setprio 0\n"
"v_mfma_f32_16x16x32_bf16 v[204:207], v[196:199], v[64:67], v[204:207]\n"
"v_mfma_f32_16x16x32_bf16 v[214:217], v[196:199], v[68:71], v[214:217]\n"
"s_waitcnt lgkmcnt(0)\n"
"v_cvt_pk_bf16_f32 v246, v232, v233\n"
"v_mfma_f32_16x16x32_bf16 v[200:203], v[240:243], v[72:75], v[200:203]\n"
"v_cvt_pk_bf16_f32 v221, v230, v231\n"
"v_mfma_f32_16x16x32_bf16 v[204:207], v[240:243], v[76:79], v[204:207]\n"
"ds_read_b128 v[178:181], v12 offset:49152\n"
"v_mfma_f32_16x16x32_bf16 v[214:217], v[240:243], v[80:83], v[214:217]\n"
"v_mfma_f32_16x16x32_bf16 v[200:203], v[188:191], v[84:87], v[200:203]\n"
"ds_read_b128 v[224:227], v13 offset:49152\n"
"v_mfma_f32_16x16x32_bf16 v[204:207], v[188:191], v[88:91], v[204:207]\n"
"ds_read_b128 v[228:231], v12 offset:51200\n"
"v_mfma_f32_16x16x32_bf16 v[214:217], v[188:191], v[92:95], v[214:217]\n"
"s_waitcnt lgkmcnt(2)\n"
"ds_read_b128 v[188:191], v13 offset:51200\n"
"v_mfma_f32_16x16x32_bf16 v[232:235], v[178:181], v[28:31], 0\n"
"s_nop 0\n"
"v_cvt_pk_bf16_f32 v196, v200, v201\n"
"v_mfma_f32_16x16x32_bf16 v[240:243], v[178:181], v[32:35], 0\n"
"v_cvt_pk_bf16_f32 v197, v202, v203\n"
"v_mfma_f32_16x16x32_bf16 v[250:253], v[178:181], v[24:27], 0\n"
"s_waitcnt lgkmcnt(2)\n"
"v_mfma_f32_16x16x32_bf16 v[232:235], v[224:227], v[40:43], v[232:235]\n"
"v_cvt_pk_bf16_f32 v249, v216, v217\n"
"v_mfma_f32_16x16x32_bf16 v[240:243], v[224:227], v[44:47], v[240:243]\n"
"v_cvt_pk_bf16_f32 v222, v204, v205\n"
"v_mfma_f32_16x16x32_bf16 v[250:253], v[224:227], v[36:39], v[250:253]\n"
"s_waitcnt lgkmcnt(1)\n"
"v_mfma_f32_16x16x32_bf16 v[232:235], v[228:231], v[48:51], v[232:235]\n"
"v_cvt_pk_bf16_f32 v223, v206, v207\n"
"v_mfma_f32_16x16x32_bf16 v[240:243], v[228:231], v[52:55], v[240:243]\n"
"ds_read_b128 v[178:181], v12 offset:53248\n"
"v_mfma_f32_16x16x32_bf16 v[250:253], v[228:231], v[56:59], v[250:253]\n"
"s_waitcnt lgkmcnt(1)\n"
"v_mfma_f32_16x16x32_bf16 v[232:235], v[188:191], v[60:63], v[232:235]\n"
"ds_read_b128 v[198:201], v13 offset:53248\n"
"v_mfma_f32_16x16x32_bf16 v[240:243], v[188:191], v[64:67], v[240:243]\n"
"v_mfma_f32_16x16x32_bf16 v[250:253], v[188:191], v[68:71], v[250:253]\n"
"s_waitcnt lgkmcnt(1)\n"
"v_cvt_pk_bf16_f32 v248, v214, v215\n"
"v_mfma_f32_16x16x32_bf16 v[232:235], v[178:181], v[72:75], v[232:235]\n"
"ds_read_b128 v[202:205], v12 offset:55296\n"
"v_mfma_f32_16x16x32_bf16 v[240:243], v[178:181], v[76:79], v[240:243]\n"
"v_mfma_f32_16x16x32_bf16 v[250:253], v[178:181], v[80:83], v[250:253]\n"
"s_waitcnt lgkmcnt(1)\n"
"ds_read_b128 v[178:181], v12 offset:57344\n"
"v_mfma_f32_16x16x32_bf16 v[232:235], v[198:201], v[84:87], v[232:235]\n"
"v_mfma_f32_16x16x32_bf16 v[240:243], v[198:201], v[88:91], v[240:243]\n"
"ds_read_b128 v[190:193], v13 offset:55296\n"
"v_mfma_f32_16x16x32_bf16 v[250:253], v[198:201], v[92:95], v[250:253]\n"
"s_waitcnt lgkmcnt(2)\n"
"v_mfma_f32_16x16x32_bf16 v[206:209], v[202:205], v[28:31], 0\n"
"v_mfma_f32_16x16x32_bf16 v[216:219], v[202:205], v[32:35], 0\n"
"ds_read_b128 v[198:201], v13 offset:57344\n"
"v_mfma_f32_16x16x32_bf16 v[224:227], v[202:205], v[24:27], 0\n"
"s_waitcnt lgkmcnt(1)\n"
"ds_read_b128 v[202:205], v12 offset:59392\n"
"v_mfma_f32_16x16x32_bf16 v[206:209], v[190:193], v[40:43], v[206:209]\n"
"v_mfma_f32_16x16x32_bf16 v[216:219], v[190:193], v[44:47], v[216:219]\n"
"v_mfma_f32_16x16x32_bf16 v[224:227], v[190:193], v[36:39], v[224:227]\n"
"v_cvt_pk_bf16_f32 v188, v232, v233\n"
"v_mfma_f32_16x16x32_bf16 v[206:209], v[178:181], v[48:51], v[206:209]\n"
"ds_read_b128 v[190:193], v13 offset:59392\n"
"v_mfma_f32_16x16x32_bf16 v[216:219], v[178:181], v[52:55], v[216:219]\n"
"v_cvt_pk_bf16_f32 v214, v240, v241\n"
"v_mfma_f32_16x16x32_bf16 v[224:227], v[178:181], v[56:59], v[224:227]\n"
"s_waitcnt lgkmcnt(2)\n"
"v_mfma_f32_16x16x32_bf16 v[206:209], v[198:201], v[60:63], v[206:209]\n"
"v_mfma_f32_16x16x32_bf16 v[216:219], v[198:201], v[64:67], v[216:219]\n"
"v_cvt_pk_bf16_f32 v241, v252, v253\n"
"v_cvt_pk_bf16_f32 v215, v242, v243\n"
"v_mfma_f32_16x16x32_bf16 v[224:227], v[198:201], v[68:71], v[224:227]\n"
"s_waitcnt lgkmcnt(0)\n"
"s_waitcnt vmcnt(0)\n"
"s_barrier\n"
"v_mfma_f32_16x16x32_bf16 v[206:209], v[202:205], v[72:75], v[206:209]\n"
"v_cvt_pk_bf16_f32 v189, v234, v235\n"
"v_mfma_f32_16x16x32_bf16 v[216:219], v[202:205], v[76:79], v[216:219]\n"
"v_cvt_pk_bf16_f32 v240, v250, v251\n"
"v_mfma_f32_16x16x32_bf16 v[224:227], v[202:205], v[80:83], v[224:227]\n"
"s_setprio 2\n"
"v_mfma_f32_16x16x32_bf16 v[206:209], v[190:193], v[84:87], v[206:209]\n"
"ds_read_b128 v[180:183], v12 offset:6144\n"
"s_and_b32 s41, 160, s55\n"
"v_add_u32 v2, s41, v15\n"
"v_mfma_f32_16x16x32_bf16 v[216:219], v[190:193], v[88:91], v[216:219]\n"
"ds_read_u16 v2, v2 offset:96\n"
"v_mfma_f32_16x16x32_bf16 v[224:227], v[190:193], v[92:95], v[224:227]\n"
"ds_read_b128 v[190:193], v13 offset:6144\n"
"s_nop 0\n"
"v_cvt_pk_bf16_f32 v179, v208, v209\n"
"s_nop 2\n"
"v_cvt_pk_bf16_f32 v204, v216, v217\n"
"ds_read_b128 v[198:201], v12 offset:8192\n"
"s_waitcnt lgkmcnt(2)\n"
"v_mad_u32_u24 v2, v2, s40, v16\n"
"v_cvt_pk_bf16_f32 v230, v224, v225\n"
"v_cvt_pk_bf16_f32 v178, v206, v207\n"
"v_mfma_f32_16x16x32_bf16 v[206:209], v[180:183], v[28:31], 0\n"
"v_cvt_pk_bf16_f32 v231, v226, v227\n"
"v_cvt_pk_bf16_f32 v205, v218, v219\n"
"v_mfma_f32_16x16x32_bf16 v[216:219], v[180:183], v[32:35], 0\n"
"s_add_u32 m0, s51, 126976\n"
"s_nop 0\n"
"global_load_lds_dwordx4 v2, s[56:57]\n"
"v_mfma_f32_16x16x32_bf16 v[232:235], v[180:183], v[24:27], 0\n"
"s_waitcnt lgkmcnt(1)\n"
"v_mfma_f32_16x16x32_bf16 v[206:209], v[190:193], v[40:43], v[206:209]\n"
"s_add_u32 m0, s51, 129024\n"
"s_nop 0\n"
"global_load_lds_dwordx4 v2, s[58:59]\n"
"s_add_u32 m0, s51, 131072\n"
"s_nop 0\n"
"global_load_lds_dwordx4 v2, s[60:61]\n"
"v_mfma_f32_16x16x32_bf16 v[216:219], v[190:193], v[44:47], v[216:219]\n"
"s_and_b32 s41, 32, s55\n"
"v_add_u32 v2, s41, v15\n"
"v_mfma_f32_16x16x32_bf16 v[232:235], v[190:193], v[36:39], v[232:235]\n"
"s_waitcnt lgkmcnt(0)\n"
"ds_read_b128 v[180:183], v13 offset:8192\n"
"v_mfma_f32_16x16x32_bf16 v[206:209], v[198:201], v[48:51], v[206:209]\n"
"ds_read_b128 v[190:193], v12 offset:10240\n"
"ds_read_u16 v2, v2 offset:832\n"
"v_mfma_f32_16x16x32_bf16 v[216:219], v[198:201], v[52:55], v[216:219]\n"
"v_mfma_f32_16x16x32_bf16 v[232:235], v[198:201], v[56:59], v[232:235]\n"
"s_waitcnt lgkmcnt(2)\n"
"v_mfma_f32_16x16x32_bf16 v[206:209], v[180:183], v[60:63], v[206:209]\n"
"s_waitcnt lgkmcnt(0)\n"
"v_mad_u32_u24 v2, v2, s40, v16\n"
"v_mfma_f32_16x16x32_bf16 v[216:219], v[180:183], v[64:67], v[216:219]\n"
"v_mfma_f32_16x16x32_bf16 v[232:235], v[180:183], v[68:71], v[232:235]\n"
"v_mfma_f32_16x16x32_bf16 v[206:209], v[190:193], v[72:75], v[206:209]\n"
"ds_read_b128 v[198:201], v13 offset:10240\n"
"s_add_u32 m0, s51, 139264\n"
"s_nop 0\n"
"global_load_lds_dwordx4 v2, s[56:57]\n"
"v_mfma_f32_16x16x32_bf16 v[216:219], v[190:193], v[76:79], v[216:219]\n"
"v_mfma_f32_16x16x32_bf16 v[232:235], v[190:193], v[80:83], v[232:235]\n"
"s_add_u32 m0, s51, 141312\n"
"s_nop 0\n"
"global_load_lds_dwordx4 v2, s[58:59]\n"
"s_waitcnt lgkmcnt(0)\n"
"v_mfma_f32_16x16x32_bf16 v[206:209], v[198:201], v[84:87], v[206:209]\n"
"s_add_u32 m0, s51, 143360\n"
"s_nop 0\n"
"global_load_lds_dwordx4 v2, s[60:61]\n"
"ds_read_b128 v[180:183], v12 offset:12288\n"
"v_mfma_f32_16x16x32_bf16 v[216:219], v[198:201], v[88:91], v[216:219]\n"
"s_andn2_b32 s41, 288, s55\n"
"v_add_u32 v2, s41, v15\n"
"ds_read_b128 v[190:193], v13 offset:12288\n"
"v_mfma_f32_16x16x32_bf16 v[232:235], v[198:201], v[92:95], v[232:235]\n"
"s_waitcnt lgkmcnt(1)\n"
"ds_read_u16 v2, v2 offset:128\n"
"v_mfma_f32_16x16x32_bf16 v[200:203], v[180:183], v[28:31], 0\n"
"s_nop 0\n"
"v_cvt_pk_bf16_f32 v225, v218, v219\n"
"ds_read_b128 v[226:229], v12 offset:14336\n"
"s_setprio 0\n"
"v_mfma_f32_16x16x32_bf16 v[242:245], v[180:183], v[32:35], 0\n"
"v_mfma_f32_16x16x32_bf16 v[252:255], v[180:183], v[24:27], 0\n"
"s_waitcnt lgkmcnt(2)\n"
"v_cvt_pk_bf16_f32 v198, v206, v207\n"
"v_mfma_f32_16x16x32_bf16 v[200:203], v[190:193], v[40:43], v[200:203]\n"
"v_mfma_f32_16x16x32_bf16 v[242:245], v[190:193], v[44:47], v[242:245]\n"
"v_mfma_f32_16x16x32_bf16 v[252:255], v[190:193], v[36:39], v[252:255]\n"
"s_waitcnt lgkmcnt(1)\n"
"ds_read_b128 v[190:193], v12 offset:16384\n"
"v_mad_u32_u24 v2, v2, s40, v16\n"
"s_add_u32 m0, s51, 151552\n"
"s_nop 0\n"
"global_load_lds_dwordx4 v2, s[56:57]\n"
"s_waitcnt lgkmcnt(1)\n"
"v_mfma_f32_16x16x32_bf16 v[200:203], v[226:229], v[48:51], v[200:203]\n"
"v_cvt_pk_bf16_f32 v251, v234, v235\n"
"s_add_u32 m0, s51, 153600\n"
"s_nop 0\n"
"global_load_lds_dwordx4 v2, s[58:59]\n"
"s_add_u32 m0, s51, 155648\n"
"s_nop 0\n"
"global_load_lds_dwordx4 v2, s[60:61]\n"
"ds_read_b128 v[180:183], v13 offset:14336\n"
"s_barrier\n"
"v_mfma_f32_16x16x32_bf16 v[242:245], v[226:229], v[52:55], v[242:245]\n"
"v_cvt_pk_bf16_f32 v250, v232, v233\n"
"v_mfma_f32_16x16x32_bf16 v[252:255], v[226:229], v[56:59], v[252:255]\n"
"s_add_u32 s42, s48, 0\n"
"s_addc_u32 s43, s49, 0\n"
"s_add_u32 m0, s50, 24576\n"
"s_nop 0\n"
"global_load_lds_dwordx4 v3, s[42:43]\n"
"s_waitcnt lgkmcnt(0)\n"
"v_mfma_f32_16x16x32_bf16 v[200:203], v[180:183], v[60:63], v[200:203]\n"
"s_add_u32 m0, s50, 25600\n"
"s_nop 0\n"
"global_load_lds_dwordx4 v4, s[42:43]\n"
"v_mfma_f32_16x16x32_bf16 v[242:245], v[180:183], v[64:67], v[242:245]\n"
"v_cvt_pk_bf16_f32 v199, v208, v209\n"
"v_cvt_pk_bf16_f32 v224, v216, v217\n"
"ds_read_b128 v[206:209], v13 offset:16384\n"
"v_mfma_f32_16x16x32_bf16 v[252:255], v[180:183], v[68:71], v[252:255]\n"
"v_mfma_f32_16x16x32_bf16 v[200:203], v[190:193], v[72:75], v[200:203]\n"
"s_add_u32 m0, s50, 26624\n"
"s_nop 0\n"
"global_load_lds_dwordx4 v5, s[42:43]\n"
"ds_read_b128 v[216:219], v12 offset:20480\n"
"ds_read_b128 v[180:183], v12 offset:18432\n"
"v_mfma_f32_16x16x32_bf16 v[242:245], v[190:193], v[76:79], v[242:245]\n"
"v_mfma_f32_16x16x32_bf16 v[252:255], v[190:193], v[80:83], v[252:255]\n"
"s_waitcnt lgkmcnt(2)\n"
"ds_read_b128 v[190:193], v13 offset:18432\n"
"v_mfma_f32_16x16x32_bf16 v[200:203], v[206:209], v[84:87], v[200:203]\n"
"v_mfma_f32_16x16x32_bf16 v[242:245], v[206:209], v[88:91], v[242:245]\n"
"v_mfma_f32_16x16x32_bf16 v[252:255], v[206:209], v[92:95], v[252:255]\n"
"s_waitcnt lgkmcnt(1)\n"
"v_mfma_f32_16x16x32_bf16 v[226:229], v[180:183], v[28:31], 0\n"
"v_mfma_f32_16x16x32_bf16 v[112:115], v[180:183], v[32:35], 0\n"
"s_nop 3\n"
"v_cvt_pk_bf16_f32 v206, v242, v243\n"
"v_mfma_f32_16x16x32_bf16 v[116:119], v[180:183], v[24:27], 0\n"
"s_waitcnt lgkmcnt(0)\n"
"v_cvt_pk_bf16_f32 v180, v200, v201\n"
"v_cvt_pk_bf16_f32 v181, v202, v203\n"
"v_cvt_pk_bf16_f32 v207, v244, v245\n"
"v_mfma_f32_16x16x32_bf16 v[226:229], v[190:193], v[40:43], v[226:229]\n"
"s_add_u32 s42, s48, 18432\n"
"s_addc_u32 s43, s49, 0\n"
"s_add_u32 m0, s50, 27648\n"
"s_nop 0\n"
"global_load_lds_dwordx4 v3, s[42:43]\n"
"v_mfma_f32_16x16x32_bf16 v[112:115], v[190:193], v[44:47], v[112:115]\n"
"v_cvt_pk_bf16_f32 v232, v252, v253\n"
"ds_read_b128 v[104:107], v13 offset:20480\n"
"v_mfma_f32_16x16x32_bf16 v[116:119], v[190:193], v[36:39], v[116:119]\n"
"v_mfma_f32_16x16x32_bf16 v[226:229], v[216:219], v[48:51], v[226:229]\n"
"s_add_u32 m0, s50, 28672\n"
"s_nop 0\n"
"global_load_lds_dwordx4 v4, s[42:43]\n"
"v_mfma_f32_16x16x32_bf16 v[112:115], v[216:219], v[52:55], v[112:115]\n"
"v_mfma_f32_16x16x32_bf16 v[116:119], v[216:219], v[56:59], v[116:119]\n"
"s_waitcnt lgkmcnt(0)\n"
"ds_read_b128 v[190:193], v12 offset:22528\n"
"v_mfma_f32_16x16x32_bf16 v[226:229], v[104:107], v[60:63], v[226:229]\n"
"s_add_u32 m0, s50, 29696\n"
"s_nop 0\n"
"global_load_lds_dwordx4 v5, s[42:43]\n"
"v_cvt_pk_bf16_f32 v233, v254, v255\n"
"s_waitcnt vmcnt(9)\n"
"s_barrier\n"
"s_waitcnt vmcnt(3)\n"
"ds_read_b64_tr_b16 v[254:255], v10 offset:54080\n"
"v_mfma_f32_16x16x32_bf16 v[112:115], v[104:107], v[64:67], v[112:115]\n"
"ds_read_b64_tr_b16 v[100:101], v11 offset:53256\n"
"ds_read_b64_tr_b16 v[98:99], v11 offset:54016\n"
"ds_read_b64_tr_b16 v[252:253], v10 offset:53312\n"
"v_mfma_f32_16x16x32_bf16 v[116:119], v[104:107], v[68:71], v[116:119]\n"
"s_waitcnt lgkmcnt(4)\n"
"v_mfma_f32_16x16x32_bf16 v[226:229], v[190:193], v[72:75], v[226:229]\n"
"v_mov_b32 v219, 0x3f803f80\n"
"ds_read_b64_tr_b16 v[96:97], v11 offset:53248\n"
"v_mfma_f32_16x16x32_bf16 v[112:115], v[190:193], v[76:79], v[112:115]\n"
"ds_read_b128 v[200:203], v13 offset:22528\n"
"ds_read_b64_tr_b16 v[102:103], v11 offset:54024\n"
"s_waitcnt vmcnt(0)\n"
"ds_read_b64_tr_b16 v[104:105], v11 offset:56320\n"
"ds_read_b64_tr_b16 v[108:109], v11 offset:56328\n"
"v_mov_b32 v217, 0x3f803f80\n"
"v_mov_b32 v218, 0x3f803f80\n"
"v_mfma_f32_16x16x32_bf16 v[116:119], v[190:193], v[80:83], v[116:119]\n"
"s_waitcnt lgkmcnt(2)\n"
"s_add_u32 s42, s48, 36864\n"
"s_addc_u32 s43, s49, 0\n"
"s_add_u32 m0, s50, 24576\n"
"s_nop 0\n"
"global_load_lds_dwordx4 v3, s[42:43]\n"
"v_mfma_f32_16x16x32_bf16 v[226:229], v[200:203], v[84:87], v[226:229]\n"
"s_nop 7\n"
"v_cvt_pk_bf16_f32 v182, v226, v227\n"
"v_cvt_pk_bf16_f32 v183, v228, v229\n"
"v_mov_b32 v193, 0\n"
"ds_read_b64_tr_b16 v[138:139], v10 offset:57152\n"
"v_mov_b32 v216, 0x3f803f80\n"
"ds_read_b64_tr_b16 v[106:107], v11 offset:57088\n"
"v_mfma_f32_16x16x32_bf16 v[112:115], v[200:203], v[88:91], v[112:115]\n"
"s_add_u32 m0, s50, 25600\n"
"s_nop 0\n"
"global_load_lds_dwordx4 v4, s[42:43]\n"
"ds_read_b64_tr_b16 v[136:137], v10 offset:56384\n"
"ds_read_b64_tr_b16 v[110:111], v11 offset:57096\n"
"v_mfma_f32_16x16x32_bf16 v[116:119], v[200:203], v[92:95], v[116:119]\n"
"s_nop 1\n"
"v_cvt_pk_bf16_f32 v208, v112, v113\n"
"s_add_u32 m0, s50, 26624\n"
"s_nop 0\n"
"global_load_lds_dwordx4 v5, s[42:43]\n"
"s_waitcnt lgkmcnt(0)\n"
"s_add_u32 s42, s48, 55296\n"
"s_addc_u32 s43, s49, 0\n"
"s_add_u32 m0, s50, 27648\n"
"s_nop 0\n"
"global_load_lds_dwordx4 v3, s[42:43]\n"
"s_add_u32 m0, s50, 28672\n"
"s_nop 0\n"
"global_load_lds_dwordx4 v4, s[42:43]\n"
"s_add_u32 m0, s50, 29696\n"
"s_nop 0\n"
"global_load_lds_dwordx4 v5, s[42:43]\n"
"s_barrier\n"
"v_cvt_pk_bf16_f32 v209, v114, v115\n"
"v_cvt_pk_bf16_f32 v235, v118, v119\n"
"ds_read2st64_b32 v[190:191], v9 offset0:112 offset1:113\n"
"s_waitcnt vmcnt(3)\n"
"ds_read_b64_tr_b16 v[2:3], v11 offset:54016\n"
"ds_read_b64_tr_b16 v[114:115], v11 offset:54024\n"
"ds_read_b32 v192, v9 offset:29184\n"
"ds_read_b64_tr_b16 v[112:113], v11 offset:53256\n"
"s_waitcnt lgkmcnt(1)\n"
"v_mfma_f32_16x16x32_bf16 v[226:229], v[216:219], v[190:193], 0\n"
"ds_read_b64_tr_b16 v[146:147], v10 offset:54080\n"
"ds_read2st64_b32 v[200:201], v9 offset0:116 offset1:117\n"
"v_mfma_f32_16x16x32_bf16 v[140:143], v[186:189], v[190:193], 0\n"
"ds_read_b64_tr_b16 v[144:145], v10 offset:53312\n"
"ds_read2st64_b32 v[202:203], v9 offset0:118 offset1:119\n"
"v_cvt_pk_bf16_f32 v234, v116, v117\n"
"v_mfma_f32_16x16x32_bf16 v[242:245], v[212:215], v[190:193], 0\n"
"ds_read_b64_tr_b16 v[0:1], v11 offset:53248\n"
"v_mfma_f32_16x16x32_bf16 v[124:127], v[238:241], v[190:193], 0\n"
"s_waitcnt lgkmcnt(1)\n"
"ds_read2st64_b32 v[192:193], v9 offset0:130 offset1:131\n"
"v_mfma_f32_16x16x32_bf16 v[226:229], v[216:219], v[200:203], v[226:229]\n"
"v_mfma_f32_16x16x32_bf16 v[140:143], v[194:197], v[200:203], v[140:143]\n"
"ds_read_b32 v130, v9 offset:9216\n"
"ds_read2st64_b32 v[190:191], v9 offset0:128 offset1:129\n"
"s_waitcnt vmcnt(0)\n"
"ds_read_b64_tr_b16 v[118:119], v11 offset:57088\n"
"v_mfma_f32_16x16x32_bf16 v[242:245], v[220:223], v[200:203], v[242:245]\n"
"ds_read_b64_tr_b16 v[120:121], v11 offset:56328\n"
"ds_read_b64_tr_b16 v[116:117], v11 offset:56320\n"
"v_mfma_f32_16x16x32_bf16 v[124:127], v[246:249], v[200:203], v[124:127]\n"
"s_waitcnt lgkmcnt(6)\n"
"s_add_u32 s42, s48, 12288\n"
"s_addc_u32 s43, s49, 0\n"
"s_add_u32 m0, s50, 24576\n"
"s_nop 0\n"
"global_load_lds_dwordx4 v6, s[42:43]\n"
"ds_read_b32 v200, v9 offset:33792\n"
"v_mov_b32 v131, 0\n"
"s_waitcnt lgkmcnt(4)\n"
"v_mfma_f32_16x16x32_bf16 v[226:229], v[216:219], v[190:193], v[226:229]\n"
"s_setprio 2\n"
"v_mov_b32 v201, 0\n"
"ds_read2st64_b32 v[202:203], v9 offset0:134 offset1:135\n"
"s_add_u32 m0, s50, 25600\n"
"s_nop 0\n"
"global_load_lds_dwordx4 v7, s[42:43]\n"
"v_mfma_f32_16x16x32_bf16 v[140:143], v[178:181], v[190:193], v[140:143]\n"
"v_mfma_f32_16x16x32_bf16 v[242:245], v[204:207], v[190:193], v[242:245]\n"
"ds_read_b64_tr_b16 v[150:151], v10 offset:57152\n"
"v_mov_b32 v129, 0\n"
"v_mfma_f32_16x16x32_bf16 v[124:127], v[230:233], v[190:193], v[124:127]\n"
"v_mov_b32 v128, 0\n"
"s_add_u32 m0, s50, 26624\n"
"s_nop 0\n"
"global_load_lds_dwordx4 v8, s[42:43]\n"
"ds_read_b64_tr_b16 v[148:149], v10 offset:56384\n"
"s_waitcnt lgkmcnt(2)\n"
"v_mfma_f32_16x16x32_bf16 v[226:229], v[216:219], v[200:203], v[226:229]\n"
"v_mfma_f32_16x16x32_bf16 v[226:229], v[216:219], v[128:131], v[226:229]\n"
"ds_read_b64_tr_b16 v[122:123], v11 offset:57096\n"
"s_nop 6\n"
"v_rcp_f32 v227, v226\n"
"v_mfma_f32_16x16x32_bf16 v[124:127], v[234:237], v[200:203], v[124:127]\n"
"ds_read_b128 v[190:193], v12 offset:24576\n"
"v_add_u32 v226, 0xd4800000, v226\n"
"v_mfma_f32_16x16x32_bf16 v[124:127], v[248:251], v[128:131], v[124:127]\n"
"s_waitcnt lgkmcnt(1)\n"
"s_add_u32 s42, s48, 49152\n"
"s_addc_u32 s43, s49, 0\n"
"s_add_u32 m0, s50, 27648\n"
"s_nop 0\n"
"global_load_lds_dwordx4 v6, s[42:43]\n"
"v_mfma_f32_16x16x32_bf16 v[140:143], v[182:185], v[200:203], v[140:143]\n"
"s_nop 0\n"
"v_mul_f32 v127, v127, v227\n"
"v_mfma_f32_16x16x32_bf16 v[140:143], v[196:199], v[128:131], v[140:143]\n"
"v_cmp_lt_u32 vcc, 0x28000000, v226\n"
"v_mul_f32 v126, v126, v227\n"
"v_mfma_f32_16x16x32_bf16 v[242:245], v[208:211], v[200:203], v[242:245]\n"
"ds_read_b128 v[200:203], v12 offset:26624\n"
"v_mul_f32 v125, v125, v227\n"
"v_mfma_f32_16x16x32_bf16 v[242:245], v[222:225], v[128:131], v[242:245]\n"
"s_waitcnt lgkmcnt(1)\n"
"ds_read_b128 v[216:219], v13 offset:24576\n"
"v_mfma_f32_16x16x32_bf16 v[128:131], v[190:193], v[28:31], 0\n"
"v_mul_f32 v141, v141, v227\n"
"v_mul_f32 v124, v124, v227\n"
"v_cvt_pk_bf16_f32 v236, v124, v125\n"
"s_or_b64 s[62:63], s[62:63], vcc\n"
"s_add_u32 m0, s50, 28672\n"
"s_nop 0\n"
"global_load_lds_dwordx4 v7, s[42:43]\n"
"s_add_u32 m0, s50, 29696\n"
"s_nop 0\n"
"global_load_lds_dwordx4 v8, s[42:43]\n"
"v_cvt_pk_bf16_f32 v237, v126, v127\n"
"v_mfma_f32_16x16x32_bf16 v[132:135], v[190:193], v[32:35], 0\n"
"s_andn2_b32 s41, 608, s55\n"
"v_add_u32 v8, s41, v15\n"
"v_mul_f32 v143, v143, v227\n"
"ds_read_u16 v8, v8 offset:288\n"
"v_mfma_f32_16x16x32_bf16 v[152:155], v[190:193], v[24:27], 0\n"
"s_waitcnt lgkmcnt(1)\n"
"v_mul_f32 v140, v140, v227\n"
"v_mfma_f32_16x16x32_bf16 v[128:131], v[216:219], v[40:43], v[128:131]\n"
"ds_read_b128 v[190:193], v13 offset:26624\n"
"v_mul_f32 v142, v142, v227\n"
"v_mfma_f32_16x16x32_bf16 v[132:135], v[216:219], v[44:47], v[132:135]\n"
"v_mul_f32 v242, v242, v227\n"
"v_cvt_pk_bf16_f32 v140, v140, v141\n"
"v_mfma_f32_16x16x32_bf16 v[152:155], v[216:219], v[36:39], v[152:155]\n"
"ds_read_b128 v[216:219], v12 offset:28672\n"
"v_mul_f32 v244, v244, v227\n"
"v_mfma_f32_16x16x32_bf16 v[128:131], v[200:203], v[48:51], v[128:131]\n"
"s_waitcnt lgkmcnt(2)\n"
"s_setprio 0\n"
"v_mul_f32 v243, v243, v227\n"
"s_setprio 2\n"
"v_mad_u32_u24 v8, v8, s40, v16\n"
"v_mfma_f32_16x16x32_bf16 v[132:135], v[200:203], v[52:55], v[132:135]\n"
"v_mul_f32 v245, v245, v227\n"
"v_mfma_f32_16x16x32_bf16 v[152:155], v[200:203], v[56:59], v[152:155]\n"
"s_waitcnt lgkmcnt(1)\n"
"ds_read_b128 v[200:203], v13 offset:28672\n"
"v_mfma_f32_16x16x32_bf16 v[128:131], v[190:193], v[60:63], v[128:131]\n"
"s_add_u32 m0, s51, 114688\n"
"s_nop 0\n"
"global_load_lds_dwordx4 v8, s[56:57]\n"
"v_mfma_f32_16x16x32_bf16 v[132:135], v[190:193], v[64:67], v[132:135]\n"
"v_cvt_pk_bf16_f32 v141, v142, v143\n"
"v_cvt_pk_bf16_f32 v143, v244, v245\n"
"v_mfma_f32_16x16x32_bf16 v[152:155], v[190:193], v[68:71], v[152:155]\n"
"s_waitcnt lgkmcnt(1)\n"
"s_setprio 0\n"
"ds_read_b128 v[190:193], v12 offset:30720\n"
"v_mfma_f32_16x16x32_bf16 v[128:131], v[216:219], v[72:75], v[128:131]\n"
"v_mfma_f32_16x16x32_bf16 v[132:135], v[216:219], v[76:79], v[132:135]\n"
"v_mfma_f32_16x16x32_bf16 v[152:155], v[216:219], v[80:83], v[152:155]\n"
"s_waitcnt lgkmcnt(1)\n"
"v_mfma_f32_16x16x32_bf16 v[128:131], v[200:203], v[84:87], v[128:131]\n"
"v_cvt_pk_bf16_f32 v142, v242, v243\n"
"v_mfma_f32_16x16x32_bf16 v[132:135], v[200:203], v[88:91], v[132:135]\n"
"s_add_u32 m0, s51, 116736\n"
"s_nop 0\n"
"global_load_lds_dwordx4 v8, s[58:59]\n"
"v_mfma_f32_16x16x32_bf16 v[152:155], v[200:203], v[92:95], v[152:155]\n"
"s_waitcnt lgkmcnt(0)\n"
"ds_read_b128 v[200:203], v12 offset:32768\n"
"v_mfma_f32_16x16x32_bf16 v[156:159], v[190:193], v[28:31], 0\n"
"ds_read_b128 v[226:229], v13 offset:30720\n"
"v_mfma_f32_16x16x32_bf16 v[160:163], v[190:193], v[32:35], 0\n"
"s_add_u32 m0, s51, 118784\n"
"s_nop 0\n"
"global_load_lds_dwordx4 v8, s[60:61]\n"
"v_mfma_f32_16x16x32_bf16 v[164:167], v[190:193], v[24:27], 0\n"
"s_waitcnt vmcnt(21)\n"
"s_barrier\n"
"s_waitcnt vmcnt(6)\n"
"ds_read_b64_tr_b16 v[6:7], v11 offset:54016\n"
"v_cvt_pk_bf16_f32 v242, v152, v153\n"
"ds_read_b128 v[168:171], v13 offset:32768\n"
"s_waitcnt lgkmcnt(2)\n"
"v_mfma_f32_16x16x32_bf16 v[156:159], v[226:229], v[40:43], v[156:159]\n"
"v_cvt_pk_bf16_f32 v190, v128, v129\n"
"v_cvt_pk_bf16_f32 v243, v154, v155\n"
"s_mov_b32 m0, s54\n"
"ds_read_b64_tr_b16 v[4:5], v11 offset:53248\n"
"v_cvt_pk_bf16_f32 v216, v132, v133\n"
"v_mfma_f32_16x16x32_bf16 v[160:163], v[226:229], v[44:47], v[160:163]\n"
"v_cvt_pk_bf16_f32 v217, v134, v135\n"
"v_mfma_f32_16x16x32_bf16 v[164:167], v[226:229], v[36:39], v[164:167]\n"
"v_cvt_pk_bf16_f32 v191, v130, v131\n"
"ds_read_b64_tr_b16 v[226:227], v10 offset:53312\n"
"ds_read_b64_tr_b16 v[126:127], v11 offset:54024\n"
"v_mfma_f32_16x16x32_bf16 v[156:159], v[200:203], v[48:51], v[156:159]\n"
"ds_read_b64_tr_b16 v[124:125], v11 offset:53256\n"
"ds_read_b64_tr_b16 v[228:229], v10 offset:54080\n"
"v_mfma_f32_16x16x32_bf16 v[160:163], v[200:203], v[52:55], v[160:163]\n"
"s_waitcnt vmcnt(3)\n"
"ds_read_b64_tr_b16 v[128:129], v11 offset:56320\n"
"ds_read_b64_tr_b16 v[152:153], v10 offset:56384\n"
"ds_read_b64_tr_b16 v[132:133], v11 offset:56328\n"
"v_mfma_f32_16x16x32_bf16 v[164:167], v[200:203], v[56:59], v[164:167]\n"
"s_waitcnt lgkmcnt(8)\n"
"ds_read_b64_tr_b16 v[134:135], v11 offset:57096\n"
"ds_read_b64_tr_b16 v[154:155], v10 offset:57152\n"
"v_mfma_f32_16x16x32_bf16 v[156:159], v[168:171], v[60:63], v[156:159]\n"
"s_and_b32 s41, 480, s55\n"
"v_add_u32 v8, s41, v15\n"
"ds_write_addtid_b32 v236 offset:33280\n"
"ds_read_b64_tr_b16 v[130:131], v11 offset:57088\n"
"v_mfma_f32_16x16x32_bf16 v[160:163], v[168:171], v[64:67], v[160:163]\n"
"ds_read_b128 v[200:203], v12 offset:34816\n"
"ds_write_addtid_b32 v141 offset:32512\n"
"ds_write_addtid_b32 v237 offset:33536\n"
"v_mfma_f32_16x16x32_bf16 v[164:167], v[168:171], v[68:71], v[164:167]\n"
"ds_write_addtid_b32 v143 offset:33024\n"
"ds_read_u16 v8, v8 offset:448\n"
"ds_write_addtid_b32 v140 offset:32256\n"
"s_waitcnt lgkmcnt(6)\n"
"ds_write_b128 v17, v[252:255] offset:53248\n"
"ds_read_b128 v[252:255], v13 offset:34816\n"
"s_waitcnt lgkmcnt(7)\n"
"v_mfma_f32_16x16x32_bf16 v[156:159], v[200:203], v[72:75], v[156:159]\n"
"ds_write_addtid_b32 v142 offset:32768\n"
"ds_write_b128 v17, v[136:139] offset:54272\n"
"v_mfma_f32_16x16x32_bf16 v[160:163], v[200:203], v[76:79], v[160:163]\n"
"ds_write_b128 v17, v[144:147] offset:55296\n"
"v_mfma_f32_16x16x32_bf16 v[164:167], v[200:203], v[80:83], v[164:167]\n"
"s_waitcnt lgkmcnt(3)\n"
"v_mov_b32 v137, 0x3f803f80\n"
"v_mfma_f32_16x16x32_bf16 v[156:159], v[252:255], v[84:87], v[156:159]\n"
"s_waitcnt lgkmcnt(2)\n"
"s_waitcnt vmcnt(0)\n"
"s_barrier\n"
"v_mov_b32 v136, 0x3f803f80\n"
"v_mfma_f32_16x16x32_bf16 v[160:163], v[252:255], v[88:91], v[160:163]\n"
"ds_read2st64_b32 v[140:141], v9 offset0:120 offset1:121\n"
"v_mfma_f32_16x16x32_bf16 v[164:167], v[252:255], v[92:95], v[164:167]\n"
"ds_write_b128 v17, v[148:151] offset:56320\n"
"s_nop 6\n"
"v_cvt_pk_bf16_f32 v252, v164, v165\n"
"v_mov_b32 v138, 0x3f803f80\n"
"v_mov_b32 v139, 0x3f803f80\n"
"ds_write_b128 v17, v[226:229] offset:57344\n"
"v_cvt_pk_bf16_f32 v226, v160, v161\n"
"v_mad_u32_u24 v8, v8, s40, v16\n"
"v_cvt_pk_bf16_f32 v200, v156, v157\n"
"ds_read2st64_b32 v[150:151], v9 offset0:128 offset1:129\n"
"s_add_u32 m0, s51, 126976\n"
"s_nop 0\n"
"global_load_lds_dwordx4 v8, s[56:57]\n"
"ds_read2st64_b32 v[144:145], v9 offset0:124 offset1:125\n"
"ds_read2st64_b32 v[142:143], v9 offset0:122 offset1:123\n"
"v_cvt_pk_bf16_f32 v201, v158, v159\n"
"ds_write_b128 v17, v[152:155] offset:58368\n"
"s_waitcnt lgkmcnt(1)\n"
"v_mfma_f32_16x16x32_bf16 v[156:159], v[136:139], v[140:143], 0\n"
"ds_read2st64_b32 v[146:147], v9 offset0:126 offset1:127\n"
"v_cvt_pk_bf16_f32 v227, v162, v163\n"
"v_mfma_f32_16x16x32_bf16 v[160:163], v[186:189], v[140:143], 0\n"
"s_add_u32 m0, s51, 129024\n"
"s_nop 0\n"
"global_load_lds_dwordx4 v8, s[58:59]\n"
"ds_read2st64_b32 v[152:153], v9 offset0:130 offset1:131\n"
"v_cvt_pk_bf16_f32 v253, v166, v167\n"
"v_mov_b32 v154, 0\n"
"v_mfma_f32_16x16x32_bf16 v[164:167], v[212:215], v[140:143], 0\n"
"ds_read2st64_b32 v[172:173], v14 offset0:150 offset1:151\n"
"v_mfma_f32_16x16x32_bf16 v[168:171], v[238:241], v[140:143], 0\n"
"s_waitcnt lgkmcnt(2)\n"
"s_setprio 2\n"
"v_mfma_f32_16x16x32_bf16 v[156:159], v[136:139], v[144:147], v[156:159]\n"
"s_waitcnt lgkmcnt(1)\n"
"v_mov_b32 v155, 0\n"
"v_mfma_f32_16x16x32_bf16 v[156:159], v[136:139], v[150:153], v[156:159]\n"
"v_mfma_f32_16x16x32_bf16 v[168:171], v[250:253], v[144:147], v[168:171]\n"
"s_add_u32 m0, s51, 131072\n"
"s_nop 0\n"
"global_load_lds_dwordx4 v8, s[60:61]\n"
"s_and_b32 s41, 32, s55\n"
"v_add_u32 v255, s41, v15\n"
"v_mov_b32 v148, 0\n"
"v_mov_b32 v149, 0\n"
"s_nop 1\n"
"v_mfma_f32_16x16x32_bf16 v[168:171], v[240:243], v[148:151], v[168:171]\n"
"ds_read2st64_b32 v[138:139], v14 offset0:128 offset1:129\n"
"v_mfma_f32_16x16x32_bf16 v[168:171], v[248:251], v[152:155], v[168:171]\n"
"v_mfma_f32_16x16x32_bf16 v[160:163], v[198:201], v[144:147], v[160:163]\n"
"v_rcp_f32 v8, v156\n"
"v_mfma_f32_16x16x32_bf16 v[160:163], v[188:191], v[148:151], v[160:163]\n"
"ds_read_b128 v[140:143], v17 offset:53248\n"
"ds_read2st64_b32 v[136:137], v14 offset0:126 offset1:127\n"
"v_mfma_f32_16x16x32_bf16 v[160:163], v[196:199], v[152:155], v[160:163]\n"
"ds_read2st64_b32 v[174:175], v14 offset0:152 offset1:153\n"
"v_add_u32 v156, 0xd4800000, v156\n"
"v_mfma_f32_16x16x32_bf16 v[164:167], v[224:227], v[144:147], v[164:167]\n"
"v_cmp_lt_u32 vcc, 0x28000000, v156\n"
"v_mul_f32 v169, v169, v8\n"
"v_mfma_f32_16x16x32_bf16 v[164:167], v[214:217], v[148:151], v[164:167]\n"
"v_mul_f32 v168, v168, v8\n"
"ds_read_b128 v[148:151], v17 offset:54272\n"
"ds_read_u16 v255, v255 offset:480\n"
"v_mfma_f32_16x16x32_bf16 v[164:167], v[222:225], v[152:155], v[164:167]\n"
"s_waitcnt lgkmcnt(3)\n"
"v_mul_f32 v161, v161, v8\n"
"v_cvt_pk_bf16_f32 v244, v168, v169\n"
"v_mfma_f32_16x16x32_bf16 v[144:147], v[96:99], v[136:139], 0\n"
"ds_read2st64_b32 v[158:159], v14 offset0:176 offset1:177\n"
"v_mfma_f32_16x16x32_bf16 v[152:155], v[100:103], v[136:139], 0\n"
"s_or_b64 s[62:63], s[62:63], vcc\n"
"ds_read2st64_b32 v[156:157], v14 offset0:174 offset1:175\n"
"v_mul_f32 v171, v171, v8\n"
"v_mul_f32 v170, v170, v8\n"
"v_cvt_pk_bf16_f32 v245, v170, v171\n"
"v_mfma_f32_16x16x32_bf16 v[168:171], v[140:143], v[136:139], 0\n"
"s_waitcnt lgkmcnt(4)\n"
"ds_read2st64_b32 v[140:141], v14 offset0:198 offset1:199\n"
"v_mul_f32 v160, v160, v8\n"
"v_mfma_f32_16x16x32_bf16 v[144:147], v[104:107], v[172:175], v[144:147]\n"
"s_waitcnt lgkmcnt(3)\n"
"v_mad_u32_u24 v255, v255, s40, v16\n"
"v_mfma_f32_16x16x32_bf16 v[152:155], v[108:111], v[172:175], v[152:155]\n"
"v_mul_f32 v165, v165, v8\n"
"v_mul_f32 v162, v162, v8\n"
"s_add_u32 m0, s51, 139264\n"
"s_nop 0\n"
"global_load_lds_dwordx4 v255, s[56:57]\n"
"v_mfma_f32_16x16x32_bf16 v[168:171], v[148:151], v[172:175], v[168:171]\n"
"s_waitcnt lgkmcnt(1)\n"
"ds_read_b128 v[148:151], v17 offset:56320\n"
"v_mul_f32 v164, v164, v8\n"
"v_mfma_f32_16x16x32_bf16 v[144:147], v[0:3], v[156:159], v[144:147]\n"
"ds_read_b128 v[136:139], v17 offset:55296\n"
"v_cvt_pk_bf16_f32 v160, v160, v161\n"
"ds_read2st64_b32 v[142:143], v14 offset0:200 offset1:201\n"
"ds_read2st64_b32 v[172:173], v14 offset0:130 offset1:131\n"
"ds_read2st64_b32 v[174:175], v14 offset0:154 offset1:155\n"
"v_mfma_f32_16x16x32_bf16 v[152:155], v[112:115], v[156:159], v[152:155]\n"
"v_mul_f32 v167, v167, v8\n"
"s_setprio 0\n"
"v_mul_f32 v166, v166, v8\n"
"v_mul_f32 v163, v163, v8\n"
"s_add_u32 m0, s51, 141312\n"
"s_nop 0\n"
"global_load_lds_dwordx4 v255, s[58:59]\n"
"v_cvt_pk_bf16_f32 v161, v162, v163\n"
"v_cvt_pk_bf16_f32 v162, v164, v165\n"
"s_waitcnt lgkmcnt(3)\n"
"v_mfma_f32_16x16x32_bf16 v[168:171], v[136:139], v[156:159], v[168:171]\n"
"s_waitcnt lgkmcnt(2)\n"
"ds_read2st64_b32 v[158:159], v14 offset0:202 offset1:203\n"
"v_mfma_f32_16x16x32_bf16 v[144:147], v[116:119], v[140:143], v[144:147]\n"
"ds_read_b128 v[136:139], v17 offset:57344\n"
"v_mfma_f32_16x16x32_bf16 v[152:155], v[120:123], v[140:143], v[152:155]\n"
"ds_read2st64_b32 v[156:157], v14 offset0:178 offset1:179\n"
"v_mfma_f32_16x16x32_bf16 v[168:171], v[148:151], v[140:143], v[168:171]\n"
"s_waitcnt lgkmcnt(3)\n"
"ds_read_b128 v[140:143], v17 offset:58368\n"
"v_mfma_f32_16x16x32_bf16 v[144:147], v[4:7], v[172:175], v[144:147]\n"
"v_mfma_f32_16x16x32_bf16 v[152:155], v[124:127], v[172:175], v[152:155]\n"
"s_waitcnt lgkmcnt(2)\n"
"v_mfma_f32_16x16x32_bf16 v[168:171], v[136:139], v[172:175], v[168:171]\n"
"s_waitcnt lgkmcnt(1)\n"
"ds_read_b128 v[148:151], v12 offset:49152\n"
"v_cvt_pk_bf16_f32 v163, v166, v167\n"
"v_mfma_f32_16x16x32_bf16 v[144:147], v[128:131], v[156:159], v[144:147]\n"
"s_add_u32 m0, s51, 143360\n"
"s_nop 0\n"
"global_load_lds_dwordx4 v255, s[60:61]\n"
"v_mfma_f32_16x16x32_bf16 v[152:155], v[132:135], v[156:159], v[152:155]\n"
"s_barrier\n"
"s_nop 1\n"
"s_mov_b32 m0, s54\n"
"s_waitcnt lgkmcnt(1)\n"
"v_mfma_f32_16x16x32_bf16 v[168:171], v[140:143], v[156:159], v[168:171]\n"
"s_waitcnt lgkmcnt(0)\n"
"ds_read_b128 v[140:143], v12 offset:51200\n"
"ds_read_b128 v[136:139], v13 offset:49152\n"
"v_cmp_class_f32_e64 vcc, v144, s64\n"
"v_mfma_f32_16x16x32_bf16 v[156:159], v[148:151], v[28:31], 0\n"
"v_cvt_pk_bf16_f32 v144, v144, v145\n"
"v_cvt_pk_bf16_f32 v145, v146, v147\n"
"v_cvt_pk_bf16_f32 v146, v152, v153\n"
"ds_write_addtid_b32 v244 offset:33280\n"
"v_mfma_f32_16x16x32_bf16 v[164:167], v[148:151], v[32:35], 0\n"
"v_mfma_f32_16x16x32_bf16 v[172:175], v[148:151], v[24:27], 0\n"
"ds_write_addtid_b32 v245 offset:33536\n"
"s_waitcnt lgkmcnt(2)\n"
"v_mfma_f32_16x16x32_bf16 v[156:159], v[136:139], v[40:43], v[156:159]\n"
"ds_read_b128 v[148:151], v13 offset:51200\n"
"v_mfma_f32_16x16x32_bf16 v[164:167], v[136:139], v[44:47], v[164:167]\n"
"v_mfma_f32_16x16x32_bf16 v[172:175], v[136:139], v[36:39], v[172:175]\n"
"ds_write_addtid_b32 v161 offset:32512\n"
"v_mfma_f32_16x16x32_bf16 v[156:159], v[140:143], v[48:51], v[156:159]\n"
"ds_write_addtid_b32 v163 offset:33024\n"
"s_or_b64 s[62:63], s[62:63], vcc\n"
"v_mfma_f32_16x16x32_bf16 v[164:167], v[140:143], v[52:55], v[164:167]\n"
"ds_read_b128 v[136:139], v12 offset:53248\n"
"v_mfma_f32_16x16x32_bf16 v[172:175], v[140:143], v[56:59], v[172:175]\n"
"ds_read_b128 v[140:143], v13 offset:53248\n"
"ds_write_addtid_b32 v162 offset:32768\n"
"v_cvt_pk_bf16_f32 v168, v168, v169\n"
"s_waitcnt lgkmcnt(5)\n"
"v_mfma_f32_16x16x32_bf16 v[156:159], v[148:151], v[60:63], v[156:159]\n"
"ds_write_addtid_b32 v160 offset:32256\n"
"s_mov_b32 m0, s68\n"
"v_mfma_f32_16x16x32_bf16 v[164:167], v[148:151], v[64:67], v[164:167]\n"
"ds_write_addtid_b32 v146 offset:45568\n"
"v_mfma_f32_16x16x32_bf16 v[172:175], v[148:151], v[68:71], v[172:175]\n"
"s_waitcnt lgkmcnt(4)\n"
"v_cvt_pk_bf16_f32 v169, v170, v171\n"
"v_cvt_pk_bf16_f32 v147, v154, v155\n"
"v_mfma_f32_16x16x32_bf16 v[156:159], v[136:139], v[72:75], v[156:159]\n"
"ds_read_b128 v[148:151], v12 offset:55296\n"
"v_mfma_f32_16x16x32_bf16 v[164:167], v[136:139], v[76:79], v[164:167]\n"
"ds_write_addtid_b32 v145 offset:45312\n"
"v_mfma_f32_16x16x32_bf16 v[172:175], v[136:139], v[80:83], v[172:175]\n"
"s_waitcnt lgkmcnt(5)\n"
"ds_read_b128 v[136:139], v13 offset:55296\n"
"v_mfma_f32_16x16x32_bf16 v[156:159], v[140:143], v[84:87], v[156:159]\n"
"s_setprio 2\n"
"ds_write_addtid_b32 v169 offset:46336\n"
"v_mfma_f32_16x16x32_bf16 v[164:167], v[140:143], v[88:91], v[164:167]\n"
"ds_write_addtid_b32 v168 offset:46080\n"
"v_mfma_f32_16x16x32_bf16 v[172:175], v[140:143], v[92:95], v[172:175]\n"
"ds_write_addtid_b32 v144 offset:45056\n"
"ds_write_addtid_b32 v147 offset:45824\n"
"s_waitcnt lgkmcnt(6)\n"
"v_mfma_f32_16x16x32_bf16 v[144:147], v[148:151], v[28:31], 0\n"
"ds_read_b128 v[140:143], v12 offset:57344\n"
"v_cvt_pk_bf16_f32 v185, v158, v159\n"
"v_mfma_f32_16x16x32_bf16 v[152:155], v[148:151], v[32:35], 0\n"
"v_cvt_pk_bf16_f32 v211, v166, v167\n"
"v_cvt_pk_bf16_f32 v184, v156, v157\n"
"v_cvt_pk_bf16_f32 v210, v164, v165\n"
"v_mfma_f32_16x16x32_bf16 v[156:159], v[148:151], v[24:27], 0\n"
"s_waitcnt lgkmcnt(5)\n"
"ds_read_b128 v[148:151], v13 offset:57344\n"
"v_cvt_pk_bf16_f32 v236, v172, v173\n"
"v_mfma_f32_16x16x32_bf16 v[144:147], v[136:139], v[40:43], v[144:147]\n"
"v_cvt_pk_bf16_f32 v237, v174, v175\n"
"s_setprio 0\n"
"v_mfma_f32_16x16x32_bf16 v[152:155], v[136:139], v[44:47], v[152:155]\n"
"v_mfma_f32_16x16x32_bf16 v[156:159], v[136:139], v[36:39], v[156:159]\n"
"s_waitcnt lgkmcnt(1)\n"
"ds_read_b128 v[136:139], v12 offset:59392\n"
"v_mfma_f32_16x16x32_bf16 v[144:147], v[140:143], v[48:51], v[144:147]\n"
"v_mfma_f32_16x16x32_bf16 v[152:155], v[140:143], v[52:55], v[152:155]\n"
"v_mfma_f32_16x16x32_bf16 v[156:159], v[140:143], v[56:59], v[156:159]\n"
"s_waitcnt lgkmcnt(1)\n"
"ds_read_b128 v[140:143], v13 offset:59392\n"
"v_mfma_f32_16x16x32_bf16 v[144:147], v[148:151], v[60:63], v[144:147]\n"
"v_mfma_f32_16x16x32_bf16 v[152:155], v[148:151], v[64:67], v[152:155]\n"
"v_mfma_f32_16x16x32_bf16 v[156:159], v[148:151], v[68:71], v[156:159]\n"
"s_waitcnt lgkmcnt(1)\n"
"v_mov_b32 v151, 0x3f803f80\n"
"v_mfma_f32_16x16x32_bf16 v[144:147], v[136:139], v[72:75], v[144:147]\n"
"v_mov_b32 v150, 0x3f803f80\n"
"v_mfma_f32_16x16x32_bf16 v[152:155], v[136:139], v[76:79], v[152:155]\n"
"v_mfma_f32_16x16x32_bf16 v[156:159], v[136:139], v[80:83], v[156:159]\n"
"s_waitcnt lgkmcnt(0)\n"
"v_mov_b32 v149, 0x3f803f80\n"
"v_mfma_f32_16x16x32_bf16 v[144:147], v[140:143], v[84:87], v[144:147]\n"
"s_waitcnt vmcnt(3)\n"
"s_barrier\n"
"v_mfma_f32_16x16x32_bf16 v[152:155], v[140:143], v[88:91], v[152:155]\n"
"ds_read2st64_b32 v[136:137], v9 offset0:112 offset1:113\n"
"v_mfma_f32_16x16x32_bf16 v[156:159], v[140:143], v[92:95], v[156:159]\n"
"ds_read2st64_b32 v[138:139], v9 offset0:114 offset1:115\n"
"s_nop 1\n"
"v_cvt_pk_bf16_f32 v193, v146, v147\n"
"s_and_b32 s41, 416, s55\n"
"v_add_u32 v8, s41, v15\n"
"ds_read2st64_b32 v[162:163], v20 offset0:2 offset1:3\n"
"ds_read_u16 v255, v21 offset:800\n"
"v_mov_b32 v148, 0x3f803f80\n"
"v_cvt_pk_bf16_f32 v244, v156, v157\n"
"v_cvt_pk_bf16_f32 v245, v158, v159\n"
"ds_read_u16 v8, v8 offset:544\n"
"ds_read2st64_b32 v[160:161], v20 offset0:0 offset1:1\n"
"v_cvt_pk_bf16_f32 v218, v152, v153\n"
"v_cvt_pk_bf16_f32 v192, v144, v145\n"
"s_waitcnt lgkmcnt(4)\n"
"v_mfma_f32_16x16x32_bf16 v[144:147], v[148:151], v[136:139], 0\n"
"v_cvt_pk_bf16_f32 v219, v154, v155\n"
"ds_read2st64_b32 v[140:141], v9 offset0:116 offset1:117\n"
"v_mfma_f32_16x16x32_bf16 v[152:155], v[180:183], v[136:139], 0\n"
"ds_read2st64_b32 v[142:143], v9 offset0:118 offset1:119\n"
"v_mfma_f32_16x16x32_bf16 v[156:159], v[206:209], v[136:139], 0\n"
"s_waitcnt lgkmcnt(3)\n"
"v_mad_u32_u24 v8, v8, s40, v16\n"
"v_mfma_f32_16x16x32_bf16 v[164:167], v[232:235], v[136:139], 0\n"
"ds_read2st64_b32 v[136:137], v9 offset0:128 offset1:129\n"
"s_add_u32 m0, s51, 151552\n"
"s_nop 0\n"
"global_load_lds_dwordx4 v8, s[56:57]\n"
"s_waitcnt lgkmcnt(1)\n"
"v_mfma_f32_16x16x32_bf16 v[144:147], v[148:151], v[140:143], v[144:147]\n"
"ds_read2st64_b32 v[228:229], v20 offset0:4 offset1:5\n"
"v_mfma_f32_16x16x32_bf16 v[152:155], v[188:191], v[140:143], v[152:155]\n"
"ds_read2st64_b32 v[138:139], v9 offset0:130 offset1:131\n"
"v_mfma_f32_16x16x32_bf16 v[156:159], v[214:217], v[140:143], v[156:159]\n"
"v_mad_u32_u24 v254, v255, s40, v18\n"
"s_setprio 2\n"
"v_mfma_f32_16x16x32_bf16 v[164:167], v[240:243], v[140:143], v[164:167]\n"
"global_store_dwordx4 v254, v[160:163], s[66:67] sc1\n"
"v_mad_u32_u24 v255, v255, s40, v19\n"
"ds_read2st64_b32 v[140:141], v9 offset0:132 offset1:133\n"
"s_waitcnt lgkmcnt(1)\n"
"v_mfma_f32_16x16x32_bf16 v[144:147], v[148:151], v[136:139], v[144:147]\n"
"v_mov_b32 v160, 0\n"
"s_add_u32 m0, s51, 153600\n"
"s_nop 0\n"
"global_load_lds_dwordx4 v8, s[58:59]\n"
"ds_read2st64_b32 v[142:143], v9 offset0:134 offset1:135\n"
"v_mfma_f32_16x16x32_bf16 v[152:155], v[184:187], v[136:139], v[152:155]\n"
"ds_read2st64_b32 v[162:163], v9 offset0:36 offset1:37\n"
"v_mov_b32 v161, 0\n"
"v_mfma_f32_16x16x32_bf16 v[156:159], v[210:213], v[136:139], v[156:159]\n"
"global_store_dwordx2 v255, v[228:229], s[66:67] offset:256 sc1\n"
"v_mfma_f32_16x16x32_bf16 v[164:167], v[236:239], v[136:139], v[164:167]\n"
"s_waitcnt lgkmcnt(1)\n"
"ds_read2st64_b32 v[136:137], v14 offset0:126 offset1:127\n"
"v_mfma_f32_16x16x32_bf16 v[144:147], v[148:151], v[140:143], v[144:147]\n"
"s_waitcnt lgkmcnt(1)\n"
"v_mfma_f32_16x16x32_bf16 v[144:147], v[148:151], v[160:163], v[144:147]\n"
"ds_read2st64_b32 v[138:139], v14 offset0:128 offset1:129\n"
"v_mfma_f32_16x16x32_bf16 v[164:167], v[248:251], v[140:143], v[164:167]\n"
"s_add_u32 m0, s51, 155648\n"
"s_nop 0\n"
"global_load_lds_dwordx4 v8, s[60:61]\n"
"v_mfma_f32_16x16x32_bf16 v[164:167], v[250:253], v[160:163], v[164:167]\n"
"ds_read_b128 v[148:151], v17 offset:53248\n"
"v_mfma_f32_16x16x32_bf16 v[152:155], v[196:199], v[140:143], v[152:155]\n"
"v_rcp_f32 v8, v144\n"
"v_mfma_f32_16x16x32_bf16 v[152:155], v[198:201], v[160:163], v[152:155]\n"
"ds_read2st64_b32 v[170:171], v14 offset0:176 offset1:177\n"
"v_add_u32 v144, 0xd4800000, v144\n"
"v_mfma_f32_16x16x32_bf16 v[156:159], v[222:225], v[140:143], v[156:159]\n"
"s_nop 0\n"
"v_mul_f32 v166, v166, v8\n"
"v_mul_f32 v167, v167, v8\n"
"ds_read2st64_b32 v[142:143], v14 offset0:152 offset1:153\n"
"ds_read2st64_b32 v[140:141], v14 offset0:150 offset1:151\n"
"v_mfma_f32_16x16x32_bf16 v[156:159], v[224:227], v[160:163], v[156:159]\n"
"s_waitcnt lgkmcnt(4)\n"
"v_mul_f32 v165, v165, v8\n"
"v_cmp_lt_u32 vcc, 0x28000000, v144\n"
"v_mfma_f32_16x16x32_bf16 v[144:147], v[96:99], v[136:139], 0\n"
"v_mul_f32 v164, v164, v8\n"
"v_cvt_pk_bf16_f32 v254, v164, v165\n"
"ds_read_b128 v[160:163], v17 offset:54272\n"
"v_cvt_pk_bf16_f32 v255, v166, v167\n"
"v_mfma_f32_16x16x32_bf16 v[164:167], v[100:103], v[136:139], 0\n"
"s_waitcnt lgkmcnt(4)\n"
"ds_read2st64_b32 v[168:169], v14 offset0:174 offset1:175\n"
"v_mul_f32 v152, v152, v8\n"
"v_mfma_f32_16x16x32_bf16 v[172:175], v[148:151], v[136:139], 0\n"
"s_waitcnt lgkmcnt(2)\n"
"ds_read_b128 v[136:139], v17 offset:55296\n"
"v_mul_f32 v154, v154, v8\n"
"v_mfma_f32_16x16x32_bf16 v[144:147], v[104:107], v[140:143], v[144:147]\n"
"v_mul_f32 v153, v153, v8\n"
"v_mul_f32 v155, v155, v8\n"
"ds_read2st64_b32 v[148:149], v14 offset0:198 offset1:199\n"
"v_mul_f32 v159, v159, v8\n"
"v_mfma_f32_16x16x32_bf16 v[164:167], v[108:111], v[140:143], v[164:167]\n"
"s_waitcnt lgkmcnt(3)\n"
"ds_read2st64_b32 v[150:151], v14 offset0:200 offset1:201\n"
"v_mul_f32 v156, v156, v8\n"
"v_mfma_f32_16x16x32_bf16 v[172:175], v[160:163], v[140:143], v[172:175]\n"
"s_waitcnt lgkmcnt(3)\n"
"v_cvt_pk_bf16_f32 v152, v152, v153\n"
"ds_read_b128 v[140:143], v17 offset:56320\n"
"v_cvt_pk_bf16_f32 v153, v154, v155\n"
"v_mfma_f32_16x16x32_bf16 v[144:147], v[0:3], v[168:171], v[144:147]\n"
"ds_read2st64_b32 v[162:163], v14 offset0:202 offset1:203\n"
"v_mul_f32 v157, v157, v8\n"
"v_cvt_pk_bf16_f32 v154, v156, v157\n"
"v_mfma_f32_16x16x32_bf16 v[164:167], v[112:115], v[168:171], v[164:167]\n"
"v_mul_f32 v158, v158, v8\n"
"v_cvt_pk_bf16_f32 v155, v158, v159\n"
"ds_read2st64_b32 v[158:159], v14 offset0:154 offset1:155\n"
"s_or_b64 s[62:63], s[62:63], vcc\n"
"s_waitcnt lgkmcnt(5)\n"
"v_mfma_f32_16x16x32_bf16 v[172:175], v[136:139], v[168:171], v[172:175]\n"
"s_waitcnt lgkmcnt(3)\n"
"ds_read2st64_b32 v[156:157], v14 offset0:130 offset1:131\n"
"s_setprio 0\n"
"ds_read_b128 v[136:139], v17 offset:57344\n"
"v_mfma_f32_16x16x32_bf16 v[144:147], v[116:119], v[148:151], v[144:147]\n"
"v_mfma_f32_16x16x32_bf16 v[164:167], v[120:123], v[148:151], v[164:167]\n"
"s_waitcnt lgkmcnt(4)\n"
"ds_read2st64_b32 v[160:161], v14 offset0:178 offset1:179\n"
"v_mfma_f32_16x16x32_bf16 v[172:175], v[140:143], v[148:151], v[172:175]\n"
"s_waitcnt lgkmcnt(2)\n"
"v_mfma_f32_16x16x32_bf16 v[144:147], v[4:7], v[156:159], v[144:147]\n"
"v_mfma_f32_16x16x32_bf16 v[164:167], v[124:127], v[156:159], v[164:167]\n"
"s_waitcnt lgkmcnt(1)\n"
"ds_read_b128 v[148:151], v12 offset:18432\n"
"v_mfma_f32_16x16x32_bf16 v[172:175], v[136:139], v[156:159], v[172:175]\n"
"s_mov_b32 m0, s54\n"
"ds_read_b128 v[140:143], v17 offset:58368\n"
"s_waitcnt lgkmcnt(2)\n"
"v_mfma_f32_16x16x32_bf16 v[144:147], v[128:131], v[160:163], v[144:147]\n"
"s_waitcnt vmcnt(5)\n"
"s_barrier\n"
"v_mfma_f32_16x16x32_bf16 v[164:167], v[132:135], v[160:163], v[164:167]\n"
"s_waitcnt lgkmcnt(0)\n"
"ds_read_b128 v[156:159], v12 offset:20480\n"
"v_mfma_f32_16x16x32_bf16 v[172:175], v[140:143], v[160:163], v[172:175]\n"
"ds_write_addtid_b32 v255 offset:33536\n"
"s_nop 0\n"
"v_cmp_class_f32_e64 vcc, v144, s64\n"
"v_mfma_f32_16x16x32_bf16 v[140:143], v[148:151], v[28:31], 0\n"
"ds_write_addtid_b32 v153 offset:32512\n"
"ds_read_b128 v[136:139], v13 offset:18432\n"
"v_mfma_f32_16x16x32_bf16 v[160:163], v[148:151], v[32:35], 0\n"
"v_cvt_pk_bf16_f32 v144, v144, v145\n"
"v_cvt_pk_bf16_f32 v145, v146, v147\n"
"v_mfma_f32_16x16x32_bf16 v[168:171], v[148:151], v[24:27], 0\n"
"ds_write_addtid_b32 v254 offset:33280\n"
"v_cvt_pk_bf16_f32 v172, v172, v173\n"
"s_waitcnt lgkmcnt(1)\n"
"v_mfma_f32_16x16x32_bf16 v[140:143], v[136:139], v[40:43], v[140:143]\n"
"v_cvt_pk_bf16_f32 v173, v174, v175\n"
"v_cvt_pk_bf16_f32 v146, v164, v165\n"
"v_mfma_f32_16x16x32_bf16 v[160:163], v[136:139], v[44:47], v[160:163]\n"
"v_cvt_pk_bf16_f32 v147, v166, v167\n"
"v_mfma_f32_16x16x32_bf16 v[168:171], v[136:139], v[36:39], v[168:171]\n"
"ds_read_b128 v[136:139], v12 offset:22528\n"
"ds_read_b128 v[148:151], v13 offset:20480\n"
"v_mfma_f32_16x16x32_bf16 v[140:143], v[156:159], v[48:51], v[140:143]\n"
"ds_write_addtid_b32 v154 offset:32768\n"
"v_mfma_f32_16x16x32_bf16 v[160:163], v[156:159], v[52:55], v[160:163]\n"
"ds_write_addtid_b32 v152 offset:32256\n"
"v_mfma_f32_16x16x32_bf16 v[168:171], v[156:159], v[56:59], v[168:171]\n"
"ds_write_addtid_b32 v155 offset:33024\n"
"s_waitcnt lgkmcnt(3)\n"
"v_mfma_f32_16x16x32_bf16 v[140:143], v[148:151], v[60:63], v[140:143]\n"
"s_mov_b32 m0, s68\n"
"ds_read_b128 v[152:155], v13 offset:22528\n"
"ds_write_addtid_b32 v144 offset:45056\n"
"v_mfma_f32_16x16x32_bf16 v[160:163], v[148:151], v[64:67], v[160:163]\n"
"v_mfma_f32_16x16x32_bf16 v[168:171], v[148:151], v[68:71], v[168:171]\n"
"ds_write_addtid_b32 v172 offset:46080\n"
"v_mfma_f32_16x16x32_bf16 v[140:143], v[136:139], v[72:75], v[140:143]\n"
"ds_read_b128 v[148:151], v12 offset:24576\n"
"s_or_b64 s[62:63], s[62:63], vcc\n"
"v_mfma_f32_16x16x32_bf16 v[160:163], v[136:139], v[76:79], v[160:163]\n"
"ds_write_addtid_b32 v145 offset:45312\n"
"v_mfma_f32_16x16x32_bf16 v[168:171], v[136:139], v[80:83], v[168:171]\n"
"ds_write_addtid_b32 v147 offset:45824\n"
"s_waitcnt lgkmcnt(5)\n"
"v_mfma_f32_16x16x32_bf16 v[140:143], v[152:155], v[84:87], v[140:143]\n"
"ds_write_addtid_b32 v146 offset:45568\n"
"v_mfma_f32_16x16x32_bf16 v[160:163], v[152:155], v[88:91], v[160:163]\n"
"ds_write_addtid_b32 v173 offset:46336\n"
"v_mfma_f32_16x16x32_bf16 v[168:171], v[152:155], v[92:95], v[168:171]\n"
"s_waitcnt lgkmcnt(4)\n"
"ds_read_b128 v[152:155], v12 offset:26624\n"
"ds_read_b128 v[136:139], v13 offset:24576\n"
"v_mfma_f32_16x16x32_bf16 v[144:147], v[148:151], v[28:31], 0\n"
"v_cvt_pk_bf16_f32 v203, v142, v143\n"
"v_mfma_f32_16x16x32_bf16 v[156:159], v[148:151], v[32:35], 0\n"
"v_mfma_f32_16x16x32_bf16 v[164:167], v[148:151], v[24:27], 0\n"
"s_waitcnt lgkmcnt(0)\n"
"v_cvt_pk_bf16_f32 v202, v140, v141\n"
"v_mfma_f32_16x16x32_bf16 v[144:147], v[136:139], v[40:43], v[144:147]\n"
"ds_read_b128 v[140:143], v13 offset:26624\n"
"v_mfma_f32_16x16x32_bf16 v[156:159], v[136:139], v[44:47], v[156:159]\n"
"v_mfma_f32_16x16x32_bf16 v[164:167], v[136:139], v[36:39], v[164:167]\n"
"ds_read_b128 v[136:139], v12 offset:28672\n"
"v_mfma_f32_16x16x32_bf16 v[144:147], v[152:155], v[48:51], v[144:147]\n"
"v_cvt_pk_bf16_f32 v255, v170, v171\n"
"v_mfma_f32_16x16x32_bf16 v[156:159], v[152:155], v[52:55], v[156:159]\n"
"v_mfma_f32_16x16x32_bf16 v[164:167], v[152:155], v[56:59], v[164:167]\n"
"s_waitcnt lgkmcnt(1)\n"
"v_mfma_f32_16x16x32_bf16 v[144:147], v[140:143], v[60:63], v[144:147]\n"
"v_cvt_pk_bf16_f32 v229, v162, v163\n"
"v_mfma_f32_16x16x32_bf16 v[156:159], v[140:143], v[64:67], v[156:159]\n"
"ds_read_b128 v[148:151], v13 offset:28672\n"
"v_cvt_pk_bf16_f32 v228, v160, v161\n"
"v_mfma_f32_16x16x32_bf16 v[164:167], v[140:143], v[68:71], v[164:167]\n"
"s_waitcnt lgkmcnt(1)\n"
"v_mfma_f32_16x16x32_bf16 v[144:147], v[136:139], v[72:75], v[144:147]\n"
"v_mov_b32 v143, 0x3f803f80\n"
"v_cvt_pk_bf16_f32 v254, v168, v169\n"
"v_mfma_f32_16x16x32_bf16 v[156:159], v[136:139], v[76:79], v[156:159]\n"
"v_mov_b32 v140, 0x3f803f80\n"
"v_mov_b32 v142, 0x3f803f80\n"
"v_mfma_f32_16x16x32_bf16 v[164:167], v[136:139], v[80:83], v[164:167]\n"
"s_waitcnt lgkmcnt(0)\n"
"v_mov_b32 v141, 0x3f803f80\n"
"v_mfma_f32_16x16x32_bf16 v[144:147], v[148:151], v[84:87], v[144:147]\n"
"s_waitcnt vmcnt(0)\n"
"s_barrier\n"
"ds_read2st64_b32 v[136:137], v9 offset0:120 offset1:121\n"
"v_mfma_f32_16x16x32_bf16 v[156:159], v[148:151], v[88:91], v[156:159]\n"
"ds_read2st64_b32 v[138:139], v9 offset0:122 offset1:123\n"
"v_mfma_f32_16x16x32_bf16 v[164:167], v[148:151], v[92:95], v[164:167]\n"
"s_and_b32 s41, 416, s55\n"
"v_add_u32 v8, s41, v15\n"
"v_cvt_pk_bf16_f32 v186, v144, v145\n"
"v_cvt_pk_bf16_f32 v187, v146, v147\n"
"ds_read2st64_b32 v[150:151], v9 offset0:126 offset1:127\n"
"s_nop 0\n"
"v_cvt_pk_bf16_f32 v212, v156, v157\n"
"ds_read_b32 v149, v9 offset:32000\n"
"ds_read_u16 v8, v8 offset:576\n"
"v_cvt_pk_bf16_f32 v239, v166, v167\n"
"v_cvt_pk_bf16_f32 v213, v158, v159\n"
"s_waitcnt lgkmcnt(3)\n"
"v_mfma_f32_16x16x32_bf16 v[144:147], v[140:143], v[136:139], 0\n"
"v_mfma_f32_16x16x32_bf16 v[152:155], v[190:193], v[136:139], 0\n"
"s_waitcnt lgkmcnt(0)\n"
"v_mad_u32_u24 v8, v8, s40, v16\n"
"v_cvt_pk_bf16_f32 v238, v164, v165\n"
"ds_read_u16 v193, v21 offset:832\n"
"v_mov_b32 v148, 0\n"
"v_mfma_f32_16x16x32_bf16 v[156:159], v[216:219], v[136:139], 0\n"
"ds_read2st64_b32 v[216:217], v20 offset0:0 offset1:1\n"
"ds_read2st64_b32 v[218:219], v20 offset0:2 offset1:3\n"
"v_mfma_f32_16x16x32_bf16 v[160:163], v[242:245], v[136:139], 0\n"
"s_setprio 2\n"
"v_mov_b32 v244, 0\n"
"ds_read_b32 v245, v9 offset:33536\n"
"s_add_u32 m0, s51, 126976\n"
"s_nop 0\n"
"global_load_lds_dwordx4 v8, s[56:57]\n"
"v_mfma_f32_16x16x32_bf16 v[144:147], v[140:143], v[148:151], v[144:147]\n"
"v_mfma_f32_16x16x32_bf16 v[152:155], v[182:185], v[148:151], v[152:155]\n"
"ds_read2st64_b32 v[190:191], v9 offset0:38 offset1:39\n"
"ds_read2st64_b32 v[242:243], v9 offset0:128 offset1:129\n"
"v_mfma_f32_16x16x32_bf16 v[156:159], v[208:211], v[148:151], v[156:159]\n"
"s_waitcnt lgkmcnt(5)\n"
"v_mad_u32_u24 v192, v193, s40, v18\n"
"v_mad_u32_u24 v193, v193, s40, v19\n"
"s_waitcnt lgkmcnt(3)\n"
"global_store_dwordx4 v192, v[216:219], s[66:67] sc1\n"
"v_mfma_f32_16x16x32_bf16 v[160:163], v[234:237], v[148:151], v[160:163]\n"
"s_waitcnt lgkmcnt(0)\n"
"v_mfma_f32_16x16x32_bf16 v[144:147], v[140:143], v[242:245], v[144:147]\n"
"ds_read_b32 v137, v9 offset:34048\n"
"ds_read2st64_b32 v[138:139], v9 offset0:134 offset1:135\n"
"v_mfma_f32_16x16x32_bf16 v[152:155], v[186:189], v[242:245], v[152:155]\n"
"ds_read2st64_b32 v[10:11], v20 offset0:4 offset1:5\n"
"v_mov_b32 v188, 0\n"
"v_mov_b32 v189, 0\n"
"v_mfma_f32_16x16x32_bf16 v[156:159], v[212:215], v[242:245], v[156:159]\n"
"ds_read2st64_b32 v[214:215], v14 offset0:126 offset1:127\n"
"v_mov_b32 v136, 0\n"
"v_mfma_f32_16x16x32_bf16 v[160:163], v[238:241], v[242:245], v[160:163]\n"
"ds_read_b128 v[240:243], v17 offset:53248\n"
"s_add_u32 m0, s51, 129024\n"
"s_nop 0\n"
"global_load_lds_dwordx4 v8, s[58:59]\n"
"s_waitcnt lgkmcnt(3)\n"
"v_mfma_f32_16x16x32_bf16 v[144:147], v[140:143], v[136:139], v[144:147]\n"
"s_waitcnt lgkmcnt(2)\n"
"global_store_dwordx2 v193, v[10:11], s[66:67] offset:256 sc1\n"
"v_mfma_f32_16x16x32_bf16 v[144:147], v[140:143], v[188:191], v[144:147]\n"
"v_mfma_f32_16x16x32_bf16 v[160:163], v[250:253], v[136:139], v[160:163]\n"
"s_add_u32 m0, s51, 131072\n"
"s_nop 0\n"
"global_load_lds_dwordx4 v8, s[60:61]\n"
"s_and_b32 s41, 416, s55\n"
"v_add_u32 v245, s41, v15\n"
"v_mfma_f32_16x16x32_bf16 v[160:163], v[252:255], v[188:191], v[160:163]\n"
"v_mfma_f32_16x16x32_bf16 v[152:155], v[198:201], v[136:139], v[152:155]\n"
"v_mfma_f32_16x16x32_bf16 v[152:155], v[200:203], v[188:191], v[152:155]\n"
"v_rcp_f32 v8, v144\n"
"ds_read2st64_b32 v[216:217], v14 offset0:128 offset1:129\n"
"v_add_u32 v144, 0xd4800000, v144\n"
"v_mfma_f32_16x16x32_bf16 v[156:159], v[224:227], v[136:139], v[156:159]\n"
"ds_read2st64_b32 v[136:137], v14 offset0:150 offset1:151\n"
"v_cmp_lt_u32 vcc, 0x28000000, v144\n"
"v_mfma_f32_16x16x32_bf16 v[156:159], v[226:229], v[188:191], v[156:159]\n"
"s_nop 0\n"
"v_mul_f32 v153, v153, v8\n"
"v_mul_f32 v162, v162, v8\n"
"v_mul_f32 v163, v163, v8\n"
"v_cvt_pk_bf16_f32 v193, v162, v163\n"
"ds_read_b128 v[144:147], v17 offset:54272\n"
"s_waitcnt lgkmcnt(2)\n"
"v_mfma_f32_16x16x32_bf16 v[188:191], v[96:99], v[214:217], 0\n"
"v_mul_f32 v159, v159, v8\n"
"v_mul_f32 v160, v160, v8\n"
"v_mfma_f32_16x16x32_bf16 v[140:143], v[100:103], v[214:217], 0\n"
"v_mul_f32 v161, v161, v8\n"
"v_cvt_pk_bf16_f32 v192, v160, v161\n"
"ds_read2st64_b32 v[138:139], v14 offset0:152 offset1:153\n"
"v_mul_f32 v157, v157, v8\n"
"v_mfma_f32_16x16x32_bf16 v[148:151], v[240:243], v[214:217], 0\n"
"v_mul_f32 v155, v155, v8\n"
"v_mul_f32 v152, v152, v8\n"
"ds_read2st64_b32 v[214:215], v14 offset0:174 offset1:175\n"
"v_cvt_pk_bf16_f32 v152, v152, v153\n"
"s_or_b64 s[62:63], s[62:63], vcc\n"
"s_setprio 0\n"
"v_mul_f32 v156, v156, v8\n"
"v_mul_f32 v158, v158, v8\n"
"v_mul_f32 v154, v154, v8\n"
"ds_read2st64_b32 v[216:217], v14 offset0:176 offset1:177\n"
"ds_read_b128 v[240:243], v17 offset:55296\n"
"ds_read_u16 v245, v245 offset:608\n"
"s_waitcnt lgkmcnt(4)\n"
"v_mfma_f32_16x16x32_bf16 v[188:191], v[104:107], v[136:139], v[188:191]\n"
"v_cvt_pk_bf16_f32 v153, v154, v155\n"
"v_mfma_f32_16x16x32_bf16 v[140:143], v[108:111], v[136:139], v[140:143]\n"
"v_cvt_pk_bf16_f32 v155, v158, v159\n"
"v_cvt_pk_bf16_f32 v154, v156, v157\n"
"v_mfma_f32_16x16x32_bf16 v[148:151], v[144:147], v[136:139], v[148:151]\n"
"s_waitcnt lgkmcnt(2)\n"
"ds_read2st64_b32 v[138:139], v14 offset0:200 offset1:201\n"
"ds_read2st64_b32 v[136:137], v14 offset0:198 offset1:199\n"
"ds_read2st64_b32 v[158:159], v14 offset0:154 offset1:155\n"
"v_mfma_f32_16x16x32_bf16 v[188:191], v[0:3], v[214:217], v[188:191]\n"
"ds_read2st64_b32 v[156:157], v14 offset0:130 offset1:131\n"
"v_mfma_f32_16x16x32_bf16 v[140:143], v[112:115], v[214:217], v[140:143]\n"
"s_waitcnt lgkmcnt(4)\n"
"ds_read_b128 v[144:147], v17 offset:56320\n"
"v_mad_u32_u24 v245, v245, s40, v16\n"
"v_mfma_f32_16x16x32_bf16 v[148:151], v[240:243], v[214:217], v[148:151]\n"
"s_waitcnt lgkmcnt(3)\n"
"ds_read2st64_b32 v[242:243], v14 offset0:202 offset1:203\n"
"ds_read_b128 v[214:217], v17 offset:57344\n"
"v_mfma_f32_16x16x32_bf16 v[188:191], v[116:119], v[136:139], v[188:191]\n"
"s_add_u32 m0, s51, 114688\n"
"s_nop 0\n"
"global_load_lds_dwordx4 v245, s[56:57]\n"
"v_mfma_f32_16x16x32_bf16 v[140:143], v[120:123], v[136:139], v[140:143]\n"
"s_waitcnt lgkmcnt(2)\n"
"ds_read2st64_b32 v[240:241], v14 offset0:178 offset1:179\n"
"v_mfma_f32_16x16x32_bf16 v[148:151], v[144:147], v[136:139], v[148:151]\n"
"ds_read_b128 v[136:139], v17 offset:58368\n"
"v_mfma_f32_16x16x32_bf16 v[188:191], v[4:7], v[156:159], v[188:191]\n"
"v_mfma_f32_16x16x32_bf16 v[140:143], v[124:127], v[156:159], v[140:143]\n"
"s_waitcnt lgkmcnt(2)\n"
"ds_read_b128 v[144:147], v12 offset:36864\n"
"v_mfma_f32_16x16x32_bf16 v[148:151], v[214:217], v[156:159], v[148:151]\n"
"s_waitcnt lgkmcnt(2)\n"
"ds_read_b128 v[156:159], v12 offset:38912\n"
"v_mfma_f32_16x16x32_bf16 v[188:191], v[128:131], v[240:243], v[188:191]\n"
"s_add_u32 m0, s51, 116736\n"
"s_nop 0\n"
"global_load_lds_dwordx4 v245, s[58:59]\n"
"v_mfma_f32_16x16x32_bf16 v[140:143], v[132:135], v[240:243], v[140:143]\n"
"s_barrier\n"
"s_nop 2\n"
"v_cmp_class_f32_e64 vcc, v188, s64\n"
"v_cvt_pk_bf16_f32 v188, v188, v189\n"
"v_cvt_pk_bf16_f32 v189, v190, v191\n"
"s_waitcnt lgkmcnt(2)\n"
"v_mfma_f32_16x16x32_bf16 v[148:151], v[136:139], v[240:243], v[148:151]\n"
"s_waitcnt lgkmcnt(1)\n"
"v_mfma_f32_16x16x32_bf16 v[240:243], v[144:147], v[28:31], 0\n"
"v_cvt_pk_bf16_f32 v191, v142, v143\n"
"s_add_u32 m0, s51, 118784\n"
"s_nop 0\n"
"global_load_lds_dwordx4 v245, s[60:61]\n"
"ds_read_b128 v[214:217], v13 offset:36864\n"
"s_nop 2\n"
"s_mov_b32 m0, s54\n"
"v_cvt_pk_bf16_f32 v190, v140, v141\n"
"v_cvt_pk_bf16_f32 v148, v148, v149\n"
"v_mfma_f32_16x16x32_bf16 v[136:139], v[144:147], v[32:35], 0\n"
"v_mfma_f32_16x16x32_bf16 v[160:163], v[144:147], v[24:27], 0\n"
"v_cvt_pk_bf16_f32 v149, v150, v151\n"
"ds_write_addtid_b32 v155 offset:33024\n"
"ds_read_b128 v[144:147], v13 offset:38912\n"
"s_waitcnt lgkmcnt(2)\n"
"v_mfma_f32_16x16x32_bf16 v[240:243], v[214:217], v[40:43], v[240:243]\n"
"ds_write_addtid_b32 v193 offset:33536\n"
"v_mfma_f32_16x16x32_bf16 v[136:139], v[214:217], v[44:47], v[136:139]\n"
"v_mfma_f32_16x16x32_bf16 v[160:163], v[214:217], v[36:39], v[160:163]\n"
"ds_read_b128 v[214:217], v12 offset:40960\n"
"v_mfma_f32_16x16x32_bf16 v[240:243], v[156:159], v[48:51], v[240:243]\n"
"ds_write_addtid_b32 v154 offset:32768\n"
"v_mfma_f32_16x16x32_bf16 v[136:139], v[156:159], v[52:55], v[136:139]\n"
"ds_write_addtid_b32 v153 offset:32512\n"
"v_mfma_f32_16x16x32_bf16 v[160:163], v[156:159], v[56:59], v[160:163]\n"
"ds_write_addtid_b32 v192 offset:33280\n"
"ds_read_b128 v[140:143], v13 offset:40960\n"
"s_waitcnt lgkmcnt(6)\n"
"v_mfma_f32_16x16x32_bf16 v[240:243], v[144:147], v[60:63], v[240:243]\n"
"ds_write_addtid_b32 v152 offset:32256\n"
"s_mov_b32 m0, s68\n"
"s_nop 0\n"
"ds_write_addtid_b32 v190 offset:45568\n"
"v_mfma_f32_16x16x32_bf16 v[136:139], v[144:147], v[64:67], v[136:139]\n"
"s_or_b64 s[62:63], s[62:63], vcc\n"
"ds_write_addtid_b32 v191 offset:45824\n"
"v_mfma_f32_16x16x32_bf16 v[160:163], v[144:147], v[68:71], v[160:163]\n"
"s_waitcnt lgkmcnt(7)\n"
"v_mfma_f32_16x16x32_bf16 v[240:243], v[214:217], v[72:75], v[240:243]\n"
"ds_read_b128 v[144:147], v12 offset:43008\n"
"v_mfma_f32_16x16x32_bf16 v[136:139], v[214:217], v[76:79], v[136:139]\n"
"ds_write_addtid_b32 v189 offset:45312\n"
"v_mfma_f32_16x16x32_bf16 v[160:163], v[214:217], v[80:83], v[160:163]\n"
"s_waitcnt lgkmcnt(5)\n"
"ds_read_b128 v[190:193], v13 offset:43008\n"
"v_mfma_f32_16x16x32_bf16 v[240:243], v[140:143], v[84:87], v[240:243]\n"
"ds_write_addtid_b32 v188 offset:45056\n"
"v_mfma_f32_16x16x32_bf16 v[136:139], v[140:143], v[88:91], v[136:139]\n"
"ds_write_addtid_b32 v149 offset:46336\n"
"v_mfma_f32_16x16x32_bf16 v[160:163], v[140:143], v[92:95], v[160:163]\n"
"s_waitcnt lgkmcnt(4)\n"
"ds_read_b128 v[140:143], v12 offset:45056\n"
"s_setprio 2\n"
"v_mfma_f32_16x16x32_bf16 v[216:219], v[144:147], v[28:31], 0\n"
"ds_write_addtid_b32 v148 offset:46080\n"
"v_mfma_f32_16x16x32_bf16 v[148:151], v[144:147], v[32:35], 0\n"
"v_mfma_f32_16x16x32_bf16 v[152:155], v[144:147], v[24:27], 0\n"
"s_waitcnt lgkmcnt(4)\n"
"v_mfma_f32_16x16x32_bf16 v[216:219], v[190:193], v[40:43], v[216:219]\n"
"v_cvt_pk_bf16_f32 v189, v242, v243\n"
"v_mfma_f32_16x16x32_bf16 v[148:151], v[190:193], v[44:47], v[148:151]\n"
"v_cvt_pk_bf16_f32 v188, v240, v241\n"
"v_cvt_pk_bf16_f32 v215, v138, v139\n"
"v_mfma_f32_16x16x32_bf16 v[152:155], v[190:193], v[36:39], v[152:155]\n"
"v_cvt_pk_bf16_f32 v214, v136, v137\n"
"v_cvt_pk_bf16_f32 v241, v162, v163\n"
"s_setprio 0\n"
"ds_read_b128 v[242:245], v13 offset:45056\n"
"s_waitcnt lgkmcnt(2)\n"
"v_mfma_f32_16x16x32_bf16 v[216:219], v[140:143], v[48:51], v[216:219]\n"
"ds_read_b128 v[136:139], v13 offset:47104\n"
"v_mfma_f32_16x16x32_bf16 v[148:151], v[140:143], v[52:55], v[148:151]\n"
"ds_read_b128 v[190:193], v12 offset:47104\n"
"v_mfma_f32_16x16x32_bf16 v[152:155], v[140:143], v[56:59], v[152:155]\n"
"s_waitcnt lgkmcnt(2)\n"
"v_mfma_f32_16x16x32_bf16 v[216:219], v[242:245], v[60:63], v[216:219]\n"
"v_cvt_pk_bf16_f32 v240, v160, v161\n"
"v_mfma_f32_16x16x32_bf16 v[148:151], v[242:245], v[64:67], v[148:151]\n"
"v_mov_b32 v142, 0x3f803f80\n"
"v_mfma_f32_16x16x32_bf16 v[152:155], v[242:245], v[68:71], v[152:155]\n"
"s_waitcnt lgkmcnt(0)\n"
"v_mfma_f32_16x16x32_bf16 v[216:219], v[190:193], v[72:75], v[216:219]\n"
"v_mfma_f32_16x16x32_bf16 v[148:151], v[190:193], v[76:79], v[148:151]\n"
"v_mov_b32 v141, 0x3f803f80\n"
"v_mfma_f32_16x16x32_bf16 v[152:155], v[190:193], v[80:83], v[152:155]\n"
"v_mov_b32 v140, 0x3f803f80\n"
"v_mfma_f32_16x16x32_bf16 v[216:219], v[136:139], v[84:87], v[216:219]\n"
"s_waitcnt vmcnt(3)\n"
"s_barrier\n"
"v_mfma_f32_16x16x32_bf16 v[148:151], v[136:139], v[88:91], v[148:151]\n"
"ds_read2st64_b32 v[144:145], v9 offset0:112 offset1:113\n"
"ds_read2st64_b32 v[146:147], v9 offset0:114 offset1:115\n"
"v_mfma_f32_16x16x32_bf16 v[152:155], v[136:139], v[92:95], v[152:155]\n"
"ds_read2st64_b32 v[160:161], v9 offset0:130 offset1:131\n"
"s_andn2_b32 s41, 416, s55\n"
"v_add_u32 v8, s41, v15\n"
"v_cvt_pk_bf16_f32 v191, v218, v219\n"
"v_cvt_pk_bf16_f32 v190, v216, v217\n"
"v_cvt_pk_bf16_f32 v217, v150, v151\n"
"ds_read2st64_b32 v[136:137], v9 offset0:116 offset1:117\n"
"v_mov_b32 v143, 0x3f803f80\n"
"ds_read2st64_b32 v[168:169], v20 offset0:0 offset1:1\n"
"v_cvt_pk_bf16_f32 v216, v148, v149\n"
"s_waitcnt lgkmcnt(3)\n"
"v_mfma_f32_16x16x32_bf16 v[148:151], v[140:143], v[144:147], 0\n"
"ds_read2st64_b32 v[138:139], v9 offset0:118 offset1:119\n"
"v_cvt_pk_bf16_f32 v243, v154, v155\n"
"v_mfma_f32_16x16x32_bf16 v[164:167], v[178:181], v[144:147], 0\n"
"ds_read_u16 v8, v8 offset:640\n"
"v_cvt_pk_bf16_f32 v242, v152, v153\n"
"v_mfma_f32_16x16x32_bf16 v[152:155], v[204:207], v[144:147], 0\n"
"ds_read_u16 v245, v21 offset:864\n"
"v_mov_b32 v156, 0\n"
"ds_read2st64_b32 v[158:159], v9 offset0:128 offset1:129\n"
"v_mfma_f32_16x16x32_bf16 v[172:175], v[230:233], v[144:147], 0\n"
"s_waitcnt lgkmcnt(3)\n"
"v_mov_b32 v157, 0\n"
"v_mfma_f32_16x16x32_bf16 v[148:151], v[140:143], v[136:139], v[148:151]\n"
"s_waitcnt lgkmcnt(0)\n"
"ds_read2st64_b32 v[218:219], v20 offset0:4 offset1:5\n"
"ds_read2st64_b32 v[170:171], v20 offset0:2 offset1:3\n"
"v_mfma_f32_16x16x32_bf16 v[148:151], v[140:143], v[158:161], v[148:151]\n"
"v_mad_u32_u24 v8, v8, s40, v16\n"
"s_add_u32 m0, s51, 139264\n"
"s_nop 0\n"
"global_load_lds_dwordx4 v8, s[56:57]\n"
"v_mfma_f32_16x16x32_bf16 v[172:175], v[246:249], v[136:139], v[172:175]\n"
"v_mov_b32 v162, 0\n"
"v_mad_u32_u24 v244, v245, s40, v18\n"
"v_mov_b32 v163, 0\n"
"v_mfma_f32_16x16x32_bf16 v[172:175], v[232:235], v[156:159], v[172:175]\n"
"v_mad_u32_u24 v245, v245, s40, v19\n"
"v_mfma_f32_16x16x32_bf16 v[172:175], v[250:253], v[160:163], v[172:175]\n"
"s_waitcnt lgkmcnt(0)\n"
"ds_read2st64_b32 v[142:143], v14 offset0:128 offset1:129\n"
"global_store_dwordx4 v244, v[168:171], s[66:67] sc1\n"
"v_mfma_f32_16x16x32_bf16 v[164:167], v[194:197], v[136:139], v[164:167]\n"
"ds_read2st64_b32 v[144:145], v14 offset0:150 offset1:151\n"
"ds_read2st64_b32 v[140:141], v14 offset0:126 offset1:127\n"
"v_mfma_f32_16x16x32_bf16 v[164:167], v[180:183], v[156:159], v[164:167]\n"
"v_mfma_f32_16x16x32_bf16 v[164:167], v[198:201], v[160:163], v[164:167]\n"
"s_add_u32 m0, s51, 141312\n"
"s_nop 0\n"
"global_load_lds_dwordx4 v8, s[58:59]\n"
"global_store_dwordx2 v245, v[218:219], s[66:67] offset:256 sc1\n"
"v_mfma_f32_16x16x32_bf16 v[152:155], v[220:223], v[136:139], v[152:155]\n"
"s_setprio 2\n"
"ds_read_b128 v[136:139], v17 offset:53248\n"
"v_rcp_f32 v245, v148\n"
"v_add_u32 v148, 0xd4800000, v148\n"
"v_mul_f32 v164, v164, v245\n"
"v_mfma_f32_16x16x32_bf16 v[152:155], v[206:209], v[156:159], v[152:155]\n"
"v_mul_f32 v174, v174, v245\n"
"v_mul_f32 v172, v172, v245\n"
"v_mul_f32 v165, v165, v245\n"
"v_mul_f32 v173, v173, v245\n"
"v_mfma_f32_16x16x32_bf16 v[152:155], v[224:227], v[160:163], v[152:155]\n"
"s_waitcnt lgkmcnt(1)\n"
"ds_read_b128 v[156:159], v17 offset:54272\n"
"ds_read2st64_b32 v[146:147], v14 offset0:152 offset1:153\n"
"v_cmp_lt_u32 vcc, 0x28000000, v148\n"
"v_mfma_f32_16x16x32_bf16 v[148:151], v[96:99], v[140:143], 0\n"
"v_cvt_pk_bf16_f32 v218, v172, v173\n"
"s_add_u32 m0, s51, 143360\n"
"s_nop 0\n"
"global_load_lds_dwordx4 v8, s[60:61]\n"
"s_or_b64 s[62:63], s[62:63], vcc\n"
"v_mfma_f32_16x16x32_bf16 v[160:163], v[100:103], v[140:143], 0\n"
"s_waitcnt lgkmcnt(2)\n"
"ds_read2st64_b32 v[170:171], v14 offset0:176 offset1:177\n"
"v_mul_f32 v175, v175, v245\n"
"v_cvt_pk_bf16_f32 v219, v174, v175\n"
"v_mfma_f32_16x16x32_bf16 v[172:175], v[136:139], v[140:143], 0\n"
"s_waitcnt lgkmcnt(1)\n"
"ds_read2st64_b32 v[168:169], v14 offset0:174 offset1:175\n"
"v_mul_f32 v167, v167, v245\n"
"ds_read_b128 v[136:139], v17 offset:55296\n"
"v_mfma_f32_16x16x32_bf16 v[148:151], v[104:107], v[144:147], v[148:151]\n"
"v_cvt_pk_bf16_f32 v164, v164, v165\n"
"v_mul_f32 v166, v166, v245\n"
"ds_read2st64_b32 v[142:143], v14 offset0:200 offset1:201\n"
"ds_read2st64_b32 v[140:141], v14 offset0:198 offset1:199\n"
"v_mfma_f32_16x16x32_bf16 v[160:163], v[108:111], v[144:147], v[160:163]\n"
"v_mul_f32 v155, v155, v245\n"
"v_cvt_pk_bf16_f32 v165, v166, v167\n"
"s_setprio 0\n"
"v_mul_f32 v152, v152, v245\n"
"v_mfma_f32_16x16x32_bf16 v[172:175], v[156:159], v[144:147], v[172:175]\n"
"s_waitcnt lgkmcnt(3)\n"
"ds_read_b128 v[144:147], v17 offset:56320\n"
"v_mul_f32 v153, v153, v245\n"
"v_mfma_f32_16x16x32_bf16 v[148:151], v[0:3], v[168:171], v[148:151]\n"
"v_mul_f32 v154, v154, v245\n"
"v_cvt_pk_bf16_f32 v166, v152, v153\n"
"ds_read2st64_b32 v[152:153], v14 offset0:130 offset1:131\n"
"v_cvt_pk_bf16_f32 v167, v154, v155\n"
"ds_read2st64_b32 v[156:157], v14 offset0:178 offset1:179\n"
"v_mfma_f32_16x16x32_bf16 v[160:163], v[112:115], v[168:171], v[160:163]\n"
"s_waitcnt lgkmcnt(5)\n"
"ds_read2st64_b32 v[154:155], v14 offset0:154 offset1:155\n"
"v_mfma_f32_16x16x32_bf16 v[172:175], v[136:139], v[168:171], v[172:175]\n"
"s_waitcnt lgkmcnt(4)\n"
"ds_read_b128 v[136:139], v17 offset:57344\n"
"v_mfma_f32_16x16x32_bf16 v[148:151], v[116:119], v[140:143], v[148:151]\n"
"v_mfma_f32_16x16x32_bf16 v[160:163], v[120:123], v[140:143], v[160:163]\n"
"s_waitcnt lgkmcnt(4)\n"
"ds_read2st64_b32 v[158:159], v14 offset0:202 offset1:203\n"
"v_mfma_f32_16x16x32_bf16 v[172:175], v[144:147], v[140:143], v[172:175]\n"
"s_waitcnt lgkmcnt(2)\n"
"v_mfma_f32_16x16x32_bf16 v[148:151], v[4:7], v[152:155], v[148:151]\n"
"ds_read_b128 v[140:143], v17 offset:58368\n"
"s_mov_b32 m0, s54\n"
"v_mfma_f32_16x16x32_bf16 v[160:163], v[124:127], v[152:155], v[160:163]\n"
"s_waitcnt lgkmcnt(2)\n"
"ds_read_b128 v[144:147], v12 offset:49152\n"
"v_mfma_f32_16x16x32_bf16 v[172:175], v[136:139], v[152:155], v[172:175]\n"
"s_waitcnt lgkmcnt(2)\n"
"ds_read_b128 v[136:139], v13 offset:49152\n"
"v_mfma_f32_16x16x32_bf16 v[148:151], v[128:131], v[156:159], v[148:151]\n"
"v_mfma_f32_16x16x32_bf16 v[160:163], v[132:135], v[156:159], v[160:163]\n"
"s_barrier\n"
"s_waitcnt lgkmcnt(2)\n"
"v_mfma_f32_16x16x32_bf16 v[172:175], v[140:143], v[156:159], v[172:175]\n"
"s_waitcnt lgkmcnt(1)\n"
"v_mfma_f32_16x16x32_bf16 v[140:143], v[144:147], v[28:31], 0\n"
"ds_read_b128 v[152:155], v12 offset:51200\n"
"v_mfma_f32_16x16x32_bf16 v[156:159], v[144:147], v[32:35], 0\n"
"ds_write_addtid_b32 v219 offset:33536\n"
"v_mfma_f32_16x16x32_bf16 v[168:171], v[144:147], v[24:27], 0\n"
"ds_write_addtid_b32 v218 offset:33280\n"
"s_waitcnt lgkmcnt(3)\n"
"v_mfma_f32_16x16x32_bf16 v[140:143], v[136:139], v[40:43], v[140:143]\n"
"v_mfma_f32_16x16x32_bf16 v[156:159], v[136:139], v[44:47], v[156:159]\n"
"v_cmp_class_f32_e64 vcc, v148, s64\n"
"v_cvt_pk_bf16_f32 v148, v148, v149\n"
"v_cvt_pk_bf16_f32 v172, v172, v173\n"
"v_cvt_pk_bf16_f32 v173, v174, v175\n"
"v_cvt_pk_bf16_f32 v149, v150, v151\n"
"v_cvt_pk_bf16_f32 v151, v162, v163\n"
"v_cvt_pk_bf16_f32 v150, v160, v161\n"
"v_mfma_f32_16x16x32_bf16 v[168:171], v[136:139], v[36:39], v[168:171]\n"
"s_waitcnt lgkmcnt(2)\n"
"ds_read_b128 v[144:147], v13 offset:51200\n"
"ds_read_b128 v[136:139], v12 offset:53248\n"
"v_mfma_f32_16x16x32_bf16 v[140:143], v[152:155], v[48:51], v[140:143]\n"
"ds_write_addtid_b32 v167 offset:33024\n"
"v_mfma_f32_16x16x32_bf16 v[156:159], v[152:155], v[52:55], v[156:159]\n"
"ds_write_addtid_b32 v165 offset:32512\n"
"v_mfma_f32_16x16x32_bf16 v[168:171], v[152:155], v[56:59], v[168:171]\n"
"ds_write_addtid_b32 v166 offset:32768\n"
"s_waitcnt lgkmcnt(4)\n"
"v_mfma_f32_16x16x32_bf16 v[140:143], v[144:147], v[60:63], v[140:143]\n"
"ds_write_addtid_b32 v164 offset:32256\n"
"s_mov_b32 m0, s68\n"
"s_or_b64 s[62:63], s[62:63], vcc\n"
"ds_write_addtid_b32 v173 offset:46336\n"
"v_mfma_f32_16x16x32_bf16 v[156:159], v[144:147], v[64:67], v[156:159]\n"
"ds_write_addtid_b32 v149 offset:45312\n"
"ds_read_b128 v[152:155], v13 offset:53248\n"
"v_mfma_f32_16x16x32_bf16 v[168:171], v[144:147], v[68:71], v[168:171]\n"
"ds_write_addtid_b32 v172 offset:46080\n"
"s_waitcnt lgkmcnt(8)\n"
"v_mfma_f32_16x16x32_bf16 v[140:143], v[136:139], v[72:75], v[140:143]\n"
"ds_write_addtid_b32 v150 offset:45568\n"
"v_mfma_f32_16x16x32_bf16 v[156:159], v[136:139], v[76:79], v[156:159]\n"
"ds_write_addtid_b32 v148 offset:45056\n"
"v_mfma_f32_16x16x32_bf16 v[168:171], v[136:139], v[80:83], v[168:171]\n"
"ds_write_addtid_b32 v151 offset:45824\n"
"s_waitcnt lgkmcnt(4)\n"
"v_mfma_f32_16x16x32_bf16 v[140:143], v[152:155], v[84:87], v[140:143]\n"
"v_mov_b32 v145, 0x3f803f80\n"
"v_mov_b32 v144, 0x3f803f80\n"
"v_mfma_f32_16x16x32_bf16 v[156:159], v[152:155], v[88:91], v[156:159]\n"
"v_mfma_f32_16x16x32_bf16 v[168:171], v[152:155], v[92:95], v[168:171]\n"
"s_waitcnt lgkmcnt(0)\n"
"s_waitcnt vmcnt(5)\n"
"s_barrier\n"
"ds_read2st64_b32 v[148:149], v9 offset0:124 offset1:125\n"
"ds_read2st64_b32 v[136:137], v9 offset0:120 offset1:121\n"
"ds_read2st64_b32 v[138:139], v9 offset0:122 offset1:123\n"
"v_mov_b32 v146, 0x3f803f80\n"
"v_cvt_pk_bf16_f32 v218, v156, v157\n"
"v_cvt_pk_bf16_f32 v193, v142, v143\n"
"s_and_b32 s41, 416, s55\n"
"v_add_u32 v11, s41, v15\n"
"v_mov_b32 v147, 0x3f803f80\n"
"v_cvt_pk_bf16_f32 v244, v168, v169\n"
"ds_read2st64_b32 v[150:151], v9 offset0:126 offset1:127\n"
"ds_read_u16 v11, v11 offset:672\n"
"v_cvt_pk_bf16_f32 v192, v140, v141\n"
"s_waitcnt lgkmcnt(2)\n"
"v_mfma_f32_16x16x32_bf16 v[140:143], v[144:147], v[136:139], 0\n"
"v_cvt_pk_bf16_f32 v219, v158, v159\n"
"v_mfma_f32_16x16x32_bf16 v[152:155], v[178:181], v[136:139], 0\n"
"ds_read_b32 v164, v9 offset:32768\n"
"v_mfma_f32_16x16x32_bf16 v[156:159], v[204:207], v[136:139], 0\n"
"v_cvt_pk_bf16_f32 v245, v170, v171\n"
"v_mfma_f32_16x16x32_bf16 v[160:163], v[230:233], v[136:139], 0\n"
"s_waitcnt lgkmcnt(2)\n"
"ds_read_u16 v8, v21 offset:896\n"
"ds_read2st64_b32 v[136:137], v20 offset0:0 offset1:1\n"
"ds_read2st64_b32 v[138:139], v20 offset0:2 offset1:3\n"
"v_mfma_f32_16x16x32_bf16 v[140:143], v[144:147], v[148:151], v[140:143]\n"
"v_mfma_f32_16x16x32_bf16 v[152:155], v[188:191], v[148:151], v[152:155]\n"
"ds_read2st64_b32 v[166:167], v9 offset0:130 offset1:131\n"
"v_mfma_f32_16x16x32_bf16 v[156:159], v[214:217], v[148:151], v[156:159]\n"
"v_mov_b32 v165, 0\n"
"v_mfma_f32_16x16x32_bf16 v[160:163], v[240:243], v[148:151], v[160:163]\n"
"s_waitcnt lgkmcnt(3)\n"
"ds_read2st64_b32 v[148:149], v9 offset0:132 offset1:133\n"
"v_mad_u32_u24 v10, v8, s40, v18\n"
"v_mad_u32_u24 v8, v8, s40, v19\n"
"s_waitcnt lgkmcnt(2)\n"
"global_store_dwordx4 v10, v[136:139], s[66:67] sc1\n"
"ds_read2st64_b32 v[174:175], v20 offset0:4 offset1:5\n"
"ds_read_b32 v150, v9 offset:34304\n"
"ds_read_b32 v138, v9 offset:9728\n"
"s_waitcnt lgkmcnt(4)\n"
"v_mfma_f32_16x16x32_bf16 v[140:143], v[144:147], v[164:167], v[140:143]\n"
"v_mad_u32_u24 v11, v11, s40, v16\n"
"v_mov_b32 v136, 0\n"
"v_mfma_f32_16x16x32_bf16 v[152:155], v[192:195], v[164:167], v[152:155]\n"
"v_mov_b32 v151, 0\n"
"v_mfma_f32_16x16x32_bf16 v[156:159], v[218:221], v[164:167], v[156:159]\n"
"s_add_u32 m0, s51, 151552\n"
"s_nop 0\n"
"global_load_lds_dwordx4 v11, s[56:57]\n"
"s_waitcnt lgkmcnt(2)\n"
"global_store_dwordx2 v8, v[174:175], s[66:67] offset:256 sc1\n"
"v_mfma_f32_16x16x32_bf16 v[160:163], v[244:247], v[164:167], v[160:163]\n"
"s_waitcnt lgkmcnt(1)\n"
"s_setprio 2\n"
"ds_read2st64_b32 v[168:169], v14 offset0:150 offset1:151\n"
"v_mfma_f32_16x16x32_bf16 v[140:143], v[144:147], v[148:151], v[140:143]\n"
"s_waitcnt lgkmcnt(1)\n"
"v_mov_b32 v137, 0\n"
"v_mov_b32 v139, 0\n"
"s_nop 1\n"
"v_mfma_f32_16x16x32_bf16 v[140:143], v[144:147], v[136:139], v[140:143]\n"
"ds_read2st64_b32 v[146:147], v14 offset0:128 offset1:129\n"
"ds_read_b128 v[164:167], v17 offset:53248\n"
"v_mfma_f32_16x16x32_bf16 v[160:163], v[248:251], v[148:151], v[160:163]\n"
"ds_read2st64_b32 v[144:145], v14 offset0:126 offset1:127\n"
"v_mfma_f32_16x16x32_bf16 v[160:163], v[232:235], v[136:139], v[160:163]\n"
"s_add_u32 m0, s51, 153600\n"
"s_nop 0\n"
"global_load_lds_dwordx4 v11, s[58:59]\n"
"v_mfma_f32_16x16x32_bf16 v[152:155], v[196:199], v[148:151], v[152:155]\n"
"v_rcp_f32 v8, v140\n"
"v_mfma_f32_16x16x32_bf16 v[152:155], v[180:183], v[136:139], v[152:155]\n"
"v_add_u32 v140, 0xd4800000, v140\n"
"v_cmp_lt_u32 vcc, 0x28000000, v140\n"
"v_mfma_f32_16x16x32_bf16 v[156:159], v[222:225], v[148:151], v[156:159]\n"
"v_mul_f32 v161, v161, v8\n"
"ds_read2st64_b32 v[170:171], v14 offset0:152 offset1:153\n"
"v_mfma_f32_16x16x32_bf16 v[156:159], v[206:209], v[136:139], v[156:159]\n"
"s_waitcnt lgkmcnt(1)\n"
"ds_read_b128 v[136:139], v17 offset:54272\n"
"v_mul_f32 v160, v160, v8\n"
"v_cvt_pk_bf16_f32 v246, v160, v161\n"
"v_mfma_f32_16x16x32_bf16 v[140:143], v[96:99], v[144:147], 0\n"
"s_add_u32 m0, s51, 155648\n"
"s_nop 0\n"
"global_load_lds_dwordx4 v11, s[60:61]\n"
"v_mfma_f32_16x16x32_bf16 v[148:151], v[100:103], v[144:147], 0\n"
"ds_read2st64_b32 v[160:161], v14 offset0:174 offset1:175\n"
"v_mul_f32 v163, v163, v8\n"
"s_or_b64 s[62:63], s[62:63], vcc\n"
"v_mul_f32 v162, v162, v8\n"
"v_cvt_pk_bf16_f32 v247, v162, v163\n"
"v_mfma_f32_16x16x32_bf16 v[172:175], v[164:167], v[144:147], 0\n"
"s_waitcnt lgkmcnt(2)\n"
"ds_read_b128 v[144:147], v17 offset:55296\n"
"ds_read2st64_b32 v[162:163], v14 offset0:176 offset1:177\n"
"v_mfma_f32_16x16x32_bf16 v[140:143], v[104:107], v[168:171], v[140:143]\n"
"ds_read2st64_b32 v[166:167], v14 offset0:200 offset1:201\n"
"v_mul_f32 v152, v152, v8\n"
"v_mul_f32 v153, v153, v8\n"
"v_mul_f32 v157, v157, v8\n"
"v_mfma_f32_16x16x32_bf16 v[148:151], v[108:111], v[168:171], v[148:151]\n"
"s_waitcnt lgkmcnt(4)\n"
"ds_read2st64_b32 v[164:165], v14 offset0:198 offset1:199\n"
"v_mul_f32 v154, v154, v8\n"
"v_mfma_f32_16x16x32_bf16 v[172:175], v[136:139], v[168:171], v[172:175]\n"
"s_waitcnt lgkmcnt(2)\n"
"v_mul_f32 v155, v155, v8\n"
"v_cvt_pk_bf16_f32 v152, v152, v153\n"
"v_cvt_pk_bf16_f32 v153, v154, v155\n"
"v_mfma_f32_16x16x32_bf16 v[140:143], v[0:3], v[160:163], v[140:143]\n"
"ds_read_b128 v[136:139], v17 offset:56320\n"
"v_mul_f32 v156, v156, v8\n"
"v_mfma_f32_16x16x32_bf16 v[148:151], v[112:115], v[160:163], v[148:151]\n"
"v_cvt_pk_bf16_f32 v154, v156, v157\n"
"v_mul_f32 v159, v159, v8\n"
"ds_read2st64_b32 v[156:157], v14 offset0:130 offset1:131\n"
"v_mfma_f32_16x16x32_bf16 v[172:175], v[144:147], v[160:163], v[172:175]\n"
"s_waitcnt lgkmcnt(2)\n"
"v_mul_f32 v158, v158, v8\n"
"v_cvt_pk_bf16_f32 v155, v158, v159\n"
"v_mfma_f32_16x16x32_bf16 v[140:143], v[116:119], v[164:167], v[140:143]\n"
"ds_read2st64_b32 v[158:159], v14 offset0:154 offset1:155\n"
"v_mfma_f32_16x16x32_bf16 v[148:151], v[120:123], v[164:167], v[148:151]\n"
"s_waitcnt lgkmcnt(2)\n"
"ds_read2st64_b32 v[162:163], v14 offset0:202 offset1:203\n"
"ds_read_b128 v[144:147], v17 offset:57344\n"
"ds_read2st64_b32 v[160:161], v14 offset0:178 offset1:179\n"
"v_mfma_f32_16x16x32_bf16 v[172:175], v[136:139], v[164:167], v[172:175]\n"
"s_waitcnt lgkmcnt(3)\n"
"s_setprio 0\n"
"v_mfma_f32_16x16x32_bf16 v[140:143], v[4:7], v[156:159], v[140:143]\n"
"s_mov_b32 m0, s54\n"
"ds_read_b128 v[164:167], v12 offset:24576\n"
"v_mfma_f32_16x16x32_bf16 v[148:151], v[124:127], v[156:159], v[148:151]\n"
"s_waitcnt lgkmcnt(2)\n"
"ds_read_b128 v[136:139], v17 offset:58368\n"
"v_mfma_f32_16x16x32_bf16 v[172:175], v[144:147], v[156:159], v[172:175]\n"
"s_waitcnt lgkmcnt(2)\n"
"v_mfma_f32_16x16x32_bf16 v[140:143], v[128:131], v[160:163], v[140:143]\n"
"s_barrier\n"
"v_mfma_f32_16x16x32_bf16 v[148:151], v[132:135], v[160:163], v[148:151]\n"
"s_waitcnt lgkmcnt(0)\n"
"v_mfma_f32_16x16x32_bf16 v[172:175], v[136:139], v[160:163], v[172:175]\n"
"ds_read_b128 v[156:159], v12 offset:26624\n"
"v_mfma_f32_16x16x32_bf16 v[136:139], v[164:167], v[28:31], 0\n"
"ds_read_b128 v[144:147], v13 offset:24576\n"
"s_nop 0\n"
"v_cmp_class_f32_e64 vcc, v140, s64\n"
"v_cvt_pk_bf16_f32 v140, v140, v141\n"
"v_cvt_pk_bf16_f32 v141, v142, v143\n"
"ds_write_addtid_b32 v246 offset:33280\n"
"v_mfma_f32_16x16x32_bf16 v[160:163], v[164:167], v[32:35], 0\n"
"v_mfma_f32_16x16x32_bf16 v[168:171], v[164:167], v[24:27], 0\n"
"v_cvt_pk_bf16_f32 v143, v150, v151\n"
"v_cvt_pk_bf16_f32 v142, v148, v149\n"
"ds_write_addtid_b32 v154 offset:32768\n"
"s_waitcnt lgkmcnt(2)\n"
"v_mfma_f32_16x16x32_bf16 v[136:139], v[144:147], v[40:43], v[136:139]\n"
"v_cvt_pk_bf16_f32 v172, v172, v173\n"
"v_mfma_f32_16x16x32_bf16 v[160:163], v[144:147], v[44:47], v[160:163]\n"
"ds_write_addtid_b32 v155 offset:33024\n"
"v_mfma_f32_16x16x32_bf16 v[168:171], v[144:147], v[36:39], v[168:171]\n"
"ds_read_b128 v[144:147], v12 offset:28672\n"
"v_mfma_f32_16x16x32_bf16 v[136:139], v[156:159], v[48:51], v[136:139]\n"
"ds_read_b128 v[164:167], v13 offset:26624\n"
"v_mfma_f32_16x16x32_bf16 v[160:163], v[156:159], v[52:55], v[160:163]\n"
"ds_write_addtid_b32 v153 offset:32512\n"
"ds_write_addtid_b32 v152 offset:32256\n"
"ds_write_addtid_b32 v247 offset:33536\n"
"s_mov_b32 m0, s68\n"
"v_cvt_pk_bf16_f32 v173, v174, v175\n"
"v_mfma_f32_16x16x32_bf16 v[168:171], v[156:159], v[56:59], v[168:171]\n"
"ds_write_addtid_b32 v172 offset:46080\n"
"s_waitcnt lgkmcnt(4)\n"
"v_mfma_f32_16x16x32_bf16 v[136:139], v[164:167], v[60:63], v[136:139]\n"
"ds_write_addtid_b32 v143 offset:45824\n"
"ds_write_addtid_b32 v173 offset:46336\n"
"v_mfma_f32_16x16x32_bf16 v[160:163], v[164:167], v[64:67], v[160:163]\n"
"ds_read_b128 v[148:151], v13 offset:28672\n"
"ds_write_addtid_b32 v141 offset:45312\n"
"v_mfma_f32_16x16x32_bf16 v[168:171], v[164:167], v[68:71], v[168:171]\n"
"v_mfma_f32_16x16x32_bf16 v[136:139], v[144:147], v[72:75], v[136:139]\n"
"ds_write_addtid_b32 v142 offset:45568\n"
"ds_write_addtid_b32 v140 offset:45056\n"
"v_mov_b32 v140, 0x3f803f80\n"
"v_mfma_f32_16x16x32_bf16 v[160:163], v[144:147], v[76:79], v[160:163]\n"
"s_or_b64 s[62:63], s[62:63], vcc\n"
"v_mfma_f32_16x16x32_bf16 v[168:171], v[144:147], v[80:83], v[168:171]\n"
"s_waitcnt lgkmcnt(3)\n"
"v_mfma_f32_16x16x32_bf16 v[136:139], v[148:151], v[84:87], v[136:139]\n"
"v_mfma_f32_16x16x32_bf16 v[160:163], v[148:151], v[88:91], v[160:163]\n"
"v_mfma_f32_16x16x32_bf16 v[168:171], v[148:151], v[92:95], v[168:171]\n"
"s_waitcnt lgkmcnt(0)\n"
"s_waitcnt vmcnt(5)\n"
"s_barrier\n"
"ds_read2st64_b32 v[144:145], v9 offset0:112 offset1:113\n"
"ds_read2st64_b32 v[146:147], v9 offset0:114 offset1:115\n"
"v_mov_b32 v142, 0x3f803f80\n"
"s_nop 0\n"
"v_cvt_pk_bf16_f32 v221, v162, v163\n"
"v_cvt_pk_bf16_f32 v195, v138, v139\n"
"v_mov_b32 v143, 0x3f803f80\n"
"v_cvt_pk_bf16_f32 v194, v136, v137\n"
"ds_read2st64_b32 v[150:151], v9 offset0:118 offset1:119\n"
"v_cvt_pk_bf16_f32 v247, v170, v171\n"
"ds_read2st64_b32 v[148:149], v9 offset0:116 offset1:117\n"
"ds_read_u16 v8, v21 offset:928\n"
"v_mov_b32 v141, 0x3f803f80\n"
"s_waitcnt lgkmcnt(3)\n"
"s_nop 0\n"
"v_mfma_f32_16x16x32_bf16 v[136:139], v[140:143], v[144:147], 0\n"
"v_cvt_pk_bf16_f32 v246, v168, v169\n"
"v_cvt_pk_bf16_f32 v220, v160, v161\n"
"v_mfma_f32_16x16x32_bf16 v[152:155], v[180:183], v[144:147], 0\n"
"ds_read2st64_b32 v[166:167], v9 offset0:130 offset1:131\n"
"v_mfma_f32_16x16x32_bf16 v[156:159], v[206:209], v[144:147], 0\n"
"s_and_b32 s41, 416, s55\n"
"v_add_u32 v11, s41, v15\n"
"v_mfma_f32_16x16x32_bf16 v[160:163], v[232:235], v[144:147], 0\n"
"s_waitcnt lgkmcnt(2)\n"
"ds_read2st64_b32 v[144:145], v20 offset0:0 offset1:1\n"
"ds_read2st64_b32 v[164:165], v9 offset0:128 offset1:129\n"
"ds_read2st64_b32 v[146:147], v20 offset0:2 offset1:3\n"
"v_mfma_f32_16x16x32_bf16 v[136:139], v[140:143], v[148:151], v[136:139]\n"
"s_waitcnt lgkmcnt(4)\n"
"v_mad_u32_u24 v10, v8, s40, v18\n"
"v_mfma_f32_16x16x32_bf16 v[152:155], v[190:193], v[148:151], v[152:155]\n"
"ds_read_u16 v11, v11 offset:704\n"
"v_mfma_f32_16x16x32_bf16 v[156:159], v[216:219], v[148:151], v[156:159]\n"
"ds_read2st64_b32 v[174:175], v20 offset0:4 offset1:5\n"
"v_mad_u32_u24 v8, v8, s40, v19\n"
"v_mfma_f32_16x16x32_bf16 v[160:163], v[242:245], v[148:151], v[160:163]\n"
"s_waitcnt lgkmcnt(2)\n"
"global_store_dwordx4 v10, v[144:147], s[66:67] sc1\n"
"v_mfma_f32_16x16x32_bf16 v[136:139], v[140:143], v[164:167], v[136:139]\n"
"ds_read2st64_b32 v[148:149], v9 offset0:132 offset1:133\n"
"ds_read2st64_b32 v[146:147], v9 offset0:36 offset1:37\n"
"v_mfma_f32_16x16x32_bf16 v[152:155], v[194:197], v[164:167], v[152:155]\n"
"s_waitcnt lgkmcnt(3)\n"
"v_mad_u32_u24 v11, v11, s40, v16\n"
"s_add_u32 m0, s51, 126976\n"
"s_nop 0\n"
"global_load_lds_dwordx4 v11, s[56:57]\n"
"ds_read2st64_b32 v[150:151], v9 offset0:134 offset1:135\n"
"v_mfma_f32_16x16x32_bf16 v[156:159], v[220:223], v[164:167], v[156:159]\n"
"s_waitcnt lgkmcnt(3)\n"
"v_mov_b32 v144, 0\n"
"v_mov_b32 v145, 0\n"
"global_store_dwordx2 v8, v[174:175], s[66:67] offset:256 sc1\n"
"v_mfma_f32_16x16x32_bf16 v[160:163], v[246:249], v[164:167], v[160:163]\n"
"s_waitcnt lgkmcnt(0)\n"
"v_mfma_f32_16x16x32_bf16 v[136:139], v[140:143], v[148:151], v[136:139]\n"
"v_mfma_f32_16x16x32_bf16 v[136:139], v[140:143], v[144:147], v[136:139]\n"
"s_add_u32 m0, s51, 129024\n"
"s_nop 0\n"
"global_load_lds_dwordx4 v11, s[58:59]\n"
"ds_read_b128 v[164:167], v17 offset:53248\n"
"v_mfma_f32_16x16x32_bf16 v[160:163], v[250:253], v[148:151], v[160:163]\n"
"s_setprio 2\n"
"ds_read2st64_b32 v[142:143], v14 offset0:128 offset1:129\n"
"ds_read2st64_b32 v[140:141], v14 offset0:126 offset1:127\n"
"v_mfma_f32_16x16x32_bf16 v[160:163], v[234:237], v[144:147], v[160:163]\n"
"v_rcp_f32 v8, v136\n"
"v_mfma_f32_16x16x32_bf16 v[152:155], v[198:201], v[148:151], v[152:155]\n"
"ds_read2st64_b32 v[170:171], v14 offset0:152 offset1:153\n"
"v_mfma_f32_16x16x32_bf16 v[152:155], v[182:185], v[144:147], v[152:155]\n"
"ds_read2st64_b32 v[168:169], v14 offset0:150 offset1:151\n"
"v_add_u32 v136, 0xd4800000, v136\n"
"v_mfma_f32_16x16x32_bf16 v[156:159], v[224:227], v[148:151], v[156:159]\n"
"v_cmp_lt_u32 vcc, 0x28000000, v136\n"
"v_mul_f32 v162, v162, v8\n"
"v_mul_f32 v160, v160, v8\n"
"v_mul_f32 v161, v161, v8\n"
"v_mul_f32 v163, v163, v8\n"
"v_mfma_f32_16x16x32_bf16 v[156:159], v[208:211], v[144:147], v[156:159]\n"
"s_waitcnt lgkmcnt(2)\n"
"ds_read_b128 v[136:139], v17 offset:54272\n"
"v_cvt_pk_bf16_f32 v248, v160, v161\n"
"v_mfma_f32_16x16x32_bf16 v[144:147], v[96:99], v[140:143], 0\n"
"s_add_u32 m0, s51, 131072\n"
"s_nop 0\n"
"global_load_lds_dwordx4 v11, s[60:61]\n"
"v_mfma_f32_16x16x32_bf16 v[148:151], v[100:103], v[140:143], 0\n"
"v_cvt_pk_bf16_f32 v249, v162, v163\n"
"v_mul_f32 v156, v156, v8\n"
"v_mul_f32 v153, v153, v8\n"
"ds_read2st64_b32 v[160:161], v14 offset0:174 offset1:175\n"
"ds_read2st64_b32 v[162:163], v14 offset0:176 offset1:177\n"
"v_mfma_f32_16x16x32_bf16 v[172:175], v[164:167], v[140:143], 0\n"
"s_waitcnt lgkmcnt(3)\n"
"v_mfma_f32_16x16x32_bf16 v[144:147], v[104:107], v[168:171], v[144:147]\n"
"ds_read_b128 v[140:143], v17 offset:55296\n"
"v_mul_f32 v154, v154, v8\n"
"v_mul_f32 v152, v152, v8\n"
"v_mul_f32 v158, v158, v8\n"
"v_mfma_f32_16x16x32_bf16 v[148:151], v[108:111], v[168:171], v[148:151]\n"
"s_waitcnt lgkmcnt(3)\n"
"ds_read2st64_b32 v[164:165], v14 offset0:198 offset1:199\n"
"ds_read2st64_b32 v[166:167], v14 offset0:200 offset1:201\n"
"v_mfma_f32_16x16x32_bf16 v[172:175], v[136:139], v[168:171], v[172:175]\n"
"s_waitcnt lgkmcnt(3)\n"
"ds_read_b128 v[136:139], v17 offset:56320\n"
"v_cvt_pk_bf16_f32 v152, v152, v153\n"
"v_mfma_f32_16x16x32_bf16 v[144:147], v[0:3], v[160:163], v[144:147]\n"
"v_mul_f32 v155, v155, v8\n"
"v_cvt_pk_bf16_f32 v153, v154, v155\n"
"s_or_b64 s[62:63], s[62:63], vcc\n"
"v_mul_f32 v157, v157, v8\n"
"v_cvt_pk_bf16_f32 v154, v156, v157\n"
"v_mfma_f32_16x16x32_bf16 v[148:151], v[112:115], v[160:163], v[148:151]\n"
"s_waitcnt lgkmcnt(3)\n"
"v_mul_f32 v159, v159, v8\n"
"v_cvt_pk_bf16_f32 v155, v158, v159\n"
"v_mfma_f32_16x16x32_bf16 v[172:175], v[140:143], v[160:163], v[172:175]\n"
"s_setprio 0\n"
"ds_read2st64_b32 v[158:159], v14 offset0:154 offset1:155\n"
"ds_read2st64_b32 v[156:157], v14 offset0:130 offset1:131\n"
"ds_read_b128 v[140:143], v17 offset:57344\n"
"s_waitcnt lgkmcnt(4)\n"
"v_mfma_f32_16x16x32_bf16 v[144:147], v[116:119], v[164:167], v[144:147]\n"
"ds_read2st64_b32 v[162:163], v14 offset0:202 offset1:203\n"
"ds_read2st64_b32 v[160:161], v14 offset0:178 offset1:179\n"
"v_mfma_f32_16x16x32_bf16 v[148:151], v[120:123], v[164:167], v[148:151]\n"
"s_waitcnt lgkmcnt(5)\n"
"v_mfma_f32_16x16x32_bf16 v[172:175], v[136:139], v[164:167], v[172:175]\n"
"s_waitcnt lgkmcnt(3)\n"
"ds_read_b128 v[136:139], v17 offset:58368\n"
"v_mfma_f32_16x16x32_bf16 v[144:147], v[4:7], v[156:159], v[144:147]\n"
"v_mfma_f32_16x16x32_bf16 v[148:151], v[124:127], v[156:159], v[148:151]\n"
"s_waitcnt lgkmcnt(3)\n"
"v_mfma_f32_16x16x32_bf16 v[172:175], v[140:143], v[156:159], v[172:175]\n"
"s_waitcnt lgkmcnt(1)\n"
"ds_read_b128 v[164:167], v12 offset:12288\n"
"v_mfma_f32_16x16x32_bf16 v[144:147], v[128:131], v[160:163], v[144:147]\n"
"s_mov_b32 m0, s54\n"
"ds_read_b128 v[156:159], v12 offset:14336\n"
"v_mfma_f32_16x16x32_bf16 v[148:151], v[132:135], v[160:163], v[148:151]\n"
"s_barrier\n"
"s_nop 3\n"
"v_cmp_class_f32_e64 vcc, v144, s64\n"
"v_cvt_pk_bf16_f32 v144, v144, v145\n"
"ds_read_b128 v[140:143], v13 offset:12288\n"
"s_waitcnt lgkmcnt(3)\n"
"v_mfma_f32_16x16x32_bf16 v[172:175], v[136:139], v[160:163], v[172:175]\n"
"ds_write_addtid_b32 v248 offset:33280\n"
"v_cvt_pk_bf16_f32 v145, v146, v147\n"
"v_cvt_pk_bf16_f32 v147, v150, v151\n"
"s_waitcnt lgkmcnt(3)\n"
"v_mfma_f32_16x16x32_bf16 v[136:139], v[164:167], v[28:31], 0\n"
"v_mfma_f32_16x16x32_bf16 v[160:163], v[164:167], v[32:35], 0\n"
"v_mfma_f32_16x16x32_bf16 v[168:171], v[164:167], v[24:27], 0\n"
"ds_read_b128 v[164:167], v13 offset:14336\n"
"ds_write_addtid_b32 v249 offset:33536\n"
"ds_write_addtid_b32 v152 offset:32256\n"
"s_waitcnt lgkmcnt(4)\n"
"v_mfma_f32_16x16x32_bf16 v[136:139], v[140:143], v[40:43], v[136:139]\n"
"v_cvt_pk_bf16_f32 v146, v148, v149\n"
"v_mfma_f32_16x16x32_bf16 v[160:163], v[140:143], v[44:47], v[160:163]\n"
"v_cvt_pk_bf16_f32 v172, v172, v173\n"
"v_mfma_f32_16x16x32_bf16 v[168:171], v[140:143], v[36:39], v[168:171]\n"
"ds_write_addtid_b32 v153 offset:32512\n"
"s_or_b64 s[62:63], s[62:63], vcc\n"
"ds_read_b128 v[140:143], v12 offset:16384\n"
"v_mfma_f32_16x16x32_bf16 v[136:139], v[156:159], v[48:51], v[136:139]\n"
"v_cvt_pk_bf16_f32 v173, v174, v175\n"
"v_mfma_f32_16x16x32_bf16 v[160:163], v[156:159], v[52:55], v[160:163]\n"
"ds_read_b128 v[148:151], v13 offset:16384\n"
"ds_write_addtid_b32 v154 offset:32768\n"
"v_mfma_f32_16x16x32_bf16 v[168:171], v[156:159], v[56:59], v[168:171]\n"
"ds_write_addtid_b32 v155 offset:33024\n"
"s_waitcnt lgkmcnt(7)\n"
"v_mfma_f32_16x16x32_bf16 v[136:139], v[164:167], v[60:63], v[136:139]\n"
"s_mov_b32 m0, s68\n"
"s_nop 0\n"
"ds_write_addtid_b32 v172 offset:46080\n"
"v_mfma_f32_16x16x32_bf16 v[160:163], v[164:167], v[64:67], v[160:163]\n"
"ds_write_addtid_b32 v146 offset:45568\n"
"v_mfma_f32_16x16x32_bf16 v[168:171], v[164:167], v[68:71], v[168:171]\n"
"ds_write_addtid_b32 v145 offset:45312\n"
"s_waitcnt lgkmcnt(6)\n"
"v_mfma_f32_16x16x32_bf16 v[136:139], v[140:143], v[72:75], v[136:139]\n"
"ds_write_addtid_b32 v173 offset:46336\n"
"v_mfma_f32_16x16x32_bf16 v[160:163], v[140:143], v[76:79], v[160:163]\n"
"v_mfma_f32_16x16x32_bf16 v[168:171], v[140:143], v[80:83], v[168:171]\n"
"ds_write_addtid_b32 v147 offset:45824\n"
"s_waitcnt lgkmcnt(7)\n"
"v_mfma_f32_16x16x32_bf16 v[136:139], v[148:151], v[84:87], v[136:139]\n"
"ds_write_addtid_b32 v144 offset:45056\n"
"v_mfma_f32_16x16x32_bf16 v[160:163], v[148:151], v[88:91], v[160:163]\n"
"v_mfma_f32_16x16x32_bf16 v[168:171], v[148:151], v[92:95], v[168:171]\n"
"s_nop 4\n"
"v_cvt_pk_bf16_f32 v196, v136, v137\n"
"s_waitcnt lgkmcnt(0)\n"
"s_waitcnt vmcnt(5)\n"
"s_barrier\n"
"ds_read2st64_b32 v[146:147], v9 offset0:122 offset1:123\n"
"v_mov_b32 v142, 0x3f803f80\n"
"ds_read2st64_b32 v[144:145], v9 offset0:120 offset1:121\n"
"v_cvt_pk_bf16_f32 v222, v160, v161\n"
"v_mov_b32 v143, 0x3f803f80\n"
"v_mov_b32 v141, 0x3f803f80\n"
"v_mov_b32 v140, 0x3f803f80\n"
"v_cvt_pk_bf16_f32 v197, v138, v139\n"
"v_cvt_pk_bf16_f32 v223, v162, v163\n"
"v_mov_b32 v148, 0\n"
"ds_read_b32 v149, v9 offset:32000\n"
"ds_read2st64_b32 v[150:151], v9 offset0:126 offset1:127\n"
"s_waitcnt lgkmcnt(2)\n"
"v_mfma_f32_16x16x32_bf16 v[136:139], v[140:143], v[144:147], 0\n"
"v_mfma_f32_16x16x32_bf16 v[152:155], v[200:203], v[144:147], 0\n"
"ds_read_u16 v8, v21 offset:960\n"
"s_setprio 2\n"
"v_mfma_f32_16x16x32_bf16 v[200:203], v[226:229], v[144:147], 0\n"
"s_and_b32 s41, 416, s55\n"
"v_add_u32 v229, s41, v15\n"
"v_cvt_pk_bf16_f32 v249, v170, v171\n"
"v_mfma_f32_16x16x32_bf16 v[156:159], v[252:255], v[144:147], 0\n"
"s_waitcnt lgkmcnt(1)\n"
"ds_read2st64_b32 v[254:255], v20 offset0:2 offset1:3\n"
"ds_read2st64_b32 v[252:253], v20 offset0:0 offset1:1\n"
"v_mfma_f32_16x16x32_bf16 v[136:139], v[140:143], v[148:151], v[136:139]\n"
"v_cvt_pk_bf16_f32 v248, v168, v169\n"
"v_mov_b32 v144, 0\n"
"ds_read_b32 v145, v9 offset:33024\n"
"v_mfma_f32_16x16x32_bf16 v[152:155], v[182:185], v[148:151], v[152:155]\n"
"ds_read_u16 v229, v229 offset:736\n"
"v_mfma_f32_16x16x32_bf16 v[200:203], v[208:211], v[148:151], v[200:203]\n"
"s_waitcnt lgkmcnt(4)\n"
"ds_read2st64_b32 v[146:147], v9 offset0:130 offset1:131\n"
"v_mad_u32_u24 v228, v8, s40, v18\n"
"v_mad_u32_u24 v8, v8, s40, v19\n"
"ds_read2st64_b32 v[226:227], v20 offset0:4 offset1:5\n"
"v_mfma_f32_16x16x32_bf16 v[156:159], v[234:237], v[148:151], v[156:159]\n"
"s_waitcnt lgkmcnt(4)\n"
"global_store_dwordx4 v228, v[252:255], s[66:67] sc1\n"
"s_waitcnt lgkmcnt(1)\n"
"v_mfma_f32_16x16x32_bf16 v[136:139], v[140:143], v[144:147], v[136:139]\n"
"ds_read2st64_b32 v[254:255], v9 offset0:38 offset1:39\n"
"v_mad_u32_u24 v229, v229, s40, v16\n"
"s_add_u32 m0, s51, 114688\n"
"s_nop 0\n"
"global_load_lds_dwordx4 v229, s[56:57]\n"
"ds_read2st64_b32 v[148:149], v9 offset0:132 offset1:133\n"
"ds_read_b32 v151, v9 offset:34560\n"
"v_mfma_f32_16x16x32_bf16 v[152:155], v[192:195], v[144:147], v[152:155]\n"
"v_mov_b32 v253, 0\n"
"v_mov_b32 v150, 0\n"
"v_mfma_f32_16x16x32_bf16 v[200:203], v[218:221], v[144:147], v[200:203]\n"
"s_waitcnt lgkmcnt(3)\n"
"global_store_dwordx2 v8, v[226:227], s[66:67] offset:256 sc1\n"
"v_mfma_f32_16x16x32_bf16 v[156:159], v[244:247], v[144:147], v[156:159]\n"
"s_waitcnt lgkmcnt(0)\n"
"v_mov_b32 v252, 0\n"
"ds_read_b128 v[144:147], v17 offset:53248\n"
"v_mfma_f32_16x16x32_bf16 v[136:139], v[140:143], v[148:151], v[136:139]\n"
"v_mfma_f32_16x16x32_bf16 v[136:139], v[140:143], v[252:255], v[136:139]\n"
"ds_read2st64_b32 v[140:141], v14 offset0:126 offset1:127\n"
"s_add_u32 m0, s51, 116736\n"
"s_nop 0\n"
"global_load_lds_dwordx4 v229, s[58:59]\n"
"ds_read2st64_b32 v[142:143], v14 offset0:128 offset1:129\n"
"v_mfma_f32_16x16x32_bf16 v[156:159], v[248:251], v[148:151], v[156:159]\n"
"ds_read2st64_b32 v[160:161], v14 offset0:150 offset1:151\n"
"v_mfma_f32_16x16x32_bf16 v[156:159], v[236:239], v[252:255], v[156:159]\n"
"ds_read2st64_b32 v[162:163], v14 offset0:152 offset1:153\n"
"v_mfma_f32_16x16x32_bf16 v[152:155], v[196:199], v[148:151], v[152:155]\n"
"v_rcp_f32 v8, v136\n"
"v_add_u32 v136, 0xd4800000, v136\n"
"v_cmp_lt_u32 vcc, 0x28000000, v136\n"
"v_mfma_f32_16x16x32_bf16 v[152:155], v[184:187], v[252:255], v[152:155]\n"
"v_mfma_f32_16x16x32_bf16 v[200:203], v[222:225], v[148:151], v[200:203]\n"
"ds_read2st64_b32 v[148:149], v14 offset0:174 offset1:175\n"
"ds_read_b128 v[224:227], v17 offset:54272\n"
"v_mfma_f32_16x16x32_bf16 v[200:203], v[210:213], v[252:255], v[200:203]\n"
"s_or_b64 s[62:63], s[62:63], vcc\n"
"v_mul_f32 v157, v157, v8\n"
"v_mul_f32 v156, v156, v8\n"
"ds_read2st64_b32 v[150:151], v14 offset0:176 offset1:177\n"
"s_waitcnt lgkmcnt(5)\n"
"v_mfma_f32_16x16x32_bf16 v[252:255], v[96:99], v[140:143], 0\n"
"v_cvt_pk_bf16_f32 v250, v156, v157\n"
"s_add_u32 m0, s51, 118784\n"
"s_nop 0\n"
"global_load_lds_dwordx4 v229, s[60:61]\n"
"v_mul_f32 v158, v158, v8\n"
"v_mfma_f32_16x16x32_bf16 v[136:139], v[100:103], v[140:143], 0\n"
"v_mul_f32 v159, v159, v8\n"
"v_cvt_pk_bf16_f32 v251, v158, v159\n"
"v_mfma_f32_16x16x32_bf16 v[156:159], v[144:147], v[140:143], 0\n"
"s_waitcnt lgkmcnt(3)\n"
"ds_read2st64_b32 v[144:145], v14 offset0:198 offset1:199\n"
"v_mul_f32 v152, v152, v8\n"
"v_mfma_f32_16x16x32_bf16 v[252:255], v[104:107], v[160:163], v[252:255]\n"
"v_mul_f32 v201, v201, v8\n"
"v_mul_f32 v153, v153, v8\n"
"ds_read_b128 v[140:143], v17 offset:55296\n"
"v_mul_f32 v154, v154, v8\n"
"v_mfma_f32_16x16x32_bf16 v[136:139], v[108:111], v[160:163], v[136:139]\n"
"s_waitcnt lgkmcnt(3)\n"
"ds_read2st64_b32 v[146:147], v14 offset0:200 offset1:201\n"
"v_cvt_pk_bf16_f32 v152, v152, v153\n"
"v_mfma_f32_16x16x32_bf16 v[156:159], v[224:227], v[160:163], v[156:159]\n"
"s_waitcnt lgkmcnt(3)\n"
"v_mul_f32 v155, v155, v8\n"
"v_cvt_pk_bf16_f32 v153, v154, v155\n"
"v_mfma_f32_16x16x32_bf16 v[252:255], v[0:3], v[148:151], v[252:255]\n"
"ds_read2st64_b32 v[224:225], v14 offset0:130 offset1:131\n"
"v_mul_f32 v200, v200, v8\n"
"v_cvt_pk_bf16_f32 v154, v200, v201\n"
"v_mul_f32 v203, v203, v8\n"
"v_mfma_f32_16x16x32_bf16 v[136:139], v[112:115], v[148:151], v[136:139]\n"
"s_waitcnt lgkmcnt(2)\n"
"ds_read_b128 v[198:201], v17 offset:56320\n"
"ds_read2st64_b32 v[226:227], v14 offset0:154 offset1:155\n"
"v_mfma_f32_16x16x32_bf16 v[156:159], v[140:143], v[148:151], v[156:159]\n"
"s_waitcnt lgkmcnt(3)\n"
"ds_read_b128 v[140:143], v17 offset:57344\n"
"v_mul_f32 v202, v202, v8\n"
"v_cvt_pk_bf16_f32 v155, v202, v203\n"
"v_mfma_f32_16x16x32_bf16 v[252:255], v[116:119], v[144:147], v[252:255]\n"
"s_setprio 0\n"
"ds_read2st64_b32 v[148:149], v14 offset0:178 offset1:179\n"
"v_mfma_f32_16x16x32_bf16 v[136:139], v[120:123], v[144:147], v[136:139]\n"
"s_waitcnt lgkmcnt(3)\n"
"ds_read2st64_b32 v[150:151], v14 offset0:202 offset1:203\n"
"v_mfma_f32_16x16x32_bf16 v[156:159], v[198:201], v[144:147], v[156:159]\n"
"s_waitcnt lgkmcnt(3)\n"
"ds_read_b128 v[144:147], v12 offset:43008\n"
"v_mfma_f32_16x16x32_bf16 v[252:255], v[4:7], v[224:227], v[252:255]\n"
"v_mfma_f32_16x16x32_bf16 v[136:139], v[124:127], v[224:227], v[136:139]\n"
"s_waitcnt lgkmcnt(3)\n"
"v_mfma_f32_16x16x32_bf16 v[156:159], v[140:143], v[224:227], v[156:159]\n"
"s_waitcnt lgkmcnt(1)\n"
"ds_read_b128 v[198:201], v17 offset:58368\n"
"v_mfma_f32_16x16x32_bf16 v[252:255], v[128:131], v[148:151], v[252:255]\n"
"v_mfma_f32_16x16x32_bf16 v[136:139], v[132:135], v[148:151], v[136:139]\n"
"s_waitcnt vmcnt(5)\n"
"s_barrier\n"
"s_mov_b32 m0, s54\n"
"s_nop 0\n"
"ds_write_addtid_b32 v251 offset:33536\n"
"ds_read_b128 v[224:227], v13 offset:43008\n"
"s_waitcnt lgkmcnt(2)\n"
"v_mfma_f32_16x16x32_bf16 v[156:159], v[198:201], v[148:151], v[156:159]\n"
"v_cmp_class_f32_e64 vcc, v252, s64\n"
"v_cvt_pk_bf16_f32 v252, v252, v253\n"
"v_cvt_pk_bf16_f32 v253, v254, v255\n"
"ds_read_b128 v[140:143], v12 offset:45056\n"
"v_cvt_pk_bf16_f32 v255, v138, v139\n"
"v_cvt_pk_bf16_f32 v254, v136, v137\n"
"v_mfma_f32_16x16x32_bf16 v[200:203], v[144:147], v[28:31], 0\n"
"v_mfma_f32_16x16x32_bf16 v[148:151], v[144:147], v[32:35], 0\n"
"ds_write_addtid_b32 v250 offset:33280\n"
"v_mfma_f32_16x16x32_bf16 v[160:163], v[144:147], v[24:27], 0\n"
"s_waitcnt lgkmcnt(2)\n"
"ds_read_b128 v[144:147], v13 offset:45056\n"
"v_mfma_f32_16x16x32_bf16 v[200:203], v[224:227], v[40:43], v[200:203]\n"
"v_mfma_f32_16x16x32_bf16 v[148:151], v[224:227], v[44:47], v[148:151]\n"
"ds_write_addtid_b32 v152 offset:32256\n"
"v_mfma_f32_16x16x32_bf16 v[160:163], v[224:227], v[36:39], v[160:163]\n"
"s_waitcnt lgkmcnt(3)\n"
"v_cvt_pk_bf16_f32 v156, v156, v157\n"
"ds_read_b128 v[136:139], v13 offset:47104\n"
"ds_read_b128 v[224:227], v12 offset:47104\n"
"v_mfma_f32_16x16x32_bf16 v[200:203], v[140:143], v[48:51], v[200:203]\n"
"ds_write_addtid_b32 v154 offset:32768\n"
"v_mfma_f32_16x16x32_bf16 v[148:151], v[140:143], v[52:55], v[148:151]\n"
"ds_write_addtid_b32 v155 offset:33024\n"
"v_mfma_f32_16x16x32_bf16 v[160:163], v[140:143], v[56:59], v[160:163]\n"
"s_waitcnt lgkmcnt(5)\n"
"v_mfma_f32_16x16x32_bf16 v[200:203], v[144:147], v[60:63], v[200:203]\n"
"ds_write_addtid_b32 v153 offset:32512\n"
"s_mov_b32 m0, s68\n"
"v_mfma_f32_16x16x32_bf16 v[148:151], v[144:147], v[64:67], v[148:151]\n"
"s_or_b64 s[62:63], s[62:63], vcc\n"
"v_cvt_pk_bf16_f32 v157, v158, v159\n"
"v_mfma_f32_16x16x32_bf16 v[160:163], v[144:147], v[68:71], v[160:163]\n"
"ds_write_addtid_b32 v252 offset:45056\n"
"s_waitcnt lgkmcnt(4)\n"
"v_mfma_f32_16x16x32_bf16 v[200:203], v[224:227], v[72:75], v[200:203]\n"
"ds_write_addtid_b32 v254 offset:45568\n"
"ds_write_addtid_b32 v253 offset:45312\n"
"v_mfma_f32_16x16x32_bf16 v[148:151], v[224:227], v[76:79], v[148:151]\n"
"ds_write_addtid_b32 v156 offset:46080\n"
"v_mfma_f32_16x16x32_bf16 v[160:163], v[224:227], v[80:83], v[160:163]\n"
"ds_read_b128 v[226:229], v13 offset:49152\n"
"ds_read_b128 v[250:253], v12 offset:49152\n"
"v_mfma_f32_16x16x32_bf16 v[200:203], v[136:139], v[84:87], v[200:203]\n"
"ds_write_addtid_b32 v157 offset:46336\n"
"v_mfma_f32_16x16x32_bf16 v[148:151], v[136:139], v[88:91], v[148:151]\n"
"ds_read_b128 v[140:143], v12 offset:51200\n"
"v_mfma_f32_16x16x32_bf16 v[160:163], v[136:139], v[92:95], v[160:163]\n"
"s_waitcnt lgkmcnt(2)\n"
"v_mfma_f32_16x16x32_bf16 v[136:139], v[250:253], v[28:31], 0\n"
"ds_write_addtid_b32 v255 offset:45824\n"
"v_mfma_f32_16x16x32_bf16 v[144:147], v[250:253], v[32:35], 0\n"
"v_mfma_f32_16x16x32_bf16 v[152:155], v[250:253], v[24:27], 0\n"
"ds_read_b128 v[252:255], v13 offset:51200\n"
"v_mfma_f32_16x16x32_bf16 v[136:139], v[226:229], v[40:43], v[136:139]\n"
"v_mfma_f32_16x16x32_bf16 v[144:147], v[226:229], v[44:47], v[144:147]\n"
"v_cvt_pk_bf16_f32 v250, v160, v161\n"
"v_mfma_f32_16x16x32_bf16 v[152:155], v[226:229], v[36:39], v[152:155]\n"
"s_waitcnt lgkmcnt(2)\n"
"v_cvt_pk_bf16_f32 v198, v200, v201\n"
"v_mfma_f32_16x16x32_bf16 v[136:139], v[140:143], v[48:51], v[136:139]\n"
"v_cvt_pk_bf16_f32 v199, v202, v203\n"
"v_mfma_f32_16x16x32_bf16 v[144:147], v[140:143], v[52:55], v[144:147]\n"
"ds_read_b128 v[226:229], v13 offset:53248\n"
"ds_read_b128 v[200:203], v12 offset:53248\n"
"v_mfma_f32_16x16x32_bf16 v[152:155], v[140:143], v[56:59], v[152:155]\n"
"s_waitcnt lgkmcnt(2)\n"
"v_mov_b32 v141, 0x3f803f80\n"
"v_mfma_f32_16x16x32_bf16 v[136:139], v[252:255], v[60:63], v[136:139]\n"
"v_cvt_pk_bf16_f32 v225, v150, v151\n"
"v_mfma_f32_16x16x32_bf16 v[144:147], v[252:255], v[64:67], v[144:147]\n"
"v_cvt_pk_bf16_f32 v224, v148, v149\n"
"v_mfma_f32_16x16x32_bf16 v[152:155], v[252:255], v[68:71], v[152:155]\n"
"s_waitcnt lgkmcnt(0)\n"
"v_cvt_pk_bf16_f32 v251, v162, v163\n"
"v_mov_b32 v140, 0x3f803f80\n"
"v_mfma_f32_16x16x32_bf16 v[136:139], v[200:203], v[72:75], v[136:139]\n"
"s_barrier\n"
"v_mfma_f32_16x16x32_bf16 v[144:147], v[200:203], v[76:79], v[144:147]\n"
"ds_read2st64_b32 v[150:151], v9 offset0:114 offset1:115\n"
"v_mfma_f32_16x16x32_bf16 v[152:155], v[200:203], v[80:83], v[152:155]\n"
"v_mfma_f32_16x16x32_bf16 v[136:139], v[226:229], v[84:87], v[136:139]\n"
"ds_read_b32 v149, v9 offset:28928\n"
"v_mov_b32 v142, 0x3f803f80\n"
"v_mfma_f32_16x16x32_bf16 v[144:147], v[226:229], v[88:91], v[144:147]\n"
"v_mov_b32 v143, 0x3f803f80\n"
"v_mfma_f32_16x16x32_bf16 v[152:155], v[226:229], v[92:95], v[152:155]\n"
"s_and_b32 s41, 416, s55\n"
"v_add_u32 v8, s41, v15\n"
"s_nop 0\n"
"v_cvt_pk_bf16_f32 v200, v136, v137\n"
"v_mov_b32 v148, 0\n"
"s_nop 0\n"
"v_cvt_pk_bf16_f32 v226, v144, v145\n"
"ds_read2st64_b32 v[136:137], v9 offset0:116 offset1:117\n"
"v_cvt_pk_bf16_f32 v201, v138, v139\n"
"ds_read_u16 v15, v21 offset:992\n"
"ds_read2st64_b32 v[138:139], v9 offset0:118 offset1:119\n"
"v_cvt_pk_bf16_f32 v253, v154, v155\n"
"v_cvt_pk_bf16_f32 v252, v152, v153\n"
"v_cvt_pk_bf16_f32 v227, v146, v147\n"
"ds_read_u16 v8, v8 offset:768\n"
"s_waitcnt lgkmcnt(4)\n"
"v_mfma_f32_16x16x32_bf16 v[144:147], v[140:143], v[148:151], 0\n"
"ds_read2st64_b32 v[168:169], v9 offset0:132 offset1:133\n"
"v_mfma_f32_16x16x32_bf16 v[152:155], v[180:183], v[148:151], 0\n"
"ds_read2st64_b32 v[160:161], v20 offset0:0 offset1:1\n"
"ds_read2st64_b32 v[162:163], v20 offset0:2 offset1:3\n"
"v_mfma_f32_16x16x32_bf16 v[156:159], v[206:209], v[148:151], 0\n"
"s_waitcnt lgkmcnt(3)\n"
"v_mad_u32_u24 v8, v8, s40, v16\n"
"v_mfma_f32_16x16x32_bf16 v[164:167], v[232:235], v[148:151], 0\n"
"s_add_u32 m0, s51, 139264\n"
"s_nop 0\n"
"global_load_lds_dwordx4 v8, s[56:57]\n"
"v_mfma_f32_16x16x32_bf16 v[144:147], v[140:143], v[136:139], v[144:147]\n"
"ds_read_b32 v149, v9 offset:33024\n"
"ds_read2st64_b32 v[150:151], v9 offset0:130 offset1:131\n"
"v_mfma_f32_16x16x32_bf16 v[152:155], v[184:187], v[136:139], v[152:155]\n"
"v_mov_b32 v148, 0\n"
"v_mfma_f32_16x16x32_bf16 v[156:159], v[210:213], v[136:139], v[156:159]\n"
"v_mad_u32_u24 v16, v15, s40, v18\n"
"v_mfma_f32_16x16x32_bf16 v[164:167], v[236:239], v[136:139], v[164:167]\n"
"s_waitcnt lgkmcnt(2)\n"
"ds_read2st64_b32 v[254:255], v20 offset0:4 offset1:5\n"
"ds_read2st64_b32 v[170:171], v9 offset0:134 offset1:135\n"
"global_store_dwordx4 v16, v[160:163], s[66:67] sc1\n"
"s_waitcnt lgkmcnt(2)\n"
"s_setprio 2\n"
"v_mfma_f32_16x16x32_bf16 v[144:147], v[140:143], v[148:151], v[144:147]\n"
"s_add_u32 m0, s51, 141312\n"
"s_nop 0\n"
"global_load_lds_dwordx4 v8, s[58:59]\n"
"s_waitcnt lgkmcnt(0)\n"
"v_mfma_f32_16x16x32_bf16 v[144:147], v[140:143], v[168:171], v[144:147]\n"
"ds_read_b128 v[140:143], v17 offset:53248\n"
"v_mfma_f32_16x16x32_bf16 v[164:167], v[242:245], v[148:151], v[164:167]\n"
"v_mad_u32_u24 v15, v15, s40, v19\n"
"v_mfma_f32_16x16x32_bf16 v[164:167], v[246:249], v[168:171], v[164:167]\n"
"s_setprio 0\n"
"ds_read2st64_b32 v[136:137], v14 offset0:126 offset1:127\n"
"ds_read2st64_b32 v[138:139], v14 offset0:128 offset1:129\n"
"global_store_dwordx2 v15, v[254:255], s[66:67] offset:256 sc1\n"
"v_mfma_f32_16x16x32_bf16 v[152:155], v[190:193], v[148:151], v[152:155]\n"
"s_setprio 2\n"
"v_mfma_f32_16x16x32_bf16 v[152:155], v[194:197], v[168:171], v[152:155]\n"
"v_rcp_f32 v16, v144\n"
"s_add_u32 m0, s51, 143360\n"
"s_nop 0\n"
"global_load_lds_dwordx4 v8, s[60:61]\n"
"v_mul_f32 v166, v166, v16\n"
"v_mfma_f32_16x16x32_bf16 v[156:159], v[216:219], v[148:151], v[156:159]\n"
"v_mul_f32 v165, v165, v16\n"
"v_mul_f32 v164, v164, v16\n"
"v_add_u32 v144, 0xd4800000, v144\n"
"v_cmp_lt_u32 vcc, 0x28000000, v144\n"
"v_mfma_f32_16x16x32_bf16 v[156:159], v[220:223], v[168:171], v[156:159]\n"
"s_waitcnt lgkmcnt(0)\n"
"ds_read2st64_b32 v[146:147], v14 offset0:152 offset1:153\n"
"v_cvt_pk_bf16_f32 v254, v164, v165\n"
"v_mfma_f32_16x16x32_bf16 v[148:151], v[96:99], v[136:139], 0\n"
"ds_read2st64_b32 v[144:145], v14 offset0:150 offset1:151\n"
"v_mul_f32 v167, v167, v16\n"
"ds_read_b128 v[168:171], v17 offset:54272\n"
"v_mul_f32 v153, v153, v16\n"
"v_mul_f32 v152, v152, v16\n"
"v_cvt_pk_bf16_f32 v152, v152, v153\n"
"v_mul_f32 v158, v158, v16\n"
"v_mfma_f32_16x16x32_bf16 v[160:163], v[100:103], v[136:139], 0\n"
"s_or_b64 s[62:63], s[62:63], vcc\n"
"v_mul_f32 v157, v157, v16\n"
"v_mul_f32 v156, v156, v16\n"
"v_mul_f32 v154, v154, v16\n"
"v_cvt_pk_bf16_f32 v255, v166, v167\n"
"v_mfma_f32_16x16x32_bf16 v[164:167], v[140:143], v[136:139], 0\n"
"s_waitcnt lgkmcnt(1)\n"
"ds_read_b128 v[140:143], v17 offset:55296\n"
"ds_read2st64_b32 v[136:137], v14 offset0:174 offset1:175\n"
"v_mfma_f32_16x16x32_bf16 v[148:151], v[104:107], v[144:147], v[148:151]\n"
"ds_read2st64_b32 v[138:139], v14 offset0:176 offset1:177\n"
"v_mul_f32 v155, v155, v16\n"
"v_cvt_pk_bf16_f32 v153, v154, v155\n"
"v_mfma_f32_16x16x32_bf16 v[160:163], v[108:111], v[144:147], v[160:163]\n"
"s_waitcnt lgkmcnt(3)\n"
"v_mul_f32 v159, v159, v16\n"
"v_cvt_pk_bf16_f32 v155, v158, v159\n"
"v_mfma_f32_16x16x32_bf16 v[164:167], v[168:171], v[144:147], v[164:167]\n"
"s_setprio 0\n"
"ds_read2st64_b32 v[146:147], v14 offset0:200 offset1:201\n"
"v_cvt_pk_bf16_f32 v154, v156, v157\n"
"ds_read2st64_b32 v[144:145], v14 offset0:198 offset1:199\n"
"s_waitcnt lgkmcnt(2)\n"
"v_mfma_f32_16x16x32_bf16 v[148:151], v[0:3], v[136:139], v[148:151]\n"
"ds_read2st64_b32 v[168:169], v14 offset0:130 offset1:131\n"
"ds_read_b128 v[156:159], v17 offset:56320\n"
"v_mfma_f32_16x16x32_bf16 v[160:163], v[112:115], v[136:139], v[160:163]\n"
"ds_read2st64_b32 v[170:171], v14 offset0:154 offset1:155\n"
"v_mfma_f32_16x16x32_bf16 v[164:167], v[140:143], v[136:139], v[164:167]\n"
"s_waitcnt lgkmcnt(3)\n"
"ds_read2st64_b32 v[140:141], v14 offset0:178 offset1:179\n"
"v_mfma_f32_16x16x32_bf16 v[148:151], v[116:119], v[144:147], v[148:151]\n"
"ds_read_b128 v[136:139], v17 offset:57344\n"
"ds_read2st64_b32 v[142:143], v14 offset0:202 offset1:203\n"
"v_mfma_f32_16x16x32_bf16 v[160:163], v[120:123], v[144:147], v[160:163]\n"
"s_waitcnt lgkmcnt(4)\n"
"v_mfma_f32_16x16x32_bf16 v[164:167], v[156:159], v[144:147], v[164:167]\n"
"s_waitcnt lgkmcnt(3)\n"
"ds_read_b128 v[144:147], v17 offset:58368\n"
"v_mfma_f32_16x16x32_bf16 v[148:151], v[4:7], v[168:171], v[148:151]\n"
"ds_read_b128 v[156:159], v12 offset:24576\n"
"v_mfma_f32_16x16x32_bf16 v[160:163], v[124:127], v[168:171], v[160:163]\n"
"s_waitcnt lgkmcnt(3)\n"
"v_mfma_f32_16x16x32_bf16 v[164:167], v[136:139], v[168:171], v[164:167]\n"
"s_waitcnt lgkmcnt(2)\n"
"ds_read_b128 v[136:139], v13 offset:24576\n"
"v_mfma_f32_16x16x32_bf16 v[148:151], v[128:131], v[140:143], v[148:151]\n"
"v_mfma_f32_16x16x32_bf16 v[160:163], v[132:135], v[140:143], v[160:163]\n"
"s_waitcnt vmcnt(5)\n"
"s_barrier\n"
"s_waitcnt lgkmcnt(2)\n"
"v_mfma_f32_16x16x32_bf16 v[164:167], v[144:147], v[140:143], v[164:167]\n"
"s_waitcnt lgkmcnt(1)\n"
"v_mfma_f32_16x16x32_bf16 v[140:143], v[156:159], v[28:31], 0\n"
"s_nop 0\n"
"v_cmp_class_f32_e64 vcc, v148, s64\n"
"v_cvt_pk_bf16_f32 v148, v148, v149\n"
"ds_read_b128 v[144:147], v12 offset:26624\n"
"v_mfma_f32_16x16x32_bf16 v[168:171], v[156:159], v[32:35], 0\n"
"v_cvt_pk_bf16_f32 v149, v150, v151\n"
"s_mov_b32 m0, s54\n"
"v_mfma_f32_16x16x32_bf16 v[172:175], v[156:159], v[24:27], 0\n"
"s_waitcnt lgkmcnt(1)\n"
"v_cvt_pk_bf16_f32 v164, v164, v165\n"
"v_cvt_pk_bf16_f32 v165, v166, v167\n"
"ds_read_b128 v[156:159], v13 offset:26624\n"
"v_mfma_f32_16x16x32_bf16 v[140:143], v[136:139], v[40:43], v[140:143]\n"
"ds_write_addtid_b32 v154 offset:32768\n"
"v_cvt_pk_bf16_f32 v150, v160, v161\n"
"ds_write_addtid_b32 v155 offset:33024\n"
"v_mfma_f32_16x16x32_bf16 v[168:171], v[136:139], v[44:47], v[168:171]\n"
"v_cvt_pk_bf16_f32 v151, v162, v163\n"
"ds_write_addtid_b32 v254 offset:33280\n"
"v_mfma_f32_16x16x32_bf16 v[172:175], v[136:139], v[36:39], v[172:175]\n"
"ds_write_addtid_b32 v152 offset:32256\n"
"s_waitcnt lgkmcnt(5)\n"
"v_mfma_f32_16x16x32_bf16 v[140:143], v[144:147], v[48:51], v[140:143]\n"
"ds_read_b128 v[136:139], v12 offset:28672\n"
"ds_write_addtid_b32 v153 offset:32512\n"
"v_mfma_f32_16x16x32_bf16 v[168:171], v[144:147], v[52:55], v[168:171]\n"
"ds_write_addtid_b32 v255 offset:33536\n"
"s_mov_b32 m0, s68\n"
"s_or_b64 s[62:63], s[62:63], vcc\n"
"ds_write_addtid_b32 v149 offset:45312\n"
"v_mfma_f32_16x16x32_bf16 v[172:175], v[144:147], v[56:59], v[172:175]\n"
"s_waitcnt lgkmcnt(8)\n"
"ds_read_b128 v[144:147], v13 offset:28672\n"
"v_mfma_f32_16x16x32_bf16 v[140:143], v[156:159], v[60:63], v[140:143]\n"
"ds_write_addtid_b32 v148 offset:45056\n"
"v_mfma_f32_16x16x32_bf16 v[168:171], v[156:159], v[64:67], v[168:171]\n"
"ds_write_addtid_b32 v150 offset:45568\n"
"v_mfma_f32_16x16x32_bf16 v[172:175], v[156:159], v[68:71], v[172:175]\n"
"ds_write_addtid_b32 v151 offset:45824\n"
"s_waitcnt lgkmcnt(7)\n"
"v_mfma_f32_16x16x32_bf16 v[140:143], v[136:139], v[72:75], v[140:143]\n"
"ds_write_addtid_b32 v164 offset:46080\n"
"v_mfma_f32_16x16x32_bf16 v[168:171], v[136:139], v[76:79], v[168:171]\n"
"ds_write_addtid_b32 v165 offset:46336\n"
"v_mfma_f32_16x16x32_bf16 v[172:175], v[136:139], v[80:83], v[172:175]\n"
"s_waitcnt lgkmcnt(5)\n"
"v_mov_b32 v151, 0x3f803f80\n"
"v_mfma_f32_16x16x32_bf16 v[140:143], v[144:147], v[84:87], v[140:143]\n"
"v_mov_b32 v149, 0x3f803f80\n"
"v_mfma_f32_16x16x32_bf16 v[168:171], v[144:147], v[88:91], v[168:171]\n"
"s_waitcnt lgkmcnt(0)\n"
"s_barrier\n"
"v_mfma_f32_16x16x32_bf16 v[172:175], v[144:147], v[92:95], v[172:175]\n"
"ds_read2st64_b32 v[138:139], v9 offset0:122 offset1:123\n"
"ds_read2st64_b32 v[136:137], v9 offset0:120 offset1:121\n"
"s_nop 2\n"
"v_cvt_pk_bf16_f32 v229, v170, v171\n"
"v_cvt_pk_bf16_f32 v228, v168, v169\n"
"v_mov_b32 v150, 0x3f803f80\n"
"v_cvt_pk_bf16_f32 v203, v142, v143\n"
"v_mov_b32 v148, 0x3f803f80\n"
"v_cvt_pk_bf16_f32 v202, v140, v141\n"
"ds_read2st64_b32 v[144:145], v9 offset0:124 offset1:125\n"
"ds_read2st64_b32 v[162:163], v20 offset0:2 offset1:3\n"
"ds_read2st64_b32 v[146:147], v9 offset0:126 offset1:127\n"
"s_waitcnt lgkmcnt(3)\n"
"v_mfma_f32_16x16x32_bf16 v[140:143], v[148:151], v[136:139], 0\n"
"ds_read_u16 v16, v21 offset:1024\n"
"v_mfma_f32_16x16x32_bf16 v[152:155], v[178:181], v[136:139], 0\n"
"v_cvt_pk_bf16_f32 v254, v172, v173\n"
"v_cvt_pk_bf16_f32 v255, v174, v175\n"
"ds_read2st64_b32 v[160:161], v20 offset0:0 offset1:1\n"
"v_mfma_f32_16x16x32_bf16 v[156:159], v[204:207], v[136:139], 0\n"
"v_mfma_f32_16x16x32_bf16 v[164:167], v[230:233], v[136:139], 0\n"
"s_waitcnt lgkmcnt(1)\n"
"ds_read2st64_b32 v[138:139], v9 offset0:130 offset1:131\n"
"ds_read2st64_b32 v[136:137], v9 offset0:128 offset1:129\n"
"ds_read2st64_b32 v[230:231], v20 offset0:4 offset1:5\n"
"v_mad_u32_u24 v15, v16, s40, v18\n"
"v_mfma_f32_16x16x32_bf16 v[140:143], v[148:151], v[144:147], v[140:143]\n"
"v_mov_b32 v168, 0\n"
"v_mfma_f32_16x16x32_bf16 v[152:155], v[188:191], v[144:147], v[152:155]\n"
"v_mfma_f32_16x16x32_bf16 v[156:159], v[214:217], v[144:147], v[156:159]\n"
"ds_read_b32 v170, v9 offset:33792\n"
"v_mfma_f32_16x16x32_bf16 v[164:167], v[240:243], v[144:147], v[164:167]\n"
"s_waitcnt lgkmcnt(2)\n"
"s_setprio 2\n"
"v_mfma_f32_16x16x32_bf16 v[140:143], v[148:151], v[136:139], v[140:143]\n"
"ds_read_b32 v146, v9 offset:9728\n"
"v_mov_b32 v144, 0\n"
"ds_read_b32 v172, v9 offset:34304\n"
"v_mov_b32 v145, 0\n"
"v_mfma_f32_16x16x32_bf16 v[152:155], v[198:201], v[136:139], v[152:155]\n"
"global_store_dwordx4 v15, v[160:163], s[66:67] sc1\n"
"v_mfma_f32_16x16x32_bf16 v[156:159], v[224:227], v[136:139], v[156:159]\n"
"v_mov_b32 v147, 0\n"
"v_mov_b32 v171, 0\n"
"v_mov_b32 v174, 0\n"
"v_mfma_f32_16x16x32_bf16 v[164:167], v[250:253], v[136:139], v[164:167]\n"
"s_waitcnt lgkmcnt(0)\n"
"v_mad_u32_u24 v16, v16, s40, v19\n"
"v_mov_b32 v173, 0\n"
"s_nop 1\n"
"v_mfma_f32_16x16x32_bf16 v[140:143], v[148:151], v[170:173], v[140:143]\n"
"global_store_dwordx2 v16, v[230:231], s[66:67] offset:256 sc1\n"
"v_mov_b32 v169, 0\n"
"v_mfma_f32_16x16x32_bf16 v[140:143], v[148:151], v[144:147], v[140:143]\n"
"s_nop 0\n"
"v_mfma_f32_16x16x32_bf16 v[164:167], v[232:235], v[168:171], v[164:167]\n"
"v_mov_b32 v175, 0\n"
"s_nop 1\n"
"v_mfma_f32_16x16x32_bf16 v[164:167], v[244:247], v[172:175], v[164:167]\n"
"v_mfma_f32_16x16x32_bf16 v[164:167], v[252:255], v[144:147], v[164:167]\n"
"s_nop 0\n"
"v_rcp_f32 v16, v140\n"
"v_mfma_f32_16x16x32_bf16 v[152:155], v[180:183], v[168:171], v[152:155]\n"
"ds_read2st64_b32 v[138:139], v14 offset0:128 offset1:129\n"
"v_mfma_f32_16x16x32_bf16 v[152:155], v[192:195], v[172:175], v[152:155]\n"
"ds_read2st64_b32 v[136:137], v14 offset0:126 offset1:127\n"
"v_add_u32 v140, 0xd4800000, v140\n"
"ds_read2st64_b32 v[160:161], v14 offset0:150 offset1:151\n"
"v_mul_f32 v166, v166, v16\n"
"v_mfma_f32_16x16x32_bf16 v[152:155], v[200:203], v[144:147], v[152:155]\n"
"v_cmp_lt_u32 vcc, 0x28000000, v140\n"
"v_mfma_f32_16x16x32_bf16 v[156:159], v[206:209], v[168:171], v[156:159]\n"
"v_mul_f32 v164, v164, v16\n"
"v_mul_f32 v165, v165, v16\n"
"ds_read_b128 v[140:143], v17 offset:54272\n"
"v_mfma_f32_16x16x32_bf16 v[156:159], v[218:221], v[172:175], v[156:159]\n"
"ds_read_b128 v[148:151], v17 offset:53248\n"
"v_cvt_pk_bf16_f32 v240, v164, v165\n"
"v_mfma_f32_16x16x32_bf16 v[156:159], v[226:229], v[144:147], v[156:159]\n"
"s_waitcnt lgkmcnt(3)\n"
"ds_read2st64_b32 v[162:163], v14 offset0:152 offset1:153\n"
"v_mfma_f32_16x16x32_bf16 v[144:147], v[96:99], v[136:139], 0\n"
"v_mul_f32 v167, v167, v16\n"
"v_mul_f32 v153, v153, v16\n"
"ds_read2st64_b32 v[170:171], v14 offset0:176 offset1:177\n"
"v_mfma_f32_16x16x32_bf16 v[172:175], v[100:103], v[136:139], 0\n"
"s_or_b64 s[62:63], s[62:63], vcc\n"
"v_mul_f32 v152, v152, v16\n"
"v_cvt_pk_bf16_f32 v241, v166, v167\n"
"s_waitcnt lgkmcnt(2)\n"
"v_mfma_f32_16x16x32_bf16 v[164:167], v[148:151], v[136:139], 0\n"
"s_waitcnt lgkmcnt(1)\n"
"ds_read2st64_b32 v[168:169], v14 offset0:174 offset1:175\n"
"v_mfma_f32_16x16x32_bf16 v[144:147], v[104:107], v[160:163], v[144:147]\n"
"v_mul_f32 v155, v155, v16\n"
"v_cvt_pk_bf16_f32 v152, v152, v153\n"
"ds_read2st64_b32 v[148:149], v14 offset0:198 offset1:199\n"
"ds_read_b128 v[136:139], v17 offset:55296\n"
"v_mfma_f32_16x16x32_bf16 v[172:175], v[108:111], v[160:163], v[172:175]\n"
"v_mul_f32 v154, v154, v16\n"
"v_mul_f32 v156, v156, v16\n"
"v_mfma_f32_16x16x32_bf16 v[164:167], v[140:143], v[160:163], v[164:167]\n"
"s_waitcnt lgkmcnt(2)\n"
"ds_read2st64_b32 v[150:151], v14 offset0:200 offset1:201\n"
"ds_read2st64_b32 v[160:161], v14 offset0:130 offset1:131\n"
"ds_read_b128 v[140:143], v17 offset:56320\n"
"v_mfma_f32_16x16x32_bf16 v[144:147], v[0:3], v[168:171], v[144:147]\n"
"v_mul_f32 v158, v158, v16\n"
"v_mul_f32 v157, v157, v16\n"
"v_mfma_f32_16x16x32_bf16 v[172:175], v[112:115], v[168:171], v[172:175]\n"
"s_waitcnt lgkmcnt(3)\n"
"v_cvt_pk_bf16_f32 v153, v154, v155\n"
"v_cvt_pk_bf16_f32 v154, v156, v157\n"
"v_mfma_f32_16x16x32_bf16 v[164:167], v[136:139], v[168:171], v[164:167]\n"
"s_waitcnt lgkmcnt(2)\n"
"ds_read_b128 v[136:139], v17 offset:57344\n"
"ds_read2st64_b32 v[162:163], v14 offset0:154 offset1:155\n"
"v_mfma_f32_16x16x32_bf16 v[144:147], v[116:119], v[148:151], v[144:147]\n"
"s_setprio 0\n"
"v_mul_f32 v159, v159, v16\n"
"v_cvt_pk_bf16_f32 v155, v158, v159\n"
"ds_read2st64_b32 v[158:159], v14 offset0:202 offset1:203\n"
"v_mfma_f32_16x16x32_bf16 v[172:175], v[120:123], v[148:151], v[172:175]\n"
"s_waitcnt lgkmcnt(3)\n"
"ds_read2st64_b32 v[156:157], v14 offset0:178 offset1:179\n"
"v_mfma_f32_16x16x32_bf16 v[164:167], v[140:143], v[148:151], v[164:167]\n"
"s_waitcnt lgkmcnt(2)\n"
"v_mfma_f32_16x16x32_bf16 v[144:147], v[4:7], v[160:163], v[144:147]\n"
"ds_read_b128 v[148:151], v12 offset:12288\n"
"v_mfma_f32_16x16x32_bf16 v[172:175], v[124:127], v[160:163], v[172:175]\n"
"ds_read_b128 v[140:143], v17 offset:58368\n"
"v_mfma_f32_16x16x32_bf16 v[164:167], v[136:139], v[160:163], v[164:167]\n"
"s_waitcnt lgkmcnt(2)\n"
"ds_read_b128 v[136:139], v13 offset:12288\n"
"v_mfma_f32_16x16x32_bf16 v[144:147], v[128:131], v[156:159], v[144:147]\n"
"s_waitcnt vmcnt(2)\n"
"s_barrier\n"
"v_mfma_f32_16x16x32_bf16 v[172:175], v[132:135], v[156:159], v[172:175]\n"
"s_waitcnt lgkmcnt(1)\n"
"v_mfma_f32_16x16x32_bf16 v[164:167], v[140:143], v[156:159], v[164:167]\n"
"ds_read_b128 v[156:159], v12 offset:14336\n"
"v_mfma_f32_16x16x32_bf16 v[140:143], v[148:151], v[28:31], 0\n"
"v_mfma_f32_16x16x32_bf16 v[160:163], v[148:151], v[32:35], 0\n"
"s_mov_b32 m0, s54\n"
"s_nop 0\n"
"ds_write_addtid_b32 v241 offset:33536\n"
"v_mfma_f32_16x16x32_bf16 v[168:171], v[148:151], v[24:27], 0\n"
"s_waitcnt lgkmcnt(2)\n"
"v_cmp_class_f32_e64 vcc, v144, s64\n"
"ds_read_b128 v[148:151], v13 offset:14336\n"
"v_mfma_f32_16x16x32_bf16 v[140:143], v[136:139], v[40:43], v[140:143]\n"
"ds_write_addtid_b32 v154 offset:32768\n"
"ds_write_addtid_b32 v240 offset:33280\n"
"v_mfma_f32_16x16x32_bf16 v[160:163], v[136:139], v[44:47], v[160:163]\n"
"v_cvt_pk_bf16_f32 v144, v144, v145\n"
"v_mfma_f32_16x16x32_bf16 v[168:171], v[136:139], v[36:39], v[168:171]\n"
"s_waitcnt lgkmcnt(4)\n"
"ds_read_b128 v[136:139], v12 offset:16384\n"
"v_cvt_pk_bf16_f32 v145, v146, v147\n"
"v_cvt_pk_bf16_f32 v146, v172, v173\n"
"v_mfma_f32_16x16x32_bf16 v[140:143], v[156:159], v[48:51], v[140:143]\n"
"ds_write_addtid_b32 v155 offset:33024\n"
"ds_write_addtid_b32 v153 offset:32512\n"
"v_mfma_f32_16x16x32_bf16 v[160:163], v[156:159], v[52:55], v[160:163]\n"
"v_cvt_pk_bf16_f32 v164, v164, v165\n"
"v_cvt_pk_bf16_f32 v165, v166, v167\n"
"v_mfma_f32_16x16x32_bf16 v[168:171], v[156:159], v[56:59], v[168:171]\n"
"ds_write_addtid_b32 v152 offset:32256\n"
"ds_read_b128 v[152:155], v13 offset:16384\n"
"s_waitcnt lgkmcnt(7)\n"
"v_mfma_f32_16x16x32_bf16 v[140:143], v[148:151], v[60:63], v[140:143]\n"
"s_mov_b32 m0, s68\n"
"s_nop 0\n"
"ds_write_addtid_b32 v146 offset:45568\n"
"v_mfma_f32_16x16x32_bf16 v[160:163], v[148:151], v[64:67], v[160:163]\n"
"ds_write_addtid_b32 v164 offset:46080\n"
"v_cvt_pk_bf16_f32 v147, v174, v175\n"
"ds_write_addtid_b32 v165 offset:46336\n"
"v_mfma_f32_16x16x32_bf16 v[168:171], v[148:151], v[68:71], v[168:171]\n"
"s_waitcnt lgkmcnt(7)\n"
"v_mfma_f32_16x16x32_bf16 v[140:143], v[136:139], v[72:75], v[140:143]\n"
"ds_write_addtid_b32 v144 offset:45056\n"
"v_mfma_f32_16x16x32_bf16 v[160:163], v[136:139], v[76:79], v[160:163]\n"
"ds_write_addtid_b32 v145 offset:45312\n"
"s_or_b64 s[62:63], s[62:63], vcc\n"
"v_mfma_f32_16x16x32_bf16 v[168:171], v[136:139], v[80:83], v[168:171]\n"
"ds_write_addtid_b32 v147 offset:45824\n"
"s_waitcnt lgkmcnt(6)\n"
"v_mfma_f32_16x16x32_bf16 v[140:143], v[152:155], v[84:87], v[140:143]\n"
"v_mfma_f32_16x16x32_bf16 v[160:163], v[152:155], v[88:91], v[160:163]\n"
"v_mfma_f32_16x16x32_bf16 v[168:171], v[152:155], v[92:95], v[168:171]\n"
"s_waitcnt lgkmcnt(0)\n"
"s_barrier\n"
"ds_read2st64_b32 v[144:145], v9 offset0:112 offset1:113\n"
"s_nop 3\n"
"v_cvt_pk_bf16_f32 v204, v160, v161\n"
"ds_read2st64_b32 v[146:147], v9 offset0:114 offset1:115\n"
"v_cvt_pk_bf16_f32 v178, v140, v141\n"
"v_mov_b32 v137, 0x3f803f80\n"
"v_mov_b32 v136, 0x3f803f80\n"
"ds_read2st64_b32 v[148:149], v9 offset0:116 offset1:117\n"
"v_cvt_pk_bf16_f32 v179, v142, v143\n"
"ds_read_u16 v16, v21 offset:1056\n"
"v_mov_b32 v138, 0x3f803f80\n"
"v_mov_b32 v139, 0x3f803f80\n"
"v_cvt_pk_bf16_f32 v231, v170, v171\n"
"ds_read2st64_b32 v[150:151], v9 offset0:118 offset1:119\n"
"ds_read2st64_b32 v[240:241], v20 offset0:4 offset1:5\n"
"s_waitcnt lgkmcnt(4)\n"
"v_mfma_f32_16x16x32_bf16 v[140:143], v[136:139], v[144:147], 0\n"
"v_cvt_pk_bf16_f32 v230, v168, v169\n"
"v_cvt_pk_bf16_f32 v205, v162, v163\n"
"ds_read2st64_b32 v[160:161], v20 offset0:0 offset1:1\n"
"v_mfma_f32_16x16x32_bf16 v[152:155], v[182:185], v[144:147], 0\n"
"v_mfma_f32_16x16x32_bf16 v[156:159], v[208:211], v[144:147], 0\n"
"ds_read2st64_b32 v[162:163], v20 offset0:2 offset1:3\n"
"v_mfma_f32_16x16x32_bf16 v[164:167], v[234:237], v[144:147], 0\n"
"s_waitcnt lgkmcnt(3)\n"
"ds_read2st64_b32 v[146:147], v9 offset0:130 offset1:131\n"
"ds_read2st64_b32 v[144:145], v9 offset0:128 offset1:129\n"
"v_mfma_f32_16x16x32_bf16 v[140:143], v[136:139], v[148:151], v[140:143]\n"
"v_mfma_f32_16x16x32_bf16 v[152:155], v[190:193], v[148:151], v[152:155]\n"
"v_mad_u32_u24 v15, v16, s40, v18\n"
"v_mfma_f32_16x16x32_bf16 v[156:159], v[216:219], v[148:151], v[156:159]\n"
"v_mad_u32_u24 v16, v16, s40, v19\n"
"v_mfma_f32_16x16x32_bf16 v[164:167], v[242:245], v[148:151], v[164:167]\n"
"s_waitcnt lgkmcnt(2)\n"
"global_store_dwordx4 v15, v[160:163], s[66:67] sc1\n"
"s_waitcnt lgkmcnt(0)\n"
"v_mfma_f32_16x16x32_bf16 v[140:143], v[136:139], v[144:147], v[140:143]\n"
"ds_read2st64_b32 v[148:149], v9 offset0:132 offset1:133\n"
"v_mfma_f32_16x16x32_bf16 v[152:155], v[178:181], v[144:147], v[152:155]\n"
"ds_read2st64_b32 v[150:151], v9 offset0:134 offset1:135\n"
"ds_read2st64_b32 v[162:163], v9 offset0:36 offset1:37\n"
"global_store_dwordx2 v16, v[240:241], s[66:67] offset:256 sc1\n"
"v_mfma_f32_16x16x32_bf16 v[156:159], v[204:207], v[144:147], v[156:159]\n"
"v_mov_b32 v160, 0\n"
"v_mov_b32 v161, 0\n"
"v_mfma_f32_16x16x32_bf16 v[164:167], v[230:233], v[144:147], v[164:167]\n"
"s_waitcnt lgkmcnt(1)\n"
"v_mfma_f32_16x16x32_bf16 v[140:143], v[136:139], v[148:151], v[140:143]\n"
"s_waitcnt lgkmcnt(0)\n"
"ds_read2st64_b32 v[170:171], v14 offset0:152 offset1:153\n"
"v_mfma_f32_16x16x32_bf16 v[140:143], v[136:139], v[160:163], v[140:143]\n"
"ds_read2st64_b32 v[138:139], v14 offset0:128 offset1:129\n"
"s_setprio 2\n"
"v_mfma_f32_16x16x32_bf16 v[164:167], v[252:255], v[148:151], v[164:167]\n"
"ds_read2st64_b32 v[136:137], v14 offset0:126 offset1:127\n"
"v_mfma_f32_16x16x32_bf16 v[164:167], v[244:247], v[160:163], v[164:167]\n"
"ds_read_b128 v[144:147], v17 offset:53248\n"
"v_mfma_f32_16x16x32_bf16 v[152:155], v[200:203], v[148:151], v[152:155]\n"
"s_nop 0\n"
"v_rcp_f32 v16, v140\n"
"v_mfma_f32_16x16x32_bf16 v[152:155], v[192:195], v[160:163], v[152:155]\n"
"ds_read2st64_b32 v[168:169], v14 offset0:150 offset1:151\n"
"v_mfma_f32_16x16x32_bf16 v[156:159], v[226:229], v[148:151], v[156:159]\n"
"s_nop 0\n"
"v_mul_f32 v167, v167, v16\n"
"v_add_u32 v140, 0xd4800000, v140\n"
"v_mfma_f32_16x16x32_bf16 v[156:159], v[218:221], v[160:163], v[156:159]\n"
"s_waitcnt lgkmcnt(2)\n"
"ds_read2st64_b32 v[162:163], v14 offset0:176 offset1:177\n"
"v_cmp_lt_u32 vcc, 0x28000000, v140\n"
"ds_read_b128 v[140:143], v17 offset:54272\n"
"v_mfma_f32_16x16x32_bf16 v[148:151], v[96:99], v[136:139], 0\n"
"v_mul_f32 v155, v155, v16\n"
"v_mul_f32 v165, v165, v16\n"
"v_mul_f32 v164, v164, v16\n"
"v_mul_f32 v166, v166, v16\n"
"ds_read2st64_b32 v[160:161], v14 offset0:174 offset1:175\n"
"v_mul_f32 v152, v152, v16\n"
"v_mfma_f32_16x16x32_bf16 v[172:175], v[100:103], v[136:139], 0\n"
"s_waitcnt lgkmcnt(4)\n"
"v_cvt_pk_bf16_f32 v241, v166, v167\n"
"v_cvt_pk_bf16_f32 v240, v164, v165\n"
"v_mfma_f32_16x16x32_bf16 v[164:167], v[144:147], v[136:139], 0\n"
"s_waitcnt lgkmcnt(3)\n"
"ds_read_b128 v[136:139], v17 offset:55296\n"
"v_mfma_f32_16x16x32_bf16 v[148:151], v[104:107], v[168:171], v[148:151]\n"
"v_mul_f32 v153, v153, v16\n"
"s_or_b64 s[62:63], s[62:63], vcc\n"
"v_mfma_f32_16x16x32_bf16 v[172:175], v[108:111], v[168:171], v[172:175]\n"
"s_waitcnt lgkmcnt(2)\n"
"ds_read2st64_b32 v[146:147], v14 offset0:200 offset1:201\n"
"v_mfma_f32_16x16x32_bf16 v[164:167], v[140:143], v[168:171], v[164:167]\n"
"s_waitcnt lgkmcnt(2)\n"
"ds_read2st64_b32 v[144:145], v14 offset0:198 offset1:199\n"
"v_mfma_f32_16x16x32_bf16 v[148:151], v[0:3], v[160:163], v[148:151]\n"
"ds_read2st64_b32 v[170:171], v14 offset0:154 offset1:155\n"
"v_cvt_pk_bf16_f32 v152, v152, v153\n"
"ds_read2st64_b32 v[168:169], v14 offset0:130 offset1:131\n"
"v_mfma_f32_16x16x32_bf16 v[172:175], v[112:115], v[160:163], v[172:175]\n"
"s_waitcnt lgkmcnt(4)\n"
"v_mul_f32 v159, v159, v16\n"
"v_mul_f32 v158, v158, v16\n"
"ds_read_b128 v[140:143], v17 offset:56320\n"
"v_mfma_f32_16x16x32_bf16 v[164:167], v[136:139], v[160:163], v[164:167]\n"
"s_waitcnt lgkmcnt(3)\n"
"v_mul_f32 v156, v156, v16\n"
"s_setprio 0\n"
"ds_read_b128 v[136:139], v17 offset:57344\n"
"v_mfma_f32_16x16x32_bf16 v[148:151], v[116:119], v[144:147], v[148:151]\n"
"v_mul_f32 v154, v154, v16\n"
"v_cvt_pk_bf16_f32 v153, v154, v155\n"
"v_mfma_f32_16x16x32_bf16 v[172:175], v[120:123], v[144:147], v[172:175]\n"
"s_waitcnt lgkmcnt(1)\n"
"v_mul_f32 v157, v157, v16\n"
"v_cvt_pk_bf16_f32 v154, v156, v157\n"
"v_cvt_pk_bf16_f32 v155, v158, v159\n"
"v_mfma_f32_16x16x32_bf16 v[164:167], v[140:143], v[144:147], v[164:167]\n"
"ds_read2st64_b32 v[158:159], v14 offset0:202 offset1:203\n"
"ds_read2st64_b32 v[156:157], v14 offset0:178 offset1:179\n"
"v_mfma_f32_16x16x32_bf16 v[148:151], v[4:7], v[168:171], v[148:151]\n"
"ds_read_b128 v[140:143], v17 offset:58368\n"
"v_mfma_f32_16x16x32_bf16 v[172:175], v[124:127], v[168:171], v[172:175]\n"
"s_waitcnt lgkmcnt(3)\n"
"v_mfma_f32_16x16x32_bf16 v[164:167], v[136:139], v[168:171], v[164:167]\n"
"s_waitcnt lgkmcnt(1)\n"
"v_mfma_f32_16x16x32_bf16 v[148:151], v[128:131], v[156:159], v[148:151]\n"
"ds_read_b128 v[144:147], v12 offset:36864\n"
"ds_read_b128 v[136:139], v13 offset:36864\n"
"v_mfma_f32_16x16x32_bf16 v[172:175], v[132:135], v[156:159], v[172:175]\n"
"s_barrier\n"
"s_waitcnt lgkmcnt(2)\n"
"v_mfma_f32_16x16x32_bf16 v[164:167], v[140:143], v[156:159], v[164:167]\n"
"s_mov_b32 m0, s54\n"
"s_nop 0\n"
"v_cmp_class_f32_e64 vcc, v148, s64\n"
"s_nop 4\n"
"v_cvt_pk_bf16_f32 v164, v164, v165\n"
"s_waitcnt lgkmcnt(1)\n"
"v_mfma_f32_16x16x32_bf16 v[140:143], v[144:147], v[28:31], 0\n"
"v_mfma_f32_16x16x32_bf16 v[156:159], v[144:147], v[32:35], 0\n"
"ds_write_addtid_b32 v153 offset:32512\n"
"v_mfma_f32_16x16x32_bf16 v[32:35], v[144:147], v[24:27], 0\n"
"s_waitcnt lgkmcnt(1)\n"
"ds_read_b128 v[24:27], v13 offset:38912\n"
"v_mfma_f32_16x16x32_bf16 v[140:143], v[136:139], v[40:43], v[140:143]\n"
"ds_read_b128 v[28:31], v12 offset:38912\n"
"v_cvt_pk_bf16_f32 v148, v148, v149\n"
"v_cvt_pk_bf16_f32 v149, v150, v151\n"
"v_cvt_pk_bf16_f32 v165, v166, v167\n"
"v_cvt_pk_bf16_f32 v151, v174, v175\n"
"ds_write_addtid_b32 v240 offset:33280\n"
"v_mfma_f32_16x16x32_bf16 v[156:159], v[136:139], v[44:47], v[156:159]\n"
"ds_write_addtid_b32 v154 offset:32768\n"
"s_or_b64 s[62:63], s[62:63], vcc\n"
"ds_write_addtid_b32 v155 offset:33024\n"
"v_mfma_f32_16x16x32_bf16 v[32:35], v[136:139], v[36:39], v[32:35]\n"
"ds_write_addtid_b32 v241 offset:33536\n"
"ds_write_addtid_b32 v152 offset:32256\n"
"v_cvt_pk_bf16_f32 v150, v172, v173\n"
"s_waitcnt lgkmcnt(5)\n"
"v_mfma_f32_16x16x32_bf16 v[140:143], v[28:31], v[48:51], v[140:143]\n"
"ds_read_b128 v[36:39], v12 offset:40960\n"
"v_mfma_f32_16x16x32_bf16 v[156:159], v[28:31], v[52:55], v[156:159]\n"
"s_mov_b32 m0, s68\n"
"v_mfma_f32_16x16x32_bf16 v[32:35], v[28:31], v[56:59], v[32:35]\n"
"v_mfma_f32_16x16x32_bf16 v[140:143], v[24:27], v[60:63], v[140:143]\n"
"ds_write_addtid_b32 v151 offset:45824\n"
"ds_write_addtid_b32 v149 offset:45312\n"
"v_mfma_f32_16x16x32_bf16 v[156:159], v[24:27], v[64:67], v[156:159]\n"
"ds_write_addtid_b32 v164 offset:46080\n"
"ds_write_addtid_b32 v165 offset:46336\n"
"v_mfma_f32_16x16x32_bf16 v[32:35], v[24:27], v[68:71], v[32:35]\n"
"s_waitcnt lgkmcnt(4)\n"
"ds_read_b128 v[28:31], v13 offset:40960\n"
"v_mfma_f32_16x16x32_bf16 v[140:143], v[36:39], v[72:75], v[140:143]\n"
"v_mfma_f32_16x16x32_bf16 v[156:159], v[36:39], v[76:79], v[156:159]\n"
"ds_write_addtid_b32 v148 offset:45056\n"
"ds_write_addtid_b32 v150 offset:45568\n"
"v_mov_b32 v24, 0x3f803f80\n"
"v_mov_b32 v25, 0x3f803f80\n"
"v_mfma_f32_16x16x32_bf16 v[32:35], v[36:39], v[80:83], v[32:35]\n"
"s_waitcnt lgkmcnt(2)\n"
"v_mfma_f32_16x16x32_bf16 v[140:143], v[28:31], v[84:87], v[140:143]\n"
"v_mfma_f32_16x16x32_bf16 v[156:159], v[28:31], v[88:91], v[156:159]\n"
"v_mov_b32 v36, 0\n"
"v_mov_b32 v26, 0x3f803f80\n"
"v_mfma_f32_16x16x32_bf16 v[32:35], v[28:31], v[92:95], v[32:35]\n"
"s_waitcnt lgkmcnt(0)\n"
"v_mov_b32 v27, 0x3f803f80\n"
"s_barrier\n"
"ds_read2st64_b32 v[38:39], v9 offset0:122 offset1:123\n"
"ds_read_b32 v37, v9 offset:30976\n"
"ds_read2st64_b32 v[30:31], v9 offset0:130 offset1:131\n"
"v_cvt_pk_bf16_f32 v206, v156, v157\n"
"s_setprio 2\n"
"ds_read2st64_b32 v[28:29], v9 offset0:128 offset1:129\n"
"v_cvt_pk_bf16_f32 v232, v32, v33\n"
"v_cvt_pk_bf16_f32 v180, v140, v141\n"
"v_cvt_pk_bf16_f32 v233, v34, v35\n"
"s_waitcnt lgkmcnt(2)\n"
"v_mfma_f32_16x16x32_bf16 v[32:35], v[24:27], v[36:39], 0\n"
"ds_read_u16 v16, v21 offset:1088\n"
"v_cvt_pk_bf16_f32 v181, v142, v143\n"
"v_cvt_pk_bf16_f32 v207, v158, v159\n"
"v_mfma_f32_16x16x32_bf16 v[40:43], v[182:185], v[36:39], 0\n"
"ds_read2st64_b32 v[44:45], v20 offset0:0 offset1:1\n"
"v_mfma_f32_16x16x32_bf16 v[48:51], v[208:211], v[36:39], 0\n"
"ds_read2st64_b32 v[46:47], v20 offset0:2 offset1:3\n"
"v_mfma_f32_16x16x32_bf16 v[52:55], v[234:237], v[36:39], 0\n"
"s_waitcnt lgkmcnt(3)\n"
"ds_read_b32 v37, v9 offset:34048\n"
"ds_read2st64_b32 v[38:39], v9 offset0:134 offset1:135\n"
"v_mfma_f32_16x16x32_bf16 v[32:35], v[24:27], v[28:31], v[32:35]\n"
"ds_read2st64_b32 v[240:241], v20 offset0:4 offset1:5\n"
"v_mfma_f32_16x16x32_bf16 v[40:43], v[178:181], v[28:31], v[40:43]\n"
"s_waitcnt lgkmcnt(5)\n"
"v_mov_b32 v36, 0\n"
"v_mad_u32_u24 v15, v16, s40, v18\n"
"ds_read2st64_b32 v[60:61], v9 offset0:126 offset1:127\n"
"v_mfma_f32_16x16x32_bf16 v[48:51], v[204:207], v[28:31], v[48:51]\n"
"s_waitcnt lgkmcnt(4)\n"
"global_store_dwordx4 v15, v[44:47], s[66:67] sc1\n"
"v_mfma_f32_16x16x32_bf16 v[52:55], v[230:233], v[28:31], v[52:55]\n"
"s_waitcnt lgkmcnt(2)\n"
"ds_read_b32 v31, v9 offset:9984\n"
"ds_read2st64_b32 v[58:59], v9 offset0:124 offset1:125\n"
"v_mfma_f32_16x16x32_bf16 v[32:35], v[24:27], v[36:39], v[32:35]\n"
"v_mad_u32_u24 v16, v16, s40, v19\n"
"v_mov_b32 v30, 0\n"
"v_mfma_f32_16x16x32_bf16 v[40:43], v[192:195], v[36:39], v[40:43]\n"
"v_mov_b32 v56, 0\n"
"v_mfma_f32_16x16x32_bf16 v[48:51], v[218:221], v[36:39], v[48:51]\n"
"v_mov_b32 v29, 0\n"
"v_mov_b32 v28, 0\n"
"v_mfma_f32_16x16x32_bf16 v[52:55], v[244:247], v[36:39], v[52:55]\n"
"s_waitcnt lgkmcnt(3)\n"
"global_store_dwordx2 v16, v[240:241], s[66:67] offset:256 sc1\n"
"s_waitcnt lgkmcnt(0)\n"
"v_mfma_f32_16x16x32_bf16 v[32:35], v[24:27], v[58:61], v[32:35]\n"
"ds_read2st64_b32 v[182:183], v14 offset0:126 offset1:127\n"
"v_mov_b32 v62, 0\n"
"v_mfma_f32_16x16x32_bf16 v[32:35], v[24:27], v[28:31], v[32:35]\n"
"v_mov_b32 v63, 0\n"
"v_mov_b32 v57, 0\n"
"s_nop 1\n"
"v_mfma_f32_16x16x32_bf16 v[52:55], v[236:239], v[56:59], v[52:55]\n"
"v_mfma_f32_16x16x32_bf16 v[52:55], v[248:251], v[60:63], v[52:55]\n"
"v_mfma_f32_16x16x32_bf16 v[52:55], v[252:255], v[28:31], v[52:55]\n"
"v_mfma_f32_16x16x32_bf16 v[40:43], v[184:187], v[56:59], v[40:43]\n"
"ds_read2st64_b32 v[184:185], v14 offset0:128 offset1:129\n"
"v_mfma_f32_16x16x32_bf16 v[40:43], v[196:199], v[60:63], v[40:43]\n"
"ds_read2st64_b32 v[196:197], v14 offset0:150 offset1:151\n"
"ds_read_b128 v[186:189], v17 offset:53248\n"
"v_mfma_f32_16x16x32_bf16 v[40:43], v[200:203], v[28:31], v[40:43]\n"
"v_rcp_f32 v16, v32\n"
"v_add_u32 v32, 0xd4800000, v32\n"
"v_mfma_f32_16x16x32_bf16 v[48:51], v[210:213], v[56:59], v[48:51]\n"
"ds_read2st64_b32 v[212:213], v14 offset0:174 offset1:175\n"
"ds_read_b128 v[208:211], v17 offset:54272\n"
"v_mfma_f32_16x16x32_bf16 v[48:51], v[222:225], v[60:63], v[48:51]\n"
"ds_read2st64_b32 v[198:199], v14 offset0:152 offset1:153\n"
"v_mul_f32 v53, v53, v16\n"
"v_cmp_lt_u32 vcc, 0x28000000, v32\n"
"v_mul_f32 v40, v40, v16\n"
"v_mfma_f32_16x16x32_bf16 v[48:51], v[226:229], v[28:31], v[48:51]\n"
"s_waitcnt lgkmcnt(5)\n"
"v_mul_f32 v52, v52, v16\n"
"v_mul_f32 v55, v55, v16\n"
"v_mul_f32 v54, v54, v16\n"
"v_mfma_f32_16x16x32_bf16 v[222:225], v[96:99], v[182:185], 0\n"
"ds_read_b128 v[248:251], v17 offset:55296\n"
"ds_read2st64_b32 v[214:215], v14 offset0:176 offset1:177\n"
"v_mfma_f32_16x16x32_bf16 v[234:237], v[100:103], v[182:185], 0\n"
"s_waitcnt lgkmcnt(5)\n"
"v_cvt_pk_bf16_f32 v241, v54, v55\n"
"v_mul_f32 v42, v42, v16\n"
"v_mfma_f32_16x16x32_bf16 v[24:27], v[186:189], v[182:185], 0\n"
"ds_read_b128 v[186:189], v17 offset:56320\n"
"ds_read2st64_b32 v[182:183], v14 offset0:198 offset1:199\n"
"v_cvt_pk_bf16_f32 v240, v52, v53\n"
"v_mul_f32 v41, v41, v16\n"
"v_cvt_pk_bf16_f32 v40, v40, v41\n"
"s_waitcnt lgkmcnt(4)\n"
"v_mfma_f32_16x16x32_bf16 v[222:225], v[104:107], v[196:199], v[222:225]\n"
"ds_read2st64_b32 v[184:185], v14 offset0:200 offset1:201\n"
"v_mfma_f32_16x16x32_bf16 v[234:237], v[108:111], v[196:199], v[234:237]\n"
"s_or_b64 s[62:63], s[62:63], vcc\n"
"v_mul_f32 v43, v43, v16\n"
"v_cvt_pk_bf16_f32 v41, v42, v43\n"
"v_mfma_f32_16x16x32_bf16 v[24:27], v[208:211], v[196:199], v[24:27]\n"
"s_waitcnt lgkmcnt(3)\n"
"ds_read2st64_b32 v[196:197], v14 offset0:130 offset1:131\n"
"ds_read2st64_b32 v[210:211], v14 offset0:202 offset1:203\n"
"v_mfma_f32_16x16x32_bf16 v[222:225], v[0:3], v[212:215], v[222:225]\n"
"ds_read2st64_b32 v[208:209], v14 offset0:178 offset1:179\n"
"v_mul_f32 v48, v48, v16\n"
"v_mfma_f32_16x16x32_bf16 v[234:237], v[112:115], v[212:215], v[234:237]\n"
"v_mul_f32 v51, v51, v16\n"
"v_mul_f32 v49, v49, v16\n"
"v_mfma_f32_16x16x32_bf16 v[24:27], v[248:251], v[212:215], v[24:27]\n"
"s_mov_b32 m0, s54\n"
"ds_read2st64_b32 v[198:199], v14 offset0:154 offset1:155\n"
"ds_write_addtid_b32 v40 offset:6144\n"
"v_mul_f32 v50, v50, v16\n"
"v_cvt_pk_bf16_f32 v42, v48, v49\n"
"s_waitcnt lgkmcnt(5)\n"
"v_mfma_f32_16x16x32_bf16 v[222:225], v[116:119], v[182:185], v[222:225]\n"
"s_setprio 0\n"
"ds_write_addtid_b32 v41 offset:6400\n"
"ds_write_addtid_b32 v42 offset:6656\n"
"ds_write_addtid_b32 v241 offset:7424\n"
"v_cvt_pk_bf16_f32 v43, v50, v51\n"
"v_mfma_f32_16x16x32_bf16 v[234:237], v[120:123], v[182:185], v[234:237]\n"
"ds_write_addtid_b32 v240 offset:7168\n"
"ds_write_addtid_b32 v43 offset:6912\n"
"v_mfma_f32_16x16x32_bf16 v[24:27], v[186:189], v[182:185], v[24:27]\n"
"s_waitcnt lgkmcnt(6)\n"
"v_mov_b32 v215, 0x3f803f80\n"
"ds_read_b128 v[182:185], v17 offset:57344\n"
"v_mfma_f32_16x16x32_bf16 v[222:225], v[4:7], v[196:199], v[222:225]\n"
"v_mov_b32 v213, 0x3f803f80\n"
"v_mov_b32 v214, 0x3f803f80\n"
"v_mfma_f32_16x16x32_bf16 v[234:237], v[124:127], v[196:199], v[234:237]\n"
"s_waitcnt lgkmcnt(1)\n"
"ds_read_b128 v[186:189], v17 offset:58368\n"
"s_barrier\n"
"ds_read2st64_b32 v[238:239], v9 offset0:112 offset1:113\n"
"s_waitcnt lgkmcnt(2)\n"
"v_mfma_f32_16x16x32_bf16 v[24:27], v[182:185], v[196:199], v[24:27]\n"
"ds_read2st64_b32 v[240:241], v9 offset0:114 offset1:115\n"
"v_subrev_u32 v16, 24576, v9\n"
"v_mfma_f32_16x16x32_bf16 v[222:225], v[128:131], v[208:211], v[222:225]\n"
"s_setprio 2\n"
"ds_read2st64_b32 v[30:31], v16 offset0:16 offset1:17\n"
"ds_read2st64_b32 v[184:185], v9 offset0:118 offset1:119\n"
"v_mov_b32 v212, 0x3f803f80\n"
"v_mfma_f32_16x16x32_bf16 v[234:237], v[132:135], v[208:211], v[234:237]\n"
"s_waitcnt lgkmcnt(4)\n"
"ds_read2st64_b32 v[182:183], v9 offset0:116 offset1:117\n"
"v_mfma_f32_16x16x32_bf16 v[24:27], v[186:189], v[208:211], v[24:27]\n"
"s_waitcnt lgkmcnt(3)\n"
"v_mfma_f32_16x16x32_bf16 v[186:189], v[212:215], v[238:241], 0\n"
"ds_read2st64_b32 v[32:33], v16 offset0:18 offset1:19\n"
"v_cmp_class_f32_e64 vcc, v222, s64\n"
"v_mfma_f32_16x16x32_bf16 v[196:199], v[190:193], v[238:241], 0\n"
"v_cvt_pk_bf16_f32 v222, v222, v223\n"
"v_cvt_pk_bf16_f32 v223, v224, v225\n"
"v_mfma_f32_16x16x32_bf16 v[208:211], v[216:219], v[238:241], 0\n"
"v_cvt_pk_bf16_f32 v224, v234, v235\n"
"v_cvt_pk_bf16_f32 v225, v236, v237\n"
"v_mfma_f32_16x16x32_bf16 v[234:237], v[242:245], v[238:241], 0\n"
"s_waitcnt lgkmcnt(1)\n"
"v_mov_b32 v28, 0\n"
"v_mov_b32 v29, 0\n"
"v_mfma_f32_16x16x32_bf16 v[186:189], v[212:215], v[182:185], v[186:189]\n"
"s_waitcnt lgkmcnt(0)\n"
"s_setprio 0\n"
"ds_read2st64_b32 v[190:191], v14 offset0:26 offset1:27\n"
"v_mfma_f32_16x16x32_bf16 v[186:189], v[212:215], v[30:33], v[186:189]\n"
"s_setprio 2\n"
"v_cvt_pk_bf16_f32 v24, v24, v25\n"
"v_mov_b32 v35, 0\n"
"v_mfma_f32_16x16x32_bf16 v[234:237], v[252:255], v[182:185], v[234:237]\n"
"v_cvt_pk_bf16_f32 v25, v26, v27\n"
"v_mov_b32 v34, 0\n"
"v_mfma_f32_16x16x32_bf16 v[234:237], v[244:247], v[28:31], v[234:237]\n"
"s_setprio 0\n"
"v_rcp_f32 v16, v186\n"
"v_mfma_f32_16x16x32_bf16 v[234:237], v[230:233], v[32:35], v[234:237]\n"
"s_mov_b32 m0, s54\n"
"s_or_b64 s[62:63], s[62:63], vcc\n"
"v_add_u32 v186, 0xd4800000, v186\n"
"v_mfma_f32_16x16x32_bf16 v[196:199], v[200:203], v[182:185], v[196:199]\n"
"v_cmp_lt_u32 vcc, 0x28000000, v186\n"
"v_mfma_f32_16x16x32_bf16 v[196:199], v[192:195], v[28:31], v[196:199]\n"
"ds_read_b128 v[192:195], v17 offset:53248\n"
"ds_read2st64_b32 v[188:189], v14 offset0:24 offset1:25\n"
"v_mfma_f32_16x16x32_bf16 v[196:199], v[178:181], v[32:35], v[196:199]\n"
"v_mul_f32 v234, v234, v16\n"
"v_mul_f32 v235, v235, v16\n"
"v_mul_f32 v237, v237, v16\n"
"v_cvt_pk_bf16_f32 v216, v234, v235\n"
"v_mul_f32 v236, v236, v16\n"
"v_mfma_f32_16x16x32_bf16 v[208:211], v[226:229], v[182:185], v[208:211]\n"
"s_or_b64 s[62:63], s[62:63], vcc\n"
"ds_write_addtid_b32 v216 offset:33280\n"
"v_cvt_pk_bf16_f32 v217, v236, v237\n"
"v_mfma_f32_16x16x32_bf16 v[208:211], v[218:221], v[28:31], v[208:211]\n"
"ds_write_addtid_b32 v217 offset:33536\n"
"v_mul_f32 v197, v197, v16\n"
"v_mfma_f32_16x16x32_bf16 v[208:211], v[204:207], v[32:35], v[208:211]\n"
"s_waitcnt lgkmcnt(2)\n"
"ds_read2st64_b32 v[180:181], v14 offset0:50 offset1:51\n"
"v_mul_f32 v196, v196, v16\n"
"v_cvt_pk_bf16_f32 v196, v196, v197\n"
"v_mfma_f32_16x16x32_bf16 v[182:185], v[96:99], v[188:191], 0\n"
"ds_read2st64_b32 v[178:179], v14 offset0:48 offset1:49\n"
"ds_write_addtid_b32 v196 offset:32256\n"
"v_mul_f32 v198, v198, v16\n"
"v_mul_f32 v211, v211, v16\n"
"v_mul_f32 v210, v210, v16\n"
"v_mul_f32 v199, v199, v16\n"
"v_mul_f32 v208, v208, v16\n"
"v_cvt_pk_bf16_f32 v197, v198, v199\n"
"v_cvt_pk_bf16_f32 v199, v210, v211\n"
"v_mfma_f32_16x16x32_bf16 v[200:203], v[100:103], v[188:191], 0\n"
"ds_write_addtid_b32 v197 offset:32512\n"
"ds_write_addtid_b32 v199 offset:33024\n"
"v_mfma_f32_16x16x32_bf16 v[204:207], v[192:195], v[188:191], 0\n"
"ds_read_b128 v[186:189], v17 offset:54272\n"
"ds_read2st64_b32 v[192:193], v14 offset0:74 offset1:75\n"
"v_mul_f32 v209, v209, v16\n"
"v_cvt_pk_bf16_f32 v198, v208, v209\n"
"ds_write_addtid_b32 v198 offset:32768\n"
"s_mov_b32 m0, s68\n"
"ds_read_b128 v[194:197], v17 offset:55296\n"
"ds_read2st64_b32 v[190:191], v14 offset0:72 offset1:73\n"
"s_waitcnt lgkmcnt(8)\n"
"v_mfma_f32_16x16x32_bf16 v[182:185], v[104:107], v[178:181], v[182:185]\n"
"ds_write_addtid_b32 v222 offset:45056\n"
"ds_write_addtid_b32 v225 offset:45824\n"
"v_mfma_f32_16x16x32_bf16 v[200:203], v[108:111], v[178:181], v[200:203]\n"
"ds_write_addtid_b32 v24 offset:46080\n"
"s_waitcnt lgkmcnt(7)\n"
"v_mfma_f32_16x16x32_bf16 v[204:207], v[186:189], v[178:181], v[204:207]\n"
"ds_read2st64_b32 v[208:209], v14 offset0:28 offset1:29\n"
"ds_read2st64_b32 v[178:179], v14 offset0:96 offset1:97\n"
"ds_read2st64_b32 v[180:181], v14 offset0:98 offset1:99\n"
"ds_write_addtid_b32 v25 offset:46336\n"
"ds_read2st64_b32 v[210:211], v14 offset0:52 offset1:53\n"
"s_waitcnt lgkmcnt(8)\n"
"v_mfma_f32_16x16x32_bf16 v[182:185], v[0:3], v[190:193], v[182:185]\n"
"ds_write_addtid_b32 v224 offset:45568\n"
"ds_read_b128 v[186:189], v17 offset:56320\n"
"ds_write_addtid_b32 v223 offset:45312\n"
"v_mfma_f32_16x16x32_bf16 v[200:203], v[112:115], v[190:193], v[200:203]\n"
"ds_read2st64_b32 v[214:215], v14 offset0:100 offset1:101\n"
"v_mfma_f32_16x16x32_bf16 v[204:207], v[194:197], v[190:193], v[204:207]\n"
"s_waitcnt lgkmcnt(6)\n"
"v_mfma_f32_16x16x32_bf16 v[182:185], v[116:119], v[178:181], v[182:185]\n"
"ds_read2st64_b32 v[212:213], v14 offset0:76 offset1:77\n"
"v_mfma_f32_16x16x32_bf16 v[200:203], v[120:123], v[178:181], v[200:203]\n"
"s_waitcnt lgkmcnt(3)\n"
"v_mfma_f32_16x16x32_bf16 v[204:207], v[186:189], v[178:181], v[204:207]\n"
"ds_read_b128 v[194:197], v17 offset:58368\n"
"ds_read_b128 v[190:193], v17 offset:57344\n"
"v_mfma_f32_16x16x32_bf16 v[182:185], v[4:7], v[208:211], v[182:185]\n"
"s_waitcnt lgkmcnt(2)\n"
"ds_read_b128 v[186:189], v17 offset:53248\n"
"s_barrier\n"
"ds_read2st64_b32 v[178:179], v14 offset0:126 offset1:127\n"
"v_mfma_f32_16x16x32_bf16 v[200:203], v[124:127], v[208:211], v[200:203]\n"
"s_waitcnt lgkmcnt(2)\n"
"ds_read2st64_b32 v[180:181], v14 offset0:128 offset1:129\n"
"s_setprio 2\n"
"v_mfma_f32_16x16x32_bf16 v[204:207], v[190:193], v[208:211], v[204:207]\n"
"ds_read_u16 v16, v21 offset:1120\n"
"v_mfma_f32_16x16x32_bf16 v[182:185], v[128:131], v[212:215], v[182:185]\n"
"ds_read2st64_b32 v[192:193], v14 offset0:152 offset1:153\n"
"v_mfma_f32_16x16x32_bf16 v[200:203], v[132:135], v[212:215], v[200:203]\n"
"ds_read2st64_b32 v[190:191], v14 offset0:150 offset1:151\n"
"v_mfma_f32_16x16x32_bf16 v[204:207], v[194:197], v[212:215], v[204:207]\n"
"s_waitcnt lgkmcnt(3)\n"
"ds_read2st64_b32 v[210:211], v14 offset0:176 offset1:177\n"
"ds_read_b128 v[194:197], v17 offset:54272\n"
"v_mfma_f32_16x16x32_bf16 v[212:215], v[96:99], v[178:181], 0\n"
"ds_read2st64_b32 v[208:209], v14 offset0:174 offset1:175\n"
"v_cmp_class_f32_e64 vcc, v182, s64\n"
"v_mfma_f32_16x16x32_bf16 v[216:219], v[100:103], v[178:181], 0\n"
"s_waitcnt lgkmcnt(5)\n"
"v_cvt_pk_bf16_f32 v182, v182, v183\n"
"v_mad_u32_u24 v21, v16, s40, v18\n"
"v_mfma_f32_16x16x32_bf16 v[220:223], v[186:189], v[178:181], 0\n"
"s_waitcnt lgkmcnt(3)\n"
"ds_read2st64_b32 v[178:179], v20 offset0:0 offset1:1\n"
"v_cvt_pk_bf16_f32 v183, v184, v185\n"
"v_mfma_f32_16x16x32_bf16 v[212:215], v[104:107], v[190:193], v[212:215]\n"
"ds_read_b128 v[186:189], v17 offset:55296\n"
"v_cvt_pk_bf16_f32 v184, v200, v201\n"
"v_mfma_f32_16x16x32_bf16 v[216:219], v[108:111], v[190:193], v[216:219]\n"
"s_waitcnt lgkmcnt(3)\n"
"ds_read2st64_b32 v[180:181], v20 offset0:2 offset1:3\n"
"v_cvt_pk_bf16_f32 v185, v202, v203\n"
"v_mfma_f32_16x16x32_bf16 v[220:223], v[194:197], v[190:193], v[220:223]\n"
"s_waitcnt lgkmcnt(3)\n"
"ds_read2st64_b32 v[192:193], v14 offset0:200 offset1:201\n"
"v_mad_u32_u24 v16, v16, s40, v19\n"
"v_mfma_f32_16x16x32_bf16 v[212:215], v[0:3], v[208:211], v[212:215]\n"
"s_or_b64 s[62:63], s[62:63], vcc\n"
"ds_read2st64_b32 v[190:191], v14 offset0:198 offset1:199\n"
"v_cvt_pk_bf16_f32 v204, v204, v205\n"
"v_mfma_f32_16x16x32_bf16 v[216:219], v[112:115], v[208:211], v[216:219]\n"
"ds_read2st64_b32 v[200:201], v14 offset0:130 offset1:131\n"
"s_mov_b32 m0, s68\n"
"ds_read_b128 v[196:199], v17 offset:56320\n"
"ds_read2st64_b32 v[194:195], v20 offset0:4 offset1:5\n"
"s_waitcnt lgkmcnt(6)\n"
"v_mfma_f32_16x16x32_bf16 v[220:223], v[186:189], v[208:211], v[220:223]\n"
"s_waitcnt lgkmcnt(5)\n"
"ds_read2st64_b32 v[202:203], v14 offset0:154 offset1:155\n"
"global_store_dwordx4 v21, v[178:181], s[66:67] sc1\n"
"ds_read2st64_b32 v[186:187], v14 offset0:178 offset1:179\n"
"s_waitcnt lgkmcnt(5)\n"
"v_mfma_f32_16x16x32_bf16 v[212:215], v[116:119], v[190:193], v[212:215]\n"
"ds_read_b128 v[178:181], v17 offset:57344\n"
"v_cvt_pk_bf16_f32 v205, v206, v207\n"
"v_mfma_f32_16x16x32_bf16 v[216:219], v[120:123], v[190:193], v[216:219]\n"
"s_waitcnt lgkmcnt(4)\n"
"ds_read2st64_b32 v[188:189], v14 offset0:202 offset1:203\n"
"v_mfma_f32_16x16x32_bf16 v[220:223], v[196:199], v[190:193], v[220:223]\n"
"s_waitcnt lgkmcnt(4)\n"
"global_store_dwordx2 v16, v[194:195], s[66:67] offset:256 sc1\n"
"ds_read_b128 v[190:193], v17 offset:58368\n"
"s_waitcnt lgkmcnt(4)\n"
"v_mfma_f32_16x16x32_bf16 v[212:215], v[4:7], v[200:203], v[212:215]\n"
"ds_write_addtid_b32 v182 offset:51456\n"
"v_mfma_f32_16x16x32_bf16 v[216:219], v[124:127], v[200:203], v[216:219]\n"
"ds_write_addtid_b32 v184 offset:51968\n"
"s_waitcnt lgkmcnt(4)\n"
"v_mfma_f32_16x16x32_bf16 v[220:223], v[178:181], v[200:203], v[220:223]\n"
"ds_write_addtid_b32 v183 offset:51712\n"
"s_waitcnt lgkmcnt(4)\n"
"v_mfma_f32_16x16x32_bf16 v[212:215], v[128:131], v[186:189], v[212:215]\n"
"ds_write_addtid_b32 v204 offset:52480\n"
"ds_write_addtid_b32 v185 offset:52224\n"
"v_mfma_f32_16x16x32_bf16 v[216:219], v[132:135], v[186:189], v[216:219]\n"
"s_setprio 0\n"
"ds_write_addtid_b32 v205 offset:52736\n"
"s_waitcnt lgkmcnt(6)\n"
"v_mfma_f32_16x16x32_bf16 v[220:223], v[190:193], v[186:189], v[220:223]\n"
"s_nop 0\n"
"v_cmp_class_f32_e64 vcc, v212, s64\n"
"v_cvt_pk_bf16_f32 v212, v212, v213\n"
"v_cvt_pk_bf16_f32 v213, v214, v215\n"
"v_cvt_pk_bf16_f32 v215, v218, v219\n"
"v_cvt_pk_bf16_f32 v214, v216, v217\n"
"s_nop 0\n"
"s_or_b64 s[62:63], s[62:63], vcc\n"
"ds_write_addtid_b32 v214 offset:58368\n"
"ds_write_addtid_b32 v213 offset:58112\n"
"v_cvt_pk_bf16_f32 v220, v220, v221\n"
"v_cvt_pk_bf16_f32 v221, v222, v223\n"
"s_cmp_lg_u64 s[62:63], 0\n"
"s_cselect_b32 s41, 1, 0\n"
"v_mov_b32 v178, s65\n"
"v_mov_b32 v179, s41\n"
"ds_write_b32 v178, v179 offset:0\n"
"ds_write_addtid_b32 v221 offset:59136\n"
"ds_write_addtid_b32 v215 offset:58624\n"
"ds_write_addtid_b32 v220 offset:58880\n"
"ds_write_addtid_b32 v212 offset:57856\n"
"s_waitcnt lgkmcnt(0)\n"
"s_barrier\n"
"ds_read2st64_b32 v[180:181], v20 offset0:27 offset1:28\n"
"ds_read2st64_b32 v[178:179], v20 offset0:25 offset1:26\n"
"ds_read2st64_b32 v[182:183], v20 offset0:29 offset1:30\n"
"ds_read2st64_b32 v[184:185], v20 offset0:50 offset1:51\n"
"ds_read2st64_b32 v[254:255], v20 offset0:54 offset1:55\n"
"s_waitcnt lgkmcnt(3)\n"
"global_store_dwordx4 v177, v[178:181], s[66:67] sc1\n"
"s_waitcnt lgkmcnt(2)\n"
"global_store_dwordx2 v23, v[182:183], s[66:67] offset:256 sc1\n"
"ds_read2st64_b32 v[186:187], v20 offset0:52 offset1:53\n"
"s_waitcnt lgkmcnt(0)\n"
"global_store_dwordx4 v176, v[184:187], s[66:67] sc1\n"
"global_store_dwordx2 v22, v[254:255], s[66:67] offset:256 sc1\n"
    );
    PEND;
  }
  const u32x4 fl = *LP(const u32x4, FLAG);
  if (__builtin_expect(UNI(fl[0] | fl[1] | fl[2] | fl[3]) != 0, 0)) {
    asm volatile("s_waitcnt vmcnt(0) lgkmcnt(0)\n\ts_barrier" ::: "memory");
    __builtin_amdgcn_s_setprio(0);
    Regs R;
    int w2 = w;
    asm volatile("" : "+s"(w2), "+s"(G.t0), "+s"(G.sr), "+s"(G.sc));
    prologue(x, wqkv, wo, rpb, G, w2, R);
    BODY(
"s_add_u32 s50, s54, 28672\n"
"s_bitcmp1_b32 s45, 0\n"
"s_cselect_b32 s53, -1, 0\n"
"s_lshl_b32 s52, s53, 16\n"
"s_setprio 1\n"
"s_mov_b32 s60, 0x3e553b95\n"
"s_mov_b32 s61, 0x3fb8aa3b\n"
"v_add_u32 v27, 77824, v3\n"
"v_mov_b32 v24, 0\n"
"v_mov_b32 v25, 0\n"
"v_mov_b32 v178, 0xf1800000\n"
"v_mov_b32 v179, 0xf1800000\n"
"v_mov_b32 v180, 0xf1800000\n"
"v_mov_b32 v181, 0xf1800000\n"
"s_bfm_b64 exec, 18, 0\n"
"ds_write_b128 v9, v[178:181] offset:0\n"
"s_mov_b64 exec, -1\n"
"s_waitcnt vmcnt(4)\n"
"ds_read_u16 v13, v13 offset:0\n"
"ds_read_u16 v14, v14 offset:0\n"
"ds_read_u16 v15, v15 offset:0\n"
"ds_read_u16 v16, v16 offset:0\n"
"ds_read_u16 v17, v17 offset:0\n"
"ds_read_u16 v18, v18 offset:0\n"
"ds_read_u16 v12, v12 offset:0\n"
"ds_read_u16 v20, v20 offset:0\n"
"ds_read_u16 v21, v21 offset:0\n"
"ds_read_u16 v22, v22 offset:0\n"
"ds_read_u16 v23, v23 offset:0\n"
"s_waitcnt lgkmcnt(10)\n"
"v_lshlrev_b32 v13, 16, v13\n"
"s_waitcnt lgkmcnt(9)\n"
"v_lshlrev_b32 v14, 16, v14\n"
"v_mul_f32 v13, s61, v13\n"
"s_waitcnt lgkmcnt(8)\n"
"v_lshlrev_b32 v15, 16, v15\n"
"v_mul_f32 v14, s61, v14\n"
"s_waitcnt lgkmcnt(7)\n"
"v_lshlrev_b32 v16, 16, v16\n"
"v_mul_f32 v15, s61, v15\n"
"s_waitcnt lgkmcnt(6)\n"
"v_lshlrev_b32 v17, 16, v17\n"
"v_mul_f32 v16, s61, v16\n"
"s_waitcnt lgkmcnt(5)\n"
"v_lshlrev_b32 v18, 16, v18\n"
"v_mul_f32 v17, s61, v17\n"
"s_waitcnt lgkmcnt(4)\n"
"v_lshlrev_b32 v12, 16, v12\n"
"v_mul_f32 v18, s61, v18\n"
"s_waitcnt lgkmcnt(3)\n"
"v_lshlrev_b32 v20, 16, v20\n"
"v_mul_f32 v12, s61, v12\n"
"s_waitcnt lgkmcnt(2)\n"
"v_lshlrev_b32 v21, 16, v21\n"
"v_mul_f32 v20, s61, v20\n"
"s_waitcnt lgkmcnt(1)\n"
"v_lshlrev_b32 v22, 16, v22\n"
"v_mul_f32 v21, s61, v21\n"
"s_waitcnt lgkmcnt(0)\n"
"v_lshlrev_b32 v23, 16, v23\n"
"v_mul_f32 v22, s61, v22\n"
"v_mul_f32 v23, s61, v23\n"
"v_mov_b32 v178, v13\n"
"v_mov_b32 v179, v14\n"
"v_mov_b32 v180, v15\n"
"v_mov_b32 v181, v16\n"
"v_mov_b32 v182, v17\n"
"v_mov_b32 v183, v18\n"
"s_bfm_b64 exec, 40, 0\n"
"ds_write_b128 v8, v[180:183] offset:0\n"
"ds_write_b128 v8, v[14:17] offset:64\n"
"ds_write_b128 v8, v[178:181] offset:128\n"
"ds_write_b128 v8, v[12:15] offset:192\n"
"ds_write_b128 v8, v[20:23] offset:256\n"
"s_mov_b64 exec, -1\n"
"s_add_u32 s42, s46, 37248\n"
"s_addc_u32 s43, s47, 0\n"
"s_add_u32 m0, s50, 4294945792\n"
"s_nop 0\n"
"global_load_lds_dwordx4 v0, s[42:43]\n"
"s_add_u32 m0, s50, 4294946816\n"
"s_nop 0\n"
"global_load_lds_dwordx4 v1, s[42:43]\n"
"s_add_u32 m0, s50, 4294947840\n"
"s_nop 0\n"
"global_load_lds_dwordx4 v2, s[42:43]\n"
"s_add_u32 s42, s46, 74112\n"
"s_addc_u32 s43, s47, 0\n"
"s_add_u32 m0, s50, 49152\n"
"s_nop 0\n"
"global_load_lds_dwordx4 v0, s[42:43]\n"
"s_add_u32 m0, s50, 50176\n"
"s_nop 0\n"
"global_load_lds_dwordx4 v1, s[42:43]\n"
"s_add_u32 m0, s50, 51200\n"
"s_nop 0\n"
"global_load_lds_dwordx4 v2, s[42:43]\n"
"s_barrier\n"
"s_waitcnt vmcnt(6)\n"
"ds_read_b64_tr_b16 v[8:9], v3 offset:4096\n"
"ds_read_b64_tr_b16 v[10:11], v3 offset:4864\n"
"ds_read_b128 v[178:181], v4 offset:24576\n"
"ds_read_b64_tr_b16 v[12:13], v3 offset:4128\n"
"ds_read_b64_tr_b16 v[14:15], v3 offset:4896\n"
"ds_read_b64_tr_b16 v[16:17], v3 offset:4160\n"
"ds_read_b64_tr_b16 v[18:19], v3 offset:4928\n"
"ds_read_b128 v[182:185], v4 offset:30720\n"
"ds_read_b128 v[186:189], v4 offset:36864\n"
"s_waitcnt lgkmcnt(6)\n"
"v_mfma_f32_16x16x32_bf16 v[196:199], v[8:11], v[178:181], 0\n"
"s_waitcnt lgkmcnt(4)\n"
"v_mfma_f32_16x16x32_bf16 v[190:193], v[12:15], v[178:181], 0\n"
"s_waitcnt lgkmcnt(2)\n"
"v_mfma_f32_16x16x32_bf16 v[212:215], v[16:19], v[178:181], 0\n"
"s_waitcnt lgkmcnt(1)\n"
"ds_read_b128 v[216:219], v4 offset:43008\n"
"v_mfma_f32_16x16x32_bf16 v[202:205], v[8:11], v[182:185], 0\n"
"v_mfma_f32_16x16x32_bf16 v[220:223], v[12:15], v[182:185], 0\n"
"v_mfma_f32_16x16x32_bf16 v[236:239], v[16:19], v[182:185], 0\n"
"s_waitcnt lgkmcnt(1)\n"
"ds_read_b128 v[240:243], v4 offset:49152\n"
"v_mfma_f32_16x16x32_bf16 v[226:229], v[8:11], v[186:189], 0\n"
"v_mfma_f32_16x16x32_bf16 v[244:247], v[12:15], v[186:189], 0\n"
"v_mfma_f32_16x16x32_bf16 v[248:251], v[16:19], v[186:189], 0\n"
"s_waitcnt lgkmcnt(1)\n"
"ds_read_b128 v[252:255], v4 offset:55296\n"
"v_mfma_f32_16x16x32_bf16 v[232:235], v[8:11], v[216:219], 0\n"
"v_mfma_f32_16x16x32_bf16 v[182:185], v[12:15], v[216:219], 0\n"
"v_mfma_f32_16x16x32_bf16 v[84:87], v[16:19], v[216:219], 0\n"
"s_waitcnt vmcnt(3)\n"
"ds_read_b64_tr_b16 v[20:21], v3 offset:7168\n"
"s_waitcnt lgkmcnt(2)\n"
"v_mfma_f32_16x16x32_bf16 v[208:211], v[8:11], v[240:243], 0\n"
"ds_read_b64_tr_b16 v[22:23], v3 offset:7936\n"
"v_mfma_f32_16x16x32_bf16 v[186:189], v[12:15], v[240:243], 0\n"
"ds_read_b128 v[36:39], v5 offset:24576\n"
"v_mfma_f32_16x16x32_bf16 v[216:219], v[16:19], v[240:243], 0\n"
"s_waitcnt lgkmcnt(3)\n"
"ds_read_b64_tr_b16 v[28:29], v3 offset:7200\n"
"v_mfma_f32_16x16x32_bf16 v[178:181], v[8:11], v[252:255], 0\n"
"ds_read_b64_tr_b16 v[30:31], v3 offset:7968\n"
"v_mfma_f32_16x16x32_bf16 v[88:91], v[12:15], v[252:255], 0\n"
"ds_read_b64_tr_b16 v[32:33], v3 offset:7232\n"
"ds_read_b64_tr_b16 v[34:35], v3 offset:8000\n"
"v_mfma_f32_16x16x32_bf16 v[240:243], v[16:19], v[252:255], 0\n"
"s_waitcnt lgkmcnt(4)\n"
"ds_read_b128 v[40:43], v5 offset:30720\n"
"ds_read_b128 v[44:47], v5 offset:36864\n"
"v_mfma_f32_16x16x32_bf16 v[196:199], v[20:23], v[36:39], v[196:199]\n"
"s_waitcnt lgkmcnt(4)\n"
"v_mfma_f32_16x16x32_bf16 v[190:193], v[28:31], v[36:39], v[190:193]\n"
"s_waitcnt lgkmcnt(2)\n"
"v_mfma_f32_16x16x32_bf16 v[212:215], v[32:35], v[36:39], v[212:215]\n"
"s_waitcnt lgkmcnt(1)\n"
"ds_read_b128 v[48:51], v5 offset:43008\n"
"v_mfma_f32_16x16x32_bf16 v[202:205], v[20:23], v[40:43], v[202:205]\n"
"v_mfma_f32_16x16x32_bf16 v[220:223], v[28:31], v[40:43], v[220:223]\n"
"v_mfma_f32_16x16x32_bf16 v[236:239], v[32:35], v[40:43], v[236:239]\n"
"s_waitcnt lgkmcnt(1)\n"
"ds_read_b128 v[252:255], v5 offset:49152\n"
"v_mfma_f32_16x16x32_bf16 v[226:229], v[20:23], v[44:47], v[226:229]\n"
"v_mfma_f32_16x16x32_bf16 v[244:247], v[28:31], v[44:47], v[244:247]\n"
"v_mfma_f32_16x16x32_bf16 v[248:251], v[32:35], v[44:47], v[248:251]\n"
"s_waitcnt lgkmcnt(1)\n"
"ds_read_b128 v[52:55], v5 offset:55296\n"
"v_mfma_f32_16x16x32_bf16 v[232:235], v[20:23], v[48:51], v[232:235]\n"
"v_mfma_f32_16x16x32_bf16 v[182:185], v[28:31], v[48:51], v[182:185]\n"
"v_mfma_f32_16x16x32_bf16 v[84:87], v[32:35], v[48:51], v[84:87]\n"
"s_waitcnt vmcnt(0)\n"
"ds_read_b64_tr_b16 v[36:37], v27 offset:0\n"
"s_waitcnt lgkmcnt(2)\n"
"v_mfma_f32_16x16x32_bf16 v[208:211], v[20:23], v[252:255], v[208:211]\n"
"ds_read_b64_tr_b16 v[38:39], v27 offset:768\n"
"v_mfma_f32_16x16x32_bf16 v[186:189], v[28:31], v[252:255], v[186:189]\n"
"ds_read_b128 v[56:59], v4 offset:26624\n"
"v_mfma_f32_16x16x32_bf16 v[216:219], v[32:35], v[252:255], v[216:219]\n"
"s_waitcnt lgkmcnt(3)\n"
"ds_read_b64_tr_b16 v[40:41], v27 offset:32\n"
"v_mfma_f32_16x16x32_bf16 v[178:181], v[20:23], v[52:55], v[178:181]\n"
"ds_read_b64_tr_b16 v[42:43], v27 offset:800\n"
"v_mfma_f32_16x16x32_bf16 v[88:91], v[28:31], v[52:55], v[88:91]\n"
"ds_read_b64_tr_b16 v[44:45], v27 offset:64\n"
"ds_read_b64_tr_b16 v[46:47], v27 offset:832\n"
"v_mfma_f32_16x16x32_bf16 v[240:243], v[32:35], v[52:55], v[240:243]\n"
"s_waitcnt lgkmcnt(4)\n"
"ds_read_b128 v[252:255], v4 offset:32768\n"
"ds_read_b128 v[60:63], v4 offset:38912\n"
"v_mfma_f32_16x16x32_bf16 v[196:199], v[36:39], v[56:59], v[196:199]\n"
"s_waitcnt lgkmcnt(4)\n"
"v_mfma_f32_16x16x32_bf16 v[190:193], v[40:43], v[56:59], v[190:193]\n"
"s_waitcnt lgkmcnt(2)\n"
"v_mfma_f32_16x16x32_bf16 v[212:215], v[44:47], v[56:59], v[212:215]\n"
"s_waitcnt lgkmcnt(1)\n"
"ds_read_b128 v[64:67], v4 offset:45056\n"
"v_mfma_f32_16x16x32_bf16 v[202:205], v[36:39], v[252:255], v[202:205]\n"
"v_mfma_f32_16x16x32_bf16 v[220:223], v[40:43], v[252:255], v[220:223]\n"
"v_mfma_f32_16x16x32_bf16 v[236:239], v[44:47], v[252:255], v[236:239]\n"
"s_waitcnt lgkmcnt(1)\n"
"ds_read_b128 v[68:71], v4 offset:51200\n"
"v_mfma_f32_16x16x32_bf16 v[226:229], v[36:39], v[60:63], v[226:229]\n"
"v_mfma_f32_16x16x32_bf16 v[244:247], v[40:43], v[60:63], v[244:247]\n"
"v_mfma_f32_16x16x32_bf16 v[248:251], v[44:47], v[60:63], v[248:251]\n"
"s_waitcnt lgkmcnt(1)\n"
"ds_read_b128 v[72:75], v4 offset:57344\n"
"v_mfma_f32_16x16x32_bf16 v[232:235], v[36:39], v[64:67], v[232:235]\n"
"v_mfma_f32_16x16x32_bf16 v[182:185], v[40:43], v[64:67], v[182:185]\n"
"v_mfma_f32_16x16x32_bf16 v[84:87], v[44:47], v[64:67], v[84:87]\n"
"s_barrier\n"
"ds_read_b64_tr_b16 v[48:49], v27 offset:3072\n"
"s_waitcnt lgkmcnt(2)\n"
"v_mfma_f32_16x16x32_bf16 v[208:211], v[36:39], v[68:71], v[208:211]\n"
"ds_read_b64_tr_b16 v[50:51], v27 offset:3840\n"
"v_mfma_f32_16x16x32_bf16 v[186:189], v[40:43], v[68:71], v[186:189]\n"
"ds_read_b128 v[252:255], v5 offset:26624\n"
"v_mfma_f32_16x16x32_bf16 v[216:219], v[44:47], v[68:71], v[216:219]\n"
"s_waitcnt lgkmcnt(3)\n"
"ds_read_b64_tr_b16 v[52:53], v27 offset:3104\n"
"v_mfma_f32_16x16x32_bf16 v[178:181], v[36:39], v[72:75], v[178:181]\n"
"ds_read_b64_tr_b16 v[54:55], v27 offset:3872\n"
"v_mfma_f32_16x16x32_bf16 v[88:91], v[40:43], v[72:75], v[88:91]\n"
"ds_read_b64_tr_b16 v[56:57], v27 offset:3136\n"
"ds_read_b64_tr_b16 v[58:59], v27 offset:3904\n"
"v_mfma_f32_16x16x32_bf16 v[240:243], v[44:47], v[72:75], v[240:243]\n"
"s_waitcnt lgkmcnt(4)\n"
"ds_read_b128 v[60:63], v5 offset:32768\n"
"ds_read_b128 v[64:67], v5 offset:38912\n"
"v_mfma_f32_16x16x32_bf16 v[196:199], v[48:51], v[252:255], v[196:199]\n"
"s_waitcnt lgkmcnt(4)\n"
"v_mfma_f32_16x16x32_bf16 v[190:193], v[52:55], v[252:255], v[190:193]\n"
"s_waitcnt lgkmcnt(2)\n"
"v_mfma_f32_16x16x32_bf16 v[212:215], v[56:59], v[252:255], v[212:215]\n"
"s_waitcnt lgkmcnt(1)\n"
"ds_read_b128 v[68:71], v5 offset:45056\n"
"v_mfma_f32_16x16x32_bf16 v[202:205], v[48:51], v[60:63], v[202:205]\n"
"v_mfma_f32_16x16x32_bf16 v[220:223], v[52:55], v[60:63], v[220:223]\n"
"v_mfma_f32_16x16x32_bf16 v[236:239], v[56:59], v[60:63], v[236:239]\n"
"s_waitcnt lgkmcnt(1)\n"
"ds_read_b128 v[72:75], v5 offset:51200\n"
"v_mfma_f32_16x16x32_bf16 v[226:229], v[48:51], v[64:67], v[226:229]\n"
"v_mfma_f32_16x16x32_bf16 v[244:247], v[52:55], v[64:67], v[244:247]\n"
"v_mfma_f32_16x16x32_bf16 v[248:251], v[56:59], v[64:67], v[248:251]\n"
"s_waitcnt lgkmcnt(1)\n"
"ds_read_b128 v[252:255], v5 offset:57344\n"
"v_mfma_f32_16x16x32_bf16 v[232:235], v[48:51], v[68:71], v[232:235]\n"
"v_mfma_f32_16x16x32_bf16 v[182:185], v[52:55], v[68:71], v[182:185]\n"
"v_mfma_f32_16x16x32_bf16 v[84:87], v[56:59], v[68:71], v[84:87]\n"
"s_barrier\n"
"ds_read_b64_tr_b16 v[60:61], v27 offset:24576\n"
"s_waitcnt lgkmcnt(2)\n"
"v_mfma_f32_16x16x32_bf16 v[208:211], v[48:51], v[72:75], v[208:211]\n"
"ds_read_b64_tr_b16 v[62:63], v27 offset:25344\n"
"v_mfma_f32_16x16x32_bf16 v[186:189], v[52:55], v[72:75], v[186:189]\n"
"ds_read_b128 v[92:95], v4 offset:28672\n"
"v_mfma_f32_16x16x32_bf16 v[216:219], v[56:59], v[72:75], v[216:219]\n"
"s_waitcnt lgkmcnt(3)\n"
"ds_read_b64_tr_b16 v[64:65], v27 offset:24608\n"
"v_mfma_f32_16x16x32_bf16 v[178:181], v[48:51], v[252:255], v[178:181]\n"
"ds_read_b64_tr_b16 v[66:67], v27 offset:25376\n"
"v_mfma_f32_16x16x32_bf16 v[88:91], v[52:55], v[252:255], v[88:91]\n"
"ds_read_b64_tr_b16 v[68:69], v27 offset:24640\n"
"ds_read_b64_tr_b16 v[70:71], v27 offset:25408\n"
"v_mfma_f32_16x16x32_bf16 v[240:243], v[56:59], v[252:255], v[240:243]\n"
"s_waitcnt lgkmcnt(4)\n"
"ds_read_b128 v[96:99], v4 offset:34816\n"
"ds_read_b128 v[100:103], v4 offset:40960\n"
"v_mfma_f32_16x16x32_bf16 v[196:199], v[60:63], v[92:95], v[196:199]\n"
"s_waitcnt lgkmcnt(4)\n"
"ds_read_b64_tr_b16 v[72:73], v27 offset:27648\n"
"ds_read_b64_tr_b16 v[74:75], v27 offset:28416\n"
"ds_read_b64_tr_b16 v[76:77], v27 offset:27680\n"
"v_mfma_f32_16x16x32_bf16 v[190:193], v[64:67], v[92:95], v[190:193]\n"
"s_waitcnt lgkmcnt(5)\n"
"ds_read_b64_tr_b16 v[78:79], v27 offset:28448\n"
"ds_read_b64_tr_b16 v[80:81], v27 offset:27712\n"
"v_mfma_f32_16x16x32_bf16 v[212:215], v[68:71], v[92:95], v[212:215]\n"
"s_waitcnt lgkmcnt(6)\n"
"ds_read_b64_tr_b16 v[82:83], v27 offset:28480\n"
"v_mfma_f32_16x16x32_bf16 v[202:205], v[60:63], v[96:99], v[202:205]\n"
"ds_read_b128 v[252:255], v4 offset:47104\n"
"v_mfma_f32_16x16x32_bf16 v[220:223], v[64:67], v[96:99], v[220:223]\n"
"v_mfma_f32_16x16x32_bf16 v[236:239], v[68:71], v[96:99], v[236:239]\n"
"s_waitcnt lgkmcnt(7)\n"
"ds_read_b128 v[104:107], v5 offset:28672\n"
"v_mfma_f32_16x16x32_bf16 v[226:229], v[60:63], v[100:103], v[226:229]\n"
"ds_read_b128 v[92:95], v5 offset:34816\n"
"v_mfma_f32_16x16x32_bf16 v[244:247], v[64:67], v[100:103], v[244:247]\n"
"s_waitcnt lgkmcnt(3)\n"
"ds_read_b128 v[108:111], v5 offset:40960\n"
"s_barrier\n"
"ds_read_b128 v[96:99], v5 offset:47104\n"
"v_mfma_f32_16x16x32_bf16 v[248:251], v[68:71], v[100:103], v[248:251]\n"
"s_waitcnt lgkmcnt(4)\n"
"ds_read_b128 v[112:115], v4 offset:53248\n"
"ds_read_b128 v[116:119], v5 offset:53248\n"
"v_mfma_f32_16x16x32_bf16 v[232:235], v[60:63], v[252:255], v[232:235]\n"
"ds_read_b128 v[120:123], v4 offset:59392\n"
"ds_read_b128 v[124:127], v5 offset:59392\n"
"v_mfma_f32_16x16x32_bf16 v[182:185], v[64:67], v[252:255], v[182:185]\n"
"v_mfma_f32_16x16x32_bf16 v[84:87], v[68:71], v[252:255], v[84:87]\n"
"s_waitcnt lgkmcnt(3)\n"
"v_mfma_f32_16x16x32_bf16 v[208:211], v[60:63], v[112:115], v[208:211]\n"
"v_mfma_f32_16x16x32_bf16 v[186:189], v[64:67], v[112:115], v[186:189]\n"
"v_mfma_f32_16x16x32_bf16 v[216:219], v[68:71], v[112:115], v[216:219]\n"
"s_waitcnt lgkmcnt(1)\n"
"v_mfma_f32_16x16x32_bf16 v[178:181], v[60:63], v[120:123], v[178:181]\n"
"v_mfma_f32_16x16x32_bf16 v[88:91], v[64:67], v[120:123], v[88:91]\n"
"v_mfma_f32_16x16x32_bf16 v[240:243], v[68:71], v[120:123], v[240:243]\n"
"v_mfma_f32_16x16x32_bf16 v[196:199], v[72:75], v[104:107], v[196:199]\n"
"v_mfma_f32_16x16x32_bf16 v[190:193], v[76:79], v[104:107], v[190:193]\n"
"v_mfma_f32_16x16x32_bf16 v[212:215], v[80:83], v[104:107], v[212:215]\n"
"v_mfma_f32_16x16x32_bf16 v[202:205], v[72:75], v[92:95], v[202:205]\n"
"v_mfma_f32_16x16x32_bf16 v[220:223], v[76:79], v[92:95], v[220:223]\n"
"v_mfma_f32_16x16x32_bf16 v[236:239], v[80:83], v[92:95], v[236:239]\n"
"v_mfma_f32_16x16x32_bf16 v[226:229], v[72:75], v[108:111], v[226:229]\n"
"v_mfma_f32_16x16x32_bf16 v[244:247], v[76:79], v[108:111], v[244:247]\n"
"v_mfma_f32_16x16x32_bf16 v[248:251], v[80:83], v[108:111], v[248:251]\n"
"v_cvt_pk_bf16_f32 v196, v196, v197\n"
"v_cvt_pk_bf16_f32 v197, v198, v199\n"
"v_cvt_pk_bf16_f32 v198, v190, v191\n"
"v_cvt_pk_bf16_f32 v199, v192, v193\n"
"v_cvt_pk_bf16_f32 v200, v212, v213\n"
"v_cvt_pk_bf16_f32 v201, v214, v215\n"
"v_cvt_pk_bf16_f32 v202, v202, v203\n"
"v_cvt_pk_bf16_f32 v203, v204, v205\n"
"v_cvt_pk_bf16_f32 v204, v220, v221\n"
"v_cvt_pk_bf16_f32 v205, v222, v223\n"
"v_cvt_pk_bf16_f32 v206, v236, v237\n"
"v_cvt_pk_bf16_f32 v207, v238, v239\n"
"v_cvt_pk_bf16_f32 v226, v226, v227\n"
"v_cvt_pk_bf16_f32 v227, v228, v229\n"
"v_mfma_f32_16x16x32_bf16 v[232:235], v[72:75], v[96:99], v[232:235]\n"
"v_cvt_pk_bf16_f32 v228, v244, v245\n"
"v_cvt_pk_bf16_f32 v229, v246, v247\n"
"v_mfma_f32_16x16x32_bf16 v[182:185], v[76:79], v[96:99], v[182:185]\n"
"v_cvt_pk_bf16_f32 v230, v248, v249\n"
"v_cvt_pk_bf16_f32 v231, v250, v251\n"
"v_mfma_f32_16x16x32_bf16 v[84:87], v[80:83], v[96:99], v[84:87]\n"
"v_mfma_f32_16x16x32_bf16 v[208:211], v[72:75], v[116:119], v[208:211]\n"
"v_mfma_f32_16x16x32_bf16 v[186:189], v[76:79], v[116:119], v[186:189]\n"
"v_cvt_pk_bf16_f32 v232, v232, v233\n"
"v_cvt_pk_bf16_f32 v233, v234, v235\n"
"v_mfma_f32_16x16x32_bf16 v[216:219], v[80:83], v[116:119], v[216:219]\n"
"s_waitcnt lgkmcnt(0)\n"
"v_cvt_pk_bf16_f32 v234, v182, v183\n"
"v_cvt_pk_bf16_f32 v235, v184, v185\n"
"v_mfma_f32_16x16x32_bf16 v[178:181], v[72:75], v[124:127], v[178:181]\n"
"v_cvt_pk_bf16_f32 v236, v84, v85\n"
"v_cvt_pk_bf16_f32 v237, v86, v87\n"
"v_cvt_pk_bf16_f32 v208, v208, v209\n"
"v_cvt_pk_bf16_f32 v209, v210, v211\n"
"v_cvt_pk_bf16_f32 v210, v186, v187\n"
"v_cvt_pk_bf16_f32 v211, v188, v189\n"
"v_mfma_f32_16x16x32_bf16 v[88:91], v[76:79], v[124:127], v[88:91]\n"
"v_cvt_pk_bf16_f32 v212, v216, v217\n"
"v_cvt_pk_bf16_f32 v213, v218, v219\n"
"v_mfma_f32_16x16x32_bf16 v[240:243], v[80:83], v[124:127], v[240:243]\n"
"s_barrier\n"
"s_add_u32 s42, s46, 0\n"
"s_addc_u32 s43, s47, 0\n"
"s_add_u32 m0, s50, 24576\n"
"s_nop 0\n"
"global_load_lds_dwordx4 v0, s[42:43]\n"
"s_add_u32 m0, s50, 25600\n"
"s_nop 0\n"
"global_load_lds_dwordx4 v1, s[42:43]\n"
"s_add_u32 m0, s50, 26624\n"
"s_nop 0\n"
"global_load_lds_dwordx4 v2, s[42:43]\n"
"s_barrier\n"
"s_add_u32 s42, s46, 36864\n"
"s_addc_u32 s43, s47, 0\n"
"s_add_u32 m0, s50, 27648\n"
"s_nop 0\n"
"global_load_lds_dwordx4 v0, s[42:43]\n"
"s_add_u32 m0, s50, 28672\n"
"s_nop 0\n"
"global_load_lds_dwordx4 v1, s[42:43]\n"
"s_add_u32 m0, s50, 29696\n"
"s_nop 0\n"
"global_load_lds_dwordx4 v2, s[42:43]\n"
"s_add_u32 s42, s46, 73728\n"
"s_addc_u32 s43, s47, 0\n"
"s_add_u32 m0, s50, 4294942720\n"
"s_nop 0\n"
"global_load_lds_dwordx4 v0, s[42:43]\n"
"s_add_u32 m0, s50, 4294943744\n"
"s_nop 0\n"
"global_load_lds_dwordx4 v1, s[42:43]\n"
"s_add_u32 m0, s50, 4294944768\n"
"s_nop 0\n"
"global_load_lds_dwordx4 v2, s[42:43]\n"
"s_add_u32 s42, s46, 110592\n"
"s_addc_u32 s43, s47, 0\n"
"s_add_u32 m0, s50, 4294945792\n"
"s_nop 0\n"
"global_load_lds_dwordx4 v0, s[42:43]\n"
"s_add_u32 m0, s50, 4294946816\n"
"s_nop 0\n"
"global_load_lds_dwordx4 v1, s[42:43]\n"
"s_add_u32 m0, s50, 4294947840\n"
"s_nop 0\n"
"global_load_lds_dwordx4 v2, s[42:43]\n"
"s_add_u32 s42, s46, 147456\n"
"s_addc_u32 s43, s47, 0\n"
"s_add_u32 m0, s50, 49152\n"
"s_nop 0\n"
"global_load_lds_dwordx4 v0, s[42:43]\n"
"s_add_u32 m0, s50, 50176\n"
"s_nop 0\n"
"global_load_lds_dwordx4 v1, s[42:43]\n"
"s_add_u32 m0, s50, 51200\n"
"s_nop 0\n"
"global_load_lds_dwordx4 v2, s[42:43]\n"
"s_add_u32 s42, s46, 184320\n"
"s_addc_u32 s43, s47, 0\n"
"s_add_u32 m0, s50, 52224\n"
"s_nop 0\n"
"global_load_lds_dwordx4 v0, s[42:43]\n"
"s_add_u32 m0, s50, 53248\n"
"s_nop 0\n"
"global_load_lds_dwordx4 v1, s[42:43]\n"
"s_add_u32 m0, s50, 54272\n"
"s_nop 0\n"
"global_load_lds_dwordx4 v2, s[42:43]\n"
"s_barrier\n"
"ds_read_b128 v[184:187], v4 offset:6144\n"
"s_waitcnt vmcnt(15)\n"
"ds_read_b128 v[188:191], v5 offset:6144\n"
"s_waitcnt vmcnt(12)\n"
"v_cvt_pk_bf16_f32 v178, v178, v179\n"
"v_cvt_pk_bf16_f32 v179, v180, v181\n"
"v_cvt_pk_bf16_f32 v180, v88, v89\n"
"v_cvt_pk_bf16_f32 v181, v90, v91\n"
"v_cvt_pk_bf16_f32 v182, v240, v241\n"
"v_cvt_pk_bf16_f32 v183, v242, v243\n"
"ds_read_b64_tr_b16 v[84:85], v3 offset:53248\n"
"s_sub_u32 m0, s50, 28672\n"
"ds_read_b64_tr_b16 v[86:87], v3 offset:54016\n"
"ds_read_b64_tr_b16 v[88:89], v3 offset:53280\n"
"ds_read_b64_tr_b16 v[90:91], v3 offset:54048\n"
"s_waitcnt lgkmcnt(5)\n"
"v_mfma_f32_16x16x32_bf16 v[238:241], v[8:11], v[184:187], 0\n"
"ds_read_b64_tr_b16 v[92:93], v3 offset:53312\n"
"v_mfma_f32_16x16x32_bf16 v[192:195], v[12:15], v[184:187], 0\n"
"ds_read_b128 v[214:217], v4 offset:8192\n"
"v_mfma_f32_16x16x32_bf16 v[218:221], v[16:19], v[184:187], 0\n"
"s_waitcnt lgkmcnt(6)\n"
"ds_read_b64_tr_b16 v[94:95], v3 offset:54080\n"
"v_mfma_f32_16x16x32_bf16 v[238:241], v[20:23], v[188:191], v[238:241]\n"
"s_waitcnt vmcnt(9)\n"
"ds_read_b64_tr_b16 v[96:97], v3 offset:56320\n"
"v_mfma_f32_16x16x32_bf16 v[192:195], v[28:31], v[188:191], v[192:195]\n"
"ds_read_b64_tr_b16 v[98:99], v3 offset:57088\n"
"ds_read_b64_tr_b16 v[100:101], v3 offset:56352\n"
"v_mfma_f32_16x16x32_bf16 v[218:221], v[32:35], v[188:191], v[218:221]\n"
"s_waitcnt lgkmcnt(4)\n"
"ds_read_b64_tr_b16 v[102:103], v3 offset:57120\n"
"ds_read_b64_tr_b16 v[222:223], v3 offset:56384\n"
"v_mfma_f32_16x16x32_bf16 v[238:241], v[36:39], v[214:217], v[238:241]\n"
"ds_read_b64_tr_b16 v[224:225], v3 offset:57152\n"
"ds_read_b128 v[184:187], v5 offset:8192\n"
"v_mfma_f32_16x16x32_bf16 v[192:195], v[40:43], v[214:217], v[192:195]\n"
"s_waitcnt vmcnt(6)\n"
"ds_read_b64_tr_b16 v[104:105], v3 offset:4096\n"
"v_mfma_f32_16x16x32_bf16 v[218:221], v[44:47], v[214:217], v[218:221]\n"
"ds_read_b64_tr_b16 v[106:107], v3 offset:4864\n"
"ds_read_b64_tr_b16 v[108:109], v3 offset:4128\n"
"ds_read_b64_tr_b16 v[110:111], v3 offset:4896\n"
"ds_read_b64_tr_b16 v[244:245], v3 offset:4160\n"
"ds_read_b64_tr_b16 v[246:247], v3 offset:4928\n"
"ds_read_b128 v[188:191], v4 offset:10240\n"
"ds_read_b64_tr_b16 v[112:113], v3 offset:7168\n"
"s_waitcnt lgkmcnt(8)\n"
"v_mfma_f32_16x16x32_bf16 v[238:241], v[48:51], v[184:187], v[238:241]\n"
"ds_read_b64_tr_b16 v[114:115], v3 offset:7936\n"
"v_mfma_f32_16x16x32_bf16 v[192:195], v[52:55], v[184:187], v[192:195]\n"
"ds_read_b128 v[214:217], v5 offset:10240\n"
"v_mfma_f32_16x16x32_bf16 v[218:221], v[56:59], v[184:187], v[218:221]\n"
"s_waitcnt vmcnt(3)\n"
"ds_read_b64_tr_b16 v[116:117], v3 offset:7200\n"
"ds_read_b64_tr_b16 v[118:119], v3 offset:7968\n"
"ds_read_b64_tr_b16 v[248:249], v3 offset:7232\n"
"ds_read_b64_tr_b16 v[250:251], v3 offset:8000\n"
"s_waitcnt lgkmcnt(7)\n"
"v_mfma_f32_16x16x32_bf16 v[238:241], v[60:63], v[188:191], v[238:241]\n"
"ds_read_b64_tr_b16 v[0:1], v27 offset:0\n"
"v_mfma_f32_16x16x32_bf16 v[192:195], v[64:67], v[188:191], v[192:195]\n"
"ds_read_b128 v[184:187], v4 offset:0\n"
"v_mfma_f32_16x16x32_bf16 v[218:221], v[68:71], v[188:191], v[218:221]\n"
"s_waitcnt lgkmcnt(6)\n"
"ds_read_b64_tr_b16 v[2:3], v27 offset:768\n"
"v_mfma_f32_16x16x32_bf16 v[238:241], v[72:75], v[214:217], v[238:241]\n"
"s_waitcnt vmcnt(0)\n"
"ds_read_b64_tr_b16 v[120:121], v27 offset:32\n"
"v_mfma_f32_16x16x32_bf16 v[192:195], v[76:79], v[214:217], v[192:195]\n"
"ds_read_b64_tr_b16 v[122:123], v27 offset:800\n"
"ds_read_b64_tr_b16 v[252:253], v27 offset:64\n"
"v_mfma_f32_16x16x32_bf16 v[218:221], v[80:83], v[214:217], v[218:221]\n"
"ds_read_b64_tr_b16 v[254:255], v27 offset:832\n"
"ds_read_b128 v[188:191], v5 offset:0\n"
"v_cvt_pk_bf16_f32 v238, v238, v239\n"
"v_cvt_pk_bf16_f32 v239, v240, v241\n"
"s_waitcnt lgkmcnt(6)\n"
"v_mfma_f32_16x16x32_bf16 v[132:135], v[84:87], v[184:187], 0\n"
"ds_read_b64_tr_b16 v[124:125], v27 offset:3072\n"
"v_cvt_pk_bf16_f32 v240, v192, v193\n"
"v_cvt_pk_bf16_f32 v241, v194, v195\n"
"v_mfma_f32_16x16x32_bf16 v[214:217], v[88:91], v[184:187], 0\n"
"ds_read_b64_tr_b16 v[126:127], v27 offset:3840\n"
"v_cvt_pk_bf16_f32 v242, v218, v219\n"
"v_cvt_pk_bf16_f32 v243, v220, v221\n"
"v_mfma_f32_16x16x32_bf16 v[136:139], v[92:95], v[184:187], 0\n"
"s_waitcnt lgkmcnt(2)\n"
"ds_read_b64_tr_b16 v[128:129], v27 offset:3104\n"
"ds_read_b64_tr_b16 v[130:131], v27 offset:3872\n"
"ds_read_b64_tr_b16 v[140:141], v27 offset:3136\n"
"v_mfma_f32_16x16x32_bf16 v[132:135], v[96:99], v[188:191], v[132:135]\n"
"ds_read_b64_tr_b16 v[142:143], v27 offset:3904\n"
"ds_read_b128 v[192:195], v4 offset:2048\n"
"v_mfma_f32_16x16x32_bf16 v[214:217], v[100:103], v[188:191], v[214:217]\n"
"ds_read_b128 v[184:187], v5 offset:2048\n"
"v_mfma_f32_16x16x32_bf16 v[136:139], v[222:225], v[188:191], v[136:139]\n"
"s_waitcnt lgkmcnt(2)\n"
"s_barrier\n"
"s_waitcnt lgkmcnt(1)\n"
"v_mfma_f32_16x16x32_bf16 v[132:135], v[104:107], v[192:195], v[132:135]\n"
"v_mfma_f32_16x16x32_bf16 v[214:217], v[108:111], v[192:195], v[214:217]\n"
"ds_read_b128 v[218:221], v4 offset:4096\n"
"v_mfma_f32_16x16x32_bf16 v[136:139], v[244:247], v[192:195], v[136:139]\n"
"s_waitcnt lgkmcnt(1)\n"
"v_mfma_f32_16x16x32_bf16 v[132:135], v[112:115], v[184:187], v[132:135]\n"
"v_mfma_f32_16x16x32_bf16 v[214:217], v[116:119], v[184:187], v[214:217]\n"
"ds_read_b128 v[188:191], v5 offset:4096\n"
"v_mfma_f32_16x16x32_bf16 v[136:139], v[248:251], v[184:187], v[136:139]\n"
"s_waitcnt lgkmcnt(1)\n"
"v_mfma_f32_16x16x32_bf16 v[132:135], v[0:3], v[218:221], v[132:135]\n"
"v_mfma_f32_16x16x32_bf16 v[214:217], v[120:123], v[218:221], v[214:217]\n"
"ds_read_b128 v[192:195], v4 offset:12288\n"
"v_mfma_f32_16x16x32_bf16 v[136:139], v[252:255], v[218:221], v[136:139]\n"
"s_waitcnt lgkmcnt(1)\n"
"v_mfma_f32_16x16x32_bf16 v[132:135], v[124:127], v[188:191], v[132:135]\n"
"v_mfma_f32_16x16x32_bf16 v[214:217], v[128:131], v[188:191], v[214:217]\n"
"ds_read_b128 v[144:147], v5 offset:12288\n"
"v_mfma_f32_16x16x32_bf16 v[136:139], v[140:143], v[188:191], v[136:139]\n"
"s_waitcnt lgkmcnt(1)\n"
"v_mfma_f32_16x16x32_bf16 v[184:187], v[8:11], v[192:195], 0\n"
"v_mfma_f32_16x16x32_bf16 v[148:151], v[12:15], v[192:195], 0\n"
"ds_read_b128 v[218:221], v4 offset:14336\n"
"v_mfma_f32_16x16x32_bf16 v[152:155], v[16:19], v[192:195], 0\n"
"s_waitcnt lgkmcnt(1)\n"
"v_mfma_f32_16x16x32_bf16 v[184:187], v[20:23], v[144:147], v[184:187]\n"
"v_mul_f32 v132, s60, v132\n"
"v_mul_f32 v133, s60, v133\n"
"v_mfma_f32_16x16x32_bf16 v[148:151], v[28:31], v[144:147], v[148:151]\n"
"ds_read_b128 v[188:191], v5 offset:14336\n"
"v_cvt_pk_bf16_f32 v132, v132, v133\n"
"v_mfma_f32_16x16x32_bf16 v[152:155], v[32:35], v[144:147], v[152:155]\n"
"v_mul_f32 v134, s60, v134\n"
"v_mul_f32 v135, s60, v135\n"
"v_cvt_pk_bf16_f32 v133, v134, v135\n"
"v_mul_f32 v214, s60, v214\n"
"s_waitcnt lgkmcnt(1)\n"
"v_mfma_f32_16x16x32_bf16 v[184:187], v[36:39], v[218:221], v[184:187]\n"
"v_mul_f32 v215, s60, v215\n"
"v_mul_f32 v216, s60, v216\n"
"v_mfma_f32_16x16x32_bf16 v[148:151], v[40:43], v[218:221], v[148:151]\n"
"ds_read_b128 v[144:147], v4 offset:16384\n"
"v_mul_f32 v217, s60, v217\n"
"v_mfma_f32_16x16x32_bf16 v[152:155], v[44:47], v[218:221], v[152:155]\n"
"s_waitcnt lgkmcnt(1)\n"
"v_cvt_pk_bf16_f32 v134, v214, v215\n"
"v_cvt_pk_bf16_f32 v135, v216, v217\n"
"v_mfma_f32_16x16x32_bf16 v[184:187], v[48:51], v[188:191], v[184:187]\n"
"ds_write_b128 v6, v[222:225] offset:5120\n"
"v_mfma_f32_16x16x32_bf16 v[148:151], v[52:55], v[188:191], v[148:151]\n"
"ds_read_b128 v[156:159], v5 offset:16384\n"
"v_mul_f32 v136, s60, v136\n"
"v_mfma_f32_16x16x32_bf16 v[152:155], v[56:59], v[188:191], v[152:155]\n"
"s_waitcnt lgkmcnt(2)\n"
"v_mul_f32 v137, s60, v137\n"
"v_mul_f32 v138, s60, v138\n"
"v_mfma_f32_16x16x32_bf16 v[184:187], v[60:63], v[144:147], v[184:187]\n"
"v_mul_f32 v139, s60, v139\n"
"v_cvt_pk_bf16_f32 v26, v136, v137\n"
"v_mfma_f32_16x16x32_bf16 v[148:151], v[64:67], v[144:147], v[148:151]\n"
"ds_read_b128 v[214:217], v4 offset:18432\n"
"v_cvt_pk_bf16_f32 v27, v138, v139\n"
"v_mfma_f32_16x16x32_bf16 v[152:155], v[68:71], v[144:147], v[152:155]\n"
"s_waitcnt lgkmcnt(1)\n"
"v_mfma_f32_16x16x32_bf16 v[184:187], v[72:75], v[156:159], v[184:187]\n"
"v_mfma_f32_16x16x32_bf16 v[148:151], v[76:79], v[156:159], v[148:151]\n"
"ds_read_b128 v[218:221], v5 offset:18432\n"
"v_mfma_f32_16x16x32_bf16 v[152:155], v[80:83], v[156:159], v[152:155]\n"
"s_waitcnt lgkmcnt(1)\n"
"v_mfma_f32_16x16x32_bf16 v[190:193], v[8:11], v[214:217], 0\n"
"v_mfma_f32_16x16x32_bf16 v[136:139], v[12:15], v[214:217], 0\n"
"ds_read_b128 v[144:147], v4 offset:20480\n"
"v_mfma_f32_16x16x32_bf16 v[160:163], v[16:19], v[214:217], 0\n"
"s_waitcnt lgkmcnt(1)\n"
"v_mfma_f32_16x16x32_bf16 v[190:193], v[20:23], v[218:221], v[190:193]\n"
"ds_write_b128 v6, v[244:247] offset:6144\n"
"v_cvt_pk_bf16_f32 v184, v184, v185\n"
"v_cvt_pk_bf16_f32 v185, v186, v187\n"
"v_cvt_pk_bf16_f32 v186, v148, v149\n"
"v_cvt_pk_bf16_f32 v187, v150, v151\n"
"v_cvt_pk_bf16_f32 v188, v152, v153\n"
"v_cvt_pk_bf16_f32 v189, v154, v155\n"
"v_mfma_f32_16x16x32_bf16 v[136:139], v[28:31], v[218:221], v[136:139]\n"
"ds_read_b128 v[214:217], v5 offset:20480\n"
"v_bfe_u32 v195, v6, 2, 6\n"
"v_mfma_f32_16x16x32_bf16 v[160:163], v[32:35], v[218:221], v[160:163]\n"
"s_waitcnt lgkmcnt(2)\n"
"ds_read_b32 v195, v195 offset:1216\n"
"v_mfma_f32_16x16x32_bf16 v[190:193], v[36:39], v[144:147], v[190:193]\n"
"s_add_u32 s68, s50, 0\n"
"v_mfma_f32_16x16x32_bf16 v[136:139], v[40:43], v[144:147], v[136:139]\n"
"ds_read_b128 v[148:151], v4 offset:22528\n"
"v_mfma_f32_16x16x32_bf16 v[160:163], v[44:47], v[144:147], v[160:163]\n"
"s_waitcnt lgkmcnt(2)\n"
"v_mfma_f32_16x16x32_bf16 v[190:193], v[48:51], v[214:217], v[190:193]\n"
"v_mfma_f32_16x16x32_bf16 v[136:139], v[52:55], v[214:217], v[136:139]\n"
"s_add_u32 s69, s50, 1040\n"
"ds_read_b128 v[218:221], v5 offset:22528\n"
"s_waitcnt lgkmcnt(2)\n"
"v_add_u32_sdwa v177, v195, v7 dst_sel:DWORD dst_unused:UNUSED_PAD src0_sel:WORD_0 src1_sel:DWORD\n"
"v_mfma_f32_16x16x32_bf16 v[160:163], v[56:59], v[214:217], v[160:163]\n"
"s_waitcnt lgkmcnt(1)\n"
"v_max_u32 v176, s68, v177\n"
"v_mfma_f32_16x16x32_bf16 v[190:193], v[60:63], v[148:151], v[190:193]\n"
"v_mfma_f32_16x16x32_bf16 v[136:139], v[64:67], v[148:151], v[136:139]\n"
"ds_read_b128 v[144:147], v176 offset:16\n"
"v_min_u32 v175, s69, v177\n"
"v_mfma_f32_16x16x32_bf16 v[160:163], v[68:71], v[148:151], v[160:163]\n"
"s_waitcnt lgkmcnt(1)\n"
"v_mfma_f32_16x16x32_bf16 v[190:193], v[72:75], v[218:221], v[190:193]\n"
"ds_read_b128 v[214:217], v176 offset:32\n"
"v_mfma_f32_16x16x32_bf16 v[136:139], v[76:79], v[218:221], v[136:139]\n"
"s_add_u32 s70, s50, 4294966992\n"
"ds_write_b128 v6, v[248:251] offset:7168\n"
"v_mfma_f32_16x16x32_bf16 v[160:163], v[80:83], v[218:221], v[160:163]\n"
"s_waitcnt lgkmcnt(2)\n"
"ds_read_b128 v[148:151], v175 offset:1344\n"
"v_mfma_f32_16x16x32_bf16 v[144:147], v[202:205], v[132:135], v[144:147]\n"
"v_mfma_f32_16x16x32_bf16 v[144:147], v[204:207], v[24:27], v[144:147]\n"
"ds_read_b128 v[152:155], v175 offset:1360\n"
"v_cvt_pk_bf16_f32 v190, v190, v191\n"
"v_cvt_pk_bf16_f32 v191, v192, v193\n"
"v_cvt_pk_bf16_f32 v192, v136, v137\n"
"v_cvt_pk_bf16_f32 v193, v138, v139\n"
"v_cvt_pk_bf16_f32 v194, v160, v161\n"
"v_cvt_pk_bf16_f32 v195, v162, v163\n"
"s_waitcnt lgkmcnt(3)\n"
"v_mfma_f32_16x16x32_bf16 v[214:217], v[208:211], v[132:135], v[214:217]\n"
"v_max3_f32 v220, v144, v145, v146\n"
"v_max_f32 v220, v220, v147\n"
"v_mfma_f32_16x16x32_bf16 v[214:217], v[210:213], v[24:27], v[214:217]\n"
"s_waitcnt lgkmcnt(1)\n"
"ds_read_b128 v[136:139], v176 offset:0\n"
"v_min_u32 v174, s70, v177\n"
"v_mfma_f32_16x16x32_bf16 v[148:151], v[226:229], v[132:135], v[148:151]\n"
"v_mfma_f32_16x16x32_bf16 v[148:151], v[228:231], v[24:27], v[148:151]\n"
"s_waitcnt lgkmcnt(1)\n"
"ds_read_b128 v[156:159], v174 offset:2688\n"
"s_nop 0\n"
"v_max3_f32 v220, v220, v214, v215\n"
"v_max3_f32 v220, v220, v216, v217\n"
"v_mfma_f32_16x16x32_bf16 v[152:155], v[232:235], v[132:135], v[152:155]\n"
"v_mfma_f32_16x16x32_bf16 v[152:155], v[234:237], v[24:27], v[152:155]\n"
"s_waitcnt lgkmcnt(1)\n"
"ds_read_b128 v[144:147], v175 offset:1376\n"
"v_max3_f32 v220, v220, v148, v149\n"
"v_max3_f32 v220, v220, v150, v151\n"
"v_mfma_f32_16x16x32_bf16 v[136:139], v[196:199], v[132:135], v[136:139]\n"
"v_mfma_f32_16x16x32_bf16 v[136:139], v[198:201], v[24:27], v[136:139]\n"
"ds_read_b128 v[214:217], v174 offset:2704\n"
"ds_write_b128 v6, v[252:255] offset:8192\n"
"v_max3_f32 v220, v220, v152, v153\n"
"v_max3_f32 v220, v220, v154, v155\n"
"s_waitcnt lgkmcnt(3)\n"
"v_mfma_f32_16x16x32_bf16 v[156:159], v[178:181], v[132:135], v[156:159]\n"
"v_mfma_f32_16x16x32_bf16 v[156:159], v[180:183], v[24:27], v[156:159]\n"
"s_waitcnt lgkmcnt(2)\n"
"ds_read_b128 v[148:151], v174 offset:2720\n"
"v_max3_f32 v220, v220, v136, v137\n"
"v_max3_f32 v220, v220, v138, v139\n"
"v_mfma_f32_16x16x32_bf16 v[144:147], v[238:241], v[132:135], v[144:147]\n"
"v_mfma_f32_16x16x32_bf16 v[144:147], v[240:243], v[24:27], v[144:147]\n"
"s_waitcnt lgkmcnt(2)\n"
"ds_read_b128 v[152:155], v176 offset:16\n"
"v_max3_f32 v220, v220, v156, v157\n"
"v_max3_f32 v220, v220, v158, v159\n"
"v_mfma_f32_16x16x32_bf16 v[214:217], v[184:187], v[132:135], v[214:217]\n"
"v_mfma_f32_16x16x32_bf16 v[214:217], v[186:189], v[24:27], v[214:217]\n"
"s_waitcnt lgkmcnt(1)\n"
"ds_read_b128 v[136:139], v176 offset:32\n"
"v_max3_f32 v220, v220, v144, v145\n"
"v_max3_f32 v220, v220, v146, v147\n"
"v_mfma_f32_16x16x32_bf16 v[148:151], v[190:193], v[132:135], v[148:151]\n"
"v_mfma_f32_16x16x32_bf16 v[148:151], v[192:195], v[24:27], v[148:151]\n"
"s_waitcnt lgkmcnt(1)\n"
"ds_read_b128 v[156:159], v175 offset:1344\n"
"v_max3_f32 v220, v220, v214, v215\n"
"v_max3_f32 v220, v220, v216, v217\n"
"v_mfma_f32_16x16x32_bf16 v[152:155], v[202:205], v[132:135], v[152:155]\n"
"v_mfma_f32_16x16x32_bf16 v[152:155], v[204:207], v[24:27], v[152:155]\n"
"ds_read_b128 v[144:147], v175 offset:1360\n"
"s_nop 0\n"
"v_max3_f32 v220, v220, v148, v149\n"
"v_max3_f32 v220, v220, v150, v151\n"
"v_mov_b32 v221, v220\n"
"s_nop 1\n"
"v_permlane16_swap_b32 v220, v221\n"
"v_max_f32 v220, v220, v221\n"
"v_mov_b32 v221, v220\n"
"s_nop 1\n"
"v_permlane32_swap_b32 v220, v221\n"
"v_max_f32 v220, v220, v221\n"
"v_sub_f32 v152, v152, v220\n"
"v_exp_f32 v152, v152\n"
"v_sub_f32 v153, v153, v220\n"
"v_exp_f32 v153, v153\n"
"v_sub_f32 v154, v154, v220\n"
"v_exp_f32 v154, v154\n"
"v_sub_f32 v155, v155, v220\n"
"v_exp_f32 v155, v155\n"
"s_waitcnt lgkmcnt(2)\n"
"v_mfma_f32_16x16x32_bf16 v[136:139], v[208:211], v[132:135], v[136:139]\n"
"v_cvt_pk_bf16_f32 v152, v152, v153\n"
"v_cvt_pk_bf16_f32 v153, v154, v155\n"
"v_mfma_f32_16x16x32_bf16 v[136:139], v[210:213], v[24:27], v[136:139]\n"
"ds_write_addtid_b32 v152 offset:53248\n"
"ds_read_b128 v[214:217], v176 offset:0\n"
"ds_write_addtid_b32 v153 offset:53504\n"
"s_waitcnt lgkmcnt(4)\n"
"v_mfma_f32_16x16x32_bf16 v[156:159], v[226:229], v[132:135], v[156:159]\n"
"ds_write_b128 v6, v[140:143] offset:9216\n"
"v_mfma_f32_16x16x32_bf16 v[156:159], v[228:231], v[24:27], v[156:159]\n"
"ds_read_b128 v[148:151], v174 offset:2688\n"
"v_sub_f32 v136, v136, v220\n"
"v_exp_f32 v136, v136\n"
"v_sub_f32 v137, v137, v220\n"
"v_exp_f32 v137, v137\n"
"s_nop 0\n"
"v_cvt_pk_bf16_f32 v136, v136, v137\n"
"ds_write_addtid_b32 v136 offset:53760\n"
"s_waitcnt lgkmcnt(6)\n"
"v_mfma_f32_16x16x32_bf16 v[144:147], v[232:235], v[132:135], v[144:147]\n"
"v_sub_f32 v156, v156, v220\n"
"v_exp_f32 v156, v156\n"
"v_sub_f32 v157, v157, v220\n"
"v_exp_f32 v157, v157\n"
"v_sub_f32 v158, v158, v220\n"
"v_exp_f32 v158, v158\n"
"v_sub_f32 v159, v159, v220\n"
"v_exp_f32 v159, v159\n"
"v_cvt_pk_bf16_f32 v156, v156, v157\n"
"v_cvt_pk_bf16_f32 v157, v158, v159\n"
"v_mfma_f32_16x16x32_bf16 v[144:147], v[234:237], v[24:27], v[144:147]\n"
"ds_write_addtid_b32 v156 offset:54272\n"
"ds_write_addtid_b32 v157 offset:54528\n"
"s_waitcnt lgkmcnt(6)\n"
"v_mfma_f32_16x16x32_bf16 v[214:217], v[196:199], v[132:135], v[214:217]\n"
"ds_read_b128 v[136:139], v175 offset:1376\n"
"v_mfma_f32_16x16x32_bf16 v[214:217], v[198:201], v[24:27], v[214:217]\n"
"ds_read_b128 v[152:155], v174 offset:2704\n"
"s_nop 0\n"
"v_sub_f32 v144, v144, v220\n"
"v_exp_f32 v144, v144\n"
"v_sub_f32 v145, v145, v220\n"
"v_exp_f32 v145, v145\n"
"v_sub_f32 v146, v146, v220\n"
"v_exp_f32 v146, v146\n"
"v_sub_f32 v147, v147, v220\n"
"v_exp_f32 v147, v147\n"
"v_cvt_pk_bf16_f32 v144, v144, v145\n"
"v_sub_f32 v214, v214, v220\n"
"v_exp_f32 v214, v214\n"
"v_cvt_pk_bf16_f32 v145, v146, v147\n"
"v_sub_f32 v215, v215, v220\n"
"v_exp_f32 v215, v215\n"
"v_sub_f32 v216, v216, v220\n"
"v_exp_f32 v216, v216\n"
"v_sub_f32 v217, v217, v220\n"
"v_exp_f32 v217, v217\n"
"v_cvt_pk_bf16_f32 v214, v214, v215\n"
"v_cvt_pk_bf16_f32 v215, v216, v217\n"
"ds_write_addtid_b32 v144 offset:54784\n"
"ds_write_addtid_b32 v145 offset:55040\n"
"s_waitcnt lgkmcnt(7)\n"
"v_mfma_f32_16x16x32_bf16 v[148:151], v[178:181], v[132:135], v[148:151]\n"
"v_mfma_f32_16x16x32_bf16 v[148:151], v[180:183], v[24:27], v[148:151]\n"
"s_waitcnt lgkmcnt(0)\n"
"ds_read_b128 v[196:199], v174 offset:2720\n"
"s_barrier\n"
"ds_write_addtid_b32 v214 offset:58880\n"
"ds_write_addtid_b32 v215 offset:59136\n"
"v_mfma_f32_16x16x32_bf16 v[136:139], v[238:241], v[132:135], v[136:139]\n"
"v_mfma_f32_16x16x32_bf16 v[136:139], v[240:243], v[24:27], v[136:139]\n"
"ds_read_b128 v[216:219], v4 offset:24576\n"
"v_sub_f32 v148, v148, v220\n"
"v_exp_f32 v148, v148\n"
"v_sub_f32 v149, v149, v220\n"
"v_exp_f32 v149, v149\n"
"v_sub_f32 v150, v150, v220\n"
"v_exp_f32 v150, v150\n"
"v_sub_f32 v151, v151, v220\n"
"v_exp_f32 v151, v151\n"
"v_cvt_pk_bf16_f32 v148, v148, v149\n"
"v_cvt_pk_bf16_f32 v149, v150, v151\n"
"v_sub_f32 v136, v136, v220\n"
"v_exp_f32 v136, v136\n"
"v_sub_f32 v137, v137, v220\n"
"v_exp_f32 v137, v137\n"
"ds_write_addtid_b32 v148 offset:57344\n"
"v_cvt_pk_bf16_f32 v136, v136, v137\n"
"ds_write_addtid_b32 v149 offset:57600\n"
"v_mfma_f32_16x16x32_bf16 v[152:155], v[184:187], v[132:135], v[152:155]\n"
"ds_write_addtid_b32 v136 offset:33792\n"
"v_mfma_f32_16x16x32_bf16 v[152:155], v[186:189], v[24:27], v[152:155]\n"
"s_waitcnt lgkmcnt(6)\n"
"v_mfma_f32_16x16x32_bf16 v[196:199], v[190:193], v[132:135], v[196:199]\n"
"ds_read_b128 v[144:147], v5 offset:24576\n"
"v_mfma_f32_16x16x32_bf16 v[196:199], v[192:195], v[24:27], v[196:199]\n"
"s_waitcnt lgkmcnt(4)\n"
"v_mfma_f32_16x16x32_bf16 v[148:151], v[84:87], v[216:219], 0\n"
"v_mfma_f32_16x16x32_bf16 v[156:159], v[88:91], v[216:219], 0\n"
"ds_read_b128 v[136:139], v4 offset:26624\n"
"v_mfma_f32_16x16x32_bf16 v[160:163], v[92:95], v[216:219], 0\n"
"s_waitcnt lgkmcnt(1)\n"
"v_mfma_f32_16x16x32_bf16 v[148:151], v[96:99], v[144:147], v[148:151]\n"
"v_sub_f32 v152, v152, v220\n"
"v_exp_f32 v152, v152\n"
"v_sub_f32 v153, v153, v220\n"
"v_exp_f32 v153, v153\n"
"v_sub_f32 v154, v154, v220\n"
"v_exp_f32 v154, v154\n"
"v_sub_f32 v155, v155, v220\n"
"v_exp_f32 v155, v155\n"
"v_cvt_pk_bf16_f32 v152, v152, v153\n"
"v_cvt_pk_bf16_f32 v153, v154, v155\n"
"v_mfma_f32_16x16x32_bf16 v[156:159], v[100:103], v[144:147], v[156:159]\n"
"ds_write_addtid_b32 v152 offset:57856\n"
"ds_write_addtid_b32 v153 offset:58112\n"
"v_mfma_f32_16x16x32_bf16 v[160:163], v[222:225], v[144:147], v[160:163]\n"
"s_waitcnt lgkmcnt(2)\n"
"ds_read_b128 v[198:201], v5 offset:26624\n"
"v_mfma_f32_16x16x32_bf16 v[148:151], v[104:107], v[136:139], v[148:151]\n"
"v_sub_f32 v196, v196, v220\n"
"v_exp_f32 v196, v196\n"
"v_mfma_f32_16x16x32_bf16 v[156:159], v[108:111], v[136:139], v[156:159]\n"
"ds_read_b128 v[132:135], v4 offset:28672\n"
"v_sub_f32 v197, v197, v220\n"
"v_exp_f32 v197, v197\n"
"v_mfma_f32_16x16x32_bf16 v[160:163], v[244:247], v[136:139], v[160:163]\n"
"v_cvt_pk_bf16_f32 v196, v196, v197\n"
"ds_write_addtid_b32 v196 offset:58368\n"
"s_waitcnt lgkmcnt(2)\n"
"v_mfma_f32_16x16x32_bf16 v[148:151], v[112:115], v[198:201], v[148:151]\n"
"v_mfma_f32_16x16x32_bf16 v[156:159], v[116:119], v[198:201], v[156:159]\n"
"ds_read_b128 v[218:221], v5 offset:28672\n"
"v_mfma_f32_16x16x32_bf16 v[160:163], v[248:251], v[198:201], v[160:163]\n"
"s_waitcnt lgkmcnt(2)\n"
"v_mfma_f32_16x16x32_bf16 v[148:151], v[0:3], v[132:135], v[148:151]\n"
"s_waitcnt lgkmcnt(1)\n"
"s_barrier\n"
"v_mfma_f32_16x16x32_bf16 v[156:159], v[120:123], v[132:135], v[156:159]\n"
"ds_read_b128 v[222:225], v4 offset:36864\n"
"v_mfma_f32_16x16x32_bf16 v[160:163], v[252:255], v[132:135], v[160:163]\n"
"s_waitcnt lgkmcnt(1)\n"
"v_mfma_f32_16x16x32_bf16 v[148:151], v[124:127], v[218:221], v[148:151]\n"
"v_mfma_f32_16x16x32_bf16 v[156:159], v[128:131], v[218:221], v[156:159]\n"
"ds_read_b128 v[196:199], v5 offset:36864\n"
"v_mfma_f32_16x16x32_bf16 v[160:163], v[140:143], v[218:221], v[160:163]\n"
"s_waitcnt lgkmcnt(1)\n"
"v_mfma_f32_16x16x32_bf16 v[214:217], v[8:11], v[222:225], 0\n"
"v_mfma_f32_16x16x32_bf16 v[248:251], v[12:15], v[222:225], 0\n"
"ds_read_b128 v[244:247], v4 offset:38912\n"
"v_mfma_f32_16x16x32_bf16 v[252:255], v[16:19], v[222:225], 0\n"
"s_waitcnt lgkmcnt(1)\n"
"v_mfma_f32_16x16x32_bf16 v[214:217], v[20:23], v[196:199], v[214:217]\n"
"v_mul_f32 v148, s60, v148\n"
"v_mul_f32 v149, s60, v149\n"
"v_mfma_f32_16x16x32_bf16 v[248:251], v[28:31], v[196:199], v[248:251]\n"
"ds_read_b128 v[218:221], v5 offset:38912\n"
"v_cvt_pk_bf16_f32 v148, v148, v149\n"
"v_mfma_f32_16x16x32_bf16 v[252:255], v[32:35], v[196:199], v[252:255]\n"
"s_waitcnt lgkmcnt(1)\n"
"v_mul_f32 v150, s60, v150\n"
"v_mul_f32 v151, s60, v151\n"
"v_mfma_f32_16x16x32_bf16 v[214:217], v[36:39], v[244:247], v[214:217]\n"
"v_cvt_pk_bf16_f32 v149, v150, v151\n"
"v_mul_f32 v156, s60, v156\n"
"v_mfma_f32_16x16x32_bf16 v[248:251], v[40:43], v[244:247], v[248:251]\n"
"ds_read_b128 v[196:199], v4 offset:40960\n"
"v_mul_f32 v157, s60, v157\n"
"v_mfma_f32_16x16x32_bf16 v[252:255], v[44:47], v[244:247], v[252:255]\n"
"v_mul_f32 v158, s60, v158\n"
"v_mul_f32 v159, s60, v159\n"
"v_cvt_pk_bf16_f32 v150, v156, v157\n"
"v_cvt_pk_bf16_f32 v151, v158, v159\n"
"s_waitcnt lgkmcnt(1)\n"
"v_mfma_f32_16x16x32_bf16 v[214:217], v[48:51], v[218:221], v[214:217]\n"
"v_mul_f32 v160, s60, v160\n"
"v_mul_f32 v161, s60, v161\n"
"v_mfma_f32_16x16x32_bf16 v[248:251], v[52:55], v[218:221], v[248:251]\n"
"ds_read_b128 v[222:225], v5 offset:40960\n"
"v_mul_f32 v162, s60, v162\n"
"v_mfma_f32_16x16x32_bf16 v[252:255], v[56:59], v[218:221], v[252:255]\n"
"s_waitcnt lgkmcnt(1)\n"
"v_mul_f32 v163, s60, v163\n"
"v_cvt_pk_bf16_f32 v26, v160, v161\n"
"v_mfma_f32_16x16x32_bf16 v[214:217], v[60:63], v[196:199], v[214:217]\n"
"v_cvt_pk_bf16_f32 v27, v162, v163\n"
"v_mfma_f32_16x16x32_bf16 v[248:251], v[64:67], v[196:199], v[248:251]\n"
"ds_read_b128 v[132:135], v4 offset:43008\n"
"v_mfma_f32_16x16x32_bf16 v[252:255], v[68:71], v[196:199], v[252:255]\n"
"s_waitcnt lgkmcnt(1)\n"
"v_mfma_f32_16x16x32_bf16 v[214:217], v[72:75], v[222:225], v[214:217]\n"
"v_mfma_f32_16x16x32_bf16 v[248:251], v[76:79], v[222:225], v[248:251]\n"
"ds_read_b128 v[136:139], v5 offset:43008\n"
"v_mfma_f32_16x16x32_bf16 v[252:255], v[80:83], v[222:225], v[252:255]\n"
"s_waitcnt lgkmcnt(1)\n"
"v_mfma_f32_16x16x32_bf16 v[244:247], v[8:11], v[132:135], 0\n"
"v_mfma_f32_16x16x32_bf16 v[140:143], v[12:15], v[132:135], 0\n"
"ds_read_b128 v[196:199], v4 offset:45056\n"
"v_mfma_f32_16x16x32_bf16 v[144:147], v[16:19], v[132:135], 0\n"
"s_waitcnt lgkmcnt(1)\n"
"v_mfma_f32_16x16x32_bf16 v[244:247], v[20:23], v[136:139], v[244:247]\n"
"v_cvt_pk_bf16_f32 v214, v214, v215\n"
"v_cvt_pk_bf16_f32 v215, v216, v217\n"
"v_cvt_pk_bf16_f32 v216, v248, v249\n"
"v_cvt_pk_bf16_f32 v217, v250, v251\n"
"v_cvt_pk_bf16_f32 v218, v252, v253\n"
"v_cvt_pk_bf16_f32 v219, v254, v255\n"
"v_mfma_f32_16x16x32_bf16 v[140:143], v[28:31], v[136:139], v[140:143]\n"
"ds_read_b128 v[220:223], v5 offset:45056\n"
"v_bfe_u32 v225, v6, 2, 6\n"
"v_mfma_f32_16x16x32_bf16 v[144:147], v[32:35], v[136:139], v[144:147]\n"
"s_waitcnt lgkmcnt(1)\n"
"ds_read_b32 v225, v225 offset:1280\n"
"v_mfma_f32_16x16x32_bf16 v[244:247], v[36:39], v[196:199], v[244:247]\n"
"s_add_u32 s71, s50, 4294967280\n"
"v_mfma_f32_16x16x32_bf16 v[140:143], v[40:43], v[196:199], v[140:143]\n"
"ds_read_b128 v[248:251], v4 offset:47104\n"
"v_mfma_f32_16x16x32_bf16 v[144:147], v[44:47], v[196:199], v[144:147]\n"
"s_waitcnt lgkmcnt(2)\n"
"v_mfma_f32_16x16x32_bf16 v[244:247], v[48:51], v[220:223], v[244:247]\n"
"v_mfma_f32_16x16x32_bf16 v[140:143], v[52:55], v[220:223], v[140:143]\n"
"s_add_u32 s72, s50, 1024\n"
"ds_read_b128 v[252:255], v5 offset:47104\n"
"s_waitcnt lgkmcnt(2)\n"
"v_add_u32_sdwa v224, v225, v7 dst_sel:DWORD dst_unused:UNUSED_PAD src0_sel:WORD_0 src1_sel:DWORD\n"
"v_mfma_f32_16x16x32_bf16 v[144:147], v[56:59], v[220:223], v[144:147]\n"
"s_waitcnt lgkmcnt(1)\n"
"v_max_u32 v201, s71, v224\n"
"v_mfma_f32_16x16x32_bf16 v[244:247], v[60:63], v[248:251], v[244:247]\n"
"v_mfma_f32_16x16x32_bf16 v[140:143], v[64:67], v[248:251], v[140:143]\n"
"ds_read_b128 v[196:199], v201 offset:16\n"
"v_min_u32 v200, s72, v224\n"
"v_mfma_f32_16x16x32_bf16 v[144:147], v[68:71], v[248:251], v[144:147]\n"
"s_waitcnt lgkmcnt(1)\n"
"v_mfma_f32_16x16x32_bf16 v[244:247], v[72:75], v[252:255], v[244:247]\n"
"ds_read_b128 v[220:223], v201 offset:32\n"
"v_mfma_f32_16x16x32_bf16 v[140:143], v[76:79], v[252:255], v[140:143]\n"
"v_mfma_f32_16x16x32_bf16 v[144:147], v[80:83], v[252:255], v[144:147]\n"
"s_waitcnt lgkmcnt(1)\n"
"ds_read_b128 v[132:135], v200 offset:1376\n"
"v_mfma_f32_16x16x32_bf16 v[196:199], v[202:205], v[148:151], v[196:199]\n"
"v_mfma_f32_16x16x32_bf16 v[196:199], v[204:207], v[24:27], v[196:199]\n"
"s_waitcnt lgkmcnt(1)\n"
"ds_read_b128 v[136:139], v200 offset:1360\n"
"v_mfma_f32_16x16x32_bf16 v[220:223], v[208:211], v[148:151], v[220:223]\n"
"v_cvt_pk_bf16_f32 v244, v244, v245\n"
"v_cvt_pk_bf16_f32 v245, v246, v247\n"
"v_cvt_pk_bf16_f32 v246, v140, v141\n"
"v_cvt_pk_bf16_f32 v247, v142, v143\n"
"v_cvt_pk_bf16_f32 v248, v144, v145\n"
"v_cvt_pk_bf16_f32 v249, v146, v147\n"
"v_max3_f32 v250, v196, v197, v198\n"
"v_max_f32 v250, v250, v199\n"
"v_mfma_f32_16x16x32_bf16 v[220:223], v[210:213], v[24:27], v[220:223]\n"
"s_waitcnt lgkmcnt(1)\n"
"ds_read_b128 v[252:255], v201 offset:48\n"
"v_mfma_f32_16x16x32_bf16 v[132:135], v[238:241], v[148:151], v[132:135]\n"
"v_mfma_f32_16x16x32_bf16 v[132:135], v[240:243], v[24:27], v[132:135]\n"
"s_waitcnt lgkmcnt(1)\n"
"ds_read_b128 v[140:143], v200 offset:1392\n"
"s_nop 1\n"
"v_max3_f32 v250, v250, v220, v221\n"
"v_max3_f32 v250, v250, v222, v223\n"
"v_mfma_f32_16x16x32_bf16 v[136:139], v[232:235], v[148:151], v[136:139]\n"
"v_mfma_f32_16x16x32_bf16 v[136:139], v[234:237], v[24:27], v[136:139]\n"
"s_waitcnt lgkmcnt(1)\n"
"ds_read_b128 v[196:199], v201 offset:16\n"
"v_max3_f32 v250, v250, v132, v133\n"
"v_max3_f32 v250, v250, v134, v135\n"
"v_mfma_f32_16x16x32_bf16 v[252:255], v[214:217], v[148:151], v[252:255]\n"
"v_mfma_f32_16x16x32_bf16 v[252:255], v[216:219], v[24:27], v[252:255]\n"
"s_waitcnt lgkmcnt(1)\n"
"ds_read_b128 v[220:223], v201 offset:32\n"
"v_max3_f32 v250, v250, v136, v137\n"
"v_max3_f32 v250, v250, v138, v139\n"
"v_mfma_f32_16x16x32_bf16 v[140:143], v[244:247], v[148:151], v[140:143]\n"
"v_mfma_f32_16x16x32_bf16 v[140:143], v[246:249], v[24:27], v[140:143]\n"
"s_waitcnt lgkmcnt(1)\n"
"ds_read_b128 v[132:135], v200 offset:1376\n"
"v_max3_f32 v250, v250, v252, v253\n"
"v_max3_f32 v250, v250, v254, v255\n"
"v_mfma_f32_16x16x32_bf16 v[196:199], v[202:205], v[148:151], v[196:199]\n"
"v_mfma_f32_16x16x32_bf16 v[196:199], v[204:207], v[24:27], v[196:199]\n"
"ds_read_b128 v[136:139], v200 offset:1360\n"
"s_nop 0\n"
"v_max3_f32 v250, v250, v140, v141\n"
"v_max3_f32 v250, v250, v142, v143\n"
"v_mov_b32 v251, v250\n"
"s_nop 1\n"
"v_permlane16_swap_b32 v250, v251\n"
"v_max_f32 v250, v250, v251\n"
"v_mov_b32 v251, v250\n"
"s_nop 1\n"
"v_permlane32_swap_b32 v250, v251\n"
"v_max_f32 v250, v250, v251\n"
"v_sub_f32 v196, v196, v250\n"
"v_exp_f32 v196, v196\n"
"v_sub_f32 v197, v197, v250\n"
"v_exp_f32 v197, v197\n"
"v_sub_f32 v198, v198, v250\n"
"v_exp_f32 v198, v198\n"
"v_sub_f32 v199, v199, v250\n"
"v_exp_f32 v199, v199\n"
"s_waitcnt lgkmcnt(2)\n"
"v_mfma_f32_16x16x32_bf16 v[220:223], v[208:211], v[148:151], v[220:223]\n"
"v_cvt_pk_bf16_f32 v196, v196, v197\n"
"v_cvt_pk_bf16_f32 v197, v198, v199\n"
"v_mfma_f32_16x16x32_bf16 v[220:223], v[210:213], v[24:27], v[220:223]\n"
"ds_write_addtid_b32 v196 offset:55296\n"
"ds_read_b128 v[252:255], v201 offset:48\n"
"ds_write_addtid_b32 v197 offset:55552\n"
"s_waitcnt lgkmcnt(4)\n"
"v_mfma_f32_16x16x32_bf16 v[132:135], v[238:241], v[148:151], v[132:135]\n"
"v_mfma_f32_16x16x32_bf16 v[132:135], v[240:243], v[24:27], v[132:135]\n"
"ds_read_b128 v[140:143], v200 offset:1392\n"
"s_nop 0\n"
"v_sub_f32 v220, v220, v250\n"
"v_exp_f32 v220, v220\n"
"v_sub_f32 v221, v221, v250\n"
"v_exp_f32 v221, v221\n"
"v_sub_f32 v222, v222, v250\n"
"v_exp_f32 v222, v222\n"
"v_sub_f32 v223, v223, v250\n"
"v_exp_f32 v223, v223\n"
"v_sub_f32 v132, v132, v250\n"
"v_exp_f32 v132, v132\n"
"v_sub_f32 v133, v133, v250\n"
"v_exp_f32 v133, v133\n"
"v_sub_f32 v134, v134, v250\n"
"v_exp_f32 v134, v134\n"
"v_sub_f32 v135, v135, v250\n"
"v_exp_f32 v135, v135\n"
"v_cvt_pk_bf16_f32 v220, v220, v221\n"
"v_cvt_pk_bf16_f32 v132, v132, v133\n"
"v_cvt_pk_bf16_f32 v221, v222, v223\n"
"v_cvt_pk_bf16_f32 v133, v134, v135\n"
"ds_write_addtid_b32 v220 offset:55808\n"
"ds_write_addtid_b32 v132 offset:56320\n"
"ds_write_addtid_b32 v221 offset:56064\n"
"s_waitcnt lgkmcnt(7)\n"
"v_mfma_f32_16x16x32_bf16 v[136:139], v[232:235], v[148:151], v[136:139]\n"
"ds_write_addtid_b32 v133 offset:56576\n"
"v_mfma_f32_16x16x32_bf16 v[136:139], v[234:237], v[24:27], v[136:139]\n"
"s_waitcnt lgkmcnt(6)\n"
"ds_read_b128 v[196:199], v4 offset:30720\n"
"v_mfma_f32_16x16x32_bf16 v[252:255], v[214:217], v[148:151], v[252:255]\n"
"v_mfma_f32_16x16x32_bf16 v[252:255], v[216:219], v[24:27], v[252:255]\n"
"s_waitcnt lgkmcnt(1)\n"
"s_barrier\n"
"s_nop 1\n"
"v_sub_f32 v136, v136, v250\n"
"v_exp_f32 v136, v136\n"
"v_sub_f32 v137, v137, v250\n"
"v_exp_f32 v137, v137\n"
"v_sub_f32 v138, v138, v250\n"
"v_exp_f32 v138, v138\n"
"v_sub_f32 v139, v139, v250\n"
"v_exp_f32 v139, v139\n"
"v_cvt_pk_bf16_f32 v136, v136, v137\n"
"v_cvt_pk_bf16_f32 v137, v138, v139\n"
"v_sub_f32 v252, v252, v250\n"
"v_exp_f32 v252, v252\n"
"v_sub_f32 v253, v253, v250\n"
"v_exp_f32 v253, v253\n"
"v_sub_f32 v254, v254, v250\n"
"v_exp_f32 v254, v254\n"
"v_sub_f32 v255, v255, v250\n"
"v_exp_f32 v255, v255\n"
"v_cvt_pk_bf16_f32 v252, v252, v253\n"
"v_cvt_pk_bf16_f32 v253, v254, v255\n"
"ds_write_addtid_b32 v136 offset:57856\n"
"ds_write_addtid_b32 v137 offset:58112\n"
"v_mfma_f32_16x16x32_bf16 v[140:143], v[244:247], v[148:151], v[140:143]\n"
"ds_read_b128 v[220:223], v5 offset:30720\n"
"v_mfma_f32_16x16x32_bf16 v[140:143], v[246:249], v[24:27], v[140:143]\n"
"ds_write_addtid_b32 v252 offset:57344\n"
"s_waitcnt lgkmcnt(4)\n"
"v_mfma_f32_16x16x32_bf16 v[132:135], v[84:87], v[196:199], 0\n"
"ds_read_b128 v[144:147], v6 offset:5120\n"
"v_mfma_f32_16x16x32_bf16 v[136:139], v[88:91], v[196:199], 0\n"
"ds_read_b128 v[148:151], v4 offset:32768\n"
"v_mfma_f32_16x16x32_bf16 v[152:155], v[92:95], v[196:199], 0\n"
"ds_write_addtid_b32 v253 offset:57600\n"
"s_waitcnt lgkmcnt(4)\n"
"v_mfma_f32_16x16x32_bf16 v[132:135], v[96:99], v[220:223], v[132:135]\n"
"ds_read_b128 v[156:159], v6 offset:6144\n"
"v_mfma_f32_16x16x32_bf16 v[136:139], v[100:103], v[220:223], v[136:139]\n"
"s_waitcnt lgkmcnt(3)\n"
"ds_read_b128 v[160:163], v5 offset:32768\n"
"v_mfma_f32_16x16x32_bf16 v[152:155], v[144:147], v[220:223], v[152:155]\n"
"s_waitcnt lgkmcnt(3)\n"
"v_sub_f32 v140, v140, v250\n"
"v_exp_f32 v140, v140\n"
"v_mfma_f32_16x16x32_bf16 v[132:135], v[104:107], v[148:151], v[132:135]\n"
"ds_read_b128 v[196:199], v6 offset:7168\n"
"v_sub_f32 v141, v141, v250\n"
"v_exp_f32 v141, v141\n"
"v_sub_f32 v142, v142, v250\n"
"v_exp_f32 v142, v142\n"
"v_mfma_f32_16x16x32_bf16 v[136:139], v[108:111], v[148:151], v[136:139]\n"
"ds_read_b128 v[220:223], v4 offset:34816\n"
"v_cvt_pk_bf16_f32 v140, v140, v141\n"
"v_sub_f32 v143, v143, v250\n"
"v_exp_f32 v143, v143\n"
"s_waitcnt lgkmcnt(3)\n"
"v_mfma_f32_16x16x32_bf16 v[152:155], v[156:159], v[148:151], v[152:155]\n"
"ds_write_addtid_b32 v140 offset:56832\n"
"v_cvt_pk_bf16_f32 v141, v142, v143\n"
"s_waitcnt lgkmcnt(3)\n"
"v_mfma_f32_16x16x32_bf16 v[132:135], v[112:115], v[160:163], v[132:135]\n"
"ds_write_addtid_b32 v141 offset:57088\n"
"v_mfma_f32_16x16x32_bf16 v[136:139], v[116:119], v[160:163], v[136:139]\n"
"s_waitcnt lgkmcnt(3)\n"
"ds_read_b128 v[250:253], v6 offset:8192\n"
"v_mfma_f32_16x16x32_bf16 v[152:155], v[196:199], v[160:163], v[152:155]\n"
"s_waitcnt lgkmcnt(3)\n"
"ds_read_b128 v[144:147], v5 offset:34816\n"
"v_mfma_f32_16x16x32_bf16 v[132:135], v[0:3], v[220:223], v[132:135]\n"
"ds_read_b128 v[148:151], v6 offset:9216\n"
"v_mfma_f32_16x16x32_bf16 v[136:139], v[120:123], v[220:223], v[136:139]\n"
"s_waitcnt lgkmcnt(3)\n"
"ds_read_b128 v[140:143], v4 offset:49152\n"
"s_barrier\n"
"s_waitcnt lgkmcnt(3)\n"
"v_mfma_f32_16x16x32_bf16 v[152:155], v[250:253], v[220:223], v[152:155]\n"
"s_waitcnt lgkmcnt(2)\n"
"v_mfma_f32_16x16x32_bf16 v[132:135], v[124:127], v[144:147], v[132:135]\n"
"v_mfma_f32_16x16x32_bf16 v[136:139], v[128:131], v[144:147], v[136:139]\n"
"s_waitcnt lgkmcnt(1)\n"
"ds_read_b128 v[156:159], v5 offset:49152\n"
"v_mfma_f32_16x16x32_bf16 v[152:155], v[148:151], v[144:147], v[152:155]\n"
"s_waitcnt lgkmcnt(1)\n"
"v_mfma_f32_16x16x32_bf16 v[196:199], v[8:11], v[140:143], 0\n"
"v_mfma_f32_16x16x32_bf16 v[160:163], v[12:15], v[140:143], 0\n"
"ds_read_b128 v[220:223], v4 offset:51200\n"
"v_mfma_f32_16x16x32_bf16 v[250:253], v[16:19], v[140:143], 0\n"
"s_waitcnt lgkmcnt(1)\n"
"v_mfma_f32_16x16x32_bf16 v[196:199], v[20:23], v[156:159], v[196:199]\n"
"v_mul_f32 v132, s60, v132\n"
"v_mul_f32 v133, s60, v133\n"
"v_mfma_f32_16x16x32_bf16 v[160:163], v[28:31], v[156:159], v[160:163]\n"
"ds_read_b128 v[144:147], v5 offset:51200\n"
"v_cvt_pk_bf16_f32 v132, v132, v133\n"
"v_mfma_f32_16x16x32_bf16 v[250:253], v[32:35], v[156:159], v[250:253]\n"
"s_waitcnt lgkmcnt(1)\n"
"v_mul_f32 v134, s60, v134\n"
"v_mul_f32 v135, s60, v135\n"
"v_mfma_f32_16x16x32_bf16 v[196:199], v[36:39], v[220:223], v[196:199]\n"
"v_cvt_pk_bf16_f32 v133, v134, v135\n"
"v_mul_f32 v136, s60, v136\n"
"v_mfma_f32_16x16x32_bf16 v[160:163], v[40:43], v[220:223], v[160:163]\n"
"ds_read_b128 v[140:143], v4 offset:53248\n"
"v_mul_f32 v137, s60, v137\n"
"v_mfma_f32_16x16x32_bf16 v[250:253], v[44:47], v[220:223], v[250:253]\n"
"v_mul_f32 v138, s60, v138\n"
"v_mul_f32 v139, s60, v139\n"
"v_cvt_pk_bf16_f32 v134, v136, v137\n"
"v_cvt_pk_bf16_f32 v135, v138, v139\n"
"s_waitcnt lgkmcnt(1)\n"
"v_mfma_f32_16x16x32_bf16 v[196:199], v[48:51], v[144:147], v[196:199]\n"
"v_mul_f32 v152, s60, v152\n"
"v_mul_f32 v153, s60, v153\n"
"v_mfma_f32_16x16x32_bf16 v[160:163], v[52:55], v[144:147], v[160:163]\n"
"ds_read_b128 v[148:151], v5 offset:53248\n"
"v_mul_f32 v154, s60, v154\n"
"v_mfma_f32_16x16x32_bf16 v[250:253], v[56:59], v[144:147], v[250:253]\n"
"s_waitcnt lgkmcnt(1)\n"
"v_mul_f32 v155, s60, v155\n"
"v_cvt_pk_bf16_f32 v26, v152, v153\n"
"v_mfma_f32_16x16x32_bf16 v[196:199], v[60:63], v[140:143], v[196:199]\n"
"v_cvt_pk_bf16_f32 v27, v154, v155\n"
"v_mfma_f32_16x16x32_bf16 v[160:163], v[64:67], v[140:143], v[160:163]\n"
"ds_read_b128 v[136:139], v4 offset:55296\n"
"v_mfma_f32_16x16x32_bf16 v[250:253], v[68:71], v[140:143], v[250:253]\n"
"s_waitcnt lgkmcnt(1)\n"
"v_mfma_f32_16x16x32_bf16 v[196:199], v[72:75], v[148:151], v[196:199]\n"
"v_mfma_f32_16x16x32_bf16 v[160:163], v[76:79], v[148:151], v[160:163]\n"
"ds_read_b128 v[144:147], v5 offset:55296\n"
"v_mfma_f32_16x16x32_bf16 v[250:253], v[80:83], v[148:151], v[250:253]\n"
"s_waitcnt lgkmcnt(1)\n"
"v_mfma_f32_16x16x32_bf16 v[220:223], v[8:11], v[136:139], 0\n"
"v_mfma_f32_16x16x32_bf16 v[152:155], v[12:15], v[136:139], 0\n"
"ds_read_b128 v[140:143], v4 offset:57344\n"
"v_mfma_f32_16x16x32_bf16 v[156:159], v[16:19], v[136:139], 0\n"
"s_waitcnt lgkmcnt(1)\n"
"v_mfma_f32_16x16x32_bf16 v[220:223], v[20:23], v[144:147], v[220:223]\n"
"v_cvt_pk_bf16_f32 v196, v196, v197\n"
"v_cvt_pk_bf16_f32 v197, v198, v199\n"
"v_cvt_pk_bf16_f32 v198, v160, v161\n"
"v_cvt_pk_bf16_f32 v199, v162, v163\n"
"v_cvt_pk_bf16_f32 v200, v250, v251\n"
"v_cvt_pk_bf16_f32 v201, v252, v253\n"
"v_mfma_f32_16x16x32_bf16 v[152:155], v[28:31], v[144:147], v[152:155]\n"
"ds_read_b128 v[136:139], v5 offset:57344\n"
"v_bfe_u32 v225, v6, 2, 6\n"
"v_mfma_f32_16x16x32_bf16 v[156:159], v[32:35], v[144:147], v[156:159]\n"
"s_waitcnt lgkmcnt(1)\n"
"ds_read_b32 v225, v225 offset:1344\n"
"v_mfma_f32_16x16x32_bf16 v[220:223], v[36:39], v[140:143], v[220:223]\n"
"v_mfma_f32_16x16x32_bf16 v[152:155], v[40:43], v[140:143], v[152:155]\n"
"s_add_u32 s69, s50, 4294966976\n"
"ds_read_b128 v[250:253], v4 offset:59392\n"
"v_mfma_f32_16x16x32_bf16 v[156:159], v[44:47], v[140:143], v[156:159]\n"
"s_waitcnt lgkmcnt(2)\n"
"v_mfma_f32_16x16x32_bf16 v[220:223], v[48:51], v[136:139], v[220:223]\n"
"v_mfma_f32_16x16x32_bf16 v[152:155], v[52:55], v[136:139], v[152:155]\n"
"s_waitcnt lgkmcnt(1)\n"
"ds_read_b128 v[144:147], v5 offset:59392\n"
"v_add_u32_sdwa v224, v225, v7 dst_sel:DWORD dst_unused:UNUSED_PAD src0_sel:WORD_0 src1_sel:DWORD\n"
"v_mfma_f32_16x16x32_bf16 v[156:159], v[56:59], v[136:139], v[156:159]\n"
"s_add_u32 s73, s50, 4294967280\n"
"v_min_u32 v177, s69, v224\n"
"s_waitcnt lgkmcnt(1)\n"
"v_mfma_f32_16x16x32_bf16 v[220:223], v[60:63], v[250:253], v[220:223]\n"
"v_mfma_f32_16x16x32_bf16 v[152:155], v[64:67], v[250:253], v[152:155]\n"
"ds_read_b128 v[140:143], v177 offset:2704\n"
"v_max_u32 v176, s73, v224\n"
"v_mfma_f32_16x16x32_bf16 v[156:159], v[68:71], v[250:253], v[156:159]\n"
"s_waitcnt lgkmcnt(1)\n"
"v_mfma_f32_16x16x32_bf16 v[220:223], v[72:75], v[144:147], v[220:223]\n"
"s_add_u32 s68, s50, 4294965936\n"
"ds_read_b128 v[136:139], v177 offset:2720\n"
"v_mfma_f32_16x16x32_bf16 v[152:155], v[76:79], v[144:147], v[152:155]\n"
"v_mfma_f32_16x16x32_bf16 v[156:159], v[80:83], v[144:147], v[156:159]\n"
"s_waitcnt lgkmcnt(1)\n"
"ds_read_b128 v[148:151], v176 offset:32\n"
"v_max_u32 v251, s68, v224\n"
"v_mfma_f32_16x16x32_bf16 v[140:143], v[184:187], v[132:135], v[140:143]\n"
"v_mfma_f32_16x16x32_bf16 v[140:143], v[186:189], v[24:27], v[140:143]\n"
"ds_read_b128 v[252:255], v176 offset:48\n"
"v_cvt_pk_bf16_f32 v220, v220, v221\n"
"v_cvt_pk_bf16_f32 v221, v222, v223\n"
"v_cvt_pk_bf16_f32 v222, v152, v153\n"
"v_cvt_pk_bf16_f32 v223, v154, v155\n"
"v_cvt_pk_bf16_f32 v224, v156, v157\n"
"v_cvt_pk_bf16_f32 v225, v158, v159\n"
"s_waitcnt lgkmcnt(2)\n"
"v_mfma_f32_16x16x32_bf16 v[136:139], v[190:193], v[132:135], v[136:139]\n"
"v_max3_f32 v174, v140, v141, v142\n"
"v_max_f32 v174, v174, v143\n"
"v_mfma_f32_16x16x32_bf16 v[136:139], v[192:195], v[24:27], v[136:139]\n"
"s_waitcnt lgkmcnt(1)\n"
"ds_read_b128 v[144:147], v251 offset:1392\n"
"v_mfma_f32_16x16x32_bf16 v[148:151], v[208:211], v[132:135], v[148:151]\n"
"v_mfma_f32_16x16x32_bf16 v[148:151], v[210:213], v[24:27], v[148:151]\n"
"s_waitcnt lgkmcnt(1)\n"
"ds_read_b128 v[152:155], v176 offset:16\n"
"s_nop 1\n"
"v_max3_f32 v174, v174, v136, v137\n"
"v_max3_f32 v174, v174, v138, v139\n"
"v_mfma_f32_16x16x32_bf16 v[252:255], v[214:217], v[132:135], v[252:255]\n"
"v_mfma_f32_16x16x32_bf16 v[252:255], v[216:219], v[24:27], v[252:255]\n"
"s_waitcnt lgkmcnt(1)\n"
"ds_read_b128 v[140:143], v251 offset:1360\n"
"v_max3_f32 v174, v174, v148, v149\n"
"v_max3_f32 v174, v174, v150, v151\n"
"v_mfma_f32_16x16x32_bf16 v[144:147], v[244:247], v[132:135], v[144:147]\n"
"v_mfma_f32_16x16x32_bf16 v[144:147], v[246:249], v[24:27], v[144:147]\n"
"s_waitcnt lgkmcnt(1)\n"
"ds_read_b128 v[136:139], v251 offset:1376\n"
"v_max3_f32 v174, v174, v252, v253\n"
"v_max3_f32 v174, v174, v254, v255\n"
"v_mfma_f32_16x16x32_bf16 v[152:155], v[202:205], v[132:135], v[152:155]\n"
"v_mfma_f32_16x16x32_bf16 v[152:155], v[204:207], v[24:27], v[152:155]\n"
"s_waitcnt lgkmcnt(1)\n"
"ds_read_b128 v[148:151], v177 offset:2736\n"
"v_max3_f32 v174, v174, v144, v145\n"
"v_max3_f32 v174, v174, v146, v147\n"
"v_mfma_f32_16x16x32_bf16 v[140:143], v[232:235], v[132:135], v[140:143]\n"
"v_mfma_f32_16x16x32_bf16 v[140:143], v[234:237], v[24:27], v[140:143]\n"
"s_waitcnt lgkmcnt(1)\n"
"ds_read_b128 v[252:255], v177 offset:2704\n"
"v_max3_f32 v174, v174, v152, v153\n"
"v_max3_f32 v174, v174, v154, v155\n"
"v_mfma_f32_16x16x32_bf16 v[136:139], v[238:241], v[132:135], v[136:139]\n"
"v_mfma_f32_16x16x32_bf16 v[136:139], v[240:243], v[24:27], v[136:139]\n"
"s_waitcnt lgkmcnt(1)\n"
"ds_read_b128 v[144:147], v177 offset:2720\n"
"v_max3_f32 v174, v174, v140, v141\n"
"v_max3_f32 v174, v174, v142, v143\n"
"v_mfma_f32_16x16x32_bf16 v[148:151], v[196:199], v[132:135], v[148:151]\n"
"v_mfma_f32_16x16x32_bf16 v[148:151], v[198:201], v[24:27], v[148:151]\n"
"s_waitcnt lgkmcnt(1)\n"
"ds_read_b128 v[152:155], v176 offset:32\n"
"v_max3_f32 v174, v174, v136, v137\n"
"v_max3_f32 v174, v174, v138, v139\n"
"v_mfma_f32_16x16x32_bf16 v[252:255], v[184:187], v[132:135], v[252:255]\n"
"v_mfma_f32_16x16x32_bf16 v[252:255], v[186:189], v[24:27], v[252:255]\n"
"ds_read_b128 v[140:143], v176 offset:48\n"
"s_nop 0\n"
"v_max3_f32 v174, v174, v148, v149\n"
"v_max3_f32 v174, v174, v150, v151\n"
"v_mov_b32 v175, v174\n"
"s_nop 1\n"
"v_permlane16_swap_b32 v174, v175\n"
"v_max_f32 v174, v174, v175\n"
"v_mov_b32 v175, v174\n"
"s_nop 1\n"
"v_permlane32_swap_b32 v174, v175\n"
"v_max_f32 v174, v174, v175\n"
"v_sub_f32 v252, v252, v174\n"
"v_exp_f32 v252, v252\n"
"v_sub_f32 v253, v253, v174\n"
"v_exp_f32 v253, v253\n"
"v_sub_f32 v254, v254, v174\n"
"v_exp_f32 v254, v254\n"
"v_sub_f32 v255, v255, v174\n"
"v_exp_f32 v255, v255\n"
"s_waitcnt lgkmcnt(2)\n"
"v_mfma_f32_16x16x32_bf16 v[144:147], v[190:193], v[132:135], v[144:147]\n"
"v_cvt_pk_bf16_f32 v252, v252, v253\n"
"v_cvt_pk_bf16_f32 v253, v254, v255\n"
"v_mfma_f32_16x16x32_bf16 v[144:147], v[192:195], v[24:27], v[144:147]\n"
"ds_write_addtid_b32 v252 offset:53248\n"
"ds_read_b128 v[136:139], v251 offset:1392\n"
"ds_write_addtid_b32 v253 offset:53504\n"
"s_waitcnt lgkmcnt(4)\n"
"v_mfma_f32_16x16x32_bf16 v[152:155], v[208:211], v[132:135], v[152:155]\n"
"v_mfma_f32_16x16x32_bf16 v[152:155], v[210:213], v[24:27], v[152:155]\n"
"ds_read_b128 v[148:151], v176 offset:16\n"
"s_nop 0\n"
"v_sub_f32 v144, v144, v174\n"
"v_exp_f32 v144, v144\n"
"v_sub_f32 v145, v145, v174\n"
"v_exp_f32 v145, v145\n"
"v_sub_f32 v146, v146, v174\n"
"v_exp_f32 v146, v146\n"
"v_sub_f32 v147, v147, v174\n"
"v_exp_f32 v147, v147\n"
"v_sub_f32 v152, v152, v174\n"
"v_exp_f32 v152, v152\n"
"v_sub_f32 v153, v153, v174\n"
"v_exp_f32 v153, v153\n"
"v_sub_f32 v154, v154, v174\n"
"v_exp_f32 v154, v154\n"
"v_sub_f32 v155, v155, v174\n"
"v_exp_f32 v155, v155\n"
"v_cvt_pk_bf16_f32 v144, v144, v145\n"
"v_cvt_pk_bf16_f32 v145, v146, v147\n"
"v_cvt_pk_bf16_f32 v152, v152, v153\n"
"v_cvt_pk_bf16_f32 v153, v154, v155\n"
"ds_write_addtid_b32 v144 offset:53760\n"
"ds_write_addtid_b32 v145 offset:54016\n"
"s_waitcnt lgkmcnt(6)\n"
"v_mfma_f32_16x16x32_bf16 v[140:143], v[214:217], v[132:135], v[140:143]\n"
"ds_write_addtid_b32 v152 offset:54272\n"
"v_mfma_f32_16x16x32_bf16 v[140:143], v[216:219], v[24:27], v[140:143]\n"
"ds_read_b128 v[252:255], v251 offset:1360\n"
"ds_write_addtid_b32 v153 offset:54528\n"
"s_waitcnt lgkmcnt(7)\n"
"v_mfma_f32_16x16x32_bf16 v[136:139], v[244:247], v[132:135], v[136:139]\n"
"v_mfma_f32_16x16x32_bf16 v[136:139], v[246:249], v[24:27], v[136:139]\n"
"ds_read_b128 v[144:147], v251 offset:1376\n"
"s_nop 1\n"
"v_sub_f32 v140, v140, v174\n"
"v_exp_f32 v140, v140\n"
"v_sub_f32 v141, v141, v174\n"
"v_exp_f32 v141, v141\n"
"v_sub_f32 v142, v142, v174\n"
"v_exp_f32 v142, v142\n"
"v_sub_f32 v143, v143, v174\n"
"v_exp_f32 v143, v143\n"
"v_sub_f32 v136, v136, v174\n"
"v_exp_f32 v136, v136\n"
"v_sub_f32 v137, v137, v174\n"
"v_exp_f32 v137, v137\n"
"v_sub_f32 v138, v138, v174\n"
"v_exp_f32 v138, v138\n"
"v_sub_f32 v139, v139, v174\n"
"v_exp_f32 v139, v139\n"
"v_cvt_pk_bf16_f32 v140, v140, v141\n"
"v_cvt_pk_bf16_f32 v136, v136, v137\n"
"v_cvt_pk_bf16_f32 v141, v142, v143\n"
"v_cvt_pk_bf16_f32 v137, v138, v139\n"
"ds_write_addtid_b32 v140 offset:54784\n"
"ds_write_addtid_b32 v136 offset:33792\n"
"ds_write_addtid_b32 v141 offset:55040\n"
"s_waitcnt lgkmcnt(9)\n"
"v_mfma_f32_16x16x32_bf16 v[148:151], v[202:205], v[132:135], v[148:151]\n"
"ds_write_addtid_b32 v137 offset:34048\n"
"v_mfma_f32_16x16x32_bf16 v[148:151], v[204:207], v[24:27], v[148:151]\n"
"s_waitcnt lgkmcnt(6)\n"
"ds_read_b128 v[152:155], v177 offset:2736\n"
"v_mfma_f32_16x16x32_bf16 v[252:255], v[232:235], v[132:135], v[252:255]\n"
"v_mfma_f32_16x16x32_bf16 v[252:255], v[234:237], v[24:27], v[252:255]\n"
"s_waitcnt lgkmcnt(1)\n"
"ds_read_b128 v[140:143], v4 offset:12288\n"
"s_barrier\n"
"s_nop 0\n"
"v_sub_f32 v148, v148, v174\n"
"v_exp_f32 v148, v148\n"
"v_sub_f32 v149, v149, v174\n"
"v_exp_f32 v149, v149\n"
"v_sub_f32 v150, v150, v174\n"
"v_exp_f32 v150, v150\n"
"v_sub_f32 v151, v151, v174\n"
"v_exp_f32 v151, v151\n"
"v_sub_f32 v252, v252, v174\n"
"v_exp_f32 v252, v252\n"
"v_sub_f32 v253, v253, v174\n"
"v_exp_f32 v253, v253\n"
"v_cvt_pk_bf16_f32 v148, v148, v149\n"
"v_cvt_pk_bf16_f32 v149, v150, v151\n"
"v_sub_f32 v254, v254, v174\n"
"v_exp_f32 v254, v254\n"
"v_sub_f32 v255, v255, v174\n"
"v_exp_f32 v255, v255\n"
"v_cvt_pk_bf16_f32 v252, v252, v253\n"
"v_cvt_pk_bf16_f32 v253, v254, v255\n"
"ds_write_addtid_b32 v148 offset:57856\n"
"ds_write_addtid_b32 v149 offset:58112\n"
"v_mfma_f32_16x16x32_bf16 v[144:147], v[238:241], v[132:135], v[144:147]\n"
"ds_write_addtid_b32 v252 offset:58368\n"
"v_mfma_f32_16x16x32_bf16 v[144:147], v[240:243], v[24:27], v[144:147]\n"
"ds_write_addtid_b32 v253 offset:58624\n"
"s_waitcnt lgkmcnt(5)\n"
"v_mfma_f32_16x16x32_bf16 v[152:155], v[196:199], v[132:135], v[152:155]\n"
"ds_read_b128 v[202:205], v5 offset:12288\n"
"v_mfma_f32_16x16x32_bf16 v[152:155], v[198:201], v[24:27], v[152:155]\n"
"s_waitcnt lgkmcnt(5)\n"
"v_mfma_f32_16x16x32_bf16 v[136:139], v[84:87], v[140:143], 0\n"
"ds_read_b128 v[148:151], v6 offset:5120\n"
"v_mfma_f32_16x16x32_bf16 v[156:159], v[88:91], v[140:143], 0\n"
"ds_read_b128 v[250:253], v4 offset:14336\n"
"v_sub_f32 v144, v144, v174\n"
"v_exp_f32 v144, v144\n"
"v_sub_f32 v145, v145, v174\n"
"v_exp_f32 v145, v145\n"
"v_sub_f32 v146, v146, v174\n"
"v_exp_f32 v146, v146\n"
"v_sub_f32 v147, v147, v174\n"
"v_exp_f32 v147, v147\n"
"v_mfma_f32_16x16x32_bf16 v[132:135], v[92:95], v[140:143], 0\n"
"s_waitcnt lgkmcnt(2)\n"
"v_cvt_pk_bf16_f32 v144, v144, v145\n"
"v_cvt_pk_bf16_f32 v145, v146, v147\n"
"v_mfma_f32_16x16x32_bf16 v[136:139], v[96:99], v[202:205], v[136:139]\n"
"ds_write_addtid_b32 v144 offset:58880\n"
"v_mfma_f32_16x16x32_bf16 v[156:159], v[100:103], v[202:205], v[156:159]\n"
"ds_write_addtid_b32 v145 offset:59136\n"
"s_waitcnt lgkmcnt(3)\n"
"v_mfma_f32_16x16x32_bf16 v[132:135], v[148:151], v[202:205], v[132:135]\n"
"s_waitcnt lgkmcnt(2)\n"
"ds_read_b128 v[140:143], v6 offset:6144\n"
"v_sub_f32 v152, v152, v174\n"
"v_exp_f32 v152, v152\n"
"v_mfma_f32_16x16x32_bf16 v[136:139], v[104:107], v[250:253], v[136:139]\n"
"ds_read_b128 v[160:163], v5 offset:14336\n"
"v_sub_f32 v153, v153, v174\n"
"v_exp_f32 v153, v153\n"
"v_mfma_f32_16x16x32_bf16 v[156:159], v[108:111], v[250:253], v[156:159]\n"
"ds_read_b128 v[202:205], v6 offset:7168\n"
"v_sub_f32 v154, v154, v174\n"
"v_exp_f32 v154, v154\n"
"v_sub_f32 v155, v155, v174\n"
"v_exp_f32 v155, v155\n"
"v_cvt_pk_bf16_f32 v152, v152, v153\n"
"s_waitcnt lgkmcnt(2)\n"
"v_mfma_f32_16x16x32_bf16 v[132:135], v[140:143], v[250:253], v[132:135]\n"
"s_waitcnt lgkmcnt(1)\n"
"ds_read_b128 v[144:147], v4 offset:16384\n"
"v_cvt_pk_bf16_f32 v153, v154, v155\n"
"v_mfma_f32_16x16x32_bf16 v[136:139], v[112:115], v[160:163], v[136:139]\n"
"ds_write_addtid_b32 v152 offset:57344\n"
"v_mfma_f32_16x16x32_bf16 v[156:159], v[116:119], v[160:163], v[156:159]\n"
"ds_write_addtid_b32 v153 offset:57600\n"
"s_waitcnt lgkmcnt(3)\n"
"v_mfma_f32_16x16x32_bf16 v[132:135], v[202:205], v[160:163], v[132:135]\n"
"s_waitcnt lgkmcnt(2)\n"
"ds_read_b128 v[140:143], v6 offset:8192\n"
"ds_read_b128 v[148:151], v5 offset:16384\n"
"v_mfma_f32_16x16x32_bf16 v[136:139], v[0:3], v[144:147], v[136:139]\n"
"ds_read_b128 v[164:167], v6 offset:9216\n"
"v_mfma_f32_16x16x32_bf16 v[156:159], v[120:123], v[144:147], v[156:159]\n"
"s_waitcnt lgkmcnt(3)\n"
"s_barrier\n"
"s_waitcnt lgkmcnt(2)\n"
"v_mfma_f32_16x16x32_bf16 v[132:135], v[140:143], v[144:147], v[132:135]\n"
"s_waitcnt lgkmcnt(1)\n"
"ds_read_b128 v[202:205], v4 offset:18432\n"
"v_mfma_f32_16x16x32_bf16 v[136:139], v[124:127], v[148:151], v[136:139]\n"
"v_mfma_f32_16x16x32_bf16 v[156:159], v[128:131], v[148:151], v[156:159]\n"
"s_waitcnt lgkmcnt(1)\n"
"v_mfma_f32_16x16x32_bf16 v[132:135], v[164:167], v[148:151], v[132:135]\n"
"s_waitcnt lgkmcnt(0)\n"
"ds_read_b128 v[152:155], v5 offset:18432\n"
"v_mfma_f32_16x16x32_bf16 v[250:253], v[8:11], v[202:205], 0\n"
"v_mfma_f32_16x16x32_bf16 v[140:143], v[12:15], v[202:205], 0\n"
"v_mfma_f32_16x16x32_bf16 v[144:147], v[16:19], v[202:205], 0\n"
"s_waitcnt lgkmcnt(0)\n"
"ds_read_b128 v[160:163], v4 offset:20480\n"
"v_mfma_f32_16x16x32_bf16 v[250:253], v[20:23], v[152:155], v[250:253]\n"
"v_mul_f32 v136, s60, v136\n"
"v_mul_f32 v137, s60, v137\n"
"v_mfma_f32_16x16x32_bf16 v[140:143], v[28:31], v[152:155], v[140:143]\n"
"v_cvt_pk_bf16_f32 v136, v136, v137\n"
"v_mul_f32 v138, s60, v138\n"
"v_mfma_f32_16x16x32_bf16 v[144:147], v[32:35], v[152:155], v[144:147]\n"
"ds_read_b128 v[202:205], v5 offset:20480\n"
"v_mul_f32 v139, s60, v139\n"
"v_cvt_pk_bf16_f32 v137, v138, v139\n"
"v_mul_f32 v156, s60, v156\n"
"v_mul_f32 v157, s60, v157\n"
"v_mul_f32 v158, s60, v158\n"
"v_mul_f32 v159, s60, v159\n"
"v_cvt_pk_bf16_f32 v138, v156, v157\n"
"s_waitcnt lgkmcnt(1)\n"
"v_mfma_f32_16x16x32_bf16 v[250:253], v[36:39], v[160:163], v[250:253]\n"
"v_cvt_pk_bf16_f32 v139, v158, v159\n"
"v_mul_f32 v132, s60, v132\n"
"v_mfma_f32_16x16x32_bf16 v[140:143], v[40:43], v[160:163], v[140:143]\n"
"v_mul_f32 v133, s60, v133\n"
"v_mul_f32 v134, s60, v134\n"
"v_mfma_f32_16x16x32_bf16 v[144:147], v[44:47], v[160:163], v[144:147]\n"
"s_waitcnt lgkmcnt(0)\n"
"ds_read_b128 v[148:151], v4 offset:22528\n"
"v_mul_f32 v135, s60, v135\n"
"v_mfma_f32_16x16x32_bf16 v[250:253], v[48:51], v[202:205], v[250:253]\n"
"v_cvt_pk_bf16_f32 v26, v132, v133\n"
"v_cvt_pk_bf16_f32 v27, v134, v135\n"
"v_mfma_f32_16x16x32_bf16 v[140:143], v[52:55], v[202:205], v[140:143]\n"
"v_mfma_f32_16x16x32_bf16 v[144:147], v[56:59], v[202:205], v[144:147]\n"
"s_waitcnt lgkmcnt(0)\n"
"ds_read_b128 v[152:155], v5 offset:22528\n"
"v_mfma_f32_16x16x32_bf16 v[250:253], v[60:63], v[148:151], v[250:253]\n"
"v_mfma_f32_16x16x32_bf16 v[140:143], v[64:67], v[148:151], v[140:143]\n"
"v_mfma_f32_16x16x32_bf16 v[144:147], v[68:71], v[148:151], v[144:147]\n"
"s_waitcnt lgkmcnt(0)\n"
"ds_read_b128 v[132:135], v4 offset:36864\n"
"v_mfma_f32_16x16x32_bf16 v[250:253], v[72:75], v[152:155], v[250:253]\n"
"v_mfma_f32_16x16x32_bf16 v[140:143], v[76:79], v[152:155], v[140:143]\n"
"v_mfma_f32_16x16x32_bf16 v[144:147], v[80:83], v[152:155], v[144:147]\n"
"s_waitcnt lgkmcnt(0)\n"
"ds_read_b128 v[156:159], v5 offset:36864\n"
"v_mfma_f32_16x16x32_bf16 v[202:205], v[8:11], v[132:135], 0\n"
"v_mfma_f32_16x16x32_bf16 v[148:151], v[12:15], v[132:135], 0\n"
"v_mfma_f32_16x16x32_bf16 v[160:163], v[16:19], v[132:135], 0\n"
"s_waitcnt lgkmcnt(0)\n"
"ds_read_b128 v[164:167], v4 offset:38912\n"
"v_mfma_f32_16x16x32_bf16 v[202:205], v[20:23], v[156:159], v[202:205]\n"
"v_cvt_pk_bf16_f32 v250, v250, v251\n"
"v_cvt_pk_bf16_f32 v251, v252, v253\n"
"v_cvt_pk_bf16_f32 v252, v140, v141\n"
"v_cvt_pk_bf16_f32 v253, v142, v143\n"
"v_cvt_pk_bf16_f32 v254, v144, v145\n"
"v_cvt_pk_bf16_f32 v255, v146, v147\n"
"v_mfma_f32_16x16x32_bf16 v[148:151], v[28:31], v[156:159], v[148:151]\n"
"v_mfma_f32_16x16x32_bf16 v[160:163], v[32:35], v[156:159], v[160:163]\n"
"s_waitcnt lgkmcnt(0)\n"
"ds_read_b128 v[132:135], v5 offset:38912\n"
"v_mfma_f32_16x16x32_bf16 v[202:205], v[36:39], v[164:167], v[202:205]\n"
"v_bfe_u32 v177, v6, 2, 6\n"
"v_mfma_f32_16x16x32_bf16 v[148:151], v[40:43], v[164:167], v[148:151]\n"
"ds_read_b32 v177, v177 offset:1408\n"
"v_mfma_f32_16x16x32_bf16 v[160:163], v[44:47], v[164:167], v[160:163]\n"
"s_add_u32 s70, s50, 4294967264\n"
"ds_read_b128 v[140:143], v4 offset:40960\n"
"s_waitcnt lgkmcnt(2)\n"
"v_mfma_f32_16x16x32_bf16 v[202:205], v[48:51], v[132:135], v[202:205]\n"
"v_mfma_f32_16x16x32_bf16 v[148:151], v[52:55], v[132:135], v[148:151]\n"
"v_mfma_f32_16x16x32_bf16 v[160:163], v[56:59], v[132:135], v[160:163]\n"
"s_waitcnt lgkmcnt(1)\n"
"ds_read_b128 v[144:147], v5 offset:40960\n"
"v_add_u32_sdwa v176, v177, v7 dst_sel:DWORD dst_unused:UNUSED_PAD src0_sel:WORD_0 src1_sel:DWORD\n"
"s_nop 0\n"
"v_max_u32 v175, s70, v176\n"
"v_add_u32_sdwa v177, sext(v177), v176 dst_sel:DWORD dst_unused:UNUSED_PAD src0_sel:WORD_1 src1_sel:DWORD\n"
"s_waitcnt lgkmcnt(1)\n"
"v_mfma_f32_16x16x32_bf16 v[202:205], v[60:63], v[140:143], v[202:205]\n"
"s_add_u32 s72, s50, 4294966960\n"
"v_max_u32 v174, s70, v177\n"
"v_mfma_f32_16x16x32_bf16 v[148:151], v[64:67], v[140:143], v[148:151]\n"
"v_mfma_f32_16x16x32_bf16 v[160:163], v[68:71], v[140:143], v[160:163]\n"
"s_waitcnt lgkmcnt(0)\n"
"ds_read_b128 v[132:135], v175 offset:48\n"
"v_min_u32 v173, s72, v176\n"
"v_mfma_f32_16x16x32_bf16 v[202:205], v[72:75], v[144:147], v[202:205]\n"
"v_mfma_f32_16x16x32_bf16 v[148:151], v[76:79], v[144:147], v[148:151]\n"
"ds_read_b128 v[152:155], v174 offset:64\n"
"v_mfma_f32_16x16x32_bf16 v[160:163], v[80:83], v[144:147], v[160:163]\n"
"s_waitcnt lgkmcnt(1)\n"
"v_mfma_f32_16x16x32_bf16 v[132:135], v[214:217], v[136:139], v[132:135]\n"
"s_add_u32 s71, s50, 1008\n"
"ds_read_b128 v[140:143], v173 offset:2720\n"
"v_mfma_f32_16x16x32_bf16 v[132:135], v[216:219], v[24:27], v[132:135]\n"
"s_waitcnt lgkmcnt(1)\n"
"v_mfma_f32_16x16x32_bf16 v[152:155], v[220:223], v[136:139], v[152:155]\n"
"ds_read_b128 v[156:159], v173 offset:2736\n"
"v_cvt_pk_bf16_f32 v202, v202, v203\n"
"v_cvt_pk_bf16_f32 v203, v204, v205\n"
"v_cvt_pk_bf16_f32 v204, v148, v149\n"
"v_cvt_pk_bf16_f32 v205, v150, v151\n"
"v_cvt_pk_bf16_f32 v206, v160, v161\n"
"v_cvt_pk_bf16_f32 v207, v162, v163\n"
"v_mfma_f32_16x16x32_bf16 v[152:155], v[222:225], v[24:27], v[152:155]\n"
"s_waitcnt lgkmcnt(1)\n"
"v_max3_f32 v170, v132, v133, v134\n"
"v_max_f32 v170, v170, v135\n"
"v_mfma_f32_16x16x32_bf16 v[140:143], v[190:193], v[136:139], v[140:143]\n"
"ds_read_b128 v[144:147], v175 offset:32\n"
"v_min_u32 v169, s71, v176\n"
"v_mfma_f32_16x16x32_bf16 v[140:143], v[192:195], v[24:27], v[140:143]\n"
"s_waitcnt lgkmcnt(1)\n"
"v_max3_f32 v170, v170, v152, v153\n"
"v_max3_f32 v170, v170, v154, v155\n"
"v_mfma_f32_16x16x32_bf16 v[156:159], v[196:199], v[136:139], v[156:159]\n"
"ds_read_b128 v[132:135], v169 offset:1376\n"
"v_min_u32 v168, s71, v177\n"
"v_mfma_f32_16x16x32_bf16 v[156:159], v[198:201], v[24:27], v[156:159]\n"
"s_waitcnt lgkmcnt(1)\n"
"v_max3_f32 v170, v170, v140, v141\n"
"v_max3_f32 v170, v170, v142, v143\n"
"v_mfma_f32_16x16x32_bf16 v[144:147], v[208:211], v[136:139], v[144:147]\n"
"ds_read_b128 v[148:151], v169 offset:1392\n"
"v_min_u32 v176, s72, v177\n"
"v_mfma_f32_16x16x32_bf16 v[144:147], v[210:213], v[24:27], v[144:147]\n"
"s_waitcnt lgkmcnt(1)\n"
"v_max3_f32 v170, v170, v156, v157\n"
"v_max3_f32 v170, v170, v158, v159\n"
"v_mfma_f32_16x16x32_bf16 v[132:135], v[238:241], v[136:139], v[132:135]\n"
"ds_read_b128 v[140:143], v168 offset:1408\n"
"v_mfma_f32_16x16x32_bf16 v[132:135], v[240:243], v[24:27], v[132:135]\n"
"s_waitcnt lgkmcnt(1)\n"
"s_nop 0\n"
"v_max3_f32 v170, v170, v144, v145\n"
"v_max3_f32 v170, v170, v146, v147\n"
"v_mfma_f32_16x16x32_bf16 v[148:151], v[244:247], v[136:139], v[148:151]\n"
"ds_read_b128 v[152:155], v176 offset:2752\n"
"v_mfma_f32_16x16x32_bf16 v[148:151], v[246:249], v[24:27], v[148:151]\n"
"s_waitcnt lgkmcnt(1)\n"
"v_max3_f32 v170, v170, v132, v133\n"
"v_max3_f32 v170, v170, v134, v135\n"
"v_mfma_f32_16x16x32_bf16 v[140:143], v[250:253], v[136:139], v[140:143]\n"
"ds_read_b128 v[144:147], v175 offset:48\n"
"v_mfma_f32_16x16x32_bf16 v[140:143], v[252:255], v[24:27], v[140:143]\n"
"s_waitcnt lgkmcnt(1)\n"
"s_nop 0\n"
"v_max3_f32 v170, v170, v148, v149\n"
"v_max3_f32 v170, v170, v150, v151\n"
"v_mfma_f32_16x16x32_bf16 v[152:155], v[202:205], v[136:139], v[152:155]\n"
"ds_read_b128 v[132:135], v174 offset:64\n"
"v_mfma_f32_16x16x32_bf16 v[152:155], v[204:207], v[24:27], v[152:155]\n"
"s_waitcnt lgkmcnt(1)\n"
"v_max3_f32 v170, v170, v140, v141\n"
"v_max3_f32 v170, v170, v142, v143\n"
"v_mfma_f32_16x16x32_bf16 v[144:147], v[214:217], v[136:139], v[144:147]\n"
"ds_read_b128 v[148:151], v173 offset:2720\n"
"v_mfma_f32_16x16x32_bf16 v[144:147], v[216:219], v[24:27], v[144:147]\n"
"s_nop 1\n"
"v_max3_f32 v170, v170, v152, v153\n"
"v_max3_f32 v170, v170, v154, v155\n"
"v_mov_b32 v171, v170\n"
"s_nop 1\n"
"v_permlane16_swap_b32 v170, v171\n"
"v_max_f32 v170, v170, v171\n"
"v_mov_b32 v171, v170\n"
"s_nop 1\n"
"v_permlane32_swap_b32 v170, v171\n"
"v_max_f32 v170, v170, v171\n"
"v_sub_f32 v144, v144, v170\n"
"v_exp_f32 v144, v144\n"
"v_sub_f32 v145, v145, v170\n"
"v_exp_f32 v145, v145\n"
"v_sub_f32 v146, v146, v170\n"
"v_exp_f32 v146, v146\n"
"v_sub_f32 v147, v147, v170\n"
"v_exp_f32 v147, v147\n"
"s_waitcnt lgkmcnt(1)\n"
"v_mfma_f32_16x16x32_bf16 v[132:135], v[220:223], v[136:139], v[132:135]\n"
"ds_read_b128 v[214:217], v173 offset:2736\n"
"v_cvt_pk_bf16_f32 v144, v144, v145\n"
"v_mfma_f32_16x16x32_bf16 v[132:135], v[222:225], v[24:27], v[132:135]\n"
"ds_write_addtid_b32 v144 offset:55296\n"
"v_cvt_pk_bf16_f32 v145, v146, v147\n"
"ds_write_addtid_b32 v145 offset:55552\n"
"s_waitcnt lgkmcnt(3)\n"
"v_mfma_f32_16x16x32_bf16 v[148:151], v[190:193], v[136:139], v[148:151]\n"
"ds_read_b128 v[218:221], v175 offset:32\n"
"v_mfma_f32_16x16x32_bf16 v[148:151], v[192:195], v[24:27], v[148:151]\n"
"s_nop 0\n"
"v_sub_f32 v132, v132, v170\n"
"v_exp_f32 v132, v132\n"
"v_sub_f32 v133, v133, v170\n"
"v_exp_f32 v133, v133\n"
"v_sub_f32 v134, v134, v170\n"
"v_exp_f32 v134, v134\n"
"v_sub_f32 v135, v135, v170\n"
"v_exp_f32 v135, v135\n"
"v_sub_f32 v150, v150, v170\n"
"v_exp_f32 v150, v150\n"
"v_sub_f32 v151, v151, v170\n"
"v_exp_f32 v151, v151\n"
"v_cvt_pk_bf16_f32 v132, v132, v133\n"
"v_cvt_pk_bf16_f32 v133, v134, v135\n"
"v_cvt_pk_bf16_f32 v149, v150, v151\n"
"ds_write_addtid_b32 v132 offset:55808\n"
"ds_write_addtid_b32 v133 offset:56064\n"
"s_waitcnt lgkmcnt(5)\n"
"v_mfma_f32_16x16x32_bf16 v[214:217], v[196:199], v[136:139], v[214:217]\n"
"ds_read_b128 v[222:225], v169 offset:1376\n"
"v_mfma_f32_16x16x32_bf16 v[214:217], v[198:201], v[24:27], v[214:217]\n"
"ds_write_addtid_b32 v149 offset:56576\n"
"s_waitcnt lgkmcnt(4)\n"
"v_mfma_f32_16x16x32_bf16 v[218:221], v[208:211], v[136:139], v[218:221]\n"
"ds_read_b128 v[140:143], v169 offset:1392\n"
"v_mfma_f32_16x16x32_bf16 v[218:221], v[210:213], v[24:27], v[218:221]\n"
"s_nop 2\n"
"v_sub_f32 v214, v214, v170\n"
"v_exp_f32 v214, v214\n"
"v_sub_f32 v215, v215, v170\n"
"v_exp_f32 v215, v215\n"
"v_sub_f32 v216, v216, v170\n"
"v_exp_f32 v216, v216\n"
"v_sub_f32 v217, v217, v170\n"
"v_exp_f32 v217, v217\n"
"v_cvt_pk_bf16_f32 v214, v214, v215\n"
"v_cvt_pk_bf16_f32 v215, v216, v217\n"
"v_sub_f32 v220, v220, v170\n"
"v_exp_f32 v220, v220\n"
"v_sub_f32 v221, v221, v170\n"
"v_exp_f32 v221, v221\n"
"ds_write_addtid_b32 v214 offset:56832\n"
"v_cvt_pk_bf16_f32 v219, v220, v221\n"
"ds_write_addtid_b32 v215 offset:57088\n"
"s_waitcnt lgkmcnt(4)\n"
"v_mfma_f32_16x16x32_bf16 v[222:225], v[238:241], v[136:139], v[222:225]\n"
"ds_read_b128 v[208:211], v168 offset:1408\n"
"v_mfma_f32_16x16x32_bf16 v[222:225], v[240:243], v[24:27], v[222:225]\n"
"s_waitcnt lgkmcnt(1)\n"
"s_barrier\n"
"ds_write_addtid_b32 v219 offset:58112\n"
"v_mfma_f32_16x16x32_bf16 v[140:143], v[244:247], v[136:139], v[140:143]\n"
"ds_read_b128 v[212:215], v176 offset:2752\n"
"v_mfma_f32_16x16x32_bf16 v[140:143], v[246:249], v[24:27], v[140:143]\n"
"s_nop 1\n"
"v_sub_f32 v224, v224, v170\n"
"v_exp_f32 v224, v224\n"
"v_sub_f32 v225, v225, v170\n"
"v_exp_f32 v225, v225\n"
"s_nop 0\n"
"v_cvt_pk_bf16_f32 v223, v224, v225\n"
"ds_write_addtid_b32 v223 offset:58624\n"
"s_waitcnt lgkmcnt(3)\n"
"v_mfma_f32_16x16x32_bf16 v[208:211], v[250:253], v[136:139], v[208:211]\n"
"ds_read_b128 v[216:219], v4 offset:43008\n"
"v_sub_f32 v140, v140, v170\n"
"v_exp_f32 v140, v140\n"
"v_sub_f32 v141, v141, v170\n"
"v_exp_f32 v141, v141\n"
"v_sub_f32 v142, v142, v170\n"
"v_exp_f32 v142, v142\n"
"v_sub_f32 v143, v143, v170\n"
"v_exp_f32 v143, v143\n"
"v_mfma_f32_16x16x32_bf16 v[208:211], v[252:255], v[24:27], v[208:211]\n"
"v_cvt_pk_bf16_f32 v140, v140, v141\n"
"v_cvt_pk_bf16_f32 v141, v142, v143\n"
"ds_write_addtid_b32 v140 offset:58880\n"
"ds_write_addtid_b32 v141 offset:59136\n"
"s_waitcnt lgkmcnt(4)\n"
"v_mfma_f32_16x16x32_bf16 v[212:215], v[202:205], v[136:139], v[212:215]\n"
"v_mfma_f32_16x16x32_bf16 v[212:215], v[204:207], v[24:27], v[212:215]\n"
"s_waitcnt lgkmcnt(2)\n"
"ds_read_b128 v[220:223], v5 offset:43008\n"
"v_mfma_f32_16x16x32_bf16 v[132:135], v[84:87], v[216:219], 0\n"
"v_sub_f32 v208, v208, v170\n"
"v_exp_f32 v208, v208\n"
"v_sub_f32 v209, v209, v170\n"
"v_exp_f32 v209, v209\n"
"v_mfma_f32_16x16x32_bf16 v[136:139], v[88:91], v[216:219], 0\n"
"ds_read_b128 v[140:143], v6 offset:5120\n"
"v_cvt_pk_bf16_f32 v208, v208, v209\n"
"v_sub_f32 v210, v210, v170\n"
"v_exp_f32 v210, v210\n"
"v_sub_f32 v211, v211, v170\n"
"v_exp_f32 v211, v211\n"
"v_mfma_f32_16x16x32_bf16 v[144:147], v[92:95], v[216:219], 0\n"
"s_waitcnt lgkmcnt(1)\n"
"ds_read_b128 v[148:151], v4 offset:45056\n"
"v_cvt_pk_bf16_f32 v209, v210, v211\n"
"v_mfma_f32_16x16x32_bf16 v[132:135], v[96:99], v[220:223], v[132:135]\n"
"ds_write_addtid_b32 v208 offset:34304\n"
"v_mfma_f32_16x16x32_bf16 v[136:139], v[100:103], v[220:223], v[136:139]\n"
"ds_write_addtid_b32 v209 offset:34560\n"
"s_waitcnt lgkmcnt(3)\n"
"v_mfma_f32_16x16x32_bf16 v[144:147], v[140:143], v[220:223], v[144:147]\n"
"ds_read_b128 v[216:219], v6 offset:6144\n"
"v_sub_f32 v212, v212, v170\n"
"v_exp_f32 v212, v212\n"
"v_sub_f32 v213, v213, v170\n"
"v_exp_f32 v213, v213\n"
"s_waitcnt lgkmcnt(3)\n"
"v_mfma_f32_16x16x32_bf16 v[132:135], v[104:107], v[148:151], v[132:135]\n"
"ds_read_b128 v[208:211], v5 offset:45056\n"
"v_cvt_pk_bf16_f32 v212, v212, v213\n"
"v_mfma_f32_16x16x32_bf16 v[136:139], v[108:111], v[148:151], v[136:139]\n"
"ds_read_b128 v[220:223], v6 offset:7168\n"
"v_sub_f32 v214, v214, v170\n"
"v_exp_f32 v214, v214\n"
"v_sub_f32 v215, v215, v170\n"
"v_exp_f32 v215, v215\n"
"s_waitcnt lgkmcnt(2)\n"
"v_mfma_f32_16x16x32_bf16 v[144:147], v[216:219], v[148:151], v[144:147]\n"
"ds_read_b128 v[140:143], v4 offset:47104\n"
"v_cvt_pk_bf16_f32 v213, v214, v215\n"
"ds_write_addtid_b32 v212 offset:57344\n"
"ds_write_addtid_b32 v213 offset:57600\n"
"s_waitcnt lgkmcnt(4)\n"
"v_mfma_f32_16x16x32_bf16 v[132:135], v[112:115], v[208:211], v[132:135]\n"
"v_mfma_f32_16x16x32_bf16 v[136:139], v[116:119], v[208:211], v[136:139]\n"
"s_waitcnt lgkmcnt(3)\n"
"ds_read_b128 v[216:219], v6 offset:8192\n"
"v_mfma_f32_16x16x32_bf16 v[144:147], v[220:223], v[208:211], v[144:147]\n"
"s_waitcnt lgkmcnt(3)\n"
"ds_read_b128 v[148:151], v5 offset:47104\n"
"v_mfma_f32_16x16x32_bf16 v[132:135], v[0:3], v[140:143], v[132:135]\n"
"s_waitcnt lgkmcnt(2)\n"
"s_barrier\n"
"v_mfma_f32_16x16x32_bf16 v[136:139], v[120:123], v[140:143], v[136:139]\n"
"s_waitcnt lgkmcnt(1)\n"
"ds_read_b128 v[212:215], v6 offset:9216\n"
"v_mfma_f32_16x16x32_bf16 v[144:147], v[216:219], v[140:143], v[144:147]\n"
"s_waitcnt lgkmcnt(1)\n"
"ds_read_b128 v[220:223], v4 offset:24576\n"
"v_mfma_f32_16x16x32_bf16 v[132:135], v[124:127], v[148:151], v[132:135]\n"
"v_mfma_f32_16x16x32_bf16 v[136:139], v[128:131], v[148:151], v[136:139]\n"
"s_waitcnt lgkmcnt(1)\n"
"v_mfma_f32_16x16x32_bf16 v[144:147], v[212:215], v[148:151], v[144:147]\n"
"s_waitcnt lgkmcnt(0)\n"
"ds_read_b128 v[152:155], v5 offset:24576\n"
"v_mfma_f32_16x16x32_bf16 v[208:211], v[8:11], v[220:223], 0\n"
"v_mfma_f32_16x16x32_bf16 v[140:143], v[12:15], v[220:223], 0\n"
"v_mfma_f32_16x16x32_bf16 v[156:159], v[16:19], v[220:223], 0\n"
"s_waitcnt lgkmcnt(0)\n"
"ds_read_b128 v[216:219], v4 offset:26624\n"
"v_mfma_f32_16x16x32_bf16 v[208:211], v[20:23], v[152:155], v[208:211]\n"
"v_mul_f32 v132, s60, v132\n"
"v_mul_f32 v133, s60, v133\n"
"v_mfma_f32_16x16x32_bf16 v[140:143], v[28:31], v[152:155], v[140:143]\n"
"v_cvt_pk_bf16_f32 v132, v132, v133\n"
"v_mul_f32 v134, s60, v134\n"
"v_mfma_f32_16x16x32_bf16 v[156:159], v[32:35], v[152:155], v[156:159]\n"
"ds_read_b128 v[212:215], v5 offset:26624\n"
"v_mul_f32 v135, s60, v135\n"
"v_cvt_pk_bf16_f32 v133, v134, v135\n"
"v_mul_f32 v136, s60, v136\n"
"v_mul_f32 v137, s60, v137\n"
"v_mul_f32 v138, s60, v138\n"
"v_mul_f32 v139, s60, v139\n"
"s_waitcnt lgkmcnt(1)\n"
"v_mfma_f32_16x16x32_bf16 v[208:211], v[36:39], v[216:219], v[208:211]\n"
"v_cvt_pk_bf16_f32 v134, v136, v137\n"
"v_cvt_pk_bf16_f32 v135, v138, v139\n"
"v_mfma_f32_16x16x32_bf16 v[140:143], v[40:43], v[216:219], v[140:143]\n"
"v_mul_f32 v144, s60, v144\n"
"v_mul_f32 v145, s60, v145\n"
"v_mfma_f32_16x16x32_bf16 v[156:159], v[44:47], v[216:219], v[156:159]\n"
"s_waitcnt lgkmcnt(0)\n"
"ds_read_b128 v[220:223], v4 offset:28672\n"
"v_mul_f32 v146, s60, v146\n"
"v_mul_f32 v147, s60, v147\n"
"v_mfma_f32_16x16x32_bf16 v[208:211], v[48:51], v[212:215], v[208:211]\n"
"v_cvt_pk_bf16_f32 v26, v144, v145\n"
"v_cvt_pk_bf16_f32 v27, v146, v147\n"
"v_mfma_f32_16x16x32_bf16 v[140:143], v[52:55], v[212:215], v[140:143]\n"
"v_mfma_f32_16x16x32_bf16 v[156:159], v[56:59], v[212:215], v[156:159]\n"
"s_waitcnt lgkmcnt(0)\n"
"ds_read_b128 v[136:139], v5 offset:28672\n"
"v_mfma_f32_16x16x32_bf16 v[208:211], v[60:63], v[220:223], v[208:211]\n"
"v_mfma_f32_16x16x32_bf16 v[140:143], v[64:67], v[220:223], v[140:143]\n"
"v_mfma_f32_16x16x32_bf16 v[156:159], v[68:71], v[220:223], v[156:159]\n"
"s_waitcnt lgkmcnt(0)\n"
"ds_read_b128 v[144:147], v4 offset:30720\n"
"v_mfma_f32_16x16x32_bf16 v[208:211], v[72:75], v[136:139], v[208:211]\n"
"v_mfma_f32_16x16x32_bf16 v[140:143], v[76:79], v[136:139], v[140:143]\n"
"v_mfma_f32_16x16x32_bf16 v[156:159], v[80:83], v[136:139], v[156:159]\n"
"s_waitcnt lgkmcnt(0)\n"
"ds_read_b128 v[148:151], v5 offset:30720\n"
"v_mfma_f32_16x16x32_bf16 v[214:217], v[8:11], v[144:147], 0\n"
"v_mfma_f32_16x16x32_bf16 v[220:223], v[12:15], v[144:147], 0\n"
"v_mfma_f32_16x16x32_bf16 v[152:155], v[16:19], v[144:147], 0\n"
"s_waitcnt lgkmcnt(0)\n"
"ds_read_b128 v[160:163], v4 offset:32768\n"
"v_mfma_f32_16x16x32_bf16 v[214:217], v[20:23], v[148:151], v[214:217]\n"
"v_cvt_pk_bf16_f32 v208, v208, v209\n"
"v_cvt_pk_bf16_f32 v209, v210, v211\n"
"v_cvt_pk_bf16_f32 v210, v140, v141\n"
"v_cvt_pk_bf16_f32 v211, v142, v143\n"
"v_cvt_pk_bf16_f32 v212, v156, v157\n"
"v_cvt_pk_bf16_f32 v213, v158, v159\n"
"v_mfma_f32_16x16x32_bf16 v[220:223], v[28:31], v[148:151], v[220:223]\n"
"v_mfma_f32_16x16x32_bf16 v[152:155], v[32:35], v[148:151], v[152:155]\n"
"s_waitcnt lgkmcnt(0)\n"
"ds_read_b128 v[136:139], v5 offset:32768\n"
"v_mfma_f32_16x16x32_bf16 v[214:217], v[36:39], v[160:163], v[214:217]\n"
"v_bfe_u32 v225, v6, 2, 6\n"
"v_mfma_f32_16x16x32_bf16 v[220:223], v[40:43], v[160:163], v[220:223]\n"
"ds_read_b32 v225, v225 offset:1472\n"
"v_mfma_f32_16x16x32_bf16 v[152:155], v[44:47], v[160:163], v[152:155]\n"
"s_add_u32 s68, s50, 4294966992\n"
"ds_read_b128 v[140:143], v4 offset:34816\n"
"s_waitcnt lgkmcnt(2)\n"
"v_mfma_f32_16x16x32_bf16 v[214:217], v[48:51], v[136:139], v[214:217]\n"
"v_mfma_f32_16x16x32_bf16 v[220:223], v[52:55], v[136:139], v[220:223]\n"
"v_mfma_f32_16x16x32_bf16 v[152:155], v[56:59], v[136:139], v[152:155]\n"
"s_waitcnt lgkmcnt(1)\n"
"ds_read_b128 v[144:147], v5 offset:34816\n"
"v_add_u32_sdwa v224, v225, v7 dst_sel:DWORD dst_unused:UNUSED_PAD src0_sel:WORD_0 src1_sel:DWORD\n"
"s_nop 0\n"
"v_min_u32 v177, s68, v224\n"
"s_waitcnt lgkmcnt(1)\n"
"v_mfma_f32_16x16x32_bf16 v[214:217], v[60:63], v[140:143], v[214:217]\n"
"s_add_u32 s73, s50, 4294965952\n"
"v_mfma_f32_16x16x32_bf16 v[220:223], v[64:67], v[140:143], v[220:223]\n"
"v_mfma_f32_16x16x32_bf16 v[152:155], v[68:71], v[140:143], v[152:155]\n"
"s_waitcnt lgkmcnt(0)\n"
"ds_read_b128 v[136:139], v177 offset:2688\n"
"v_max_u32 v176, s73, v224\n"
"v_mfma_f32_16x16x32_bf16 v[214:217], v[72:75], v[144:147], v[214:217]\n"
"v_mfma_f32_16x16x32_bf16 v[220:223], v[76:79], v[144:147], v[220:223]\n"
"ds_read_b128 v[148:151], v177 offset:2704\n"
"v_mfma_f32_16x16x32_bf16 v[152:155], v[80:83], v[144:147], v[152:155]\n"
"s_waitcnt lgkmcnt(1)\n"
"v_mfma_f32_16x16x32_bf16 v[136:139], v[178:181], v[132:135], v[136:139]\n"
"ds_read_b128 v[140:143], v176 offset:1344\n"
"v_mfma_f32_16x16x32_bf16 v[136:139], v[180:183], v[24:27], v[136:139]\n"
"s_waitcnt lgkmcnt(1)\n"
"v_mfma_f32_16x16x32_bf16 v[148:151], v[184:187], v[132:135], v[148:151]\n"
"ds_read_b128 v[156:159], v176 offset:1360\n"
"v_cvt_pk_bf16_f32 v214, v214, v215\n"
"v_cvt_pk_bf16_f32 v215, v216, v217\n"
"v_cvt_pk_bf16_f32 v216, v220, v221\n"
"v_cvt_pk_bf16_f32 v217, v222, v223\n"
"v_cvt_pk_bf16_f32 v218, v152, v153\n"
"v_cvt_pk_bf16_f32 v219, v154, v155\n"
"v_mfma_f32_16x16x32_bf16 v[148:151], v[186:189], v[24:27], v[148:151]\n"
"s_waitcnt lgkmcnt(1)\n"
"v_max3_f32 v224, v136, v137, v138\n"
"v_max_f32 v224, v224, v139\n"
"v_mfma_f32_16x16x32_bf16 v[140:143], v[226:229], v[132:135], v[140:143]\n"
"ds_read_b128 v[220:223], v176 offset:1376\n"
"v_mfma_f32_16x16x32_bf16 v[140:143], v[228:231], v[24:27], v[140:143]\n"
"s_waitcnt lgkmcnt(1)\n"
"s_nop 0\n"
"v_max3_f32 v224, v224, v148, v149\n"
"v_max3_f32 v224, v224, v150, v151\n"
"v_mfma_f32_16x16x32_bf16 v[156:159], v[232:235], v[132:135], v[156:159]\n"
"ds_read_b128 v[136:139], v177 offset:2720\n"
"v_mfma_f32_16x16x32_bf16 v[156:159], v[234:237], v[24:27], v[156:159]\n"
"s_waitcnt lgkmcnt(1)\n"
"v_max3_f32 v224, v224, v140, v141\n"
"v_max3_f32 v224, v224, v142, v143\n"
"v_mfma_f32_16x16x32_bf16 v[220:223], v[238:241], v[132:135], v[220:223]\n"
"ds_read_b128 v[144:147], v177 offset:2688\n"
"v_mfma_f32_16x16x32_bf16 v[220:223], v[240:243], v[24:27], v[220:223]\n"
"s_waitcnt lgkmcnt(1)\n"
"s_nop 0\n"
"v_max3_f32 v224, v224, v156, v157\n"
"v_max3_f32 v224, v224, v158, v159\n"
"v_mfma_f32_16x16x32_bf16 v[136:139], v[190:193], v[132:135], v[136:139]\n"
"ds_read_b128 v[140:143], v177 offset:2704\n"
"v_mfma_f32_16x16x32_bf16 v[136:139], v[192:195], v[24:27], v[136:139]\n"
"s_waitcnt lgkmcnt(1)\n"
"v_max3_f32 v224, v224, v220, v221\n"
"v_max3_f32 v224, v224, v222, v223\n"
"v_mfma_f32_16x16x32_bf16 v[144:147], v[178:181], v[132:135], v[144:147]\n"
"ds_read_b128 v[148:151], v176 offset:1344\n"
"v_mfma_f32_16x16x32_bf16 v[144:147], v[180:183], v[24:27], v[144:147]\n"
"s_nop 1\n"
"v_max3_f32 v224, v224, v136, v137\n"
"v_max3_f32 v224, v224, v138, v139\n"
"v_mov_b32 v225, v224\n"
"s_nop 1\n"
"v_permlane16_swap_b32 v224, v225\n"
"v_max_f32 v224, v224, v225\n"
"v_mov_b32 v225, v224\n"
"s_nop 1\n"
"v_permlane32_swap_b32 v224, v225\n"
"v_max_f32 v224, v224, v225\n"
"v_sub_f32 v144, v144, v224\n"
"v_exp_f32 v144, v144\n"
"v_sub_f32 v145, v145, v224\n"
"v_exp_f32 v145, v145\n"
"v_sub_f32 v146, v146, v224\n"
"v_exp_f32 v146, v146\n"
"v_sub_f32 v147, v147, v224\n"
"v_exp_f32 v147, v147\n"
"s_waitcnt lgkmcnt(1)\n"
"v_mfma_f32_16x16x32_bf16 v[140:143], v[184:187], v[132:135], v[140:143]\n"
"ds_read_b128 v[220:223], v176 offset:1360\n"
"v_cvt_pk_bf16_f32 v144, v144, v145\n"
"v_mfma_f32_16x16x32_bf16 v[140:143], v[186:189], v[24:27], v[140:143]\n"
"ds_write_addtid_b32 v144 offset:53248\n"
"v_cvt_pk_bf16_f32 v145, v146, v147\n"
"ds_write_addtid_b32 v145 offset:53504\n"
"s_waitcnt lgkmcnt(3)\n"
"v_mfma_f32_16x16x32_bf16 v[148:151], v[226:229], v[132:135], v[148:151]\n"
"ds_read_b128 v[136:139], v176 offset:1376\n"
"v_mfma_f32_16x16x32_bf16 v[148:151], v[228:231], v[24:27], v[148:151]\n"
"s_nop 0\n"
"v_sub_f32 v140, v140, v224\n"
"v_exp_f32 v140, v140\n"
"v_sub_f32 v141, v141, v224\n"
"v_exp_f32 v141, v141\n"
"v_sub_f32 v142, v142, v224\n"
"v_exp_f32 v142, v142\n"
"v_sub_f32 v143, v143, v224\n"
"v_exp_f32 v143, v143\n"
"v_sub_f32 v148, v148, v224\n"
"v_exp_f32 v148, v148\n"
"v_sub_f32 v149, v149, v224\n"
"v_exp_f32 v149, v149\n"
"v_sub_f32 v150, v150, v224\n"
"v_exp_f32 v150, v150\n"
"v_sub_f32 v151, v151, v224\n"
"v_exp_f32 v151, v151\n"
"v_cvt_pk_bf16_f32 v140, v140, v141\n"
"v_cvt_pk_bf16_f32 v148, v148, v149\n"
"v_cvt_pk_bf16_f32 v141, v142, v143\n"
"v_cvt_pk_bf16_f32 v149, v150, v151\n"
"ds_write_addtid_b32 v140 offset:53760\n"
"ds_write_addtid_b32 v148 offset:54272\n"
"ds_write_addtid_b32 v141 offset:54016\n"
"s_waitcnt lgkmcnt(6)\n"
"v_mfma_f32_16x16x32_bf16 v[220:223], v[232:235], v[132:135], v[220:223]\n"
"ds_write_addtid_b32 v149 offset:54528\n"
"v_mfma_f32_16x16x32_bf16 v[220:223], v[234:237], v[24:27], v[220:223]\n"
"s_waitcnt lgkmcnt(4)\n"
"ds_read_b128 v[144:147], v177 offset:2720\n"
"v_mfma_f32_16x16x32_bf16 v[136:139], v[238:241], v[132:135], v[136:139]\n"
"ds_read_b128 v[152:155], v4 offset:55296\n"
"v_mfma_f32_16x16x32_bf16 v[136:139], v[240:243], v[24:27], v[136:139]\n"
"s_waitcnt lgkmcnt(2)\n"
"s_barrier\n"
"s_nop 0\n"
"v_sub_f32 v220, v220, v224\n"
"v_exp_f32 v220, v220\n"
"v_sub_f32 v221, v221, v224\n"
"v_exp_f32 v221, v221\n"
"v_sub_f32 v222, v222, v224\n"
"v_exp_f32 v222, v222\n"
"v_sub_f32 v223, v223, v224\n"
"v_exp_f32 v223, v223\n"
"v_cvt_pk_bf16_f32 v220, v220, v221\n"
"v_cvt_pk_bf16_f32 v221, v222, v223\n"
"v_sub_f32 v136, v136, v224\n"
"v_exp_f32 v136, v136\n"
"v_sub_f32 v137, v137, v224\n"
"v_exp_f32 v137, v137\n"
"v_sub_f32 v138, v138, v224\n"
"v_exp_f32 v138, v138\n"
"v_sub_f32 v139, v139, v224\n"
"v_exp_f32 v139, v139\n"
"v_cvt_pk_bf16_f32 v136, v136, v137\n"
"v_cvt_pk_bf16_f32 v137, v138, v139\n"
"ds_write_addtid_b32 v220 offset:54784\n"
"ds_write_addtid_b32 v221 offset:55040\n"
"s_waitcnt lgkmcnt(3)\n"
"v_mfma_f32_16x16x32_bf16 v[144:147], v[190:193], v[132:135], v[144:147]\n"
"ds_write_addtid_b32 v136 offset:57856\n"
"v_mfma_f32_16x16x32_bf16 v[144:147], v[192:195], v[24:27], v[144:147]\n"
"s_waitcnt lgkmcnt(3)\n"
"ds_read_b128 v[140:143], v5 offset:55296\n"
"v_mfma_f32_16x16x32_bf16 v[148:151], v[84:87], v[152:155], 0\n"
"ds_write_addtid_b32 v137 offset:58112\n"
"v_mfma_f32_16x16x32_bf16 v[156:159], v[88:91], v[152:155], 0\n"
"ds_read_b128 v[220:223], v6 offset:5120\n"
"v_mfma_f32_16x16x32_bf16 v[132:135], v[92:95], v[152:155], 0\n"
"s_waitcnt lgkmcnt(2)\n"
"ds_read_b128 v[160:163], v4 offset:57344\n"
"v_mfma_f32_16x16x32_bf16 v[148:151], v[96:99], v[140:143], v[148:151]\n"
"v_sub_f32 v144, v144, v224\n"
"v_exp_f32 v144, v144\n"
"v_mfma_f32_16x16x32_bf16 v[156:159], v[100:103], v[140:143], v[156:159]\n"
"s_waitcnt lgkmcnt(1)\n"
"ds_read_b128 v[136:139], v6 offset:6144\n"
"v_mfma_f32_16x16x32_bf16 v[132:135], v[220:223], v[140:143], v[132:135]\n"
"s_waitcnt lgkmcnt(1)\n"
"ds_read_b128 v[152:155], v5 offset:57344\n"
"v_mfma_f32_16x16x32_bf16 v[148:151], v[104:107], v[160:163], v[148:151]\n"
"v_sub_f32 v145, v145, v224\n"
"v_exp_f32 v145, v145\n"
"v_mfma_f32_16x16x32_bf16 v[156:159], v[108:111], v[160:163], v[156:159]\n"
"ds_read_b128 v[164:167], v6 offset:7168\n"
"v_sub_f32 v146, v146, v224\n"
"v_exp_f32 v146, v146\n"
"v_sub_f32 v147, v147, v224\n"
"v_exp_f32 v147, v147\n"
"s_waitcnt lgkmcnt(2)\n"
"v_mfma_f32_16x16x32_bf16 v[132:135], v[136:139], v[160:163], v[132:135]\n"
"s_waitcnt lgkmcnt(1)\n"
"ds_read_b128 v[140:143], v4 offset:59392\n"
"v_cvt_pk_bf16_f32 v144, v144, v145\n"
"v_cvt_pk_bf16_f32 v145, v146, v147\n"
"v_mfma_f32_16x16x32_bf16 v[148:151], v[112:115], v[152:155], v[148:151]\n"
"ds_write_addtid_b32 v144 offset:57344\n"
"v_mfma_f32_16x16x32_bf16 v[156:159], v[116:119], v[152:155], v[156:159]\n"
"ds_write_addtid_b32 v145 offset:57600\n"
"s_waitcnt lgkmcnt(3)\n"
"v_mfma_f32_16x16x32_bf16 v[132:135], v[164:167], v[152:155], v[132:135]\n"
"s_waitcnt lgkmcnt(2)\n"
"ds_read_b128 v[136:139], v6 offset:8192\n"
"ds_read_b128 v[160:163], v5 offset:59392\n"
"v_mfma_f32_16x16x32_bf16 v[148:151], v[0:3], v[140:143], v[148:151]\n"
"v_mfma_f32_16x16x32_bf16 v[156:159], v[120:123], v[140:143], v[156:159]\n"
"s_waitcnt lgkmcnt(2)\n"
"s_barrier\n"
"ds_read_b128 v[144:147], v6 offset:9216\n"
"s_waitcnt lgkmcnt(2)\n"
"v_mfma_f32_16x16x32_bf16 v[132:135], v[136:139], v[140:143], v[132:135]\n"
"s_waitcnt lgkmcnt(1)\n"
"v_mfma_f32_16x16x32_bf16 v[148:151], v[124:127], v[160:163], v[148:151]\n"
"ds_read_b128 v[152:155], v4 offset:49152\n"
"v_mfma_f32_16x16x32_bf16 v[156:159], v[128:131], v[160:163], v[156:159]\n"
"s_waitcnt lgkmcnt(1)\n"
"v_mfma_f32_16x16x32_bf16 v[132:135], v[144:147], v[160:163], v[132:135]\n"
"s_waitcnt lgkmcnt(0)\n"
"v_mfma_f32_16x16x32_bf16 v[220:223], v[8:11], v[152:155], 0\n"
"ds_read_b128 v[164:167], v5 offset:49152\n"
"v_mfma_f32_16x16x32_bf16 v[136:139], v[12:15], v[152:155], 0\n"
"v_mfma_f32_16x16x32_bf16 v[140:143], v[16:19], v[152:155], 0\n"
"s_waitcnt lgkmcnt(0)\n"
"v_mfma_f32_16x16x32_bf16 v[220:223], v[20:23], v[164:167], v[220:223]\n"
"ds_read_b128 v[168:171], v4 offset:51200\n"
"v_mul_f32 v148, s60, v148\n"
"v_mfma_f32_16x16x32_bf16 v[136:139], v[28:31], v[164:167], v[136:139]\n"
"v_mul_f32 v149, s60, v149\n"
"v_cvt_pk_bf16_f32 v148, v148, v149\n"
"v_mfma_f32_16x16x32_bf16 v[140:143], v[32:35], v[164:167], v[140:143]\n"
"v_mul_f32 v150, s60, v150\n"
"v_mul_f32 v151, s60, v151\n"
"v_cvt_pk_bf16_f32 v149, v150, v151\n"
"v_mul_f32 v156, s60, v156\n"
"v_mul_f32 v157, s60, v157\n"
"v_mul_f32 v158, s60, v158\n"
"v_mul_f32 v159, s60, v159\n"
"v_cvt_pk_bf16_f32 v150, v156, v157\n"
"v_cvt_pk_bf16_f32 v151, v158, v159\n"
"v_mul_f32 v132, s60, v132\n"
"v_mul_f32 v133, s60, v133\n"
"v_mul_f32 v134, s60, v134\n"
"s_waitcnt lgkmcnt(0)\n"
"v_mfma_f32_16x16x32_bf16 v[220:223], v[36:39], v[168:171], v[220:223]\n"
"ds_read_b128 v[144:147], v5 offset:51200\n"
"v_mul_f32 v135, s60, v135\n"
"v_mfma_f32_16x16x32_bf16 v[136:139], v[40:43], v[168:171], v[136:139]\n"
"v_cvt_pk_bf16_f32 v26, v132, v133\n"
"v_cvt_pk_bf16_f32 v27, v134, v135\n"
"v_mfma_f32_16x16x32_bf16 v[140:143], v[44:47], v[168:171], v[140:143]\n"
"s_waitcnt lgkmcnt(0)\n"
"v_bfe_u32 v225, v6, 2, 6\n"
"ds_read_b32 v225, v225 offset:1536\n"
"v_mfma_f32_16x16x32_bf16 v[220:223], v[48:51], v[144:147], v[220:223]\n"
"ds_read_b128 v[152:155], v4 offset:53248\n"
"v_mfma_f32_16x16x32_bf16 v[136:139], v[52:55], v[144:147], v[136:139]\n"
"s_add_u32 s70, s50, 4294964608\n"
"v_mfma_f32_16x16x32_bf16 v[140:143], v[56:59], v[144:147], v[140:143]\n"
"s_waitcnt lgkmcnt(1)\n"
"v_add_u32_sdwa v177, v225, v7 dst_sel:DWORD dst_unused:UNUSED_PAD src0_sel:WORD_0 src1_sel:DWORD\n"
"s_waitcnt lgkmcnt(0)\n"
"v_mfma_f32_16x16x32_bf16 v[220:223], v[60:63], v[152:155], v[220:223]\n"
"ds_read_b128 v[132:135], v5 offset:53248\n"
"v_max_u32 v176, s70, v177\n"
"v_mfma_f32_16x16x32_bf16 v[136:139], v[64:67], v[152:155], v[136:139]\n"
"v_mfma_f32_16x16x32_bf16 v[140:143], v[68:71], v[152:155], v[140:143]\n"
"s_add_u32 s71, s50, 4294965648\n"
"s_waitcnt lgkmcnt(0)\n"
"v_mfma_f32_16x16x32_bf16 v[220:223], v[72:75], v[132:135], v[220:223]\n"
"ds_read_b128 v[144:147], v176 offset:2688\n"
"v_mfma_f32_16x16x32_bf16 v[136:139], v[76:79], v[132:135], v[136:139]\n"
"v_min_u32 v175, s71, v177\n"
"v_mfma_f32_16x16x32_bf16 v[140:143], v[80:83], v[132:135], v[140:143]\n"
"s_waitcnt lgkmcnt(0)\n"
"ds_read_b128 v[152:155], v176 offset:2704\n"
"v_mfma_f32_16x16x32_bf16 v[144:147], v[178:181], v[148:151], v[144:147]\n"
"v_mfma_f32_16x16x32_bf16 v[144:147], v[180:183], v[24:27], v[144:147]\n"
"ds_read_b128 v[156:159], v175 offset:4032\n"
"v_cvt_pk_bf16_f32 v220, v220, v221\n"
"v_cvt_pk_bf16_f32 v221, v222, v223\n"
"v_cvt_pk_bf16_f32 v222, v136, v137\n"
"v_cvt_pk_bf16_f32 v223, v138, v139\n"
"v_cvt_pk_bf16_f32 v224, v140, v141\n"
"v_cvt_pk_bf16_f32 v225, v142, v143\n"
"s_waitcnt lgkmcnt(1)\n"
"v_mfma_f32_16x16x32_bf16 v[152:155], v[184:187], v[148:151], v[152:155]\n"
"s_add_u32 s69, s50, 4294965952\n"
"v_max3_f32 v172, v144, v145, v146\n"
"v_max_f32 v172, v172, v147\n"
"v_mfma_f32_16x16x32_bf16 v[152:155], v[186:189], v[24:27], v[152:155]\n"
"s_waitcnt lgkmcnt(0)\n"
"ds_read_b128 v[132:135], v175 offset:4048\n"
"v_mfma_f32_16x16x32_bf16 v[156:159], v[208:211], v[148:151], v[156:159]\n"
"v_max_u32 v174, s69, v177\n"
"v_mfma_f32_16x16x32_bf16 v[156:159], v[210:213], v[24:27], v[156:159]\n"
"s_waitcnt lgkmcnt(0)\n"
"ds_read_b128 v[136:139], v176 offset:2720\n"
"s_nop 0\n"
"v_max3_f32 v172, v172, v152, v153\n"
"v_max3_f32 v172, v172, v154, v155\n"
"v_mfma_f32_16x16x32_bf16 v[132:135], v[214:217], v[148:151], v[132:135]\n"
"v_mfma_f32_16x16x32_bf16 v[132:135], v[216:219], v[24:27], v[132:135]\n"
"s_waitcnt lgkmcnt(0)\n"
"ds_read_b128 v[140:143], v174 offset:1344\n"
"v_max3_f32 v172, v172, v156, v157\n"
"v_max3_f32 v172, v172, v158, v159\n"
"v_mfma_f32_16x16x32_bf16 v[136:139], v[190:193], v[148:151], v[136:139]\n"
"v_mfma_f32_16x16x32_bf16 v[136:139], v[192:195], v[24:27], v[136:139]\n"
"s_waitcnt lgkmcnt(0)\n"
"ds_read_b128 v[144:147], v174 offset:1360\n"
"v_max3_f32 v172, v172, v132, v133\n"
"v_max3_f32 v172, v172, v134, v135\n"
"v_mfma_f32_16x16x32_bf16 v[140:143], v[226:229], v[148:151], v[140:143]\n"
"v_mfma_f32_16x16x32_bf16 v[140:143], v[228:231], v[24:27], v[140:143]\n"
"s_waitcnt lgkmcnt(0)\n"
"ds_read_b128 v[152:155], v174 offset:1376\n"
"v_max3_f32 v172, v172, v136, v137\n"
"v_max3_f32 v172, v172, v138, v139\n"
"v_mfma_f32_16x16x32_bf16 v[144:147], v[232:235], v[148:151], v[144:147]\n"
"v_mfma_f32_16x16x32_bf16 v[144:147], v[234:237], v[24:27], v[144:147]\n"
"s_waitcnt lgkmcnt(0)\n"
"ds_read_b128 v[132:135], v175 offset:4064\n"
"v_max3_f32 v172, v172, v140, v141\n"
"v_max3_f32 v172, v172, v142, v143\n"
"v_mfma_f32_16x16x32_bf16 v[152:155], v[238:241], v[148:151], v[152:155]\n"
"v_mfma_f32_16x16x32_bf16 v[152:155], v[240:243], v[24:27], v[152:155]\n"
"s_waitcnt lgkmcnt(0)\n"
"ds_read_b128 v[136:139], v176 offset:2688\n"
"v_max3_f32 v172, v172, v144, v145\n"
"v_max3_f32 v172, v172, v146, v147\n"
"v_mfma_f32_16x16x32_bf16 v[132:135], v[220:223], v[148:151], v[132:135]\n"
"v_mfma_f32_16x16x32_bf16 v[132:135], v[222:225], v[24:27], v[132:135]\n"
"s_waitcnt lgkmcnt(0)\n"
"ds_read_b128 v[140:143], v176 offset:2704\n"
"v_max3_f32 v172, v172, v152, v153\n"
"v_max3_f32 v172, v172, v154, v155\n"
"v_mfma_f32_16x16x32_bf16 v[136:139], v[178:181], v[148:151], v[136:139]\n"
"v_mfma_f32_16x16x32_bf16 v[136:139], v[180:183], v[24:27], v[136:139]\n"
"ds_read_b128 v[144:147], v175 offset:4032\n"
"s_nop 0\n"
"v_max3_f32 v172, v172, v132, v133\n"
"v_max3_f32 v172, v172, v134, v135\n"
"v_mov_b32 v173, v172\n"
"s_nop 1\n"
"v_permlane16_swap_b32 v172, v173\n"
"v_max_f32 v172, v172, v173\n"
"v_mov_b32 v173, v172\n"
"s_nop 1\n"
"v_permlane32_swap_b32 v172, v173\n"
"v_max_f32 v172, v172, v173\n"
"v_sub_f32 v136, v136, v172\n"
"v_exp_f32 v136, v136\n"
"v_sub_f32 v137, v137, v172\n"
"v_exp_f32 v137, v137\n"
"v_sub_f32 v138, v138, v172\n"
"v_exp_f32 v138, v138\n"
"v_sub_f32 v139, v139, v172\n"
"v_exp_f32 v139, v139\n"
"s_waitcnt lgkmcnt(1)\n"
"v_mfma_f32_16x16x32_bf16 v[140:143], v[184:187], v[148:151], v[140:143]\n"
"v_cvt_pk_bf16_f32 v136, v136, v137\n"
"v_cvt_pk_bf16_f32 v137, v138, v139\n"
"v_mfma_f32_16x16x32_bf16 v[140:143], v[186:189], v[24:27], v[140:143]\n"
"ds_read_b128 v[132:135], v175 offset:4048\n"
"ds_write_addtid_b32 v136 offset:55296\n"
"ds_write_addtid_b32 v137 offset:55552\n"
"s_waitcnt lgkmcnt(3)\n"
"v_mfma_f32_16x16x32_bf16 v[144:147], v[208:211], v[148:151], v[144:147]\n"
"v_mfma_f32_16x16x32_bf16 v[144:147], v[210:213], v[24:27], v[144:147]\n"
"ds_read_b128 v[152:155], v176 offset:2720\n"
"s_nop 0\n"
"v_sub_f32 v140, v140, v172\n"
"v_exp_f32 v140, v140\n"
"v_sub_f32 v141, v141, v172\n"
"v_exp_f32 v141, v141\n"
"v_sub_f32 v142, v142, v172\n"
"v_exp_f32 v142, v142\n"
"v_sub_f32 v143, v143, v172\n"
"v_exp_f32 v143, v143\n"
"v_sub_f32 v144, v144, v172\n"
"v_exp_f32 v144, v144\n"
"v_sub_f32 v145, v145, v172\n"
"v_exp_f32 v145, v145\n"
"v_cvt_pk_bf16_f32 v140, v140, v141\n"
"v_sub_f32 v146, v146, v172\n"
"v_exp_f32 v146, v146\n"
"v_cvt_pk_bf16_f32 v141, v142, v143\n"
"v_sub_f32 v147, v147, v172\n"
"v_exp_f32 v147, v147\n"
"v_cvt_pk_bf16_f32 v144, v144, v145\n"
"v_cvt_pk_bf16_f32 v145, v146, v147\n"
"ds_write_addtid_b32 v140 offset:55808\n"
"ds_write_addtid_b32 v141 offset:56064\n"
"s_waitcnt lgkmcnt(5)\n"
"v_mfma_f32_16x16x32_bf16 v[132:135], v[214:217], v[148:151], v[132:135]\n"
"ds_write_addtid_b32 v144 offset:56320\n"
"v_mfma_f32_16x16x32_bf16 v[132:135], v[216:219], v[24:27], v[132:135]\n"
"ds_read_b128 v[136:139], v174 offset:1344\n"
"ds_write_addtid_b32 v145 offset:56576\n"
"s_waitcnt lgkmcnt(5)\n"
"v_mfma_f32_16x16x32_bf16 v[152:155], v[190:193], v[148:151], v[152:155]\n"
"v_mfma_f32_16x16x32_bf16 v[152:155], v[192:195], v[24:27], v[152:155]\n"
"ds_read_b128 v[140:143], v174 offset:1360\n"
"s_nop 1\n"
"v_sub_f32 v132, v132, v172\n"
"v_exp_f32 v132, v132\n"
"v_sub_f32 v133, v133, v172\n"
"v_exp_f32 v133, v133\n"
"v_sub_f32 v134, v134, v172\n"
"v_exp_f32 v134, v134\n"
"v_sub_f32 v135, v135, v172\n"
"v_exp_f32 v135, v135\n"
"v_sub_f32 v152, v152, v172\n"
"v_exp_f32 v152, v152\n"
"v_cvt_pk_bf16_f32 v132, v132, v133\n"
"v_cvt_pk_bf16_f32 v133, v134, v135\n"
"v_sub_f32 v153, v153, v172\n"
"v_exp_f32 v153, v153\n"
"ds_write_addtid_b32 v132 offset:56832\n"
"v_cvt_pk_bf16_f32 v152, v152, v153\n"
"ds_write_addtid_b32 v133 offset:57088\n"
"s_waitcnt lgkmcnt(4)\n"
"v_mfma_f32_16x16x32_bf16 v[136:139], v[226:229], v[148:151], v[136:139]\n"
"ds_write_addtid_b32 v152 offset:34304\n"
"v_mfma_f32_16x16x32_bf16 v[136:139], v[228:231], v[24:27], v[136:139]\n"
"s_waitcnt lgkmcnt(3)\n"
"ds_read_b128 v[144:147], v174 offset:1376\n"
"v_mfma_f32_16x16x32_bf16 v[140:143], v[232:235], v[148:151], v[140:143]\n"
"v_mfma_f32_16x16x32_bf16 v[140:143], v[234:237], v[24:27], v[140:143]\n"
"s_waitcnt lgkmcnt(1)\n"
"ds_read_b128 v[132:135], v175 offset:4064\n"
"s_barrier\n"
"s_nop 0\n"
"v_sub_f32 v136, v136, v172\n"
"v_exp_f32 v136, v136\n"
"v_sub_f32 v137, v137, v172\n"
"v_exp_f32 v137, v137\n"
"v_sub_f32 v138, v138, v172\n"
"v_exp_f32 v138, v138\n"
"v_sub_f32 v139, v139, v172\n"
"v_exp_f32 v139, v139\n"
"v_sub_f32 v140, v140, v172\n"
"v_exp_f32 v140, v140\n"
"v_cvt_pk_bf16_f32 v136, v136, v137\n"
"v_cvt_pk_bf16_f32 v137, v138, v139\n"
"v_sub_f32 v141, v141, v172\n"
"v_exp_f32 v141, v141\n"
"v_sub_f32 v142, v142, v172\n"
"v_exp_f32 v142, v142\n"
"v_sub_f32 v143, v143, v172\n"
"v_exp_f32 v143, v143\n"
"v_cvt_pk_bf16_f32 v140, v140, v141\n"
"v_cvt_pk_bf16_f32 v141, v142, v143\n"
"ds_write_addtid_b32 v136 offset:57856\n"
"ds_write_addtid_b32 v137 offset:58112\n"
"s_waitcnt lgkmcnt(3)\n"
"v_mfma_f32_16x16x32_bf16 v[144:147], v[238:241], v[148:151], v[144:147]\n"
"ds_write_addtid_b32 v140 offset:58368\n"
"v_mfma_f32_16x16x32_bf16 v[144:147], v[240:243], v[24:27], v[144:147]\n"
"ds_read_b128 v[226:229], v4 offset:18432\n"
"ds_write_addtid_b32 v141 offset:58624\n"
"s_waitcnt lgkmcnt(5)\n"
"v_mfma_f32_16x16x32_bf16 v[132:135], v[220:223], v[148:151], v[132:135]\n"
"v_mfma_f32_16x16x32_bf16 v[132:135], v[222:225], v[24:27], v[132:135]\n"
"s_waitcnt lgkmcnt(1)\n"
"v_mfma_f32_16x16x32_bf16 v[136:139], v[84:87], v[226:229], 0\n"
"ds_read_b128 v[152:155], v5 offset:18432\n"
"v_mfma_f32_16x16x32_bf16 v[156:159], v[88:91], v[226:229], 0\n"
"v_mfma_f32_16x16x32_bf16 v[160:163], v[92:95], v[226:229], 0\n"
"ds_read_b128 v[140:143], v6 offset:5120\n"
"v_sub_f32 v144, v144, v172\n"
"v_exp_f32 v144, v144\n"
"v_sub_f32 v145, v145, v172\n"
"v_exp_f32 v145, v145\n"
"v_sub_f32 v132, v132, v172\n"
"v_exp_f32 v132, v132\n"
"v_cvt_pk_bf16_f32 v144, v144, v145\n"
"s_waitcnt lgkmcnt(1)\n"
"v_mfma_f32_16x16x32_bf16 v[136:139], v[96:99], v[152:155], v[136:139]\n"
"ds_read_b128 v[226:229], v4 offset:20480\n"
"v_mfma_f32_16x16x32_bf16 v[156:159], v[100:103], v[152:155], v[156:159]\n"
"ds_write_addtid_b32 v144 offset:58880\n"
"s_waitcnt lgkmcnt(2)\n"
"v_mfma_f32_16x16x32_bf16 v[160:163], v[140:143], v[152:155], v[160:163]\n"
"s_waitcnt lgkmcnt(1)\n"
"ds_read_b128 v[148:151], v6 offset:6144\n"
"v_sub_f32 v133, v133, v172\n"
"v_exp_f32 v133, v133\n"
"v_mfma_f32_16x16x32_bf16 v[136:139], v[104:107], v[226:229], v[136:139]\n"
"ds_read_b128 v[164:167], v5 offset:20480\n"
"v_cvt_pk_bf16_f32 v132, v132, v133\n"
"v_mfma_f32_16x16x32_bf16 v[156:159], v[108:111], v[226:229], v[156:159]\n"
"ds_write_addtid_b32 v132 offset:57344\n"
"s_waitcnt lgkmcnt(2)\n"
"v_mfma_f32_16x16x32_bf16 v[160:163], v[148:151], v[226:229], v[160:163]\n"
"s_waitcnt lgkmcnt(1)\n"
"ds_read_b128 v[140:143], v6 offset:7168\n"
"v_mfma_f32_16x16x32_bf16 v[136:139], v[112:115], v[164:167], v[136:139]\n"
"s_waitcnt lgkmcnt(1)\n"
"s_barrier\n"
"ds_read_b128 v[144:147], v4 offset:22528\n"
"v_mfma_f32_16x16x32_bf16 v[156:159], v[116:119], v[164:167], v[156:159]\n"
"s_waitcnt lgkmcnt(1)\n"
"v_mfma_f32_16x16x32_bf16 v[160:163], v[140:143], v[164:167], v[160:163]\n"
"s_waitcnt lgkmcnt(0)\n"
"ds_read_b128 v[132:135], v6 offset:8192\n"
"v_mfma_f32_16x16x32_bf16 v[136:139], v[0:3], v[144:147], v[136:139]\n"
"ds_read_b128 v[148:151], v5 offset:22528\n"
"v_mfma_f32_16x16x32_bf16 v[156:159], v[120:123], v[144:147], v[156:159]\n"
"s_waitcnt lgkmcnt(1)\n"
"v_mfma_f32_16x16x32_bf16 v[160:163], v[132:135], v[144:147], v[160:163]\n"
"s_waitcnt lgkmcnt(0)\n"
"ds_read_b128 v[152:155], v6 offset:9216\n"
"v_mfma_f32_16x16x32_bf16 v[136:139], v[124:127], v[148:151], v[136:139]\n"
"ds_read_b128 v[140:143], v4 offset:12288\n"
"v_mfma_f32_16x16x32_bf16 v[156:159], v[128:131], v[148:151], v[156:159]\n"
"s_waitcnt lgkmcnt(1)\n"
"v_mfma_f32_16x16x32_bf16 v[160:163], v[152:155], v[148:151], v[160:163]\n"
"s_waitcnt lgkmcnt(0)\n"
"v_mfma_f32_16x16x32_bf16 v[226:229], v[8:11], v[140:143], 0\n"
"ds_read_b128 v[132:135], v5 offset:12288\n"
"v_mfma_f32_16x16x32_bf16 v[144:147], v[12:15], v[140:143], 0\n"
"v_mfma_f32_16x16x32_bf16 v[164:167], v[16:19], v[140:143], 0\n"
"s_waitcnt lgkmcnt(0)\n"
"v_mfma_f32_16x16x32_bf16 v[226:229], v[20:23], v[132:135], v[226:229]\n"
"ds_read_b128 v[168:171], v4 offset:14336\n"
"v_mul_f32 v136, s60, v136\n"
"v_mfma_f32_16x16x32_bf16 v[144:147], v[28:31], v[132:135], v[144:147]\n"
"v_mul_f32 v137, s60, v137\n"
"v_cvt_pk_bf16_f32 v136, v136, v137\n"
"v_mfma_f32_16x16x32_bf16 v[164:167], v[32:35], v[132:135], v[164:167]\n"
"v_mul_f32 v138, s60, v138\n"
"v_mul_f32 v139, s60, v139\n"
"v_cvt_pk_bf16_f32 v137, v138, v139\n"
"v_mul_f32 v156, s60, v156\n"
"v_mul_f32 v157, s60, v157\n"
"v_mul_f32 v158, s60, v158\n"
"v_mul_f32 v159, s60, v159\n"
"v_cvt_pk_bf16_f32 v138, v156, v157\n"
"v_cvt_pk_bf16_f32 v139, v158, v159\n"
"v_mul_f32 v160, s60, v160\n"
"v_mul_f32 v161, s60, v161\n"
"v_mul_f32 v162, s60, v162\n"
"s_waitcnt lgkmcnt(0)\n"
"v_mfma_f32_16x16x32_bf16 v[226:229], v[36:39], v[168:171], v[226:229]\n"
"ds_read_b128 v[132:135], v5 offset:14336\n"
"v_mul_f32 v163, s60, v163\n"
"v_mfma_f32_16x16x32_bf16 v[144:147], v[40:43], v[168:171], v[144:147]\n"
"v_cvt_pk_bf16_f32 v26, v160, v161\n"
"v_cvt_pk_bf16_f32 v27, v162, v163\n"
"v_mfma_f32_16x16x32_bf16 v[164:167], v[44:47], v[168:171], v[164:167]\n"
"s_waitcnt lgkmcnt(0)\n"
"v_bfe_u32 v231, v6, 2, 6\n"
"ds_read_b32 v231, v231 offset:1600\n"
"v_mfma_f32_16x16x32_bf16 v[226:229], v[48:51], v[132:135], v[226:229]\n"
"ds_read_b128 v[140:143], v4 offset:16384\n"
"v_mfma_f32_16x16x32_bf16 v[144:147], v[52:55], v[132:135], v[144:147]\n"
"s_add_u32 s73, s50, 4294966976\n"
"v_mfma_f32_16x16x32_bf16 v[164:167], v[56:59], v[132:135], v[164:167]\n"
"s_waitcnt lgkmcnt(1)\n"
"v_add_u32_sdwa v177, v231, v7 dst_sel:DWORD dst_unused:UNUSED_PAD src0_sel:WORD_0 src1_sel:DWORD\n"
"s_waitcnt lgkmcnt(0)\n"
"v_mfma_f32_16x16x32_bf16 v[226:229], v[60:63], v[140:143], v[226:229]\n"
"ds_read_b128 v[148:151], v5 offset:16384\n"
"v_min_u32 v176, s73, v177\n"
"v_mfma_f32_16x16x32_bf16 v[144:147], v[64:67], v[140:143], v[144:147]\n"
"v_mfma_f32_16x16x32_bf16 v[164:167], v[68:71], v[140:143], v[164:167]\n"
"s_add_u32 s68, s50, 4294965632\n"
"s_waitcnt lgkmcnt(0)\n"
"v_mfma_f32_16x16x32_bf16 v[226:229], v[72:75], v[148:151], v[226:229]\n"
"ds_read_b128 v[132:135], v176 offset:2704\n"
"v_mfma_f32_16x16x32_bf16 v[144:147], v[76:79], v[148:151], v[144:147]\n"
"v_min_u32 v175, s68, v177\n"
"v_mfma_f32_16x16x32_bf16 v[164:167], v[80:83], v[148:151], v[164:167]\n"
"s_waitcnt lgkmcnt(0)\n"
"ds_read_b128 v[140:143], v176 offset:2720\n"
"v_mfma_f32_16x16x32_bf16 v[132:135], v[184:187], v[136:139], v[132:135]\n"
"v_mfma_f32_16x16x32_bf16 v[132:135], v[186:189], v[24:27], v[132:135]\n"
"ds_read_b128 v[152:155], v175 offset:4048\n"
"v_cvt_pk_bf16_f32 v226, v226, v227\n"
"v_cvt_pk_bf16_f32 v227, v228, v229\n"
"v_cvt_pk_bf16_f32 v228, v144, v145\n"
"v_cvt_pk_bf16_f32 v229, v146, v147\n"
"v_cvt_pk_bf16_f32 v230, v164, v165\n"
"v_cvt_pk_bf16_f32 v231, v166, v167\n"
"s_waitcnt lgkmcnt(1)\n"
"v_mfma_f32_16x16x32_bf16 v[140:143], v[190:193], v[136:139], v[140:143]\n"
"s_add_u32 s72, s50, 4294965936\n"
"v_max3_f32 v172, v132, v133, v134\n"
"v_max_f32 v172, v172, v135\n"
"v_mfma_f32_16x16x32_bf16 v[140:143], v[192:195], v[24:27], v[140:143]\n"
"s_waitcnt lgkmcnt(0)\n"
"ds_read_b128 v[144:147], v175 offset:4064\n"
"v_mfma_f32_16x16x32_bf16 v[152:155], v[214:217], v[136:139], v[152:155]\n"
"v_max_u32 v174, s72, v177\n"
"v_mfma_f32_16x16x32_bf16 v[152:155], v[216:219], v[24:27], v[152:155]\n"
"s_waitcnt lgkmcnt(0)\n"
"ds_read_b128 v[148:151], v176 offset:2736\n"
"s_nop 0\n"
"v_max3_f32 v172, v172, v140, v141\n"
"v_max3_f32 v172, v172, v142, v143\n"
"v_mfma_f32_16x16x32_bf16 v[144:147], v[220:223], v[136:139], v[144:147]\n"
"v_mfma_f32_16x16x32_bf16 v[144:147], v[222:225], v[24:27], v[144:147]\n"
"s_waitcnt lgkmcnt(0)\n"
"ds_read_b128 v[132:135], v174 offset:1360\n"
"v_max3_f32 v172, v172, v152, v153\n"
"v_max3_f32 v172, v172, v154, v155\n"
"v_mfma_f32_16x16x32_bf16 v[148:151], v[196:199], v[136:139], v[148:151]\n"
"v_mfma_f32_16x16x32_bf16 v[148:151], v[198:201], v[24:27], v[148:151]\n"
"s_waitcnt lgkmcnt(0)\n"
"ds_read_b128 v[140:143], v174 offset:1376\n"
"v_max3_f32 v172, v172, v144, v145\n"
"v_max3_f32 v172, v172, v146, v147\n"
"v_mfma_f32_16x16x32_bf16 v[132:135], v[232:235], v[136:139], v[132:135]\n"
"v_mfma_f32_16x16x32_bf16 v[132:135], v[234:237], v[24:27], v[132:135]\n"
"s_waitcnt lgkmcnt(0)\n"
"ds_read_b128 v[152:155], v174 offset:1392\n"
"v_max3_f32 v172, v172, v148, v149\n"
"v_max3_f32 v172, v172, v150, v151\n"
"v_mfma_f32_16x16x32_bf16 v[140:143], v[238:241], v[136:139], v[140:143]\n"
"v_mfma_f32_16x16x32_bf16 v[140:143], v[240:243], v[24:27], v[140:143]\n"
"s_waitcnt lgkmcnt(0)\n"
"ds_read_b128 v[144:147], v175 offset:4080\n"
"v_max3_f32 v172, v172, v132, v133\n"
"v_max3_f32 v172, v172, v134, v135\n"
"v_mfma_f32_16x16x32_bf16 v[152:155], v[244:247], v[136:139], v[152:155]\n"
"v_mfma_f32_16x16x32_bf16 v[152:155], v[246:249], v[24:27], v[152:155]\n"
"s_waitcnt lgkmcnt(0)\n"
"ds_read_b128 v[148:151], v176 offset:2704\n"
"v_max3_f32 v172, v172, v140, v141\n"
"v_max3_f32 v172, v172, v142, v143\n"
"v_mfma_f32_16x16x32_bf16 v[144:147], v[226:229], v[136:139], v[144:147]\n"
"v_mfma_f32_16x16x32_bf16 v[144:147], v[228:231], v[24:27], v[144:147]\n"
"s_waitcnt lgkmcnt(0)\n"
"ds_read_b128 v[132:135], v176 offset:2720\n"
"v_max3_f32 v172, v172, v152, v153\n"
"v_max3_f32 v172, v172, v154, v155\n"
"v_mfma_f32_16x16x32_bf16 v[148:151], v[184:187], v[136:139], v[148:151]\n"
"v_mfma_f32_16x16x32_bf16 v[148:151], v[186:189], v[24:27], v[148:151]\n"
"ds_read_b128 v[140:143], v175 offset:4048\n"
"s_nop 0\n"
"v_max3_f32 v172, v172, v144, v145\n"
"v_max3_f32 v172, v172, v146, v147\n"
"v_mov_b32 v173, v172\n"
"s_nop 1\n"
"v_permlane16_swap_b32 v172, v173\n"
"v_max_f32 v172, v172, v173\n"
"v_mov_b32 v173, v172\n"
"s_nop 1\n"
"v_permlane32_swap_b32 v172, v173\n"
"v_max_f32 v172, v172, v173\n"
"v_sub_f32 v148, v148, v172\n"
"v_exp_f32 v148, v148\n"
"v_sub_f32 v149, v149, v172\n"
"v_exp_f32 v149, v149\n"
"v_sub_f32 v150, v150, v172\n"
"v_exp_f32 v150, v150\n"
"v_sub_f32 v151, v151, v172\n"
"v_exp_f32 v151, v151\n"
"s_waitcnt lgkmcnt(1)\n"
"v_mfma_f32_16x16x32_bf16 v[132:135], v[190:193], v[136:139], v[132:135]\n"
"v_cvt_pk_bf16_f32 v148, v148, v149\n"
"v_cvt_pk_bf16_f32 v149, v150, v151\n"
"v_mfma_f32_16x16x32_bf16 v[132:135], v[192:195], v[24:27], v[132:135]\n"
"ds_read_b128 v[144:147], v175 offset:4064\n"
"ds_write_addtid_b32 v148 offset:53248\n"
"ds_write_addtid_b32 v149 offset:53504\n"
"s_waitcnt lgkmcnt(3)\n"
"v_mfma_f32_16x16x32_bf16 v[140:143], v[214:217], v[136:139], v[140:143]\n"
"v_mfma_f32_16x16x32_bf16 v[140:143], v[216:219], v[24:27], v[140:143]\n"
"ds_read_b128 v[152:155], v176 offset:2736\n"
"s_nop 0\n"
"v_sub_f32 v132, v132, v172\n"
"v_exp_f32 v132, v132\n"
"v_sub_f32 v133, v133, v172\n"
"v_exp_f32 v133, v133\n"
"v_sub_f32 v134, v134, v172\n"
"v_exp_f32 v134, v134\n"
"v_sub_f32 v135, v135, v172\n"
"v_exp_f32 v135, v135\n"
"v_sub_f32 v140, v140, v172\n"
"v_exp_f32 v140, v140\n"
"v_sub_f32 v141, v141, v172\n"
"v_exp_f32 v141, v141\n"
"v_sub_f32 v142, v142, v172\n"
"v_exp_f32 v142, v142\n"
"v_sub_f32 v143, v143, v172\n"
"v_exp_f32 v143, v143\n"
"v_cvt_pk_bf16_f32 v132, v132, v133\n"
"v_cvt_pk_bf16_f32 v133, v134, v135\n"
"v_cvt_pk_bf16_f32 v140, v140, v141\n"
"v_cvt_pk_bf16_f32 v141, v142, v143\n"
"ds_write_addtid_b32 v132 offset:53760\n"
"ds_write_addtid_b32 v133 offset:54016\n"
"s_waitcnt lgkmcnt(5)\n"
"v_mfma_f32_16x16x32_bf16 v[144:147], v[220:223], v[136:139], v[144:147]\n"
"ds_write_addtid_b32 v140 offset:54272\n"
"v_mfma_f32_16x16x32_bf16 v[144:147], v[222:225], v[24:27], v[144:147]\n"
"ds_read_b128 v[148:151], v174 offset:1360\n"
"ds_write_addtid_b32 v141 offset:54528\n"
"s_waitcnt lgkmcnt(5)\n"
"v_mfma_f32_16x16x32_bf16 v[152:155], v[196:199], v[136:139], v[152:155]\n"
"v_mfma_f32_16x16x32_bf16 v[152:155], v[198:201], v[24:27], v[152:155]\n"
"ds_read_b128 v[132:135], v174 offset:1376\n"
"s_nop 1\n"
"v_sub_f32 v144, v144, v172\n"
"v_exp_f32 v144, v144\n"
"v_sub_f32 v145, v145, v172\n"
"v_exp_f32 v145, v145\n"
"v_sub_f32 v146, v146, v172\n"
"v_exp_f32 v146, v146\n"
"v_sub_f32 v147, v147, v172\n"
"v_exp_f32 v147, v147\n"
"v_sub_f32 v152, v152, v172\n"
"v_exp_f32 v152, v152\n"
"v_sub_f32 v153, v153, v172\n"
"v_exp_f32 v153, v153\n"
"v_sub_f32 v154, v154, v172\n"
"v_exp_f32 v154, v154\n"
"v_cvt_pk_bf16_f32 v144, v144, v145\n"
"v_cvt_pk_bf16_f32 v145, v146, v147\n"
"v_sub_f32 v155, v155, v172\n"
"v_exp_f32 v155, v155\n"
"v_cvt_pk_bf16_f32 v152, v152, v153\n"
"v_cvt_pk_bf16_f32 v153, v154, v155\n"
"ds_write_addtid_b32 v144 offset:54784\n"
"ds_write_addtid_b32 v145 offset:55040\n"
"s_waitcnt lgkmcnt(4)\n"
"v_mfma_f32_16x16x32_bf16 v[148:151], v[232:235], v[136:139], v[148:151]\n"
"ds_write_addtid_b32 v152 offset:33792\n"
"v_mfma_f32_16x16x32_bf16 v[148:151], v[234:237], v[24:27], v[148:151]\n"
"ds_write_addtid_b32 v153 offset:34048\n"
"s_waitcnt lgkmcnt(4)\n"
"v_mfma_f32_16x16x32_bf16 v[132:135], v[238:241], v[136:139], v[132:135]\n"
"ds_read_b128 v[140:143], v174 offset:1392\n"
"v_mfma_f32_16x16x32_bf16 v[132:135], v[240:243], v[24:27], v[132:135]\n"
"s_waitcnt lgkmcnt(1)\n"
"ds_read_b128 v[144:147], v175 offset:4080\n"
"s_barrier\n"
"v_sub_f32 v148, v148, v172\n"
"v_exp_f32 v148, v148\n"
"v_sub_f32 v149, v149, v172\n"
"v_exp_f32 v149, v149\n"
"v_sub_f32 v150, v150, v172\n"
"v_exp_f32 v150, v150\n"
"v_sub_f32 v151, v151, v172\n"
"v_exp_f32 v151, v151\n"
"v_sub_f32 v132, v132, v172\n"
"v_exp_f32 v132, v132\n"
"v_cvt_pk_bf16_f32 v148, v148, v149\n"
"v_cvt_pk_bf16_f32 v149, v150, v151\n"
"v_sub_f32 v133, v133, v172\n"
"v_exp_f32 v133, v133\n"
"v_sub_f32 v134, v134, v172\n"
"v_exp_f32 v134, v134\n"
"v_sub_f32 v135, v135, v172\n"
"v_exp_f32 v135, v135\n"
"v_cvt_pk_bf16_f32 v132, v132, v133\n"
"v_cvt_pk_bf16_f32 v133, v134, v135\n"
"ds_write_addtid_b32 v148 offset:57856\n"
"ds_write_addtid_b32 v149 offset:58112\n"
"s_waitcnt lgkmcnt(3)\n"
"v_mfma_f32_16x16x32_bf16 v[140:143], v[244:247], v[136:139], v[140:143]\n"
"ds_write_addtid_b32 v132 offset:58368\n"
"v_mfma_f32_16x16x32_bf16 v[140:143], v[246:249], v[24:27], v[140:143]\n"
"ds_read_b128 v[232:235], v4 offset:43008\n"
"ds_write_addtid_b32 v133 offset:58624\n"
"s_waitcnt lgkmcnt(5)\n"
"v_mfma_f32_16x16x32_bf16 v[144:147], v[226:229], v[136:139], v[144:147]\n"
"v_mfma_f32_16x16x32_bf16 v[144:147], v[228:231], v[24:27], v[144:147]\n"
"s_waitcnt lgkmcnt(1)\n"
"v_mfma_f32_16x16x32_bf16 v[148:151], v[84:87], v[232:235], 0\n"
"ds_read_b128 v[152:155], v5 offset:43008\n"
"v_mfma_f32_16x16x32_bf16 v[156:159], v[88:91], v[232:235], 0\n"
"v_mfma_f32_16x16x32_bf16 v[160:163], v[92:95], v[232:235], 0\n"
"ds_read_b128 v[132:135], v6 offset:5120\n"
"v_sub_f32 v140, v140, v172\n"
"v_exp_f32 v140, v140\n"
"v_sub_f32 v141, v141, v172\n"
"v_exp_f32 v141, v141\n"
"v_sub_f32 v142, v142, v172\n"
"v_exp_f32 v142, v142\n"
"v_cvt_pk_bf16_f32 v140, v140, v141\n"
"v_sub_f32 v143, v143, v172\n"
"v_exp_f32 v143, v143\n"
"s_waitcnt lgkmcnt(1)\n"
"v_mfma_f32_16x16x32_bf16 v[148:151], v[96:99], v[152:155], v[148:151]\n"
"ds_write_addtid_b32 v140 offset:58880\n"
"v_cvt_pk_bf16_f32 v141, v142, v143\n"
"v_mfma_f32_16x16x32_bf16 v[156:159], v[100:103], v[152:155], v[156:159]\n"
"ds_read_b128 v[232:235], v4 offset:45056\n"
"ds_write_addtid_b32 v141 offset:59136\n"
"s_waitcnt lgkmcnt(3)\n"
"v_mfma_f32_16x16x32_bf16 v[160:163], v[132:135], v[152:155], v[160:163]\n"
"ds_read_b128 v[136:139], v6 offset:6144\n"
"v_sub_f32 v144, v144, v172\n"
"v_exp_f32 v144, v144\n"
"v_sub_f32 v145, v145, v172\n"
"v_exp_f32 v145, v145\n"
"v_sub_f32 v146, v146, v172\n"
"v_exp_f32 v146, v146\n"
"v_sub_f32 v147, v147, v172\n"
"v_exp_f32 v147, v147\n"
"v_cvt_pk_bf16_f32 v144, v144, v145\n"
"v_cvt_pk_bf16_f32 v145, v146, v147\n"
"s_waitcnt lgkmcnt(2)\n"
"v_mfma_f32_16x16x32_bf16 v[148:151], v[104:107], v[232:235], v[148:151]\n"
"ds_read_b128 v[132:135], v5 offset:45056\n"
"v_mfma_f32_16x16x32_bf16 v[156:159], v[108:111], v[232:235], v[156:159]\n"
"ds_write_addtid_b32 v144 offset:57344\n"
"s_waitcnt lgkmcnt(2)\n"
"v_mfma_f32_16x16x32_bf16 v[160:163], v[136:139], v[232:235], v[160:163]\n"
"ds_read_b128 v[140:143], v6 offset:7168\n"
"ds_write_addtid_b32 v145 offset:57600\n"
"s_waitcnt lgkmcnt(3)\n"
"v_mfma_f32_16x16x32_bf16 v[148:151], v[112:115], v[132:135], v[148:151]\n"
"ds_read_b128 v[152:155], v4 offset:47104\n"
"v_mfma_f32_16x16x32_bf16 v[156:159], v[116:119], v[132:135], v[156:159]\n"
"s_waitcnt lgkmcnt(1)\n"
"s_barrier\n"
"v_mfma_f32_16x16x32_bf16 v[160:163], v[140:143], v[132:135], v[160:163]\n"
"s_waitcnt lgkmcnt(0)\n"
"ds_read_b128 v[136:139], v6 offset:8192\n"
"v_mfma_f32_16x16x32_bf16 v[148:151], v[0:3], v[152:155], v[148:151]\n"
"ds_read_b128 v[144:147], v5 offset:47104\n"
"v_mfma_f32_16x16x32_bf16 v[156:159], v[120:123], v[152:155], v[156:159]\n"
"s_waitcnt lgkmcnt(1)\n"
"v_mfma_f32_16x16x32_bf16 v[160:163], v[136:139], v[152:155], v[160:163]\n"
"s_waitcnt lgkmcnt(0)\n"
"ds_read_b128 v[164:167], v6 offset:9216\n"
"v_mfma_f32_16x16x32_bf16 v[148:151], v[124:127], v[144:147], v[148:151]\n"
"ds_read_b128 v[132:135], v4 offset:36864\n"
"v_mfma_f32_16x16x32_bf16 v[156:159], v[128:131], v[144:147], v[156:159]\n"
"s_waitcnt lgkmcnt(1)\n"
"v_mfma_f32_16x16x32_bf16 v[160:163], v[164:167], v[144:147], v[160:163]\n"
"s_waitcnt lgkmcnt(0)\n"
"v_mfma_f32_16x16x32_bf16 v[232:235], v[8:11], v[132:135], 0\n"
"ds_read_b128 v[136:139], v5 offset:36864\n"
"v_mfma_f32_16x16x32_bf16 v[140:143], v[12:15], v[132:135], 0\n"
"v_mfma_f32_16x16x32_bf16 v[152:155], v[16:19], v[132:135], 0\n"
"s_waitcnt lgkmcnt(0)\n"
"v_mfma_f32_16x16x32_bf16 v[232:235], v[20:23], v[136:139], v[232:235]\n"
"ds_read_b128 v[168:171], v4 offset:38912\n"
"v_mul_f32 v148, s60, v148\n"
"v_mfma_f32_16x16x32_bf16 v[140:143], v[28:31], v[136:139], v[140:143]\n"
"v_mul_f32 v149, s60, v149\n"
"v_cvt_pk_bf16_f32 v148, v148, v149\n"
"v_mfma_f32_16x16x32_bf16 v[152:155], v[32:35], v[136:139], v[152:155]\n"
"v_mul_f32 v150, s60, v150\n"
"v_mul_f32 v151, s60, v151\n"
"v_cvt_pk_bf16_f32 v149, v150, v151\n"
"v_mul_f32 v156, s60, v156\n"
"v_mul_f32 v157, s60, v157\n"
"v_mul_f32 v158, s60, v158\n"
"v_mul_f32 v159, s60, v159\n"
"v_cvt_pk_bf16_f32 v150, v156, v157\n"
"v_cvt_pk_bf16_f32 v151, v158, v159\n"
"v_mul_f32 v160, s60, v160\n"
"v_mul_f32 v161, s60, v161\n"
"v_mul_f32 v162, s60, v162\n"
"s_waitcnt lgkmcnt(0)\n"
"v_mfma_f32_16x16x32_bf16 v[232:235], v[36:39], v[168:171], v[232:235]\n"
"ds_read_b128 v[132:135], v5 offset:38912\n"
"v_mul_f32 v163, s60, v163\n"
"v_mfma_f32_16x16x32_bf16 v[140:143], v[40:43], v[168:171], v[140:143]\n"
"v_cvt_pk_bf16_f32 v26, v160, v161\n"
"v_cvt_pk_bf16_f32 v27, v162, v163\n"
"v_mfma_f32_16x16x32_bf16 v[152:155], v[44:47], v[168:171], v[152:155]\n"
"s_waitcnt lgkmcnt(0)\n"
"v_bfe_u32 v177, v6, 2, 6\n"
"ds_read_b32 v177, v177 offset:1664\n"
"v_mfma_f32_16x16x32_bf16 v[232:235], v[48:51], v[132:135], v[232:235]\n"
"ds_read_b128 v[136:139], v4 offset:40960\n"
"v_mfma_f32_16x16x32_bf16 v[140:143], v[52:55], v[132:135], v[140:143]\n"
"s_add_u32 s69, s50, 4294965920\n"
"v_mfma_f32_16x16x32_bf16 v[152:155], v[56:59], v[132:135], v[152:155]\n"
"s_waitcnt lgkmcnt(1)\n"
"v_add_u32_sdwa v176, v177, v7 dst_sel:DWORD dst_unused:UNUSED_PAD src0_sel:WORD_0 src1_sel:DWORD\n"
"s_nop 0\n"
"v_add_u32_sdwa v177, sext(v177), v176 dst_sel:DWORD dst_unused:UNUSED_PAD src0_sel:WORD_1 src1_sel:DWORD\n"
"s_waitcnt lgkmcnt(0)\n"
"v_mfma_f32_16x16x32_bf16 v[232:235], v[60:63], v[136:139], v[232:235]\n"
"ds_read_b128 v[144:147], v5 offset:40960\n"
"v_max_u32 v175, s69, v176\n"
"v_mfma_f32_16x16x32_bf16 v[140:143], v[64:67], v[136:139], v[140:143]\n"
"v_mfma_f32_16x16x32_bf16 v[152:155], v[68:71], v[136:139], v[152:155]\n"
"s_add_u32 s70, s50, 4294966960\n"
"v_max_u32 v174, s69, v177\n"
"s_waitcnt lgkmcnt(0)\n"
"v_mfma_f32_16x16x32_bf16 v[232:235], v[72:75], v[144:147], v[232:235]\n"
"ds_read_b128 v[132:135], v175 offset:1392\n"
"v_mfma_f32_16x16x32_bf16 v[140:143], v[76:79], v[144:147], v[140:143]\n"
"v_min_u32 v173, s70, v176\n"
"v_mfma_f32_16x16x32_bf16 v[152:155], v[80:83], v[144:147], v[152:155]\n"
"s_waitcnt lgkmcnt(0)\n"
"ds_read_b128 v[136:139], v174 offset:1408\n"
"v_mfma_f32_16x16x32_bf16 v[132:135], v[244:247], v[148:151], v[132:135]\n"
"v_mfma_f32_16x16x32_bf16 v[132:135], v[246:249], v[24:27], v[132:135]\n"
"ds_read_b128 v[156:159], v173 offset:2720\n"
"v_cvt_pk_bf16_f32 v232, v232, v233\n"
"v_cvt_pk_bf16_f32 v233, v234, v235\n"
"v_cvt_pk_bf16_f32 v234, v140, v141\n"
"v_cvt_pk_bf16_f32 v235, v142, v143\n"
"v_cvt_pk_bf16_f32 v236, v152, v153\n"
"v_cvt_pk_bf16_f32 v237, v154, v155\n"
"s_waitcnt lgkmcnt(1)\n"
"v_mfma_f32_16x16x32_bf16 v[136:139], v[250:253], v[148:151], v[136:139]\n"
"v_max3_f32 v170, v132, v133, v134\n"
"v_max_f32 v170, v170, v135\n"
"v_mfma_f32_16x16x32_bf16 v[136:139], v[252:255], v[24:27], v[136:139]\n"
"s_waitcnt lgkmcnt(0)\n"
"ds_read_b128 v[140:143], v173 offset:2736\n"
"v_min_u32 v172, s70, v177\n"
"v_mfma_f32_16x16x32_bf16 v[156:159], v[190:193], v[148:151], v[156:159]\n"
"s_add_u32 s71, s50, 4294965616\n"
"v_mfma_f32_16x16x32_bf16 v[156:159], v[192:195], v[24:27], v[156:159]\n"
"s_waitcnt lgkmcnt(0)\n"
"ds_read_b128 v[132:135], v172 offset:2752\n"
"v_max3_f32 v170, v170, v136, v137\n"
"v_max3_f32 v170, v170, v138, v139\n"
"v_mfma_f32_16x16x32_bf16 v[140:143], v[196:199], v[148:151], v[140:143]\n"
"v_min_u32 v169, s71, v176\n"
"v_mfma_f32_16x16x32_bf16 v[140:143], v[198:201], v[24:27], v[140:143]\n"
"s_waitcnt lgkmcnt(0)\n"
"ds_read_b128 v[144:147], v175 offset:1376\n"
"v_max3_f32 v170, v170, v156, v157\n"
"v_max3_f32 v170, v170, v158, v159\n"
"v_mfma_f32_16x16x32_bf16 v[132:135], v[202:205], v[148:151], v[132:135]\n"
"v_mfma_f32_16x16x32_bf16 v[132:135], v[204:207], v[24:27], v[132:135]\n"
"s_waitcnt lgkmcnt(0)\n"
"ds_read_b128 v[136:139], v169 offset:4064\n"
"v_max3_f32 v170, v170, v140, v141\n"
"v_max3_f32 v170, v170, v142, v143\n"
"v_mfma_f32_16x16x32_bf16 v[144:147], v[238:241], v[148:151], v[144:147]\n"
"v_min_u32 v168, s71, v177\n"
"v_mfma_f32_16x16x32_bf16 v[144:147], v[240:243], v[24:27], v[144:147]\n"
"s_waitcnt lgkmcnt(0)\n"
"ds_read_b128 v[152:155], v169 offset:4080\n"
"v_max3_f32 v170, v170, v132, v133\n"
"v_max3_f32 v170, v170, v134, v135\n"
"v_mfma_f32_16x16x32_bf16 v[136:139], v[220:223], v[148:151], v[136:139]\n"
"v_mfma_f32_16x16x32_bf16 v[136:139], v[222:225], v[24:27], v[136:139]\n"
"s_waitcnt lgkmcnt(0)\n"
"ds_read_b128 v[140:143], v168 offset:4096\n"
"v_max3_f32 v170, v170, v144, v145\n"
"v_max3_f32 v170, v170, v146, v147\n"
"v_mfma_f32_16x16x32_bf16 v[152:155], v[226:229], v[148:151], v[152:155]\n"
"v_mfma_f32_16x16x32_bf16 v[152:155], v[228:231], v[24:27], v[152:155]\n"
"s_waitcnt lgkmcnt(0)\n"
"ds_read_b128 v[132:135], v175 offset:1392\n"
"v_max3_f32 v170, v170, v136, v137\n"
"v_max3_f32 v170, v170, v138, v139\n"
"v_mfma_f32_16x16x32_bf16 v[140:143], v[232:235], v[148:151], v[140:143]\n"
"v_mfma_f32_16x16x32_bf16 v[140:143], v[234:237], v[24:27], v[140:143]\n"
"s_waitcnt lgkmcnt(0)\n"
"ds_read_b128 v[144:147], v174 offset:1408\n"
"v_max3_f32 v170, v170, v152, v153\n"
"v_max3_f32 v170, v170, v154, v155\n"
"v_mfma_f32_16x16x32_bf16 v[132:135], v[244:247], v[148:151], v[132:135]\n"
"v_mfma_f32_16x16x32_bf16 v[132:135], v[246:249], v[24:27], v[132:135]\n"
"ds_read_b128 v[136:139], v173 offset:2720\n"
"s_nop 0\n"
"v_max3_f32 v170, v170, v140, v141\n"
"v_max3_f32 v170, v170, v142, v143\n"
"v_mov_b32 v171, v170\n"
"s_nop 1\n"
"v_permlane16_swap_b32 v170, v171\n"
"v_max_f32 v170, v170, v171\n"
"v_mov_b32 v171, v170\n"
"s_nop 1\n"
"v_permlane32_swap_b32 v170, v171\n"
"v_max_f32 v170, v170, v171\n"
"v_sub_f32 v132, v132, v170\n"
"v_exp_f32 v132, v132\n"
"v_sub_f32 v133, v133, v170\n"
"v_exp_f32 v133, v133\n"
"v_sub_f32 v134, v134, v170\n"
"v_exp_f32 v134, v134\n"
"v_sub_f32 v135, v135, v170\n"
"v_exp_f32 v135, v135\n"
"s_waitcnt lgkmcnt(1)\n"
"v_mfma_f32_16x16x32_bf16 v[144:147], v[250:253], v[148:151], v[144:147]\n"
"v_cvt_pk_bf16_f32 v132, v132, v133\n"
"v_cvt_pk_bf16_f32 v133, v134, v135\n"
"v_mfma_f32_16x16x32_bf16 v[144:147], v[252:255], v[24:27], v[144:147]\n"
"ds_read_b128 v[244:247], v173 offset:2736\n"
"ds_write_addtid_b32 v132 offset:55296\n"
"ds_write_addtid_b32 v133 offset:55552\n"
"s_waitcnt lgkmcnt(3)\n"
"v_mfma_f32_16x16x32_bf16 v[136:139], v[190:193], v[148:151], v[136:139]\n"
"v_mfma_f32_16x16x32_bf16 v[136:139], v[192:195], v[24:27], v[136:139]\n"
"ds_read_b128 v[248:251], v172 offset:2752\n"
"s_nop 0\n"
"v_sub_f32 v144, v144, v170\n"
"v_exp_f32 v144, v144\n"
"v_sub_f32 v145, v145, v170\n"
"v_exp_f32 v145, v145\n"
"v_sub_f32 v146, v146, v170\n"
"v_exp_f32 v146, v146\n"
"v_sub_f32 v147, v147, v170\n"
"v_exp_f32 v147, v147\n"
"v_sub_f32 v138, v138, v170\n"
"v_exp_f32 v138, v138\n"
"v_sub_f32 v139, v139, v170\n"
"v_exp_f32 v139, v139\n"
"v_cvt_pk_bf16_f32 v144, v144, v145\n"
"v_cvt_pk_bf16_f32 v145, v146, v147\n"
"v_cvt_pk_bf16_f32 v137, v138, v139\n"
"ds_write_addtid_b32 v144 offset:55808\n"
"ds_write_addtid_b32 v145 offset:56064\n"
"s_waitcnt lgkmcnt(5)\n"
"v_mfma_f32_16x16x32_bf16 v[244:247], v[196:199], v[148:151], v[244:247]\n"
"ds_write_addtid_b32 v137 offset:56576\n"
"v_mfma_f32_16x16x32_bf16 v[244:247], v[198:201], v[24:27], v[244:247]\n"
"s_waitcnt lgkmcnt(3)\n"
"ds_read_b128 v[252:255], v175 offset:1376\n"
"v_mfma_f32_16x16x32_bf16 v[248:251], v[202:205], v[148:151], v[248:251]\n"
"v_mfma_f32_16x16x32_bf16 v[248:251], v[204:207], v[24:27], v[248:251]\n"
"ds_read_b128 v[132:135], v169 offset:4064\n"
"s_nop 2\n"
"v_sub_f32 v244, v244, v170\n"
"v_exp_f32 v244, v244\n"
"v_sub_f32 v245, v245, v170\n"
"v_exp_f32 v245, v245\n"
"v_sub_f32 v246, v246, v170\n"
"v_exp_f32 v246, v246\n"
"v_sub_f32 v247, v247, v170\n"
"v_exp_f32 v247, v247\n"
"v_sub_f32 v248, v248, v170\n"
"v_exp_f32 v248, v248\n"
"v_sub_f32 v249, v249, v170\n"
"v_exp_f32 v249, v249\n"
"v_cvt_pk_bf16_f32 v244, v244, v245\n"
"v_sub_f32 v250, v250, v170\n"
"v_exp_f32 v250, v250\n"
"v_cvt_pk_bf16_f32 v245, v246, v247\n"
"v_sub_f32 v251, v251, v170\n"
"v_exp_f32 v251, v251\n"
"v_cvt_pk_bf16_f32 v248, v248, v249\n"
"v_cvt_pk_bf16_f32 v249, v250, v251\n"
"ds_write_addtid_b32 v244 offset:56832\n"
"ds_write_addtid_b32 v245 offset:57088\n"
"s_waitcnt lgkmcnt(3)\n"
"v_mfma_f32_16x16x32_bf16 v[252:255], v[238:241], v[148:151], v[252:255]\n"
"ds_write_addtid_b32 v248 offset:34304\n"
"v_mfma_f32_16x16x32_bf16 v[252:255], v[240:243], v[24:27], v[252:255]\n"
"ds_read_b128 v[136:139], v169 offset:4080\n"
"ds_write_addtid_b32 v249 offset:34560\n"
"s_waitcnt lgkmcnt(5)\n"
"v_mfma_f32_16x16x32_bf16 v[132:135], v[220:223], v[148:151], v[132:135]\n"
"v_mfma_f32_16x16x32_bf16 v[132:135], v[222:225], v[24:27], v[132:135]\n"
"s_waitcnt lgkmcnt(0)\n"
"ds_read_b128 v[244:247], v168 offset:4096\n"
"s_barrier\n"
"v_sub_f32 v254, v254, v170\n"
"v_exp_f32 v254, v254\n"
"v_sub_f32 v255, v255, v170\n"
"v_exp_f32 v255, v255\n"
"s_nop 0\n"
"v_cvt_pk_bf16_f32 v253, v254, v255\n"
"ds_write_addtid_b32 v253 offset:59136\n"
"v_mfma_f32_16x16x32_bf16 v[136:139], v[226:229], v[148:151], v[136:139]\n"
"v_sub_f32 v134, v134, v170\n"
"v_exp_f32 v134, v134\n"
"v_sub_f32 v135, v135, v170\n"
"v_exp_f32 v135, v135\n"
"v_mfma_f32_16x16x32_bf16 v[136:139], v[228:231], v[24:27], v[136:139]\n"
"ds_read_b128 v[238:241], v4 offset:24576\n"
"v_cvt_pk_bf16_f32 v133, v134, v135\n"
"ds_write_addtid_b32 v133 offset:57600\n"
"s_waitcnt lgkmcnt(3)\n"
"v_mfma_f32_16x16x32_bf16 v[244:247], v[232:235], v[148:151], v[244:247]\n"
"v_mfma_f32_16x16x32_bf16 v[244:247], v[234:237], v[24:27], v[244:247]\n"
"s_waitcnt lgkmcnt(1)\n"
"v_mfma_f32_16x16x32_bf16 v[250:253], v[84:87], v[238:241], 0\n"
"ds_read_b128 v[140:143], v5 offset:24576\n"
"v_mfma_f32_16x16x32_bf16 v[144:147], v[88:91], v[238:241], 0\n"
"v_sub_f32 v136, v136, v170\n"
"v_exp_f32 v136, v136\n"
"v_sub_f32 v137, v137, v170\n"
"v_exp_f32 v137, v137\n"
"v_sub_f32 v138, v138, v170\n"
"v_exp_f32 v138, v138\n"
"v_mfma_f32_16x16x32_bf16 v[132:135], v[92:95], v[238:241], 0\n"
"ds_read_b128 v[148:151], v6 offset:5120\n"
"v_sub_f32 v139, v139, v170\n"
"v_exp_f32 v139, v139\n"
"v_cvt_pk_bf16_f32 v136, v136, v137\n"
"v_cvt_pk_bf16_f32 v137, v138, v139\n"
"s_waitcnt lgkmcnt(1)\n"
"v_mfma_f32_16x16x32_bf16 v[250:253], v[96:99], v[140:143], v[250:253]\n"
"ds_write_addtid_b32 v136 offset:57856\n"
"v_mfma_f32_16x16x32_bf16 v[144:147], v[100:103], v[140:143], v[144:147]\n"
"ds_read_b128 v[238:241], v4 offset:26624\n"
"v_sub_f32 v244, v244, v170\n"
"v_exp_f32 v244, v244\n"
"v_sub_f32 v245, v245, v170\n"
"v_exp_f32 v245, v245\n"
"s_waitcnt lgkmcnt(2)\n"
"v_mfma_f32_16x16x32_bf16 v[132:135], v[148:151], v[140:143], v[132:135]\n"
"ds_write_addtid_b32 v137 offset:58112\n"
"v_sub_f32 v246, v246, v170\n"
"v_exp_f32 v246, v246\n"
"v_sub_f32 v247, v247, v170\n"
"v_exp_f32 v247, v247\n"
"v_cvt_pk_bf16_f32 v244, v244, v245\n"
"v_cvt_pk_bf16_f32 v245, v246, v247\n"
"s_waitcnt lgkmcnt(1)\n"
"v_mfma_f32_16x16x32_bf16 v[250:253], v[104:107], v[238:241], v[250:253]\n"
"ds_read_b128 v[136:139], v6 offset:6144\n"
"v_mfma_f32_16x16x32_bf16 v[144:147], v[108:111], v[238:241], v[144:147]\n"
"ds_read_b128 v[140:143], v5 offset:26624\n"
"ds_write_addtid_b32 v244 offset:58368\n"
"ds_write_addtid_b32 v245 offset:58624\n"
"s_waitcnt lgkmcnt(3)\n"
"v_mfma_f32_16x16x32_bf16 v[132:135], v[136:139], v[238:241], v[132:135]\n"
"s_waitcnt lgkmcnt(2)\n"
"ds_read_b128 v[246:249], v6 offset:7168\n"
"v_mfma_f32_16x16x32_bf16 v[250:253], v[112:115], v[140:143], v[250:253]\n"
"s_waitcnt lgkmcnt(1)\n"
"s_barrier\n"
"ds_read_b128 v[148:151], v4 offset:28672\n"
"v_mfma_f32_16x16x32_bf16 v[144:147], v[116:119], v[140:143], v[144:147]\n"
"s_waitcnt lgkmcnt(1)\n"
"v_mfma_f32_16x16x32_bf16 v[132:135], v[246:249], v[140:143], v[132:135]\n"
"s_waitcnt lgkmcnt(0)\n"
"ds_read_b128 v[238:241], v6 offset:8192\n"
"v_mfma_f32_16x16x32_bf16 v[250:253], v[0:3], v[148:151], v[250:253]\n"
"ds_read_b128 v[242:245], v5 offset:28672\n"
"v_mfma_f32_16x16x32_bf16 v[144:147], v[120:123], v[148:151], v[144:147]\n"
"s_waitcnt lgkmcnt(1)\n"
"v_mfma_f32_16x16x32_bf16 v[132:135], v[238:241], v[148:151], v[132:135]\n"
"s_waitcnt lgkmcnt(0)\n"
"ds_read_b128 v[136:139], v6 offset:9216\n"
"v_mfma_f32_16x16x32_bf16 v[250:253], v[124:127], v[242:245], v[250:253]\n"
"ds_read_b128 v[246:249], v4 offset:30720\n"
"v_mfma_f32_16x16x32_bf16 v[144:147], v[128:131], v[242:245], v[144:147]\n"
"s_waitcnt lgkmcnt(1)\n"
"v_mfma_f32_16x16x32_bf16 v[132:135], v[136:139], v[242:245], v[132:135]\n"
"s_waitcnt lgkmcnt(0)\n"
"ds_read_b128 v[140:143], v5 offset:30720\n"
"v_mfma_f32_16x16x32_bf16 v[238:241], v[8:11], v[246:249], 0\n"
"v_mfma_f32_16x16x32_bf16 v[148:151], v[12:15], v[246:249], 0\n"
"v_mfma_f32_16x16x32_bf16 v[152:155], v[16:19], v[246:249], 0\n"
"ds_read_b128 v[156:159], v4 offset:32768\n"
"v_mul_f32 v250, s60, v250\n"
"v_mul_f32 v251, s60, v251\n"
"v_cvt_pk_bf16_f32 v250, v250, v251\n"
"v_mul_f32 v252, s60, v252\n"
"v_mul_f32 v253, s60, v253\n"
"v_cvt_pk_bf16_f32 v251, v252, v253\n"
"v_mul_f32 v144, s60, v144\n"
"s_waitcnt lgkmcnt(1)\n"
"v_mfma_f32_16x16x32_bf16 v[238:241], v[20:23], v[140:143], v[238:241]\n"
"v_mul_f32 v145, s60, v145\n"
"v_mul_f32 v146, s60, v146\n"
"v_mfma_f32_16x16x32_bf16 v[148:151], v[28:31], v[140:143], v[148:151]\n"
"v_mul_f32 v147, s60, v147\n"
"v_cvt_pk_bf16_f32 v252, v144, v145\n"
"v_mfma_f32_16x16x32_bf16 v[152:155], v[32:35], v[140:143], v[152:155]\n"
"s_waitcnt lgkmcnt(0)\n"
"ds_read_b128 v[242:245], v5 offset:32768\n"
"v_cvt_pk_bf16_f32 v253, v146, v147\n"
"v_mfma_f32_16x16x32_bf16 v[238:241], v[36:39], v[156:159], v[238:241]\n"
"v_mul_f32 v132, s60, v132\n"
"v_mul_f32 v133, s60, v133\n"
"v_mfma_f32_16x16x32_bf16 v[148:151], v[40:43], v[156:159], v[148:151]\n"
"v_mul_f32 v134, s60, v134\n"
"v_mul_f32 v135, s60, v135\n"
"v_mfma_f32_16x16x32_bf16 v[152:155], v[44:47], v[156:159], v[152:155]\n"
"s_waitcnt lgkmcnt(0)\n"
"ds_read_b128 v[136:139], v4 offset:34816\n"
"v_cvt_pk_bf16_f32 v26, v132, v133\n"
"v_cvt_pk_bf16_f32 v27, v134, v135\n"
"v_mfma_f32_16x16x32_bf16 v[238:241], v[48:51], v[242:245], v[238:241]\n"
"v_mfma_f32_16x16x32_bf16 v[148:151], v[52:55], v[242:245], v[148:151]\n"
"v_mfma_f32_16x16x32_bf16 v[152:155], v[56:59], v[242:245], v[152:155]\n"
"s_waitcnt lgkmcnt(0)\n"
"ds_read_b128 v[140:143], v5 offset:34816\n"
"v_mfma_f32_16x16x32_bf16 v[238:241], v[60:63], v[136:139], v[238:241]\n"
"v_mfma_f32_16x16x32_bf16 v[148:151], v[64:67], v[136:139], v[148:151]\n"
"v_mfma_f32_16x16x32_bf16 v[152:155], v[68:71], v[136:139], v[152:155]\n"
"s_waitcnt lgkmcnt(0)\n"
"ds_read_b128 v[132:135], v4 offset:49152\n"
"v_mfma_f32_16x16x32_bf16 v[238:241], v[72:75], v[140:143], v[238:241]\n"
"v_mfma_f32_16x16x32_bf16 v[148:151], v[76:79], v[140:143], v[148:151]\n"
"v_mfma_f32_16x16x32_bf16 v[152:155], v[80:83], v[140:143], v[152:155]\n"
"s_waitcnt lgkmcnt(0)\n"
"ds_read_b128 v[144:147], v5 offset:49152\n"
"v_mfma_f32_16x16x32_bf16 v[244:247], v[8:11], v[132:135], 0\n"
"v_mfma_f32_16x16x32_bf16 v[136:139], v[12:15], v[132:135], 0\n"
"v_mfma_f32_16x16x32_bf16 v[156:159], v[16:19], v[132:135], 0\n"
"s_waitcnt lgkmcnt(0)\n"
"ds_read_b128 v[160:163], v4 offset:51200\n"
"v_mfma_f32_16x16x32_bf16 v[244:247], v[20:23], v[144:147], v[244:247]\n"
"v_cvt_pk_bf16_f32 v238, v238, v239\n"
"v_cvt_pk_bf16_f32 v239, v240, v241\n"
"v_cvt_pk_bf16_f32 v240, v148, v149\n"
"v_cvt_pk_bf16_f32 v241, v150, v151\n"
"v_cvt_pk_bf16_f32 v242, v152, v153\n"
"v_cvt_pk_bf16_f32 v243, v154, v155\n"
"v_mfma_f32_16x16x32_bf16 v[136:139], v[28:31], v[144:147], v[136:139]\n"
"v_mfma_f32_16x16x32_bf16 v[156:159], v[32:35], v[144:147], v[156:159]\n"
"s_waitcnt lgkmcnt(0)\n"
"ds_read_b128 v[132:135], v5 offset:51200\n"
"v_mfma_f32_16x16x32_bf16 v[244:247], v[36:39], v[160:163], v[244:247]\n"
"v_bfe_u32 v255, v6, 2, 6\n"
"v_mfma_f32_16x16x32_bf16 v[136:139], v[40:43], v[160:163], v[136:139]\n"
"ds_read_b32 v255, v255 offset:1728\n"
"v_mfma_f32_16x16x32_bf16 v[156:159], v[44:47], v[160:163], v[156:159]\n"
"s_add_u32 s72, s50, 4294964592\n"
"ds_read_b128 v[140:143], v4 offset:53248\n"
"s_waitcnt lgkmcnt(2)\n"
"v_mfma_f32_16x16x32_bf16 v[244:247], v[48:51], v[132:135], v[244:247]\n"
"v_mfma_f32_16x16x32_bf16 v[136:139], v[52:55], v[132:135], v[136:139]\n"
"v_mfma_f32_16x16x32_bf16 v[156:159], v[56:59], v[132:135], v[156:159]\n"
"s_waitcnt lgkmcnt(1)\n"
"ds_read_b128 v[144:147], v5 offset:53248\n"
"v_add_u32_sdwa v254, v255, v7 dst_sel:DWORD dst_unused:UNUSED_PAD src0_sel:WORD_0 src1_sel:DWORD\n"
"s_waitcnt lgkmcnt(1)\n"
"v_mfma_f32_16x16x32_bf16 v[244:247], v[60:63], v[140:143], v[244:247]\n"
"v_max_u32 v177, s72, v254\n"
"v_add_u32_sdwa v255, sext(v255), v254 dst_sel:DWORD dst_unused:UNUSED_PAD src0_sel:WORD_1 src1_sel:DWORD\n"
"v_mfma_f32_16x16x32_bf16 v[136:139], v[64:67], v[140:143], v[136:139]\n"
"v_mfma_f32_16x16x32_bf16 v[156:159], v[68:71], v[140:143], v[156:159]\n"
"s_waitcnt lgkmcnt(0)\n"
"ds_read_b128 v[132:135], v177 offset:2704\n"
"v_mfma_f32_16x16x32_bf16 v[244:247], v[72:75], v[144:147], v[244:247]\n"
"v_mfma_f32_16x16x32_bf16 v[136:139], v[76:79], v[144:147], v[136:139]\n"
"s_add_u32 s73, s50, 4294965632\n"
"ds_read_b128 v[148:151], v177 offset:2720\n"
"v_max_u32 v176, s72, v255\n"
"v_mfma_f32_16x16x32_bf16 v[156:159], v[80:83], v[144:147], v[156:159]\n"
"s_waitcnt lgkmcnt(1)\n"
"v_mfma_f32_16x16x32_bf16 v[132:135], v[184:187], v[250:253], v[132:135]\n"
"ds_read_b128 v[140:143], v177 offset:2736\n"
"v_min_u32 v175, s73, v254\n"
"v_cvt_pk_bf16_f32 v244, v244, v245\n"
"v_cvt_pk_bf16_f32 v245, v246, v247\n"
"v_cvt_pk_bf16_f32 v246, v136, v137\n"
"v_cvt_pk_bf16_f32 v247, v138, v139\n"
"v_mfma_f32_16x16x32_bf16 v[132:135], v[186:189], v[24:27], v[132:135]\n"
"s_waitcnt lgkmcnt(1)\n"
"v_cvt_pk_bf16_f32 v248, v156, v157\n"
"v_cvt_pk_bf16_f32 v249, v158, v159\n"
"v_mfma_f32_16x16x32_bf16 v[148:151], v[190:193], v[250:253], v[148:151]\n"
"ds_read_b128 v[144:147], v176 offset:2752\n"
"v_mfma_f32_16x16x32_bf16 v[148:151], v[192:195], v[24:27], v[148:151]\n"
"s_waitcnt lgkmcnt(1)\n"
"s_nop 0\n"
"v_max3_f32 v254, v132, v133, v134\n"
"v_max_f32 v254, v254, v135\n"
"v_mfma_f32_16x16x32_bf16 v[140:143], v[196:199], v[250:253], v[140:143]\n"
"ds_read_b128 v[136:139], v175 offset:4048\n"
"v_mfma_f32_16x16x32_bf16 v[140:143], v[198:201], v[24:27], v[140:143]\n"
"s_waitcnt lgkmcnt(1)\n"
"v_max3_f32 v254, v254, v148, v149\n"
"v_max3_f32 v254, v254, v150, v151\n"
"v_mfma_f32_16x16x32_bf16 v[144:147], v[202:205], v[250:253], v[144:147]\n"
"ds_read_b128 v[132:135], v175 offset:4064\n"
"v_min_u32 v174, s73, v255\n"
"v_mfma_f32_16x16x32_bf16 v[144:147], v[204:207], v[24:27], v[144:147]\n"
"s_waitcnt lgkmcnt(1)\n"
"v_max3_f32 v254, v254, v140, v141\n"
"v_max3_f32 v254, v254, v142, v143\n"
"v_mfma_f32_16x16x32_bf16 v[136:139], v[214:217], v[250:253], v[136:139]\n"
"ds_read_b128 v[148:151], v175 offset:4080\n"
"v_mfma_f32_16x16x32_bf16 v[136:139], v[216:219], v[24:27], v[136:139]\n"
"s_waitcnt lgkmcnt(1)\n"
"s_nop 0\n"
"v_max3_f32 v254, v254, v144, v145\n"
"v_max3_f32 v254, v254, v146, v147\n"
"v_mfma_f32_16x16x32_bf16 v[132:135], v[220:223], v[250:253], v[132:135]\n"
"ds_read_b128 v[140:143], v174 offset:4096\n"
"v_mfma_f32_16x16x32_bf16 v[132:135], v[222:225], v[24:27], v[132:135]\n"
"s_waitcnt lgkmcnt(1)\n"
"v_max3_f32 v254, v254, v136, v137\n"
"v_max3_f32 v254, v254, v138, v139\n"
"v_mfma_f32_16x16x32_bf16 v[148:151], v[226:229], v[250:253], v[148:151]\n"
"ds_read_b128 v[144:147], v177 offset:2704\n"
"v_mfma_f32_16x16x32_bf16 v[148:151], v[228:231], v[24:27], v[148:151]\n"
"s_waitcnt lgkmcnt(1)\n"
"s_nop 0\n"
"v_max3_f32 v254, v254, v132, v133\n"
"v_max3_f32 v254, v254, v134, v135\n"
"v_mfma_f32_16x16x32_bf16 v[140:143], v[232:235], v[250:253], v[140:143]\n"
"ds_read_b128 v[136:139], v177 offset:2720\n"
"v_mfma_f32_16x16x32_bf16 v[140:143], v[234:237], v[24:27], v[140:143]\n"
"s_waitcnt lgkmcnt(1)\n"
"v_max3_f32 v254, v254, v148, v149\n"
"v_max3_f32 v254, v254, v150, v151\n"
"v_mfma_f32_16x16x32_bf16 v[144:147], v[184:187], v[250:253], v[144:147]\n"
"ds_read_b128 v[132:135], v177 offset:2736\n"
"v_mfma_f32_16x16x32_bf16 v[144:147], v[186:189], v[24:27], v[144:147]\n"
"s_nop 1\n"
"v_max3_f32 v254, v254, v140, v141\n"
"v_max3_f32 v254, v254, v142, v143\n"
"v_mov_b32 v255, v254\n"
"s_nop 1\n"
"v_permlane16_swap_b32 v254, v255\n"
"v_max_f32 v254, v254, v255\n"
"v_mov_b32 v255, v254\n"
"s_nop 1\n"
"v_permlane32_swap_b32 v254, v255\n"
"v_max_f32 v254, v254, v255\n"
"v_sub_f32 v146, v146, v254\n"
"v_exp_f32 v146, v146\n"
"v_sub_f32 v147, v147, v254\n"
"v_exp_f32 v147, v147\n"
"s_waitcnt lgkmcnt(1)\n"
"v_mfma_f32_16x16x32_bf16 v[136:139], v[190:193], v[250:253], v[136:139]\n"
"ds_read_b128 v[140:143], v176 offset:2752\n"
"v_cvt_pk_bf16_f32 v145, v146, v147\n"
"v_mfma_f32_16x16x32_bf16 v[136:139], v[192:195], v[24:27], v[136:139]\n"
"ds_write_addtid_b32 v145 offset:53504\n"
"s_waitcnt lgkmcnt(2)\n"
"v_mfma_f32_16x16x32_bf16 v[132:135], v[196:199], v[250:253], v[132:135]\n"
"ds_read_b128 v[148:151], v175 offset:4048\n"
"v_mfma_f32_16x16x32_bf16 v[132:135], v[198:201], v[24:27], v[132:135]\n"
"s_nop 2\n"
"v_sub_f32 v136, v136, v254\n"
"v_exp_f32 v136, v136\n"
"v_sub_f32 v137, v137, v254\n"
"v_exp_f32 v137, v137\n"
"v_sub_f32 v138, v138, v254\n"
"v_exp_f32 v138, v138\n"
"v_sub_f32 v139, v139, v254\n"
"v_exp_f32 v139, v139\n"
"v_sub_f32 v132, v132, v254\n"
"v_exp_f32 v132, v132\n"
"v_sub_f32 v133, v133, v254\n"
"v_exp_f32 v133, v133\n"
"v_sub_f32 v134, v134, v254\n"
"v_exp_f32 v134, v134\n"
"v_sub_f32 v135, v135, v254\n"
"v_exp_f32 v135, v135\n"
"v_cvt_pk_bf16_f32 v136, v136, v137\n"
"v_cvt_pk_bf16_f32 v137, v138, v139\n"
"v_cvt_pk_bf16_f32 v132, v132, v133\n"
"v_cvt_pk_bf16_f32 v133, v134, v135\n"
"ds_write_addtid_b32 v136 offset:53760\n"
"ds_write_addtid_b32 v137 offset:54016\n"
"s_waitcnt lgkmcnt(4)\n"
"v_mfma_f32_16x16x32_bf16 v[140:143], v[202:205], v[250:253], v[140:143]\n"
"ds_read_b128 v[144:147], v175 offset:4064\n"
"v_mfma_f32_16x16x32_bf16 v[140:143], v[204:207], v[24:27], v[140:143]\n"
"ds_write_addtid_b32 v132 offset:54272\n"
"ds_write_addtid_b32 v133 offset:54528\n"
"s_waitcnt lgkmcnt(5)\n"
"v_mfma_f32_16x16x32_bf16 v[148:151], v[214:217], v[250:253], v[148:151]\n"
"ds_read_b128 v[136:139], v175 offset:4080\n"
"v_mfma_f32_16x16x32_bf16 v[148:151], v[216:219], v[24:27], v[148:151]\n"
"s_nop 1\n"
"v_sub_f32 v140, v140, v254\n"
"v_exp_f32 v140, v140\n"
"v_sub_f32 v141, v141, v254\n"
"v_exp_f32 v141, v141\n"
"v_sub_f32 v142, v142, v254\n"
"v_exp_f32 v142, v142\n"
"v_sub_f32 v143, v143, v254\n"
"v_exp_f32 v143, v143\n"
"v_cvt_pk_bf16_f32 v140, v140, v141\n"
"v_cvt_pk_bf16_f32 v141, v142, v143\n"
"v_sub_f32 v150, v150, v254\n"
"v_exp_f32 v150, v150\n"
"v_sub_f32 v151, v151, v254\n"
"v_exp_f32 v151, v151\n"
"ds_write_addtid_b32 v140 offset:54784\n"
"v_cvt_pk_bf16_f32 v149, v150, v151\n"
"ds_write_addtid_b32 v141 offset:55040\n"
"s_waitcnt lgkmcnt(5)\n"
"v_mfma_f32_16x16x32_bf16 v[144:147], v[220:223], v[250:253], v[144:147]\n"
"ds_read_b128 v[132:135], v174 offset:4096\n"
"v_mfma_f32_16x16x32_bf16 v[144:147], v[222:225], v[24:27], v[144:147]\n"
"s_waitcnt lgkmcnt(1)\n"
"s_barrier\n"
"ds_write_addtid_b32 v149 offset:57600\n"
"v_mfma_f32_16x16x32_bf16 v[136:139], v[226:229], v[250:253], v[136:139]\n"
"ds_read_b128 v[140:143], v4 offset:55296\n"
"v_mfma_f32_16x16x32_bf16 v[136:139], v[228:231], v[24:27], v[136:139]\n"
"s_nop 1\n"
"v_sub_f32 v144, v144, v254\n"
"v_exp_f32 v144, v144\n"
"v_sub_f32 v145, v145, v254\n"
"v_exp_f32 v145, v145\n"
"v_sub_f32 v146, v146, v254\n"
"v_exp_f32 v146, v146\n"
"v_sub_f32 v147, v147, v254\n"
"v_exp_f32 v147, v147\n"
"v_cvt_pk_bf16_f32 v144, v144, v145\n"
"v_cvt_pk_bf16_f32 v145, v146, v147\n"
"v_sub_f32 v136, v136, v254\n"
"v_exp_f32 v136, v136\n"
"v_sub_f32 v137, v137, v254\n"
"v_exp_f32 v137, v137\n"
"v_sub_f32 v138, v138, v254\n"
"v_exp_f32 v138, v138\n"
"v_sub_f32 v139, v139, v254\n"
"v_exp_f32 v139, v139\n"
"v_cvt_pk_bf16_f32 v136, v136, v137\n"
"v_cvt_pk_bf16_f32 v137, v138, v139\n"
"ds_write_addtid_b32 v144 offset:57856\n"
"ds_write_addtid_b32 v145 offset:58112\n"
"s_waitcnt lgkmcnt(4)\n"
"v_mfma_f32_16x16x32_bf16 v[132:135], v[232:235], v[250:253], v[132:135]\n"
"ds_write_addtid_b32 v136 offset:58368\n"
"v_mfma_f32_16x16x32_bf16 v[132:135], v[234:237], v[24:27], v[132:135]\n"
"s_waitcnt lgkmcnt(3)\n"
"ds_read_b128 v[148:151], v5 offset:55296\n"
"v_mfma_f32_16x16x32_bf16 v[152:155], v[84:87], v[140:143], 0\n"
"ds_write_addtid_b32 v137 offset:58624\n"
"v_mfma_f32_16x16x32_bf16 v[144:147], v[88:91], v[140:143], 0\n"
"ds_read_b128 v[250:253], v6 offset:5120\n"
"v_mfma_f32_16x16x32_bf16 v[156:159], v[92:95], v[140:143], 0\n"
"s_waitcnt lgkmcnt(2)\n"
"ds_read_b128 v[160:163], v4 offset:57344\n"
"v_mfma_f32_16x16x32_bf16 v[152:155], v[96:99], v[148:151], v[152:155]\n"
"v_sub_f32 v132, v132, v254\n"
"v_exp_f32 v132, v132\n"
"v_mfma_f32_16x16x32_bf16 v[144:147], v[100:103], v[148:151], v[144:147]\n"
"s_waitcnt lgkmcnt(1)\n"
"ds_read_b128 v[136:139], v6 offset:6144\n"
"v_mfma_f32_16x16x32_bf16 v[156:159], v[250:253], v[148:151], v[156:159]\n"
"s_waitcnt lgkmcnt(1)\n"
"ds_read_b128 v[140:143], v5 offset:57344\n"
"v_mfma_f32_16x16x32_bf16 v[152:155], v[104:107], v[160:163], v[152:155]\n"
"v_sub_f32 v133, v133, v254\n"
"v_exp_f32 v133, v133\n"
"v_mfma_f32_16x16x32_bf16 v[144:147], v[108:111], v[160:163], v[144:147]\n"
"ds_read_b128 v[164:167], v6 offset:7168\n"
"v_sub_f32 v134, v134, v254\n"
"v_exp_f32 v134, v134\n"
"v_sub_f32 v135, v135, v254\n"
"v_exp_f32 v135, v135\n"
"s_waitcnt lgkmcnt(2)\n"
"v_mfma_f32_16x16x32_bf16 v[156:159], v[136:139], v[160:163], v[156:159]\n"
"s_waitcnt lgkmcnt(1)\n"
"ds_read_b128 v[148:151], v4 offset:59392\n"
"v_cvt_pk_bf16_f32 v132, v132, v133\n"
"v_cvt_pk_bf16_f32 v133, v134, v135\n"
"v_mfma_f32_16x16x32_bf16 v[152:155], v[112:115], v[140:143], v[152:155]\n"
"ds_write_addtid_b32 v132 offset:58880\n"
"v_mfma_f32_16x16x32_bf16 v[144:147], v[116:119], v[140:143], v[144:147]\n"
"ds_write_addtid_b32 v133 offset:59136\n"
"s_waitcnt lgkmcnt(3)\n"
"v_mfma_f32_16x16x32_bf16 v[156:159], v[164:167], v[140:143], v[156:159]\n"
"s_waitcnt lgkmcnt(2)\n"
"ds_read_b128 v[136:139], v6 offset:8192\n"
"ds_read_b128 v[160:163], v5 offset:59392\n"
"v_mfma_f32_16x16x32_bf16 v[152:155], v[0:3], v[148:151], v[152:155]\n"
"v_mfma_f32_16x16x32_bf16 v[144:147], v[120:123], v[148:151], v[144:147]\n"
"s_waitcnt lgkmcnt(2)\n"
"s_barrier\n"
"ds_read_b128 v[132:135], v6 offset:9216\n"
"s_waitcnt lgkmcnt(2)\n"
"v_mfma_f32_16x16x32_bf16 v[156:159], v[136:139], v[148:151], v[156:159]\n"
"s_waitcnt lgkmcnt(1)\n"
"v_mfma_f32_16x16x32_bf16 v[152:155], v[124:127], v[160:163], v[152:155]\n"
"ds_read_b128 v[140:143], v4 offset:12288\n"
"v_mfma_f32_16x16x32_bf16 v[144:147], v[128:131], v[160:163], v[144:147]\n"
"s_waitcnt lgkmcnt(1)\n"
"v_mfma_f32_16x16x32_bf16 v[156:159], v[132:135], v[160:163], v[156:159]\n"
"s_waitcnt lgkmcnt(0)\n"
"v_mfma_f32_16x16x32_bf16 v[250:253], v[8:11], v[140:143], 0\n"
"ds_read_b128 v[164:167], v5 offset:12288\n"
"v_mfma_f32_16x16x32_bf16 v[136:139], v[12:15], v[140:143], 0\n"
"v_mfma_f32_16x16x32_bf16 v[148:151], v[16:19], v[140:143], 0\n"
"s_waitcnt lgkmcnt(0)\n"
"v_mfma_f32_16x16x32_bf16 v[250:253], v[20:23], v[164:167], v[250:253]\n"
"ds_read_b128 v[168:171], v4 offset:14336\n"
"v_mul_f32 v152, s60, v152\n"
"v_mfma_f32_16x16x32_bf16 v[136:139], v[28:31], v[164:167], v[136:139]\n"
"v_mul_f32 v153, s60, v153\n"
"v_cvt_pk_bf16_f32 v152, v152, v153\n"
"v_mfma_f32_16x16x32_bf16 v[148:151], v[32:35], v[164:167], v[148:151]\n"
"v_mul_f32 v154, s60, v154\n"
"v_mul_f32 v155, s60, v155\n"
"v_cvt_pk_bf16_f32 v153, v154, v155\n"
"v_mul_f32 v144, s60, v144\n"
"v_mul_f32 v145, s60, v145\n"
"v_mul_f32 v146, s60, v146\n"
"v_mul_f32 v147, s60, v147\n"
"v_cvt_pk_bf16_f32 v154, v144, v145\n"
"v_cvt_pk_bf16_f32 v155, v146, v147\n"
"v_mul_f32 v156, s60, v156\n"
"v_mul_f32 v157, s60, v157\n"
"v_mul_f32 v158, s60, v158\n"
"s_waitcnt lgkmcnt(0)\n"
"v_mfma_f32_16x16x32_bf16 v[250:253], v[36:39], v[168:171], v[250:253]\n"
"ds_read_b128 v[132:135], v5 offset:14336\n"
"v_mul_f32 v159, s60, v159\n"
"v_mfma_f32_16x16x32_bf16 v[136:139], v[40:43], v[168:171], v[136:139]\n"
"v_cvt_pk_bf16_f32 v26, v156, v157\n"
"v_cvt_pk_bf16_f32 v27, v158, v159\n"
"v_mfma_f32_16x16x32_bf16 v[148:151], v[44:47], v[168:171], v[148:151]\n"
"s_waitcnt lgkmcnt(0)\n"
"v_bfe_u32 v255, v6, 2, 6\n"
"ds_read_b32 v255, v255 offset:1792\n"
"v_mfma_f32_16x16x32_bf16 v[250:253], v[48:51], v[132:135], v[250:253]\n"
"ds_read_b128 v[140:143], v4 offset:16384\n"
"v_mfma_f32_16x16x32_bf16 v[136:139], v[52:55], v[132:135], v[136:139]\n"
"s_add_u32 s68, s50, 4294964608\n"
"v_mfma_f32_16x16x32_bf16 v[148:151], v[56:59], v[132:135], v[148:151]\n"
"s_waitcnt lgkmcnt(1)\n"
"v_add_u32_sdwa v177, v255, v7 dst_sel:DWORD dst_unused:UNUSED_PAD src0_sel:WORD_0 src1_sel:DWORD\n"
"s_waitcnt lgkmcnt(0)\n"
"v_mfma_f32_16x16x32_bf16 v[250:253], v[60:63], v[140:143], v[250:253]\n"
"s_add_u32 s69, s50, 4294963264\n"
"ds_read_b128 v[144:147], v5 offset:16384\n"
"v_max_u32 v176, s68, v177\n"
"v_mfma_f32_16x16x32_bf16 v[136:139], v[64:67], v[140:143], v[136:139]\n"
"v_mfma_f32_16x16x32_bf16 v[148:151], v[68:71], v[140:143], v[148:151]\n"
"s_waitcnt lgkmcnt(0)\n"
"v_max_u32 v175, s69, v177\n"
"v_mfma_f32_16x16x32_bf16 v[250:253], v[72:75], v[144:147], v[250:253]\n"
"ds_read_b128 v[132:135], v176 offset:2688\n"
"v_mfma_f32_16x16x32_bf16 v[136:139], v[76:79], v[144:147], v[136:139]\n"
"v_mfma_f32_16x16x32_bf16 v[148:151], v[80:83], v[144:147], v[148:151]\n"
"s_add_u32 s70, s50, 4294964304\n"
"ds_read_b128 v[140:143], v175 offset:4032\n"
"s_waitcnt lgkmcnt(1)\n"
"v_mfma_f32_16x16x32_bf16 v[132:135], v[178:181], v[152:155], v[132:135]\n"
"v_mfma_f32_16x16x32_bf16 v[132:135], v[180:183], v[24:27], v[132:135]\n"
"ds_read_b128 v[156:159], v176 offset:2704\n"
"v_cvt_pk_bf16_f32 v250, v250, v251\n"
"v_cvt_pk_bf16_f32 v251, v252, v253\n"
"v_cvt_pk_bf16_f32 v252, v136, v137\n"
"v_cvt_pk_bf16_f32 v253, v138, v139\n"
"v_cvt_pk_bf16_f32 v254, v148, v149\n"
"v_cvt_pk_bf16_f32 v255, v150, v151\n"
"s_waitcnt lgkmcnt(1)\n"
"v_mfma_f32_16x16x32_bf16 v[140:143], v[208:211], v[152:155], v[140:143]\n"
"v_max3_f32 v172, v132, v133, v134\n"
"v_max_f32 v172, v172, v135\n"
"v_mfma_f32_16x16x32_bf16 v[140:143], v[210:213], v[24:27], v[140:143]\n"
"ds_read_b128 v[136:139], v175 offset:4048\n"
"v_min_u32 v174, s70, v177\n"
"s_mov_b64 exec, s[52:53]\n"
"v_add_u32 v174, 1040, v174\n"
"s_mov_b64 exec, -1\n"
"s_waitcnt lgkmcnt(1)\n"
"v_mfma_f32_16x16x32_bf16 v[156:159], v[184:187], v[152:155], v[156:159]\n"
"v_mfma_f32_16x16x32_bf16 v[156:159], v[186:189], v[24:27], v[156:159]\n"
"s_waitcnt lgkmcnt(0)\n"
"ds_read_b128 v[132:135], v174 offset:5376\n"
"v_max3_f32 v172, v172, v140, v141\n"
"v_max3_f32 v172, v172, v142, v143\n"
"v_mfma_f32_16x16x32_bf16 v[136:139], v[214:217], v[152:155], v[136:139]\n"
"v_mfma_f32_16x16x32_bf16 v[136:139], v[216:219], v[24:27], v[136:139]\n"
"s_waitcnt lgkmcnt(0)\n"
"ds_read_b128 v[144:147], v176 offset:2720\n"
"v_max3_f32 v172, v172, v156, v157\n"
"v_max3_f32 v172, v172, v158, v159\n"
"v_mfma_f32_16x16x32_bf16 v[132:135], v[238:241], v[152:155], v[132:135]\n"
"v_mfma_f32_16x16x32_bf16 v[132:135], v[240:243], v[24:27], v[132:135]\n"
"s_waitcnt lgkmcnt(0)\n"
"ds_read_b128 v[140:143], v175 offset:4064\n"
"v_max3_f32 v172, v172, v136, v137\n"
"v_max3_f32 v172, v172, v138, v139\n"
"v_mfma_f32_16x16x32_bf16 v[144:147], v[190:193], v[152:155], v[144:147]\n"
"v_mfma_f32_16x16x32_bf16 v[144:147], v[192:195], v[24:27], v[144:147]\n"
"s_waitcnt lgkmcnt(0)\n"
"ds_read_b128 v[148:151], v174 offset:5392\n"
"v_max3_f32 v172, v172, v132, v133\n"
"v_max3_f32 v172, v172, v134, v135\n"
"v_mfma_f32_16x16x32_bf16 v[140:143], v[220:223], v[152:155], v[140:143]\n"
"v_mfma_f32_16x16x32_bf16 v[140:143], v[222:225], v[24:27], v[140:143]\n"
"s_waitcnt lgkmcnt(0)\n"
"ds_read_b128 v[136:139], v174 offset:5408\n"
"v_max3_f32 v172, v172, v144, v145\n"
"v_max3_f32 v172, v172, v146, v147\n"
"v_mfma_f32_16x16x32_bf16 v[148:151], v[244:247], v[152:155], v[148:151]\n"
"v_mfma_f32_16x16x32_bf16 v[148:151], v[246:249], v[24:27], v[148:151]\n"
"s_waitcnt lgkmcnt(0)\n"
"ds_read_b128 v[132:135], v176 offset:2688\n"
"v_max3_f32 v172, v172, v140, v141\n"
"v_max3_f32 v172, v172, v142, v143\n"
"v_mfma_f32_16x16x32_bf16 v[136:139], v[250:253], v[152:155], v[136:139]\n"
"v_mfma_f32_16x16x32_bf16 v[136:139], v[252:255], v[24:27], v[136:139]\n"
"s_waitcnt lgkmcnt(0)\n"
"ds_read_b128 v[144:147], v175 offset:4032\n"
"v_max3_f32 v172, v172, v148, v149\n"
"v_max3_f32 v172, v172, v150, v151\n"
"v_mfma_f32_16x16x32_bf16 v[132:135], v[178:181], v[152:155], v[132:135]\n"
"v_mfma_f32_16x16x32_bf16 v[132:135], v[180:183], v[24:27], v[132:135]\n"
"ds_read_b128 v[140:143], v176 offset:2704\n"
"s_nop 0\n"
"v_max3_f32 v172, v172, v136, v137\n"
"v_max3_f32 v172, v172, v138, v139\n"
"v_mov_b32 v173, v172\n"
"s_nop 1\n"
"v_permlane16_swap_b32 v172, v173\n"
"v_max_f32 v172, v172, v173\n"
"v_mov_b32 v173, v172\n"
"s_nop 1\n"
"v_permlane32_swap_b32 v172, v173\n"
"v_max_f32 v172, v172, v173\n"
"v_sub_f32 v132, v132, v172\n"
"v_exp_f32 v132, v132\n"
"v_sub_f32 v133, v133, v172\n"
"v_exp_f32 v133, v133\n"
"v_sub_f32 v134, v134, v172\n"
"v_exp_f32 v134, v134\n"
"v_sub_f32 v135, v135, v172\n"
"v_exp_f32 v135, v135\n"
"s_waitcnt lgkmcnt(1)\n"
"v_mfma_f32_16x16x32_bf16 v[144:147], v[208:211], v[152:155], v[144:147]\n"
"v_cvt_pk_bf16_f32 v132, v132, v133\n"
"v_cvt_pk_bf16_f32 v133, v134, v135\n"
"v_mfma_f32_16x16x32_bf16 v[144:147], v[210:213], v[24:27], v[144:147]\n"
"ds_read_b128 v[178:181], v175 offset:4048\n"
"ds_write_addtid_b32 v132 offset:55296\n"
"ds_write_addtid_b32 v133 offset:55552\n"
"s_waitcnt lgkmcnt(3)\n"
"v_mfma_f32_16x16x32_bf16 v[140:143], v[184:187], v[152:155], v[140:143]\n"
"v_mfma_f32_16x16x32_bf16 v[140:143], v[186:189], v[24:27], v[140:143]\n"
"ds_read_b128 v[136:139], v174 offset:5376\n"
"s_nop 0\n"
"v_sub_f32 v144, v144, v172\n"
"v_exp_f32 v144, v144\n"
"v_sub_f32 v145, v145, v172\n"
"v_exp_f32 v145, v145\n"
"v_sub_f32 v146, v146, v172\n"
"v_exp_f32 v146, v146\n"
"v_sub_f32 v147, v147, v172\n"
"v_exp_f32 v147, v147\n"
"v_sub_f32 v140, v140, v172\n"
"v_exp_f32 v140, v140\n"
"v_sub_f32 v141, v141, v172\n"
"v_exp_f32 v141, v141\n"
"v_sub_f32 v142, v142, v172\n"
"v_exp_f32 v142, v142\n"
"v_cvt_pk_bf16_f32 v144, v144, v145\n"
"v_sub_f32 v143, v143, v172\n"
"v_exp_f32 v143, v143\n"
"v_cvt_pk_bf16_f32 v145, v146, v147\n"
"v_cvt_pk_bf16_f32 v140, v140, v141\n"
"v_cvt_pk_bf16_f32 v141, v142, v143\n"
"ds_write_addtid_b32 v144 offset:56320\n"
"ds_write_addtid_b32 v145 offset:56576\n"
"s_waitcnt lgkmcnt(5)\n"
"v_mfma_f32_16x16x32_bf16 v[178:181], v[214:217], v[152:155], v[178:181]\n"
"ds_write_addtid_b32 v140 offset:55808\n"
"v_mfma_f32_16x16x32_bf16 v[178:181], v[216:219], v[24:27], v[178:181]\n"
"ds_read_b128 v[208:211], v176 offset:2720\n"
"ds_write_addtid_b32 v141 offset:56064\n"
"s_waitcnt lgkmcnt(5)\n"
"v_mfma_f32_16x16x32_bf16 v[136:139], v[238:241], v[152:155], v[136:139]\n"
"v_mfma_f32_16x16x32_bf16 v[136:139], v[240:243], v[24:27], v[136:139]\n"
"ds_read_b128 v[132:135], v175 offset:4064\n"
"s_nop 1\n"
"v_sub_f32 v178, v178, v172\n"
"v_exp_f32 v178, v178\n"
"v_sub_f32 v179, v179, v172\n"
"v_exp_f32 v179, v179\n"
"v_sub_f32 v180, v180, v172\n"
"v_exp_f32 v180, v180\n"
"v_sub_f32 v181, v181, v172\n"
"v_exp_f32 v181, v181\n"
"v_cvt_pk_bf16_f32 v178, v178, v179\n"
"v_cvt_pk_bf16_f32 v179, v180, v181\n"
"v_sub_f32 v136, v136, v172\n"
"v_exp_f32 v136, v136\n"
"v_sub_f32 v137, v137, v172\n"
"v_exp_f32 v137, v137\n"
"v_sub_f32 v138, v138, v172\n"
"v_exp_f32 v138, v138\n"
"v_sub_f32 v139, v139, v172\n"
"v_exp_f32 v139, v139\n"
"v_cvt_pk_bf16_f32 v136, v136, v137\n"
"v_cvt_pk_bf16_f32 v137, v138, v139\n"
"ds_write_addtid_b32 v178 offset:56832\n"
"ds_write_addtid_b32 v179 offset:57088\n"
"s_waitcnt lgkmcnt(4)\n"
"v_mfma_f32_16x16x32_bf16 v[208:211], v[190:193], v[152:155], v[208:211]\n"
"v_mfma_f32_16x16x32_bf16 v[208:211], v[192:195], v[24:27], v[208:211]\n"
"s_waitcnt lgkmcnt(0)\n"
"ds_read_b128 v[180:183], v174 offset:5392\n"
"s_barrier\n"
"ds_write_addtid_b32 v136 offset:57344\n"
"ds_write_addtid_b32 v137 offset:57600\n"
"v_mfma_f32_16x16x32_bf16 v[132:135], v[220:223], v[152:155], v[132:135]\n"
"v_mfma_f32_16x16x32_bf16 v[132:135], v[222:225], v[24:27], v[132:135]\n"
"s_nop 0\n"
"ds_read_b128 v[210:213], v174 offset:5408\n"
"v_sub_f32 v208, v208, v172\n"
"v_exp_f32 v208, v208\n"
"v_sub_f32 v209, v209, v172\n"
"v_exp_f32 v209, v209\n"
"s_nop 0\n"
"v_cvt_pk_bf16_f32 v208, v208, v209\n"
"ds_write_addtid_b32 v208 offset:58368\n"
"s_waitcnt lgkmcnt(4)\n"
"v_mfma_f32_16x16x32_bf16 v[180:183], v[244:247], v[152:155], v[180:183]\n"
"v_sub_f32 v132, v132, v172\n"
"v_exp_f32 v132, v132\n"
"v_sub_f32 v133, v133, v172\n"
"v_exp_f32 v133, v133\n"
"v_mfma_f32_16x16x32_bf16 v[180:183], v[246:249], v[24:27], v[180:183]\n"
"ds_read_b128 v[238:241], v4 offset:18432\n"
"v_cvt_pk_bf16_f32 v132, v132, v133\n"
"ds_write_addtid_b32 v132 offset:58880\n"
"s_waitcnt lgkmcnt(3)\n"
"v_mfma_f32_16x16x32_bf16 v[210:213], v[250:253], v[152:155], v[210:213]\n"
"v_mfma_f32_16x16x32_bf16 v[210:213], v[252:255], v[24:27], v[210:213]\n"
"s_waitcnt lgkmcnt(1)\n"
"v_mfma_f32_16x16x32_bf16 v[136:139], v[84:87], v[238:241], 0\n"
"ds_read_b128 v[140:143], v5 offset:18432\n"
"v_mfma_f32_16x16x32_bf16 v[144:147], v[88:91], v[238:241], 0\n"
"v_sub_f32 v180, v180, v172\n"
"v_exp_f32 v180, v180\n"
"v_sub_f32 v181, v181, v172\n"
"v_exp_f32 v181, v181\n"
"v_sub_f32 v182, v182, v172\n"
"v_exp_f32 v182, v182\n"
"v_mfma_f32_16x16x32_bf16 v[132:135], v[92:95], v[238:241], 0\n"
"ds_read_b128 v[148:151], v6 offset:5120\n"
"v_sub_f32 v183, v183, v172\n"
"v_exp_f32 v183, v183\n"
"v_cvt_pk_bf16_f32 v180, v180, v181\n"
"v_cvt_pk_bf16_f32 v181, v182, v183\n"
"s_waitcnt lgkmcnt(1)\n"
"v_mfma_f32_16x16x32_bf16 v[136:139], v[96:99], v[140:143], v[136:139]\n"
"ds_write_addtid_b32 v180 offset:57856\n"
"v_mfma_f32_16x16x32_bf16 v[144:147], v[100:103], v[140:143], v[144:147]\n"
"ds_read_b128 v[238:241], v4 offset:20480\n"
"v_sub_f32 v210, v210, v172\n"
"v_exp_f32 v210, v210\n"
"v_sub_f32 v211, v211, v172\n"
"v_exp_f32 v211, v211\n"
"s_waitcnt lgkmcnt(2)\n"
"v_mfma_f32_16x16x32_bf16 v[132:135], v[148:151], v[140:143], v[132:135]\n"
"ds_write_addtid_b32 v181 offset:58112\n"
"v_cvt_pk_bf16_f32 v210, v210, v211\n"
"ds_read_b128 v[152:155], v6 offset:6144\n"
"ds_write_addtid_b32 v210 offset:34304\n"
"s_waitcnt lgkmcnt(3)\n"
"v_mfma_f32_16x16x32_bf16 v[136:139], v[104:107], v[238:241], v[136:139]\n"
"ds_read_b128 v[156:159], v5 offset:20480\n"
"v_mfma_f32_16x16x32_bf16 v[144:147], v[108:111], v[238:241], v[144:147]\n"
"s_waitcnt lgkmcnt(1)\n"
"s_barrier\n"
"v_mfma_f32_16x16x32_bf16 v[132:135], v[152:155], v[238:241], v[132:135]\n"
"s_waitcnt lgkmcnt(0)\n"
"ds_read_b128 v[178:181], v6 offset:7168\n"
"v_mfma_f32_16x16x32_bf16 v[136:139], v[112:115], v[156:159], v[136:139]\n"
"ds_read_b128 v[208:211], v4 offset:22528\n"
"v_mfma_f32_16x16x32_bf16 v[144:147], v[116:119], v[156:159], v[144:147]\n"
"s_waitcnt lgkmcnt(1)\n"
"v_mfma_f32_16x16x32_bf16 v[132:135], v[178:181], v[156:159], v[132:135]\n"
"s_waitcnt lgkmcnt(0)\n"
"ds_read_b128 v[140:143], v6 offset:8192\n"
"v_mfma_f32_16x16x32_bf16 v[136:139], v[0:3], v[208:211], v[136:139]\n"
"ds_read_b128 v[238:241], v5 offset:22528\n"
"v_mfma_f32_16x16x32_bf16 v[144:147], v[120:123], v[208:211], v[144:147]\n"
"s_waitcnt lgkmcnt(1)\n"
"v_mfma_f32_16x16x32_bf16 v[132:135], v[140:143], v[208:211], v[132:135]\n"
"s_waitcnt lgkmcnt(0)\n"
"ds_read_b128 v[148:151], v6 offset:9216\n"
"ds_read_b128 v[152:155], v4 offset:36864\n"
"v_mfma_f32_16x16x32_bf16 v[136:139], v[124:127], v[238:241], v[136:139]\n"
"v_mfma_f32_16x16x32_bf16 v[144:147], v[128:131], v[238:241], v[144:147]\n"
"s_waitcnt lgkmcnt(1)\n"
"v_mfma_f32_16x16x32_bf16 v[132:135], v[148:151], v[238:241], v[132:135]\n"
"s_waitcnt lgkmcnt(0)\n"
"ds_read_b128 v[156:159], v5 offset:36864\n"
"v_mfma_f32_16x16x32_bf16 v[178:181], v[8:11], v[152:155], 0\n"
"v_mfma_f32_16x16x32_bf16 v[208:211], v[12:15], v[152:155], 0\n"
"v_mfma_f32_16x16x32_bf16 v[140:143], v[16:19], v[152:155], 0\n"
"s_waitcnt lgkmcnt(0)\n"
"ds_read_b128 v[160:163], v4 offset:38912\n"
"v_mfma_f32_16x16x32_bf16 v[178:181], v[20:23], v[156:159], v[178:181]\n"
"v_mul_f32 v136, s60, v136\n"
"v_mul_f32 v137, s60, v137\n"
"v_mfma_f32_16x16x32_bf16 v[208:211], v[28:31], v[156:159], v[208:211]\n"
"v_cvt_pk_bf16_f32 v136, v136, v137\n"
"v_mul_f32 v138, s60, v138\n"
"v_mfma_f32_16x16x32_bf16 v[140:143], v[32:35], v[156:159], v[140:143]\n"
"s_waitcnt lgkmcnt(0)\n"
"ds_read_b128 v[238:241], v5 offset:38912\n"
"v_mul_f32 v139, s60, v139\n"
"v_mfma_f32_16x16x32_bf16 v[178:181], v[36:39], v[160:163], v[178:181]\n"
"v_cvt_pk_bf16_f32 v137, v138, v139\n"
"v_mul_f32 v144, s60, v144\n"
"v_mfma_f32_16x16x32_bf16 v[208:211], v[40:43], v[160:163], v[208:211]\n"
"v_bfe_u32 v243, v6, 2, 6\n"
"v_mul_f32 v145, s60, v145\n"
"v_mfma_f32_16x16x32_bf16 v[140:143], v[44:47], v[160:163], v[140:143]\n"
"ds_read_b128 v[148:151], v4 offset:40960\n"
"v_mul_f32 v146, s60, v146\n"
"v_mul_f32 v147, s60, v147\n"
"v_cvt_pk_bf16_f32 v138, v144, v145\n"
"v_cvt_pk_bf16_f32 v139, v146, v147\n"
"v_mul_f32 v132, s60, v132\n"
"v_mul_f32 v133, s60, v133\n"
"v_mul_f32 v134, s60, v134\n"
"s_waitcnt lgkmcnt(1)\n"
"v_mfma_f32_16x16x32_bf16 v[178:181], v[48:51], v[238:241], v[178:181]\n"
"s_add_u32 s71, s50, 4294964592\n"
"ds_read_b32 v243, v243 offset:1856\n"
"v_mul_f32 v135, s60, v135\n"
"v_mfma_f32_16x16x32_bf16 v[208:211], v[52:55], v[238:241], v[208:211]\n"
"v_cvt_pk_bf16_f32 v26, v132, v133\n"
"v_cvt_pk_bf16_f32 v27, v134, v135\n"
"v_mfma_f32_16x16x32_bf16 v[140:143], v[56:59], v[238:241], v[140:143]\n"
"s_waitcnt lgkmcnt(1)\n"
"ds_read_b128 v[144:147], v5 offset:40960\n"
"v_mfma_f32_16x16x32_bf16 v[178:181], v[60:63], v[148:151], v[178:181]\n"
"v_mfma_f32_16x16x32_bf16 v[208:211], v[64:67], v[148:151], v[208:211]\n"
"s_add_u32 s72, s50, 4294965632\n"
"v_mfma_f32_16x16x32_bf16 v[140:143], v[68:71], v[148:151], v[140:143]\n"
"s_waitcnt lgkmcnt(1)\n"
"v_add_u32_sdwa v213, v243, v7 dst_sel:DWORD dst_unused:UNUSED_PAD src0_sel:WORD_0 src1_sel:DWORD\n"
"s_nop 0\n"
"v_max_u32 v212, s71, v213\n"
"ds_read_b128 v[238:241], v212 offset:2720\n"
"s_waitcnt lgkmcnt(1)\n"
"v_mfma_f32_16x16x32_bf16 v[178:181], v[72:75], v[144:147], v[178:181]\n"
"v_min_u32 v177, s72, v213\n"
"v_mfma_f32_16x16x32_bf16 v[208:211], v[76:79], v[144:147], v[208:211]\n"
"ds_read_b128 v[132:135], v212 offset:2736\n"
"v_mfma_f32_16x16x32_bf16 v[140:143], v[80:83], v[144:147], v[140:143]\n"
"s_waitcnt lgkmcnt(1)\n"
"v_mfma_f32_16x16x32_bf16 v[238:241], v[190:193], v[136:139], v[238:241]\n"
"ds_read_b128 v[148:151], v177 offset:4048\n"
"v_mfma_f32_16x16x32_bf16 v[238:241], v[192:195], v[24:27], v[238:241]\n"
"s_waitcnt lgkmcnt(1)\n"
"v_mfma_f32_16x16x32_bf16 v[132:135], v[196:199], v[136:139], v[132:135]\n"
"s_add_u32 s73, s50, 4294964288\n"
"ds_read_b128 v[152:155], v177 offset:4064\n"
"v_cvt_pk_bf16_f32 v178, v178, v179\n"
"v_cvt_pk_bf16_f32 v179, v180, v181\n"
"v_cvt_pk_bf16_f32 v180, v208, v209\n"
"v_cvt_pk_bf16_f32 v181, v210, v211\n"
"v_cvt_pk_bf16_f32 v182, v140, v141\n"
"v_cvt_pk_bf16_f32 v183, v142, v143\n"
"v_mfma_f32_16x16x32_bf16 v[132:135], v[198:201], v[24:27], v[132:135]\n"
"s_waitcnt lgkmcnt(1)\n"
"v_max3_f32 v242, v238, v239, v240\n"
"v_max_f32 v242, v242, v241\n"
"v_mfma_f32_16x16x32_bf16 v[148:151], v[214:217], v[136:139], v[148:151]\n"
"ds_read_b128 v[208:211], v177 offset:4080\n"
"v_min_u32 v176, s73, v213\n"
"v_mfma_f32_16x16x32_bf16 v[148:151], v[216:219], v[24:27], v[148:151]\n"
"s_mov_b64 exec, s[52:53]\n"
"v_add_u32 v176, 1040, v176\n"
"s_mov_b64 exec, -1\n"
"v_max3_f32 v242, v242, v132, v133\n"
"v_max3_f32 v242, v242, v134, v135\n"
"s_waitcnt lgkmcnt(1)\n"
"v_mfma_f32_16x16x32_bf16 v[152:155], v[220:223], v[136:139], v[152:155]\n"
"ds_read_b128 v[238:241], v212 offset:2704\n"
"v_mfma_f32_16x16x32_bf16 v[152:155], v[222:225], v[24:27], v[152:155]\n"
"s_waitcnt lgkmcnt(1)\n"
"v_max3_f32 v242, v242, v148, v149\n"
"v_max3_f32 v242, v242, v150, v151\n"
"v_mfma_f32_16x16x32_bf16 v[208:211], v[226:229], v[136:139], v[208:211]\n"
"ds_read_b128 v[132:135], v176 offset:5392\n"
"v_mfma_f32_16x16x32_bf16 v[208:211], v[228:231], v[24:27], v[208:211]\n"
"s_waitcnt lgkmcnt(1)\n"
"s_nop 0\n"
"v_max3_f32 v242, v242, v152, v153\n"
"v_max3_f32 v242, v242, v154, v155\n"
"v_mfma_f32_16x16x32_bf16 v[238:241], v[184:187], v[136:139], v[238:241]\n"
"ds_read_b128 v[140:143], v176 offset:5408\n"
"v_mfma_f32_16x16x32_bf16 v[238:241], v[186:189], v[24:27], v[238:241]\n"
"s_waitcnt lgkmcnt(1)\n"
"v_max3_f32 v242, v242, v208, v209\n"
"v_max3_f32 v242, v242, v210, v211\n"
"v_mfma_f32_16x16x32_bf16 v[132:135], v[244:247], v[136:139], v[132:135]\n"
"ds_read_b128 v[144:147], v176 offset:5424\n"
"v_mfma_f32_16x16x32_bf16 v[132:135], v[246:249], v[24:27], v[132:135]\n"
"s_waitcnt lgkmcnt(1)\n"
"s_nop 0\n"
"v_max3_f32 v242, v242, v238, v239\n"
"v_max3_f32 v242, v242, v240, v241\n"
"v_mfma_f32_16x16x32_bf16 v[140:143], v[250:253], v[136:139], v[140:143]\n"
"ds_read_b128 v[208:211], v212 offset:2720\n"
"v_mfma_f32_16x16x32_bf16 v[140:143], v[252:255], v[24:27], v[140:143]\n"
"s_waitcnt lgkmcnt(1)\n"
"v_max3_f32 v242, v242, v132, v133\n"
"v_max3_f32 v242, v242, v134, v135\n"
"v_mfma_f32_16x16x32_bf16 v[144:147], v[178:181], v[136:139], v[144:147]\n"
"ds_read_b128 v[238:241], v212 offset:2736\n"
"v_mfma_f32_16x16x32_bf16 v[144:147], v[180:183], v[24:27], v[144:147]\n"
"s_waitcnt lgkmcnt(1)\n"
"s_nop 0\n"
"v_max3_f32 v242, v242, v140, v141\n"
"v_max3_f32 v242, v242, v142, v143\n"
"v_mfma_f32_16x16x32_bf16 v[208:211], v[190:193], v[136:139], v[208:211]\n"
"ds_read_b128 v[132:135], v177 offset:4048\n"
"v_mfma_f32_16x16x32_bf16 v[208:211], v[192:195], v[24:27], v[208:211]\n"
"s_nop 0\n"
"v_max3_f32 v242, v242, v144, v145\n"
"v_max3_f32 v242, v242, v146, v147\n"
"v_mov_b32 v243, v242\n"
"s_nop 1\n"
"v_permlane16_swap_b32 v242, v243\n"
"v_max_f32 v242, v242, v243\n"
"v_mov_b32 v243, v242\n"
"s_nop 1\n"
"v_permlane32_swap_b32 v242, v243\n"
"v_max_f32 v242, v242, v243\n"
"v_sub_f32 v208, v208, v242\n"
"v_exp_f32 v208, v208\n"
"v_sub_f32 v209, v209, v242\n"
"v_exp_f32 v209, v209\n"
"v_sub_f32 v210, v210, v242\n"
"v_exp_f32 v210, v210\n"
"v_sub_f32 v211, v211, v242\n"
"v_exp_f32 v211, v211\n"
"s_waitcnt lgkmcnt(1)\n"
"v_mfma_f32_16x16x32_bf16 v[238:241], v[196:199], v[136:139], v[238:241]\n"
"ds_read_b128 v[140:143], v177 offset:4064\n"
"v_cvt_pk_bf16_f32 v208, v208, v209\n"
"v_mfma_f32_16x16x32_bf16 v[238:241], v[198:201], v[24:27], v[238:241]\n"
"ds_write_addtid_b32 v208 offset:53248\n"
"v_cvt_pk_bf16_f32 v209, v210, v211\n"
"ds_write_addtid_b32 v209 offset:53504\n"
"s_waitcnt lgkmcnt(3)\n"
"v_mfma_f32_16x16x32_bf16 v[132:135], v[214:217], v[136:139], v[132:135]\n"
"ds_read_b128 v[144:147], v177 offset:4080\n"
"v_mfma_f32_16x16x32_bf16 v[132:135], v[216:219], v[24:27], v[132:135]\n"
"s_nop 0\n"
"v_sub_f32 v238, v238, v242\n"
"v_exp_f32 v238, v238\n"
"v_sub_f32 v239, v239, v242\n"
"v_exp_f32 v239, v239\n"
"v_sub_f32 v240, v240, v242\n"
"v_exp_f32 v240, v240\n"
"v_sub_f32 v241, v241, v242\n"
"v_exp_f32 v241, v241\n"
"v_sub_f32 v132, v132, v242\n"
"v_exp_f32 v132, v132\n"
"v_sub_f32 v133, v133, v242\n"
"v_exp_f32 v133, v133\n"
"v_sub_f32 v134, v134, v242\n"
"v_exp_f32 v134, v134\n"
"v_sub_f32 v135, v135, v242\n"
"v_exp_f32 v135, v135\n"
"v_cvt_pk_bf16_f32 v238, v238, v239\n"
"v_cvt_pk_bf16_f32 v239, v240, v241\n"
"v_cvt_pk_bf16_f32 v132, v132, v133\n"
"v_cvt_pk_bf16_f32 v133, v134, v135\n"
"ds_write_addtid_b32 v238 offset:53760\n"
"ds_write_addtid_b32 v239 offset:54016\n"
"s_waitcnt lgkmcnt(5)\n"
"v_mfma_f32_16x16x32_bf16 v[140:143], v[220:223], v[136:139], v[140:143]\n"
"ds_read_b128 v[208:211], v212 offset:2704\n"
"v_mfma_f32_16x16x32_bf16 v[140:143], v[222:225], v[24:27], v[140:143]\n"
"ds_write_addtid_b32 v132 offset:54272\n"
"ds_write_addtid_b32 v133 offset:54528\n"
"s_waitcnt lgkmcnt(5)\n"
"v_mfma_f32_16x16x32_bf16 v[144:147], v[226:229], v[136:139], v[144:147]\n"
"ds_read_b128 v[238:241], v176 offset:5392\n"
"v_mfma_f32_16x16x32_bf16 v[144:147], v[228:231], v[24:27], v[144:147]\n"
"s_nop 1\n"
"v_sub_f32 v140, v140, v242\n"
"v_exp_f32 v140, v140\n"
"v_sub_f32 v141, v141, v242\n"
"v_exp_f32 v141, v141\n"
"v_sub_f32 v142, v142, v242\n"
"v_exp_f32 v142, v142\n"
"v_sub_f32 v143, v143, v242\n"
"v_exp_f32 v143, v143\n"
"v_sub_f32 v144, v144, v242\n"
"v_exp_f32 v144, v144\n"
"v_sub_f32 v145, v145, v242\n"
"v_exp_f32 v145, v145\n"
"v_sub_f32 v146, v146, v242\n"
"v_exp_f32 v146, v146\n"
"v_cvt_pk_bf16_f32 v140, v140, v141\n"
"v_sub_f32 v147, v147, v242\n"
"v_exp_f32 v147, v147\n"
"v_cvt_pk_bf16_f32 v141, v142, v143\n"
"v_cvt_pk_bf16_f32 v144, v144, v145\n"
"v_cvt_pk_bf16_f32 v145, v146, v147\n"
"ds_write_addtid_b32 v140 offset:54784\n"
"ds_write_addtid_b32 v141 offset:55040\n"
"s_waitcnt lgkmcnt(5)\n"
"v_mfma_f32_16x16x32_bf16 v[208:211], v[184:187], v[136:139], v[208:211]\n"
"ds_write_addtid_b32 v144 offset:33792\n"
"v_mfma_f32_16x16x32_bf16 v[208:211], v[186:189], v[24:27], v[208:211]\n"
"ds_write_addtid_b32 v145 offset:34048\n"
"ds_read_b128 v[132:135], v176 offset:5408\n"
"s_waitcnt lgkmcnt(5)\n"
"v_mfma_f32_16x16x32_bf16 v[238:241], v[244:247], v[136:139], v[238:241]\n"
"ds_read_b128 v[140:143], v176 offset:5424\n"
"v_mfma_f32_16x16x32_bf16 v[238:241], v[246:249], v[24:27], v[238:241]\n"
"s_waitcnt lgkmcnt(2)\n"
"s_barrier\n"
"v_sub_f32 v208, v208, v242\n"
"v_exp_f32 v208, v208\n"
"v_sub_f32 v209, v209, v242\n"
"v_exp_f32 v209, v209\n"
"v_sub_f32 v210, v210, v242\n"
"v_exp_f32 v210, v210\n"
"v_sub_f32 v211, v211, v242\n"
"v_exp_f32 v211, v211\n"
"v_sub_f32 v238, v238, v242\n"
"v_exp_f32 v238, v238\n"
"v_cvt_pk_bf16_f32 v208, v208, v209\n"
"v_sub_f32 v239, v239, v242\n"
"v_exp_f32 v239, v239\n"
"v_cvt_pk_bf16_f32 v209, v210, v211\n"
"v_sub_f32 v240, v240, v242\n"
"v_exp_f32 v240, v240\n"
"v_sub_f32 v241, v241, v242\n"
"v_exp_f32 v241, v241\n"
"v_cvt_pk_bf16_f32 v238, v238, v239\n"
"v_cvt_pk_bf16_f32 v239, v240, v241\n"
"ds_write_addtid_b32 v208 offset:57856\n"
"ds_write_addtid_b32 v209 offset:58112\n"
"s_waitcnt lgkmcnt(3)\n"
"v_mfma_f32_16x16x32_bf16 v[132:135], v[250:253], v[136:139], v[132:135]\n"
"ds_read_b128 v[184:187], v4 offset:43008\n"
"v_mfma_f32_16x16x32_bf16 v[132:135], v[252:255], v[24:27], v[132:135]\n"
"ds_write_addtid_b32 v238 offset:58368\n"
"ds_write_addtid_b32 v239 offset:58624\n"
"s_waitcnt lgkmcnt(5)\n"
"v_mfma_f32_16x16x32_bf16 v[140:143], v[178:181], v[136:139], v[140:143]\n"
"v_mfma_f32_16x16x32_bf16 v[140:143], v[180:183], v[24:27], v[140:143]\n"
"s_waitcnt lgkmcnt(2)\n"
"ds_read_b128 v[208:211], v5 offset:43008\n"
"v_mfma_f32_16x16x32_bf16 v[144:147], v[84:87], v[184:187], 0\n"
"v_mfma_f32_16x16x32_bf16 v[148:151], v[88:91], v[184:187], 0\n"
"ds_read_b128 v[152:155], v6 offset:5120\n"
"v_sub_f32 v132, v132, v242\n"
"v_exp_f32 v132, v132\n"
"v_sub_f32 v133, v133, v242\n"
"v_exp_f32 v133, v133\n"
"v_sub_f32 v134, v134, v242\n"
"v_exp_f32 v134, v134\n"
"v_mfma_f32_16x16x32_bf16 v[238:241], v[92:95], v[184:187], 0\n"
"ds_read_b128 v[136:139], v4 offset:45056\n"
"v_cvt_pk_bf16_f32 v132, v132, v133\n"
"v_sub_f32 v135, v135, v242\n"
"v_exp_f32 v135, v135\n"
"s_waitcnt lgkmcnt(2)\n"
"v_mfma_f32_16x16x32_bf16 v[144:147], v[96:99], v[208:211], v[144:147]\n"
"ds_write_addtid_b32 v132 offset:58880\n"
"v_cvt_pk_bf16_f32 v133, v134, v135\n"
"v_mfma_f32_16x16x32_bf16 v[148:151], v[100:103], v[208:211], v[148:151]\n"
"ds_write_addtid_b32 v133 offset:59136\n"
"s_waitcnt lgkmcnt(3)\n"
"v_mfma_f32_16x16x32_bf16 v[238:241], v[152:155], v[208:211], v[238:241]\n"
"s_waitcnt lgkmcnt(2)\n"
"ds_read_b128 v[184:187], v6 offset:6144\n"
"v_sub_f32 v140, v140, v242\n"
"v_exp_f32 v140, v140\n"
"v_mfma_f32_16x16x32_bf16 v[144:147], v[104:107], v[136:139], v[144:147]\n"
"ds_read_b128 v[156:159], v5 offset:45056\n"
"v_sub_f32 v141, v141, v242\n"
"v_exp_f32 v141, v141\n"
"v_mfma_f32_16x16x32_bf16 v[148:151], v[108:111], v[136:139], v[148:151]\n"
"ds_read_b128 v[208:211], v6 offset:7168\n"
"v_sub_f32 v142, v142, v242\n"
"v_exp_f32 v142, v142\n"
"v_sub_f32 v143, v143, v242\n"
"v_exp_f32 v143, v143\n"
"s_waitcnt lgkmcnt(2)\n"
"v_mfma_f32_16x16x32_bf16 v[238:241], v[184:187], v[136:139], v[238:241]\n"
"s_waitcnt lgkmcnt(1)\n"
"ds_read_b128 v[132:135], v4 offset:47104\n"
"v_cvt_pk_bf16_f32 v140, v140, v141\n"
"v_cvt_pk_bf16_f32 v141, v142, v143\n"
"v_mfma_f32_16x16x32_bf16 v[144:147], v[112:115], v[156:159], v[144:147]\n"
"ds_write_addtid_b32 v140 offset:57344\n"
"v_mfma_f32_16x16x32_bf16 v[148:151], v[116:119], v[156:159], v[148:151]\n"
"ds_write_addtid_b32 v141 offset:57600\n"
"s_waitcnt lgkmcnt(3)\n"
"v_mfma_f32_16x16x32_bf16 v[238:241], v[208:211], v[156:159], v[238:241]\n"
"s_waitcnt lgkmcnt(2)\n"
"ds_read_b128 v[136:139], v6 offset:8192\n"
"ds_read_b128 v[152:155], v5 offset:47104\n"
"v_mfma_f32_16x16x32_bf16 v[144:147], v[0:3], v[132:135], v[144:147]\n"
"v_mfma_f32_16x16x32_bf16 v[148:151], v[120:123], v[132:135], v[148:151]\n"
"s_waitcnt lgkmcnt(2)\n"
"ds_read_b128 v[160:163], v6 offset:9216\n"
"s_barrier\n"
"s_waitcnt lgkmcnt(2)\n"
"v_mfma_f32_16x16x32_bf16 v[238:241], v[136:139], v[132:135], v[238:241]\n"
"s_waitcnt lgkmcnt(1)\n"
"ds_read_b128 v[208:211], v4 offset:24576\n"
"v_mfma_f32_16x16x32_bf16 v[144:147], v[124:127], v[152:155], v[144:147]\n"
"v_mfma_f32_16x16x32_bf16 v[148:151], v[128:131], v[152:155], v[148:151]\n"
"s_waitcnt lgkmcnt(1)\n"
"v_mfma_f32_16x16x32_bf16 v[238:241], v[160:163], v[152:155], v[238:241]\n"
"s_waitcnt lgkmcnt(0)\n"
"ds_read_b128 v[140:143], v5 offset:24576\n"
"v_mfma_f32_16x16x32_bf16 v[184:187], v[8:11], v[208:211], 0\n"
"v_mfma_f32_16x16x32_bf16 v[132:135], v[12:15], v[208:211], 0\n"
"v_mfma_f32_16x16x32_bf16 v[136:139], v[16:19], v[208:211], 0\n"
"s_waitcnt lgkmcnt(0)\n"
"ds_read_b128 v[156:159], v4 offset:26624\n"
"v_mfma_f32_16x16x32_bf16 v[184:187], v[20:23], v[140:143], v[184:187]\n"
"v_mul_f32 v144, s60, v144\n"
"v_mul_f32 v145, s60, v145\n"
"v_mfma_f32_16x16x32_bf16 v[132:135], v[28:31], v[140:143], v[132:135]\n"
"v_cvt_pk_bf16_f32 v144, v144, v145\n"
"v_mul_f32 v146, s60, v146\n"
"v_mfma_f32_16x16x32_bf16 v[136:139], v[32:35], v[140:143], v[136:139]\n"
"ds_read_b128 v[208:211], v5 offset:26624\n"
"v_mul_f32 v147, s60, v147\n"
"v_cvt_pk_bf16_f32 v145, v146, v147\n"
"v_mul_f32 v148, s60, v148\n"
"v_mul_f32 v149, s60, v149\n"
"v_mul_f32 v150, s60, v150\n"
"v_mul_f32 v151, s60, v151\n"
"v_cvt_pk_bf16_f32 v146, v148, v149\n"
"s_waitcnt lgkmcnt(1)\n"
"v_mfma_f32_16x16x32_bf16 v[184:187], v[36:39], v[156:159], v[184:187]\n"
"v_cvt_pk_bf16_f32 v147, v150, v151\n"
"v_mul_f32 v238, s60, v238\n"
"v_mfma_f32_16x16x32_bf16 v[132:135], v[40:43], v[156:159], v[132:135]\n"
"v_bfe_u32 v213, v6, 2, 6\n"
"v_mul_f32 v239, s60, v239\n"
"v_mfma_f32_16x16x32_bf16 v[136:139], v[44:47], v[156:159], v[136:139]\n"
"s_waitcnt lgkmcnt(0)\n"
"ds_read_b128 v[8:11], v4 offset:28672\n"
"v_mul_f32 v240, s60, v240\n"
"v_mfma_f32_16x16x32_bf16 v[184:187], v[48:51], v[208:211], v[184:187]\n"
"s_add_u32 s68, s50, 4294964576\n"
"ds_read_b32 v213, v213 offset:1920\n"
"v_mul_f32 v241, s60, v241\n"
"v_mfma_f32_16x16x32_bf16 v[132:135], v[52:55], v[208:211], v[132:135]\n"
"v_cvt_pk_bf16_f32 v26, v238, v239\n"
"v_cvt_pk_bf16_f32 v27, v240, v241\n"
"v_mfma_f32_16x16x32_bf16 v[136:139], v[56:59], v[208:211], v[136:139]\n"
"s_waitcnt lgkmcnt(1)\n"
"ds_read_b128 v[12:15], v5 offset:28672\n"
"v_mfma_f32_16x16x32_bf16 v[184:187], v[60:63], v[8:11], v[184:187]\n"
"v_mfma_f32_16x16x32_bf16 v[132:135], v[64:67], v[8:11], v[132:135]\n"
"s_waitcnt lgkmcnt(1)\n"
"v_add_u32_sdwa v212, v213, v7 dst_sel:DWORD dst_unused:UNUSED_PAD src0_sel:WORD_0 src1_sel:DWORD\n"
"s_nop 0\n"
"v_max_u32 v239, s68, v212\n"
"v_mfma_f32_16x16x32_bf16 v[136:139], v[68:71], v[8:11], v[136:139]\n"
"s_waitcnt lgkmcnt(0)\n"
"ds_read_b128 v[208:211], v239 offset:2720\n"
"v_add_u32_sdwa v213, sext(v213), v212 dst_sel:DWORD dst_unused:UNUSED_PAD src0_sel:WORD_1 src1_sel:DWORD\n"
"v_mfma_f32_16x16x32_bf16 v[184:187], v[72:75], v[12:15], v[184:187]\n"
"s_add_u32 s69, s50, 4294963232\n"
"v_max_u32 v238, s68, v213\n"
"v_mfma_f32_16x16x32_bf16 v[132:135], v[76:79], v[12:15], v[132:135]\n"
"s_add_u32 s70, s50, 4294964272\n"
"ds_read_b128 v[240:243], v239 offset:2736\n"
"v_mfma_f32_16x16x32_bf16 v[136:139], v[80:83], v[12:15], v[136:139]\n"
"s_waitcnt lgkmcnt(1)\n"
"v_max_u32 v177, s69, v213\n"
"v_mfma_f32_16x16x32_bf16 v[208:211], v[190:193], v[144:147], v[208:211]\n"
"ds_read_b128 v[8:11], v238 offset:2752\n"
"v_cvt_pk_bf16_f32 v184, v184, v185\n"
"v_cvt_pk_bf16_f32 v185, v186, v187\n"
"v_cvt_pk_bf16_f32 v186, v132, v133\n"
"v_cvt_pk_bf16_f32 v187, v134, v135\n"
"v_mfma_f32_16x16x32_bf16 v[208:211], v[192:195], v[24:27], v[208:211]\n"
"s_waitcnt lgkmcnt(1)\n"
"v_cvt_pk_bf16_f32 v188, v136, v137\n"
"v_cvt_pk_bf16_f32 v189, v138, v139\n"
"v_mfma_f32_16x16x32_bf16 v[240:243], v[196:199], v[144:147], v[240:243]\n"
"ds_read_b128 v[12:15], v177 offset:4096\n"
"v_min_u32 v176, s70, v212\n"
"v_mfma_f32_16x16x32_bf16 v[240:243], v[198:201], v[24:27], v[240:243]\n"
"v_min_u32 v175, s70, v213\n"
"s_mov_b64 exec, s[52:53]\n"
"v_add_u32 v176, 1040, v176\n"
"v_add_u32 v175, 1040, v175\n"
"s_mov_b64 exec, -1\n"
"v_max3_f32 v172, v208, v209, v210\n"
"v_max_f32 v172, v172, v211\n"
"s_waitcnt lgkmcnt(1)\n"
"v_mfma_f32_16x16x32_bf16 v[8:11], v[202:205], v[144:147], v[8:11]\n"
"ds_read_b128 v[16:19], v176 offset:5408\n"
"v_max_u32 v174, s69, v212\n"
"v_mfma_f32_16x16x32_bf16 v[8:11], v[204:207], v[24:27], v[8:11]\n"
"s_waitcnt lgkmcnt(1)\n"
"v_max3_f32 v172, v172, v240, v241\n"
"v_max3_f32 v172, v172, v242, v243\n"
"v_mfma_f32_16x16x32_bf16 v[12:15], v[232:235], v[144:147], v[12:15]\n"
"ds_read_b128 v[208:211], v174 offset:4064\n"
"v_mfma_f32_16x16x32_bf16 v[12:15], v[234:237], v[24:27], v[12:15]\n"
"s_waitcnt lgkmcnt(1)\n"
"s_nop 0\n"
"v_max3_f32 v172, v172, v8, v9\n"
"v_max3_f32 v172, v172, v10, v11\n"
"v_mfma_f32_16x16x32_bf16 v[16:19], v[250:253], v[144:147], v[16:19]\n"
"ds_read_b128 v[240:243], v174 offset:4080\n"
"v_mfma_f32_16x16x32_bf16 v[16:19], v[252:255], v[24:27], v[16:19]\n"
"s_waitcnt lgkmcnt(1)\n"
"v_max3_f32 v172, v172, v12, v13\n"
"v_max3_f32 v172, v172, v14, v15\n"
"v_mfma_f32_16x16x32_bf16 v[208:211], v[220:223], v[144:147], v[208:211]\n"
"ds_read_b128 v[8:11], v176 offset:5424\n"
"v_mfma_f32_16x16x32_bf16 v[208:211], v[222:225], v[24:27], v[208:211]\n"
"s_waitcnt lgkmcnt(1)\n"
"s_nop 0\n"
"v_max3_f32 v172, v172, v16, v17\n"
"v_max3_f32 v172, v172, v18, v19\n"
"v_mfma_f32_16x16x32_bf16 v[240:243], v[226:229], v[144:147], v[240:243]\n"
"ds_read_b128 v[12:15], v175 offset:5440\n"
"v_mfma_f32_16x16x32_bf16 v[240:243], v[228:231], v[24:27], v[240:243]\n"
"s_waitcnt lgkmcnt(1)\n"
"v_max3_f32 v172, v172, v208, v209\n"
"v_max3_f32 v172, v172, v210, v211\n"
"v_mfma_f32_16x16x32_bf16 v[8:11], v[178:181], v[144:147], v[8:11]\n"
"ds_read_b128 v[16:19], v239 offset:2720\n"
"v_mfma_f32_16x16x32_bf16 v[8:11], v[180:183], v[24:27], v[8:11]\n"
"s_waitcnt lgkmcnt(1)\n"
"s_nop 0\n"
"v_max3_f32 v172, v172, v240, v241\n"
"v_max3_f32 v172, v172, v242, v243\n"
"v_mfma_f32_16x16x32_bf16 v[12:15], v[184:187], v[144:147], v[12:15]\n"
"ds_read_b128 v[208:211], v239 offset:2736\n"
"v_mfma_f32_16x16x32_bf16 v[12:15], v[186:189], v[24:27], v[12:15]\n"
"s_waitcnt lgkmcnt(1)\n"
"v_max3_f32 v172, v172, v8, v9\n"
"v_max3_f32 v172, v172, v10, v11\n"
"v_mfma_f32_16x16x32_bf16 v[16:19], v[190:193], v[144:147], v[16:19]\n"
"ds_read_b128 v[240:243], v238 offset:2752\n"
"v_mfma_f32_16x16x32_bf16 v[16:19], v[192:195], v[24:27], v[16:19]\n"
"s_nop 1\n"
"v_max3_f32 v172, v172, v12, v13\n"
"v_max3_f32 v172, v172, v14, v15\n"
"v_mov_b32 v173, v172\n"
"s_nop 1\n"
"v_permlane16_swap_b32 v172, v173\n"
"v_max_f32 v172, v172, v173\n"
"v_mov_b32 v173, v172\n"
"s_nop 1\n"
"v_permlane32_swap_b32 v172, v173\n"
"v_max_f32 v172, v172, v173\n"
"v_sub_f32 v18, v18, v172\n"
"v_exp_f32 v18, v18\n"
"v_sub_f32 v19, v19, v172\n"
"v_exp_f32 v19, v19\n"
"s_waitcnt lgkmcnt(1)\n"
"v_mfma_f32_16x16x32_bf16 v[208:211], v[196:199], v[144:147], v[208:211]\n"
"ds_read_b128 v[190:193], v177 offset:4096\n"
"v_cvt_pk_bf16_f32 v17, v18, v19\n"
"v_mfma_f32_16x16x32_bf16 v[208:211], v[198:201], v[24:27], v[208:211]\n"
"ds_write_addtid_b32 v17 offset:55552\n"
"s_waitcnt lgkmcnt(2)\n"
"v_mfma_f32_16x16x32_bf16 v[240:243], v[202:205], v[144:147], v[240:243]\n"
"ds_read_b128 v[8:11], v176 offset:5408\n"
"v_mfma_f32_16x16x32_bf16 v[240:243], v[204:207], v[24:27], v[240:243]\n"
"s_nop 2\n"
"v_sub_f32 v208, v208, v172\n"
"v_exp_f32 v208, v208\n"
"v_sub_f32 v209, v209, v172\n"
"v_exp_f32 v209, v209\n"
"v_sub_f32 v210, v210, v172\n"
"v_exp_f32 v210, v210\n"
"v_sub_f32 v211, v211, v172\n"
"v_exp_f32 v211, v211\n"
"v_sub_f32 v240, v240, v172\n"
"v_exp_f32 v240, v240\n"
"v_sub_f32 v241, v241, v172\n"
"v_exp_f32 v241, v241\n"
"v_sub_f32 v242, v242, v172\n"
"v_exp_f32 v242, v242\n"
"v_cvt_pk_bf16_f32 v208, v208, v209\n"
"v_cvt_pk_bf16_f32 v209, v210, v211\n"
"v_sub_f32 v243, v243, v172\n"
"v_exp_f32 v243, v243\n"
"v_cvt_pk_bf16_f32 v240, v240, v241\n"
"v_cvt_pk_bf16_f32 v241, v242, v243\n"
"ds_write_addtid_b32 v208 offset:55808\n"
"ds_write_addtid_b32 v209 offset:56064\n"
"s_waitcnt lgkmcnt(4)\n"
"v_mfma_f32_16x16x32_bf16 v[190:193], v[232:235], v[144:147], v[190:193]\n"
"ds_read_b128 v[194:197], v174 offset:4064\n"
"v_mfma_f32_16x16x32_bf16 v[190:193], v[234:237], v[24:27], v[190:193]\n"
"ds_write_addtid_b32 v240 offset:56320\n"
"ds_write_addtid_b32 v241 offset:56576\n"
"s_waitcnt lgkmcnt(5)\n"
"v_mfma_f32_16x16x32_bf16 v[8:11], v[250:253], v[144:147], v[8:11]\n"
"ds_read_b128 v[198:201], v174 offset:4080\n"
"v_mfma_f32_16x16x32_bf16 v[8:11], v[252:255], v[24:27], v[8:11]\n"
"s_nop 1\n"
"v_sub_f32 v190, v190, v172\n"
"v_exp_f32 v190, v190\n"
"v_sub_f32 v191, v191, v172\n"
"v_exp_f32 v191, v191\n"
"v_sub_f32 v192, v192, v172\n"
"v_exp_f32 v192, v192\n"
"v_sub_f32 v193, v193, v172\n"
"v_exp_f32 v193, v193\n"
"v_sub_f32 v10, v10, v172\n"
"v_exp_f32 v10, v10\n"
"v_cvt_pk_bf16_f32 v190, v190, v191\n"
"v_cvt_pk_bf16_f32 v191, v192, v193\n"
"v_sub_f32 v11, v11, v172\n"
"v_exp_f32 v11, v11\n"
"ds_write_addtid_b32 v190 offset:56832\n"
"v_cvt_pk_bf16_f32 v9, v10, v11\n"
"ds_write_addtid_b32 v191 offset:57088\n"
"s_waitcnt lgkmcnt(5)\n"
"v_mfma_f32_16x16x32_bf16 v[194:197], v[220:223], v[144:147], v[194:197]\n"
"ds_read_b128 v[202:205], v176 offset:5424\n"
"v_mfma_f32_16x16x32_bf16 v[194:197], v[222:225], v[24:27], v[194:197]\n"
"ds_write_addtid_b32 v9 offset:34560\n"
"s_waitcnt lgkmcnt(4)\n"
"v_mfma_f32_16x16x32_bf16 v[198:201], v[226:229], v[144:147], v[198:201]\n"
"ds_read_b128 v[206:209], v175 offset:5440\n"
"v_mfma_f32_16x16x32_bf16 v[198:201], v[228:231], v[24:27], v[198:201]\n"
"s_waitcnt lgkmcnt(1)\n"
"s_barrier\n"
"s_nop 0\n"
"v_sub_f32 v196, v196, v172\n"
"v_exp_f32 v196, v196\n"
"v_sub_f32 v197, v197, v172\n"
"v_exp_f32 v197, v197\n"
"s_nop 0\n"
"v_cvt_pk_bf16_f32 v195, v196, v197\n"
"ds_write_addtid_b32 v195 offset:58624\n"
"v_mfma_f32_16x16x32_bf16 v[202:205], v[178:181], v[144:147], v[202:205]\n"
"ds_read_b128 v[190:193], v4 offset:30720\n"
"v_sub_f32 v198, v198, v172\n"
"v_exp_f32 v198, v198\n"
"v_sub_f32 v199, v199, v172\n"
"v_exp_f32 v199, v199\n"
"v_sub_f32 v200, v200, v172\n"
"v_exp_f32 v200, v200\n"
"v_sub_f32 v201, v201, v172\n"
"v_exp_f32 v201, v201\n"
"v_mfma_f32_16x16x32_bf16 v[202:205], v[180:183], v[24:27], v[202:205]\n"
"v_cvt_pk_bf16_f32 v198, v198, v199\n"
"v_cvt_pk_bf16_f32 v199, v200, v201\n"
"ds_write_addtid_b32 v198 offset:58880\n"
"ds_write_addtid_b32 v199 offset:59136\n"
"s_waitcnt lgkmcnt(4)\n"
"v_mfma_f32_16x16x32_bf16 v[206:209], v[184:187], v[144:147], v[206:209]\n"
"v_mfma_f32_16x16x32_bf16 v[206:209], v[186:189], v[24:27], v[206:209]\n"
"s_waitcnt lgkmcnt(2)\n"
"ds_read_b128 v[194:197], v5 offset:30720\n"
"v_mfma_f32_16x16x32_bf16 v[210:213], v[84:87], v[190:193], 0\n"
"v_sub_f32 v202, v202, v172\n"
"v_exp_f32 v202, v202\n"
"v_sub_f32 v203, v203, v172\n"
"v_exp_f32 v203, v203\n"
"v_mfma_f32_16x16x32_bf16 v[184:187], v[88:91], v[190:193], 0\n"
"ds_read_b128 v[198:201], v6 offset:5120\n"
"v_cvt_pk_bf16_f32 v202, v202, v203\n"
"v_sub_f32 v204, v204, v172\n"
"v_exp_f32 v204, v204\n"
"v_mfma_f32_16x16x32_bf16 v[232:235], v[92:95], v[190:193], 0\n"
"s_waitcnt lgkmcnt(1)\n"
"ds_read_b128 v[236:239], v4 offset:32768\n"
"v_sub_f32 v205, v205, v172\n"
"v_exp_f32 v205, v205\n"
"v_mfma_f32_16x16x32_bf16 v[210:213], v[96:99], v[194:197], v[210:213]\n"
"ds_write_addtid_b32 v202 offset:57344\n"
"v_cvt_pk_bf16_f32 v203, v204, v205\n"
"v_mfma_f32_16x16x32_bf16 v[184:187], v[100:103], v[194:197], v[184:187]\n"
"ds_write_addtid_b32 v203 offset:57600\n"
"ds_read_b128 v[188:191], v6 offset:6144\n"
"s_waitcnt lgkmcnt(4)\n"
"v_mfma_f32_16x16x32_bf16 v[232:235], v[198:201], v[194:197], v[232:235]\n"
"s_waitcnt lgkmcnt(3)\n"
"ds_read_b128 v[240:243], v5 offset:32768\n"
"v_sub_f32 v206, v206, v172\n"
"v_exp_f32 v206, v206\n"
"v_mfma_f32_16x16x32_bf16 v[210:213], v[104:107], v[236:239], v[210:213]\n"
"v_sub_f32 v207, v207, v172\n"
"v_exp_f32 v207, v207\n"
"v_mfma_f32_16x16x32_bf16 v[184:187], v[108:111], v[236:239], v[184:187]\n"
"ds_read_b128 v[192:195], v6 offset:7168\n"
"v_sub_f32 v208, v208, v172\n"
"v_exp_f32 v208, v208\n"
"v_cvt_pk_bf16_f32 v206, v206, v207\n"
"s_waitcnt lgkmcnt(2)\n"
"v_mfma_f32_16x16x32_bf16 v[232:235], v[188:191], v[236:239], v[232:235]\n"
"ds_read_b128 v[196:199], v4 offset:34816\n"
"v_bfe_u32 v4, v6, 2, 6\n"
"v_sub_f32 v209, v209, v172\n"
"v_exp_f32 v209, v209\n"
"s_waitcnt lgkmcnt(2)\n"
"v_mfma_f32_16x16x32_bf16 v[210:213], v[112:115], v[240:243], v[210:213]\n"
"s_add_u32 s71, s50, 4294963248\n"
"ds_write_addtid_b32 v206 offset:57856\n"
"v_cvt_pk_bf16_f32 v207, v208, v209\n"
"v_mfma_f32_16x16x32_bf16 v[184:187], v[116:119], v[240:243], v[184:187]\n"
"ds_write_addtid_b32 v207 offset:58112\n"
"s_waitcnt lgkmcnt(3)\n"
"v_mfma_f32_16x16x32_bf16 v[232:235], v[192:195], v[240:243], v[232:235]\n"
"s_waitcnt lgkmcnt(2)\n"
"ds_read_b32 v4, v4 offset:1984\n"
"ds_read_b128 v[188:191], v6 offset:8192\n"
"ds_read_b128 v[200:203], v5 offset:34816\n"
"v_mfma_f32_16x16x32_bf16 v[210:213], v[0:3], v[196:199], v[210:213]\n"
"s_add_u32 s72, s50, 4294964288\n"
"v_mfma_f32_16x16x32_bf16 v[184:187], v[120:123], v[196:199], v[184:187]\n"
"s_waitcnt lgkmcnt(2)\n"
"ds_read_b128 v[192:195], v6 offset:9216\n"
"v_add_u32_sdwa v5, v4, v7 dst_sel:DWORD dst_unused:UNUSED_PAD src0_sel:WORD_0 src1_sel:DWORD\n"
"s_nop 0\n"
"v_max_u32 v209, s71, v5\n"
"s_waitcnt lgkmcnt(2)\n"
"v_mfma_f32_16x16x32_bf16 v[232:235], v[188:191], v[196:199], v[232:235]\n"
"s_waitcnt lgkmcnt(1)\n"
"ds_read_b128 v[204:207], v209 offset:4048\n"
"v_min_u32 v208, s72, v5\n"
"v_mfma_f32_16x16x32_bf16 v[210:213], v[124:127], v[200:203], v[210:213]\n"
"s_mov_b64 exec, s[52:53]\n"
"v_add_u32 v208, 1040, v208\n"
"s_mov_b64 exec, -1\n"
"v_mfma_f32_16x16x32_bf16 v[184:187], v[128:131], v[200:203], v[184:187]\n"
"s_waitcnt lgkmcnt(1)\n"
"s_barrier\n"
"ds_read_b128 v[188:191], v209 offset:4064\n"
"v_mfma_f32_16x16x32_bf16 v[232:235], v[192:195], v[200:203], v[232:235]\n"
"v_mul_f32 v210, s60, v210\n"
"v_mul_f32 v211, s60, v211\n"
"v_cvt_pk_bf16_f32 v210, v210, v211\n"
"v_mul_f32 v212, s60, v212\n"
"v_mul_f32 v213, s60, v213\n"
"v_cvt_pk_bf16_f32 v211, v212, v213\n"
"v_mul_f32 v184, s60, v184\n"
"v_mul_f32 v185, s60, v185\n"
"v_mul_f32 v186, s60, v186\n"
"v_mul_f32 v187, s60, v187\n"
"v_cvt_pk_bf16_f32 v212, v184, v185\n"
"v_cvt_pk_bf16_f32 v213, v186, v187\n"
"v_mul_f32 v232, s60, v232\n"
"v_mul_f32 v233, s60, v233\n"
"s_waitcnt lgkmcnt(1)\n"
"v_mfma_f32_16x16x32_bf16 v[204:207], v[214:217], v[210:213], v[204:207]\n"
"ds_read_b128 v[192:195], v208 offset:5392\n"
"v_mul_f32 v234, s60, v234\n"
"v_mul_f32 v235, s60, v235\n"
"v_cvt_pk_bf16_f32 v26, v232, v233\n"
"v_cvt_pk_bf16_f32 v27, v234, v235\n"
"s_nop 1\n"
"v_mfma_f32_16x16x32_bf16 v[204:207], v[216:219], v[24:27], v[204:207]\n"
"s_waitcnt lgkmcnt(1)\n"
"v_mfma_f32_16x16x32_bf16 v[188:191], v[220:223], v[210:213], v[188:191]\n"
"ds_read_b128 v[184:187], v209 offset:4080\n"
"v_mfma_f32_16x16x32_bf16 v[188:191], v[222:225], v[24:27], v[188:191]\n"
"s_waitcnt lgkmcnt(1)\n"
"s_nop 2\n"
"v_max3_f32 v176, v204, v205, v206\n"
"v_max_f32 v176, v176, v207\n"
"v_mfma_f32_16x16x32_bf16 v[192:195], v[244:247], v[210:213], v[192:195]\n"
"ds_read_b128 v[196:199], v208 offset:5408\n"
"v_mfma_f32_16x16x32_bf16 v[192:195], v[246:249], v[24:27], v[192:195]\n"
"s_waitcnt lgkmcnt(1)\n"
"v_max3_f32 v176, v176, v188, v189\n"
"v_max3_f32 v176, v176, v190, v191\n"
"v_mfma_f32_16x16x32_bf16 v[184:187], v[226:229], v[210:213], v[184:187]\n"
"ds_read_b128 v[200:203], v208 offset:5424\n"
"v_mfma_f32_16x16x32_bf16 v[184:187], v[228:231], v[24:27], v[184:187]\n"
"s_waitcnt lgkmcnt(1)\n"
"s_nop 0\n"
"v_max3_f32 v176, v176, v192, v193\n"
"v_max3_f32 v176, v176, v194, v195\n"
"v_mfma_f32_16x16x32_bf16 v[196:199], v[250:253], v[210:213], v[196:199]\n"
"ds_read_b128 v[188:191], v209 offset:4048\n"
"v_mfma_f32_16x16x32_bf16 v[196:199], v[252:255], v[24:27], v[196:199]\n"
"s_waitcnt lgkmcnt(1)\n"
"v_max3_f32 v176, v176, v184, v185\n"
"v_max3_f32 v176, v176, v186, v187\n"
"v_mfma_f32_16x16x32_bf16 v[200:203], v[178:181], v[210:213], v[200:203]\n"
"ds_read_b128 v[192:195], v209 offset:4064\n"
"v_mfma_f32_16x16x32_bf16 v[200:203], v[180:183], v[24:27], v[200:203]\n"
"s_waitcnt lgkmcnt(1)\n"
"s_nop 0\n"
"v_max3_f32 v176, v176, v196, v197\n"
"v_max3_f32 v176, v176, v198, v199\n"
"v_mfma_f32_16x16x32_bf16 v[188:191], v[214:217], v[210:213], v[188:191]\n"
"ds_read_b128 v[184:187], v208 offset:5392\n"
"v_mfma_f32_16x16x32_bf16 v[188:191], v[216:219], v[24:27], v[188:191]\n"
"s_nop 0\n"
"v_max3_f32 v176, v176, v200, v201\n"
"v_max3_f32 v176, v176, v202, v203\n"
"v_mov_b32 v177, v176\n"
"s_nop 1\n"
"v_permlane16_swap_b32 v176, v177\n"
"v_max_f32 v176, v176, v177\n"
"v_mov_b32 v177, v176\n"
"s_nop 1\n"
"v_permlane32_swap_b32 v176, v177\n"
"v_max_f32 v176, v176, v177\n"
"v_sub_f32 v188, v188, v176\n"
"v_exp_f32 v188, v188\n"
"v_sub_f32 v189, v189, v176\n"
"v_exp_f32 v189, v189\n"
"v_sub_f32 v190, v190, v176\n"
"v_exp_f32 v190, v190\n"
"v_sub_f32 v191, v191, v176\n"
"v_exp_f32 v191, v191\n"
"s_waitcnt lgkmcnt(1)\n"
"v_mfma_f32_16x16x32_bf16 v[192:195], v[220:223], v[210:213], v[192:195]\n"
"ds_read_b128 v[196:199], v209 offset:4080\n"
"v_cvt_pk_bf16_f32 v188, v188, v189\n"
"v_mfma_f32_16x16x32_bf16 v[192:195], v[222:225], v[24:27], v[192:195]\n"
"ds_write_addtid_b32 v188 offset:53248\n"
"v_cvt_pk_bf16_f32 v189, v190, v191\n"
"ds_write_addtid_b32 v189 offset:53504\n"
"s_waitcnt lgkmcnt(3)\n"
"v_mfma_f32_16x16x32_bf16 v[184:187], v[244:247], v[210:213], v[184:187]\n"
"ds_read_b128 v[200:203], v208 offset:5408\n"
"v_mfma_f32_16x16x32_bf16 v[184:187], v[246:249], v[24:27], v[184:187]\n"
"s_nop 0\n"
"v_sub_f32 v192, v192, v176\n"
"v_exp_f32 v192, v192\n"
"v_sub_f32 v193, v193, v176\n"
"v_exp_f32 v193, v193\n"
"v_sub_f32 v194, v194, v176\n"
"v_exp_f32 v194, v194\n"
"v_sub_f32 v195, v195, v176\n"
"v_exp_f32 v195, v195\n"
"v_sub_f32 v184, v184, v176\n"
"v_exp_f32 v184, v184\n"
"v_sub_f32 v185, v185, v176\n"
"v_exp_f32 v185, v185\n"
"v_cvt_pk_bf16_f32 v192, v192, v193\n"
"v_sub_f32 v186, v186, v176\n"
"v_exp_f32 v186, v186\n"
"v_cvt_pk_bf16_f32 v193, v194, v195\n"
"v_sub_f32 v187, v187, v176\n"
"v_exp_f32 v187, v187\n"
"v_cvt_pk_bf16_f32 v184, v184, v185\n"
"v_cvt_pk_bf16_f32 v185, v186, v187\n"
"ds_write_addtid_b32 v192 offset:53760\n"
"ds_write_addtid_b32 v193 offset:54016\n"
"s_waitcnt lgkmcnt(5)\n"
"v_mfma_f32_16x16x32_bf16 v[196:199], v[226:229], v[210:213], v[196:199]\n"
"ds_read_b128 v[188:191], v208 offset:5424\n"
"v_mfma_f32_16x16x32_bf16 v[196:199], v[228:231], v[24:27], v[196:199]\n"
"ds_write_addtid_b32 v184 offset:54272\n"
"ds_write_addtid_b32 v185 offset:54528\n"
"s_waitcnt lgkmcnt(5)\n"
"v_mfma_f32_16x16x32_bf16 v[200:203], v[250:253], v[210:213], v[200:203]\n"
"v_mfma_f32_16x16x32_bf16 v[200:203], v[252:255], v[24:27], v[200:203]\n"
"s_nop 2\n"
"v_sub_f32 v196, v196, v176\n"
"v_exp_f32 v196, v196\n"
"v_sub_f32 v197, v197, v176\n"
"v_exp_f32 v197, v197\n"
"v_sub_f32 v198, v198, v176\n"
"v_exp_f32 v198, v198\n"
"v_sub_f32 v199, v199, v176\n"
"v_exp_f32 v199, v199\n"
"v_cvt_pk_bf16_f32 v196, v196, v197\n"
"v_cvt_pk_bf16_f32 v197, v198, v199\n"
"v_sub_f32 v200, v200, v176\n"
"v_exp_f32 v200, v200\n"
"v_sub_f32 v201, v201, v176\n"
"v_exp_f32 v201, v201\n"
"v_sub_f32 v202, v202, v176\n"
"v_exp_f32 v202, v202\n"
"v_sub_f32 v203, v203, v176\n"
"v_exp_f32 v203, v203\n"
"v_cvt_pk_bf16_f32 v200, v200, v201\n"
"v_cvt_pk_bf16_f32 v201, v202, v203\n"
"ds_write_addtid_b32 v196 offset:4096\n"
"ds_write_addtid_b32 v197 offset:4352\n"
"s_waitcnt lgkmcnt(4)\n"
"v_mfma_f32_16x16x32_bf16 v[188:191], v[178:181], v[210:213], v[188:191]\n"
"ds_write_addtid_b32 v200 offset:54784\n"
"v_mfma_f32_16x16x32_bf16 v[188:191], v[180:183], v[24:27], v[188:191]\n"
"ds_write_addtid_b32 v201 offset:55040\n"
"s_nop 6\n"
"v_sub_f32 v188, v188, v176\n"
"v_exp_f32 v188, v188\n"
"v_sub_f32 v189, v189, v176\n"
"v_exp_f32 v189, v189\n"
"s_nop 0\n"
"v_cvt_pk_bf16_f32 v188, v188, v189\n"
"ds_write_addtid_b32 v188 offset:4608\n"
"v_sub_f32 v190, v190, v176\n"
"v_exp_f32 v190, v190\n"
"v_sub_f32 v191, v191, v176\n"
"v_exp_f32 v191, v191\n"
"s_nop 0\n"
"v_cvt_pk_bf16_f32 v189, v190, v191\n"
"ds_write_addtid_b32 v189 offset:4864\n"
"s_waitcnt lgkmcnt(0)\n"
"s_barrier\n"
"s_barrier\n"
"s_barrier\n"
    ,
"s_add_u32 s50, s54, 53248\n"
"s_lshr_b32 s41, s40, 1\n"
"s_sub_u32 s55, 0, s41\n"
"s_mul_i32 s51, s41, 4096\n"
"s_lshl_b32 s41, s40, 10\n"
"s_add_u32 s51, s51, s41\n"
"s_lshl_b32 s65, s40, 2\n"
"s_add_u32 s65, s65, 32192\n"
"s_andn2_b32 s41, s45, 7\n"
"s_add_u32 s56, s56, s41\n"
"s_addc_u32 s57, s57, 0\n"
"s_add_u32 s66, s66, s41\n"
"s_addc_u32 s67, s67, 0\n"
"s_mul_i32 s68, s40, 1552\n"
"s_add_u32 s68, s68, 57344\n"
"s_setprio 0\n"
"s_movk_i32 s40, 768\n"
"s_mov_b64 s[62:63], 0\n"
"s_movk_i32 s64, 0x207\n"
"s_add_u32 s58, s56, 128\n"
"s_addc_u32 s59, s57, 0\n"
"s_add_u32 s60, s56, 256\n"
"s_addc_u32 s61, s57, 0\n"
"v_subrev_u32 v179, 24576, v10\n"
"s_waitcnt vmcnt(0)\n"
"s_barrier\n"
"s_add_u32 s42, s46, 110976\n"
"s_addc_u32 s43, s47, 0\n"
"s_add_u32 m0, s50, 27648\n"
"s_nop 0\n"
"global_load_lds_dwordx4 v0, s[42:43]\n"
"s_add_u32 m0, s50, 28672\n"
"s_nop 0\n"
"global_load_lds_dwordx4 v1, s[42:43]\n"
"s_add_u32 m0, s50, 29696\n"
"s_nop 0\n"
"global_load_lds_dwordx4 v2, s[42:43]\n"
"s_add_u32 s42, s46, 147840\n"
"s_addc_u32 s43, s47, 0\n"
"s_add_u32 m0, s50, 49152\n"
"s_nop 0\n"
"global_load_lds_dwordx4 v0, s[42:43]\n"
"s_add_u32 m0, s50, 50176\n"
"s_nop 0\n"
"global_load_lds_dwordx4 v1, s[42:43]\n"
"s_add_u32 m0, s50, 51200\n"
"s_nop 0\n"
"global_load_lds_dwordx4 v2, s[42:43]\n"
"s_add_u32 s42, s46, 184704\n"
"s_addc_u32 s43, s47, 0\n"
"s_add_u32 m0, s50, 52224\n"
"s_nop 0\n"
"global_load_lds_dwordx4 v0, s[42:43]\n"
"s_add_u32 m0, s50, 53248\n"
"s_nop 0\n"
"global_load_lds_dwordx4 v1, s[42:43]\n"
"s_add_u32 m0, s50, 54272\n"
"s_nop 0\n"
"global_load_lds_dwordx4 v2, s[42:43]\n"
"s_add_u32 s42, s46, 768\n"
"s_addc_u32 s43, s47, 0\n"
"s_add_u32 m0, s50, 0\n"
"s_nop 0\n"
"global_load_lds_dwordx4 v0, s[42:43]\n"
"s_add_u32 m0, s50, 1024\n"
"s_nop 0\n"
"global_load_lds_dwordx4 v1, s[42:43]\n"
"s_add_u32 m0, s50, 2048\n"
"s_nop 0\n"
"global_load_lds_dwordx4 v2, s[42:43]\n"
"s_waitcnt vmcnt(9)\n"
"s_barrier\n"
"s_add_u32 s42, s46, 37632\n"
"s_addc_u32 s43, s47, 0\n"
"s_add_u32 m0, s50, 3072\n"
"s_nop 0\n"
"global_load_lds_dwordx4 v0, s[42:43]\n"
"s_add_u32 m0, s50, 4096\n"
"s_nop 0\n"
"global_load_lds_dwordx4 v1, s[42:43]\n"
"s_add_u32 m0, s50, 5120\n"
"s_nop 0\n"
"global_load_lds_dwordx4 v2, s[42:43]\n"
"s_add_u32 s42, s46, 74496\n"
"s_addc_u32 s43, s47, 0\n"
"s_add_u32 m0, s50, 4294918144\n"
"s_nop 0\n"
"global_load_lds_dwordx4 v0, s[42:43]\n"
"s_add_u32 m0, s50, 4294919168\n"
"s_nop 0\n"
"global_load_lds_dwordx4 v1, s[42:43]\n"
"s_add_u32 m0, s50, 4294920192\n"
"s_nop 0\n"
"global_load_lds_dwordx4 v2, s[42:43]\n"
"s_waitcnt vmcnt(9)\n"
"s_barrier\n"
"s_add_u32 s42, s46, 111360\n"
"s_addc_u32 s43, s47, 0\n"
"s_add_u32 m0, s50, 4294921216\n"
"s_nop 0\n"
"global_load_lds_dwordx4 v0, s[42:43]\n"
"s_add_u32 m0, s50, 4294922240\n"
"s_nop 0\n"
"global_load_lds_dwordx4 v1, s[42:43]\n"
"s_add_u32 m0, s50, 4294923264\n"
"s_nop 0\n"
"global_load_lds_dwordx4 v2, s[42:43]\n"
"s_barrier\n"
"s_add_u32 s42, s46, 148224\n"
"s_addc_u32 s43, s47, 0\n"
"s_add_u32 m0, s50, 24576\n"
"s_nop 0\n"
"global_load_lds_dwordx4 v0, s[42:43]\n"
"s_add_u32 m0, s50, 25600\n"
"s_nop 0\n"
"global_load_lds_dwordx4 v1, s[42:43]\n"
"s_add_u32 m0, s50, 26624\n"
"s_nop 0\n"
"global_load_lds_dwordx4 v2, s[42:43]\n"
"s_add_u32 s42, s46, 185088\n"
"s_addc_u32 s43, s47, 0\n"
"s_add_u32 m0, s50, 27648\n"
"s_nop 0\n"
"global_load_lds_dwordx4 v0, s[42:43]\n"
"s_add_u32 m0, s50, 28672\n"
"s_nop 0\n"
"global_load_lds_dwordx4 v1, s[42:43]\n"
"s_add_u32 m0, s50, 29696\n"
"s_nop 0\n"
"global_load_lds_dwordx4 v2, s[42:43]\n"
"s_waitcnt vmcnt(15)\n"
"ds_read_b64_tr_b16 v[26:27], v10 offset:29504\n"
"s_andn2_b32 s41, 576, s55\n"
"v_add_u32 v178, s41, v15\n"
"ds_read_u16 v178, v178 offset:224\n"
"ds_read_b64_tr_b16 v[28:29], v10 offset:28672\n"
"ds_read_b64_tr_b16 v[30:31], v10 offset:29440\n"
"ds_read_b128 v[180:183], v12 offset:24576\n"
"ds_read_b64_tr_b16 v[32:33], v10 offset:28704\n"
"ds_read_b64_tr_b16 v[34:35], v10 offset:29472\n"
"ds_read_b64_tr_b16 v[24:25], v10 offset:28736\n"
"ds_read_b128 v[184:187], v12 offset:30720\n"
"ds_read_b128 v[188:191], v12 offset:36864\n"
"ds_read_u16 v23, v21 offset:1152\n"
"s_waitcnt lgkmcnt(9)\n"
"v_mad_u32_u24 v178, v178, s40, v16\n"
"s_add_u32 m0, s51, 102400\n"
"s_nop 0\n"
"global_load_lds_dwordx4 v178, s[56:57]\n"
"s_add_u32 m0, s51, 104448\n"
"s_nop 0\n"
"global_load_lds_dwordx4 v178, s[58:59]\n"
"s_add_u32 m0, s51, 106496\n"
"s_nop 0\n"
"global_load_lds_dwordx4 v178, s[60:61]\n"
"s_waitcnt vmcnt(15)\n"
"ds_read_b64_tr_b16 v[38:39], v10 offset:32576\n"
"s_and_b32 s41, 32, s55\n"
"v_add_u32 v178, s41, v15\n"
"ds_read_u16 v178, v178 offset:352\n"
"s_waitcnt lgkmcnt(2)\n"
"v_mad_u32_u24 v22, v23, s40, v18\n"
"v_mad_u32_u24 v23, v23, s40, v19\n"
"ds_read_b64_tr_b16 v[40:41], v10 offset:31744\n"
"ds_read_b64_tr_b16 v[42:43], v10 offset:32512\n"
"ds_read_b64_tr_b16 v[44:45], v10 offset:31776\n"
"ds_read_b64_tr_b16 v[46:47], v10 offset:32544\n"
"ds_read_b64_tr_b16 v[36:37], v10 offset:31808\n"
"ds_read_u16 v177, v21 offset:1184\n"
"s_waitcnt lgkmcnt(6)\n"
"v_mad_u32_u24 v178, v178, s40, v16\n"
"s_add_u32 m0, s51, 114688\n"
"s_nop 0\n"
"global_load_lds_dwordx4 v178, s[56:57]\n"
"s_add_u32 m0, s51, 116736\n"
"s_nop 0\n"
"global_load_lds_dwordx4 v178, s[58:59]\n"
"s_add_u32 m0, s51, 118784\n"
"s_nop 0\n"
"global_load_lds_dwordx4 v178, s[60:61]\n"
"v_mfma_f32_16x16x32_bf16 v[192:195], v[180:183], v[28:31], 0\n"
"s_waitcnt vmcnt(15)\n"
"ds_read_b64_tr_b16 v[48:49], v179 offset:4096\n"
"v_mfma_f32_16x16x32_bf16 v[196:199], v[180:183], v[32:35], 0\n"
"s_waitcnt lgkmcnt(1)\n"
"ds_read_b128 v[200:203], v13 offset:24576\n"
"v_mad_u32_u24 v176, v177, s40, v18\n"
"v_mfma_f32_16x16x32_bf16 v[204:207], v[180:183], v[24:27], 0\n"
"ds_read_b64_tr_b16 v[50:51], v179 offset:4864\n"
"v_mad_u32_u24 v177, v177, s40, v19\n"
"v_mfma_f32_16x16x32_bf16 v[180:183], v[184:187], v[28:31], 0\n"
"ds_read_b64_tr_b16 v[52:53], v179 offset:4128\n"
"v_mfma_f32_16x16x32_bf16 v[214:217], v[184:187], v[32:35], 0\n"
"ds_read_b128 v[208:211], v13 offset:30720\n"
"v_mfma_f32_16x16x32_bf16 v[218:221], v[184:187], v[24:27], 0\n"
"ds_read_b64_tr_b16 v[54:55], v179 offset:4896\n"
"v_mfma_f32_16x16x32_bf16 v[222:225], v[188:191], v[28:31], 0\n"
"ds_read_b64_tr_b16 v[56:57], v179 offset:4160\n"
"v_mfma_f32_16x16x32_bf16 v[226:229], v[188:191], v[32:35], 0\n"
"ds_read_b128 v[184:187], v13 offset:36864\n"
"v_mfma_f32_16x16x32_bf16 v[230:233], v[188:191], v[24:27], 0\n"
"s_waitcnt lgkmcnt(6)\n"
"ds_read_b64_tr_b16 v[58:59], v179 offset:4928\n"
"v_mfma_f32_16x16x32_bf16 v[192:195], v[200:203], v[40:43], v[192:195]\n"
"v_mfma_f32_16x16x32_bf16 v[196:199], v[200:203], v[44:47], v[196:199]\n"
"ds_read_b128 v[188:191], v12 offset:26624\n"
"v_mfma_f32_16x16x32_bf16 v[204:207], v[200:203], v[36:39], v[204:207]\n"
"s_waitcnt lgkmcnt(5)\n"
"v_mfma_f32_16x16x32_bf16 v[180:183], v[208:211], v[40:43], v[180:183]\n"
"v_mfma_f32_16x16x32_bf16 v[214:217], v[208:211], v[44:47], v[214:217]\n"
"ds_read_b128 v[200:203], v12 offset:32768\n"
"v_mfma_f32_16x16x32_bf16 v[218:221], v[208:211], v[36:39], v[218:221]\n"
"s_waitcnt lgkmcnt(3)\n"
"v_mfma_f32_16x16x32_bf16 v[222:225], v[184:187], v[40:43], v[222:225]\n"
"s_waitcnt vmcnt(12)\n"
"ds_read_b64_tr_b16 v[60:61], v179 offset:7168\n"
"v_mfma_f32_16x16x32_bf16 v[226:229], v[184:187], v[44:47], v[226:229]\n"
"ds_read_b128 v[208:211], v12 offset:38912\n"
"v_mfma_f32_16x16x32_bf16 v[230:233], v[184:187], v[36:39], v[230:233]\n"
"s_waitcnt lgkmcnt(3)\n"
"ds_read_b64_tr_b16 v[62:63], v179 offset:7936\n"
"v_mfma_f32_16x16x32_bf16 v[192:195], v[188:191], v[48:51], v[192:195]\n"
"ds_read_b64_tr_b16 v[64:65], v179 offset:7200\n"
"v_mfma_f32_16x16x32_bf16 v[196:199], v[188:191], v[52:55], v[196:199]\n"
"ds_read_b128 v[184:187], v13 offset:26624\n"
"v_mfma_f32_16x16x32_bf16 v[204:207], v[188:191], v[56:59], v[204:207]\n"
"s_waitcnt lgkmcnt(5)\n"
"ds_read_b64_tr_b16 v[66:67], v179 offset:7968\n"
"v_mfma_f32_16x16x32_bf16 v[180:183], v[200:203], v[48:51], v[180:183]\n"
"ds_read_b64_tr_b16 v[68:69], v179 offset:7232\n"
"v_mfma_f32_16x16x32_bf16 v[214:217], v[200:203], v[52:55], v[214:217]\n"
"ds_read_b64_tr_b16 v[70:71], v179 offset:8000\n"
"v_mfma_f32_16x16x32_bf16 v[218:221], v[200:203], v[56:59], v[218:221]\n"
"s_waitcnt lgkmcnt(6)\n"
"ds_read_b128 v[188:191], v13 offset:32768\n"
"v_mfma_f32_16x16x32_bf16 v[222:225], v[208:211], v[48:51], v[222:225]\n"
"s_waitcnt vmcnt(9)\n"
"ds_read_b64_tr_b16 v[72:73], v10 offset:53248\n"
"v_mfma_f32_16x16x32_bf16 v[226:229], v[208:211], v[52:55], v[226:229]\n"
"ds_read_b128 v[200:203], v13 offset:38912\n"
"v_mfma_f32_16x16x32_bf16 v[230:233], v[208:211], v[56:59], v[230:233]\n"
"s_waitcnt lgkmcnt(6)\n"
"ds_read_b64_tr_b16 v[74:75], v10 offset:54016\n"
"v_mfma_f32_16x16x32_bf16 v[192:195], v[184:187], v[60:63], v[192:195]\n"
"s_waitcnt lgkmcnt(4)\n"
"ds_read_b64_tr_b16 v[76:77], v10 offset:53280\n"
"s_barrier\n"
"v_mfma_f32_16x16x32_bf16 v[196:199], v[184:187], v[64:67], v[196:199]\n"
"ds_read_b128 v[208:211], v12 offset:28672\n"
"v_mfma_f32_16x16x32_bf16 v[204:207], v[184:187], v[68:71], v[204:207]\n"
"s_waitcnt lgkmcnt(5)\n"
"ds_read_b64_tr_b16 v[78:79], v10 offset:54048\n"
"v_mfma_f32_16x16x32_bf16 v[180:183], v[188:191], v[60:63], v[180:183]\n"
"ds_read_b64_tr_b16 v[80:81], v10 offset:53312\n"
"v_mfma_f32_16x16x32_bf16 v[214:217], v[188:191], v[64:67], v[214:217]\n"
"ds_read_b64_tr_b16 v[82:83], v10 offset:54080\n"
"v_mfma_f32_16x16x32_bf16 v[218:221], v[188:191], v[68:71], v[218:221]\n"
"s_waitcnt lgkmcnt(6)\n"
"ds_read_b128 v[184:187], v12 offset:34816\n"
"v_mfma_f32_16x16x32_bf16 v[222:225], v[200:203], v[60:63], v[222:225]\n"
"s_waitcnt vmcnt(6)\n"
"ds_read_b64_tr_b16 v[84:85], v10 offset:56320\n"
"v_mfma_f32_16x16x32_bf16 v[226:229], v[200:203], v[64:67], v[226:229]\n"
"ds_read_b128 v[188:191], v12 offset:40960\n"
"v_mfma_f32_16x16x32_bf16 v[230:233], v[200:203], v[68:71], v[230:233]\n"
"s_waitcnt lgkmcnt(6)\n"
"ds_read_b64_tr_b16 v[86:87], v10 offset:57088\n"
"v_mfma_f32_16x16x32_bf16 v[192:195], v[208:211], v[72:75], v[192:195]\n"
"s_waitcnt lgkmcnt(6)\n"
"ds_read_b64_tr_b16 v[88:89], v10 offset:56352\n"
"v_mfma_f32_16x16x32_bf16 v[196:199], v[208:211], v[76:79], v[196:199]\n"
"s_waitcnt lgkmcnt(5)\n"
"ds_read_b128 v[200:203], v13 offset:28672\n"
"v_mfma_f32_16x16x32_bf16 v[204:207], v[208:211], v[80:83], v[204:207]\n"
"s_waitcnt lgkmcnt(5)\n"
"ds_read_b64_tr_b16 v[90:91], v10 offset:57120\n"
"v_mfma_f32_16x16x32_bf16 v[180:183], v[184:187], v[72:75], v[180:183]\n"
"ds_read_b64_tr_b16 v[92:93], v10 offset:56384\n"
"v_mfma_f32_16x16x32_bf16 v[214:217], v[184:187], v[76:79], v[214:217]\n"
"ds_read_b64_tr_b16 v[94:95], v10 offset:57152\n"
"v_mfma_f32_16x16x32_bf16 v[218:221], v[184:187], v[80:83], v[218:221]\n"
"s_waitcnt lgkmcnt(6)\n"
"ds_read_b128 v[184:187], v13 offset:34816\n"
"v_mfma_f32_16x16x32_bf16 v[222:225], v[188:191], v[72:75], v[222:225]\n"
"v_mfma_f32_16x16x32_bf16 v[226:229], v[188:191], v[76:79], v[226:229]\n"
"ds_read_b128 v[238:241], v13 offset:40960\n"
"v_mfma_f32_16x16x32_bf16 v[230:233], v[188:191], v[80:83], v[230:233]\n"
"s_waitcnt lgkmcnt(5)\n"
"v_mfma_f32_16x16x32_bf16 v[192:195], v[200:203], v[84:87], v[192:195]\n"
"s_waitcnt lgkmcnt(2)\n"
"s_barrier\n"
"v_mfma_f32_16x16x32_bf16 v[196:199], v[200:203], v[88:91], v[196:199]\n"
"ds_read_b128 v[188:191], v12 offset:43008\n"
"v_mfma_f32_16x16x32_bf16 v[204:207], v[200:203], v[92:95], v[204:207]\n"
"s_waitcnt lgkmcnt(2)\n"
"v_mfma_f32_16x16x32_bf16 v[180:183], v[184:187], v[84:87], v[180:183]\n"
"v_mfma_f32_16x16x32_bf16 v[214:217], v[184:187], v[88:91], v[214:217]\n"
"ds_read_b128 v[200:203], v13 offset:43008\n"
"v_mfma_f32_16x16x32_bf16 v[218:221], v[184:187], v[92:95], v[218:221]\n"
"s_waitcnt lgkmcnt(2)\n"
"v_cvt_pk_bf16_f32 v184, v192, v193\n"
"v_cvt_pk_bf16_f32 v185, v194, v195\n"
"v_mfma_f32_16x16x32_bf16 v[222:225], v[238:241], v[84:87], v[222:225]\n"
"v_cvt_pk_bf16_f32 v210, v196, v197\n"
"v_cvt_pk_bf16_f32 v211, v198, v199\n"
"v_mfma_f32_16x16x32_bf16 v[226:229], v[238:241], v[88:91], v[226:229]\n"
"ds_read_b128 v[196:199], v12 offset:45056\n"
"v_cvt_pk_bf16_f32 v236, v204, v205\n"
"v_cvt_pk_bf16_f32 v237, v206, v207\n"
"v_mfma_f32_16x16x32_bf16 v[230:233], v[238:241], v[92:95], v[230:233]\n"
"s_waitcnt lgkmcnt(2)\n"
"v_cvt_pk_bf16_f32 v186, v180, v181\n"
"v_cvt_pk_bf16_f32 v187, v182, v183\n"
"v_mfma_f32_16x16x32_bf16 v[178:181], v[188:191], v[28:31], 0\n"
"v_cvt_pk_bf16_f32 v212, v214, v215\n"
"v_cvt_pk_bf16_f32 v213, v216, v217\n"
"v_mfma_f32_16x16x32_bf16 v[204:207], v[188:191], v[32:35], 0\n"
"ds_read_b128 v[214:217], v13 offset:45056\n"
"v_cvt_pk_bf16_f32 v238, v218, v219\n"
"v_cvt_pk_bf16_f32 v239, v220, v221\n"
"v_mfma_f32_16x16x32_bf16 v[240:243], v[188:191], v[24:27], 0\n"
"s_waitcnt lgkmcnt(2)\n"
"v_cvt_pk_bf16_f32 v194, v222, v223\n"
"v_cvt_pk_bf16_f32 v195, v224, v225\n"
"v_mfma_f32_16x16x32_bf16 v[178:181], v[200:203], v[40:43], v[178:181]\n"
"v_cvt_pk_bf16_f32 v220, v226, v227\n"
"v_cvt_pk_bf16_f32 v221, v228, v229\n"
"v_mfma_f32_16x16x32_bf16 v[204:207], v[200:203], v[44:47], v[204:207]\n"
"ds_read_b128 v[188:191], v12 offset:47104\n"
"v_cvt_pk_bf16_f32 v246, v230, v231\n"
"v_cvt_pk_bf16_f32 v247, v232, v233\n"
"v_mfma_f32_16x16x32_bf16 v[240:243], v[200:203], v[36:39], v[240:243]\n"
"s_waitcnt lgkmcnt(2)\n"
"v_mfma_f32_16x16x32_bf16 v[178:181], v[196:199], v[48:51], v[178:181]\n"
"v_mfma_f32_16x16x32_bf16 v[204:207], v[196:199], v[52:55], v[204:207]\n"
"ds_read_b128 v[200:203], v13 offset:47104\n"
"v_mfma_f32_16x16x32_bf16 v[240:243], v[196:199], v[56:59], v[240:243]\n"
"s_waitcnt lgkmcnt(2)\n"
"v_mfma_f32_16x16x32_bf16 v[178:181], v[214:217], v[60:63], v[178:181]\n"
"v_mfma_f32_16x16x32_bf16 v[204:207], v[214:217], v[64:67], v[204:207]\n"
"ds_read_b128 v[196:199], v12 offset:49152\n"
"v_mfma_f32_16x16x32_bf16 v[240:243], v[214:217], v[68:71], v[240:243]\n"
"s_waitcnt lgkmcnt(2)\n"
"v_mfma_f32_16x16x32_bf16 v[178:181], v[188:191], v[72:75], v[178:181]\n"
"v_mfma_f32_16x16x32_bf16 v[204:207], v[188:191], v[76:79], v[204:207]\n"
"ds_read_b128 v[214:217], v13 offset:49152\n"
"v_mfma_f32_16x16x32_bf16 v[240:243], v[188:191], v[80:83], v[240:243]\n"
"s_waitcnt lgkmcnt(2)\n"
"v_mfma_f32_16x16x32_bf16 v[178:181], v[200:203], v[84:87], v[178:181]\n"
"v_mfma_f32_16x16x32_bf16 v[204:207], v[200:203], v[88:91], v[204:207]\n"
"ds_read_b128 v[188:191], v12 offset:51200\n"
"v_mfma_f32_16x16x32_bf16 v[240:243], v[200:203], v[92:95], v[240:243]\n"
"s_waitcnt lgkmcnt(2)\n"
"v_mfma_f32_16x16x32_bf16 v[200:203], v[196:199], v[28:31], 0\n"
"v_mfma_f32_16x16x32_bf16 v[224:227], v[196:199], v[32:35], 0\n"
"ds_read_b128 v[228:231], v13 offset:51200\n"
"v_mfma_f32_16x16x32_bf16 v[232:235], v[196:199], v[24:27], 0\n"
"s_waitcnt lgkmcnt(2)\n"
"v_mfma_f32_16x16x32_bf16 v[200:203], v[214:217], v[40:43], v[200:203]\n"
"v_cvt_pk_bf16_f32 v196, v178, v179\n"
"v_cvt_pk_bf16_f32 v197, v180, v181\n"
"v_cvt_pk_bf16_f32 v222, v204, v205\n"
"v_cvt_pk_bf16_f32 v223, v206, v207\n"
"v_mfma_f32_16x16x32_bf16 v[224:227], v[214:217], v[44:47], v[224:227]\n"
"ds_read_b128 v[178:181], v12 offset:53248\n"
"v_cvt_pk_bf16_f32 v248, v240, v241\n"
"v_cvt_pk_bf16_f32 v249, v242, v243\n"
"v_mfma_f32_16x16x32_bf16 v[232:235], v[214:217], v[36:39], v[232:235]\n"
"s_waitcnt lgkmcnt(2)\n"
"v_mfma_f32_16x16x32_bf16 v[200:203], v[188:191], v[48:51], v[200:203]\n"
"v_mfma_f32_16x16x32_bf16 v[224:227], v[188:191], v[52:55], v[224:227]\n"
"ds_read_b128 v[204:207], v13 offset:53248\n"
"v_mfma_f32_16x16x32_bf16 v[232:235], v[188:191], v[56:59], v[232:235]\n"
"s_waitcnt lgkmcnt(2)\n"
"v_mfma_f32_16x16x32_bf16 v[200:203], v[228:231], v[60:63], v[200:203]\n"
"v_mfma_f32_16x16x32_bf16 v[224:227], v[228:231], v[64:67], v[224:227]\n"
"ds_read_b128 v[188:191], v12 offset:55296\n"
"v_mfma_f32_16x16x32_bf16 v[232:235], v[228:231], v[68:71], v[232:235]\n"
"s_waitcnt lgkmcnt(2)\n"
"v_mfma_f32_16x16x32_bf16 v[200:203], v[178:181], v[72:75], v[200:203]\n"
"v_mfma_f32_16x16x32_bf16 v[224:227], v[178:181], v[76:79], v[224:227]\n"
"ds_read_b128 v[216:219], v13 offset:55296\n"
"v_mfma_f32_16x16x32_bf16 v[232:235], v[178:181], v[80:83], v[232:235]\n"
"s_waitcnt lgkmcnt(2)\n"
"v_mfma_f32_16x16x32_bf16 v[200:203], v[204:207], v[84:87], v[200:203]\n"
"v_mfma_f32_16x16x32_bf16 v[224:227], v[204:207], v[88:91], v[224:227]\n"
"ds_read_b128 v[178:181], v12 offset:57344\n"
"v_mfma_f32_16x16x32_bf16 v[232:235], v[204:207], v[92:95], v[232:235]\n"
"s_waitcnt lgkmcnt(2)\n"
"v_mfma_f32_16x16x32_bf16 v[204:207], v[188:191], v[28:31], 0\n"
"v_mfma_f32_16x16x32_bf16 v[228:231], v[188:191], v[32:35], 0\n"
"ds_read_b128 v[242:245], v13 offset:57344\n"
"v_mfma_f32_16x16x32_bf16 v[250:253], v[188:191], v[24:27], 0\n"
"s_waitcnt lgkmcnt(2)\n"
"v_mfma_f32_16x16x32_bf16 v[204:207], v[216:219], v[40:43], v[204:207]\n"
"v_cvt_pk_bf16_f32 v188, v200, v201\n"
"v_cvt_pk_bf16_f32 v189, v202, v203\n"
"v_cvt_pk_bf16_f32 v214, v224, v225\n"
"v_cvt_pk_bf16_f32 v215, v226, v227\n"
"v_mfma_f32_16x16x32_bf16 v[228:231], v[216:219], v[44:47], v[228:231]\n"
"ds_read_b128 v[190:193], v12 offset:59392\n"
"v_cvt_pk_bf16_f32 v240, v232, v233\n"
"v_cvt_pk_bf16_f32 v241, v234, v235\n"
"v_mfma_f32_16x16x32_bf16 v[250:253], v[216:219], v[36:39], v[250:253]\n"
"s_waitcnt lgkmcnt(2)\n"
"v_mfma_f32_16x16x32_bf16 v[204:207], v[178:181], v[48:51], v[204:207]\n"
"v_mfma_f32_16x16x32_bf16 v[228:231], v[178:181], v[52:55], v[228:231]\n"
"ds_read_b128 v[198:201], v13 offset:59392\n"
"v_mfma_f32_16x16x32_bf16 v[250:253], v[178:181], v[56:59], v[250:253]\n"
"s_waitcnt lgkmcnt(2)\n"
"v_mfma_f32_16x16x32_bf16 v[204:207], v[242:245], v[60:63], v[204:207]\n"
"v_mfma_f32_16x16x32_bf16 v[228:231], v[242:245], v[64:67], v[228:231]\n"
"v_mfma_f32_16x16x32_bf16 v[250:253], v[242:245], v[68:71], v[250:253]\n"
"s_waitcnt lgkmcnt(1)\n"
"v_mfma_f32_16x16x32_bf16 v[204:207], v[190:193], v[72:75], v[204:207]\n"
"v_mfma_f32_16x16x32_bf16 v[228:231], v[190:193], v[76:79], v[228:231]\n"
"v_mfma_f32_16x16x32_bf16 v[250:253], v[190:193], v[80:83], v[250:253]\n"
"s_and_b32 s41, 32, s55\n"
"v_add_u32 v2, s41, v15\n"
"s_waitcnt lgkmcnt(0)\n"
"v_mfma_f32_16x16x32_bf16 v[204:207], v[198:201], v[84:87], v[204:207]\n"
"ds_read_u16 v2, v2 offset:832\n"
"v_mfma_f32_16x16x32_bf16 v[228:231], v[198:201], v[88:91], v[228:231]\n"
"s_waitcnt vmcnt(0)\n"
"s_barrier\n"
"ds_read_b128 v[180:183], v12 offset:6144\n"
"v_mfma_f32_16x16x32_bf16 v[250:253], v[198:201], v[92:95], v[250:253]\n"
"s_waitcnt lgkmcnt(1)\n"
"ds_read_b128 v[190:193], v13 offset:6144\n"
"v_mad_u32_u24 v2, v2, s40, v16\n"
"s_waitcnt lgkmcnt(1)\n"
"v_mfma_f32_16x16x32_bf16 v[200:203], v[180:183], v[28:31], 0\n"
"v_cvt_pk_bf16_f32 v178, v204, v205\n"
"v_cvt_pk_bf16_f32 v179, v206, v207\n"
"v_mfma_f32_16x16x32_bf16 v[206:209], v[180:183], v[32:35], 0\n"
"s_add_u32 m0, s51, 126976\n"
"s_nop 0\n"
"global_load_lds_dwordx4 v2, s[56:57]\n"
"ds_read_b128 v[216:219], v12 offset:8192\n"
"v_mfma_f32_16x16x32_bf16 v[232:235], v[180:183], v[24:27], 0\n"
"s_add_u32 m0, s51, 129024\n"
"s_nop 0\n"
"global_load_lds_dwordx4 v2, s[58:59]\n"
"s_waitcnt lgkmcnt(1)\n"
"v_mfma_f32_16x16x32_bf16 v[200:203], v[190:193], v[40:43], v[200:203]\n"
"s_add_u32 m0, s51, 131072\n"
"s_nop 0\n"
"global_load_lds_dwordx4 v2, s[60:61]\n"
"v_cvt_pk_bf16_f32 v204, v228, v229\n"
"v_cvt_pk_bf16_f32 v205, v230, v231\n"
"v_mfma_f32_16x16x32_bf16 v[206:209], v[190:193], v[44:47], v[206:209]\n"
"ds_read_b128 v[180:183], v13 offset:8192\n"
"v_cvt_pk_bf16_f32 v230, v250, v251\n"
"v_cvt_pk_bf16_f32 v231, v252, v253\n"
"v_mfma_f32_16x16x32_bf16 v[232:235], v[190:193], v[36:39], v[232:235]\n"
"s_and_b32 s41, 160, s55\n"
"v_add_u32 v2, s41, v15\n"
"s_waitcnt lgkmcnt(1)\n"
"v_mfma_f32_16x16x32_bf16 v[200:203], v[216:219], v[48:51], v[200:203]\n"
"ds_read_u16 v2, v2 offset:96\n"
"v_mfma_f32_16x16x32_bf16 v[206:209], v[216:219], v[52:55], v[206:209]\n"
"ds_read_b128 v[190:193], v12 offset:10240\n"
"v_mfma_f32_16x16x32_bf16 v[232:235], v[216:219], v[56:59], v[232:235]\n"
"s_waitcnt lgkmcnt(2)\n"
"v_mfma_f32_16x16x32_bf16 v[200:203], v[180:183], v[60:63], v[200:203]\n"
"v_mfma_f32_16x16x32_bf16 v[206:209], v[180:183], v[64:67], v[206:209]\n"
"ds_read_b128 v[216:219], v13 offset:10240\n"
"v_mfma_f32_16x16x32_bf16 v[232:235], v[180:183], v[68:71], v[232:235]\n"
"s_waitcnt lgkmcnt(2)\n"
"v_mad_u32_u24 v2, v2, s40, v16\n"
"s_waitcnt lgkmcnt(1)\n"
"v_mfma_f32_16x16x32_bf16 v[200:203], v[190:193], v[72:75], v[200:203]\n"
"s_add_u32 m0, s51, 139264\n"
"s_nop 0\n"
"global_load_lds_dwordx4 v2, s[56:57]\n"
"v_mfma_f32_16x16x32_bf16 v[206:209], v[190:193], v[76:79], v[206:209]\n"
"s_add_u32 m0, s51, 141312\n"
"s_nop 0\n"
"global_load_lds_dwordx4 v2, s[58:59]\n"
"ds_read_b128 v[180:183], v12 offset:12288\n"
"v_mfma_f32_16x16x32_bf16 v[232:235], v[190:193], v[80:83], v[232:235]\n"
"s_add_u32 m0, s51, 143360\n"
"s_nop 0\n"
"global_load_lds_dwordx4 v2, s[60:61]\n"
"s_waitcnt lgkmcnt(1)\n"
"v_mfma_f32_16x16x32_bf16 v[200:203], v[216:219], v[84:87], v[200:203]\n"
"s_andn2_b32 s41, 288, s55\n"
"v_add_u32 v2, s41, v15\n"
"v_mfma_f32_16x16x32_bf16 v[206:209], v[216:219], v[88:91], v[206:209]\n"
"ds_read_u16 v2, v2 offset:128\n"
"v_mfma_f32_16x16x32_bf16 v[232:235], v[216:219], v[92:95], v[232:235]\n"
"s_waitcnt lgkmcnt(1)\n"
"ds_read_b128 v[190:193], v13 offset:12288\n"
"v_mfma_f32_16x16x32_bf16 v[216:219], v[180:183], v[28:31], 0\n"
"v_mfma_f32_16x16x32_bf16 v[226:229], v[180:183], v[32:35], 0\n"
"ds_read_b128 v[242:245], v12 offset:14336\n"
"v_cvt_pk_bf16_f32 v198, v200, v201\n"
"v_mfma_f32_16x16x32_bf16 v[252:255], v[180:183], v[24:27], 0\n"
"s_waitcnt lgkmcnt(2)\n"
"v_cvt_pk_bf16_f32 v199, v202, v203\n"
"v_mad_u32_u24 v2, v2, s40, v16\n"
"v_cvt_pk_bf16_f32 v224, v206, v207\n"
"v_cvt_pk_bf16_f32 v225, v208, v209\n"
"v_cvt_pk_bf16_f32 v250, v232, v233\n"
"s_waitcnt lgkmcnt(1)\n"
"v_mfma_f32_16x16x32_bf16 v[216:219], v[190:193], v[40:43], v[216:219]\n"
"s_add_u32 m0, s51, 151552\n"
"s_nop 0\n"
"global_load_lds_dwordx4 v2, s[56:57]\n"
"v_mfma_f32_16x16x32_bf16 v[226:229], v[190:193], v[44:47], v[226:229]\n"
"s_add_u32 m0, s51, 153600\n"
"s_nop 0\n"
"global_load_lds_dwordx4 v2, s[58:59]\n"
"ds_read_b128 v[180:183], v13 offset:14336\n"
"v_cvt_pk_bf16_f32 v251, v234, v235\n"
"v_mfma_f32_16x16x32_bf16 v[252:255], v[190:193], v[36:39], v[252:255]\n"
"s_add_u32 m0, s51, 155648\n"
"s_nop 0\n"
"global_load_lds_dwordx4 v2, s[60:61]\n"
"s_waitcnt lgkmcnt(1)\n"
"v_mfma_f32_16x16x32_bf16 v[216:219], v[242:245], v[48:51], v[216:219]\n"
"s_barrier\n"
"s_add_u32 s42, s48, 0\n"
"s_addc_u32 s43, s49, 0\n"
"s_add_u32 m0, s50, 24576\n"
"s_nop 0\n"
"global_load_lds_dwordx4 v3, s[42:43]\n"
"v_mfma_f32_16x16x32_bf16 v[226:229], v[242:245], v[52:55], v[226:229]\n"
"ds_read_b128 v[190:193], v12 offset:16384\n"
"v_mfma_f32_16x16x32_bf16 v[252:255], v[242:245], v[56:59], v[252:255]\n"
"s_add_u32 m0, s50, 25600\n"
"s_nop 0\n"
"global_load_lds_dwordx4 v4, s[42:43]\n"
"s_waitcnt lgkmcnt(1)\n"
"v_mfma_f32_16x16x32_bf16 v[216:219], v[180:183], v[60:63], v[216:219]\n"
"v_mfma_f32_16x16x32_bf16 v[226:229], v[180:183], v[64:67], v[226:229]\n"
"ds_read_b128 v[200:203], v13 offset:16384\n"
"v_mfma_f32_16x16x32_bf16 v[252:255], v[180:183], v[68:71], v[252:255]\n"
"s_add_u32 m0, s50, 26624\n"
"s_nop 0\n"
"global_load_lds_dwordx4 v5, s[42:43]\n"
"s_waitcnt lgkmcnt(1)\n"
"v_mfma_f32_16x16x32_bf16 v[216:219], v[190:193], v[72:75], v[216:219]\n"
"v_mfma_f32_16x16x32_bf16 v[226:229], v[190:193], v[76:79], v[226:229]\n"
"ds_read_b128 v[180:183], v12 offset:18432\n"
"v_mfma_f32_16x16x32_bf16 v[252:255], v[190:193], v[80:83], v[252:255]\n"
"s_add_u32 s42, s48, 18432\n"
"s_addc_u32 s43, s49, 0\n"
"s_add_u32 m0, s50, 27648\n"
"s_nop 0\n"
"global_load_lds_dwordx4 v3, s[42:43]\n"
"s_waitcnt lgkmcnt(1)\n"
"v_mfma_f32_16x16x32_bf16 v[216:219], v[200:203], v[84:87], v[216:219]\n"
"v_mfma_f32_16x16x32_bf16 v[226:229], v[200:203], v[88:91], v[226:229]\n"
"ds_read_b128 v[190:193], v13 offset:18432\n"
"s_add_u32 m0, s50, 28672\n"
"s_nop 0\n"
"global_load_lds_dwordx4 v4, s[42:43]\n"
"v_mfma_f32_16x16x32_bf16 v[252:255], v[200:203], v[92:95], v[252:255]\n"
"s_waitcnt lgkmcnt(1)\n"
"v_mfma_f32_16x16x32_bf16 v[200:203], v[180:183], v[28:31], 0\n"
"v_mfma_f32_16x16x32_bf16 v[242:245], v[180:183], v[32:35], 0\n"
"s_add_u32 m0, s50, 29696\n"
"s_nop 0\n"
"global_load_lds_dwordx4 v5, s[42:43]\n"
"ds_read_b128 v[96:99], v12 offset:20480\n"
"v_mfma_f32_16x16x32_bf16 v[112:115], v[180:183], v[24:27], 0\n"
"s_waitcnt lgkmcnt(1)\n"
"v_cvt_pk_bf16_f32 v180, v216, v217\n"
"v_cvt_pk_bf16_f32 v181, v218, v219\n"
"v_mfma_f32_16x16x32_bf16 v[200:203], v[190:193], v[40:43], v[200:203]\n"
"v_cvt_pk_bf16_f32 v206, v226, v227\n"
"v_cvt_pk_bf16_f32 v207, v228, v229\n"
"v_mfma_f32_16x16x32_bf16 v[242:245], v[190:193], v[44:47], v[242:245]\n"
"ds_read_b128 v[216:219], v13 offset:20480\n"
"v_cvt_pk_bf16_f32 v232, v252, v253\n"
"v_cvt_pk_bf16_f32 v233, v254, v255\n"
"v_mfma_f32_16x16x32_bf16 v[112:115], v[190:193], v[36:39], v[112:115]\n"
"s_waitcnt lgkmcnt(1)\n"
"v_mfma_f32_16x16x32_bf16 v[200:203], v[96:99], v[48:51], v[200:203]\n"
"v_mfma_f32_16x16x32_bf16 v[242:245], v[96:99], v[52:55], v[242:245]\n"
"ds_read_b128 v[190:193], v12 offset:22528\n"
"v_mfma_f32_16x16x32_bf16 v[112:115], v[96:99], v[56:59], v[112:115]\n"
"s_waitcnt vmcnt(12)\n"
"s_barrier\n"
"s_waitcnt vmcnt(3)\n"
"ds_read_b64_tr_b16 v[96:97], v11 offset:53248\n"
"s_waitcnt lgkmcnt(2)\n"
"v_mfma_f32_16x16x32_bf16 v[200:203], v[216:219], v[60:63], v[200:203]\n"
"ds_read_b64_tr_b16 v[98:99], v11 offset:54016\n"
"ds_read_b64_tr_b16 v[100:101], v11 offset:53256\n"
"v_mfma_f32_16x16x32_bf16 v[242:245], v[216:219], v[64:67], v[242:245]\n"
"ds_read_b64_tr_b16 v[102:103], v11 offset:54024\n"
"ds_read_b64_tr_b16 v[226:227], v10 offset:53312\n"
"v_mfma_f32_16x16x32_bf16 v[112:115], v[216:219], v[68:71], v[112:115]\n"
"ds_read_b64_tr_b16 v[228:229], v10 offset:54080\n"
"s_waitcnt vmcnt(0)\n"
"ds_read_b64_tr_b16 v[104:105], v11 offset:56320\n"
"s_waitcnt lgkmcnt(7)\n"
"v_mfma_f32_16x16x32_bf16 v[200:203], v[190:193], v[72:75], v[200:203]\n"
"ds_read_b64_tr_b16 v[106:107], v11 offset:57088\n"
"ds_read_b64_tr_b16 v[108:109], v11 offset:56328\n"
"v_mfma_f32_16x16x32_bf16 v[242:245], v[190:193], v[76:79], v[242:245]\n"
"ds_read_b64_tr_b16 v[110:111], v11 offset:57096\n"
"v_mov_b32 v216, 0x3f803f80\n"
"v_mfma_f32_16x16x32_bf16 v[112:115], v[190:193], v[80:83], v[112:115]\n"
"ds_read_b64_tr_b16 v[252:253], v10 offset:56384\n"
"v_mov_b32 v217, 0x3f803f80\n"
"v_mov_b32 v218, 0x3f803f80\n"
"v_mov_b32 v219, 0x3f803f80\n"
"v_mov_b32 v193, 0\n"
"s_waitcnt lgkmcnt(5)\n"
"s_add_u32 s42, s48, 36864\n"
"s_addc_u32 s43, s49, 0\n"
"s_add_u32 m0, s50, 24576\n"
"s_nop 0\n"
"global_load_lds_dwordx4 v3, s[42:43]\n"
"ds_read_b64_tr_b16 v[254:255], v10 offset:57152\n"
"ds_read_b128 v[116:119], v13 offset:22528\n"
"s_add_u32 m0, s50, 25600\n"
"s_nop 0\n"
"global_load_lds_dwordx4 v4, s[42:43]\n"
"s_add_u32 m0, s50, 26624\n"
"s_nop 0\n"
"global_load_lds_dwordx4 v5, s[42:43]\n"
"s_waitcnt lgkmcnt(0)\n"
"v_mfma_f32_16x16x32_bf16 v[200:203], v[116:119], v[84:87], v[200:203]\n"
"v_mfma_f32_16x16x32_bf16 v[242:245], v[116:119], v[88:91], v[242:245]\n"
"v_mfma_f32_16x16x32_bf16 v[112:115], v[116:119], v[92:95], v[112:115]\n"
"s_add_u32 s42, s48, 55296\n"
"s_addc_u32 s43, s49, 0\n"
"s_add_u32 m0, s50, 27648\n"
"s_nop 0\n"
"global_load_lds_dwordx4 v3, s[42:43]\n"
"s_add_u32 m0, s50, 28672\n"
"s_nop 0\n"
"global_load_lds_dwordx4 v4, s[42:43]\n"
"v_cvt_pk_bf16_f32 v182, v200, v201\n"
"v_cvt_pk_bf16_f32 v183, v202, v203\n"
"v_cvt_pk_bf16_f32 v208, v242, v243\n"
"v_cvt_pk_bf16_f32 v209, v244, v245\n"
"v_cvt_pk_bf16_f32 v234, v112, v113\n"
"v_cvt_pk_bf16_f32 v235, v114, v115\n"
"s_add_u32 m0, s50, 29696\n"
"s_nop 0\n"
"global_load_lds_dwordx4 v5, s[42:43]\n"
"s_waitcnt vmcnt(15)\n"
"s_barrier\n"
"ds_read2st64_b32 v[190:191], v9 offset0:112 offset1:113\n"
"ds_read_b32 v192, v9 offset:29184\n"
"ds_read2st64_b32 v[200:201], v9 offset0:116 offset1:117\n"
"ds_read2st64_b32 v[202:203], v9 offset0:118 offset1:119\n"
"s_waitcnt lgkmcnt(2)\n"
"v_mfma_f32_16x16x32_bf16 v[242:245], v[216:219], v[190:193], 0\n"
"s_waitcnt vmcnt(3)\n"
"ds_read_b64_tr_b16 v[0:1], v11 offset:53248\n"
"v_mfma_f32_16x16x32_bf16 v[136:139], v[186:189], v[190:193], 0\n"
"ds_read_b64_tr_b16 v[2:3], v11 offset:54016\n"
"v_mfma_f32_16x16x32_bf16 v[124:127], v[212:215], v[190:193], 0\n"
"ds_read_b64_tr_b16 v[112:113], v11 offset:53256\n"
"v_mfma_f32_16x16x32_bf16 v[128:131], v[238:241], v[190:193], 0\n"
"s_waitcnt lgkmcnt(3)\n"
"ds_read_b64_tr_b16 v[114:115], v11 offset:54024\n"
"v_mfma_f32_16x16x32_bf16 v[242:245], v[216:219], v[200:203], v[242:245]\n"
"ds_read_b64_tr_b16 v[140:141], v10 offset:53312\n"
"ds_read2st64_b32 v[190:191], v9 offset0:128 offset1:129\n"
"v_mfma_f32_16x16x32_bf16 v[136:139], v[194:197], v[200:203], v[136:139]\n"
"ds_read_b64_tr_b16 v[142:143], v10 offset:54080\n"
"ds_read2st64_b32 v[192:193], v9 offset0:130 offset1:131\n"
"v_mfma_f32_16x16x32_bf16 v[124:127], v[220:223], v[200:203], v[124:127]\n"
"s_waitcnt vmcnt(0)\n"
"ds_read_b64_tr_b16 v[116:117], v11 offset:56320\n"
"v_mfma_f32_16x16x32_bf16 v[128:131], v[246:249], v[200:203], v[128:131]\n"
"s_waitcnt lgkmcnt(2)\n"
"ds_read_b64_tr_b16 v[118:119], v11 offset:57088\n"
"ds_read_b32 v200, v9 offset:33792\n"
"ds_read2st64_b32 v[202:203], v9 offset0:134 offset1:135\n"
"s_add_u32 s42, s48, 12288\n"
"s_addc_u32 s43, s49, 0\n"
"s_add_u32 m0, s50, 24576\n"
"s_nop 0\n"
"global_load_lds_dwordx4 v6, s[42:43]\n"
"s_waitcnt lgkmcnt(4)\n"
"v_mfma_f32_16x16x32_bf16 v[242:245], v[216:219], v[190:193], v[242:245]\n"
"ds_read_b64_tr_b16 v[120:121], v11 offset:56328\n"
"v_mov_b32 v201, 0\n"
"v_mfma_f32_16x16x32_bf16 v[136:139], v[178:181], v[190:193], v[136:139]\n"
"ds_read_b64_tr_b16 v[122:123], v11 offset:57096\n"
"v_mov_b32 v135, 0\n"
"v_mfma_f32_16x16x32_bf16 v[124:127], v[204:207], v[190:193], v[124:127]\n"
"s_add_u32 m0, s50, 25600\n"
"s_nop 0\n"
"global_load_lds_dwordx4 v7, s[42:43]\n"
"ds_read_b64_tr_b16 v[144:145], v10 offset:56384\n"
"v_mfma_f32_16x16x32_bf16 v[128:131], v[230:233], v[190:193], v[128:131]\n"
"s_waitcnt lgkmcnt(3)\n"
"ds_read_b64_tr_b16 v[146:147], v10 offset:57152\n"
"v_mov_b32 v132, 0\n"
"v_mfma_f32_16x16x32_bf16 v[242:245], v[216:219], v[200:203], v[242:245]\n"
"ds_read_b32 v134, v9 offset:9216\n"
"v_mov_b32 v133, 0\n"
"s_add_u32 m0, s50, 26624\n"
"s_nop 0\n"
"global_load_lds_dwordx4 v8, s[42:43]\n"
"s_waitcnt lgkmcnt(1)\n"
"s_add_u32 s42, s48, 49152\n"
"s_addc_u32 s43, s49, 0\n"
"s_add_u32 m0, s50, 27648\n"
"s_nop 0\n"
"global_load_lds_dwordx4 v6, s[42:43]\n"
"s_waitcnt lgkmcnt(0)\n"
"v_mfma_f32_16x16x32_bf16 v[242:245], v[216:219], v[132:135], v[242:245]\n"
"v_mfma_f32_16x16x32_bf16 v[128:131], v[234:237], v[200:203], v[128:131]\n"
"s_add_u32 m0, s50, 28672\n"
"s_nop 0\n"
"global_load_lds_dwordx4 v7, s[42:43]\n"
"v_mfma_f32_16x16x32_bf16 v[128:131], v[248:251], v[132:135], v[128:131]\n"
"ds_read_b128 v[190:193], v12 offset:36864\n"
"v_mfma_f32_16x16x32_bf16 v[136:139], v[182:185], v[200:203], v[136:139]\n"
"v_mfma_f32_16x16x32_bf16 v[136:139], v[196:199], v[132:135], v[136:139]\n"
"s_add_u32 m0, s50, 29696\n"
"s_nop 0\n"
"global_load_lds_dwordx4 v8, s[42:43]\n"
"s_andn2_b32 s41, 608, s55\n"
"v_add_u32 v8, s41, v15\n"
"v_rcp_f32 v243, v242\n"
"v_mfma_f32_16x16x32_bf16 v[124:127], v[208:211], v[200:203], v[124:127]\n"
"ds_read_u16 v8, v8 offset:288\n"
"v_mul_f32 v128, v128, v243\n"
"v_mfma_f32_16x16x32_bf16 v[124:127], v[222:225], v[132:135], v[124:127]\n"
"s_waitcnt lgkmcnt(1)\n"
"ds_read_b128 v[200:203], v13 offset:36864\n"
"v_mul_f32 v129, v129, v243\n"
"v_cvt_pk_bf16_f32 v244, v128, v129\n"
"v_mfma_f32_16x16x32_bf16 v[216:219], v[190:193], v[28:31], 0\n"
"v_mul_f32 v130, v130, v243\n"
"v_mul_f32 v131, v131, v243\n"
"v_mfma_f32_16x16x32_bf16 v[132:135], v[190:193], v[32:35], 0\n"
"ds_read_b128 v[4:7], v12 offset:38912\n"
"v_cvt_pk_bf16_f32 v245, v130, v131\n"
"v_mfma_f32_16x16x32_bf16 v[128:131], v[190:193], v[24:27], 0\n"
"s_waitcnt lgkmcnt(2)\n"
"v_mul_f32 v136, v136, v243\n"
"v_mul_f32 v137, v137, v243\n"
"v_cvt_pk_bf16_f32 v136, v136, v137\n"
"v_mad_u32_u24 v8, v8, s40, v16\n"
"v_mul_f32 v138, v138, v243\n"
"v_mul_f32 v139, v139, v243\n"
"v_cvt_pk_bf16_f32 v137, v138, v139\n"
"s_waitcnt lgkmcnt(1)\n"
"v_mfma_f32_16x16x32_bf16 v[216:219], v[200:203], v[40:43], v[216:219]\n"
"s_add_u32 m0, s51, 114688\n"
"s_nop 0\n"
"global_load_lds_dwordx4 v8, s[56:57]\n"
"v_mfma_f32_16x16x32_bf16 v[132:135], v[200:203], v[44:47], v[132:135]\n"
"ds_read_b128 v[190:193], v13 offset:38912\n"
"v_mul_f32 v124, v124, v243\n"
"v_mfma_f32_16x16x32_bf16 v[128:131], v[200:203], v[36:39], v[128:131]\n"
"s_waitcnt lgkmcnt(1)\n"
"v_mul_f32 v125, v125, v243\n"
"v_cvt_pk_bf16_f32 v138, v124, v125\n"
"v_mfma_f32_16x16x32_bf16 v[216:219], v[4:7], v[48:51], v[216:219]\n"
"v_mul_f32 v126, v126, v243\n"
"v_mul_f32 v127, v127, v243\n"
"v_mfma_f32_16x16x32_bf16 v[132:135], v[4:7], v[52:55], v[132:135]\n"
"ds_read_b128 v[200:203], v12 offset:40960\n"
"v_cvt_pk_bf16_f32 v139, v126, v127\n"
"v_mfma_f32_16x16x32_bf16 v[128:131], v[4:7], v[56:59], v[128:131]\n"
"s_add_u32 m0, s51, 116736\n"
"s_nop 0\n"
"global_load_lds_dwordx4 v8, s[58:59]\n"
"s_waitcnt lgkmcnt(1)\n"
"v_mfma_f32_16x16x32_bf16 v[216:219], v[190:193], v[60:63], v[216:219]\n"
"v_mfma_f32_16x16x32_bf16 v[132:135], v[190:193], v[64:67], v[132:135]\n"
"ds_read_b128 v[4:7], v13 offset:40960\n"
"v_mfma_f32_16x16x32_bf16 v[128:131], v[190:193], v[68:71], v[128:131]\n"
"s_waitcnt lgkmcnt(1)\n"
"v_mfma_f32_16x16x32_bf16 v[216:219], v[200:203], v[72:75], v[216:219]\n"
"v_mfma_f32_16x16x32_bf16 v[132:135], v[200:203], v[76:79], v[132:135]\n"
"ds_read_b128 v[190:193], v12 offset:43008\n"
"v_mfma_f32_16x16x32_bf16 v[128:131], v[200:203], v[80:83], v[128:131]\n"
"s_add_u32 m0, s51, 118784\n"
"s_nop 0\n"
"global_load_lds_dwordx4 v8, s[60:61]\n"
"s_waitcnt lgkmcnt(1)\n"
"v_mfma_f32_16x16x32_bf16 v[216:219], v[4:7], v[84:87], v[216:219]\n"
"v_mfma_f32_16x16x32_bf16 v[132:135], v[4:7], v[88:91], v[132:135]\n"
"ds_read_b128 v[200:203], v13 offset:43008\n"
"v_mfma_f32_16x16x32_bf16 v[128:131], v[4:7], v[92:95], v[128:131]\n"
"s_waitcnt lgkmcnt(1)\n"
"v_mfma_f32_16x16x32_bf16 v[148:151], v[190:193], v[28:31], 0\n"
"v_mfma_f32_16x16x32_bf16 v[152:155], v[190:193], v[32:35], 0\n"
"ds_read_b128 v[156:159], v12 offset:45056\n"
"v_mfma_f32_16x16x32_bf16 v[160:163], v[190:193], v[24:27], 0\n"
"s_waitcnt vmcnt(21)\n"
"s_barrier\n"
"s_waitcnt vmcnt(6)\n"
"ds_read_b64_tr_b16 v[4:5], v11 offset:53248\n"
"v_cvt_pk_bf16_f32 v190, v216, v217\n"
"v_cvt_pk_bf16_f32 v191, v218, v219\n"
"s_waitcnt lgkmcnt(2)\n"
"v_mfma_f32_16x16x32_bf16 v[148:151], v[200:203], v[40:43], v[148:151]\n"
"s_mov_b32 m0, s54\n"
"ds_read_b64_tr_b16 v[6:7], v11 offset:54016\n"
"v_cvt_pk_bf16_f32 v216, v132, v133\n"
"v_cvt_pk_bf16_f32 v217, v134, v135\n"
"v_mfma_f32_16x16x32_bf16 v[152:155], v[200:203], v[44:47], v[152:155]\n"
"ds_read_b64_tr_b16 v[124:125], v11 offset:53256\n"
"v_cvt_pk_bf16_f32 v242, v128, v129\n"
"v_cvt_pk_bf16_f32 v243, v130, v131\n"
"v_mfma_f32_16x16x32_bf16 v[160:163], v[200:203], v[36:39], v[160:163]\n"
"s_waitcnt lgkmcnt(3)\n"
"ds_read_b64_tr_b16 v[126:127], v11 offset:54024\n"
"ds_read_b64_tr_b16 v[200:201], v10 offset:53312\n"
"v_mfma_f32_16x16x32_bf16 v[148:151], v[156:159], v[48:51], v[148:151]\n"
"ds_read_b64_tr_b16 v[202:203], v10 offset:54080\n"
"s_waitcnt vmcnt(3)\n"
"ds_read_b64_tr_b16 v[128:129], v11 offset:56320\n"
"v_mfma_f32_16x16x32_bf16 v[152:155], v[156:159], v[52:55], v[152:155]\n"
"ds_read_b64_tr_b16 v[130:131], v11 offset:57088\n"
"ds_read_b64_tr_b16 v[132:133], v11 offset:56328\n"
"v_mfma_f32_16x16x32_bf16 v[160:163], v[156:159], v[56:59], v[160:163]\n"
"ds_read_b64_tr_b16 v[134:135], v11 offset:57096\n"
"ds_read_b64_tr_b16 v[156:157], v10 offset:56384\n"
"ds_read_b64_tr_b16 v[158:159], v10 offset:57152\n"
"ds_read_b128 v[164:167], v13 offset:45056\n"
"ds_write_addtid_b32 v244 offset:33280\n"
"ds_write_addtid_b32 v245 offset:33536\n"
"ds_write_addtid_b32 v136 offset:32256\n"
"ds_read_b128 v[168:171], v12 offset:47104\n"
"ds_write_addtid_b32 v137 offset:32512\n"
"ds_write_addtid_b32 v138 offset:32768\n"
"ds_write_addtid_b32 v139 offset:33024\n"
"s_waitcnt lgkmcnt(8)\n"
"ds_write_b128 v17, v[226:229] offset:53248\n"
"s_waitcnt lgkmcnt(8)\n"
"v_mfma_f32_16x16x32_bf16 v[148:151], v[164:167], v[60:63], v[148:151]\n"
"v_mfma_f32_16x16x32_bf16 v[152:155], v[164:167], v[64:67], v[152:155]\n"
"ds_write_b128 v17, v[252:255] offset:54272\n"
"v_mfma_f32_16x16x32_bf16 v[160:163], v[164:167], v[68:71], v[160:163]\n"
"s_waitcnt lgkmcnt(5)\n"
"ds_read_b128 v[226:229], v13 offset:47104\n"
"v_mfma_f32_16x16x32_bf16 v[148:151], v[168:171], v[72:75], v[148:151]\n"
"ds_write_b128 v17, v[140:143] offset:55296\n"
"v_mfma_f32_16x16x32_bf16 v[152:155], v[168:171], v[76:79], v[152:155]\n"
"v_mov_b32 v136, 0x3f803f80\n"
"v_mov_b32 v137, 0x3f803f80\n"
"v_mfma_f32_16x16x32_bf16 v[160:163], v[168:171], v[80:83], v[160:163]\n"
"s_and_b32 s41, 480, s55\n"
"v_add_u32 v8, s41, v15\n"
"v_mov_b32 v138, 0x3f803f80\n"
"v_mov_b32 v139, 0x3f803f80\n"
"ds_write_b128 v17, v[144:147] offset:56320\n"
"s_waitcnt lgkmcnt(2)\n"
"v_mfma_f32_16x16x32_bf16 v[148:151], v[226:229], v[84:87], v[148:151]\n"
"s_waitcnt vmcnt(0)\n"
"s_barrier\n"
"ds_read2st64_b32 v[252:253], v9 offset0:120 offset1:121\n"
"ds_read2st64_b32 v[254:255], v9 offset0:122 offset1:123\n"
"v_mfma_f32_16x16x32_bf16 v[152:155], v[226:229], v[88:91], v[152:155]\n"
"ds_write_b128 v17, v[200:203] offset:57344\n"
"v_mfma_f32_16x16x32_bf16 v[160:163], v[226:229], v[92:95], v[160:163]\n"
"ds_read_u16 v8, v8 offset:448\n"
"ds_read2st64_b32 v[140:141], v9 offset0:124 offset1:125\n"
"v_cvt_pk_bf16_f32 v200, v148, v149\n"
"v_cvt_pk_bf16_f32 v201, v150, v151\n"
"ds_read2st64_b32 v[142:143], v9 offset0:126 offset1:127\n"
"ds_write_b128 v17, v[156:159] offset:58368\n"
"s_waitcnt lgkmcnt(5)\n"
"v_mfma_f32_16x16x32_bf16 v[144:147], v[136:139], v[252:255], 0\n"
"ds_read2st64_b32 v[150:151], v9 offset0:128 offset1:129\n"
"v_cvt_pk_bf16_f32 v226, v152, v153\n"
"v_mfma_f32_16x16x32_bf16 v[156:159], v[186:189], v[252:255], 0\n"
"s_waitcnt lgkmcnt(4)\n"
"ds_read2st64_b32 v[152:153], v9 offset0:130 offset1:131\n"
"v_mad_u32_u24 v8, v8, s40, v16\n"
"v_mfma_f32_16x16x32_bf16 v[164:167], v[212:215], v[252:255], 0\n"
"s_add_u32 m0, s51, 139264\n"
"s_nop 0\n"
"global_load_lds_dwordx4 v8, s[56:57]\n"
"v_mfma_f32_16x16x32_bf16 v[168:171], v[238:241], v[252:255], 0\n"
"s_waitcnt lgkmcnt(3)\n"
"v_cvt_pk_bf16_f32 v227, v154, v155\n"
"v_cvt_pk_bf16_f32 v252, v160, v161\n"
"v_mfma_f32_16x16x32_bf16 v[144:147], v[136:139], v[140:143], v[144:147]\n"
"v_cvt_pk_bf16_f32 v253, v162, v163\n"
"v_mov_b32 v148, 0\n"
"v_mov_b32 v149, 0\n"
"v_mov_b32 v154, 0\n"
"v_mov_b32 v155, 0\n"
"s_waitcnt lgkmcnt(0)\n"
"v_mfma_f32_16x16x32_bf16 v[144:147], v[136:139], v[150:153], v[144:147]\n"
"v_mfma_f32_16x16x32_bf16 v[168:171], v[250:253], v[140:143], v[168:171]\n"
"v_mfma_f32_16x16x32_bf16 v[168:171], v[240:243], v[148:151], v[168:171]\n"
"s_add_u32 m0, s51, 141312\n"
"s_nop 0\n"
"global_load_lds_dwordx4 v8, s[58:59]\n"
"v_mfma_f32_16x16x32_bf16 v[168:171], v[248:251], v[152:155], v[168:171]\n"
"v_mfma_f32_16x16x32_bf16 v[156:159], v[198:201], v[140:143], v[156:159]\n"
"v_mfma_f32_16x16x32_bf16 v[156:159], v[188:191], v[148:151], v[156:159]\n"
"ds_read2st64_b32 v[136:137], v14 offset0:126 offset1:127\n"
"v_mfma_f32_16x16x32_bf16 v[156:159], v[196:199], v[152:155], v[156:159]\n"
"ds_read2st64_b32 v[138:139], v14 offset0:128 offset1:129\n"
"v_rcp_f32 v255, v144\n"
"v_mfma_f32_16x16x32_bf16 v[164:167], v[224:227], v[140:143], v[164:167]\n"
"ds_read_b128 v[140:143], v17 offset:53248\n"
"v_mul_f32 v168, v168, v255\n"
"v_mfma_f32_16x16x32_bf16 v[164:167], v[214:217], v[148:151], v[164:167]\n"
"s_add_u32 m0, s51, 143360\n"
"s_nop 0\n"
"global_load_lds_dwordx4 v8, s[60:61]\n"
"ds_read2st64_b32 v[144:145], v14 offset0:150 offset1:151\n"
"v_mfma_f32_16x16x32_bf16 v[164:167], v[222:225], v[152:155], v[164:167]\n"
"ds_read2st64_b32 v[146:147], v14 offset0:152 offset1:153\n"
"v_mul_f32 v169, v169, v255\n"
"v_cvt_pk_bf16_f32 v244, v168, v169\n"
"v_mul_f32 v170, v170, v255\n"
"v_mul_f32 v171, v171, v255\n"
"v_cvt_pk_bf16_f32 v245, v170, v171\n"
"s_waitcnt lgkmcnt(3)\n"
"v_mfma_f32_16x16x32_bf16 v[148:151], v[96:99], v[136:139], 0\n"
"ds_read_b128 v[152:155], v17 offset:54272\n"
"v_mul_f32 v156, v156, v255\n"
"v_mfma_f32_16x16x32_bf16 v[160:163], v[100:103], v[136:139], 0\n"
"s_waitcnt lgkmcnt(3)\n"
"ds_read2st64_b32 v[168:169], v14 offset0:174 offset1:175\n"
"v_mul_f32 v157, v157, v255\n"
"v_mfma_f32_16x16x32_bf16 v[172:175], v[140:143], v[136:139], 0\n"
"ds_read2st64_b32 v[170:171], v14 offset0:176 offset1:177\n"
"v_cvt_pk_bf16_f32 v156, v156, v157\n"
"v_mul_f32 v158, v158, v255\n"
"v_mul_f32 v159, v159, v255\n"
"s_waitcnt lgkmcnt(3)\n"
"v_mfma_f32_16x16x32_bf16 v[148:151], v[104:107], v[144:147], v[148:151]\n"
"ds_read_b128 v[136:139], v17 offset:55296\n"
"v_cvt_pk_bf16_f32 v157, v158, v159\n"
"v_mfma_f32_16x16x32_bf16 v[160:163], v[108:111], v[144:147], v[160:163]\n"
"s_waitcnt lgkmcnt(3)\n"
"ds_read2st64_b32 v[140:141], v14 offset0:198 offset1:199\n"
"v_mul_f32 v164, v164, v255\n"
"v_mfma_f32_16x16x32_bf16 v[172:175], v[152:155], v[144:147], v[172:175]\n"
"ds_read2st64_b32 v[142:143], v14 offset0:200 offset1:201\n"
"v_mul_f32 v165, v165, v255\n"
"v_cvt_pk_bf16_f32 v158, v164, v165\n"
"v_mul_f32 v166, v166, v255\n"
"v_mul_f32 v167, v167, v255\n"
"v_cvt_pk_bf16_f32 v159, v166, v167\n"
"s_waitcnt lgkmcnt(3)\n"
"v_mfma_f32_16x16x32_bf16 v[148:151], v[0:3], v[168:171], v[148:151]\n"
"ds_read_b128 v[144:147], v17 offset:56320\n"
"v_mfma_f32_16x16x32_bf16 v[160:163], v[112:115], v[168:171], v[160:163]\n"
"s_waitcnt lgkmcnt(3)\n"
"ds_read2st64_b32 v[152:153], v14 offset0:130 offset1:131\n"
"v_mfma_f32_16x16x32_bf16 v[172:175], v[136:139], v[168:171], v[172:175]\n"
"s_waitcnt lgkmcnt(2)\n"
"ds_read2st64_b32 v[154:155], v14 offset0:154 offset1:155\n"
"v_mfma_f32_16x16x32_bf16 v[148:151], v[116:119], v[140:143], v[148:151]\n"
"ds_read_b128 v[136:139], v17 offset:57344\n"
"v_mfma_f32_16x16x32_bf16 v[160:163], v[120:123], v[140:143], v[160:163]\n"
"s_waitcnt lgkmcnt(3)\n"
"ds_read2st64_b32 v[164:165], v14 offset0:178 offset1:179\n"
"v_mfma_f32_16x16x32_bf16 v[172:175], v[144:147], v[140:143], v[172:175]\n"
"s_waitcnt lgkmcnt(2)\n"
"ds_read2st64_b32 v[166:167], v14 offset0:202 offset1:203\n"
"v_mfma_f32_16x16x32_bf16 v[148:151], v[4:7], v[152:155], v[148:151]\n"
"ds_read_b128 v[140:143], v17 offset:58368\n"
"v_mfma_f32_16x16x32_bf16 v[160:163], v[124:127], v[152:155], v[160:163]\n"
"s_mov_b32 m0, s54\n"
"ds_read_b128 v[144:147], v12 offset:49152\n"
"s_waitcnt lgkmcnt(4)\n"
"v_mfma_f32_16x16x32_bf16 v[172:175], v[136:139], v[152:155], v[172:175]\n"
"s_waitcnt lgkmcnt(2)\n"
"v_mfma_f32_16x16x32_bf16 v[148:151], v[128:131], v[164:167], v[148:151]\n"
"v_mfma_f32_16x16x32_bf16 v[160:163], v[132:135], v[164:167], v[160:163]\n"
"s_barrier\n"
"ds_write_addtid_b32 v244 offset:33280\n"
"s_waitcnt lgkmcnt(2)\n"
"v_mfma_f32_16x16x32_bf16 v[172:175], v[140:143], v[164:167], v[172:175]\n"
"ds_write_addtid_b32 v245 offset:33536\n"
"ds_write_addtid_b32 v156 offset:32256\n"
"s_waitcnt lgkmcnt(3)\n"
"v_mfma_f32_16x16x32_bf16 v[136:139], v[144:147], v[28:31], 0\n"
"ds_write_addtid_b32 v157 offset:32512\n"
"v_cvt_pk_bf16_f32 v148, v148, v149\n"
"v_cvt_pk_bf16_f32 v149, v150, v151\n"
"v_cvt_pk_bf16_f32 v150, v160, v161\n"
"v_cvt_pk_bf16_f32 v151, v162, v163\n"
"v_cvt_pk_bf16_f32 v172, v172, v173\n"
"v_cvt_pk_bf16_f32 v173, v174, v175\n"
"v_mfma_f32_16x16x32_bf16 v[140:143], v[144:147], v[32:35], 0\n"
"ds_write_addtid_b32 v158 offset:32768\n"
"ds_write_addtid_b32 v159 offset:33024\n"
"s_mov_b32 m0, s68\n"
"ds_read_b128 v[152:155], v13 offset:49152\n"
"v_mfma_f32_16x16x32_bf16 v[156:159], v[144:147], v[24:27], 0\n"
"ds_write_addtid_b32 v148 offset:45056\n"
"ds_write_addtid_b32 v149 offset:45312\n"
"ds_write_addtid_b32 v150 offset:45568\n"
"ds_write_addtid_b32 v151 offset:45824\n"
"ds_write_addtid_b32 v172 offset:46080\n"
"ds_write_addtid_b32 v173 offset:46336\n"
"s_and_b32 s41, 32, s55\n"
"v_add_u32 v8, s41, v15\n"
"ds_read_u16 v8, v8 offset:480\n"
"ds_read_b128 v[144:147], v12 offset:51200\n"
"s_waitcnt lgkmcnt(8)\n"
"v_mfma_f32_16x16x32_bf16 v[136:139], v[152:155], v[40:43], v[136:139]\n"
"v_mfma_f32_16x16x32_bf16 v[140:143], v[152:155], v[44:47], v[140:143]\n"
"ds_read_b128 v[148:151], v13 offset:51200\n"
"v_mfma_f32_16x16x32_bf16 v[156:159], v[152:155], v[36:39], v[156:159]\n"
"s_waitcnt lgkmcnt(2)\n"
"v_mad_u32_u24 v8, v8, s40, v16\n"
"s_waitcnt lgkmcnt(1)\n"
"v_mfma_f32_16x16x32_bf16 v[136:139], v[144:147], v[48:51], v[136:139]\n"
"s_add_u32 m0, s51, 126976\n"
"s_nop 0\n"
"global_load_lds_dwordx4 v8, s[56:57]\n"
"v_mfma_f32_16x16x32_bf16 v[140:143], v[144:147], v[52:55], v[140:143]\n"
"ds_read_b128 v[152:155], v12 offset:53248\n"
"v_mfma_f32_16x16x32_bf16 v[156:159], v[144:147], v[56:59], v[156:159]\n"
"s_waitcnt lgkmcnt(1)\n"
"v_mfma_f32_16x16x32_bf16 v[136:139], v[148:151], v[60:63], v[136:139]\n"
"v_mfma_f32_16x16x32_bf16 v[140:143], v[148:151], v[64:67], v[140:143]\n"
"ds_read_b128 v[144:147], v13 offset:53248\n"
"v_mfma_f32_16x16x32_bf16 v[156:159], v[148:151], v[68:71], v[156:159]\n"
"s_waitcnt lgkmcnt(1)\n"
"v_mfma_f32_16x16x32_bf16 v[136:139], v[152:155], v[72:75], v[136:139]\n"
"s_add_u32 m0, s51, 129024\n"
"s_nop 0\n"
"global_load_lds_dwordx4 v8, s[58:59]\n"
"v_mfma_f32_16x16x32_bf16 v[140:143], v[152:155], v[76:79], v[140:143]\n"
"ds_read_b128 v[148:151], v12 offset:55296\n"
"v_mfma_f32_16x16x32_bf16 v[156:159], v[152:155], v[80:83], v[156:159]\n"
"s_waitcnt lgkmcnt(1)\n"
"v_mfma_f32_16x16x32_bf16 v[136:139], v[144:147], v[84:87], v[136:139]\n"
"v_mfma_f32_16x16x32_bf16 v[140:143], v[144:147], v[88:91], v[140:143]\n"
"ds_read_b128 v[152:155], v13 offset:55296\n"
"v_mfma_f32_16x16x32_bf16 v[156:159], v[144:147], v[92:95], v[156:159]\n"
"s_waitcnt lgkmcnt(1)\n"
"v_mfma_f32_16x16x32_bf16 v[144:147], v[148:151], v[28:31], 0\n"
"s_add_u32 m0, s51, 131072\n"
"s_nop 0\n"
"global_load_lds_dwordx4 v8, s[60:61]\n"
"v_mfma_f32_16x16x32_bf16 v[160:163], v[148:151], v[32:35], 0\n"
"ds_read_b128 v[164:167], v12 offset:57344\n"
"v_cvt_pk_bf16_f32 v184, v136, v137\n"
"v_mfma_f32_16x16x32_bf16 v[168:171], v[148:151], v[24:27], 0\n"
"s_waitcnt lgkmcnt(1)\n"
"v_cvt_pk_bf16_f32 v185, v138, v139\n"
"v_cvt_pk_bf16_f32 v210, v140, v141\n"
"v_mfma_f32_16x16x32_bf16 v[144:147], v[152:155], v[40:43], v[144:147]\n"
"v_cvt_pk_bf16_f32 v211, v142, v143\n"
"v_cvt_pk_bf16_f32 v236, v156, v157\n"
"v_mfma_f32_16x16x32_bf16 v[160:163], v[152:155], v[44:47], v[160:163]\n"
"ds_read_b128 v[136:139], v13 offset:57344\n"
"v_cvt_pk_bf16_f32 v237, v158, v159\n"
"v_mfma_f32_16x16x32_bf16 v[168:171], v[152:155], v[36:39], v[168:171]\n"
"s_waitcnt lgkmcnt(1)\n"
"v_mfma_f32_16x16x32_bf16 v[144:147], v[164:167], v[48:51], v[144:147]\n"
"v_mfma_f32_16x16x32_bf16 v[160:163], v[164:167], v[52:55], v[160:163]\n"
"ds_read_b128 v[140:143], v12 offset:59392\n"
"v_mfma_f32_16x16x32_bf16 v[168:171], v[164:167], v[56:59], v[168:171]\n"
"s_waitcnt lgkmcnt(1)\n"
"v_mfma_f32_16x16x32_bf16 v[144:147], v[136:139], v[60:63], v[144:147]\n"
"v_mfma_f32_16x16x32_bf16 v[160:163], v[136:139], v[64:67], v[160:163]\n"
"ds_read_b128 v[148:151], v13 offset:59392\n"
"v_mfma_f32_16x16x32_bf16 v[168:171], v[136:139], v[68:71], v[168:171]\n"
"s_waitcnt lgkmcnt(1)\n"
"v_mfma_f32_16x16x32_bf16 v[144:147], v[140:143], v[72:75], v[144:147]\n"
"v_mfma_f32_16x16x32_bf16 v[160:163], v[140:143], v[76:79], v[160:163]\n"
"v_mov_b32 v136, 0x3f803f80\n"
"v_mov_b32 v137, 0x3f803f80\n"
"v_mfma_f32_16x16x32_bf16 v[168:171], v[140:143], v[80:83], v[168:171]\n"
"s_waitcnt lgkmcnt(0)\n"
"v_mov_b32 v138, 0x3f803f80\n"
"v_mov_b32 v139, 0x3f803f80\n"
"v_mfma_f32_16x16x32_bf16 v[144:147], v[148:151], v[84:87], v[144:147]\n"
"s_waitcnt vmcnt(3)\n"
"s_barrier\n"
"ds_read2st64_b32 v[140:141], v9 offset0:112 offset1:113\n"
"ds_read2st64_b32 v[142:143], v9 offset0:114 offset1:115\n"
"v_mfma_f32_16x16x32_bf16 v[160:163], v[148:151], v[88:91], v[160:163]\n"
"s_and_b32 s41, 416, s55\n"
"v_add_u32 v8, s41, v15\n"
"v_mfma_f32_16x16x32_bf16 v[168:171], v[148:151], v[92:95], v[168:171]\n"
"ds_read2st64_b32 v[148:149], v9 offset0:116 offset1:117\n"
"v_cvt_pk_bf16_f32 v192, v144, v145\n"
"v_cvt_pk_bf16_f32 v193, v146, v147\n"
"ds_read2st64_b32 v[150:151], v9 offset0:118 offset1:119\n"
"ds_read_u16 v8, v8 offset:544\n"
"v_cvt_pk_bf16_f32 v218, v160, v161\n"
"v_cvt_pk_bf16_f32 v219, v162, v163\n"
"s_waitcnt lgkmcnt(3)\n"
"v_mfma_f32_16x16x32_bf16 v[144:147], v[136:139], v[140:143], 0\n"
"v_cvt_pk_bf16_f32 v244, v168, v169\n"
"v_cvt_pk_bf16_f32 v245, v170, v171\n"
"v_mfma_f32_16x16x32_bf16 v[152:155], v[180:183], v[140:143], 0\n"
"v_mfma_f32_16x16x32_bf16 v[156:159], v[206:209], v[140:143], 0\n"
"ds_read_u16 v255, v21 offset:800\n"
"v_mfma_f32_16x16x32_bf16 v[160:163], v[232:235], v[140:143], 0\n"
"s_waitcnt lgkmcnt(1)\n"
"ds_read2st64_b32 v[140:141], v20 offset0:0 offset1:1\n"
"v_mad_u32_u24 v8, v8, s40, v16\n"
"v_mfma_f32_16x16x32_bf16 v[144:147], v[136:139], v[148:151], v[144:147]\n"
"s_add_u32 m0, s51, 151552\n"
"s_nop 0\n"
"global_load_lds_dwordx4 v8, s[56:57]\n"
"ds_read2st64_b32 v[142:143], v20 offset0:2 offset1:3\n"
"v_mfma_f32_16x16x32_bf16 v[152:155], v[188:191], v[148:151], v[152:155]\n"
"ds_read2st64_b32 v[164:165], v9 offset0:128 offset1:129\n"
"ds_read2st64_b32 v[166:167], v9 offset0:130 offset1:131\n"
"v_mfma_f32_16x16x32_bf16 v[156:159], v[214:217], v[148:151], v[156:159]\n"
"s_waitcnt lgkmcnt(4)\n"
"ds_read2st64_b32 v[228:229], v20 offset0:4 offset1:5\n"
"v_mad_u32_u24 v254, v255, s40, v18\n"
"v_mfma_f32_16x16x32_bf16 v[160:163], v[240:243], v[148:151], v[160:163]\n"
"s_waitcnt lgkmcnt(3)\n"
"ds_read2st64_b32 v[148:149], v9 offset0:132 offset1:133\n"
"v_mad_u32_u24 v255, v255, s40, v19\n"
"ds_read2st64_b32 v[150:151], v9 offset0:134 offset1:135\n"
"global_store_dwordx4 v254, v[140:143], s[66:67] sc1\n"
"s_waitcnt lgkmcnt(3)\n"
"v_mfma_f32_16x16x32_bf16 v[144:147], v[136:139], v[164:167], v[144:147]\n"
"s_add_u32 m0, s51, 153600\n"
"s_nop 0\n"
"global_load_lds_dwordx4 v8, s[58:59]\n"
"ds_read2st64_b32 v[142:143], v9 offset0:36 offset1:37\n"
"v_mfma_f32_16x16x32_bf16 v[152:155], v[184:187], v[164:167], v[152:155]\n"
"s_waitcnt lgkmcnt(3)\n"
"global_store_dwordx2 v255, v[228:229], s[66:67] offset:256 sc1\n"
"v_mfma_f32_16x16x32_bf16 v[156:159], v[210:213], v[164:167], v[156:159]\n"
"v_mov_b32 v140, 0\n"
"v_mov_b32 v141, 0\n"
"v_mfma_f32_16x16x32_bf16 v[160:163], v[236:239], v[164:167], v[160:163]\n"
"s_waitcnt lgkmcnt(1)\n"
"v_mfma_f32_16x16x32_bf16 v[144:147], v[136:139], v[148:151], v[144:147]\n"
"s_waitcnt lgkmcnt(0)\n"
"v_mfma_f32_16x16x32_bf16 v[144:147], v[136:139], v[140:143], v[144:147]\n"
"s_add_u32 m0, s51, 155648\n"
"s_nop 0\n"
"global_load_lds_dwordx4 v8, s[60:61]\n"
"v_mfma_f32_16x16x32_bf16 v[160:163], v[248:251], v[148:151], v[160:163]\n"
"v_mfma_f32_16x16x32_bf16 v[160:163], v[250:253], v[140:143], v[160:163]\n"
"ds_read2st64_b32 v[136:137], v14 offset0:126 offset1:127\n"
"v_mfma_f32_16x16x32_bf16 v[152:155], v[196:199], v[148:151], v[152:155]\n"
"ds_read2st64_b32 v[138:139], v14 offset0:128 offset1:129\n"
"v_mfma_f32_16x16x32_bf16 v[152:155], v[198:201], v[140:143], v[152:155]\n"
"ds_read_b128 v[164:167], v17 offset:53248\n"
"v_rcp_f32 v8, v144\n"
"v_mfma_f32_16x16x32_bf16 v[156:159], v[222:225], v[148:151], v[156:159]\n"
"ds_read2st64_b32 v[144:145], v14 offset0:150 offset1:151\n"
"v_mul_f32 v160, v160, v8\n"
"v_mfma_f32_16x16x32_bf16 v[156:159], v[224:227], v[140:143], v[156:159]\n"
"ds_read2st64_b32 v[146:147], v14 offset0:152 offset1:153\n"
"v_mul_f32 v161, v161, v8\n"
"v_cvt_pk_bf16_f32 v254, v160, v161\n"
"v_mul_f32 v162, v162, v8\n"
"v_mul_f32 v163, v163, v8\n"
"v_cvt_pk_bf16_f32 v255, v162, v163\n"
"v_mul_f32 v152, v152, v8\n"
"s_waitcnt lgkmcnt(3)\n"
"v_mfma_f32_16x16x32_bf16 v[140:143], v[96:99], v[136:139], 0\n"
"ds_read_b128 v[148:151], v17 offset:54272\n"
"v_mul_f32 v153, v153, v8\n"
"v_mfma_f32_16x16x32_bf16 v[160:163], v[100:103], v[136:139], 0\n"
"s_waitcnt lgkmcnt(3)\n"
"ds_read2st64_b32 v[168:169], v14 offset0:174 offset1:175\n"
"v_cvt_pk_bf16_f32 v152, v152, v153\n"
"v_mfma_f32_16x16x32_bf16 v[172:175], v[164:167], v[136:139], 0\n"
"s_waitcnt lgkmcnt(2)\n"
"ds_read2st64_b32 v[170:171], v14 offset0:176 offset1:177\n"
"v_mul_f32 v154, v154, v8\n"
"v_mul_f32 v155, v155, v8\n"
"v_mfma_f32_16x16x32_bf16 v[140:143], v[104:107], v[144:147], v[140:143]\n"
"ds_read_b128 v[136:139], v17 offset:55296\n"
"v_cvt_pk_bf16_f32 v153, v154, v155\n"
"v_mfma_f32_16x16x32_bf16 v[160:163], v[108:111], v[144:147], v[160:163]\n"
"s_waitcnt lgkmcnt(3)\n"
"ds_read2st64_b32 v[164:165], v14 offset0:198 offset1:199\n"
"v_mul_f32 v156, v156, v8\n"
"v_mul_f32 v157, v157, v8\n"
"v_mfma_f32_16x16x32_bf16 v[172:175], v[148:151], v[144:147], v[172:175]\n"
"ds_read2st64_b32 v[166:167], v14 offset0:200 offset1:201\n"
"v_cvt_pk_bf16_f32 v154, v156, v157\n"
"v_mul_f32 v158, v158, v8\n"
"v_mul_f32 v159, v159, v8\n"
"v_cvt_pk_bf16_f32 v155, v158, v159\n"
"s_waitcnt lgkmcnt(3)\n"
"v_mfma_f32_16x16x32_bf16 v[140:143], v[0:3], v[168:171], v[140:143]\n"
"ds_read_b128 v[144:147], v17 offset:56320\n"
"v_mfma_f32_16x16x32_bf16 v[160:163], v[112:115], v[168:171], v[160:163]\n"
"s_waitcnt lgkmcnt(3)\n"
"ds_read2st64_b32 v[148:149], v14 offset0:130 offset1:131\n"
"v_mfma_f32_16x16x32_bf16 v[172:175], v[136:139], v[168:171], v[172:175]\n"
"s_waitcnt lgkmcnt(2)\n"
"ds_read2st64_b32 v[150:151], v14 offset0:154 offset1:155\n"
"v_mfma_f32_16x16x32_bf16 v[140:143], v[116:119], v[164:167], v[140:143]\n"
"ds_read_b128 v[136:139], v17 offset:57344\n"
"v_mfma_f32_16x16x32_bf16 v[160:163], v[120:123], v[164:167], v[160:163]\n"
"s_waitcnt lgkmcnt(3)\n"
"ds_read2st64_b32 v[156:157], v14 offset0:178 offset1:179\n"
"v_mfma_f32_16x16x32_bf16 v[172:175], v[144:147], v[164:167], v[172:175]\n"
"s_waitcnt lgkmcnt(2)\n"
"ds_read2st64_b32 v[158:159], v14 offset0:202 offset1:203\n"
"v_mfma_f32_16x16x32_bf16 v[140:143], v[4:7], v[148:151], v[140:143]\n"
"ds_read_b128 v[144:147], v17 offset:58368\n"
"v_mfma_f32_16x16x32_bf16 v[160:163], v[124:127], v[148:151], v[160:163]\n"
"s_waitcnt lgkmcnt(3)\n"
"ds_read_b128 v[164:167], v12 offset:18432\n"
"v_mfma_f32_16x16x32_bf16 v[172:175], v[136:139], v[148:151], v[172:175]\n"
"s_waitcnt lgkmcnt(2)\n"
"v_mfma_f32_16x16x32_bf16 v[140:143], v[128:131], v[156:159], v[140:143]\n"
"v_mfma_f32_16x16x32_bf16 v[160:163], v[132:135], v[156:159], v[160:163]\n"
"s_barrier\n"
"ds_read_b128 v[136:139], v13 offset:18432\n"
"s_waitcnt lgkmcnt(2)\n"
"v_mfma_f32_16x16x32_bf16 v[172:175], v[144:147], v[156:159], v[172:175]\n"
"s_waitcnt lgkmcnt(1)\n"
"v_mfma_f32_16x16x32_bf16 v[144:147], v[164:167], v[28:31], 0\n"
"v_mfma_f32_16x16x32_bf16 v[148:151], v[164:167], v[32:35], 0\n"
"ds_read_b128 v[156:159], v12 offset:20480\n"
"v_mfma_f32_16x16x32_bf16 v[168:171], v[164:167], v[24:27], 0\n"
"v_cvt_pk_bf16_f32 v140, v140, v141\n"
"v_cvt_pk_bf16_f32 v141, v142, v143\n"
"v_cvt_pk_bf16_f32 v142, v160, v161\n"
"v_cvt_pk_bf16_f32 v143, v162, v163\n"
"s_waitcnt lgkmcnt(1)\n"
"v_mfma_f32_16x16x32_bf16 v[144:147], v[136:139], v[40:43], v[144:147]\n"
"s_mov_b32 m0, s54\n"
"v_cvt_pk_bf16_f32 v172, v172, v173\n"
"v_cvt_pk_bf16_f32 v173, v174, v175\n"
"v_mfma_f32_16x16x32_bf16 v[148:151], v[136:139], v[44:47], v[148:151]\n"
"ds_read_b128 v[160:163], v13 offset:20480\n"
"v_mfma_f32_16x16x32_bf16 v[168:171], v[136:139], v[36:39], v[168:171]\n"
"ds_write_addtid_b32 v254 offset:33280\n"
"s_waitcnt lgkmcnt(2)\n"
"v_mfma_f32_16x16x32_bf16 v[144:147], v[156:159], v[48:51], v[144:147]\n"
"ds_write_addtid_b32 v255 offset:33536\n"
"v_mfma_f32_16x16x32_bf16 v[148:151], v[156:159], v[52:55], v[148:151]\n"
"ds_write_addtid_b32 v152 offset:32256\n"
"v_mfma_f32_16x16x32_bf16 v[168:171], v[156:159], v[56:59], v[168:171]\n"
"ds_read_b128 v[136:139], v12 offset:22528\n"
"ds_write_addtid_b32 v153 offset:32512\n"
"s_waitcnt lgkmcnt(5)\n"
"v_mfma_f32_16x16x32_bf16 v[144:147], v[160:163], v[60:63], v[144:147]\n"
"ds_write_addtid_b32 v154 offset:32768\n"
"v_mfma_f32_16x16x32_bf16 v[148:151], v[160:163], v[64:67], v[148:151]\n"
"ds_write_addtid_b32 v155 offset:33024\n"
"s_mov_b32 m0, s68\n"
"ds_read_b128 v[152:155], v13 offset:22528\n"
"v_mfma_f32_16x16x32_bf16 v[168:171], v[160:163], v[68:71], v[168:171]\n"
"ds_write_addtid_b32 v140 offset:45056\n"
"ds_write_addtid_b32 v141 offset:45312\n"
"ds_write_addtid_b32 v142 offset:45568\n"
"s_waitcnt lgkmcnt(7)\n"
"v_mfma_f32_16x16x32_bf16 v[144:147], v[136:139], v[72:75], v[144:147]\n"
"ds_write_addtid_b32 v143 offset:45824\n"
"v_mfma_f32_16x16x32_bf16 v[148:151], v[136:139], v[76:79], v[148:151]\n"
"ds_read_b128 v[140:143], v12 offset:36864\n"
"v_mfma_f32_16x16x32_bf16 v[168:171], v[136:139], v[80:83], v[168:171]\n"
"ds_write_addtid_b32 v172 offset:46080\n"
"s_waitcnt lgkmcnt(6)\n"
"v_mfma_f32_16x16x32_bf16 v[144:147], v[152:155], v[84:87], v[144:147]\n"
"ds_write_addtid_b32 v173 offset:46336\n"
"v_mfma_f32_16x16x32_bf16 v[148:151], v[152:155], v[88:91], v[148:151]\n"
"ds_read_b128 v[136:139], v13 offset:36864\n"
"v_mfma_f32_16x16x32_bf16 v[168:171], v[152:155], v[92:95], v[168:171]\n"
"s_waitcnt lgkmcnt(3)\n"
"v_mfma_f32_16x16x32_bf16 v[152:155], v[140:143], v[28:31], 0\n"
"v_mfma_f32_16x16x32_bf16 v[156:159], v[140:143], v[32:35], 0\n"
"ds_read_b128 v[160:163], v12 offset:38912\n"
"v_mfma_f32_16x16x32_bf16 v[164:167], v[140:143], v[24:27], 0\n"
"s_waitcnt lgkmcnt(1)\n"
"v_cvt_pk_bf16_f32 v202, v144, v145\n"
"v_cvt_pk_bf16_f32 v203, v146, v147\n"
"v_mfma_f32_16x16x32_bf16 v[152:155], v[136:139], v[40:43], v[152:155]\n"
"v_cvt_pk_bf16_f32 v228, v148, v149\n"
"v_cvt_pk_bf16_f32 v229, v150, v151\n"
"v_mfma_f32_16x16x32_bf16 v[156:159], v[136:139], v[44:47], v[156:159]\n"
"ds_read_b128 v[140:143], v13 offset:38912\n"
"v_cvt_pk_bf16_f32 v254, v168, v169\n"
"v_cvt_pk_bf16_f32 v255, v170, v171\n"
"v_mfma_f32_16x16x32_bf16 v[164:167], v[136:139], v[36:39], v[164:167]\n"
"s_waitcnt lgkmcnt(1)\n"
"v_mfma_f32_16x16x32_bf16 v[152:155], v[160:163], v[48:51], v[152:155]\n"
"v_mfma_f32_16x16x32_bf16 v[156:159], v[160:163], v[52:55], v[156:159]\n"
"ds_read_b128 v[136:139], v12 offset:40960\n"
"v_mfma_f32_16x16x32_bf16 v[164:167], v[160:163], v[56:59], v[164:167]\n"
"s_waitcnt lgkmcnt(1)\n"
"v_mfma_f32_16x16x32_bf16 v[152:155], v[140:143], v[60:63], v[152:155]\n"
"v_mfma_f32_16x16x32_bf16 v[156:159], v[140:143], v[64:67], v[156:159]\n"
"ds_read_b128 v[144:147], v13 offset:40960\n"
"v_mfma_f32_16x16x32_bf16 v[164:167], v[140:143], v[68:71], v[164:167]\n"
"s_waitcnt lgkmcnt(1)\n"
"v_mfma_f32_16x16x32_bf16 v[152:155], v[136:139], v[72:75], v[152:155]\n"
"v_mfma_f32_16x16x32_bf16 v[156:159], v[136:139], v[76:79], v[156:159]\n"
"v_mov_b32 v140, 0x3f803f80\n"
"v_mov_b32 v141, 0x3f803f80\n"
"v_mfma_f32_16x16x32_bf16 v[164:167], v[136:139], v[80:83], v[164:167]\n"
"s_waitcnt lgkmcnt(0)\n"
"v_mov_b32 v142, 0x3f803f80\n"
"v_mov_b32 v143, 0x3f803f80\n"
"v_mfma_f32_16x16x32_bf16 v[152:155], v[144:147], v[84:87], v[152:155]\n"
"s_waitcnt vmcnt(0)\n"
"s_barrier\n"
"ds_read2st64_b32 v[136:137], v9 offset0:120 offset1:121\n"
"ds_read2st64_b32 v[138:139], v9 offset0:122 offset1:123\n"
"v_mfma_f32_16x16x32_bf16 v[156:159], v[144:147], v[88:91], v[156:159]\n"
"s_and_b32 s41, 416, s55\n"
"v_add_u32 v8, s41, v15\n"
"v_mfma_f32_16x16x32_bf16 v[164:167], v[144:147], v[92:95], v[164:167]\n"
"ds_read_b32 v145, v9 offset:32000\n"
"v_cvt_pk_bf16_f32 v186, v152, v153\n"
"v_cvt_pk_bf16_f32 v187, v154, v155\n"
"ds_read2st64_b32 v[146:147], v9 offset0:126 offset1:127\n"
"ds_read_u16 v8, v8 offset:576\n"
"v_cvt_pk_bf16_f32 v212, v156, v157\n"
"v_cvt_pk_bf16_f32 v213, v158, v159\n"
"s_waitcnt lgkmcnt(3)\n"
"v_mfma_f32_16x16x32_bf16 v[148:151], v[140:143], v[136:139], 0\n"
"v_mov_b32 v144, 0\n"
"v_cvt_pk_bf16_f32 v238, v164, v165\n"
"v_mfma_f32_16x16x32_bf16 v[152:155], v[190:193], v[136:139], 0\n"
"v_cvt_pk_bf16_f32 v239, v166, v167\n"
"v_mfma_f32_16x16x32_bf16 v[190:193], v[216:219], v[136:139], 0\n"
"ds_read_u16 v11, v21 offset:832\n"
"v_mfma_f32_16x16x32_bf16 v[216:219], v[242:245], v[136:139], 0\n"
"s_waitcnt lgkmcnt(1)\n"
"ds_read2st64_b32 v[242:243], v20 offset0:0 offset1:1\n"
"v_mad_u32_u24 v8, v8, s40, v16\n"
"v_mfma_f32_16x16x32_bf16 v[148:151], v[140:143], v[144:147], v[148:151]\n"
"s_add_u32 m0, s51, 114688\n"
"s_nop 0\n"
"global_load_lds_dwordx4 v8, s[56:57]\n"
"ds_read2st64_b32 v[244:245], v20 offset0:2 offset1:3\n"
"v_mfma_f32_16x16x32_bf16 v[152:155], v[182:185], v[144:147], v[152:155]\n"
"ds_read2st64_b32 v[136:137], v9 offset0:128 offset1:129\n"
"v_mov_b32 v138, 0\n"
"v_mfma_f32_16x16x32_bf16 v[190:193], v[208:211], v[144:147], v[190:193]\n"
"ds_read_b32 v139, v9 offset:33536\n"
"v_mfma_f32_16x16x32_bf16 v[216:219], v[234:237], v[144:147], v[216:219]\n"
"s_waitcnt lgkmcnt(4)\n"
"ds_read2st64_b32 v[174:175], v20 offset0:4 offset1:5\n"
"v_mad_u32_u24 v10, v11, s40, v18\n"
"v_mad_u32_u24 v11, v11, s40, v19\n"
"ds_read_b32 v145, v9 offset:34048\n"
"ds_read2st64_b32 v[146:147], v9 offset0:134 offset1:135\n"
"s_waitcnt lgkmcnt(5)\n"
"global_store_dwordx4 v10, v[242:245], s[66:67] sc1\n"
"s_waitcnt lgkmcnt(3)\n"
"v_mfma_f32_16x16x32_bf16 v[148:151], v[140:143], v[136:139], v[148:151]\n"
"s_add_u32 m0, s51, 116736\n"
"s_nop 0\n"
"global_load_lds_dwordx4 v8, s[58:59]\n"
"ds_read2st64_b32 v[244:245], v9 offset0:38 offset1:39\n"
"v_mfma_f32_16x16x32_bf16 v[152:155], v[186:189], v[136:139], v[152:155]\n"
"s_waitcnt lgkmcnt(3)\n"
"global_store_dwordx2 v11, v[174:175], s[66:67] offset:256 sc1\n"
"v_mfma_f32_16x16x32_bf16 v[190:193], v[212:215], v[136:139], v[190:193]\n"
"v_mov_b32 v144, 0\n"
"v_mov_b32 v242, 0\n"
"v_mfma_f32_16x16x32_bf16 v[216:219], v[238:241], v[136:139], v[216:219]\n"
"s_waitcnt lgkmcnt(1)\n"
"v_mov_b32 v243, 0\n"
"v_mfma_f32_16x16x32_bf16 v[148:151], v[140:143], v[144:147], v[148:151]\n"
"s_waitcnt lgkmcnt(0)\n"
"v_mfma_f32_16x16x32_bf16 v[148:151], v[140:143], v[242:245], v[148:151]\n"
"s_add_u32 m0, s51, 118784\n"
"s_nop 0\n"
"global_load_lds_dwordx4 v8, s[60:61]\n"
"v_mfma_f32_16x16x32_bf16 v[216:219], v[250:253], v[144:147], v[216:219]\n"
"v_mfma_f32_16x16x32_bf16 v[216:219], v[252:255], v[242:245], v[216:219]\n"
"ds_read2st64_b32 v[136:137], v14 offset0:126 offset1:127\n"
"v_mfma_f32_16x16x32_bf16 v[152:155], v[198:201], v[144:147], v[152:155]\n"
"ds_read2st64_b32 v[138:139], v14 offset0:128 offset1:129\n"
"v_mfma_f32_16x16x32_bf16 v[152:155], v[200:203], v[242:245], v[152:155]\n"
"ds_read_b128 v[140:143], v17 offset:53248\n"
"v_rcp_f32 v8, v148\n"
"v_mfma_f32_16x16x32_bf16 v[190:193], v[224:227], v[144:147], v[190:193]\n"
"ds_read2st64_b32 v[240:241], v14 offset0:150 offset1:151\n"
"v_mul_f32 v216, v216, v8\n"
"v_mfma_f32_16x16x32_bf16 v[190:193], v[226:229], v[242:245], v[190:193]\n"
"ds_read2st64_b32 v[242:243], v14 offset0:152 offset1:153\n"
"v_mul_f32 v217, v217, v8\n"
"v_cvt_pk_bf16_f32 v244, v216, v217\n"
"v_mul_f32 v218, v218, v8\n"
"v_mul_f32 v219, v219, v8\n"
"v_cvt_pk_bf16_f32 v245, v218, v219\n"
"v_mul_f32 v152, v152, v8\n"
"s_waitcnt lgkmcnt(3)\n"
"v_mfma_f32_16x16x32_bf16 v[214:217], v[96:99], v[136:139], 0\n"
"ds_read_b128 v[144:147], v17 offset:54272\n"
"v_mul_f32 v153, v153, v8\n"
"v_mfma_f32_16x16x32_bf16 v[148:151], v[100:103], v[136:139], 0\n"
"s_waitcnt lgkmcnt(3)\n"
"ds_read2st64_b32 v[156:157], v14 offset0:174 offset1:175\n"
"v_cvt_pk_bf16_f32 v152, v152, v153\n"
"v_mfma_f32_16x16x32_bf16 v[160:163], v[140:143], v[136:139], 0\n"
"s_waitcnt lgkmcnt(2)\n"
"ds_read2st64_b32 v[158:159], v14 offset0:176 offset1:177\n"
"v_mul_f32 v154, v154, v8\n"
"v_mul_f32 v155, v155, v8\n"
"v_mfma_f32_16x16x32_bf16 v[214:217], v[104:107], v[240:243], v[214:217]\n"
"ds_read_b128 v[136:139], v17 offset:55296\n"
"v_cvt_pk_bf16_f32 v153, v154, v155\n"
"v_mfma_f32_16x16x32_bf16 v[148:151], v[108:111], v[240:243], v[148:151]\n"
"s_waitcnt lgkmcnt(3)\n"
"ds_read2st64_b32 v[140:141], v14 offset0:198 offset1:199\n"
"v_mul_f32 v190, v190, v8\n"
"v_mul_f32 v191, v191, v8\n"
"v_mfma_f32_16x16x32_bf16 v[160:163], v[144:147], v[240:243], v[160:163]\n"
"ds_read2st64_b32 v[142:143], v14 offset0:200 offset1:201\n"
"v_cvt_pk_bf16_f32 v154, v190, v191\n"
"v_mul_f32 v192, v192, v8\n"
"v_mul_f32 v193, v193, v8\n"
"v_cvt_pk_bf16_f32 v155, v192, v193\n"
"s_waitcnt lgkmcnt(3)\n"
"v_mfma_f32_16x16x32_bf16 v[214:217], v[0:3], v[156:159], v[214:217]\n"
"ds_read_b128 v[188:191], v17 offset:56320\n"
"v_mfma_f32_16x16x32_bf16 v[148:151], v[112:115], v[156:159], v[148:151]\n"
"s_waitcnt lgkmcnt(3)\n"
"ds_read2st64_b32 v[240:241], v14 offset0:130 offset1:131\n"
"v_mfma_f32_16x16x32_bf16 v[160:163], v[136:139], v[156:159], v[160:163]\n"
"s_waitcnt lgkmcnt(2)\n"
"ds_read2st64_b32 v[242:243], v14 offset0:154 offset1:155\n"
"v_mfma_f32_16x16x32_bf16 v[214:217], v[116:119], v[140:143], v[214:217]\n"
"ds_read_b128 v[136:139], v17 offset:57344\n"
"v_mfma_f32_16x16x32_bf16 v[148:151], v[120:123], v[140:143], v[148:151]\n"
"s_waitcnt lgkmcnt(3)\n"
"ds_read2st64_b32 v[144:145], v14 offset0:178 offset1:179\n"
"v_mfma_f32_16x16x32_bf16 v[160:163], v[188:191], v[140:143], v[160:163]\n"
"s_waitcnt lgkmcnt(2)\n"
"ds_read2st64_b32 v[146:147], v14 offset0:202 offset1:203\n"
"v_mfma_f32_16x16x32_bf16 v[214:217], v[4:7], v[240:243], v[214:217]\n"
"ds_read_b128 v[188:191], v17 offset:58368\n"
"v_mfma_f32_16x16x32_bf16 v[148:151], v[124:127], v[240:243], v[148:151]\n"
"s_waitcnt lgkmcnt(3)\n"
"ds_read_b128 v[140:143], v12 offset:24576\n"
"v_mfma_f32_16x16x32_bf16 v[160:163], v[136:139], v[240:243], v[160:163]\n"
"s_waitcnt lgkmcnt(2)\n"
"v_mfma_f32_16x16x32_bf16 v[214:217], v[128:131], v[144:147], v[214:217]\n"
"v_mfma_f32_16x16x32_bf16 v[148:151], v[132:135], v[144:147], v[148:151]\n"
"s_barrier\n"
"ds_read_b128 v[240:243], v13 offset:24576\n"
"s_mov_b32 m0, s54\n"
"s_waitcnt lgkmcnt(2)\n"
"v_mfma_f32_16x16x32_bf16 v[160:163], v[188:191], v[144:147], v[160:163]\n"
"ds_write_addtid_b32 v244 offset:33280\n"
"s_waitcnt lgkmcnt(2)\n"
"v_mfma_f32_16x16x32_bf16 v[190:193], v[140:143], v[28:31], 0\n"
"ds_write_addtid_b32 v245 offset:33536\n"
"v_cvt_pk_bf16_f32 v214, v214, v215\n"
"v_cvt_pk_bf16_f32 v215, v216, v217\n"
"v_cvt_pk_bf16_f32 v216, v148, v149\n"
"v_cvt_pk_bf16_f32 v217, v150, v151\n"
"v_mfma_f32_16x16x32_bf16 v[136:139], v[140:143], v[32:35], 0\n"
"ds_write_addtid_b32 v152 offset:32256\n"
"v_cvt_pk_bf16_f32 v160, v160, v161\n"
"v_cvt_pk_bf16_f32 v161, v162, v163\n"
"v_mfma_f32_16x16x32_bf16 v[144:147], v[140:143], v[24:27], 0\n"
"ds_write_addtid_b32 v153 offset:32512\n"
"ds_write_addtid_b32 v154 offset:32768\n"
"s_waitcnt lgkmcnt(5)\n"
"v_mfma_f32_16x16x32_bf16 v[190:193], v[240:243], v[40:43], v[190:193]\n"
"ds_write_addtid_b32 v155 offset:33024\n"
"s_mov_b32 m0, s68\n"
"ds_read_b128 v[140:143], v12 offset:26624\n"
"v_mfma_f32_16x16x32_bf16 v[136:139], v[240:243], v[44:47], v[136:139]\n"
"ds_write_addtid_b32 v214 offset:45056\n"
"ds_write_addtid_b32 v215 offset:45312\n"
"v_mfma_f32_16x16x32_bf16 v[144:147], v[240:243], v[36:39], v[144:147]\n"
"ds_write_addtid_b32 v216 offset:45568\n"
"ds_write_addtid_b32 v217 offset:45824\n"
"ds_write_addtid_b32 v160 offset:46080\n"
"ds_write_addtid_b32 v161 offset:46336\n"
"s_and_b32 s41, 416, s55\n"
"v_add_u32 v8, s41, v15\n"
"ds_read_u16 v8, v8 offset:608\n"
"s_waitcnt lgkmcnt(7)\n"
"v_mfma_f32_16x16x32_bf16 v[190:193], v[140:143], v[48:51], v[190:193]\n"
"ds_read_b128 v[214:217], v13 offset:26624\n"
"v_mfma_f32_16x16x32_bf16 v[136:139], v[140:143], v[52:55], v[136:139]\n"
"ds_read_b128 v[240:243], v12 offset:28672\n"
"v_mfma_f32_16x16x32_bf16 v[144:147], v[140:143], v[56:59], v[144:147]\n"
"s_waitcnt lgkmcnt(2)\n"
"v_mad_u32_u24 v8, v8, s40, v16\n"
"s_add_u32 m0, s51, 139264\n"
"s_nop 0\n"
"global_load_lds_dwordx4 v8, s[56:57]\n"
"s_waitcnt lgkmcnt(1)\n"
"v_mfma_f32_16x16x32_bf16 v[190:193], v[214:217], v[60:63], v[190:193]\n"
"v_mfma_f32_16x16x32_bf16 v[136:139], v[214:217], v[64:67], v[136:139]\n"
"ds_read_b128 v[140:143], v13 offset:28672\n"
"v_mfma_f32_16x16x32_bf16 v[144:147], v[214:217], v[68:71], v[144:147]\n"
"s_waitcnt lgkmcnt(1)\n"
"v_mfma_f32_16x16x32_bf16 v[190:193], v[240:243], v[72:75], v[190:193]\n"
"v_mfma_f32_16x16x32_bf16 v[136:139], v[240:243], v[76:79], v[136:139]\n"
"ds_read_b128 v[214:217], v12 offset:30720\n"
"v_mfma_f32_16x16x32_bf16 v[144:147], v[240:243], v[80:83], v[144:147]\n"
"s_add_u32 m0, s51, 141312\n"
"s_nop 0\n"
"global_load_lds_dwordx4 v8, s[58:59]\n"
"s_waitcnt lgkmcnt(1)\n"
"v_mfma_f32_16x16x32_bf16 v[190:193], v[140:143], v[84:87], v[190:193]\n"
"v_mfma_f32_16x16x32_bf16 v[136:139], v[140:143], v[88:91], v[136:139]\n"
"ds_read_b128 v[242:245], v13 offset:30720\n"
"v_mfma_f32_16x16x32_bf16 v[144:147], v[140:143], v[92:95], v[144:147]\n"
"s_waitcnt lgkmcnt(1)\n"
"v_mfma_f32_16x16x32_bf16 v[140:143], v[214:217], v[28:31], 0\n"
"v_mfma_f32_16x16x32_bf16 v[148:151], v[214:217], v[32:35], 0\n"
"ds_read_b128 v[152:155], v12 offset:32768\n"
"s_add_u32 m0, s51, 143360\n"
"s_nop 0\n"
"global_load_lds_dwordx4 v8, s[60:61]\n"
"v_mfma_f32_16x16x32_bf16 v[156:159], v[214:217], v[24:27], 0\n"
"s_waitcnt lgkmcnt(1)\n"
"v_cvt_pk_bf16_f32 v188, v190, v191\n"
"v_cvt_pk_bf16_f32 v189, v192, v193\n"
"v_mfma_f32_16x16x32_bf16 v[140:143], v[242:245], v[40:43], v[140:143]\n"
"v_cvt_pk_bf16_f32 v214, v136, v137\n"
"v_cvt_pk_bf16_f32 v215, v138, v139\n"
"v_mfma_f32_16x16x32_bf16 v[148:151], v[242:245], v[44:47], v[148:151]\n"
"ds_read_b128 v[190:193], v13 offset:32768\n"
"v_cvt_pk_bf16_f32 v240, v144, v145\n"
"v_cvt_pk_bf16_f32 v241, v146, v147\n"
"v_mfma_f32_16x16x32_bf16 v[156:159], v[242:245], v[36:39], v[156:159]\n"
"s_waitcnt lgkmcnt(1)\n"
"v_mfma_f32_16x16x32_bf16 v[140:143], v[152:155], v[48:51], v[140:143]\n"
"v_mfma_f32_16x16x32_bf16 v[148:151], v[152:155], v[52:55], v[148:151]\n"
"ds_read_b128 v[216:219], v12 offset:34816\n"
"v_mfma_f32_16x16x32_bf16 v[156:159], v[152:155], v[56:59], v[156:159]\n"
"s_waitcnt lgkmcnt(1)\n"
"v_mfma_f32_16x16x32_bf16 v[140:143], v[190:193], v[60:63], v[140:143]\n"
"v_mfma_f32_16x16x32_bf16 v[148:151], v[190:193], v[64:67], v[148:151]\n"
"ds_read_b128 v[242:245], v13 offset:34816\n"
"v_mfma_f32_16x16x32_bf16 v[156:159], v[190:193], v[68:71], v[156:159]\n"
"s_waitcnt lgkmcnt(1)\n"
"v_mfma_f32_16x16x32_bf16 v[140:143], v[216:219], v[72:75], v[140:143]\n"
"v_mfma_f32_16x16x32_bf16 v[148:151], v[216:219], v[76:79], v[148:151]\n"
"v_mov_b32 v136, 0x3f803f80\n"
"v_mov_b32 v137, 0x3f803f80\n"
"v_mfma_f32_16x16x32_bf16 v[156:159], v[216:219], v[80:83], v[156:159]\n"
"s_waitcnt lgkmcnt(0)\n"
"v_mov_b32 v138, 0x3f803f80\n"
"v_mov_b32 v139, 0x3f803f80\n"
"v_mfma_f32_16x16x32_bf16 v[140:143], v[242:245], v[84:87], v[140:143]\n"
"s_barrier\n"
"ds_read2st64_b32 v[144:145], v9 offset0:112 offset1:113\n"
"ds_read2st64_b32 v[146:147], v9 offset0:114 offset1:115\n"
"v_mfma_f32_16x16x32_bf16 v[148:151], v[242:245], v[88:91], v[148:151]\n"
"s_andn2_b32 s41, 416, s55\n"
"v_add_u32 v8, s41, v15\n"
"v_mfma_f32_16x16x32_bf16 v[156:159], v[242:245], v[92:95], v[156:159]\n"
"ds_read2st64_b32 v[152:153], v9 offset0:116 offset1:117\n"
"v_cvt_pk_bf16_f32 v190, v140, v141\n"
"v_cvt_pk_bf16_f32 v191, v142, v143\n"
"ds_read2st64_b32 v[154:155], v9 offset0:118 offset1:119\n"
"ds_read_u16 v8, v8 offset:640\n"
"v_cvt_pk_bf16_f32 v216, v148, v149\n"
"v_cvt_pk_bf16_f32 v217, v150, v151\n"
"s_waitcnt lgkmcnt(3)\n"
"v_mfma_f32_16x16x32_bf16 v[140:143], v[136:139], v[144:147], 0\n"
"ds_read2st64_b32 v[162:163], v9 offset0:128 offset1:129\n"
"v_cvt_pk_bf16_f32 v242, v156, v157\n"
"v_mfma_f32_16x16x32_bf16 v[148:151], v[178:181], v[144:147], 0\n"
"ds_read2st64_b32 v[164:165], v9 offset0:130 offset1:131\n"
"v_cvt_pk_bf16_f32 v243, v158, v159\n"
"v_mfma_f32_16x16x32_bf16 v[156:159], v[204:207], v[144:147], 0\n"
"ds_read_u16 v245, v21 offset:864\n"
"v_mfma_f32_16x16x32_bf16 v[168:171], v[230:233], v[144:147], 0\n"
"s_waitcnt lgkmcnt(3)\n"
"ds_read2st64_b32 v[144:145], v20 offset0:0 offset1:1\n"
"v_mad_u32_u24 v8, v8, s40, v16\n"
"v_mfma_f32_16x16x32_bf16 v[140:143], v[136:139], v[152:155], v[140:143]\n"
"s_add_u32 m0, s51, 126976\n"
"s_nop 0\n"
"global_load_lds_dwordx4 v8, s[56:57]\n"
"ds_read2st64_b32 v[146:147], v20 offset0:2 offset1:3\n"
"v_mov_b32 v160, 0\n"
"v_mov_b32 v161, 0\n"
"v_mov_b32 v166, 0\n"
"v_mov_b32 v167, 0\n"
"s_waitcnt lgkmcnt(3)\n"
"v_mfma_f32_16x16x32_bf16 v[140:143], v[136:139], v[162:165], v[140:143]\n"
"v_mfma_f32_16x16x32_bf16 v[168:171], v[246:249], v[152:155], v[168:171]\n"
"s_waitcnt lgkmcnt(2)\n"
"ds_read2st64_b32 v[218:219], v20 offset0:4 offset1:5\n"
"v_mad_u32_u24 v244, v245, s40, v18\n"
"v_mfma_f32_16x16x32_bf16 v[168:171], v[232:235], v[160:163], v[168:171]\n"
"v_mad_u32_u24 v245, v245, s40, v19\n"
"v_mfma_f32_16x16x32_bf16 v[168:171], v[250:253], v[164:167], v[168:171]\n"
"s_waitcnt lgkmcnt(1)\n"
"global_store_dwordx4 v244, v[144:147], s[66:67] sc1\n"
"v_mfma_f32_16x16x32_bf16 v[148:151], v[194:197], v[152:155], v[148:151]\n"
"s_add_u32 m0, s51, 129024\n"
"s_nop 0\n"
"global_load_lds_dwordx4 v8, s[58:59]\n"
"v_mfma_f32_16x16x32_bf16 v[148:151], v[180:183], v[160:163], v[148:151]\n"
"ds_read2st64_b32 v[136:137], v14 offset0:126 offset1:127\n"
"v_mfma_f32_16x16x32_bf16 v[148:151], v[198:201], v[164:167], v[148:151]\n"
"ds_read2st64_b32 v[138:139], v14 offset0:128 offset1:129\n"
"v_rcp_f32 v244, v140\n"
"v_mfma_f32_16x16x32_bf16 v[156:159], v[220:223], v[152:155], v[156:159]\n"
"s_waitcnt lgkmcnt(2)\n"
"global_store_dwordx2 v245, v[218:219], s[66:67] offset:256 sc1\n"
"ds_read_b128 v[140:143], v17 offset:53248\n"
"v_mfma_f32_16x16x32_bf16 v[156:159], v[206:209], v[160:163], v[156:159]\n"
"ds_read2st64_b32 v[144:145], v14 offset0:150 offset1:151\n"
"v_mul_f32 v168, v168, v244\n"
"v_mfma_f32_16x16x32_bf16 v[156:159], v[224:227], v[164:167], v[156:159]\n"
"ds_read2st64_b32 v[146:147], v14 offset0:152 offset1:153\n"
"v_mul_f32 v169, v169, v244\n"
"v_cvt_pk_bf16_f32 v218, v168, v169\n"
"v_mul_f32 v170, v170, v244\n"
"v_mul_f32 v171, v171, v244\n"
"s_waitcnt lgkmcnt(3)\n"
"v_mfma_f32_16x16x32_bf16 v[152:155], v[96:99], v[136:139], 0\n"
"s_add_u32 m0, s51, 131072\n"
"s_nop 0\n"
"global_load_lds_dwordx4 v8, s[60:61]\n"
"ds_read_b128 v[160:163], v17 offset:54272\n"
"v_mfma_f32_16x16x32_bf16 v[164:167], v[100:103], v[136:139], 0\n"
"s_waitcnt lgkmcnt(3)\n"
"ds_read2st64_b32 v[168:169], v14 offset0:174 offset1:175\n"
"v_cvt_pk_bf16_f32 v219, v170, v171\n"
"v_mfma_f32_16x16x32_bf16 v[172:175], v[140:143], v[136:139], 0\n"
"ds_read2st64_b32 v[170:171], v14 offset0:176 offset1:177\n"
"v_mul_f32 v148, v148, v244\n"
"v_mul_f32 v149, v149, v244\n"
"v_cvt_pk_bf16_f32 v148, v148, v149\n"
"s_waitcnt lgkmcnt(3)\n"
"v_mfma_f32_16x16x32_bf16 v[152:155], v[104:107], v[144:147], v[152:155]\n"
"ds_read_b128 v[136:139], v17 offset:55296\n"
"v_mul_f32 v150, v150, v244\n"
"v_mfma_f32_16x16x32_bf16 v[164:167], v[108:111], v[144:147], v[164:167]\n"
"s_waitcnt lgkmcnt(3)\n"
"ds_read2st64_b32 v[140:141], v14 offset0:198 offset1:199\n"
"v_mul_f32 v151, v151, v244\n"
"v_cvt_pk_bf16_f32 v149, v150, v151\n"
"v_mfma_f32_16x16x32_bf16 v[172:175], v[160:163], v[144:147], v[172:175]\n"
"ds_read2st64_b32 v[142:143], v14 offset0:200 offset1:201\n"
"v_mul_f32 v156, v156, v244\n"
"v_mul_f32 v157, v157, v244\n"
"v_cvt_pk_bf16_f32 v150, v156, v157\n"
"v_mul_f32 v158, v158, v244\n"
"s_waitcnt lgkmcnt(3)\n"
"v_mfma_f32_16x16x32_bf16 v[152:155], v[0:3], v[168:171], v[152:155]\n"
"ds_read_b128 v[144:147], v17 offset:56320\n"
"v_mul_f32 v159, v159, v244\n"
"v_mfma_f32_16x16x32_bf16 v[164:167], v[112:115], v[168:171], v[164:167]\n"
"s_waitcnt lgkmcnt(3)\n"
"ds_read2st64_b32 v[156:157], v14 offset0:130 offset1:131\n"
"v_cvt_pk_bf16_f32 v151, v158, v159\n"
"v_mfma_f32_16x16x32_bf16 v[172:175], v[136:139], v[168:171], v[172:175]\n"
"s_waitcnt lgkmcnt(2)\n"
"ds_read2st64_b32 v[158:159], v14 offset0:154 offset1:155\n"
"v_mfma_f32_16x16x32_bf16 v[152:155], v[116:119], v[140:143], v[152:155]\n"
"ds_read_b128 v[136:139], v17 offset:57344\n"
"v_mfma_f32_16x16x32_bf16 v[164:167], v[120:123], v[140:143], v[164:167]\n"
"s_waitcnt lgkmcnt(3)\n"
"ds_read2st64_b32 v[160:161], v14 offset0:178 offset1:179\n"
"v_mfma_f32_16x16x32_bf16 v[172:175], v[144:147], v[140:143], v[172:175]\n"
"s_waitcnt lgkmcnt(2)\n"
"ds_read2st64_b32 v[162:163], v14 offset0:202 offset1:203\n"
"v_mfma_f32_16x16x32_bf16 v[152:155], v[4:7], v[156:159], v[152:155]\n"
"ds_read_b128 v[140:143], v17 offset:58368\n"
"v_mfma_f32_16x16x32_bf16 v[164:167], v[124:127], v[156:159], v[164:167]\n"
"s_waitcnt lgkmcnt(3)\n"
"ds_read_b128 v[144:147], v12 offset:49152\n"
"v_mfma_f32_16x16x32_bf16 v[172:175], v[136:139], v[156:159], v[172:175]\n"
"s_waitcnt lgkmcnt(2)\n"
"v_mfma_f32_16x16x32_bf16 v[152:155], v[128:131], v[160:163], v[152:155]\n"
"v_mfma_f32_16x16x32_bf16 v[164:167], v[132:135], v[160:163], v[164:167]\n"
"s_waitcnt vmcnt(8)\n"
"s_barrier\n"
"ds_read_b128 v[136:139], v13 offset:49152\n"
"s_waitcnt lgkmcnt(2)\n"
"v_mfma_f32_16x16x32_bf16 v[172:175], v[140:143], v[160:163], v[172:175]\n"
"s_waitcnt lgkmcnt(1)\n"
"v_mfma_f32_16x16x32_bf16 v[140:143], v[144:147], v[28:31], 0\n"
"v_mfma_f32_16x16x32_bf16 v[156:159], v[144:147], v[32:35], 0\n"
"ds_read_b128 v[160:163], v12 offset:51200\n"
"v_mfma_f32_16x16x32_bf16 v[168:171], v[144:147], v[24:27], 0\n"
"v_cvt_pk_bf16_f32 v152, v152, v153\n"
"v_cvt_pk_bf16_f32 v153, v154, v155\n"
"v_cvt_pk_bf16_f32 v154, v164, v165\n"
"v_cvt_pk_bf16_f32 v155, v166, v167\n"
"s_waitcnt lgkmcnt(1)\n"
"v_mfma_f32_16x16x32_bf16 v[140:143], v[136:139], v[40:43], v[140:143]\n"
"s_mov_b32 m0, s54\n"
"v_cvt_pk_bf16_f32 v172, v172, v173\n"
"v_cvt_pk_bf16_f32 v173, v174, v175\n"
"v_mfma_f32_16x16x32_bf16 v[156:159], v[136:139], v[44:47], v[156:159]\n"
"ds_read_b128 v[144:147], v13 offset:51200\n"
"v_mfma_f32_16x16x32_bf16 v[168:171], v[136:139], v[36:39], v[168:171]\n"
"ds_write_addtid_b32 v218 offset:33280\n"
"s_waitcnt lgkmcnt(2)\n"
"v_mfma_f32_16x16x32_bf16 v[140:143], v[160:163], v[48:51], v[140:143]\n"
"ds_write_addtid_b32 v219 offset:33536\n"
"v_mfma_f32_16x16x32_bf16 v[156:159], v[160:163], v[52:55], v[156:159]\n"
"ds_write_addtid_b32 v148 offset:32256\n"
"v_mfma_f32_16x16x32_bf16 v[168:171], v[160:163], v[56:59], v[168:171]\n"
"ds_read_b128 v[136:139], v12 offset:53248\n"
"ds_write_addtid_b32 v149 offset:32512\n"
"s_waitcnt lgkmcnt(5)\n"
"v_mfma_f32_16x16x32_bf16 v[140:143], v[144:147], v[60:63], v[140:143]\n"
"ds_write_addtid_b32 v150 offset:32768\n"
"v_mfma_f32_16x16x32_bf16 v[156:159], v[144:147], v[64:67], v[156:159]\n"
"ds_write_addtid_b32 v151 offset:33024\n"
"s_mov_b32 m0, s68\n"
"ds_read_b128 v[148:151], v13 offset:53248\n"
"v_mfma_f32_16x16x32_bf16 v[168:171], v[144:147], v[68:71], v[168:171]\n"
"ds_write_addtid_b32 v152 offset:45056\n"
"ds_write_addtid_b32 v153 offset:45312\n"
"ds_write_addtid_b32 v154 offset:45568\n"
"s_waitcnt lgkmcnt(7)\n"
"v_mfma_f32_16x16x32_bf16 v[140:143], v[136:139], v[72:75], v[140:143]\n"
"ds_write_addtid_b32 v155 offset:45824\n"
"ds_write_addtid_b32 v172 offset:46080\n"
"v_mfma_f32_16x16x32_bf16 v[156:159], v[136:139], v[76:79], v[156:159]\n"
"ds_write_addtid_b32 v173 offset:46336\n"
"v_mov_b32 v144, 0x3f803f80\n"
"v_mfma_f32_16x16x32_bf16 v[168:171], v[136:139], v[80:83], v[168:171]\n"
"s_waitcnt lgkmcnt(6)\n"
"v_mov_b32 v145, 0x3f803f80\n"
"v_mov_b32 v146, 0x3f803f80\n"
"v_mfma_f32_16x16x32_bf16 v[140:143], v[148:151], v[84:87], v[140:143]\n"
"v_mov_b32 v147, 0x3f803f80\n"
"v_mfma_f32_16x16x32_bf16 v[156:159], v[148:151], v[88:91], v[156:159]\n"
"v_mfma_f32_16x16x32_bf16 v[168:171], v[148:151], v[92:95], v[168:171]\n"
"s_waitcnt lgkmcnt(0)\n"
"s_barrier\n"
"ds_read2st64_b32 v[136:137], v9 offset0:120 offset1:121\n"
"ds_read2st64_b32 v[138:139], v9 offset0:122 offset1:123\n"
"s_waitcnt lgkmcnt(0)\n"
"v_mfma_f32_16x16x32_bf16 v[148:151], v[144:147], v[136:139], 0\n"
"ds_read2st64_b32 v[152:153], v9 offset0:124 offset1:125\n"
"v_cvt_pk_bf16_f32 v192, v140, v141\n"
"v_mfma_f32_16x16x32_bf16 v[160:163], v[178:181], v[136:139], 0\n"
"ds_read2st64_b32 v[154:155], v9 offset0:126 offset1:127\n"
"v_cvt_pk_bf16_f32 v193, v142, v143\n"
"v_mfma_f32_16x16x32_bf16 v[140:143], v[204:207], v[136:139], 0\n"
"s_and_b32 s41, 416, s55\n"
"v_add_u32 v8, s41, v15\n"
"v_cvt_pk_bf16_f32 v218, v156, v157\n"
"v_mfma_f32_16x16x32_bf16 v[164:167], v[230:233], v[136:139], 0\n"
"ds_read_u16 v11, v21 offset:896\n"
"v_cvt_pk_bf16_f32 v219, v158, v159\n"
"v_cvt_pk_bf16_f32 v244, v168, v169\n"
"v_cvt_pk_bf16_f32 v245, v170, v171\n"
"ds_read2st64_b32 v[136:137], v20 offset0:0 offset1:1\n"
"ds_read2st64_b32 v[138:139], v20 offset0:2 offset1:3\n"
"ds_read_u16 v8, v8 offset:672\n"
"s_waitcnt lgkmcnt(4)\n"
"v_mfma_f32_16x16x32_bf16 v[148:151], v[144:147], v[152:155], v[148:151]\n"
"ds_read_b32 v156, v9 offset:32768\n"
"v_mfma_f32_16x16x32_bf16 v[160:163], v[188:191], v[152:155], v[160:163]\n"
"ds_read2st64_b32 v[158:159], v9 offset0:130 offset1:131\n"
"v_mov_b32 v157, 0\n"
"v_mfma_f32_16x16x32_bf16 v[140:143], v[214:217], v[152:155], v[140:143]\n"
"s_waitcnt lgkmcnt(5)\n"
"v_mad_u32_u24 v10, v11, s40, v18\n"
"v_mad_u32_u24 v11, v11, s40, v19\n"
"v_mfma_f32_16x16x32_bf16 v[164:167], v[240:243], v[152:155], v[164:167]\n"
"s_waitcnt lgkmcnt(3)\n"
"ds_read2st64_b32 v[174:175], v20 offset0:4 offset1:5\n"
"global_store_dwordx4 v10, v[136:139], s[66:67] sc1\n"
"s_waitcnt lgkmcnt(3)\n"
"v_mad_u32_u24 v8, v8, s40, v16\n"
"s_add_u32 m0, s51, 151552\n"
"s_nop 0\n"
"global_load_lds_dwordx4 v8, s[56:57]\n"
"s_waitcnt lgkmcnt(1)\n"
"v_mfma_f32_16x16x32_bf16 v[148:151], v[144:147], v[156:159], v[148:151]\n"
"ds_read2st64_b32 v[136:137], v9 offset0:132 offset1:133\n"
"v_mfma_f32_16x16x32_bf16 v[160:163], v[192:195], v[156:159], v[160:163]\n"
"ds_read_b32 v138, v9 offset:34304\n"
"v_mov_b32 v139, 0\n"
"v_mfma_f32_16x16x32_bf16 v[140:143], v[218:221], v[156:159], v[140:143]\n"
"ds_read_b32 v154, v9 offset:9728\n"
"v_mov_b32 v155, 0\n"
"v_mfma_f32_16x16x32_bf16 v[164:167], v[244:247], v[156:159], v[164:167]\n"
"s_waitcnt lgkmcnt(3)\n"
"global_store_dwordx2 v11, v[174:175], s[66:67] offset:256 sc1\n"
"v_mov_b32 v152, 0\n"
"v_mov_b32 v153, 0\n"
"s_add_u32 m0, s51, 153600\n"
"s_nop 0\n"
"global_load_lds_dwordx4 v8, s[58:59]\n"
"s_waitcnt lgkmcnt(1)\n"
"v_mfma_f32_16x16x32_bf16 v[148:151], v[144:147], v[136:139], v[148:151]\n"
"s_waitcnt lgkmcnt(0)\n"
"v_mfma_f32_16x16x32_bf16 v[148:151], v[144:147], v[152:155], v[148:151]\n"
"v_mfma_f32_16x16x32_bf16 v[164:167], v[248:251], v[136:139], v[164:167]\n"
"v_mfma_f32_16x16x32_bf16 v[164:167], v[232:235], v[152:155], v[164:167]\n"
"v_mfma_f32_16x16x32_bf16 v[160:163], v[196:199], v[136:139], v[160:163]\n"
"ds_read2st64_b32 v[144:145], v14 offset0:126 offset1:127\n"
"v_mfma_f32_16x16x32_bf16 v[160:163], v[180:183], v[152:155], v[160:163]\n"
"s_add_u32 m0, s51, 155648\n"
"s_nop 0\n"
"global_load_lds_dwordx4 v8, s[60:61]\n"
"ds_read2st64_b32 v[146:147], v14 offset0:128 offset1:129\n"
"v_rcp_f32 v8, v148\n"
"v_mfma_f32_16x16x32_bf16 v[140:143], v[222:225], v[136:139], v[140:143]\n"
"ds_read_b128 v[136:139], v17 offset:53248\n"
"v_mul_f32 v164, v164, v8\n"
"v_mfma_f32_16x16x32_bf16 v[140:143], v[206:209], v[152:155], v[140:143]\n"
"ds_read2st64_b32 v[148:149], v14 offset0:150 offset1:151\n"
"v_mul_f32 v165, v165, v8\n"
"v_cvt_pk_bf16_f32 v246, v164, v165\n"
"v_mul_f32 v166, v166, v8\n"
"v_mul_f32 v167, v167, v8\n"
"v_cvt_pk_bf16_f32 v247, v166, v167\n"
"v_mul_f32 v160, v160, v8\n"
"v_mul_f32 v161, v161, v8\n"
"v_cvt_pk_bf16_f32 v160, v160, v161\n"
"v_mul_f32 v162, v162, v8\n"
"v_mul_f32 v163, v163, v8\n"
"s_waitcnt lgkmcnt(2)\n"
"v_mfma_f32_16x16x32_bf16 v[152:155], v[96:99], v[144:147], 0\n"
"ds_read2st64_b32 v[150:151], v14 offset0:152 offset1:153\n"
"v_cvt_pk_bf16_f32 v161, v162, v163\n"
"v_mfma_f32_16x16x32_bf16 v[156:159], v[100:103], v[144:147], 0\n"
"s_waitcnt lgkmcnt(2)\n"
"ds_read_b128 v[164:167], v17 offset:54272\n"
"v_mul_f32 v140, v140, v8\n"
"v_mfma_f32_16x16x32_bf16 v[168:171], v[136:139], v[144:147], 0\n"
"ds_read2st64_b32 v[136:137], v14 offset0:174 offset1:175\n"
"v_mul_f32 v141, v141, v8\n"
"v_cvt_pk_bf16_f32 v162, v140, v141\n"
"v_mul_f32 v142, v142, v8\n"
"v_mul_f32 v143, v143, v8\n"
"v_cvt_pk_bf16_f32 v163, v142, v143\n"
"ds_read2st64_b32 v[138:139], v14 offset0:176 offset1:177\n"
"s_waitcnt lgkmcnt(3)\n"
"v_mfma_f32_16x16x32_bf16 v[152:155], v[104:107], v[148:151], v[152:155]\n"
"v_mfma_f32_16x16x32_bf16 v[156:159], v[108:111], v[148:151], v[156:159]\n"
"s_waitcnt lgkmcnt(2)\n"
"ds_read_b128 v[140:143], v17 offset:55296\n"
"v_mfma_f32_16x16x32_bf16 v[168:171], v[164:167], v[148:151], v[168:171]\n"
"s_waitcnt lgkmcnt(1)\n"
"ds_read2st64_b32 v[144:145], v14 offset0:198 offset1:199\n"
"ds_read2st64_b32 v[146:147], v14 offset0:200 offset1:201\n"
"v_mfma_f32_16x16x32_bf16 v[152:155], v[0:3], v[136:139], v[152:155]\n"
"v_mfma_f32_16x16x32_bf16 v[156:159], v[112:115], v[136:139], v[156:159]\n"
"s_waitcnt lgkmcnt(2)\n"
"ds_read_b128 v[148:151], v17 offset:56320\n"
"v_mfma_f32_16x16x32_bf16 v[168:171], v[140:143], v[136:139], v[168:171]\n"
"s_waitcnt lgkmcnt(1)\n"
"ds_read2st64_b32 v[136:137], v14 offset0:130 offset1:131\n"
"ds_read2st64_b32 v[138:139], v14 offset0:154 offset1:155\n"
"v_mfma_f32_16x16x32_bf16 v[152:155], v[116:119], v[144:147], v[152:155]\n"
"v_mfma_f32_16x16x32_bf16 v[156:159], v[120:123], v[144:147], v[156:159]\n"
"s_waitcnt lgkmcnt(2)\n"
"ds_read_b128 v[140:143], v17 offset:57344\n"
"v_mfma_f32_16x16x32_bf16 v[168:171], v[148:151], v[144:147], v[168:171]\n"
"s_waitcnt lgkmcnt(1)\n"
"ds_read2st64_b32 v[144:145], v14 offset0:178 offset1:179\n"
"ds_read2st64_b32 v[146:147], v14 offset0:202 offset1:203\n"
"v_mfma_f32_16x16x32_bf16 v[152:155], v[4:7], v[136:139], v[152:155]\n"
"v_mfma_f32_16x16x32_bf16 v[156:159], v[124:127], v[136:139], v[156:159]\n"
"s_waitcnt lgkmcnt(2)\n"
"ds_read_b128 v[148:151], v17 offset:58368\n"
"v_mfma_f32_16x16x32_bf16 v[168:171], v[140:143], v[136:139], v[168:171]\n"
"s_waitcnt lgkmcnt(1)\n"
"ds_read_b128 v[136:139], v12 offset:12288\n"
"v_mfma_f32_16x16x32_bf16 v[152:155], v[128:131], v[144:147], v[152:155]\n"
"v_mfma_f32_16x16x32_bf16 v[156:159], v[132:135], v[144:147], v[156:159]\n"
"s_waitcnt vmcnt(10)\n"
"s_barrier\n"
"s_waitcnt lgkmcnt(1)\n"
"v_mfma_f32_16x16x32_bf16 v[168:171], v[148:151], v[144:147], v[168:171]\n"
"s_waitcnt lgkmcnt(0)\n"
"ds_read_b128 v[140:143], v13 offset:12288\n"
"v_mfma_f32_16x16x32_bf16 v[144:147], v[136:139], v[28:31], 0\n"
"v_mfma_f32_16x16x32_bf16 v[148:151], v[136:139], v[32:35], 0\n"
"v_mfma_f32_16x16x32_bf16 v[164:167], v[136:139], v[24:27], 0\n"
"ds_read_b128 v[136:139], v12 offset:14336\n"
"v_cvt_pk_bf16_f32 v152, v152, v153\n"
"v_cvt_pk_bf16_f32 v153, v154, v155\n"
"v_cvt_pk_bf16_f32 v154, v156, v157\n"
"v_cvt_pk_bf16_f32 v168, v168, v169\n"
"v_cvt_pk_bf16_f32 v155, v158, v159\n"
"v_cvt_pk_bf16_f32 v169, v170, v171\n"
"s_waitcnt lgkmcnt(1)\n"
"v_mfma_f32_16x16x32_bf16 v[144:147], v[140:143], v[40:43], v[144:147]\n"
"s_mov_b32 m0, s54\n"
"v_mfma_f32_16x16x32_bf16 v[148:151], v[140:143], v[44:47], v[148:151]\n"
"ds_write_addtid_b32 v246 offset:33280\n"
"v_mfma_f32_16x16x32_bf16 v[164:167], v[140:143], v[36:39], v[164:167]\n"
"s_waitcnt lgkmcnt(1)\n"
"ds_read_b128 v[140:143], v13 offset:14336\n"
"v_mfma_f32_16x16x32_bf16 v[144:147], v[136:139], v[48:51], v[144:147]\n"
"ds_write_addtid_b32 v247 offset:33536\n"
"v_mfma_f32_16x16x32_bf16 v[148:151], v[136:139], v[52:55], v[148:151]\n"
"ds_write_addtid_b32 v160 offset:32256\n"
"v_mfma_f32_16x16x32_bf16 v[164:167], v[136:139], v[56:59], v[164:167]\n"
"ds_write_addtid_b32 v161 offset:32512\n"
"ds_read_b128 v[136:139], v12 offset:16384\n"
"ds_write_addtid_b32 v162 offset:32768\n"
"ds_write_addtid_b32 v163 offset:33024\n"
"s_waitcnt lgkmcnt(6)\n"
"v_mfma_f32_16x16x32_bf16 v[144:147], v[140:143], v[60:63], v[144:147]\n"
"s_mov_b32 m0, s68\n"
"v_mfma_f32_16x16x32_bf16 v[148:151], v[140:143], v[64:67], v[148:151]\n"
"ds_write_addtid_b32 v152 offset:45056\n"
"v_mfma_f32_16x16x32_bf16 v[164:167], v[140:143], v[68:71], v[164:167]\n"
"ds_read_b128 v[140:143], v13 offset:16384\n"
"ds_write_addtid_b32 v153 offset:45312\n"
"s_waitcnt lgkmcnt(5)\n"
"v_mfma_f32_16x16x32_bf16 v[144:147], v[136:139], v[72:75], v[144:147]\n"
"ds_write_addtid_b32 v154 offset:45568\n"
"v_mfma_f32_16x16x32_bf16 v[148:151], v[136:139], v[76:79], v[148:151]\n"
"ds_write_addtid_b32 v155 offset:45824\n"
"ds_write_addtid_b32 v168 offset:46080\n"
"v_mfma_f32_16x16x32_bf16 v[164:167], v[136:139], v[80:83], v[164:167]\n"
"ds_write_addtid_b32 v169 offset:46336\n"
"v_mov_b32 v136, 0x3f803f80\n"
"v_mov_b32 v137, 0x3f803f80\n"
"v_mov_b32 v138, 0x3f803f80\n"
"v_mov_b32 v139, 0x3f803f80\n"
"s_waitcnt lgkmcnt(5)\n"
"v_mfma_f32_16x16x32_bf16 v[144:147], v[140:143], v[84:87], v[144:147]\n"
"v_mfma_f32_16x16x32_bf16 v[148:151], v[140:143], v[88:91], v[148:151]\n"
"v_mfma_f32_16x16x32_bf16 v[164:167], v[140:143], v[92:95], v[164:167]\n"
"s_waitcnt lgkmcnt(0)\n"
"s_barrier\n"
"ds_read2st64_b32 v[140:141], v9 offset0:112 offset1:113\n"
"ds_read2st64_b32 v[142:143], v9 offset0:114 offset1:115\n"
"s_waitcnt lgkmcnt(0)\n"
"v_mfma_f32_16x16x32_bf16 v[152:155], v[136:139], v[140:143], 0\n"
"ds_read2st64_b32 v[156:157], v9 offset0:116 offset1:117\n"
"v_mfma_f32_16x16x32_bf16 v[160:163], v[180:183], v[140:143], 0\n"
"ds_read2st64_b32 v[158:159], v9 offset0:118 offset1:119\n"
"v_cvt_pk_bf16_f32 v194, v144, v145\n"
"v_mfma_f32_16x16x32_bf16 v[168:171], v[206:209], v[140:143], 0\n"
"s_and_b32 s41, 416, s55\n"
"v_add_u32 v8, s41, v15\n"
"v_cvt_pk_bf16_f32 v195, v146, v147\n"
"v_mfma_f32_16x16x32_bf16 v[144:147], v[232:235], v[140:143], 0\n"
"ds_read_u16 v11, v21 offset:928\n"
"v_cvt_pk_bf16_f32 v220, v148, v149\n"
"v_cvt_pk_bf16_f32 v221, v150, v151\n"
"v_cvt_pk_bf16_f32 v246, v164, v165\n"
"v_cvt_pk_bf16_f32 v247, v166, v167\n"
"ds_read2st64_b32 v[140:141], v20 offset0:0 offset1:1\n"
"ds_read2st64_b32 v[142:143], v20 offset0:2 offset1:3\n"
"ds_read_u16 v8, v8 offset:704\n"
"s_waitcnt lgkmcnt(4)\n"
"v_mfma_f32_16x16x32_bf16 v[152:155], v[136:139], v[156:159], v[152:155]\n"
"ds_read2st64_b32 v[148:149], v9 offset0:128 offset1:129\n"
"v_mfma_f32_16x16x32_bf16 v[160:163], v[190:193], v[156:159], v[160:163]\n"
"ds_read2st64_b32 v[150:151], v9 offset0:130 offset1:131\n"
"v_mfma_f32_16x16x32_bf16 v[168:171], v[216:219], v[156:159], v[168:171]\n"
"s_waitcnt lgkmcnt(5)\n"
"v_mad_u32_u24 v10, v11, s40, v18\n"
"v_mad_u32_u24 v11, v11, s40, v19\n"
"v_mfma_f32_16x16x32_bf16 v[144:147], v[242:245], v[156:159], v[144:147]\n"
"s_waitcnt lgkmcnt(3)\n"
"ds_read2st64_b32 v[174:175], v20 offset0:4 offset1:5\n"
"global_store_dwordx4 v10, v[140:143], s[66:67] sc1\n"
"s_waitcnt lgkmcnt(3)\n"
"v_mad_u32_u24 v8, v8, s40, v16\n"
"s_waitcnt lgkmcnt(1)\n"
"v_mfma_f32_16x16x32_bf16 v[152:155], v[136:139], v[148:151], v[152:155]\n"
"s_add_u32 m0, s51, 114688\n"
"s_nop 0\n"
"global_load_lds_dwordx4 v8, s[56:57]\n"
"ds_read2st64_b32 v[140:141], v9 offset0:132 offset1:133\n"
"v_mfma_f32_16x16x32_bf16 v[160:163], v[194:197], v[148:151], v[160:163]\n"
"ds_read2st64_b32 v[142:143], v9 offset0:134 offset1:135\n"
"v_mfma_f32_16x16x32_bf16 v[168:171], v[220:223], v[148:151], v[168:171]\n"
"ds_read2st64_b32 v[158:159], v9 offset0:36 offset1:37\n"
"v_mov_b32 v156, 0\n"
"v_mfma_f32_16x16x32_bf16 v[144:147], v[246:249], v[148:151], v[144:147]\n"
"s_waitcnt lgkmcnt(3)\n"
"global_store_dwordx2 v11, v[174:175], s[66:67] offset:256 sc1\n"
"v_mov_b32 v157, 0\n"
"s_waitcnt lgkmcnt(1)\n"
"v_mfma_f32_16x16x32_bf16 v[152:155], v[136:139], v[140:143], v[152:155]\n"
"s_add_u32 m0, s51, 116736\n"
"s_nop 0\n"
"global_load_lds_dwordx4 v8, s[58:59]\n"
"s_waitcnt lgkmcnt(0)\n"
"v_mfma_f32_16x16x32_bf16 v[152:155], v[136:139], v[156:159], v[152:155]\n"
"v_mfma_f32_16x16x32_bf16 v[144:147], v[250:253], v[140:143], v[144:147]\n"
"v_mfma_f32_16x16x32_bf16 v[144:147], v[234:237], v[156:159], v[144:147]\n"
"v_mfma_f32_16x16x32_bf16 v[160:163], v[198:201], v[140:143], v[160:163]\n"
"ds_read2st64_b32 v[136:137], v14 offset0:126 offset1:127\n"
"v_mfma_f32_16x16x32_bf16 v[160:163], v[182:185], v[156:159], v[160:163]\n"
"ds_read2st64_b32 v[138:139], v14 offset0:128 offset1:129\n"
"v_mfma_f32_16x16x32_bf16 v[168:171], v[224:227], v[140:143], v[168:171]\n"
"s_add_u32 m0, s51, 118784\n"
"s_nop 0\n"
"global_load_lds_dwordx4 v8, s[60:61]\n"
"v_rcp_f32 v8, v152\n"
"v_mfma_f32_16x16x32_bf16 v[168:171], v[208:211], v[156:159], v[168:171]\n"
"ds_read_b128 v[140:143], v17 offset:53248\n"
"v_mul_f32 v144, v144, v8\n"
"v_mul_f32 v145, v145, v8\n"
"v_cvt_pk_bf16_f32 v248, v144, v145\n"
"v_mul_f32 v146, v146, v8\n"
"v_mul_f32 v147, v147, v8\n"
"v_cvt_pk_bf16_f32 v249, v146, v147\n"
"v_mul_f32 v160, v160, v8\n"
"v_mul_f32 v161, v161, v8\n"
"v_cvt_pk_bf16_f32 v160, v160, v161\n"
"s_waitcnt lgkmcnt(1)\n"
"v_mfma_f32_16x16x32_bf16 v[144:147], v[96:99], v[136:139], 0\n"
"ds_read2st64_b32 v[148:149], v14 offset0:150 offset1:151\n"
"v_mul_f32 v162, v162, v8\n"
"v_mfma_f32_16x16x32_bf16 v[152:155], v[100:103], v[136:139], 0\n"
"ds_read2st64_b32 v[150:151], v14 offset0:152 offset1:153\n"
"v_mul_f32 v163, v163, v8\n"
"v_cvt_pk_bf16_f32 v161, v162, v163\n"
"v_mul_f32 v168, v168, v8\n"
"s_waitcnt lgkmcnt(2)\n"
"v_mfma_f32_16x16x32_bf16 v[156:159], v[140:143], v[136:139], 0\n"
"ds_read_b128 v[136:139], v17 offset:54272\n"
"v_mul_f32 v169, v169, v8\n"
"v_cvt_pk_bf16_f32 v162, v168, v169\n"
"v_mul_f32 v170, v170, v8\n"
"v_mul_f32 v171, v171, v8\n"
"v_cvt_pk_bf16_f32 v163, v170, v171\n"
"ds_read2st64_b32 v[140:141], v14 offset0:174 offset1:175\n"
"ds_read2st64_b32 v[142:143], v14 offset0:176 offset1:177\n"
"s_waitcnt lgkmcnt(3)\n"
"v_mfma_f32_16x16x32_bf16 v[144:147], v[104:107], v[148:151], v[144:147]\n"
"v_mfma_f32_16x16x32_bf16 v[152:155], v[108:111], v[148:151], v[152:155]\n"
"s_waitcnt lgkmcnt(2)\n"
"ds_read_b128 v[164:167], v17 offset:55296\n"
"v_mfma_f32_16x16x32_bf16 v[156:159], v[136:139], v[148:151], v[156:159]\n"
"s_waitcnt lgkmcnt(1)\n"
"ds_read2st64_b32 v[136:137], v14 offset0:198 offset1:199\n"
"ds_read2st64_b32 v[138:139], v14 offset0:200 offset1:201\n"
"v_mfma_f32_16x16x32_bf16 v[144:147], v[0:3], v[140:143], v[144:147]\n"
"v_mfma_f32_16x16x32_bf16 v[152:155], v[112:115], v[140:143], v[152:155]\n"
"s_waitcnt lgkmcnt(2)\n"
"ds_read_b128 v[148:151], v17 offset:56320\n"
"v_mfma_f32_16x16x32_bf16 v[156:159], v[164:167], v[140:143], v[156:159]\n"
"s_waitcnt lgkmcnt(1)\n"
"ds_read2st64_b32 v[140:141], v14 offset0:130 offset1:131\n"
"ds_read2st64_b32 v[142:143], v14 offset0:154 offset1:155\n"
"v_mfma_f32_16x16x32_bf16 v[144:147], v[116:119], v[136:139], v[144:147]\n"
"v_mfma_f32_16x16x32_bf16 v[152:155], v[120:123], v[136:139], v[152:155]\n"
"s_waitcnt lgkmcnt(2)\n"
"ds_read_b128 v[164:167], v17 offset:57344\n"
"v_mfma_f32_16x16x32_bf16 v[156:159], v[148:151], v[136:139], v[156:159]\n"
"s_waitcnt lgkmcnt(1)\n"
"ds_read2st64_b32 v[136:137], v14 offset0:178 offset1:179\n"
"ds_read2st64_b32 v[138:139], v14 offset0:202 offset1:203\n"
"v_mfma_f32_16x16x32_bf16 v[144:147], v[4:7], v[140:143], v[144:147]\n"
"v_mfma_f32_16x16x32_bf16 v[152:155], v[124:127], v[140:143], v[152:155]\n"
"s_waitcnt lgkmcnt(2)\n"
"ds_read_b128 v[148:151], v17 offset:58368\n"
"v_mfma_f32_16x16x32_bf16 v[156:159], v[164:167], v[140:143], v[156:159]\n"
"s_waitcnt lgkmcnt(1)\n"
"ds_read_b128 v[140:143], v12 offset:36864\n"
"v_mfma_f32_16x16x32_bf16 v[144:147], v[128:131], v[136:139], v[144:147]\n"
"v_mfma_f32_16x16x32_bf16 v[152:155], v[132:135], v[136:139], v[152:155]\n"
"s_waitcnt vmcnt(10)\n"
"s_barrier\n"
"s_waitcnt lgkmcnt(1)\n"
"v_mfma_f32_16x16x32_bf16 v[156:159], v[148:151], v[136:139], v[156:159]\n"
"s_waitcnt lgkmcnt(0)\n"
"ds_read_b128 v[136:139], v13 offset:36864\n"
"v_mfma_f32_16x16x32_bf16 v[148:151], v[140:143], v[28:31], 0\n"
"v_mfma_f32_16x16x32_bf16 v[164:167], v[140:143], v[32:35], 0\n"
"v_mfma_f32_16x16x32_bf16 v[168:171], v[140:143], v[24:27], 0\n"
"ds_read_b128 v[140:143], v12 offset:38912\n"
"v_cvt_pk_bf16_f32 v144, v144, v145\n"
"v_cvt_pk_bf16_f32 v145, v146, v147\n"
"v_cvt_pk_bf16_f32 v146, v152, v153\n"
"v_cvt_pk_bf16_f32 v156, v156, v157\n"
"v_cvt_pk_bf16_f32 v147, v154, v155\n"
"v_cvt_pk_bf16_f32 v157, v158, v159\n"
"s_waitcnt lgkmcnt(1)\n"
"v_mfma_f32_16x16x32_bf16 v[148:151], v[136:139], v[40:43], v[148:151]\n"
"s_mov_b32 m0, s54\n"
"v_mfma_f32_16x16x32_bf16 v[164:167], v[136:139], v[44:47], v[164:167]\n"
"ds_write_addtid_b32 v248 offset:33280\n"
"v_mfma_f32_16x16x32_bf16 v[168:171], v[136:139], v[36:39], v[168:171]\n"
"s_waitcnt lgkmcnt(1)\n"
"ds_read_b128 v[136:139], v13 offset:38912\n"
"v_mfma_f32_16x16x32_bf16 v[148:151], v[140:143], v[48:51], v[148:151]\n"
"ds_write_addtid_b32 v249 offset:33536\n"
"v_mfma_f32_16x16x32_bf16 v[164:167], v[140:143], v[52:55], v[164:167]\n"
"ds_write_addtid_b32 v160 offset:32256\n"
"v_mfma_f32_16x16x32_bf16 v[168:171], v[140:143], v[56:59], v[168:171]\n"
"ds_write_addtid_b32 v161 offset:32512\n"
"ds_read_b128 v[140:143], v12 offset:40960\n"
"ds_write_addtid_b32 v162 offset:32768\n"
"ds_write_addtid_b32 v163 offset:33024\n"
"s_waitcnt lgkmcnt(6)\n"
"v_mfma_f32_16x16x32_bf16 v[148:151], v[136:139], v[60:63], v[148:151]\n"
"s_mov_b32 m0, s68\n"
"v_mfma_f32_16x16x32_bf16 v[164:167], v[136:139], v[64:67], v[164:167]\n"
"ds_write_addtid_b32 v144 offset:45056\n"
"v_mfma_f32_16x16x32_bf16 v[168:171], v[136:139], v[68:71], v[168:171]\n"
"ds_read_b128 v[136:139], v13 offset:40960\n"
"ds_write_addtid_b32 v145 offset:45312\n"
"s_waitcnt lgkmcnt(5)\n"
"v_mfma_f32_16x16x32_bf16 v[148:151], v[140:143], v[72:75], v[148:151]\n"
"ds_write_addtid_b32 v146 offset:45568\n"
"v_mfma_f32_16x16x32_bf16 v[164:167], v[140:143], v[76:79], v[164:167]\n"
"ds_write_addtid_b32 v147 offset:45824\n"
"ds_write_addtid_b32 v156 offset:46080\n"
"v_mfma_f32_16x16x32_bf16 v[168:171], v[140:143], v[80:83], v[168:171]\n"
"ds_write_addtid_b32 v157 offset:46336\n"
"v_mov_b32 v140, 0x3f803f80\n"
"v_mov_b32 v141, 0x3f803f80\n"
"v_mov_b32 v142, 0x3f803f80\n"
"v_mov_b32 v143, 0x3f803f80\n"
"s_waitcnt lgkmcnt(5)\n"
"v_mfma_f32_16x16x32_bf16 v[148:151], v[136:139], v[84:87], v[148:151]\n"
"v_mfma_f32_16x16x32_bf16 v[164:167], v[136:139], v[88:91], v[164:167]\n"
"v_mfma_f32_16x16x32_bf16 v[168:171], v[136:139], v[92:95], v[168:171]\n"
"s_waitcnt vmcnt(5)\n"
"s_waitcnt lgkmcnt(0)\n"
"s_barrier\n"
"ds_read2st64_b32 v[136:137], v9 offset0:120 offset1:121\n"
"ds_read2st64_b32 v[138:139], v9 offset0:122 offset1:123\n"
"s_waitcnt lgkmcnt(0)\n"
"v_mfma_f32_16x16x32_bf16 v[144:147], v[140:143], v[136:139], 0\n"
"ds_read_b32 v153, v9 offset:32000\n"
"v_mfma_f32_16x16x32_bf16 v[156:159], v[200:203], v[136:139], 0\n"
"ds_read2st64_b32 v[154:155], v9 offset0:126 offset1:127\n"
"v_cvt_pk_bf16_f32 v196, v148, v149\n"
"v_mfma_f32_16x16x32_bf16 v[200:203], v[226:229], v[136:139], 0\n"
"s_and_b32 s41, 416, s55\n"
"v_add_u32 v8, s41, v15\n"
"v_cvt_pk_bf16_f32 v197, v150, v151\n"
"v_mfma_f32_16x16x32_bf16 v[226:229], v[252:255], v[136:139], 0\n"
"ds_read_u16 v11, v21 offset:960\n"
"v_mov_b32 v152, 0\n"
"v_cvt_pk_bf16_f32 v222, v164, v165\n"
"v_cvt_pk_bf16_f32 v223, v166, v167\n"
"v_cvt_pk_bf16_f32 v248, v168, v169\n"
"v_cvt_pk_bf16_f32 v249, v170, v171\n"
"ds_read2st64_b32 v[252:253], v20 offset0:0 offset1:1\n"
"ds_read2st64_b32 v[254:255], v20 offset0:2 offset1:3\n"
"s_waitcnt lgkmcnt(3)\n"
"v_mfma_f32_16x16x32_bf16 v[144:147], v[140:143], v[152:155], v[144:147]\n"
"ds_read_b32 v137, v9 offset:33024\n"
"v_mfma_f32_16x16x32_bf16 v[156:159], v[182:185], v[152:155], v[156:159]\n"
"ds_read2st64_b32 v[138:139], v9 offset0:130 offset1:131\n"
"v_mov_b32 v136, 0\n"
"v_mfma_f32_16x16x32_bf16 v[200:203], v[208:211], v[152:155], v[200:203]\n"
"s_waitcnt lgkmcnt(4)\n"
"ds_read_u16 v8, v8 offset:736\n"
"v_mad_u32_u24 v10, v11, s40, v18\n"
"v_mfma_f32_16x16x32_bf16 v[226:229], v[234:237], v[152:155], v[226:229]\n"
"s_waitcnt lgkmcnt(3)\n"
"ds_read2st64_b32 v[174:175], v20 offset0:4 offset1:5\n"
"v_mad_u32_u24 v11, v11, s40, v19\n"
"global_store_dwordx4 v10, v[252:255], s[66:67] sc1\n"
"s_waitcnt lgkmcnt(2)\n"
"v_mfma_f32_16x16x32_bf16 v[144:147], v[140:143], v[136:139], v[144:147]\n"
"s_waitcnt lgkmcnt(1)\n"
"ds_read2st64_b32 v[252:253], v9 offset0:132 offset1:133\n"
"v_mad_u32_u24 v8, v8, s40, v16\n"
"v_mfma_f32_16x16x32_bf16 v[156:159], v[192:195], v[136:139], v[156:159]\n"
"s_add_u32 m0, s51, 139264\n"
"s_nop 0\n"
"global_load_lds_dwordx4 v8, s[56:57]\n"
"ds_read_b32 v255, v9 offset:34560\n"
"v_mfma_f32_16x16x32_bf16 v[200:203], v[218:221], v[136:139], v[200:203]\n"
"ds_read2st64_b32 v[150:151], v9 offset0:38 offset1:39\n"
"v_mov_b32 v254, 0\n"
"v_mfma_f32_16x16x32_bf16 v[226:229], v[244:247], v[136:139], v[226:229]\n"
"s_waitcnt lgkmcnt(3)\n"
"global_store_dwordx2 v11, v[174:175], s[66:67] offset:256 sc1\n"
"v_mov_b32 v148, 0\n"
"v_mov_b32 v149, 0\n"
"s_waitcnt lgkmcnt(1)\n"
"v_mfma_f32_16x16x32_bf16 v[144:147], v[140:143], v[252:255], v[144:147]\n"
"s_add_u32 m0, s51, 141312\n"
"s_nop 0\n"
"global_load_lds_dwordx4 v8, s[58:59]\n"
"s_waitcnt lgkmcnt(0)\n"
"v_mfma_f32_16x16x32_bf16 v[144:147], v[140:143], v[148:151], v[144:147]\n"
"v_mfma_f32_16x16x32_bf16 v[226:229], v[248:251], v[252:255], v[226:229]\n"
"v_mfma_f32_16x16x32_bf16 v[226:229], v[236:239], v[148:151], v[226:229]\n"
"v_mfma_f32_16x16x32_bf16 v[156:159], v[196:199], v[252:255], v[156:159]\n"
"ds_read2st64_b32 v[136:137], v14 offset0:126 offset1:127\n"
"v_mfma_f32_16x16x32_bf16 v[156:159], v[184:187], v[148:151], v[156:159]\n"
"ds_read2st64_b32 v[138:139], v14 offset0:128 offset1:129\n"
"v_mfma_f32_16x16x32_bf16 v[200:203], v[222:225], v[252:255], v[200:203]\n"
"s_add_u32 m0, s51, 143360\n"
"s_nop 0\n"
"global_load_lds_dwordx4 v8, s[60:61]\n"
"v_rcp_f32 v8, v144\n"
"v_mfma_f32_16x16x32_bf16 v[200:203], v[210:213], v[148:151], v[200:203]\n"
"ds_read_b128 v[250:253], v17 offset:53248\n"
"v_mul_f32 v226, v226, v8\n"
"v_mul_f32 v227, v227, v8\n"
"v_cvt_pk_bf16_f32 v198, v226, v227\n"
"v_mul_f32 v228, v228, v8\n"
"v_mul_f32 v229, v229, v8\n"
"v_cvt_pk_bf16_f32 v199, v228, v229\n"
"v_mul_f32 v156, v156, v8\n"
"v_mul_f32 v157, v157, v8\n"
"v_cvt_pk_bf16_f32 v156, v156, v157\n"
"s_waitcnt lgkmcnt(1)\n"
"v_mfma_f32_16x16x32_bf16 v[224:227], v[96:99], v[136:139], 0\n"
"ds_read2st64_b32 v[140:141], v14 offset0:150 offset1:151\n"
"v_mul_f32 v158, v158, v8\n"
"v_mfma_f32_16x16x32_bf16 v[144:147], v[100:103], v[136:139], 0\n"
"ds_read2st64_b32 v[142:143], v14 offset0:152 offset1:153\n"
"v_mul_f32 v159, v159, v8\n"
"v_cvt_pk_bf16_f32 v157, v158, v159\n"
"v_mul_f32 v200, v200, v8\n"
"s_waitcnt lgkmcnt(2)\n"
"v_mfma_f32_16x16x32_bf16 v[148:151], v[250:253], v[136:139], 0\n"
"ds_read_b128 v[250:253], v17 offset:54272\n"
"v_mul_f32 v201, v201, v8\n"
"v_cvt_pk_bf16_f32 v158, v200, v201\n"
"v_mul_f32 v202, v202, v8\n"
"v_mul_f32 v203, v203, v8\n"
"v_cvt_pk_bf16_f32 v159, v202, v203\n"
"ds_read2st64_b32 v[200:201], v14 offset0:174 offset1:175\n"
"ds_read2st64_b32 v[202:203], v14 offset0:176 offset1:177\n"
"s_waitcnt lgkmcnt(3)\n"
"v_mfma_f32_16x16x32_bf16 v[224:227], v[104:107], v[140:143], v[224:227]\n"
"v_mfma_f32_16x16x32_bf16 v[144:147], v[108:111], v[140:143], v[144:147]\n"
"s_waitcnt lgkmcnt(2)\n"
"ds_read_b128 v[136:139], v17 offset:55296\n"
"v_mfma_f32_16x16x32_bf16 v[148:151], v[250:253], v[140:143], v[148:151]\n"
"s_waitcnt lgkmcnt(1)\n"
"ds_read2st64_b32 v[250:251], v14 offset0:198 offset1:199\n"
"ds_read2st64_b32 v[252:253], v14 offset0:200 offset1:201\n"
"v_mfma_f32_16x16x32_bf16 v[224:227], v[0:3], v[200:203], v[224:227]\n"
"v_mfma_f32_16x16x32_bf16 v[144:147], v[112:115], v[200:203], v[144:147]\n"
"s_waitcnt lgkmcnt(2)\n"
"ds_read_b128 v[140:143], v17 offset:56320\n"
"v_mfma_f32_16x16x32_bf16 v[148:151], v[136:139], v[200:203], v[148:151]\n"
"s_waitcnt lgkmcnt(1)\n"
"ds_read2st64_b32 v[200:201], v14 offset0:130 offset1:131\n"
"ds_read2st64_b32 v[202:203], v14 offset0:154 offset1:155\n"
"v_mfma_f32_16x16x32_bf16 v[224:227], v[116:119], v[250:253], v[224:227]\n"
"v_mfma_f32_16x16x32_bf16 v[144:147], v[120:123], v[250:253], v[144:147]\n"
"s_waitcnt lgkmcnt(2)\n"
"ds_read_b128 v[136:139], v17 offset:57344\n"
"v_mfma_f32_16x16x32_bf16 v[148:151], v[140:143], v[250:253], v[148:151]\n"
"s_waitcnt lgkmcnt(1)\n"
"ds_read2st64_b32 v[250:251], v14 offset0:178 offset1:179\n"
"ds_read2st64_b32 v[252:253], v14 offset0:202 offset1:203\n"
"v_mfma_f32_16x16x32_bf16 v[224:227], v[4:7], v[200:203], v[224:227]\n"
"v_mfma_f32_16x16x32_bf16 v[144:147], v[124:127], v[200:203], v[144:147]\n"
"s_waitcnt lgkmcnt(2)\n"
"ds_read_b128 v[140:143], v17 offset:58368\n"
"v_mfma_f32_16x16x32_bf16 v[148:151], v[136:139], v[200:203], v[148:151]\n"
"s_waitcnt lgkmcnt(1)\n"
"ds_read_b128 v[200:203], v12 offset:30720\n"
"v_mfma_f32_16x16x32_bf16 v[224:227], v[128:131], v[250:253], v[224:227]\n"
"v_mfma_f32_16x16x32_bf16 v[144:147], v[132:135], v[250:253], v[144:147]\n"
"s_barrier\n"
"s_waitcnt lgkmcnt(1)\n"
"v_mfma_f32_16x16x32_bf16 v[148:151], v[140:143], v[250:253], v[148:151]\n"
"s_waitcnt lgkmcnt(0)\n"
"ds_read_b128 v[250:253], v13 offset:30720\n"
"v_mfma_f32_16x16x32_bf16 v[136:139], v[200:203], v[28:31], 0\n"
"v_mfma_f32_16x16x32_bf16 v[140:143], v[200:203], v[32:35], 0\n"
"v_mfma_f32_16x16x32_bf16 v[152:155], v[200:203], v[24:27], 0\n"
"ds_read_b128 v[200:203], v12 offset:32768\n"
"v_cvt_pk_bf16_f32 v224, v224, v225\n"
"v_cvt_pk_bf16_f32 v225, v226, v227\n"
"v_cvt_pk_bf16_f32 v226, v144, v145\n"
"v_cvt_pk_bf16_f32 v227, v146, v147\n"
"v_cvt_pk_bf16_f32 v148, v148, v149\n"
"v_cvt_pk_bf16_f32 v149, v150, v151\n"
"s_waitcnt lgkmcnt(1)\n"
"v_mfma_f32_16x16x32_bf16 v[136:139], v[250:253], v[40:43], v[136:139]\n"
"s_mov_b32 m0, s54\n"
"v_mfma_f32_16x16x32_bf16 v[140:143], v[250:253], v[44:47], v[140:143]\n"
"ds_write_addtid_b32 v198 offset:33280\n"
"v_mfma_f32_16x16x32_bf16 v[152:155], v[250:253], v[36:39], v[152:155]\n"
"s_waitcnt lgkmcnt(1)\n"
"ds_read_b128 v[250:253], v13 offset:32768\n"
"v_mfma_f32_16x16x32_bf16 v[136:139], v[200:203], v[48:51], v[136:139]\n"
"ds_write_addtid_b32 v199 offset:33536\n"
"v_mfma_f32_16x16x32_bf16 v[140:143], v[200:203], v[52:55], v[140:143]\n"
"ds_write_addtid_b32 v156 offset:32256\n"
"v_mfma_f32_16x16x32_bf16 v[152:155], v[200:203], v[56:59], v[152:155]\n"
"ds_write_addtid_b32 v157 offset:32512\n"
"ds_read_b128 v[198:201], v12 offset:34816\n"
"ds_write_addtid_b32 v158 offset:32768\n"
"ds_write_addtid_b32 v159 offset:33024\n"
"s_waitcnt lgkmcnt(6)\n"
"v_mfma_f32_16x16x32_bf16 v[136:139], v[250:253], v[60:63], v[136:139]\n"
"s_mov_b32 m0, s68\n"
"v_mfma_f32_16x16x32_bf16 v[140:143], v[250:253], v[64:67], v[140:143]\n"
"ds_write_addtid_b32 v224 offset:45056\n"
"v_mfma_f32_16x16x32_bf16 v[152:155], v[250:253], v[68:71], v[152:155]\n"
"ds_read_b128 v[250:253], v13 offset:34816\n"
"ds_write_addtid_b32 v225 offset:45312\n"
"s_waitcnt lgkmcnt(5)\n"
"v_mfma_f32_16x16x32_bf16 v[136:139], v[198:201], v[72:75], v[136:139]\n"
"ds_write_addtid_b32 v226 offset:45568\n"
"v_mfma_f32_16x16x32_bf16 v[140:143], v[198:201], v[76:79], v[140:143]\n"
"ds_write_addtid_b32 v227 offset:45824\n"
"v_mfma_f32_16x16x32_bf16 v[152:155], v[198:201], v[80:83], v[152:155]\n"
"ds_read_b128 v[198:201], v12 offset:49152\n"
"ds_write_addtid_b32 v148 offset:46080\n"
"ds_write_addtid_b32 v149 offset:46336\n"
"s_waitcnt lgkmcnt(6)\n"
"v_mfma_f32_16x16x32_bf16 v[136:139], v[250:253], v[84:87], v[136:139]\n"
"v_mfma_f32_16x16x32_bf16 v[140:143], v[250:253], v[88:91], v[140:143]\n"
"v_mfma_f32_16x16x32_bf16 v[152:155], v[250:253], v[92:95], v[152:155]\n"
"s_waitcnt lgkmcnt(2)\n"
"ds_read_b128 v[226:229], v13 offset:49152\n"
"v_mfma_f32_16x16x32_bf16 v[252:255], v[198:201], v[28:31], 0\n"
"v_mfma_f32_16x16x32_bf16 v[144:147], v[198:201], v[32:35], 0\n"
"v_mfma_f32_16x16x32_bf16 v[148:151], v[198:201], v[24:27], 0\n"
"s_waitcnt lgkmcnt(0)\n"
"ds_read_b128 v[200:203], v12 offset:51200\n"
"v_mfma_f32_16x16x32_bf16 v[252:255], v[226:229], v[40:43], v[252:255]\n"
"v_cvt_pk_bf16_f32 v198, v136, v137\n"
"v_cvt_pk_bf16_f32 v199, v138, v139\n"
"v_cvt_pk_bf16_f32 v224, v140, v141\n"
"v_cvt_pk_bf16_f32 v225, v142, v143\n"
"v_mfma_f32_16x16x32_bf16 v[144:147], v[226:229], v[44:47], v[144:147]\n"
"v_cvt_pk_bf16_f32 v250, v152, v153\n"
"v_cvt_pk_bf16_f32 v251, v154, v155\n"
"v_mfma_f32_16x16x32_bf16 v[148:151], v[226:229], v[36:39], v[148:151]\n"
"s_waitcnt lgkmcnt(0)\n"
"ds_read_b128 v[226:229], v13 offset:51200\n"
"v_mfma_f32_16x16x32_bf16 v[252:255], v[200:203], v[48:51], v[252:255]\n"
"v_mfma_f32_16x16x32_bf16 v[144:147], v[200:203], v[52:55], v[144:147]\n"
"v_mfma_f32_16x16x32_bf16 v[148:151], v[200:203], v[56:59], v[148:151]\n"
"s_waitcnt lgkmcnt(0)\n"
"ds_read_b128 v[200:203], v12 offset:53248\n"
"v_mfma_f32_16x16x32_bf16 v[252:255], v[226:229], v[60:63], v[252:255]\n"
"v_mfma_f32_16x16x32_bf16 v[144:147], v[226:229], v[64:67], v[144:147]\n"
"v_mfma_f32_16x16x32_bf16 v[148:151], v[226:229], v[68:71], v[148:151]\n"
"s_waitcnt lgkmcnt(0)\n"
"ds_read_b128 v[226:229], v13 offset:53248\n"
"v_mfma_f32_16x16x32_bf16 v[252:255], v[200:203], v[72:75], v[252:255]\n"
"v_mfma_f32_16x16x32_bf16 v[144:147], v[200:203], v[76:79], v[144:147]\n"
"v_mfma_f32_16x16x32_bf16 v[148:151], v[200:203], v[80:83], v[148:151]\n"
"v_mov_b32 v136, 0x3f803f80\n"
"v_mov_b32 v137, 0x3f803f80\n"
"v_mov_b32 v138, 0x3f803f80\n"
"v_mov_b32 v139, 0x3f803f80\n"
"s_waitcnt lgkmcnt(0)\n"
"v_mfma_f32_16x16x32_bf16 v[252:255], v[226:229], v[84:87], v[252:255]\n"
"s_waitcnt vmcnt(5)\n"
"s_barrier\n"
"ds_read_b32 v141, v9 offset:28928\n"
"v_mov_b32 v140, 0\n"
"v_mfma_f32_16x16x32_bf16 v[144:147], v[226:229], v[88:91], v[144:147]\n"
"s_and_b32 s41, 416, s55\n"
"v_add_u32 v8, s41, v15\n"
"v_mfma_f32_16x16x32_bf16 v[148:151], v[226:229], v[92:95], v[148:151]\n"
"ds_read2st64_b32 v[142:143], v9 offset0:114 offset1:115\n"
"v_cvt_pk_bf16_f32 v200, v252, v253\n"
"v_cvt_pk_bf16_f32 v201, v254, v255\n"
"ds_read_u16 v8, v8 offset:768\n"
"s_waitcnt lgkmcnt(1)\n"
"v_mfma_f32_16x16x32_bf16 v[152:155], v[136:139], v[140:143], 0\n"
"ds_read2st64_b32 v[156:157], v9 offset0:116 offset1:117\n"
"v_cvt_pk_bf16_f32 v226, v144, v145\n"
"v_mfma_f32_16x16x32_bf16 v[160:163], v[180:183], v[140:143], 0\n"
"ds_read2st64_b32 v[158:159], v9 offset0:118 offset1:119\n"
"v_cvt_pk_bf16_f32 v227, v146, v147\n"
"v_mfma_f32_16x16x32_bf16 v[144:147], v[206:209], v[140:143], 0\n"
"ds_read_u16 v15, v21 offset:992\n"
"v_cvt_pk_bf16_f32 v252, v148, v149\n"
"v_mfma_f32_16x16x32_bf16 v[164:167], v[232:235], v[140:143], 0\n"
"s_waitcnt lgkmcnt(3)\n"
"ds_read2st64_b32 v[140:141], v20 offset0:0 offset1:1\n"
"v_cvt_pk_bf16_f32 v253, v150, v151\n"
"v_mad_u32_u24 v8, v8, s40, v16\n"
"s_add_u32 m0, s51, 126976\n"
"s_nop 0\n"
"global_load_lds_dwordx4 v8, s[56:57]\n"
"ds_read2st64_b32 v[142:143], v20 offset0:2 offset1:3\n"
"ds_read_b32 v149, v9 offset:33024\n"
"ds_read2st64_b32 v[150:151], v9 offset0:130 offset1:131\n"
"s_waitcnt lgkmcnt(5)\n"
"v_mfma_f32_16x16x32_bf16 v[152:155], v[136:139], v[156:159], v[152:155]\n"
"ds_read2st64_b32 v[168:169], v9 offset0:132 offset1:133\n"
"v_mov_b32 v148, 0\n"
"v_mfma_f32_16x16x32_bf16 v[160:163], v[184:187], v[156:159], v[160:163]\n"
"s_waitcnt lgkmcnt(5)\n"
"ds_read2st64_b32 v[170:171], v9 offset0:134 offset1:135\n"
"v_mad_u32_u24 v16, v15, s40, v18\n"
"v_mfma_f32_16x16x32_bf16 v[144:147], v[210:213], v[156:159], v[144:147]\n"
"ds_read2st64_b32 v[254:255], v20 offset0:4 offset1:5\n"
"v_mad_u32_u24 v15, v15, s40, v19\n"
"v_mfma_f32_16x16x32_bf16 v[164:167], v[236:239], v[156:159], v[164:167]\n"
"s_add_u32 m0, s51, 129024\n"
"s_nop 0\n"
"global_load_lds_dwordx4 v8, s[58:59]\n"
"s_waitcnt lgkmcnt(5)\n"
"global_store_dwordx4 v16, v[140:143], s[66:67] sc1\n"
"s_waitcnt lgkmcnt(3)\n"
"v_mfma_f32_16x16x32_bf16 v[152:155], v[136:139], v[148:151], v[152:155]\n"
"s_waitcnt lgkmcnt(1)\n"
"v_mfma_f32_16x16x32_bf16 v[152:155], v[136:139], v[168:171], v[152:155]\n"
"v_mfma_f32_16x16x32_bf16 v[164:167], v[242:245], v[148:151], v[164:167]\n"
"s_waitcnt lgkmcnt(0)\n"
"global_store_dwordx2 v15, v[254:255], s[66:67] offset:256 sc1\n"
"v_mfma_f32_16x16x32_bf16 v[164:167], v[246:249], v[168:171], v[164:167]\n"
"ds_read2st64_b32 v[136:137], v14 offset0:126 offset1:127\n"
"v_mfma_f32_16x16x32_bf16 v[160:163], v[190:193], v[148:151], v[160:163]\n"
"s_add_u32 m0, s51, 131072\n"
"s_nop 0\n"
"global_load_lds_dwordx4 v8, s[60:61]\n"
"ds_read2st64_b32 v[138:139], v14 offset0:128 offset1:129\n"
"v_rcp_f32 v16, v152\n"
"v_mfma_f32_16x16x32_bf16 v[160:163], v[194:197], v[168:171], v[160:163]\n"
"ds_read_b128 v[140:143], v17 offset:53248\n"
"v_mul_f32 v164, v164, v16\n"
"v_mfma_f32_16x16x32_bf16 v[144:147], v[216:219], v[148:151], v[144:147]\n"
"ds_read2st64_b32 v[148:149], v14 offset0:150 offset1:151\n"
"v_mul_f32 v165, v165, v16\n"
"v_mfma_f32_16x16x32_bf16 v[144:147], v[220:223], v[168:171], v[144:147]\n"
"ds_read2st64_b32 v[150:151], v14 offset0:152 offset1:153\n"
"v_cvt_pk_bf16_f32 v254, v164, v165\n"
"v_mul_f32 v166, v166, v16\n"
"v_mul_f32 v167, v167, v16\n"
"v_cvt_pk_bf16_f32 v255, v166, v167\n"
"v_mul_f32 v160, v160, v16\n"
"v_mul_f32 v161, v161, v16\n"
"s_waitcnt lgkmcnt(3)\n"
"v_mfma_f32_16x16x32_bf16 v[152:155], v[96:99], v[136:139], 0\n"
"ds_read_b128 v[156:159], v17 offset:54272\n"
"v_cvt_pk_bf16_f32 v160, v160, v161\n"
"v_mfma_f32_16x16x32_bf16 v[164:167], v[100:103], v[136:139], 0\n"
"s_waitcnt lgkmcnt(3)\n"
"ds_read2st64_b32 v[168:169], v14 offset0:174 offset1:175\n"
"v_mul_f32 v162, v162, v16\n"
"v_mfma_f32_16x16x32_bf16 v[172:175], v[140:143], v[136:139], 0\n"
"s_waitcnt lgkmcnt(2)\n"
"ds_read2st64_b32 v[170:171], v14 offset0:176 offset1:177\n"
"v_mul_f32 v163, v163, v16\n"
"v_cvt_pk_bf16_f32 v161, v162, v163\n"
"v_mfma_f32_16x16x32_bf16 v[152:155], v[104:107], v[148:151], v[152:155]\n"
"ds_read_b128 v[136:139], v17 offset:55296\n"
"v_mul_f32 v144, v144, v16\n"
"v_mfma_f32_16x16x32_bf16 v[164:167], v[108:111], v[148:151], v[164:167]\n"
"s_waitcnt lgkmcnt(3)\n"
"ds_read2st64_b32 v[140:141], v14 offset0:198 offset1:199\n"
"v_mul_f32 v145, v145, v16\n"
"v_cvt_pk_bf16_f32 v162, v144, v145\n"
"v_mfma_f32_16x16x32_bf16 v[172:175], v[156:159], v[148:151], v[172:175]\n"
"ds_read2st64_b32 v[142:143], v14 offset0:200 offset1:201\n"
"v_mul_f32 v146, v146, v16\n"
"v_mul_f32 v147, v147, v16\n"
"v_cvt_pk_bf16_f32 v163, v146, v147\n"
"s_waitcnt lgkmcnt(3)\n"
"v_mfma_f32_16x16x32_bf16 v[152:155], v[0:3], v[168:171], v[152:155]\n"
"ds_read_b128 v[144:147], v17 offset:56320\n"
"v_mfma_f32_16x16x32_bf16 v[164:167], v[112:115], v[168:171], v[164:167]\n"
"s_waitcnt lgkmcnt(3)\n"
"ds_read2st64_b32 v[148:149], v14 offset0:130 offset1:131\n"
"v_mfma_f32_16x16x32_bf16 v[172:175], v[136:139], v[168:171], v[172:175]\n"
"s_waitcnt lgkmcnt(2)\n"
"ds_read2st64_b32 v[150:151], v14 offset0:154 offset1:155\n"
"v_mfma_f32_16x16x32_bf16 v[152:155], v[116:119], v[140:143], v[152:155]\n"
"ds_read_b128 v[136:139], v17 offset:57344\n"
"v_mfma_f32_16x16x32_bf16 v[164:167], v[120:123], v[140:143], v[164:167]\n"
"s_waitcnt lgkmcnt(3)\n"
"ds_read2st64_b32 v[156:157], v14 offset0:178 offset1:179\n"
"v_mfma_f32_16x16x32_bf16 v[172:175], v[144:147], v[140:143], v[172:175]\n"
"s_waitcnt lgkmcnt(2)\n"
"ds_read2st64_b32 v[158:159], v14 offset0:202 offset1:203\n"
"v_mfma_f32_16x16x32_bf16 v[152:155], v[4:7], v[148:151], v[152:155]\n"
"ds_read_b128 v[140:143], v17 offset:58368\n"
"v_mfma_f32_16x16x32_bf16 v[164:167], v[124:127], v[148:151], v[164:167]\n"
"s_waitcnt lgkmcnt(3)\n"
"ds_read_b128 v[144:147], v12 offset:12288\n"
"v_mfma_f32_16x16x32_bf16 v[172:175], v[136:139], v[148:151], v[172:175]\n"
"s_waitcnt lgkmcnt(2)\n"
"v_mfma_f32_16x16x32_bf16 v[152:155], v[128:131], v[156:159], v[152:155]\n"
"v_mfma_f32_16x16x32_bf16 v[164:167], v[132:135], v[156:159], v[164:167]\n"
"s_barrier\n"
"ds_read_b128 v[136:139], v13 offset:12288\n"
"s_waitcnt lgkmcnt(2)\n"
"v_mfma_f32_16x16x32_bf16 v[172:175], v[140:143], v[156:159], v[172:175]\n"
"s_waitcnt lgkmcnt(1)\n"
"v_mfma_f32_16x16x32_bf16 v[140:143], v[144:147], v[28:31], 0\n"
"v_mfma_f32_16x16x32_bf16 v[148:151], v[144:147], v[32:35], 0\n"
"ds_read_b128 v[156:159], v12 offset:14336\n"
"v_mfma_f32_16x16x32_bf16 v[168:171], v[144:147], v[24:27], 0\n"
"v_cvt_pk_bf16_f32 v152, v152, v153\n"
"v_cvt_pk_bf16_f32 v153, v154, v155\n"
"v_cvt_pk_bf16_f32 v154, v164, v165\n"
"v_cvt_pk_bf16_f32 v155, v166, v167\n"
"s_waitcnt lgkmcnt(1)\n"
"v_mfma_f32_16x16x32_bf16 v[140:143], v[136:139], v[40:43], v[140:143]\n"
"s_mov_b32 m0, s54\n"
"v_cvt_pk_bf16_f32 v172, v172, v173\n"
"v_cvt_pk_bf16_f32 v173, v174, v175\n"
"v_mfma_f32_16x16x32_bf16 v[148:151], v[136:139], v[44:47], v[148:151]\n"
"ds_read_b128 v[144:147], v13 offset:14336\n"
"v_mfma_f32_16x16x32_bf16 v[168:171], v[136:139], v[36:39], v[168:171]\n"
"ds_write_addtid_b32 v254 offset:33280\n"
"s_waitcnt lgkmcnt(2)\n"
"v_mfma_f32_16x16x32_bf16 v[140:143], v[156:159], v[48:51], v[140:143]\n"
"ds_write_addtid_b32 v255 offset:33536\n"
"v_mfma_f32_16x16x32_bf16 v[148:151], v[156:159], v[52:55], v[148:151]\n"
"ds_write_addtid_b32 v160 offset:32256\n"
"v_mfma_f32_16x16x32_bf16 v[168:171], v[156:159], v[56:59], v[168:171]\n"
"ds_read_b128 v[136:139], v12 offset:16384\n"
"ds_write_addtid_b32 v161 offset:32512\n"
"s_waitcnt lgkmcnt(5)\n"
"v_mfma_f32_16x16x32_bf16 v[140:143], v[144:147], v[60:63], v[140:143]\n"
"ds_write_addtid_b32 v162 offset:32768\n"
"v_mfma_f32_16x16x32_bf16 v[148:151], v[144:147], v[64:67], v[148:151]\n"
"ds_write_addtid_b32 v163 offset:33024\n"
"s_mov_b32 m0, s68\n"
"ds_read_b128 v[156:159], v13 offset:16384\n"
"v_mfma_f32_16x16x32_bf16 v[168:171], v[144:147], v[68:71], v[168:171]\n"
"ds_write_addtid_b32 v152 offset:45056\n"
"ds_write_addtid_b32 v153 offset:45312\n"
"ds_write_addtid_b32 v154 offset:45568\n"
"s_waitcnt lgkmcnt(7)\n"
"v_mfma_f32_16x16x32_bf16 v[140:143], v[136:139], v[72:75], v[140:143]\n"
"ds_write_addtid_b32 v155 offset:45824\n"
"ds_write_addtid_b32 v172 offset:46080\n"
"v_mfma_f32_16x16x32_bf16 v[148:151], v[136:139], v[76:79], v[148:151]\n"
"ds_write_addtid_b32 v173 offset:46336\n"
"v_mov_b32 v144, 0x3f803f80\n"
"v_mfma_f32_16x16x32_bf16 v[168:171], v[136:139], v[80:83], v[168:171]\n"
"s_waitcnt lgkmcnt(6)\n"
"v_mov_b32 v145, 0x3f803f80\n"
"v_mov_b32 v146, 0x3f803f80\n"
"v_mfma_f32_16x16x32_bf16 v[140:143], v[156:159], v[84:87], v[140:143]\n"
"v_mov_b32 v147, 0x3f803f80\n"
"v_mfma_f32_16x16x32_bf16 v[148:151], v[156:159], v[88:91], v[148:151]\n"
"v_mfma_f32_16x16x32_bf16 v[168:171], v[156:159], v[92:95], v[168:171]\n"
"s_waitcnt vmcnt(5)\n"
"s_waitcnt lgkmcnt(0)\n"
"s_barrier\n"
"ds_read2st64_b32 v[136:137], v9 offset0:120 offset1:121\n"
"ds_read2st64_b32 v[138:139], v9 offset0:122 offset1:123\n"
"s_waitcnt lgkmcnt(0)\n"
"v_mfma_f32_16x16x32_bf16 v[152:155], v[144:147], v[136:139], 0\n"
"ds_read2st64_b32 v[156:157], v9 offset0:124 offset1:125\n"
"v_cvt_pk_bf16_f32 v202, v140, v141\n"
"v_mfma_f32_16x16x32_bf16 v[160:163], v[178:181], v[136:139], 0\n"
"ds_read2st64_b32 v[158:159], v9 offset0:126 offset1:127\n"
"v_cvt_pk_bf16_f32 v203, v142, v143\n"
"v_mfma_f32_16x16x32_bf16 v[140:143], v[204:207], v[136:139], 0\n"
"v_cvt_pk_bf16_f32 v228, v148, v149\n"
"v_cvt_pk_bf16_f32 v229, v150, v151\n"
"v_mfma_f32_16x16x32_bf16 v[148:151], v[230:233], v[136:139], 0\n"
"ds_read_u16 v16, v21 offset:1024\n"
"v_cvt_pk_bf16_f32 v254, v168, v169\n"
"v_cvt_pk_bf16_f32 v255, v170, v171\n"
"ds_read2st64_b32 v[136:137], v20 offset0:0 offset1:1\n"
"ds_read2st64_b32 v[138:139], v20 offset0:2 offset1:3\n"
"s_waitcnt lgkmcnt(3)\n"
"v_mfma_f32_16x16x32_bf16 v[152:155], v[144:147], v[156:159], v[152:155]\n"
"ds_read2st64_b32 v[164:165], v9 offset0:128 offset1:129\n"
"v_mfma_f32_16x16x32_bf16 v[160:163], v[188:191], v[156:159], v[160:163]\n"
"ds_read2st64_b32 v[166:167], v9 offset0:130 offset1:131\n"
"v_mfma_f32_16x16x32_bf16 v[140:143], v[214:217], v[156:159], v[140:143]\n"
"s_waitcnt lgkmcnt(4)\n"
"v_mad_u32_u24 v15, v16, s40, v18\n"
"v_mad_u32_u24 v16, v16, s40, v19\n"
"v_mfma_f32_16x16x32_bf16 v[148:151], v[240:243], v[156:159], v[148:151]\n"
"s_waitcnt lgkmcnt(2)\n"
"ds_read2st64_b32 v[240:241], v20 offset0:4 offset1:5\n"
"global_store_dwordx4 v15, v[136:139], s[66:67] sc1\n"
"s_waitcnt lgkmcnt(1)\n"
"v_mfma_f32_16x16x32_bf16 v[152:155], v[144:147], v[164:167], v[152:155]\n"
"ds_read_b32 v170, v9 offset:33792\n"
"v_mfma_f32_16x16x32_bf16 v[160:163], v[198:201], v[164:167], v[160:163]\n"
"ds_read_b32 v172, v9 offset:34304\n"
"v_mov_b32 v171, 0\n"
"v_mfma_f32_16x16x32_bf16 v[140:143], v[224:227], v[164:167], v[140:143]\n"
"ds_read_b32 v138, v9 offset:9728\n"
"v_mov_b32 v173, 0\n"
"v_mfma_f32_16x16x32_bf16 v[148:151], v[250:253], v[164:167], v[148:151]\n"
"s_waitcnt lgkmcnt(3)\n"
"global_store_dwordx2 v16, v[240:241], s[66:67] offset:256 sc1\n"
"v_mov_b32 v139, 0\n"
"v_mov_b32 v136, 0\n"
"v_mov_b32 v137, 0\n"
"v_mov_b32 v168, 0\n"
"v_mov_b32 v169, 0\n"
"s_waitcnt lgkmcnt(1)\n"
"v_mfma_f32_16x16x32_bf16 v[152:155], v[144:147], v[170:173], v[152:155]\n"
"s_waitcnt lgkmcnt(0)\n"
"v_mov_b32 v174, 0\n"
"v_mov_b32 v175, 0\n"
"v_mfma_f32_16x16x32_bf16 v[152:155], v[144:147], v[136:139], v[152:155]\n"
"v_mfma_f32_16x16x32_bf16 v[148:151], v[232:235], v[168:171], v[148:151]\n"
"v_mfma_f32_16x16x32_bf16 v[148:151], v[244:247], v[172:175], v[148:151]\n"
"v_mfma_f32_16x16x32_bf16 v[148:151], v[252:255], v[136:139], v[148:151]\n"
"v_mfma_f32_16x16x32_bf16 v[160:163], v[180:183], v[168:171], v[160:163]\n"
"v_mfma_f32_16x16x32_bf16 v[160:163], v[192:195], v[172:175], v[160:163]\n"
"v_mfma_f32_16x16x32_bf16 v[160:163], v[200:203], v[136:139], v[160:163]\n"
"ds_read2st64_b32 v[144:145], v14 offset0:126 offset1:127\n"
"v_mfma_f32_16x16x32_bf16 v[140:143], v[206:209], v[168:171], v[140:143]\n"
"ds_read2st64_b32 v[146:147], v14 offset0:128 offset1:129\n"
"v_rcp_f32 v16, v152\n"
"v_mfma_f32_16x16x32_bf16 v[140:143], v[218:221], v[172:175], v[140:143]\n"
"ds_read_b128 v[152:155], v17 offset:53248\n"
"v_mul_f32 v148, v148, v16\n"
"v_mfma_f32_16x16x32_bf16 v[140:143], v[226:229], v[136:139], v[140:143]\n"
"ds_read2st64_b32 v[136:137], v14 offset0:150 offset1:151\n"
"v_mul_f32 v149, v149, v16\n"
"v_cvt_pk_bf16_f32 v240, v148, v149\n"
"v_mul_f32 v150, v150, v16\n"
"v_mul_f32 v151, v151, v16\n"
"v_cvt_pk_bf16_f32 v241, v150, v151\n"
"v_mul_f32 v160, v160, v16\n"
"v_mul_f32 v161, v161, v16\n"
"v_cvt_pk_bf16_f32 v160, v160, v161\n"
"v_mul_f32 v162, v162, v16\n"
"v_mul_f32 v163, v163, v16\n"
"s_waitcnt lgkmcnt(2)\n"
"v_mfma_f32_16x16x32_bf16 v[148:151], v[96:99], v[144:147], 0\n"
"ds_read2st64_b32 v[138:139], v14 offset0:152 offset1:153\n"
"v_cvt_pk_bf16_f32 v161, v162, v163\n"
"v_mfma_f32_16x16x32_bf16 v[156:159], v[100:103], v[144:147], 0\n"
"s_waitcnt lgkmcnt(2)\n"
"ds_read_b128 v[164:167], v17 offset:54272\n"
"v_mul_f32 v140, v140, v16\n"
"v_mfma_f32_16x16x32_bf16 v[168:171], v[152:155], v[144:147], 0\n"
"ds_read2st64_b32 v[144:145], v14 offset0:174 offset1:175\n"
"v_mul_f32 v141, v141, v16\n"
"v_cvt_pk_bf16_f32 v162, v140, v141\n"
"v_mul_f32 v142, v142, v16\n"
"v_mul_f32 v143, v143, v16\n"
"v_cvt_pk_bf16_f32 v163, v142, v143\n"
"ds_read2st64_b32 v[146:147], v14 offset0:176 offset1:177\n"
"s_waitcnt lgkmcnt(3)\n"
"v_mfma_f32_16x16x32_bf16 v[148:151], v[104:107], v[136:139], v[148:151]\n"
"v_mfma_f32_16x16x32_bf16 v[156:159], v[108:111], v[136:139], v[156:159]\n"
"s_waitcnt lgkmcnt(2)\n"
"ds_read_b128 v[140:143], v17 offset:55296\n"
"v_mfma_f32_16x16x32_bf16 v[168:171], v[164:167], v[136:139], v[168:171]\n"
"s_waitcnt lgkmcnt(1)\n"
"ds_read2st64_b32 v[136:137], v14 offset0:198 offset1:199\n"
"ds_read2st64_b32 v[138:139], v14 offset0:200 offset1:201\n"
"v_mfma_f32_16x16x32_bf16 v[148:151], v[0:3], v[144:147], v[148:151]\n"
"v_mfma_f32_16x16x32_bf16 v[156:159], v[112:115], v[144:147], v[156:159]\n"
"s_waitcnt lgkmcnt(2)\n"
"ds_read_b128 v[152:155], v17 offset:56320\n"
"v_mfma_f32_16x16x32_bf16 v[168:171], v[140:143], v[144:147], v[168:171]\n"
"s_waitcnt lgkmcnt(1)\n"
"ds_read2st64_b32 v[140:141], v14 offset0:130 offset1:131\n"
"ds_read2st64_b32 v[142:143], v14 offset0:154 offset1:155\n"
"v_mfma_f32_16x16x32_bf16 v[148:151], v[116:119], v[136:139], v[148:151]\n"
"v_mfma_f32_16x16x32_bf16 v[156:159], v[120:123], v[136:139], v[156:159]\n"
"s_waitcnt lgkmcnt(2)\n"
"ds_read_b128 v[144:147], v17 offset:57344\n"
"v_mfma_f32_16x16x32_bf16 v[168:171], v[152:155], v[136:139], v[168:171]\n"
"s_waitcnt lgkmcnt(1)\n"
"ds_read2st64_b32 v[136:137], v14 offset0:178 offset1:179\n"
"ds_read2st64_b32 v[138:139], v14 offset0:202 offset1:203\n"
"v_mfma_f32_16x16x32_bf16 v[148:151], v[4:7], v[140:143], v[148:151]\n"
"v_mfma_f32_16x16x32_bf16 v[156:159], v[124:127], v[140:143], v[156:159]\n"
"s_waitcnt lgkmcnt(2)\n"
"ds_read_b128 v[152:155], v17 offset:58368\n"
"v_mfma_f32_16x16x32_bf16 v[168:171], v[144:147], v[140:143], v[168:171]\n"
"s_waitcnt lgkmcnt(1)\n"
"ds_read_b128 v[140:143], v12 offset:36864\n"
"v_mfma_f32_16x16x32_bf16 v[148:151], v[128:131], v[136:139], v[148:151]\n"
"v_mfma_f32_16x16x32_bf16 v[156:159], v[132:135], v[136:139], v[156:159]\n"
"s_barrier\n"
"s_waitcnt lgkmcnt(1)\n"
"v_mfma_f32_16x16x32_bf16 v[168:171], v[152:155], v[136:139], v[168:171]\n"
"s_waitcnt lgkmcnt(0)\n"
"ds_read_b128 v[136:139], v13 offset:36864\n"
"v_mfma_f32_16x16x32_bf16 v[144:147], v[140:143], v[28:31], 0\n"
"v_mfma_f32_16x16x32_bf16 v[152:155], v[140:143], v[32:35], 0\n"
"v_mfma_f32_16x16x32_bf16 v[164:167], v[140:143], v[24:27], 0\n"
"ds_read_b128 v[140:143], v12 offset:38912\n"
"v_cvt_pk_bf16_f32 v148, v148, v149\n"
"v_cvt_pk_bf16_f32 v149, v150, v151\n"
"v_cvt_pk_bf16_f32 v150, v156, v157\n"
"v_cvt_pk_bf16_f32 v168, v168, v169\n"
"v_cvt_pk_bf16_f32 v151, v158, v159\n"
"v_cvt_pk_bf16_f32 v169, v170, v171\n"
"s_waitcnt lgkmcnt(1)\n"
"v_mfma_f32_16x16x32_bf16 v[144:147], v[136:139], v[40:43], v[144:147]\n"
"s_mov_b32 m0, s54\n"
"v_mfma_f32_16x16x32_bf16 v[152:155], v[136:139], v[44:47], v[152:155]\n"
"ds_write_addtid_b32 v240 offset:33280\n"
"v_mfma_f32_16x16x32_bf16 v[164:167], v[136:139], v[36:39], v[164:167]\n"
"s_waitcnt lgkmcnt(1)\n"
"ds_read_b128 v[136:139], v13 offset:38912\n"
"v_mfma_f32_16x16x32_bf16 v[144:147], v[140:143], v[48:51], v[144:147]\n"
"ds_write_addtid_b32 v241 offset:33536\n"
"v_mfma_f32_16x16x32_bf16 v[152:155], v[140:143], v[52:55], v[152:155]\n"
"ds_write_addtid_b32 v160 offset:32256\n"
"v_mfma_f32_16x16x32_bf16 v[164:167], v[140:143], v[56:59], v[164:167]\n"
"ds_write_addtid_b32 v161 offset:32512\n"
"ds_read_b128 v[140:143], v12 offset:40960\n"
"ds_write_addtid_b32 v162 offset:32768\n"
"ds_write_addtid_b32 v163 offset:33024\n"
"s_waitcnt lgkmcnt(6)\n"
"v_mfma_f32_16x16x32_bf16 v[144:147], v[136:139], v[60:63], v[144:147]\n"
"s_mov_b32 m0, s68\n"
"v_mfma_f32_16x16x32_bf16 v[152:155], v[136:139], v[64:67], v[152:155]\n"
"ds_write_addtid_b32 v148 offset:45056\n"
"v_mfma_f32_16x16x32_bf16 v[164:167], v[136:139], v[68:71], v[164:167]\n"
"ds_read_b128 v[136:139], v13 offset:40960\n"
"ds_write_addtid_b32 v149 offset:45312\n"
"s_waitcnt lgkmcnt(5)\n"
"v_mfma_f32_16x16x32_bf16 v[144:147], v[140:143], v[72:75], v[144:147]\n"
"ds_write_addtid_b32 v150 offset:45568\n"
"v_mfma_f32_16x16x32_bf16 v[152:155], v[140:143], v[76:79], v[152:155]\n"
"ds_write_addtid_b32 v151 offset:45824\n"
"ds_write_addtid_b32 v168 offset:46080\n"
"v_mfma_f32_16x16x32_bf16 v[164:167], v[140:143], v[80:83], v[164:167]\n"
"ds_write_addtid_b32 v169 offset:46336\n"
"v_mov_b32 v140, 0x3f803f80\n"
"v_mov_b32 v141, 0x3f803f80\n"
"v_mov_b32 v142, 0x3f803f80\n"
"v_mov_b32 v143, 0x3f803f80\n"
"s_waitcnt lgkmcnt(5)\n"
"v_mfma_f32_16x16x32_bf16 v[144:147], v[136:139], v[84:87], v[144:147]\n"
"v_mfma_f32_16x16x32_bf16 v[152:155], v[136:139], v[88:91], v[152:155]\n"
"v_mfma_f32_16x16x32_bf16 v[164:167], v[136:139], v[92:95], v[164:167]\n"
"s_waitcnt vmcnt(2)\n"
"s_waitcnt lgkmcnt(0)\n"
"s_barrier\n"
"ds_read2st64_b32 v[136:137], v9 offset0:112 offset1:113\n"
"ds_read2st64_b32 v[138:139], v9 offset0:114 offset1:115\n"
"s_waitcnt lgkmcnt(0)\n"
"v_mfma_f32_16x16x32_bf16 v[148:151], v[140:143], v[136:139], 0\n"
"ds_read2st64_b32 v[156:157], v9 offset0:116 offset1:117\n"
"v_mfma_f32_16x16x32_bf16 v[160:163], v[182:185], v[136:139], 0\n"
"ds_read2st64_b32 v[158:159], v9 offset0:118 offset1:119\n"
"v_cvt_pk_bf16_f32 v178, v144, v145\n"
"v_mfma_f32_16x16x32_bf16 v[168:171], v[208:211], v[136:139], 0\n"
"ds_read_u16 v16, v21 offset:1056\n"
"v_cvt_pk_bf16_f32 v179, v146, v147\n"
"v_mfma_f32_16x16x32_bf16 v[144:147], v[234:237], v[136:139], 0\n"
"ds_read2st64_b32 v[136:137], v20 offset0:0 offset1:1\n"
"v_cvt_pk_bf16_f32 v204, v152, v153\n"
"v_cvt_pk_bf16_f32 v205, v154, v155\n"
"v_cvt_pk_bf16_f32 v230, v164, v165\n"
"v_cvt_pk_bf16_f32 v231, v166, v167\n"
"ds_read2st64_b32 v[138:139], v20 offset0:2 offset1:3\n"
"ds_read2st64_b32 v[152:153], v9 offset0:128 offset1:129\n"
"ds_read2st64_b32 v[154:155], v9 offset0:130 offset1:131\n"
"s_waitcnt lgkmcnt(5)\n"
"v_mfma_f32_16x16x32_bf16 v[148:151], v[140:143], v[156:159], v[148:151]\n"
"v_mfma_f32_16x16x32_bf16 v[160:163], v[190:193], v[156:159], v[160:163]\n"
"s_waitcnt lgkmcnt(4)\n"
"v_mad_u32_u24 v15, v16, s40, v18\n"
"v_mad_u32_u24 v16, v16, s40, v19\n"
"v_mfma_f32_16x16x32_bf16 v[168:171], v[216:219], v[156:159], v[168:171]\n"
"ds_read2st64_b32 v[240:241], v20 offset0:4 offset1:5\n"
"v_mfma_f32_16x16x32_bf16 v[144:147], v[242:245], v[156:159], v[144:147]\n"
"s_waitcnt lgkmcnt(1)\n"
"ds_read2st64_b32 v[156:157], v9 offset0:132 offset1:133\n"
"ds_read2st64_b32 v[158:159], v9 offset0:134 offset1:135\n"
"v_mfma_f32_16x16x32_bf16 v[148:151], v[140:143], v[152:155], v[148:151]\n"
"global_store_dwordx4 v15, v[136:139], s[66:67] sc1\n"
"s_nop 1\n"
"ds_read2st64_b32 v[138:139], v9 offset0:36 offset1:37\n"
"v_mfma_f32_16x16x32_bf16 v[160:163], v[178:181], v[152:155], v[160:163]\n"
"v_mov_b32 v136, 0\n"
"v_mov_b32 v137, 0\n"
"v_mfma_f32_16x16x32_bf16 v[168:171], v[204:207], v[152:155], v[168:171]\n"
"s_waitcnt lgkmcnt(3)\n"
"global_store_dwordx2 v16, v[240:241], s[66:67] offset:256 sc1\n"
"v_mfma_f32_16x16x32_bf16 v[144:147], v[230:233], v[152:155], v[144:147]\n"
"s_waitcnt lgkmcnt(1)\n"
"v_mfma_f32_16x16x32_bf16 v[148:151], v[140:143], v[156:159], v[148:151]\n"
"s_waitcnt lgkmcnt(0)\n"
"v_mfma_f32_16x16x32_bf16 v[148:151], v[140:143], v[136:139], v[148:151]\n"
"v_mfma_f32_16x16x32_bf16 v[144:147], v[252:255], v[156:159], v[144:147]\n"
"v_mfma_f32_16x16x32_bf16 v[144:147], v[244:247], v[136:139], v[144:147]\n"
"ds_read2st64_b32 v[140:141], v14 offset0:126 offset1:127\n"
"v_mfma_f32_16x16x32_bf16 v[160:163], v[200:203], v[156:159], v[160:163]\n"
"ds_read2st64_b32 v[142:143], v14 offset0:128 offset1:129\n"
"v_mfma_f32_16x16x32_bf16 v[160:163], v[192:195], v[136:139], v[160:163]\n"
"ds_read_b128 v[152:155], v17 offset:53248\n"
"v_mfma_f32_16x16x32_bf16 v[168:171], v[226:229], v[156:159], v[168:171]\n"
"ds_read2st64_b32 v[156:157], v14 offset0:150 offset1:151\n"
"v_rcp_f32 v16, v148\n"
"v_mfma_f32_16x16x32_bf16 v[168:171], v[218:221], v[136:139], v[168:171]\n"
"ds_read2st64_b32 v[158:159], v14 offset0:152 offset1:153\n"
"v_mul_f32 v144, v144, v16\n"
"v_mul_f32 v145, v145, v16\n"
"v_cvt_pk_bf16_f32 v240, v144, v145\n"
"v_mul_f32 v146, v146, v16\n"
"v_mul_f32 v147, v147, v16\n"
"v_cvt_pk_bf16_f32 v241, v146, v147\n"
"s_waitcnt lgkmcnt(3)\n"
"v_mfma_f32_16x16x32_bf16 v[136:139], v[96:99], v[140:143], 0\n"
"ds_read_b128 v[144:147], v17 offset:54272\n"
"v_mul_f32 v160, v160, v16\n"
"v_mfma_f32_16x16x32_bf16 v[148:151], v[100:103], v[140:143], 0\n"
"s_waitcnt lgkmcnt(3)\n"
"ds_read2st64_b32 v[164:165], v14 offset0:174 offset1:175\n"
"v_mul_f32 v161, v161, v16\n"
"v_mfma_f32_16x16x32_bf16 v[172:175], v[152:155], v[140:143], 0\n"
"s_waitcnt lgkmcnt(2)\n"
"ds_read2st64_b32 v[166:167], v14 offset0:176 offset1:177\n"
"v_cvt_pk_bf16_f32 v160, v160, v161\n"
"v_mul_f32 v162, v162, v16\n"
"v_mfma_f32_16x16x32_bf16 v[136:139], v[104:107], v[156:159], v[136:139]\n"
"ds_read_b128 v[140:143], v17 offset:55296\n"
"v_mul_f32 v163, v163, v16\n"
"v_mfma_f32_16x16x32_bf16 v[148:151], v[108:111], v[156:159], v[148:151]\n"
"s_waitcnt lgkmcnt(3)\n"
"ds_read2st64_b32 v[152:153], v14 offset0:198 offset1:199\n"
"v_cvt_pk_bf16_f32 v161, v162, v163\n"
"v_mul_f32 v168, v168, v16\n"
"v_mfma_f32_16x16x32_bf16 v[172:175], v[144:147], v[156:159], v[172:175]\n"
"ds_read2st64_b32 v[154:155], v14 offset0:200 offset1:201\n"
"v_mul_f32 v169, v169, v16\n"
"v_cvt_pk_bf16_f32 v162, v168, v169\n"
"v_mul_f32 v170, v170, v16\n"
"v_mul_f32 v171, v171, v16\n"
"v_cvt_pk_bf16_f32 v163, v170, v171\n"
"s_waitcnt lgkmcnt(3)\n"
"v_mfma_f32_16x16x32_bf16 v[136:139], v[0:3], v[164:167], v[136:139]\n"
"ds_read_b128 v[144:147], v17 offset:56320\n"
"v_mfma_f32_16x16x32_bf16 v[148:151], v[112:115], v[164:167], v[148:151]\n"
"s_waitcnt lgkmcnt(3)\n"
"ds_read2st64_b32 v[156:157], v14 offset0:130 offset1:131\n"
"v_mfma_f32_16x16x32_bf16 v[172:175], v[140:143], v[164:167], v[172:175]\n"
"s_waitcnt lgkmcnt(2)\n"
"ds_read2st64_b32 v[158:159], v14 offset0:154 offset1:155\n"
"v_mfma_f32_16x16x32_bf16 v[136:139], v[116:119], v[152:155], v[136:139]\n"
"ds_read_b128 v[140:143], v17 offset:57344\n"
"v_mfma_f32_16x16x32_bf16 v[148:151], v[120:123], v[152:155], v[148:151]\n"
"s_waitcnt lgkmcnt(3)\n"
"ds_read2st64_b32 v[164:165], v14 offset0:178 offset1:179\n"
"v_mfma_f32_16x16x32_bf16 v[172:175], v[144:147], v[152:155], v[172:175]\n"
"s_waitcnt lgkmcnt(2)\n"
"ds_read2st64_b32 v[166:167], v14 offset0:202 offset1:203\n"
"v_mfma_f32_16x16x32_bf16 v[136:139], v[4:7], v[156:159], v[136:139]\n"
"ds_read_b128 v[144:147], v17 offset:58368\n"
"v_mfma_f32_16x16x32_bf16 v[148:151], v[124:127], v[156:159], v[148:151]\n"
"s_waitcnt lgkmcnt(3)\n"
"ds_read_b128 v[152:155], v12 offset:24576\n"
"v_mfma_f32_16x16x32_bf16 v[172:175], v[140:143], v[156:159], v[172:175]\n"
"s_waitcnt lgkmcnt(2)\n"
"v_mfma_f32_16x16x32_bf16 v[136:139], v[128:131], v[164:167], v[136:139]\n"
"v_mfma_f32_16x16x32_bf16 v[148:151], v[132:135], v[164:167], v[148:151]\n"
"s_barrier\n"
"ds_read_b128 v[140:143], v13 offset:24576\n"
"s_waitcnt lgkmcnt(2)\n"
"v_mfma_f32_16x16x32_bf16 v[172:175], v[144:147], v[164:167], v[172:175]\n"
"s_waitcnt lgkmcnt(1)\n"
"v_mfma_f32_16x16x32_bf16 v[144:147], v[152:155], v[28:31], 0\n"
"v_mfma_f32_16x16x32_bf16 v[28:31], v[152:155], v[32:35], 0\n"
"ds_read_b128 v[32:35], v12 offset:26624\n"
"v_mfma_f32_16x16x32_bf16 v[156:159], v[152:155], v[24:27], 0\n"
"v_cvt_pk_bf16_f32 v136, v136, v137\n"
"v_cvt_pk_bf16_f32 v137, v138, v139\n"
"v_cvt_pk_bf16_f32 v138, v148, v149\n"
"v_cvt_pk_bf16_f32 v139, v150, v151\n"
"s_waitcnt lgkmcnt(1)\n"
"v_mfma_f32_16x16x32_bf16 v[144:147], v[140:143], v[40:43], v[144:147]\n"
"s_mov_b32 m0, s54\n"
"v_cvt_pk_bf16_f32 v172, v172, v173\n"
"v_cvt_pk_bf16_f32 v173, v174, v175\n"
"v_mfma_f32_16x16x32_bf16 v[28:31], v[140:143], v[44:47], v[28:31]\n"
"ds_read_b128 v[24:27], v13 offset:26624\n"
"v_mfma_f32_16x16x32_bf16 v[156:159], v[140:143], v[36:39], v[156:159]\n"
"ds_write_addtid_b32 v240 offset:33280\n"
"s_waitcnt lgkmcnt(2)\n"
"v_mfma_f32_16x16x32_bf16 v[144:147], v[32:35], v[48:51], v[144:147]\n"
"ds_write_addtid_b32 v241 offset:33536\n"
"v_mfma_f32_16x16x32_bf16 v[28:31], v[32:35], v[52:55], v[28:31]\n"
"ds_write_addtid_b32 v160 offset:32256\n"
"v_mfma_f32_16x16x32_bf16 v[156:159], v[32:35], v[56:59], v[156:159]\n"
"ds_read_b128 v[32:35], v12 offset:28672\n"
"ds_write_addtid_b32 v161 offset:32512\n"
"s_waitcnt lgkmcnt(5)\n"
"v_mfma_f32_16x16x32_bf16 v[144:147], v[24:27], v[60:63], v[144:147]\n"
"ds_write_addtid_b32 v162 offset:32768\n"
"v_mfma_f32_16x16x32_bf16 v[28:31], v[24:27], v[64:67], v[28:31]\n"
"ds_write_addtid_b32 v163 offset:33024\n"
"s_mov_b32 m0, s68\n"
"ds_read_b128 v[36:39], v13 offset:28672\n"
"v_mfma_f32_16x16x32_bf16 v[156:159], v[24:27], v[68:71], v[156:159]\n"
"ds_write_addtid_b32 v136 offset:45056\n"
"ds_write_addtid_b32 v137 offset:45312\n"
"ds_write_addtid_b32 v138 offset:45568\n"
"s_waitcnt lgkmcnt(7)\n"
"v_mfma_f32_16x16x32_bf16 v[144:147], v[32:35], v[72:75], v[144:147]\n"
"ds_write_addtid_b32 v139 offset:45824\n"
"ds_write_addtid_b32 v172 offset:46080\n"
"v_mfma_f32_16x16x32_bf16 v[28:31], v[32:35], v[76:79], v[28:31]\n"
"ds_write_addtid_b32 v173 offset:46336\n"
"v_mov_b32 v24, 0x3f803f80\n"
"v_mfma_f32_16x16x32_bf16 v[156:159], v[32:35], v[80:83], v[156:159]\n"
"s_waitcnt lgkmcnt(6)\n"
"v_mov_b32 v25, 0x3f803f80\n"
"v_mov_b32 v26, 0x3f803f80\n"
"v_mfma_f32_16x16x32_bf16 v[144:147], v[36:39], v[84:87], v[144:147]\n"
"v_mov_b32 v27, 0x3f803f80\n"
"v_mov_b32 v32, 0\n"
"v_mfma_f32_16x16x32_bf16 v[28:31], v[36:39], v[88:91], v[28:31]\n"
"v_mfma_f32_16x16x32_bf16 v[156:159], v[36:39], v[92:95], v[156:159]\n"
"s_waitcnt lgkmcnt(0)\n"
"s_barrier\n"
"ds_read_b32 v33, v9 offset:30976\n"
"ds_read2st64_b32 v[34:35], v9 offset0:122 offset1:123\n"
"ds_read2st64_b32 v[36:37], v9 offset0:128 offset1:129\n"
"v_cvt_pk_bf16_f32 v180, v144, v145\n"
"v_cvt_pk_bf16_f32 v181, v146, v147\n"
"ds_read2st64_b32 v[38:39], v9 offset0:130 offset1:131\n"
"v_cvt_pk_bf16_f32 v206, v28, v29\n"
"v_cvt_pk_bf16_f32 v207, v30, v31\n"
"v_cvt_pk_bf16_f32 v232, v156, v157\n"
"v_cvt_pk_bf16_f32 v233, v158, v159\n"
"s_waitcnt lgkmcnt(2)\n"
"v_mfma_f32_16x16x32_bf16 v[28:31], v[24:27], v[32:35], 0\n"
"v_mfma_f32_16x16x32_bf16 v[40:43], v[182:185], v[32:35], 0\n"
"v_mfma_f32_16x16x32_bf16 v[44:47], v[208:211], v[32:35], 0\n"
"ds_read_u16 v16, v21 offset:1088\n"
"v_mfma_f32_16x16x32_bf16 v[48:51], v[234:237], v[32:35], 0\n"
"s_waitcnt lgkmcnt(1)\n"
"ds_read2st64_b32 v[32:33], v20 offset0:0 offset1:1\n"
"ds_read2st64_b32 v[34:35], v20 offset0:2 offset1:3\n"
"v_mfma_f32_16x16x32_bf16 v[28:31], v[24:27], v[36:39], v[28:31]\n"
"ds_read_b32 v53, v9 offset:34048\n"
"v_mov_b32 v52, 0\n"
"v_mfma_f32_16x16x32_bf16 v[40:43], v[178:181], v[36:39], v[40:43]\n"
"ds_read2st64_b32 v[54:55], v9 offset0:134 offset1:135\n"
"v_mfma_f32_16x16x32_bf16 v[44:47], v[204:207], v[36:39], v[44:47]\n"
"ds_read2st64_b32 v[240:241], v20 offset0:4 offset1:5\n"
"v_mfma_f32_16x16x32_bf16 v[48:51], v[230:233], v[36:39], v[48:51]\n"
"s_waitcnt lgkmcnt(5)\n"
"ds_read2st64_b32 v[58:59], v9 offset0:124 offset1:125\n"
"v_mad_u32_u24 v15, v16, s40, v18\n"
"v_mad_u32_u24 v16, v16, s40, v19\n"
"s_waitcnt lgkmcnt(4)\n"
"global_store_dwordx4 v15, v[32:35], s[66:67] sc1\n"
"ds_read2st64_b32 v[60:61], v9 offset0:126 offset1:127\n"
"s_waitcnt lgkmcnt(3)\n"
"v_mfma_f32_16x16x32_bf16 v[28:31], v[24:27], v[52:55], v[28:31]\n"
"ds_read_b32 v35, v9 offset:9984\n"
"v_mfma_f32_16x16x32_bf16 v[40:43], v[192:195], v[52:55], v[40:43]\n"
"s_waitcnt lgkmcnt(3)\n"
"global_store_dwordx2 v16, v[240:241], s[66:67] offset:256 sc1\n"
"v_mfma_f32_16x16x32_bf16 v[44:47], v[218:221], v[52:55], v[44:47]\n"
"v_mov_b32 v34, 0\n"
"v_mov_b32 v32, 0\n"
"v_mfma_f32_16x16x32_bf16 v[48:51], v[244:247], v[52:55], v[48:51]\n"
"s_waitcnt lgkmcnt(1)\n"
"v_mov_b32 v33, 0\n"
"v_mov_b32 v56, 0\n"
"v_mov_b32 v57, 0\n"
"v_mfma_f32_16x16x32_bf16 v[28:31], v[24:27], v[58:61], v[28:31]\n"
"s_waitcnt lgkmcnt(0)\n"
"v_mov_b32 v62, 0\n"
"v_mov_b32 v63, 0\n"
"v_mfma_f32_16x16x32_bf16 v[28:31], v[24:27], v[32:35], v[28:31]\n"
"v_mfma_f32_16x16x32_bf16 v[48:51], v[236:239], v[56:59], v[48:51]\n"
"v_mfma_f32_16x16x32_bf16 v[48:51], v[248:251], v[60:63], v[48:51]\n"
"v_mfma_f32_16x16x32_bf16 v[48:51], v[252:255], v[32:35], v[48:51]\n"
"v_mfma_f32_16x16x32_bf16 v[40:43], v[184:187], v[56:59], v[40:43]\n"
"v_mfma_f32_16x16x32_bf16 v[40:43], v[196:199], v[60:63], v[40:43]\n"
"ds_read2st64_b32 v[182:183], v14 offset0:126 offset1:127\n"
"v_mfma_f32_16x16x32_bf16 v[40:43], v[200:203], v[32:35], v[40:43]\n"
"ds_read2st64_b32 v[184:185], v14 offset0:128 offset1:129\n"
"v_rcp_f32 v16, v28\n"
"v_mfma_f32_16x16x32_bf16 v[44:47], v[210:213], v[56:59], v[44:47]\n"
"ds_read_b128 v[186:189], v17 offset:53248\n"
"v_mul_f32 v48, v48, v16\n"
"v_mfma_f32_16x16x32_bf16 v[44:47], v[222:225], v[60:63], v[44:47]\n"
"ds_read2st64_b32 v[196:197], v14 offset0:150 offset1:151\n"
"v_mul_f32 v49, v49, v16\n"
"v_mfma_f32_16x16x32_bf16 v[44:47], v[226:229], v[32:35], v[44:47]\n"
"ds_read2st64_b32 v[198:199], v14 offset0:152 offset1:153\n"
"v_cvt_pk_bf16_f32 v240, v48, v49\n"
"v_mul_f32 v50, v50, v16\n"
"v_mul_f32 v51, v51, v16\n"
"v_cvt_pk_bf16_f32 v241, v50, v51\n"
"v_mul_f32 v40, v40, v16\n"
"v_mul_f32 v41, v41, v16\n"
"s_waitcnt lgkmcnt(3)\n"
"v_mfma_f32_16x16x32_bf16 v[208:211], v[96:99], v[182:185], 0\n"
"ds_read_b128 v[212:215], v17 offset:54272\n"
"v_cvt_pk_bf16_f32 v40, v40, v41\n"
"v_mfma_f32_16x16x32_bf16 v[222:225], v[100:103], v[182:185], 0\n"
"s_waitcnt lgkmcnt(3)\n"
"ds_read2st64_b32 v[234:235], v14 offset0:174 offset1:175\n"
"v_mul_f32 v42, v42, v16\n"
"v_mfma_f32_16x16x32_bf16 v[248:251], v[186:189], v[182:185], 0\n"
"s_waitcnt lgkmcnt(2)\n"
"ds_read2st64_b32 v[236:237], v14 offset0:176 offset1:177\n"
"v_mul_f32 v43, v43, v16\n"
"v_cvt_pk_bf16_f32 v41, v42, v43\n"
"v_mfma_f32_16x16x32_bf16 v[208:211], v[104:107], v[196:199], v[208:211]\n"
"ds_read_b128 v[182:185], v17 offset:55296\n"
"v_mul_f32 v44, v44, v16\n"
"v_mfma_f32_16x16x32_bf16 v[222:225], v[108:111], v[196:199], v[222:225]\n"
"s_waitcnt lgkmcnt(3)\n"
"ds_read2st64_b32 v[186:187], v14 offset0:198 offset1:199\n"
"v_mul_f32 v45, v45, v16\n"
"v_cvt_pk_bf16_f32 v42, v44, v45\n"
"v_mfma_f32_16x16x32_bf16 v[248:251], v[212:215], v[196:199], v[248:251]\n"
"ds_read2st64_b32 v[188:189], v14 offset0:200 offset1:201\n"
"v_mul_f32 v46, v46, v16\n"
"v_mul_f32 v47, v47, v16\n"
"v_cvt_pk_bf16_f32 v43, v46, v47\n"
"s_waitcnt lgkmcnt(3)\n"
"v_mfma_f32_16x16x32_bf16 v[208:211], v[0:3], v[234:237], v[208:211]\n"
"s_mov_b32 m0, s54\n"
"ds_read_b128 v[196:199], v17 offset:56320\n"
"v_mfma_f32_16x16x32_bf16 v[222:225], v[112:115], v[234:237], v[222:225]\n"
"s_waitcnt lgkmcnt(3)\n"
"ds_read2st64_b32 v[212:213], v14 offset0:130 offset1:131\n"
"v_mfma_f32_16x16x32_bf16 v[248:251], v[182:185], v[234:237], v[248:251]\n"
"s_waitcnt lgkmcnt(2)\n"
"ds_read2st64_b32 v[214:215], v14 offset0:154 offset1:155\n"
"ds_read2st64_b32 v[182:183], v14 offset0:178 offset1:179\n"
"v_mfma_f32_16x16x32_bf16 v[208:211], v[116:119], v[186:189], v[208:211]\n"
"ds_read2st64_b32 v[184:185], v14 offset0:202 offset1:203\n"
"v_mfma_f32_16x16x32_bf16 v[222:225], v[120:123], v[186:189], v[222:225]\n"
"ds_write_addtid_b32 v240 offset:7168\n"
"ds_write_addtid_b32 v241 offset:7424\n"
"s_waitcnt lgkmcnt(6)\n"
"v_mfma_f32_16x16x32_bf16 v[248:251], v[196:199], v[186:189], v[248:251]\n"
"ds_write_addtid_b32 v40 offset:6144\n"
"ds_write_addtid_b32 v41 offset:6400\n"
"ds_write_addtid_b32 v42 offset:6656\n"
"s_waitcnt lgkmcnt(7)\n"
"v_mfma_f32_16x16x32_bf16 v[208:211], v[4:7], v[212:215], v[208:211]\n"
"ds_write_addtid_b32 v43 offset:6912\n"
"ds_read_b128 v[186:189], v17 offset:57344\n"
"v_mfma_f32_16x16x32_bf16 v[222:225], v[124:127], v[212:215], v[222:225]\n"
"ds_read_b128 v[196:199], v17 offset:58368\n"
"v_mov_b32 v234, 0x3f803f80\n"
"v_mov_b32 v235, 0x3f803f80\n"
"v_mov_b32 v236, 0x3f803f80\n"
"v_mov_b32 v237, 0x3f803f80\n"
"s_waitcnt lgkmcnt(2)\n"
"s_barrier\n"
"ds_read2st64_b32 v[238:239], v9 offset0:112 offset1:113\n"
"ds_read2st64_b32 v[240:241], v9 offset0:114 offset1:115\n"
"s_waitcnt lgkmcnt(3)\n"
"v_mfma_f32_16x16x32_bf16 v[248:251], v[186:189], v[212:215], v[248:251]\n"
"v_mfma_f32_16x16x32_bf16 v[208:211], v[128:131], v[182:185], v[208:211]\n"
"v_mfma_f32_16x16x32_bf16 v[222:225], v[132:135], v[182:185], v[222:225]\n"
"s_waitcnt lgkmcnt(2)\n"
"v_subrev_u32 v16, 24576, v9\n"
"v_mfma_f32_16x16x32_bf16 v[248:251], v[196:199], v[182:185], v[248:251]\n"
"s_waitcnt lgkmcnt(0)\n"
"ds_read2st64_b32 v[182:183], v9 offset0:116 offset1:117\n"
"ds_read2st64_b32 v[184:185], v9 offset0:118 offset1:119\n"
"v_mfma_f32_16x16x32_bf16 v[186:189], v[234:237], v[238:241], 0\n"
"ds_read2st64_b32 v[26:27], v16 offset0:16 offset1:17\n"
"v_mfma_f32_16x16x32_bf16 v[196:199], v[190:193], v[238:241], 0\n"
"ds_read2st64_b32 v[28:29], v16 offset0:18 offset1:19\n"
"v_cvt_pk_bf16_f32 v208, v208, v209\n"
"v_mfma_f32_16x16x32_bf16 v[212:215], v[216:219], v[238:241], 0\n"
"v_cvt_pk_bf16_f32 v209, v210, v211\n"
"v_cvt_pk_bf16_f32 v210, v222, v223\n"
"v_mfma_f32_16x16x32_bf16 v[8:11], v[242:245], v[238:241], 0\n"
"v_mov_b32 v24, 0\n"
"v_mov_b32 v25, 0\n"
"v_cvt_pk_bf16_f32 v211, v224, v225\n"
"v_cvt_pk_bf16_f32 v248, v248, v249\n"
"v_cvt_pk_bf16_f32 v249, v250, v251\n"
"s_waitcnt lgkmcnt(2)\n"
"v_mfma_f32_16x16x32_bf16 v[186:189], v[234:237], v[182:185], v[186:189]\n"
"s_waitcnt lgkmcnt(0)\n"
"v_mov_b32 v30, 0\n"
"v_mov_b32 v31, 0\n"
"v_mfma_f32_16x16x32_bf16 v[186:189], v[234:237], v[26:29], v[186:189]\n"
"v_mfma_f32_16x16x32_bf16 v[8:11], v[252:255], v[182:185], v[8:11]\n"
"v_mfma_f32_16x16x32_bf16 v[8:11], v[244:247], v[24:27], v[8:11]\n"
"v_mfma_f32_16x16x32_bf16 v[8:11], v[230:233], v[28:31], v[8:11]\n"
"s_mov_b32 m0, s54\n"
"v_mfma_f32_16x16x32_bf16 v[196:199], v[200:203], v[182:185], v[196:199]\n"
"v_mfma_f32_16x16x32_bf16 v[196:199], v[192:195], v[24:27], v[196:199]\n"
"s_nop 1\n"
"ds_read2st64_b32 v[188:189], v14 offset0:24 offset1:25\n"
"v_mfma_f32_16x16x32_bf16 v[196:199], v[178:181], v[28:31], v[196:199]\n"
"ds_read2st64_b32 v[190:191], v14 offset0:26 offset1:27\n"
"v_rcp_f32 v16, v186\n"
"v_mfma_f32_16x16x32_bf16 v[212:215], v[226:229], v[182:185], v[212:215]\n"
"ds_read_b128 v[178:181], v17 offset:53248\n"
"v_mul_f32 v8, v8, v16\n"
"v_mul_f32 v9, v9, v16\n"
"v_mul_f32 v10, v10, v16\n"
"v_mul_f32 v11, v11, v16\n"
"v_mul_f32 v196, v196, v16\n"
"v_mul_f32 v197, v197, v16\n"
"v_cvt_pk_bf16_f32 v196, v196, v197\n"
"v_mul_f32 v198, v198, v16\n"
"v_mul_f32 v199, v199, v16\n"
"v_cvt_pk_bf16_f32 v197, v198, v199\n"
"v_cvt_pk_bf16_f32 v250, v8, v9\n"
"v_cvt_pk_bf16_f32 v251, v10, v11\n"
"v_mfma_f32_16x16x32_bf16 v[212:215], v[218:221], v[24:27], v[212:215]\n"
"ds_write_addtid_b32 v250 offset:33280\n"
"ds_write_addtid_b32 v251 offset:33536\n"
"v_mfma_f32_16x16x32_bf16 v[212:215], v[204:207], v[28:31], v[212:215]\n"
"ds_write_addtid_b32 v196 offset:32256\n"
"ds_write_addtid_b32 v197 offset:32512\n"
"s_waitcnt lgkmcnt(5)\n"
"v_mfma_f32_16x16x32_bf16 v[182:185], v[96:99], v[188:191], 0\n"
"ds_read2st64_b32 v[192:193], v14 offset0:48 offset1:49\n"
"ds_read2st64_b32 v[194:195], v14 offset0:50 offset1:51\n"
"v_mfma_f32_16x16x32_bf16 v[200:203], v[100:103], v[188:191], 0\n"
"s_waitcnt lgkmcnt(6)\n"
"ds_read_b128 v[204:207], v17 offset:54272\n"
"v_mfma_f32_16x16x32_bf16 v[216:219], v[178:181], v[188:191], 0\n"
"ds_read2st64_b32 v[178:179], v14 offset0:72 offset1:73\n"
"v_mul_f32 v212, v212, v16\n"
"v_mul_f32 v213, v213, v16\n"
"v_mul_f32 v214, v214, v16\n"
"v_mul_f32 v215, v215, v16\n"
"v_cvt_pk_bf16_f32 v198, v212, v213\n"
"v_cvt_pk_bf16_f32 v199, v214, v215\n"
"ds_write_addtid_b32 v198 offset:32768\n"
"ds_write_addtid_b32 v199 offset:33024\n"
"s_mov_b32 m0, s68\n"
"ds_read2st64_b32 v[180:181], v14 offset0:74 offset1:75\n"
"s_waitcnt lgkmcnt(5)\n"
"v_mfma_f32_16x16x32_bf16 v[182:185], v[104:107], v[192:195], v[182:185]\n"
"ds_write_addtid_b32 v208 offset:45056\n"
"ds_write_addtid_b32 v209 offset:45312\n"
"v_mfma_f32_16x16x32_bf16 v[200:203], v[108:111], v[192:195], v[200:203]\n"
"ds_write_addtid_b32 v210 offset:45568\n"
"ds_write_addtid_b32 v211 offset:45824\n"
"s_waitcnt lgkmcnt(8)\n"
"v_mfma_f32_16x16x32_bf16 v[216:219], v[204:207], v[192:195], v[216:219]\n"
"ds_write_addtid_b32 v248 offset:46080\n"
"ds_write_addtid_b32 v249 offset:46336\n"
"ds_read_b128 v[186:189], v17 offset:55296\n"
"ds_read2st64_b32 v[190:191], v14 offset0:96 offset1:97\n"
"s_waitcnt lgkmcnt(8)\n"
"v_mfma_f32_16x16x32_bf16 v[182:185], v[0:3], v[178:181], v[182:185]\n"
"ds_read2st64_b32 v[192:193], v14 offset0:98 offset1:99\n"
"v_mfma_f32_16x16x32_bf16 v[200:203], v[112:115], v[178:181], v[200:203]\n"
"s_waitcnt lgkmcnt(2)\n"
"ds_read_b128 v[194:197], v17 offset:56320\n"
"ds_read2st64_b32 v[204:205], v14 offset0:28 offset1:29\n"
"ds_read2st64_b32 v[206:207], v14 offset0:52 offset1:53\n"
"v_mfma_f32_16x16x32_bf16 v[216:219], v[186:189], v[178:181], v[216:219]\n"
"s_waitcnt lgkmcnt(3)\n"
"ds_read2st64_b32 v[178:179], v14 offset0:76 offset1:77\n"
"ds_read2st64_b32 v[180:181], v14 offset0:100 offset1:101\n"
"v_mfma_f32_16x16x32_bf16 v[182:185], v[116:119], v[190:193], v[182:185]\n"
"ds_read_b128 v[186:189], v17 offset:57344\n"
"v_mfma_f32_16x16x32_bf16 v[200:203], v[120:123], v[190:193], v[200:203]\n"
"s_waitcnt lgkmcnt(5)\n"
"v_mfma_f32_16x16x32_bf16 v[216:219], v[194:197], v[190:193], v[216:219]\n"
"s_waitcnt lgkmcnt(3)\n"
"v_mfma_f32_16x16x32_bf16 v[182:185], v[4:7], v[204:207], v[182:185]\n"
"ds_read_b128 v[190:193], v17 offset:58368\n"
"v_mfma_f32_16x16x32_bf16 v[200:203], v[124:127], v[204:207], v[200:203]\n"
"s_waitcnt lgkmcnt(2)\n"
"s_barrier\n"
"ds_read2st64_b32 v[194:195], v14 offset0:126 offset1:127\n"
"s_waitcnt lgkmcnt(2)\n"
"v_mfma_f32_16x16x32_bf16 v[216:219], v[186:189], v[204:207], v[216:219]\n"
"ds_read2st64_b32 v[196:197], v14 offset0:128 offset1:129\n"
"v_mfma_f32_16x16x32_bf16 v[182:185], v[128:131], v[178:181], v[182:185]\n"
"ds_read_b128 v[186:189], v17 offset:53248\n"
"v_mfma_f32_16x16x32_bf16 v[200:203], v[132:135], v[178:181], v[200:203]\n"
"s_waitcnt lgkmcnt(3)\n"
"ds_read2st64_b32 v[204:205], v14 offset0:150 offset1:151\n"
"v_mfma_f32_16x16x32_bf16 v[216:219], v[190:193], v[178:181], v[216:219]\n"
"s_waitcnt lgkmcnt(2)\n"
"ds_read2st64_b32 v[206:207], v14 offset0:152 offset1:153\n"
"v_mfma_f32_16x16x32_bf16 v[178:181], v[96:99], v[194:197], 0\n"
"ds_read_b128 v[190:193], v17 offset:54272\n"
"v_mfma_f32_16x16x32_bf16 v[208:211], v[100:103], v[194:197], 0\n"
"s_waitcnt lgkmcnt(3)\n"
"ds_read2st64_b32 v[212:213], v14 offset0:174 offset1:175\n"
"v_cvt_pk_bf16_f32 v182, v182, v183\n"
"v_mfma_f32_16x16x32_bf16 v[220:223], v[186:189], v[194:197], 0\n"
"s_waitcnt lgkmcnt(2)\n"
"ds_read2st64_b32 v[214:215], v14 offset0:176 offset1:177\n"
"v_cvt_pk_bf16_f32 v183, v184, v185\n"
"v_mfma_f32_16x16x32_bf16 v[178:181], v[104:107], v[204:207], v[178:181]\n"
"ds_read_u16 v16, v21 offset:1120\n"
"v_cvt_pk_bf16_f32 v184, v200, v201\n"
"v_mfma_f32_16x16x32_bf16 v[208:211], v[108:111], v[204:207], v[208:211]\n"
"ds_read2st64_b32 v[186:187], v20 offset0:0 offset1:1\n"
"v_cvt_pk_bf16_f32 v185, v202, v203\n"
"v_cvt_pk_bf16_f32 v216, v216, v217\n"
"v_cvt_pk_bf16_f32 v217, v218, v219\n"
"s_waitcnt lgkmcnt(4)\n"
"v_mfma_f32_16x16x32_bf16 v[220:223], v[190:193], v[204:207], v[220:223]\n"
"ds_read2st64_b32 v[188:189], v20 offset0:2 offset1:3\n"
"ds_read_b128 v[190:193], v17 offset:55296\n"
"ds_read2st64_b32 v[194:195], v14 offset0:198 offset1:199\n"
"ds_read2st64_b32 v[196:197], v14 offset0:200 offset1:201\n"
"s_waitcnt lgkmcnt(6)\n"
"v_mfma_f32_16x16x32_bf16 v[178:181], v[0:3], v[212:215], v[178:181]\n"
"s_waitcnt lgkmcnt(5)\n"
"ds_read2st64_b32 v[218:219], v20 offset0:4 offset1:5\n"
"v_mad_u32_u24 v21, v16, s40, v18\n"
"v_mfma_f32_16x16x32_bf16 v[208:211], v[112:115], v[212:215], v[208:211]\n"
"ds_read_b128 v[198:201], v17 offset:56320\n"
"v_mad_u32_u24 v16, v16, s40, v19\n"
"ds_read2st64_b32 v[202:203], v14 offset0:130 offset1:131\n"
"ds_read2st64_b32 v[204:205], v14 offset0:154 offset1:155\n"
"s_waitcnt lgkmcnt(6)\n"
"v_mfma_f32_16x16x32_bf16 v[220:223], v[190:193], v[212:215], v[220:223]\n"
"s_mov_b32 m0, s68\n"
"global_store_dwordx4 v21, v[186:189], s[66:67] sc1\n"
"s_waitcnt lgkmcnt(4)\n"
"v_mfma_f32_16x16x32_bf16 v[178:181], v[116:119], v[194:197], v[178:181]\n"
"ds_read_b128 v[186:189], v17 offset:57344\n"
"v_mfma_f32_16x16x32_bf16 v[208:211], v[120:123], v[194:197], v[208:211]\n"
"s_waitcnt lgkmcnt(3)\n"
"ds_read2st64_b32 v[190:191], v14 offset0:178 offset1:179\n"
"v_mfma_f32_16x16x32_bf16 v[220:223], v[198:201], v[194:197], v[220:223]\n"
"s_waitcnt lgkmcnt(2)\n"
"ds_read2st64_b32 v[192:193], v14 offset0:202 offset1:203\n"
"v_mfma_f32_16x16x32_bf16 v[178:181], v[4:7], v[202:205], v[178:181]\n"
"global_store_dwordx2 v16, v[218:219], s[66:67] offset:256 sc1\n"
"ds_write_addtid_b32 v182 offset:51456\n"
"v_mfma_f32_16x16x32_bf16 v[208:211], v[124:127], v[202:205], v[208:211]\n"
"ds_write_addtid_b32 v183 offset:51712\n"
"ds_read_b128 v[194:197], v17 offset:58368\n"
"s_waitcnt lgkmcnt(5)\n"
"v_mfma_f32_16x16x32_bf16 v[220:223], v[186:189], v[202:205], v[220:223]\n"
"ds_write_addtid_b32 v184 offset:51968\n"
"ds_write_addtid_b32 v185 offset:52224\n"
"ds_write_addtid_b32 v216 offset:52480\n"
"s_waitcnt lgkmcnt(6)\n"
"v_mfma_f32_16x16x32_bf16 v[178:181], v[128:131], v[190:193], v[178:181]\n"
"ds_write_addtid_b32 v217 offset:52736\n"
"v_mfma_f32_16x16x32_bf16 v[208:211], v[132:135], v[190:193], v[208:211]\n"
"s_waitcnt lgkmcnt(4)\n"
"v_mfma_f32_16x16x32_bf16 v[220:223], v[194:197], v[190:193], v[220:223]\n"
"s_nop 3\n"
"v_cvt_pk_bf16_f32 v178, v178, v179\n"
"ds_write_addtid_b32 v178 offset:57856\n"
"v_cvt_pk_bf16_f32 v179, v180, v181\n"
"ds_write_addtid_b32 v179 offset:58112\n"
"v_cvt_pk_bf16_f32 v180, v208, v209\n"
"ds_write_addtid_b32 v180 offset:58368\n"
"v_cvt_pk_bf16_f32 v181, v210, v211\n"
"ds_write_addtid_b32 v181 offset:58624\n"
"v_cvt_pk_bf16_f32 v220, v220, v221\n"
"v_cvt_pk_bf16_f32 v221, v222, v223\n"
"ds_write_addtid_b32 v220 offset:58880\n"
"ds_write_addtid_b32 v221 offset:59136\n"
"s_waitcnt lgkmcnt(0)\n"
"s_barrier\n"
"ds_read2st64_b32 v[178:179], v20 offset0:25 offset1:26\n"
"ds_read2st64_b32 v[180:181], v20 offset0:27 offset1:28\n"
"ds_read2st64_b32 v[182:183], v20 offset0:29 offset1:30\n"
"ds_read2st64_b32 v[184:185], v20 offset0:50 offset1:51\n"
"ds_read2st64_b32 v[186:187], v20 offset0:52 offset1:53\n"
"ds_read2st64_b32 v[254:255], v20 offset0:54 offset1:55\n"
"s_waitcnt lgkmcnt(4)\n"
"global_store_dwordx4 v22, v[178:181], s[66:67] sc1\n"
"s_waitcnt lgkmcnt(3)\n"
"global_store_dwordx2 v23, v[182:183], s[66:67] offset:256 sc1\n"
"s_waitcnt lgkmcnt(1)\n"
"global_store_dwordx4 v176, v[184:187], s[66:67] sc1\n"
"s_waitcnt lgkmcnt(0)\n"
"global_store_dwordx2 v177, v[254:255], s[66:67] offset:256 sc1\n"
    );
  }
}

extern "C" void kernel_launch(void* const* d_in, const int* in_sizes, int n_in,
                              void* d_out, int out_size, void* d_ws, size_t ws_size,
                              hipStream_t stream) {
  dinat_fused<<<256, 512, 0, stream>>>((const uint16_t*)d_in[0], (const uint16_t*)d_in[1], (const uint16_t*)d_in[2],
                                       (const uint16_t*)d_in[3], (uint16_t*)d_out
  );
}
```

```cpp
#include <hip/hip_runtime.h>
#include <hip/hip_bf16.h>
#include <stdint.h>

typedef __attribute__((ext_vector_type(4))) float f32x4;
typedef __attribute__((ext_vector_type(4))) unsigned u32x4;
typedef __attribute__((ext_vector_type(8))) unsigned u32x8;

#define LDS_AS __attribute__((address_space(3)))
#define DI __device__ __forceinline__
#define SB __builtin_amdgcn_sched_barrier(0)
#define OPQ(v) asm volatile("" : "+v"(v))
#define UNI(v) __builtin_amdgcn_readfirstlane(v)

#define TOFF 0
#define TE 1216
#define RAW 2048
#define PFD 3584
#define O45 2048
#define FLAG 32192
#define TMPA 28672
#define BB 24576
#define PN 6
#define CPF 1
#define RBF 1
#define HTA 0
#define SS0 8
#define OID 1
#define PFC 430
#define XR 102400
#define TMPB 53248
#define RESA 4096
#define RESB 77824
#define NREG 36
#define UD0 0x1312030201001110ULL
#define UD1 0x0409050605040807ULL
#define UD2 0x2928171615141918ULL
#define UD3 0x1D1C0D0C0B0A1B1AULL
#define UD4 0x2025212221202423ULL
#define UD5 0x4342333231304140ULL
#define UD6 0x2726373635343938ULL
#define UD7 0x3D3C2D2C2B2A3B3AULL
#define UD8 0x4B4A4D4C47464948ULL
#define UD9 0x6362535251506160ULL
#define UD10 0x4559445755545856ULL
#define UD11 0x6D6C5D5C5B5A6B6AULL
#define UD12 0x6469656665646867ULL
#define UD13 0x6469656665646867ULL
#define UD14 0x6469656665646867ULL
#define UD15 0x6469656665646867ULL
#define CLOB "v24", "v25", "v26", "v27", "v28", "v29", "v30", "v31", "v32", "v33", "v34", "v35", "v36", "v37", "v38", "v39", "v40", "v41", "v42", "v43", "v44", "v45", "v46", "v47", "v48", "v49", "v50", "v51", "v52", "v53", "v54", "v55", "v56", "v57", "v58", "v59", "v60", "v61", "v62", "v63", "v64", "v65", "v66", "v67", "v68", "v69", "v70", "v71", "v72", "v73", "v74", "v75", "v76", "v77", "v78", "v79", "v80", "v81", "v82", "v83", "v84", "v85", "v86", "v87", "v88", "v89", "v90", "v91", "v92", "v93", "v94", "v95", "v96", "v97", "v98", "v99", "v100", "v101", "v102", "v103", "v104", "v105", "v106", "v107", "v108", "v109", "v110", "v111", "v112", "v113", "v114", "v115", "v116", "v117", "v118", "v119", "v120", "v121", "v122", "v123", "v124", "v125", "v126", "v127", "v128", "v129", "v130", "v131", "v132", "v133", "v134", "v135", "v136", "v137", "v138", "v139", "v140", "v141", "v142", "v143", "v144", "v145", "v146", "v147", "v148", "v149", "v150", "v151", "v152", "v153", "v154", "v155", "v156", "v157", "v158", "v159", "v160", "v161", "v162", "v163", "v164", "v165", "v166", "v167", "v168", "v169", "v170", "v171", "v172", "v173", "v174", "v175", "v176", "v177", "v178", "v179", "v180", "v181", "v182", "v183", "v184", "v185", "v186", "v187", "v188", "v189", "v190", "v191", "v192", "v193", "v194", "v195", "v196", "v197", "v198", "v199", "v200", "v201", "v202", "v203", "v204", "v205", "v206", "v207", "v208", "v209", "v210", "v211", "v212", "v213", "v214", "v215", "v216", "v217", "v218", "v219", "v220", "v221", "v222", "v223", "v224", "v225", "v226", "v227", "v228", "v229", "v230", "v231", "v232", "v233", "v234", "v235", "v236", "v237", "v238", "v239", "v240", "v241", "v242", "v243", "v244", "v245", "v246", "v247", "v248", "v249", "v250", "v251", "v252", "v253", "v254", "v255"
#define PRE "s_and_b32 s40, s44, 3\n" "s_mul_i32 s41, s40, 96\n" "s_add_u32 s46, s46, s41\n" "s_addc_u32 s47, s47, 0\n" "s_mul_i32 s54, s40, 6144\n"
#define LDS_BYTES 163840
#define LOG2E 1.4426950408889634f
#define LP(T, o) ((LDS_AS T*)(size_t)(unsigned)(o))

DI const char* uni(const void* p) {
  unsigned long long sv = (unsigned long long)p;
  unsigned lo = UNI((unsigned)sv), hi = UNI((unsigned)(sv >> 32));
  return (const char*)(((unsigned long long)hi << 32) | lo);
}
#define DMA(src, voff, dst) asm volatile("s_mov_b32 m0, %2\n\ts_nop 0\n\tglobal_load_lds_dwordx4 %0, %1" :: "v"(voff), "s"(src), "s"(dst) : "memory", "m0")
#define DMA4(src, voff, dst) asm volatile("s_mov_b32 m0, %2\n\ts_nop 0\n\tglobal_load_lds_dword %0, %1" :: "v"(voff), "s"(src), "s"(dst) : "memory", "m0")
#define RAWB() asm volatile("s_barrier" ::: "memory")
#define WVM(n) asm volatile("s_waitcnt vmcnt(%0)" :: "n"(n) : "memory")
#define PSTART
#define PEND
#define PPARM
#define SPARM
#define SARG
#define PCLOB

struct Geo { int t0, sr, sc, er, ec, so; unsigned long long pc; };
DI int tok_idx(const Geo& G, int hr, int hc) {
  return G.t0 + G.sr * (hr - (hr >= 17 ? G.er : 0)) + G.sc * (hc - (hc >= 17 ? G.ec : 0));
}
template <int STR, int HB>
DI void mk_lo(int l, unsigned (&lo)[3], int h = 0) {
#pragma unroll
  for (int i = 0; i < 3; ++i) {
    const int idx = 64 * i + l, t = (idx * 171) >> 10, c = idx - 6 * t;
    const int r = HB ? ((t >> 3) & 1) * HB + ((t >> 1) & 8) + (t & 7) : ((t & ~12) | ((t & 4) << 1) | ((t & 8) >> 1));
    lo[i] = r * STR + c * 16 + (HB ? (c < 4 ? 64 * h : 192 + 32 * h) : 0);
    OPQ(lo[i]);
  }
}

DI int lane_id() {
  int l;
  asm volatile("v_mbcnt_lo_u32_b32 %0, -1, 0\n\tv_mbcnt_hi_u32_b32 %0, -1, %0" : "=v"(l));
  return l;
}
struct Regs { unsigned r[12]; u32x8 p; u32x4 q; };
#define XDIR(a0, b0, a1, b1, dst) do { \
  const unsigned xv = xl + UNI(h >= 2 ? a1 * x1 + b1 * x2 : a0 * x1 + b0 * x2); \
  DMA(xs, xv, sXB + (dst)); DMA(xs + 128, xv, sXB + (dst) + 2048); DMA(xs + 256, xv, sXB + (dst) + 4096); \
} while (0)

#define SEL(m, a, b) (__builtin_amdgcn_inverse_ballot_w64(m) ? (a) : (b))
#define Q4(m) (0x1111111111111111ULL * (m))
DI void prologue(const uint16_t* x, const uint16_t* wqkv, const uint16_t* wo, const uint16_t* rpb, const void* keep, const Geo& G, int w, Regs& R) {
  const int l = lane_id() & 63, h = w & 3;
  const int u = l & 15, g = l >> 4;
  const int cu = (blockIdx.x >> 3) & 31;
  const unsigned ra = UNI(1152 * (6 * cu + h)), rb2 = UNI(1152 * (6 * cu + 4 + (h & 1)));
  const int tk = 8 * (h & 1) + (l >> 3);
  unsigned r0 = min(((338 * h) & ~3) + 4 * l, 1348), r1 = min(r0 + 128, 1348u);
  unsigned ck16 = 16 * ((l & 7) ^ (tk >> 1)), xl = (G.t0 + G.sr * (tk >> 2) + G.sc * (tk & 3)) * 768 + ck16;
  const unsigned sXB = UNI(6144 * (h >> 1) + 1024 * (h & 1));
  const unsigned x1 = UNI(3072 * G.sr), x2 = UNI(3072 * G.sc);
  unsigned pk = ((l & 1) ? ra : rb2) + 128 * ((l >> 1) + (l < 12 ? 3 : -6));
  OPQ(r0); OPQ(r1); OPQ(xl); OPQ(pk);
  asm volatile("" :: "s"(ra), "s"(rb2), "s"(sXB), "s"(x1), "s"(x2), "s"(G.so));
  SB;
  asm volatile("" :: "s"(x), "s"(wqkv), "s"(wo), "s"(rpb), "s"(keep));
  SB;
  if (w < 4) {
    const char* rp = uni(rpb);
    DMA4(rp, r0, UNI(RAW + 384 * h));
    DMA4(rp, r1, UNI(RAW + 384 * h + 128));
    asm volatile("s_mov_b32 m0, %4\n\ts_bfm_b64 exec, %5, 0\n\tglobal_load_lds_dword %0, %2\n\ts_mov_b64 exec, 15\n\tglobal_load_lds_dword %1, %3\n\ts_mov_b64 exec, -1"
                 :: "v"(pk), "v"(128 * l), "s"(uni(wqkv)), "s"(G.pc + 128 * UNI(min(16 * cu + 4 * h, PFC - 3))), "s"(PFD), "n"(2 * PN) : "memory", "m0");
  } else {
    const char* xs = uni((const char*)x + G.so);
  XDIR(0, 0, 0, 1, 126976); SB;
  XDIR(1, 0, 1, 1, 139264); SB;
  XDIR(0, 2, 2, 0, 151552); SB;
  }
  SB;
  unsigned lo3[3];
  mk_lo<1152, 0>(l, lo3);
  R.r[0] = lo3[0]; R.r[1] = lo3[1]; R.r[2] = lo3[2];
  unsigned wrow = 6144 * h + (16 * (g >> 1) + 4 * (g & 1) + (u >> 2)) * 96;
  unsigned xa = XR + u * 128 + 16 * (g ^ (u >> 1));
  {
    const int e = min(64 * w + l, 399), blk = e >> 4, a = (blk * 205) >> 10, b = blk - 5 * a;
    *LP(uint16_t, TOFF + 2 * e) = tok_idx(G, 4 * a + ((e >> 2) & 3), 4 * b + (e & 3));
  }
  OPQ(wrow); OPQ(xa);
  SB;
  int w2 = w;
  asm volatile("" : "+s"(w2));
  if (w2 > 3) {
    unsigned lo4[3], lo5[3];
    mk_lo<384, 16>(l, lo4, h); mk_lo<384, 48>(l, lo5, h);
#pragma unroll
    for (int i = 0; i < 3; ++i) { R.r[3 + i] = lo4[i]; R.r[6 + i] = lo5[i]; }
    R.r[9] = 4 * l + BB + 6144 * h; R.r[10] = BB + wrow + 8 * (u & 3); R.r[11] = BB + wrow + 16 * (u & 3);
    R.p = (u32x8){xa, xa ^ 64, 4u * l, 2u * tk, ck16, 16u * l + BB + 6144 * h, 16u * u, 8u * u};
    R.q = (u32x4){XR + 1552u * (u >> 2) + 64u * (u & 3) + 16u * h + 4u * g, 2u * (4 * h + g), 1152u * (6 * cu + SEL(0x1C7ULL, h, 4 + (h & 1))) + 128u * (l % 3) + SEL(0x3FULL, 0u, 768u) - 96 * h,
                  2304u * cu + 128u * min(5 * h + l, 17)};
    const unsigned long long d0 = h == 0 ? UD0 : (h == 1 ? UD4 : (h == 2 ? UD8 : UD12)), d1 = h == 0 ? UD1 : (h == 1 ? UD5 : (h == 2 ? UD9 : UD13));
    const unsigned long long d2 = h == 0 ? UD2 : (h == 1 ? UD6 : (h == 2 ? UD10 : UD14)), d3 = h == 0 ? UD3 : (h == 1 ? UD7 : (h == 2 ? UD11 : UD15));
    const unsigned long long d = g < 2 ? (g == 0 ? d0 : d1) : (g == 2 ? d2 : d3);
    const int by = (int)(d >> (8 * (u >> 1))) & 255, c = by & 15, r = 2 * (by >> 4) + (u & 1);
    const int e = 16 * min(4 * h + g, 12) + u;
    *LP(uint16_t, TOFF + 800 + 2 * e) = G.t0 + G.sr * (r + 3) + G.sc * (c + 3);
    const int cp = 64 * (c & 3) - 16 * (c >> 2);
    const int dl = (G.ec && c >= 11) ? 16 * c + 16 - cp : 0;
    *LP(unsigned, TE + 4 * e) = (unsigned)(TMPA + 64 + cp - 336 * r) | ((unsigned)dl << 16);
  } else {
    const char* wq_ = uni((const char*)wqkv + 96 * h);
    const unsigned sW = UNI(TMPA + 6144 * h);
   SB;
   SB;
  DMA(wq_ + 384, lo3[0], sW + (-24576)); SB;
  DMA(wq_ + 384, lo3[1], sW + (-23552)); SB;
  DMA(wq_ + 384, lo3[2], sW + (-22528)); SB;
    R.r[3] = wrow + 8 * (u & 3); R.r[4] = xa; R.r[5] = xa ^ 64; R.r[6] = 16 * l + 6144 * h; R.r[7] = 6144 * h + 336 * g;
    *LP(uint16_t, PFD + 256) = 0xF180;
    const int row = l >> 2, qd = l & 3, s2 = UNI(2 * G.sc);
    const int rho = row + SEL(0xFFFFFFFFF0000000ULL, -7, 3);
    const int src = UNI(RAW + 384 * h + ((338 * h) & 2) + (G.sr < 0 ? 312 : 0) + (G.sc < 0 ? 24 : 0)) + UNI(G.sr < 0 ? -26 : 26) * rho;
    R.r[8] = sW + 336 * row + 16 * qd + SEL(0xFFFFFFFFF0000000ULL, 96, 64);
    R.r[9] = sW + SEL(0x3FFULL, 336 * l + SEL(0x380ULL, 416, 384), 16 * l + SEL(0x3C00ULL, -160, SEL(0xC000ULL, 2192, 3200)));
    R.r[10] = R.r[11] = 0;
    const int b0 = src + 4 * s2 * qd, b1 = src - s2 * qd;
    unsigned ng = PFD + 256;
    OPQ(ng);
    R.p = (u32x8){SEL(Q4(6), b0, ng), SEL(Q4(6), b0 + s2, ng), SEL(Q4(2), b0 + 2 * s2, ng), SEL(Q4(3), b0 + 3 * s2, ng),
                  SEL(Q4(3), b0 + 4 * s2, ng), SEL(Q4(3), b0 + 5 * s2, ng), SEL(Q4(1), b0 + 6 * s2, ng), 0};
    R.q = (u32x4){SEL(Q4(7), b1 + 8 * s2, ng), SEL(Q4(7), b1 + 2 * s2, ng), SEL(Q4(6), b1 + 3 * s2, ng), SEL(Q4(4), b1 + 4 * s2, ng)};
  }
}
#define BODY(ta, tb) do { \
  const char *pq = uni(wqkv), *px = uni(x), *po = uni(out); \
  asm volatile(PRE "s_waitcnt lgkmcnt(0)\ns_cmp_lt_u32 s44, 4\ns_cbranch_scc0 1f\n" ta "s_branch 2f\n1:\n" tb "2:\n" \
  : "+{v0}"(R.r[0]), "+{v1}"(R.r[1]), "+{v2}"(R.r[2]), "+{v3}"(R.r[3]), "+{v4}"(R.r[4]), "+{v5}"(R.r[5]), "+{v6}"(R.r[6]), "+{v7}"(R.r[7]), \
    "+{v8}"(R.r[8]), "+{v9}"(R.r[9]), "+{v10}"(R.r[10]), "+{v11}"(R.r[11]), "+{v[12:19]}"(R.p), "+{v[20:23]}"(R.q), \
    "+{s[46:47]}"(pq), "+{s[56:57]}"(px), "+{s[66:67]}"(po) \
  : "{s44}"(w), "{s45}"(G.so | G.er), "{s[48:49]}"(uni(wo)) \
  : CLOB, "s40", "s41", "s42", "s43", "s50", "s51", "s52", "s53", "s54", "s55", "s58", "s59", "s60", "s61", "s62", "s63", "s64", "s65", \
    "s68", "s69", "s70", "s71", "s72", "s73", "vcc", "scc", "m0", "memory" PCLOB); } while (0)

__global__ __launch_bounds__(512, 2) void dinat_fused(const uint16_t* __restrict__ x, const uint16_t* __restrict__ wqkv,
                                                      const uint16_t* __restrict__ wo, const uint16_t* __restrict__ rpb,
                                                      uint16_t* __restrict__ out PPARM) {
  __shared__ __attribute__((aligned(256))) char smem[LDS_BYTES];
  PSTART;
  asm volatile("" :: "s"((unsigned)(size_t)(LDS_AS char*)smem));
  const int tid = threadIdx.x;
  const int w = UNI(tid >> 6);
  Geo G;
  asm volatile("s_getpc_b64 %0" : "=s"(G.pc));
  if (CPF) DMA4(G.pc, 128 * ((tid >> 4) + 2), UNI(PFD));
  SB;
  const int bidx = blockIdx.x;
  const int xcd = bidx & 7, idx = bidx >> 3;
  const int ta = 2 * (xcd >> 1) + (idx >> 4), tb = (idx >> 1) & 7;
  G.sr = (ta & 1) ? 224 : -224; G.sc = (tb & 1) ? 1 : -1;
  G.t0 = 224 * (14 * ta + ((ta & 1) ? -3 : 16)) + 14 * tb + ((tb & 1) ? -3 : 16);
  G.er = (ta == 0 || ta == 7) ? 7 : 0; G.ec = (tb == 0 || tb == 7) ? 7 : 0;
  G.so = ((xcd & 1) * 224 + (idx & 1)) * 384;
  {
    Regs R;
    prologue(x, wqkv, wo, rpb, out, G, w, R);
    BODY(
"s_add_u32 s50, s54, 28672\n"
"s_bitcmp1_b32 s45, 0\n"
"s_cselect_b32 s53, -1, 0\n"
"s_lshl_b32 s52, s53, 16\n"
"s_setprio 1\n"
"s_mov_b32 s60, 0x3e553b95\n"
"s_mov_b32 s61, 0x3fb8aa3b\n"
"v_add_u32 v27, 77824, v3\n"
"v_mov_b32 v24, 0\n"
"v_mov_b32 v25, 0\n"
"v_mov_b32 v178, 0xf1800000\n"
"v_mov_b32 v179, 0xf1800000\n"
"v_mov_b32 v180, 0xf1800000\n"
"v_mov_b32 v181, 0xf1800000\n"
"s_bfm_b64 exec, 18, 0\n"
"ds_write_b128 v9, v[178:181] offset:0\n"
"s_mov_b64 exec, -1\n"
"s_waitcnt vmcnt(5)\n"
"ds_read_u16 v13, v13 offset:0\n"
"ds_read_u16 v14, v14 offset:0\n"
"ds_read_u16 v15, v15 offset:0\n"
"ds_read_u16 v16, v16 offset:0\n"
"ds_read_u16 v17, v17 offset:0\n"
"ds_read_u16 v18, v18 offset:0\n"
"ds_read_u16 v12, v12 offset:0\n"
"ds_read_u16 v20, v20 offset:0\n"
"ds_read_u16 v21, v21 offset:0\n"
"ds_read_u16 v22, v22 offset:0\n"
"ds_read_u16 v23, v23 offset:0\n"
"s_waitcnt lgkmcnt(10)\n"
"v_lshlrev_b32 v13, 16, v13\n"
"s_waitcnt lgkmcnt(9)\n"
"v_lshlrev_b32 v14, 16, v14\n"
"v_mul_f32 v13, s61, v13\n"
"s_waitcnt lgkmcnt(8)\n"
"v_lshlrev_b32 v15, 16, v15\n"
"v_mul_f32 v14, s61, v14\n"
"s_waitcnt lgkmcnt(7)\n"
"v_lshlrev_b32 v16, 16, v16\n"
"v_mul_f32 v15, s61, v15\n"
"s_waitcnt lgkmcnt(6)\n"
"v_lshlrev_b32 v17, 16, v17\n"
"v_mul_f32 v16, s61, v16\n"
"s_waitcnt lgkmcnt(5)\n"
"v_lshlrev_b32 v18, 16, v18\n"
"v_mul_f32 v17, s61, v17\n"
"s_waitcnt lgkmcnt(4)\n"
"v_lshlrev_b32 v12, 16, v12\n"
"v_mul_f32 v18, s61, v18\n"
"s_waitcnt lgkmcnt(3)\n"
"v_lshlrev_b32 v20, 16, v20\n"
"v_mul_f32 v12, s61, v12\n"
"s_waitcnt lgkmcnt(2)\n"
"v_lshlrev_b32 v21, 16, v21\n"
"v_mul_f32 v20, s61, v20\n"
"s_waitcnt lgkmcnt(1)\n"
"v_lshlrev_b32 v22, 16, v22\n"
"v_mul_f32 v21, s61, v21\n"
"s_waitcnt lgkmcnt(0)\n"
"v_lshlrev_b32 v23, 16, v23\n"
"v_mul_f32 v22, s61, v22\n"
"v_mul_f32 v23, s61, v23\n"
"v_mov_b32 v178, v13\n"
"v_mov_b32 v179, v14\n"
"v_mov_b32 v180, v15\n"
"v_mov_b32 v181, v16\n"
"v_mov_b32 v182, v17\n"
"v_mov_b32 v183, v18\n"
"s_bfm_b64 exec, 40, 0\n"
"ds_write_b128 v8, v[180:183] offset:0\n"
"ds_write_b128 v8, v[14:17] offset:64\n"
"ds_write_b128 v8, v[178:181] offset:128\n"
"ds_write_b128 v8, v[12:15] offset:192\n"
"ds_write_b128 v8, v[20:23] offset:256\n"
"s_mov_b64 exec, -1\n"
"s_waitcnt lgkmcnt(0)\n"
"s_add_u32 s42, s46, 37248\n"
"s_addc_u32 s43, s47, 0\n"
"s_add_u32 m0, s50, 4294945792\n"
"s_nop 0\n"
"global_load_lds_dwordx4 v0, s[42:43]\n"
"s_add_u32 m0, s50, 4294946816\n"
"s_nop 0\n"
"global_load_lds_dwordx4 v1, s[42:43]\n"
"s_add_u32 m0, s50, 4294947840\n"
"s_nop 0\n"
"global_load_lds_dwordx4 v2, s[42:43]\n"
"s_add_u32 s42, s46, 74112\n"
"s_addc_u32 s43, s47, 0\n"
"s_add_u32 m0, s50, 49152\n"
"s_nop 0\n"
"global_load_lds_dwordx4 v0, s[42:43]\n"
"s_add_u32 m0, s50, 50176\n"
"s_nop 0\n"
"global_load_lds_dwordx4 v1, s[42:43]\n"
"s_add_u32 m0, s50, 51200\n"
"s_nop 0\n"
"global_load_lds_dwordx4 v2, s[42:43]\n"
"s_barrier\n"
"ds_read_b128 v[178:181], v4 offset:24576\n"
"s_waitcnt vmcnt(6)\n"
"ds_read_b64_tr_b16 v[8:9], v3 offset:4096\n"
"ds_read_b64_tr_b16 v[10:11], v3 offset:4864\n"
"ds_read_b64_tr_b16 v[12:13], v3 offset:4128\n"
"ds_read_b64_tr_b16 v[14:15], v3 offset:4896\n"
"ds_read_b64_tr_b16 v[16:17], v3 offset:4160\n"
"ds_read_b64_tr_b16 v[18:19], v3 offset:4928\n"
"ds_read_b128 v[182:185], v4 offset:30720\n"
"ds_read_b128 v[186:189], v4 offset:36864\n"
"s_waitcnt lgkmcnt(6)\n"
"v_mfma_f32_16x16x32_bf16 v[196:199], v[8:11], v[178:181], 0\n"
"s_waitcnt lgkmcnt(4)\n"
"v_mfma_f32_16x16x32_bf16 v[212:215], v[12:15], v[178:181], 0\n"
"s_waitcnt lgkmcnt(2)\n"
"ds_read_b128 v[190:193], v4 offset:43008\n"
"v_mfma_f32_16x16x32_bf16 v[216:219], v[16:19], v[178:181], 0\n"
"s_waitcnt lgkmcnt(2)\n"
"ds_read_b128 v[178:181], v4 offset:49152\n"
"v_mfma_f32_16x16x32_bf16 v[202:205], v[8:11], v[182:185], 0\n"
"v_mfma_f32_16x16x32_bf16 v[220:223], v[12:15], v[182:185], 0\n"
"v_mfma_f32_16x16x32_bf16 v[236:239], v[16:19], v[182:185], 0\n"
"s_waitcnt lgkmcnt(2)\n"
"v_mfma_f32_16x16x32_bf16 v[226:229], v[8:11], v[186:189], 0\n"
"s_waitcnt vmcnt(3)\n"
"ds_read_b64_tr_b16 v[22:23], v3 offset:7936\n"
"v_mfma_f32_16x16x32_bf16 v[240:243], v[12:15], v[186:189], 0\n"
"ds_read_b64_tr_b16 v[20:21], v3 offset:7168\n"
"v_mfma_f32_16x16x32_bf16 v[244:247], v[16:19], v[186:189], 0\n"
"s_waitcnt lgkmcnt(3)\n"
"ds_read_b128 v[182:185], v4 offset:55296\n"
"v_mfma_f32_16x16x32_bf16 v[232:235], v[8:11], v[190:193], 0\n"
"ds_read_b128 v[36:39], v5 offset:36864\n"
"v_mfma_f32_16x16x32_bf16 v[248:251], v[12:15], v[190:193], 0\n"
"ds_read_b64_tr_b16 v[28:29], v3 offset:7200\n"
"v_mfma_f32_16x16x32_bf16 v[252:255], v[16:19], v[190:193], 0\n"
"s_waitcnt lgkmcnt(5)\n"
"ds_read_b128 v[186:189], v5 offset:24576\n"
"v_mfma_f32_16x16x32_bf16 v[208:211], v[8:11], v[178:181], 0\n"
"ds_read_b64_tr_b16 v[30:31], v3 offset:7968\n"
"v_mfma_f32_16x16x32_bf16 v[190:193], v[12:15], v[178:181], 0\n"
"ds_read_b64_tr_b16 v[34:35], v3 offset:8000\n"
"v_mfma_f32_16x16x32_bf16 v[84:87], v[16:19], v[178:181], 0\n"
"s_waitcnt lgkmcnt(5)\n"
"v_mfma_f32_16x16x32_bf16 v[178:181], v[8:11], v[182:185], 0\n"
"ds_read_b64_tr_b16 v[32:33], v3 offset:7232\n"
"v_mfma_f32_16x16x32_bf16 v[88:91], v[12:15], v[182:185], 0\n"
"ds_read_b128 v[40:43], v5 offset:43008\n"
"v_mfma_f32_16x16x32_bf16 v[92:95], v[16:19], v[182:185], 0\n"
"s_waitcnt lgkmcnt(4)\n"
"ds_read_b128 v[182:185], v5 offset:30720\n"
"v_mfma_f32_16x16x32_bf16 v[196:199], v[20:23], v[186:189], v[196:199]\n"
"s_waitcnt lgkmcnt(4)\n"
"v_mfma_f32_16x16x32_bf16 v[212:215], v[28:31], v[186:189], v[212:215]\n"
"s_waitcnt lgkmcnt(2)\n"
"v_mfma_f32_16x16x32_bf16 v[216:219], v[32:35], v[186:189], v[216:219]\n"
"s_waitcnt lgkmcnt(0)\n"
"v_mfma_f32_16x16x32_bf16 v[202:205], v[20:23], v[182:185], v[202:205]\n"
"ds_read_b128 v[186:189], v5 offset:49152\n"
"v_mfma_f32_16x16x32_bf16 v[220:223], v[28:31], v[182:185], v[220:223]\n"
"v_mfma_f32_16x16x32_bf16 v[236:239], v[32:35], v[182:185], v[236:239]\n"
"ds_read_b128 v[182:185], v5 offset:55296\n"
"v_mfma_f32_16x16x32_bf16 v[226:229], v[20:23], v[36:39], v[226:229]\n"
"v_mfma_f32_16x16x32_bf16 v[240:243], v[28:31], v[36:39], v[240:243]\n"
"s_waitcnt vmcnt(0)\n"
"ds_read_b128 v[48:51], v4 offset:26624\n"
"v_mfma_f32_16x16x32_bf16 v[244:247], v[32:35], v[36:39], v[244:247]\n"
"ds_read_b64_tr_b16 v[36:37], v27 offset:0\n"
"v_mfma_f32_16x16x32_bf16 v[232:235], v[20:23], v[40:43], v[232:235]\n"
"v_mfma_f32_16x16x32_bf16 v[248:251], v[28:31], v[40:43], v[248:251]\n"
"ds_read_b64_tr_b16 v[44:45], v27 offset:64\n"
"v_mfma_f32_16x16x32_bf16 v[252:255], v[32:35], v[40:43], v[252:255]\n"
"s_waitcnt lgkmcnt(4)\n"
"ds_read_b64_tr_b16 v[40:41], v27 offset:32\n"
"v_mfma_f32_16x16x32_bf16 v[208:211], v[20:23], v[186:189], v[208:211]\n"
"ds_read_b64_tr_b16 v[38:39], v27 offset:768\n"
"v_mfma_f32_16x16x32_bf16 v[190:193], v[28:31], v[186:189], v[190:193]\n"
"ds_read_b64_tr_b16 v[46:47], v27 offset:832\n"
"v_mfma_f32_16x16x32_bf16 v[84:87], v[32:35], v[186:189], v[84:87]\n"
"s_waitcnt lgkmcnt(6)\n"
"ds_read_b64_tr_b16 v[42:43], v27 offset:800\n"
"v_mfma_f32_16x16x32_bf16 v[178:181], v[20:23], v[182:185], v[178:181]\n"
"ds_read_b128 v[52:55], v4 offset:45056\n"
"v_mfma_f32_16x16x32_bf16 v[88:91], v[28:31], v[182:185], v[88:91]\n"
"v_mfma_f32_16x16x32_bf16 v[92:95], v[32:35], v[182:185], v[92:95]\n"
"s_waitcnt lgkmcnt(3)\n"
"ds_read_b128 v[182:185], v4 offset:32768\n"
"v_mfma_f32_16x16x32_bf16 v[196:199], v[36:39], v[48:51], v[196:199]\n"
"s_waitcnt lgkmcnt(2)\n"
"ds_read_b128 v[186:189], v4 offset:38912\n"
"v_mfma_f32_16x16x32_bf16 v[212:215], v[40:43], v[48:51], v[212:215]\n"
"v_mfma_f32_16x16x32_bf16 v[216:219], v[44:47], v[48:51], v[216:219]\n"
"s_waitcnt lgkmcnt(1)\n"
"v_mfma_f32_16x16x32_bf16 v[202:205], v[36:39], v[182:185], v[202:205]\n"
"ds_read_b128 v[56:59], v4 offset:51200\n"
"v_mfma_f32_16x16x32_bf16 v[220:223], v[40:43], v[182:185], v[220:223]\n"
"s_barrier\n"
"v_mfma_f32_16x16x32_bf16 v[236:239], v[44:47], v[182:185], v[236:239]\n"
"s_waitcnt lgkmcnt(1)\n"
"ds_read_b64_tr_b16 v[48:49], v27 offset:3072\n"
"v_mfma_f32_16x16x32_bf16 v[226:229], v[36:39], v[186:189], v[226:229]\n"
"v_mfma_f32_16x16x32_bf16 v[240:243], v[40:43], v[186:189], v[240:243]\n"
"ds_read_b128 v[182:185], v4 offset:57344\n"
"v_mfma_f32_16x16x32_bf16 v[244:247], v[44:47], v[186:189], v[244:247]\n"
"v_mfma_f32_16x16x32_bf16 v[232:235], v[36:39], v[52:55], v[232:235]\n"
"ds_read_b128 v[64:67], v5 offset:45056\n"
"v_mfma_f32_16x16x32_bf16 v[248:251], v[40:43], v[52:55], v[248:251]\n"
"ds_read_b128 v[186:189], v5 offset:26624\n"
"v_mfma_f32_16x16x32_bf16 v[252:255], v[44:47], v[52:55], v[252:255]\n"
"s_waitcnt lgkmcnt(4)\n"
"ds_read_b64_tr_b16 v[50:51], v27 offset:3840\n"
"v_mfma_f32_16x16x32_bf16 v[208:211], v[36:39], v[56:59], v[208:211]\n"
"ds_read_b64_tr_b16 v[52:53], v27 offset:3104\n"
"v_mfma_f32_16x16x32_bf16 v[190:193], v[40:43], v[56:59], v[190:193]\n"
"v_mfma_f32_16x16x32_bf16 v[84:87], v[44:47], v[56:59], v[84:87]\n"
"s_waitcnt lgkmcnt(4)\n"
"ds_read_b64_tr_b16 v[58:59], v27 offset:3904\n"
"v_mfma_f32_16x16x32_bf16 v[178:181], v[36:39], v[182:185], v[178:181]\n"
"ds_read_b64_tr_b16 v[54:55], v27 offset:3872\n"
"v_mfma_f32_16x16x32_bf16 v[88:91], v[40:43], v[182:185], v[88:91]\n"
"ds_read_b64_tr_b16 v[56:57], v27 offset:3136\n"
"v_mfma_f32_16x16x32_bf16 v[92:95], v[44:47], v[182:185], v[92:95]\n"
"s_waitcnt lgkmcnt(4)\n"
"ds_read_b128 v[182:185], v5 offset:32768\n"
"v_mfma_f32_16x16x32_bf16 v[196:199], v[48:51], v[186:189], v[196:199]\n"
"s_waitcnt lgkmcnt(2)\n"
"ds_read_b128 v[80:83], v4 offset:28672\n"
"v_mfma_f32_16x16x32_bf16 v[212:215], v[52:55], v[186:189], v[212:215]\n"
"s_waitcnt lgkmcnt(2)\n"
"ds_read_b128 v[60:63], v5 offset:38912\n"
"v_mfma_f32_16x16x32_bf16 v[216:219], v[56:59], v[186:189], v[216:219]\n"
"s_waitcnt lgkmcnt(2)\n"
"v_mfma_f32_16x16x32_bf16 v[202:205], v[48:51], v[182:185], v[202:205]\n"
"ds_read_b128 v[186:189], v5 offset:51200\n"
"v_mfma_f32_16x16x32_bf16 v[220:223], v[52:55], v[182:185], v[220:223]\n"
"v_mfma_f32_16x16x32_bf16 v[236:239], v[56:59], v[182:185], v[236:239]\n"
"s_waitcnt lgkmcnt(1)\n"
"v_mfma_f32_16x16x32_bf16 v[226:229], v[48:51], v[60:63], v[226:229]\n"
"ds_read_b128 v[182:185], v5 offset:57344\n"
"v_mfma_f32_16x16x32_bf16 v[240:243], v[52:55], v[60:63], v[240:243]\n"
"v_mfma_f32_16x16x32_bf16 v[244:247], v[56:59], v[60:63], v[244:247]\n"
"s_barrier\n"
"v_mfma_f32_16x16x32_bf16 v[232:235], v[48:51], v[64:67], v[232:235]\n"
"ds_read_b64_tr_b16 v[70:71], v27 offset:25408\n"
"v_mfma_f32_16x16x32_bf16 v[248:251], v[52:55], v[64:67], v[248:251]\n"
"ds_read_b64_tr_b16 v[60:61], v27 offset:24576\n"
"v_mfma_f32_16x16x32_bf16 v[252:255], v[56:59], v[64:67], v[252:255]\n"
"s_waitcnt lgkmcnt(3)\n"
"ds_read_b64_tr_b16 v[64:65], v27 offset:24608\n"
"v_mfma_f32_16x16x32_bf16 v[208:211], v[48:51], v[186:189], v[208:211]\n"
"ds_read_b64_tr_b16 v[66:67], v27 offset:25376\n"
"v_mfma_f32_16x16x32_bf16 v[190:193], v[52:55], v[186:189], v[190:193]\n"
"ds_read_b64_tr_b16 v[62:63], v27 offset:25344\n"
"v_mfma_f32_16x16x32_bf16 v[84:87], v[56:59], v[186:189], v[84:87]\n"
"s_waitcnt lgkmcnt(5)\n"
"ds_read_b64_tr_b16 v[68:69], v27 offset:24640\n"
"v_mfma_f32_16x16x32_bf16 v[178:181], v[48:51], v[182:185], v[178:181]\n"
"ds_read_b64_tr_b16 v[72:73], v27 offset:27648\n"
"v_mfma_f32_16x16x32_bf16 v[88:91], v[52:55], v[182:185], v[88:91]\n"
"ds_read_b64_tr_b16 v[74:75], v27 offset:28416\n"
"v_mfma_f32_16x16x32_bf16 v[92:95], v[56:59], v[182:185], v[92:95]\n"
"s_waitcnt lgkmcnt(3)\n"
"ds_read_b128 v[182:185], v4 offset:34816\n"
"ds_read_b64_tr_b16 v[78:79], v27 offset:28448\n"
"v_mfma_f32_16x16x32_bf16 v[196:199], v[60:63], v[80:83], v[196:199]\n"
"ds_read_b64_tr_b16 v[76:77], v27 offset:27680\n"
"v_mfma_f32_16x16x32_bf16 v[212:215], v[64:67], v[80:83], v[212:215]\n"
"s_waitcnt lgkmcnt(5)\n"
"v_mfma_f32_16x16x32_bf16 v[216:219], v[68:71], v[80:83], v[216:219]\n"
"s_waitcnt lgkmcnt(2)\n"
"ds_read_b64_tr_b16 v[82:83], v27 offset:28480\n"
"ds_read_b64_tr_b16 v[80:81], v27 offset:27712\n"
"v_mfma_f32_16x16x32_bf16 v[202:205], v[60:63], v[182:185], v[202:205]\n"
"v_mfma_f32_16x16x32_bf16 v[220:223], v[64:67], v[182:185], v[220:223]\n"
"ds_read_b128 v[186:189], v4 offset:40960\n"
"ds_read_b128 v[108:111], v4 offset:53248\n"
"v_mfma_f32_16x16x32_bf16 v[236:239], v[68:71], v[182:185], v[236:239]\n"
"s_waitcnt lgkmcnt(2)\n"
"s_barrier\n"
"ds_read_b128 v[96:99], v4 offset:47104\n"
"s_waitcnt lgkmcnt(2)\n"
"v_mfma_f32_16x16x32_bf16 v[226:229], v[60:63], v[186:189], v[226:229]\n"
"ds_read_b128 v[100:103], v5 offset:34816\n"
"v_mfma_f32_16x16x32_bf16 v[240:243], v[64:67], v[186:189], v[240:243]\n"
"v_mfma_f32_16x16x32_bf16 v[244:247], v[68:71], v[186:189], v[244:247]\n"
"s_waitcnt lgkmcnt(1)\n"
"v_mfma_f32_16x16x32_bf16 v[232:235], v[60:63], v[96:99], v[232:235]\n"
"ds_read_b128 v[182:185], v5 offset:28672\n"
"ds_read_b128 v[116:119], v4 offset:59392\n"
"v_mfma_f32_16x16x32_bf16 v[248:251], v[64:67], v[96:99], v[248:251]\n"
"v_mfma_f32_16x16x32_bf16 v[252:255], v[68:71], v[96:99], v[252:255]\n"
"ds_read_b128 v[186:189], v5 offset:47104\n"
"ds_read_b128 v[104:107], v5 offset:40960\n"
"ds_read_b128 v[112:115], v5 offset:53248\n"
"v_mfma_f32_16x16x32_bf16 v[208:211], v[60:63], v[108:111], v[208:211]\n"
"v_mfma_f32_16x16x32_bf16 v[190:193], v[64:67], v[108:111], v[190:193]\n"
"ds_read_b128 v[120:123], v5 offset:59392\n"
"v_mfma_f32_16x16x32_bf16 v[84:87], v[68:71], v[108:111], v[84:87]\n"
"s_waitcnt lgkmcnt(4)\n"
"v_mfma_f32_16x16x32_bf16 v[178:181], v[60:63], v[116:119], v[178:181]\n"
"v_mfma_f32_16x16x32_bf16 v[88:91], v[64:67], v[116:119], v[88:91]\n"
"v_mfma_f32_16x16x32_bf16 v[92:95], v[68:71], v[116:119], v[92:95]\n"
"v_mfma_f32_16x16x32_bf16 v[196:199], v[72:75], v[182:185], v[196:199]\n"
"v_mfma_f32_16x16x32_bf16 v[212:215], v[76:79], v[182:185], v[212:215]\n"
"v_mfma_f32_16x16x32_bf16 v[216:219], v[80:83], v[182:185], v[216:219]\n"
"v_mfma_f32_16x16x32_bf16 v[202:205], v[72:75], v[100:103], v[202:205]\n"
"v_mfma_f32_16x16x32_bf16 v[220:223], v[76:79], v[100:103], v[220:223]\n"
"v_mfma_f32_16x16x32_bf16 v[236:239], v[80:83], v[100:103], v[236:239]\n"
"s_waitcnt lgkmcnt(2)\n"
"v_mfma_f32_16x16x32_bf16 v[226:229], v[72:75], v[104:107], v[226:229]\n"
"v_mfma_f32_16x16x32_bf16 v[240:243], v[76:79], v[104:107], v[240:243]\n"
"v_mfma_f32_16x16x32_bf16 v[244:247], v[80:83], v[104:107], v[244:247]\n"
"v_mfma_f32_16x16x32_bf16 v[232:235], v[72:75], v[186:189], v[232:235]\n"
"v_mfma_f32_16x16x32_bf16 v[248:251], v[76:79], v[186:189], v[248:251]\n"
"v_mfma_f32_16x16x32_bf16 v[252:255], v[80:83], v[186:189], v[252:255]\n"
"s_waitcnt lgkmcnt(1)\n"
"v_mfma_f32_16x16x32_bf16 v[208:211], v[72:75], v[112:115], v[208:211]\n"
"v_mfma_f32_16x16x32_bf16 v[190:193], v[76:79], v[112:115], v[190:193]\n"
"s_nop 1\n"
"v_cvt_pk_bf16_f32 v231, v246, v247\n"
"v_mfma_f32_16x16x32_bf16 v[84:87], v[80:83], v[112:115], v[84:87]\n"
"s_waitcnt lgkmcnt(0)\n"
"v_cvt_pk_bf16_f32 v206, v236, v237\n"
"v_cvt_pk_bf16_f32 v196, v196, v197\n"
"v_mfma_f32_16x16x32_bf16 v[178:181], v[72:75], v[120:123], v[178:181]\n"
"v_mfma_f32_16x16x32_bf16 v[88:91], v[76:79], v[120:123], v[88:91]\n"
"v_mfma_f32_16x16x32_bf16 v[92:95], v[80:83], v[120:123], v[92:95]\n"
"s_barrier\n"
"v_cvt_pk_bf16_f32 v197, v198, v199\n"
"v_cvt_pk_bf16_f32 v237, v254, v255\n"
"v_cvt_pk_bf16_f32 v208, v208, v209\n"
"v_cvt_pk_bf16_f32 v226, v226, v227\n"
"v_cvt_pk_bf16_f32 v230, v244, v245\n"
"v_cvt_pk_bf16_f32 v201, v218, v219\n"
"s_add_u32 s42, s46, 0\n"
"s_addc_u32 s43, s47, 0\n"
"s_add_u32 m0, s50, 24576\n"
"s_nop 0\n"
"global_load_lds_dwordx4 v0, s[42:43]\n"
"v_cvt_pk_bf16_f32 v227, v228, v229\n"
"v_cvt_pk_bf16_f32 v232, v232, v233\n"
"v_cvt_pk_bf16_f32 v233, v234, v235\n"
"v_cvt_pk_bf16_f32 v198, v212, v213\n"
"v_cvt_pk_bf16_f32 v200, v216, v217\n"
"s_add_u32 m0, s50, 25600\n"
"s_nop 0\n"
"global_load_lds_dwordx4 v1, s[42:43]\n"
"s_add_u32 m0, s50, 26624\n"
"s_nop 0\n"
"global_load_lds_dwordx4 v2, s[42:43]\n"
"v_cvt_pk_bf16_f32 v202, v202, v203\n"
"v_cvt_pk_bf16_f32 v203, v204, v205\n"
"v_cvt_pk_bf16_f32 v205, v222, v223\n"
"v_cvt_pk_bf16_f32 v209, v210, v211\n"
"v_cvt_pk_bf16_f32 v183, v94, v95\n"
"s_barrier\n"
"v_cvt_pk_bf16_f32 v178, v178, v179\n"
"s_add_u32 s42, s46, 36864\n"
"s_addc_u32 s43, s47, 0\n"
"s_add_u32 m0, s50, 27648\n"
"s_nop 0\n"
"global_load_lds_dwordx4 v0, s[42:43]\n"
"s_add_u32 m0, s50, 28672\n"
"s_nop 0\n"
"global_load_lds_dwordx4 v1, s[42:43]\n"
"v_cvt_pk_bf16_f32 v179, v180, v181\n"
"s_add_u32 m0, s50, 29696\n"
"s_nop 0\n"
"global_load_lds_dwordx4 v2, s[42:43]\n"
"v_cvt_pk_bf16_f32 v235, v250, v251\n"
"s_add_u32 s42, s46, 73728\n"
"s_addc_u32 s43, s47, 0\n"
"s_add_u32 m0, s50, 4294942720\n"
"s_nop 0\n"
"global_load_lds_dwordx4 v0, s[42:43]\n"
"v_cvt_pk_bf16_f32 v234, v248, v249\n"
"s_add_u32 m0, s50, 4294943744\n"
"s_nop 0\n"
"global_load_lds_dwordx4 v1, s[42:43]\n"
"v_cvt_pk_bf16_f32 v212, v84, v85\n"
"v_cvt_pk_bf16_f32 v180, v88, v89\n"
"v_cvt_pk_bf16_f32 v213, v86, v87\n"
"v_cvt_pk_bf16_f32 v181, v90, v91\n"
"s_add_u32 m0, s50, 4294944768\n"
"s_nop 0\n"
"global_load_lds_dwordx4 v2, s[42:43]\n"
"s_add_u32 s42, s46, 110592\n"
"s_addc_u32 s43, s47, 0\n"
"s_add_u32 m0, s50, 4294945792\n"
"s_nop 0\n"
"global_load_lds_dwordx4 v0, s[42:43]\n"
"v_cvt_pk_bf16_f32 v211, v192, v193\n"
"v_cvt_pk_bf16_f32 v210, v190, v191\n"
"s_add_u32 m0, s50, 4294946816\n"
"s_nop 0\n"
"global_load_lds_dwordx4 v1, s[42:43]\n"
"s_add_u32 m0, s50, 4294947840\n"
"s_nop 0\n"
"global_load_lds_dwordx4 v2, s[42:43]\n"
"v_cvt_pk_bf16_f32 v199, v214, v215\n"
"s_add_u32 s42, s46, 147456\n"
"s_addc_u32 s43, s47, 0\n"
"s_add_u32 m0, s50, 49152\n"
"s_nop 0\n"
"global_load_lds_dwordx4 v0, s[42:43]\n"
"s_add_u32 m0, s50, 50176\n"
"s_nop 0\n"
"global_load_lds_dwordx4 v1, s[42:43]\n"
"v_cvt_pk_bf16_f32 v228, v240, v241\n"
"s_add_u32 m0, s50, 51200\n"
"s_nop 0\n"
"global_load_lds_dwordx4 v2, s[42:43]\n"
"s_add_u32 s42, s46, 184320\n"
"s_addc_u32 s43, s47, 0\n"
"s_add_u32 m0, s50, 52224\n"
"s_nop 0\n"
"global_load_lds_dwordx4 v0, s[42:43]\n"
"v_cvt_pk_bf16_f32 v207, v238, v239\n"
"s_add_u32 m0, s50, 53248\n"
"s_nop 0\n"
"global_load_lds_dwordx4 v1, s[42:43]\n"
"v_cvt_pk_bf16_f32 v204, v220, v221\n"
"s_add_u32 m0, s50, 54272\n"
"s_nop 0\n"
"global_load_lds_dwordx4 v2, s[42:43]\n"
"v_cvt_pk_bf16_f32 v236, v252, v253\n"
"s_barrier\n"
"ds_read_b128 v[184:187], v4 offset:6144\n"
"s_waitcnt vmcnt(15)\n"
"ds_read_b64_tr_b16 v[86:87], v3 offset:54016\n"
"s_waitcnt vmcnt(12)\n"
"ds_read_b64_tr_b16 v[96:97], v3 offset:56320\n"
"ds_read_b128 v[188:191], v5 offset:6144\n"
"v_cvt_pk_bf16_f32 v229, v242, v243\n"
"s_waitcnt lgkmcnt(3)\n"
"v_mfma_f32_16x16x32_bf16 v[238:241], v[8:11], v[184:187], 0\n"
"v_cvt_pk_bf16_f32 v182, v92, v93\n"
"s_waitcnt vmcnt(9)\n"
"s_sub_u32 m0, s50, 28672\n"
"v_mfma_f32_16x16x32_bf16 v[214:217], v[12:15], v[184:187], 0\n"
"ds_read_b64_tr_b16 v[104:105], v3 offset:4096\n"
"s_waitcnt vmcnt(6)\n"
"v_mfma_f32_16x16x32_bf16 v[218:221], v[16:19], v[184:187], 0\n"
"s_waitcnt lgkmcnt(1)\n"
"v_mfma_f32_16x16x32_bf16 v[238:241], v[20:23], v[188:191], v[238:241]\n"
"ds_read_b64_tr_b16 v[88:89], v3 offset:53280\n"
"v_mfma_f32_16x16x32_bf16 v[214:217], v[28:31], v[188:191], v[214:217]\n"
"ds_read_b64_tr_b16 v[106:107], v3 offset:4864\n"
"ds_read_b128 v[192:195], v4 offset:8192\n"
"ds_read_b64_tr_b16 v[222:223], v3 offset:56384\n"
"ds_read_b64_tr_b16 v[94:95], v3 offset:54080\n"
"ds_read_b64_tr_b16 v[92:93], v3 offset:53312\n"
"ds_read_b64_tr_b16 v[102:103], v3 offset:57120\n"
"v_mfma_f32_16x16x32_bf16 v[218:221], v[32:35], v[188:191], v[218:221]\n"
"s_waitcnt lgkmcnt(4)\n"
"ds_read_b64_tr_b16 v[224:225], v3 offset:57152\n"
"v_mfma_f32_16x16x32_bf16 v[238:241], v[36:39], v[192:195], v[238:241]\n"
"ds_read_b64_tr_b16 v[244:245], v3 offset:4160\n"
"ds_read_b128 v[184:187], v5 offset:8192\n"
"ds_read_b64_tr_b16 v[114:115], v3 offset:7936\n"
"v_mfma_f32_16x16x32_bf16 v[214:217], v[40:43], v[192:195], v[214:217]\n"
"ds_read_b64_tr_b16 v[100:101], v3 offset:56352\n"
"ds_read_b64_tr_b16 v[98:99], v3 offset:57088\n"
"ds_read_b64_tr_b16 v[84:85], v3 offset:53248\n"
"v_mfma_f32_16x16x32_bf16 v[218:221], v[44:47], v[192:195], v[218:221]\n"
"ds_read_b64_tr_b16 v[90:91], v3 offset:54048\n"
"s_waitcnt vmcnt(3)\n"
"ds_read_b128 v[188:191], v4 offset:10240\n"
"ds_read_b64_tr_b16 v[116:117], v3 offset:7200\n"
"s_waitcnt lgkmcnt(7)\n"
"v_mfma_f32_16x16x32_bf16 v[238:241], v[48:51], v[184:187], v[238:241]\n"
"ds_read_b64_tr_b16 v[108:109], v3 offset:4128\n"
"ds_read_b64_tr_b16 v[110:111], v3 offset:4896\n"
"v_mfma_f32_16x16x32_bf16 v[214:217], v[52:55], v[184:187], v[214:217]\n"
"ds_read_b64_tr_b16 v[248:249], v3 offset:7232\n"
"ds_read_b128 v[192:195], v5 offset:10240\n"
"v_mfma_f32_16x16x32_bf16 v[218:221], v[56:59], v[184:187], v[218:221]\n"
"s_waitcnt lgkmcnt(5)\n"
"ds_read_b64_tr_b16 v[112:113], v3 offset:7168\n"
"ds_read_b64_tr_b16 v[246:247], v3 offset:4928\n"
"v_mfma_f32_16x16x32_bf16 v[238:241], v[60:63], v[188:191], v[238:241]\n"
"ds_read_b64_tr_b16 v[250:251], v3 offset:8000\n"
"ds_read_b64_tr_b16 v[120:121], v27 offset:32\n"
"v_mfma_f32_16x16x32_bf16 v[214:217], v[64:67], v[188:191], v[214:217]\n"
"ds_read_b128 v[184:187], v4 offset:0\n"
"v_mfma_f32_16x16x32_bf16 v[218:221], v[68:71], v[188:191], v[218:221]\n"
"ds_read_b128 v[188:191], v5 offset:0\n"
"ds_read_b64_tr_b16 v[118:119], v3 offset:7968\n"
"s_waitcnt vmcnt(0)\n"
"ds_read_b64_tr_b16 v[130:131], v27 offset:3872\n"
"s_waitcnt lgkmcnt(8)\n"
"v_mfma_f32_16x16x32_bf16 v[238:241], v[72:75], v[192:195], v[238:241]\n"
"ds_read_b64_tr_b16 v[0:1], v27 offset:0\n"
"ds_read_b64_tr_b16 v[122:123], v27 offset:800\n"
"ds_read_b64_tr_b16 v[2:3], v27 offset:768\n"
"v_mfma_f32_16x16x32_bf16 v[214:217], v[76:79], v[192:195], v[214:217]\n"
"ds_read_b64_tr_b16 v[254:255], v27 offset:832\n"
"ds_read_b64_tr_b16 v[128:129], v27 offset:3104\n"
"v_mfma_f32_16x16x32_bf16 v[218:221], v[80:83], v[192:195], v[218:221]\n"
"s_waitcnt lgkmcnt(8)\n"
"ds_read_b64_tr_b16 v[252:253], v27 offset:64\n"
"v_cvt_pk_bf16_f32 v238, v238, v239\n"
"v_mfma_f32_16x16x32_bf16 v[132:135], v[84:87], v[184:187], 0\n"
"ds_read_b64_tr_b16 v[126:127], v27 offset:3840\n"
"v_cvt_pk_bf16_f32 v239, v240, v241\n"
"v_cvt_pk_bf16_f32 v240, v214, v215\n"
"v_mfma_f32_16x16x32_bf16 v[192:195], v[88:91], v[184:187], 0\n"
"ds_read_b64_tr_b16 v[214:215], v27 offset:3136\n"
"v_cvt_pk_bf16_f32 v243, v220, v221\n"
"v_cvt_pk_bf16_f32 v242, v218, v219\n"
"v_mfma_f32_16x16x32_bf16 v[218:221], v[92:95], v[184:187], 0\n"
"s_waitcnt lgkmcnt(10)\n"
"ds_read_b128 v[184:187], v4 offset:2048\n"
"v_cvt_pk_bf16_f32 v241, v216, v217\n"
"v_mfma_f32_16x16x32_bf16 v[132:135], v[96:99], v[188:191], v[132:135]\n"
"ds_read_b64_tr_b16 v[216:217], v27 offset:3904\n"
"v_mfma_f32_16x16x32_bf16 v[192:195], v[100:103], v[188:191], v[192:195]\n"
"ds_read_b64_tr_b16 v[124:125], v27 offset:3072\n"
"ds_read_b128 v[136:139], v5 offset:2048\n"
"v_mfma_f32_16x16x32_bf16 v[218:221], v[222:225], v[188:191], v[218:221]\n"
"s_waitcnt lgkmcnt(3)\n"
"v_mfma_f32_16x16x32_bf16 v[132:135], v[104:107], v[184:187], v[132:135]\n"
"v_mfma_f32_16x16x32_bf16 v[192:195], v[108:111], v[184:187], v[192:195]\n"
"v_mfma_f32_16x16x32_bf16 v[218:221], v[244:247], v[184:187], v[218:221]\n"
"s_waitcnt lgkmcnt(0)\n"
"v_mfma_f32_16x16x32_bf16 v[132:135], v[112:115], v[136:139], v[132:135]\n"
"ds_read_b128 v[188:191], v4 offset:4096\n"
"v_mfma_f32_16x16x32_bf16 v[192:195], v[116:119], v[136:139], v[192:195]\n"
"s_barrier\n"
"v_bfe_u32 v177, v6, 2, 6\n"
"ds_read_b128 v[184:187], v5 offset:4096\n"
"v_mfma_f32_16x16x32_bf16 v[218:221], v[248:251], v[136:139], v[218:221]\n"
"s_waitcnt lgkmcnt(1)\n"
"ds_read_b32 v177, v177 offset:1216\n"
"v_mfma_f32_16x16x32_bf16 v[132:135], v[0:3], v[188:191], v[132:135]\n"
"v_mfma_f32_16x16x32_bf16 v[192:195], v[120:123], v[188:191], v[192:195]\n"
"v_mfma_f32_16x16x32_bf16 v[218:221], v[252:255], v[188:191], v[218:221]\n"
"s_waitcnt lgkmcnt(1)\n"
"v_mfma_f32_16x16x32_bf16 v[132:135], v[124:127], v[184:187], v[132:135]\n"
"ds_read_b128 v[136:139], v4 offset:12288\n"
"v_mfma_f32_16x16x32_bf16 v[192:195], v[128:131], v[184:187], v[192:195]\n"
"s_waitcnt lgkmcnt(1)\n"
"ds_read_b128 v[140:143], v4 offset:14336\n"
"ds_read_b128 v[188:191], v5 offset:12288\n"
"v_add_u32_sdwa v176, v177, v7 dst_sel:DWORD dst_unused:UNUSED_PAD src0_sel:WORD_0 src1_sel:DWORD\n"
"v_mfma_f32_16x16x32_bf16 v[218:221], v[214:217], v[184:187], v[218:221]\n"
"s_waitcnt lgkmcnt(2)\n"
"v_mfma_f32_16x16x32_bf16 v[184:187], v[8:11], v[136:139], 0\n"
"s_add_u32 s68, s50, 0\n"
"v_mul_f32 v133, s60, v133\n"
"v_mfma_f32_16x16x32_bf16 v[144:147], v[12:15], v[136:139], 0\n"
"v_mul_f32 v134, s60, v134\n"
"v_max_u32 v177, s68, v176\n"
"v_mfma_f32_16x16x32_bf16 v[152:155], v[16:19], v[136:139], 0\n"
"s_waitcnt lgkmcnt(0)\n"
"ds_read_b128 v[136:139], v5 offset:14336\n"
"v_mul_f32 v135, s60, v135\n"
"v_mfma_f32_16x16x32_bf16 v[184:187], v[20:23], v[188:191], v[184:187]\n"
"v_mul_f32 v132, s60, v132\n"
"v_mul_f32 v193, s60, v193\n"
"v_mfma_f32_16x16x32_bf16 v[144:147], v[28:31], v[188:191], v[144:147]\n"
"ds_read_b128 v[148:151], v177 offset:16\n"
"v_mul_f32 v194, s60, v194\n"
"v_mul_f32 v192, s60, v192\n"
"v_cvt_pk_bf16_f32 v132, v132, v133\n"
"v_mfma_f32_16x16x32_bf16 v[152:155], v[32:35], v[188:191], v[152:155]\n"
"ds_read_b128 v[188:191], v4 offset:16384\n"
"v_mul_f32 v219, s60, v219\n"
"v_mfma_f32_16x16x32_bf16 v[184:187], v[36:39], v[140:143], v[184:187]\n"
"v_cvt_pk_bf16_f32 v133, v134, v135\n"
"v_cvt_pk_bf16_f32 v134, v192, v193\n"
"v_mfma_f32_16x16x32_bf16 v[144:147], v[40:43], v[140:143], v[144:147]\n"
"v_mul_f32 v195, s60, v195\n"
"v_cvt_pk_bf16_f32 v135, v194, v195\n"
"v_mul_f32 v220, s60, v220\n"
"v_mul_f32 v221, s60, v221\n"
"v_mul_f32 v218, s60, v218\n"
"v_cvt_pk_bf16_f32 v27, v220, v221\n"
"v_cvt_pk_bf16_f32 v26, v218, v219\n"
"ds_read_b128 v[218:221], v177 offset:32\n"
"s_waitcnt lgkmcnt(2)\n"
"v_mfma_f32_16x16x32_bf16 v[148:151], v[202:205], v[132:135], v[148:151]\n"
"s_add_u32 s69, s50, 1040\n"
"v_min_u32 v175, s69, v176\n"
"v_mfma_f32_16x16x32_bf16 v[148:151], v[204:207], v[24:27], v[148:151]\n"
"ds_write_b128 v6, v[222:225] offset:5120\n"
"v_mfma_f32_16x16x32_bf16 v[152:155], v[44:47], v[140:143], v[152:155]\n"
"v_mfma_f32_16x16x32_bf16 v[184:187], v[48:51], v[136:139], v[184:187]\n"
"v_mfma_f32_16x16x32_bf16 v[144:147], v[52:55], v[136:139], v[144:147]\n"
"v_mfma_f32_16x16x32_bf16 v[152:155], v[56:59], v[136:139], v[152:155]\n"
"s_waitcnt lgkmcnt(2)\n"
"ds_read_b128 v[192:195], v5 offset:16384\n"
"v_mfma_f32_16x16x32_bf16 v[184:187], v[60:63], v[188:191], v[184:187]\n"
"ds_read_b128 v[140:143], v175 offset:1344\n"
"ds_read_b128 v[136:139], v4 offset:18432\n"
"v_mfma_f32_16x16x32_bf16 v[144:147], v[64:67], v[188:191], v[144:147]\n"
"s_waitcnt lgkmcnt(4)\n"
"v_mfma_f32_16x16x32_bf16 v[218:221], v[208:211], v[132:135], v[218:221]\n"
"ds_read_b128 v[156:159], v5 offset:18432\n"
"v_mfma_f32_16x16x32_bf16 v[218:221], v[210:213], v[24:27], v[218:221]\n"
"v_exp_f32 v148, v148\n"
"v_exp_f32 v149, v149\n"
"s_nop 0\n"
"v_cvt_pk_bf16_f32 v148, v148, v149\n"
"v_exp_f32 v150, v150\n"
"v_mfma_f32_16x16x32_bf16 v[152:155], v[68:71], v[188:191], v[152:155]\n"
"ds_write_addtid_b32 v148 offset:53248\n"
"v_exp_f32 v151, v151\n"
"s_waitcnt lgkmcnt(4)\n"
"v_mfma_f32_16x16x32_bf16 v[184:187], v[72:75], v[192:195], v[184:187]\n"
"v_cvt_pk_bf16_f32 v149, v150, v151\n"
"v_mfma_f32_16x16x32_bf16 v[144:147], v[76:79], v[192:195], v[144:147]\n"
"ds_write_addtid_b32 v149 offset:53504\n"
"v_mfma_f32_16x16x32_bf16 v[152:155], v[80:83], v[192:195], v[152:155]\n"
"ds_write_b128 v6, v[244:247] offset:6144\n"
"s_waitcnt lgkmcnt(4)\n"
"v_mfma_f32_16x16x32_bf16 v[190:193], v[8:11], v[136:139], 0\n"
"s_nop 0\n"
"v_cvt_pk_bf16_f32 v184, v184, v185\n"
"v_cvt_pk_bf16_f32 v185, v186, v187\n"
"v_exp_f32 v218, v218\n"
"v_mfma_f32_16x16x32_bf16 v[140:143], v[226:229], v[132:135], v[140:143]\n"
"v_cvt_pk_bf16_f32 v188, v152, v153\n"
"v_cvt_pk_bf16_f32 v187, v146, v147\n"
"v_mfma_f32_16x16x32_bf16 v[140:143], v[228:231], v[24:27], v[140:143]\n"
"v_cvt_pk_bf16_f32 v186, v144, v145\n"
"v_cvt_pk_bf16_f32 v189, v154, v155\n"
"ds_read_b128 v[144:147], v175 offset:1360\n"
"v_mfma_f32_16x16x32_bf16 v[148:151], v[12:15], v[136:139], 0\n"
"ds_read_b128 v[152:155], v4 offset:20480\n"
"v_mfma_f32_16x16x32_bf16 v[160:163], v[16:19], v[136:139], 0\n"
"s_waitcnt lgkmcnt(5)\n"
"v_exp_f32 v219, v219\n"
"v_mfma_f32_16x16x32_bf16 v[190:193], v[20:23], v[156:159], v[190:193]\n"
"v_exp_f32 v140, v140\n"
"v_mfma_f32_16x16x32_bf16 v[148:151], v[28:31], v[156:159], v[148:151]\n"
"v_cvt_pk_bf16_f32 v218, v218, v219\n"
"ds_write_addtid_b32 v218 offset:53760\n"
"ds_read_b128 v[218:221], v5 offset:20480\n"
"v_mfma_f32_16x16x32_bf16 v[160:163], v[32:35], v[156:159], v[160:163]\n"
"s_waitcnt lgkmcnt(3)\n"
"v_exp_f32 v141, v141\n"
"v_mfma_f32_16x16x32_bf16 v[144:147], v[232:235], v[132:135], v[144:147]\n"
"ds_read_b128 v[156:159], v4 offset:22528\n"
"v_exp_f32 v142, v142\n"
"v_mfma_f32_16x16x32_bf16 v[144:147], v[234:237], v[24:27], v[144:147]\n"
"s_waitcnt lgkmcnt(3)\n"
"ds_read_b128 v[136:139], v177 offset:0\n"
"v_mfma_f32_16x16x32_bf16 v[190:193], v[36:39], v[152:155], v[190:193]\n"
"v_exp_f32 v143, v143\n"
"v_mfma_f32_16x16x32_bf16 v[148:151], v[40:43], v[152:155], v[148:151]\n"
"ds_write_b128 v6, v[248:251] offset:7168\n"
"v_cvt_pk_bf16_f32 v140, v140, v141\n"
"v_cvt_pk_bf16_f32 v141, v142, v143\n"
"v_mfma_f32_16x16x32_bf16 v[160:163], v[44:47], v[152:155], v[160:163]\n"
"s_add_u32 s70, s50, 4294966992\n"
"ds_write_addtid_b32 v140 offset:54272\n"
"s_waitcnt lgkmcnt(4)\n"
"v_mfma_f32_16x16x32_bf16 v[190:193], v[48:51], v[218:221], v[190:193]\n"
"ds_write_addtid_b32 v141 offset:54528\n"
"v_exp_f32 v144, v144\n"
"v_exp_f32 v145, v145\n"
"v_mfma_f32_16x16x32_bf16 v[148:151], v[52:55], v[218:221], v[148:151]\n"
"s_waitcnt lgkmcnt(3)\n"
"ds_read_b128 v[140:143], v5 offset:22528\n"
"v_exp_f32 v146, v146\n"
"v_min_u32 v177, s70, v176\n"
"v_mfma_f32_16x16x32_bf16 v[136:139], v[196:199], v[132:135], v[136:139]\n"
"v_exp_f32 v147, v147\n"
"v_mfma_f32_16x16x32_bf16 v[136:139], v[198:201], v[24:27], v[136:139]\n"
"ds_read_b128 v[152:155], v177 offset:2688\n"
"v_cvt_pk_bf16_f32 v144, v144, v145\n"
"v_cvt_pk_bf16_f32 v145, v146, v147\n"
"v_mfma_f32_16x16x32_bf16 v[160:163], v[56:59], v[218:221], v[160:163]\n"
"ds_write_addtid_b32 v144 offset:54784\n"
"ds_write_addtid_b32 v145 offset:55040\n"
"v_mfma_f32_16x16x32_bf16 v[190:193], v[60:63], v[156:159], v[190:193]\n"
"s_nop 0\n"
"v_exp_f32 v136, v136\n"
"v_mfma_f32_16x16x32_bf16 v[148:151], v[64:67], v[156:159], v[148:151]\n"
"v_exp_f32 v137, v137\n"
"v_mfma_f32_16x16x32_bf16 v[160:163], v[68:71], v[156:159], v[160:163]\n"
"s_waitcnt lgkmcnt(3)\n"
"ds_read_b128 v[218:221], v175 offset:1376\n"
"v_mfma_f32_16x16x32_bf16 v[190:193], v[72:75], v[140:143], v[190:193]\n"
"s_waitcnt lgkmcnt(3)\n"
"v_cvt_pk_bf16_f32 v136, v136, v137\n"
"v_mfma_f32_16x16x32_bf16 v[152:155], v[178:181], v[132:135], v[152:155]\n"
"v_exp_f32 v138, v138\n"
"v_mfma_f32_16x16x32_bf16 v[152:155], v[180:183], v[24:27], v[152:155]\n"
"s_waitcnt lgkmcnt(1)\n"
"s_barrier\n"
"v_mfma_f32_16x16x32_bf16 v[148:151], v[76:79], v[140:143], v[148:151]\n"
"ds_read_b128 v[164:167], v4 offset:38912\n"
"ds_read_b128 v[196:199], v4 offset:36864\n"
"v_mfma_f32_16x16x32_bf16 v[160:163], v[80:83], v[140:143], v[160:163]\n"
"ds_write_addtid_b32 v136 offset:58880\n"
"v_cvt_pk_bf16_f32 v190, v190, v191\n"
"v_cvt_pk_bf16_f32 v191, v192, v193\n"
"v_exp_f32 v139, v139\n"
"s_nop 0\n"
"v_cvt_pk_bf16_f32 v137, v138, v139\n"
"v_cvt_pk_bf16_f32 v192, v148, v149\n"
"ds_read_b128 v[144:147], v5 offset:36864\n"
"s_waitcnt lgkmcnt(2)\n"
"v_mfma_f32_16x16x32_bf16 v[140:143], v[84:87], v[196:199], 0\n"
"ds_write_b128 v6, v[252:255] offset:8192\n"
"v_mfma_f32_16x16x32_bf16 v[156:159], v[88:91], v[196:199], 0\n"
"ds_write_addtid_b32 v137 offset:59136\n"
"v_cvt_pk_bf16_f32 v194, v160, v161\n"
"v_mfma_f32_16x16x32_bf16 v[136:139], v[92:95], v[196:199], 0\n"
"v_exp_f32 v152, v152\n"
"v_mfma_f32_16x16x32_bf16 v[218:221], v[238:241], v[132:135], v[218:221]\n"
"ds_read_b128 v[196:199], v177 offset:2704\n"
"v_cvt_pk_bf16_f32 v195, v162, v163\n"
"v_mfma_f32_16x16x32_bf16 v[218:221], v[240:243], v[24:27], v[218:221]\n"
"s_waitcnt lgkmcnt(3)\n"
"v_exp_f32 v153, v153\n"
"v_exp_f32 v154, v154\n"
"v_exp_f32 v155, v155\n"
"v_mfma_f32_16x16x32_bf16 v[140:143], v[96:99], v[144:147], v[140:143]\n"
"v_cvt_pk_bf16_f32 v152, v152, v153\n"
"v_cvt_pk_bf16_f32 v193, v150, v151\n"
"v_mfma_f32_16x16x32_bf16 v[156:159], v[100:103], v[144:147], v[156:159]\n"
"ds_read_b128 v[148:151], v5 offset:38912\n"
"v_mfma_f32_16x16x32_bf16 v[136:139], v[222:225], v[144:147], v[136:139]\n"
"ds_read_b128 v[144:147], v177 offset:2720\n"
"v_mfma_f32_16x16x32_bf16 v[140:143], v[104:107], v[164:167], v[140:143]\n"
"v_exp_f32 v218, v218\n"
"v_mfma_f32_16x16x32_bf16 v[156:159], v[108:111], v[164:167], v[156:159]\n"
"ds_write_addtid_b32 v152 offset:57344\n"
"s_waitcnt lgkmcnt(3)\n"
"v_mfma_f32_16x16x32_bf16 v[196:199], v[184:187], v[132:135], v[196:199]\n"
"ds_read_b128 v[220:223], v4 offset:40960\n"
"v_cvt_pk_bf16_f32 v153, v154, v155\n"
"ds_write_addtid_b32 v153 offset:57600\n"
"v_mfma_f32_16x16x32_bf16 v[196:199], v[186:189], v[24:27], v[196:199]\n"
"v_exp_f32 v219, v219\n"
"v_mfma_f32_16x16x32_bf16 v[136:139], v[244:247], v[164:167], v[136:139]\n"
"s_waitcnt lgkmcnt(4)\n"
"ds_read_b128 v[244:247], v5 offset:40960\n"
"v_mfma_f32_16x16x32_bf16 v[140:143], v[112:115], v[148:151], v[140:143]\n"
"ds_write_b128 v6, v[214:217] offset:9216\n"
"v_mfma_f32_16x16x32_bf16 v[156:159], v[116:119], v[148:151], v[156:159]\n"
"v_bfe_u32 v225, v6, 2, 6\n"
"v_cvt_pk_bf16_f32 v218, v218, v219\n"
"v_mfma_f32_16x16x32_bf16 v[136:139], v[248:251], v[148:151], v[136:139]\n"
"s_waitcnt lgkmcnt(3)\n"
"ds_read_b128 v[248:251], v4 offset:24576\n"
"v_mfma_f32_16x16x32_bf16 v[140:143], v[0:3], v[220:223], v[140:143]\n"
"ds_read_b32 v225, v225 offset:1280\n"
"v_mfma_f32_16x16x32_bf16 v[144:147], v[190:193], v[132:135], v[144:147]\n"
"s_add_u32 s71, s50, 4294967280\n"
"v_mfma_f32_16x16x32_bf16 v[144:147], v[192:195], v[24:27], v[144:147]\n"
"v_exp_f32 v196, v196\n"
"v_mfma_f32_16x16x32_bf16 v[156:159], v[120:123], v[220:223], v[156:159]\n"
"ds_write_addtid_b32 v218 offset:33792\n"
"v_exp_f32 v197, v197\n"
"v_exp_f32 v198, v198\n"
"v_mfma_f32_16x16x32_bf16 v[136:139], v[252:255], v[220:223], v[136:139]\n"
"s_waitcnt lgkmcnt(4)\n"
"ds_read_b128 v[218:221], v5 offset:24576\n"
"v_cvt_pk_bf16_f32 v196, v196, v197\n"
"v_mfma_f32_16x16x32_bf16 v[140:143], v[124:127], v[244:247], v[140:143]\n"
"ds_write_addtid_b32 v196 offset:57856\n"
"v_exp_f32 v199, v199\n"
"v_mfma_f32_16x16x32_bf16 v[156:159], v[128:131], v[244:247], v[156:159]\n"
"s_waitcnt lgkmcnt(3)\n"
"v_cvt_pk_bf16_f32 v197, v198, v199\n"
"v_exp_f32 v144, v144\n"
"v_add_u32_sdwa v224, v225, v7 dst_sel:DWORD dst_unused:UNUSED_PAD src0_sel:WORD_0 src1_sel:DWORD\n"
"v_mfma_f32_16x16x32_bf16 v[136:139], v[214:217], v[244:247], v[136:139]\n"
"ds_write_addtid_b32 v197 offset:58112\n"
"v_mul_f32 v140, s60, v140\n"
"v_mfma_f32_16x16x32_bf16 v[214:217], v[8:11], v[248:251], 0\n"
"ds_read_b128 v[196:199], v4 offset:26624\n"
"v_exp_f32 v145, v145\n"
"v_mul_f32 v142, s60, v142\n"
"v_mul_f32 v157, s60, v157\n"
"v_mfma_f32_16x16x32_bf16 v[252:255], v[12:15], v[248:251], 0\n"
"v_mul_f32 v158, s60, v158\n"
"v_max_u32 v225, s71, v224\n"
"v_mfma_f32_16x16x32_bf16 v[132:135], v[16:19], v[248:251], 0\n"
"s_waitcnt lgkmcnt(3)\n"
"v_cvt_pk_bf16_f32 v144, v144, v145\n"
"v_mul_f32 v139, s60, v139\n"
"v_mfma_f32_16x16x32_bf16 v[214:217], v[20:23], v[218:221], v[214:217]\n"
"ds_write_addtid_b32 v144 offset:58368\n"
"ds_read_b128 v[248:251], v225 offset:16\n"
"v_mul_f32 v141, s60, v141\n"
"v_mul_f32 v138, s60, v138\n"
"v_cvt_pk_bf16_f32 v140, v140, v141\n"
"v_mfma_f32_16x16x32_bf16 v[252:255], v[28:31], v[218:221], v[252:255]\n"
"v_mul_f32 v137, s60, v137\n"
"v_mul_f32 v143, s60, v143\n"
"v_cvt_pk_bf16_f32 v141, v142, v143\n"
"v_mfma_f32_16x16x32_bf16 v[132:135], v[32:35], v[218:221], v[132:135]\n"
"s_waitcnt lgkmcnt(2)\n"
"ds_read_b128 v[244:247], v4 offset:28672\n"
"v_mul_f32 v156, s60, v156\n"
"v_mfma_f32_16x16x32_bf16 v[214:217], v[36:39], v[196:199], v[214:217]\n"
"ds_read_b128 v[218:221], v5 offset:26624\n"
"v_mul_f32 v159, s60, v159\n"
"v_cvt_pk_bf16_f32 v27, v138, v139\n"
"v_cvt_pk_bf16_f32 v142, v156, v157\n"
"v_cvt_pk_bf16_f32 v143, v158, v159\n"
"v_mfma_f32_16x16x32_bf16 v[252:255], v[40:43], v[196:199], v[252:255]\n"
"s_waitcnt lgkmcnt(2)\n"
"v_mul_f32 v136, s60, v136\n"
"v_cvt_pk_bf16_f32 v26, v136, v137\n"
"v_mfma_f32_16x16x32_bf16 v[248:251], v[202:205], v[140:143], v[248:251]\n"
"s_barrier\n"
"v_mfma_f32_16x16x32_bf16 v[248:251], v[204:207], v[24:27], v[248:251]\n"
"ds_read_b128 v[136:139], v225 offset:32\n"
"v_mfma_f32_16x16x32_bf16 v[132:135], v[44:47], v[196:199], v[132:135]\n"
"s_waitcnt lgkmcnt(1)\n"
"ds_read_b128 v[196:199], v5 offset:28672\n"
"v_mfma_f32_16x16x32_bf16 v[214:217], v[48:51], v[218:221], v[214:217]\n"
"v_mfma_f32_16x16x32_bf16 v[252:255], v[52:55], v[218:221], v[252:255]\n"
"v_mfma_f32_16x16x32_bf16 v[132:135], v[56:59], v[218:221], v[132:135]\n"
"ds_read_b128 v[220:223], v4 offset:30720\n"
"v_mfma_f32_16x16x32_bf16 v[214:217], v[60:63], v[244:247], v[214:217]\n"
"v_mfma_f32_16x16x32_bf16 v[252:255], v[64:67], v[244:247], v[252:255]\n"
"v_exp_f32 v248, v248\n"
"v_mfma_f32_16x16x32_bf16 v[132:135], v[68:71], v[244:247], v[132:135]\n"
"s_waitcnt lgkmcnt(1)\n"
"v_mfma_f32_16x16x32_bf16 v[214:217], v[72:75], v[196:199], v[214:217]\n"
"v_mfma_f32_16x16x32_bf16 v[136:139], v[208:211], v[140:143], v[136:139]\n"
"v_exp_f32 v249, v249\n"
"s_add_u32 s72, s50, 1024\n"
"v_cvt_pk_bf16_f32 v248, v248, v249\n"
"v_mfma_f32_16x16x32_bf16 v[136:139], v[210:213], v[24:27], v[136:139]\n"
"ds_read_b128 v[144:147], v5 offset:30720\n"
"ds_write_addtid_b32 v248 offset:55296\n"
"v_min_u32 v201, s72, v224\n"
"v_mfma_f32_16x16x32_bf16 v[252:255], v[76:79], v[196:199], v[252:255]\n"
"v_exp_f32 v250, v250\n"
"v_cvt_pk_bf16_f32 v214, v214, v215\n"
"v_cvt_pk_bf16_f32 v215, v216, v217\n"
"v_mfma_f32_16x16x32_bf16 v[132:135], v[80:83], v[196:199], v[132:135]\n"
"s_waitcnt lgkmcnt(2)\n"
"v_mfma_f32_16x16x32_bf16 v[244:247], v[8:11], v[220:223], 0\n"
"ds_read_b128 v[196:199], v201 offset:1376\n"
"v_mfma_f32_16x16x32_bf16 v[148:151], v[12:15], v[220:223], 0\n"
"ds_read_b128 v[152:155], v4 offset:32768\n"
"v_cvt_pk_bf16_f32 v217, v254, v255\n"
"v_exp_f32 v251, v251\n"
"s_nop 0\n"
"v_cvt_pk_bf16_f32 v249, v250, v251\n"
"v_cvt_pk_bf16_f32 v216, v252, v253\n"
"ds_write_addtid_b32 v249 offset:55552\n"
"v_cvt_pk_bf16_f32 v219, v134, v135\n"
"v_cvt_pk_bf16_f32 v218, v132, v133\n"
"v_mfma_f32_16x16x32_bf16 v[250:253], v[16:19], v[220:223], 0\n"
"s_waitcnt lgkmcnt(4)\n"
"v_mfma_f32_16x16x32_bf16 v[244:247], v[20:23], v[144:147], v[244:247]\n"
"v_exp_f32 v136, v136\n"
"ds_read_b128 v[220:223], v5 offset:32768\n"
"v_exp_f32 v137, v137\n"
"v_mfma_f32_16x16x32_bf16 v[148:151], v[28:31], v[144:147], v[148:151]\n"
"v_cvt_pk_bf16_f32 v136, v136, v137\n"
"ds_write_addtid_b32 v136 offset:55808\n"
"v_mfma_f32_16x16x32_bf16 v[250:253], v[32:35], v[144:147], v[250:253]\n"
"s_waitcnt lgkmcnt(4)\n"
"v_mfma_f32_16x16x32_bf16 v[196:199], v[238:241], v[140:143], v[196:199]\n"
"v_exp_f32 v138, v138\n"
"v_mfma_f32_16x16x32_bf16 v[196:199], v[240:243], v[24:27], v[196:199]\n"
"s_waitcnt lgkmcnt(3)\n"
"ds_read_b128 v[132:135], v4 offset:34816\n"
"v_mfma_f32_16x16x32_bf16 v[244:247], v[36:39], v[152:155], v[244:247]\n"
"v_exp_f32 v139, v139\n"
"ds_read_b128 v[144:147], v201 offset:1360\n"
"v_mfma_f32_16x16x32_bf16 v[148:151], v[40:43], v[152:155], v[148:151]\n"
"v_cvt_pk_bf16_f32 v137, v138, v139\n"
"v_mfma_f32_16x16x32_bf16 v[250:253], v[44:47], v[152:155], v[250:253]\n"
"v_exp_f32 v196, v196\n"
"ds_write_addtid_b32 v137 offset:56064\n"
"s_waitcnt lgkmcnt(4)\n"
"v_mfma_f32_16x16x32_bf16 v[244:247], v[48:51], v[220:223], v[244:247]\n"
"v_mfma_f32_16x16x32_bf16 v[148:151], v[52:55], v[220:223], v[148:151]\n"
"v_exp_f32 v197, v197\n"
"v_mfma_f32_16x16x32_bf16 v[250:253], v[56:59], v[220:223], v[250:253]\n"
"s_waitcnt lgkmcnt(2)\n"
"v_mfma_f32_16x16x32_bf16 v[244:247], v[60:63], v[132:135], v[244:247]\n"
"s_waitcnt lgkmcnt(1)\n"
"v_mfma_f32_16x16x32_bf16 v[144:147], v[232:235], v[140:143], v[144:147]\n"
"ds_read_b128 v[220:223], v4 offset:43008\n"
"ds_read_b128 v[136:139], v5 offset:34816\n"
"v_cvt_pk_bf16_f32 v196, v196, v197\n"
"v_mfma_f32_16x16x32_bf16 v[144:147], v[234:237], v[24:27], v[144:147]\n"
"v_mfma_f32_16x16x32_bf16 v[148:151], v[64:67], v[132:135], v[148:151]\n"
"ds_write_addtid_b32 v196 offset:56320\n"
"v_mfma_f32_16x16x32_bf16 v[250:253], v[68:71], v[132:135], v[250:253]\n"
"s_waitcnt lgkmcnt(1)\n"
"ds_read_b128 v[132:135], v225 offset:48\n"
"v_mfma_f32_16x16x32_bf16 v[244:247], v[72:75], v[136:139], v[244:247]\n"
"v_exp_f32 v198, v198\n"
"v_mfma_f32_16x16x32_bf16 v[148:151], v[76:79], v[136:139], v[148:151]\n"
"ds_read_b128 v[152:155], v5 offset:43008\n"
"v_mfma_f32_16x16x32_bf16 v[250:253], v[80:83], v[136:139], v[250:253]\n"
"v_mfma_f32_16x16x32_bf16 v[136:139], v[84:87], v[220:223], 0\n"
"v_exp_f32 v199, v199\n"
"s_nop 1\n"
"v_cvt_pk_bf16_f32 v244, v244, v245\n"
"v_cvt_pk_bf16_f32 v245, v246, v247\n"
"v_cvt_pk_bf16_f32 v246, v148, v149\n"
"v_cvt_pk_bf16_f32 v197, v198, v199\n"
"ds_write_addtid_b32 v197 offset:56576\n"
"v_cvt_pk_bf16_f32 v247, v150, v151\n"
"v_mfma_f32_16x16x32_bf16 v[148:151], v[88:91], v[220:223], 0\n"
"ds_read_b128 v[156:159], v4 offset:45056\n"
"v_cvt_pk_bf16_f32 v249, v252, v253\n"
"v_cvt_pk_bf16_f32 v248, v250, v251\n"
"ds_read_b128 v[196:199], v6 offset:5120\n"
"s_waitcnt lgkmcnt(2)\n"
"s_barrier\n"
"v_mfma_f32_16x16x32_bf16 v[250:253], v[92:95], v[220:223], 0\n"
"v_exp_f32 v144, v144\n"
"v_mfma_f32_16x16x32_bf16 v[132:135], v[214:217], v[140:143], v[132:135]\n"
"ds_read_b128 v[220:223], v6 offset:6144\n"
"v_mfma_f32_16x16x32_bf16 v[132:135], v[216:219], v[24:27], v[132:135]\n"
"v_exp_f32 v145, v145\n"
"ds_read_b128 v[164:167], v201 offset:1392\n"
"v_mfma_f32_16x16x32_bf16 v[136:139], v[96:99], v[152:155], v[136:139]\n"
"v_exp_f32 v146, v146\n"
"v_mfma_f32_16x16x32_bf16 v[148:151], v[100:103], v[152:155], v[148:151]\n"
"ds_read_b128 v[160:163], v5 offset:45056\n"
"v_cvt_pk_bf16_f32 v144, v144, v145\n"
"ds_write_addtid_b32 v144 offset:57856\n"
"s_waitcnt lgkmcnt(4)\n"
"v_mfma_f32_16x16x32_bf16 v[250:253], v[196:199], v[152:155], v[250:253]\n"
"v_exp_f32 v147, v147\n"
"ds_read_b128 v[196:199], v6 offset:7168\n"
"v_mfma_f32_16x16x32_bf16 v[136:139], v[104:107], v[156:159], v[136:139]\n"
"v_cvt_pk_bf16_f32 v145, v146, v147\n"
"v_mfma_f32_16x16x32_bf16 v[148:151], v[108:111], v[156:159], v[148:151]\n"
"ds_write_addtid_b32 v145 offset:58112\n"
"s_waitcnt lgkmcnt(5)\n"
"v_mfma_f32_16x16x32_bf16 v[250:253], v[220:223], v[156:159], v[250:253]\n"
"s_waitcnt lgkmcnt(3)\n"
"ds_read_b128 v[220:223], v4 offset:47104\n"
"v_exp_f32 v132, v132\n"
"ds_read_b128 v[152:155], v5 offset:47104\n"
"v_mfma_f32_16x16x32_bf16 v[136:139], v[112:115], v[160:163], v[136:139]\n"
"ds_read_b128 v[144:147], v6 offset:8192\n"
"v_mfma_f32_16x16x32_bf16 v[148:151], v[116:119], v[160:163], v[148:151]\n"
"v_mfma_f32_16x16x32_bf16 v[164:167], v[244:247], v[140:143], v[164:167]\n"
"v_exp_f32 v133, v133\n"
"v_mfma_f32_16x16x32_bf16 v[164:167], v[246:249], v[24:27], v[164:167]\n"
"s_waitcnt lgkmcnt(4)\n"
"ds_read_b128 v[156:159], v4 offset:49152\n"
"v_bfe_u32 v255, v6, 2, 6\n"
"v_mfma_f32_16x16x32_bf16 v[250:253], v[196:199], v[160:163], v[250:253]\n"
"ds_read_b128 v[140:143], v6 offset:9216\n"
"s_add_u32 s69, s50, 4294966976\n"
"s_waitcnt lgkmcnt(4)\n"
"v_mfma_f32_16x16x32_bf16 v[136:139], v[0:3], v[220:223], v[136:139]\n"
"v_exp_f32 v134, v134\n"
"v_cvt_pk_bf16_f32 v132, v132, v133\n"
"v_exp_f32 v135, v135\n"
"ds_write_addtid_b32 v132 offset:57344\n"
"v_mfma_f32_16x16x32_bf16 v[148:151], v[120:123], v[220:223], v[148:151]\n"
"s_waitcnt lgkmcnt(3)\n"
"ds_read_b32 v255, v255 offset:1344\n"
"v_mfma_f32_16x16x32_bf16 v[250:253], v[144:147], v[220:223], v[250:253]\n"
"ds_read_b128 v[220:223], v5 offset:49152\n"
"v_mfma_f32_16x16x32_bf16 v[136:139], v[124:127], v[152:155], v[136:139]\n"
"v_cvt_pk_bf16_f32 v133, v134, v135\n"
"ds_write_addtid_b32 v133 offset:57600\n"
"ds_read_b128 v[132:135], v4 offset:51200\n"
"v_mfma_f32_16x16x32_bf16 v[148:151], v[128:131], v[152:155], v[148:151]\n"
"s_waitcnt lgkmcnt(5)\n"
"v_exp_f32 v164, v164\n"
"v_mfma_f32_16x16x32_bf16 v[250:253], v[140:143], v[152:155], v[250:253]\n"
"s_waitcnt lgkmcnt(3)\n"
"v_add_u32_sdwa v225, v255, v7 dst_sel:DWORD dst_unused:UNUSED_PAD src0_sel:WORD_0 src1_sel:DWORD\n"
"s_nop 0\n"
"v_min_u32 v177, s69, v225\n"
"ds_read_b128 v[152:155], v177 offset:2704\n"
"v_mfma_f32_16x16x32_bf16 v[196:199], v[8:11], v[156:159], 0\n"
"v_exp_f32 v165, v165\n"
"v_mfma_f32_16x16x32_bf16 v[140:143], v[12:15], v[156:159], 0\n"
"v_mul_f32 v150, s60, v150\n"
"v_cvt_pk_bf16_f32 v164, v164, v165\n"
"v_mul_f32 v136, s60, v136\n"
"v_mfma_f32_16x16x32_bf16 v[144:147], v[16:19], v[156:159], 0\n"
"s_waitcnt lgkmcnt(3)\n"
"v_mul_f32 v137, s60, v137\n"
"v_exp_f32 v166, v166\n"
"v_mfma_f32_16x16x32_bf16 v[196:199], v[20:23], v[220:223], v[196:199]\n"
"v_mul_f32 v138, s60, v138\n"
"v_exp_f32 v167, v167\n"
"v_mul_f32 v139, s60, v139\n"
"v_cvt_pk_bf16_f32 v165, v166, v167\n"
"v_mfma_f32_16x16x32_bf16 v[140:143], v[28:31], v[220:223], v[140:143]\n"
"ds_write_addtid_b32 v164 offset:56832\n"
"ds_write_addtid_b32 v165 offset:57088\n"
"v_cvt_pk_bf16_f32 v136, v136, v137\n"
"v_mul_f32 v149, s60, v149\n"
"v_mfma_f32_16x16x32_bf16 v[144:147], v[32:35], v[220:223], v[144:147]\n"
"s_waitcnt lgkmcnt(3)\n"
"v_mul_f32 v253, s60, v253\n"
"v_mul_f32 v250, s60, v250\n"
"v_cvt_pk_bf16_f32 v137, v138, v139\n"
"v_mfma_f32_16x16x32_bf16 v[196:199], v[36:39], v[132:135], v[196:199]\n"
"v_mul_f32 v151, s60, v151\n"
"v_cvt_pk_bf16_f32 v139, v150, v151\n"
"v_mul_f32 v148, s60, v148\n"
"v_mfma_f32_16x16x32_bf16 v[140:143], v[40:43], v[132:135], v[140:143]\n"
"ds_read_b128 v[220:223], v5 offset:51200\n"
"v_mul_f32 v252, s60, v252\n"
"v_cvt_pk_bf16_f32 v138, v148, v149\n"
"v_cvt_pk_bf16_f32 v27, v252, v253\n"
"s_waitcnt lgkmcnt(3)\n"
"v_mfma_f32_16x16x32_bf16 v[152:155], v[184:187], v[136:139], v[152:155]\n"
"s_waitcnt lgkmcnt(1)\n"
"s_barrier\n"
"ds_read_b128 v[148:151], v4 offset:53248\n"
"v_mul_f32 v251, s60, v251\n"
"v_cvt_pk_bf16_f32 v26, v250, v251\n"
"s_nop 1\n"
"v_mfma_f32_16x16x32_bf16 v[152:155], v[186:189], v[24:27], v[152:155]\n"
"v_mfma_f32_16x16x32_bf16 v[144:147], v[44:47], v[132:135], v[144:147]\n"
"s_add_u32 s73, s50, 4294967280\n"
"ds_read_b128 v[250:253], v177 offset:2720\n"
"v_max_u32 v176, s73, v225\n"
"s_waitcnt lgkmcnt(2)\n"
"v_mfma_f32_16x16x32_bf16 v[196:199], v[48:51], v[220:223], v[196:199]\n"
"v_mfma_f32_16x16x32_bf16 v[140:143], v[52:55], v[220:223], v[140:143]\n"
"v_mfma_f32_16x16x32_bf16 v[144:147], v[56:59], v[220:223], v[144:147]\n"
"s_waitcnt lgkmcnt(1)\n"
"ds_read_b128 v[220:223], v5 offset:53248\n"
"v_exp_f32 v152, v152\n"
"v_mfma_f32_16x16x32_bf16 v[196:199], v[60:63], v[148:151], v[196:199]\n"
"ds_read_b128 v[132:135], v4 offset:55296\n"
"ds_read_b128 v[156:159], v176 offset:32\n"
"v_mfma_f32_16x16x32_bf16 v[140:143], v[64:67], v[148:151], v[140:143]\n"
"s_waitcnt lgkmcnt(3)\n"
"v_exp_f32 v153, v153\n"
"v_mfma_f32_16x16x32_bf16 v[250:253], v[190:193], v[136:139], v[250:253]\n"
"v_exp_f32 v154, v154\n"
"v_mfma_f32_16x16x32_bf16 v[250:253], v[192:195], v[24:27], v[250:253]\n"
"v_mfma_f32_16x16x32_bf16 v[144:147], v[68:71], v[148:151], v[144:147]\n"
"s_waitcnt lgkmcnt(2)\n"
"ds_read_b128 v[148:151], v5 offset:55296\n"
"v_cvt_pk_bf16_f32 v152, v152, v153\n"
"v_mfma_f32_16x16x32_bf16 v[196:199], v[72:75], v[220:223], v[196:199]\n"
"v_exp_f32 v155, v155\n"
"v_mfma_f32_16x16x32_bf16 v[140:143], v[76:79], v[220:223], v[140:143]\n"
"v_cvt_pk_bf16_f32 v153, v154, v155\n"
"v_mfma_f32_16x16x32_bf16 v[144:147], v[80:83], v[220:223], v[144:147]\n"
"ds_write_addtid_b32 v152 offset:53248\n"
"ds_write_addtid_b32 v153 offset:53504\n"
"v_exp_f32 v250, v250\n"
"s_waitcnt lgkmcnt(4)\n"
"v_mfma_f32_16x16x32_bf16 v[220:223], v[8:11], v[132:135], 0\n"
"s_waitcnt lgkmcnt(3)\n"
"v_cvt_pk_bf16_f32 v196, v196, v197\n"
"v_cvt_pk_bf16_f32 v197, v198, v199\n"
"v_cvt_pk_bf16_f32 v199, v142, v143\n"
"v_mfma_f32_16x16x32_bf16 v[156:159], v[208:211], v[136:139], v[156:159]\n"
"v_cvt_pk_bf16_f32 v198, v140, v141\n"
"v_mfma_f32_16x16x32_bf16 v[156:159], v[210:213], v[24:27], v[156:159]\n"
"v_cvt_pk_bf16_f32 v201, v146, v147\n"
"v_cvt_pk_bf16_f32 v200, v144, v145\n"
"v_mfma_f32_16x16x32_bf16 v[144:147], v[12:15], v[132:135], 0\n"
"ds_read_b128 v[140:143], v176 offset:48\n"
"v_mfma_f32_16x16x32_bf16 v[152:155], v[16:19], v[132:135], 0\n"
"s_waitcnt lgkmcnt(3)\n"
"v_exp_f32 v251, v251\n"
"v_mfma_f32_16x16x32_bf16 v[220:223], v[20:23], v[148:151], v[220:223]\n"
"ds_read_b128 v[132:135], v4 offset:57344\n"
"v_exp_f32 v252, v252\n"
"v_mfma_f32_16x16x32_bf16 v[144:147], v[28:31], v[148:151], v[144:147]\n"
"s_add_u32 s68, s50, 4294965936\n"
"v_cvt_pk_bf16_f32 v250, v250, v251\n"
"v_mfma_f32_16x16x32_bf16 v[152:155], v[32:35], v[148:151], v[152:155]\n"
"v_exp_f32 v253, v253\n"
"ds_write_addtid_b32 v250 offset:53760\n"
"v_cvt_pk_bf16_f32 v251, v252, v253\n"
"ds_write_addtid_b32 v251 offset:54016\n"
"s_waitcnt lgkmcnt(3)\n"
"v_mfma_f32_16x16x32_bf16 v[140:143], v[214:217], v[136:139], v[140:143]\n"
"v_exp_f32 v156, v156\n"
"v_mfma_f32_16x16x32_bf16 v[140:143], v[216:219], v[24:27], v[140:143]\n"
"s_waitcnt lgkmcnt(2)\n"
"ds_read_b128 v[252:255], v5 offset:57344\n"
"v_max_u32 v250, s68, v225\n"
"v_mfma_f32_16x16x32_bf16 v[220:223], v[36:39], v[132:135], v[220:223]\n"
"v_exp_f32 v157, v157\n"
"v_exp_f32 v158, v158\n"
"ds_read_b128 v[148:151], v250 offset:1392\n"
"v_mfma_f32_16x16x32_bf16 v[144:147], v[40:43], v[132:135], v[144:147]\n"
"ds_read_b128 v[160:163], v176 offset:16\n"
"v_mfma_f32_16x16x32_bf16 v[152:155], v[44:47], v[132:135], v[152:155]\n"
"s_waitcnt lgkmcnt(2)\n"
"v_mfma_f32_16x16x32_bf16 v[220:223], v[48:51], v[252:255], v[220:223]\n"
"v_exp_f32 v159, v159\n"
"v_exp_f32 v140, v140\n"
"v_cvt_pk_bf16_f32 v156, v156, v157\n"
"v_cvt_pk_bf16_f32 v157, v158, v159\n"
"v_mfma_f32_16x16x32_bf16 v[144:147], v[52:55], v[252:255], v[144:147]\n"
"s_waitcnt lgkmcnt(1)\n"
"ds_read_b128 v[132:135], v4 offset:59392\n"
"v_exp_f32 v141, v141\n"
"v_mfma_f32_16x16x32_bf16 v[148:151], v[244:247], v[136:139], v[148:151]\n"
"ds_write_addtid_b32 v156 offset:54272\n"
"ds_write_addtid_b32 v157 offset:54528\n"
"ds_read_b128 v[156:159], v5 offset:59392\n"
"v_mfma_f32_16x16x32_bf16 v[148:151], v[246:249], v[24:27], v[148:151]\n"
"v_cvt_pk_bf16_f32 v140, v140, v141\n"
"v_mfma_f32_16x16x32_bf16 v[152:155], v[56:59], v[252:255], v[152:155]\n"
"s_waitcnt lgkmcnt(3)\n"
"v_exp_f32 v142, v142\n"
"v_mfma_f32_16x16x32_bf16 v[220:223], v[60:63], v[132:135], v[220:223]\n"
"ds_read_b128 v[252:255], v4 offset:12288\n"
"v_mfma_f32_16x16x32_bf16 v[144:147], v[64:67], v[132:135], v[144:147]\n"
"v_exp_f32 v143, v143\n"
"v_mfma_f32_16x16x32_bf16 v[152:155], v[68:71], v[132:135], v[152:155]\n"
"s_waitcnt lgkmcnt(1)\n"
"v_cvt_pk_bf16_f32 v141, v142, v143\n"
"v_mfma_f32_16x16x32_bf16 v[220:223], v[72:75], v[156:159], v[220:223]\n"
"ds_write_addtid_b32 v140 offset:54784\n"
"ds_write_addtid_b32 v141 offset:55040\n"
"v_mfma_f32_16x16x32_bf16 v[160:163], v[202:205], v[136:139], v[160:163]\n"
"v_mfma_f32_16x16x32_bf16 v[160:163], v[204:207], v[24:27], v[160:163]\n"
"ds_read_b128 v[202:205], v250 offset:1360\n"
"v_mfma_f32_16x16x32_bf16 v[144:147], v[76:79], v[156:159], v[144:147]\n"
"v_mfma_f32_16x16x32_bf16 v[152:155], v[80:83], v[156:159], v[152:155]\n"
"v_exp_f32 v148, v148\n"
"v_exp_f32 v149, v149\n"
"v_exp_f32 v150, v150\n"
"v_exp_f32 v151, v151\n"
"v_cvt_pk_bf16_f32 v220, v220, v221\n"
"s_waitcnt lgkmcnt(3)\n"
"v_mfma_f32_16x16x32_bf16 v[140:143], v[84:87], v[252:255], 0\n"
"ds_read_b128 v[132:135], v5 offset:12288\n"
"v_cvt_pk_bf16_f32 v148, v148, v149\n"
"ds_write_addtid_b32 v148 offset:33792\n"
"v_cvt_pk_bf16_f32 v149, v150, v151\n"
"ds_write_addtid_b32 v149 offset:34048\n"
"v_cvt_pk_bf16_f32 v221, v222, v223\n"
"v_cvt_pk_bf16_f32 v223, v146, v147\n"
"v_cvt_pk_bf16_f32 v222, v144, v145\n"
"v_mfma_f32_16x16x32_bf16 v[144:147], v[88:91], v[252:255], 0\n"
"v_cvt_pk_bf16_f32 v224, v152, v153\n"
"v_cvt_pk_bf16_f32 v225, v154, v155\n"
"v_mfma_f32_16x16x32_bf16 v[152:155], v[92:95], v[252:255], 0\n"
"s_waitcnt lgkmcnt(3)\n"
"v_exp_f32 v160, v160\n"
"ds_read_b128 v[252:255], v4 offset:14336\n"
"v_mfma_f32_16x16x32_bf16 v[202:205], v[232:235], v[136:139], v[202:205]\n"
"ds_read_b128 v[148:151], v6 offset:5120\n"
"v_mfma_f32_16x16x32_bf16 v[202:205], v[234:237], v[24:27], v[202:205]\n"
"s_waitcnt lgkmcnt(2)\n"
"ds_read_b128 v[156:159], v250 offset:1376\n"
"s_barrier\n"
"v_mfma_f32_16x16x32_bf16 v[140:143], v[96:99], v[132:135], v[140:143]\n"
"v_exp_f32 v161, v161\n"
"v_exp_f32 v162, v162\n"
"v_mfma_f32_16x16x32_bf16 v[144:147], v[100:103], v[132:135], v[144:147]\n"
"s_waitcnt lgkmcnt(1)\n"
"ds_read_b128 v[164:167], v6 offset:6144\n"
"v_cvt_pk_bf16_f32 v160, v160, v161\n"
"v_mfma_f32_16x16x32_bf16 v[152:155], v[148:151], v[132:135], v[152:155]\n"
"v_exp_f32 v163, v163\n"
"v_mfma_f32_16x16x32_bf16 v[140:143], v[104:107], v[252:255], v[140:143]\n"
"ds_read_b128 v[132:135], v5 offset:14336\n"
"v_mfma_f32_16x16x32_bf16 v[144:147], v[108:111], v[252:255], v[144:147]\n"
"ds_write_addtid_b32 v160 offset:57856\n"
"v_cvt_pk_bf16_f32 v161, v162, v163\n"
"ds_write_addtid_b32 v161 offset:58112\n"
"s_waitcnt lgkmcnt(4)\n"
"v_mfma_f32_16x16x32_bf16 v[156:159], v[238:241], v[136:139], v[156:159]\n"
"ds_read_b128 v[160:163], v4 offset:16384\n"
"v_mfma_f32_16x16x32_bf16 v[156:159], v[240:243], v[24:27], v[156:159]\n"
"s_waitcnt lgkmcnt(4)\n"
"v_exp_f32 v202, v202\n"
"ds_read_b128 v[148:151], v6 offset:7168\n"
"v_mfma_f32_16x16x32_bf16 v[152:155], v[164:167], v[252:255], v[152:155]\n"
"s_waitcnt lgkmcnt(4)\n"
"ds_read_b128 v[164:167], v177 offset:2736\n"
"v_mfma_f32_16x16x32_bf16 v[140:143], v[112:115], v[132:135], v[140:143]\n"
"v_exp_f32 v203, v203\n"
"s_nop 0\n"
"v_cvt_pk_bf16_f32 v202, v202, v203\n"
"v_mfma_f32_16x16x32_bf16 v[144:147], v[116:119], v[132:135], v[144:147]\n"
"v_exp_f32 v204, v204\n"
"v_exp_f32 v205, v205\n"
"ds_write_addtid_b32 v202 offset:58368\n"
"v_cvt_pk_bf16_f32 v203, v204, v205\n"
"s_waitcnt lgkmcnt(2)\n"
"v_mfma_f32_16x16x32_bf16 v[152:155], v[148:151], v[132:135], v[152:155]\n"
"ds_read_b128 v[132:135], v5 offset:16384\n"
"v_mfma_f32_16x16x32_bf16 v[140:143], v[0:3], v[160:163], v[140:143]\n"
"s_waitcnt lgkmcnt(2)\n"
"ds_read_b128 v[250:253], v6 offset:8192\n"
"v_mfma_f32_16x16x32_bf16 v[164:167], v[196:199], v[136:139], v[164:167]\n"
"ds_write_addtid_b32 v203 offset:58624\n"
"ds_read_b128 v[202:205], v6 offset:9216\n"
"v_mfma_f32_16x16x32_bf16 v[164:167], v[198:201], v[24:27], v[164:167]\n"
"v_exp_f32 v156, v156\n"
"ds_read_b128 v[136:139], v4 offset:18432\n"
"v_mfma_f32_16x16x32_bf16 v[144:147], v[120:123], v[160:163], v[144:147]\n"
"s_waitcnt lgkmcnt(3)\n"
"v_exp_f32 v157, v157\n"
"v_bfe_u32 v177, v6, 2, 6\n"
"v_mfma_f32_16x16x32_bf16 v[152:155], v[250:253], v[160:163], v[152:155]\n"
"v_exp_f32 v158, v158\n"
"v_mfma_f32_16x16x32_bf16 v[140:143], v[124:127], v[132:135], v[140:143]\n"
"ds_read_b128 v[148:151], v5 offset:18432\n"
"v_cvt_pk_bf16_f32 v156, v156, v157\n"
"ds_read_b32 v177, v177 offset:1408\n"
"v_mfma_f32_16x16x32_bf16 v[144:147], v[128:131], v[132:135], v[144:147]\n"
"s_nop 3\n"
"v_mul_f32 v141, s60, v141\n"
"v_mul_f32 v140, s60, v140\n"
"s_waitcnt lgkmcnt(3)\n"
"v_mfma_f32_16x16x32_bf16 v[152:155], v[202:205], v[132:135], v[152:155]\n"
"s_waitcnt lgkmcnt(2)\n"
"v_exp_f32 v159, v159\n"
"v_mfma_f32_16x16x32_bf16 v[250:253], v[8:11], v[136:139], 0\n"
"ds_read_b128 v[202:205], v4 offset:20480\n"
"v_mfma_f32_16x16x32_bf16 v[132:135], v[12:15], v[136:139], 0\n"
"ds_write_addtid_b32 v156 offset:58880\n"
"v_cvt_pk_bf16_f32 v157, v158, v159\n"
"s_waitcnt lgkmcnt(2)\n"
"v_add_u32_sdwa v176, v177, v7 dst_sel:DWORD dst_unused:UNUSED_PAD src0_sel:WORD_0 src1_sel:DWORD\n"
"ds_write_addtid_b32 v157 offset:59136\n"
"v_mfma_f32_16x16x32_bf16 v[156:159], v[16:19], v[136:139], 0\n"
"s_add_u32 s70, s50, 4294967264\n"
"v_mul_f32 v152, s60, v152\n"
"v_mfma_f32_16x16x32_bf16 v[250:253], v[20:23], v[148:151], v[250:253]\n"
"v_exp_f32 v164, v164\n"
"v_exp_f32 v165, v165\n"
"s_nop 0\n"
"v_cvt_pk_bf16_f32 v164, v164, v165\n"
"v_mul_f32 v155, s60, v155\n"
"v_exp_f32 v166, v166\n"
"v_mfma_f32_16x16x32_bf16 v[132:135], v[28:31], v[148:151], v[132:135]\n"
"v_exp_f32 v167, v167\n"
"v_mfma_f32_16x16x32_bf16 v[156:159], v[32:35], v[148:151], v[156:159]\n"
"s_waitcnt lgkmcnt(2)\n"
"ds_read_b128 v[148:151], v5 offset:20480\n"
"v_mfma_f32_16x16x32_bf16 v[250:253], v[36:39], v[202:205], v[250:253]\n"
"ds_write_addtid_b32 v164 offset:57344\n"
"v_mul_f32 v145, s60, v145\n"
"v_mul_f32 v144, s60, v144\n"
"v_mul_f32 v142, s60, v142\n"
"v_cvt_pk_bf16_f32 v165, v166, v167\n"
"v_mul_f32 v147, s60, v147\n"
"v_mul_f32 v143, s60, v143\n"
"ds_write_addtid_b32 v165 offset:57600\n"
"v_max_u32 v207, s70, v176\n"
"v_cvt_pk_bf16_f32 v140, v140, v141\n"
"v_mul_f32 v153, s60, v153\n"
"v_cvt_pk_bf16_f32 v26, v152, v153\n"
"v_mul_f32 v146, s60, v146\n"
"v_cvt_pk_bf16_f32 v141, v142, v143\n"
"v_cvt_pk_bf16_f32 v142, v144, v145\n"
"ds_read_b128 v[136:139], v207 offset:48\n"
"v_mfma_f32_16x16x32_bf16 v[132:135], v[40:43], v[202:205], v[132:135]\n"
"s_waitcnt lgkmcnt(1)\n"
"v_mul_f32 v154, s60, v154\n"
"v_cvt_pk_bf16_f32 v143, v146, v147\n"
"ds_read_b128 v[144:147], v4 offset:22528\n"
"s_barrier\n"
"v_add_u32_sdwa v177, sext(v177), v176 dst_sel:DWORD dst_unused:UNUSED_PAD src0_sel:WORD_1 src1_sel:DWORD\n"
"v_cvt_pk_bf16_f32 v27, v154, v155\n"
"s_waitcnt lgkmcnt(1)\n"
"v_mfma_f32_16x16x32_bf16 v[136:139], v[214:217], v[140:143], v[136:139]\n"
"s_add_u32 s72, s50, 4294966960\n"
"v_max_u32 v206, s70, v177\n"
"v_mfma_f32_16x16x32_bf16 v[136:139], v[216:219], v[24:27], v[136:139]\n"
"ds_read_b128 v[214:217], v206 offset:64\n"
"v_min_u32 v206, s72, v176\n"
"v_mfma_f32_16x16x32_bf16 v[156:159], v[44:47], v[202:205], v[156:159]\n"
"ds_read_b128 v[202:205], v5 offset:22528\n"
"v_mfma_f32_16x16x32_bf16 v[250:253], v[48:51], v[148:151], v[250:253]\n"
"v_mfma_f32_16x16x32_bf16 v[132:135], v[52:55], v[148:151], v[132:135]\n"
"v_mfma_f32_16x16x32_bf16 v[156:159], v[56:59], v[148:151], v[156:159]\n"
"s_waitcnt lgkmcnt(2)\n"
"v_mfma_f32_16x16x32_bf16 v[250:253], v[60:63], v[144:147], v[250:253]\n"
"v_mfma_f32_16x16x32_bf16 v[132:135], v[64:67], v[144:147], v[132:135]\n"
"s_waitcnt lgkmcnt(1)\n"
"v_exp_f32 v136, v136\n"
"v_mfma_f32_16x16x32_bf16 v[214:217], v[220:223], v[140:143], v[214:217]\n"
"ds_read_b128 v[218:221], v4 offset:24576\n"
"v_mfma_f32_16x16x32_bf16 v[214:217], v[222:225], v[24:27], v[214:217]\n"
"v_exp_f32 v137, v137\n"
"v_exp_f32 v138, v138\n"
"v_mfma_f32_16x16x32_bf16 v[156:159], v[68:71], v[144:147], v[156:159]\n"
"s_waitcnt lgkmcnt(1)\n"
"v_mfma_f32_16x16x32_bf16 v[250:253], v[72:75], v[202:205], v[250:253]\n"
"ds_read_b128 v[222:225], v206 offset:2720\n"
"v_mfma_f32_16x16x32_bf16 v[132:135], v[76:79], v[202:205], v[132:135]\n"
"v_exp_f32 v139, v139\n"
"v_cvt_pk_bf16_f32 v136, v136, v137\n"
"v_cvt_pk_bf16_f32 v137, v138, v139\n"
"v_mfma_f32_16x16x32_bf16 v[156:159], v[80:83], v[202:205], v[156:159]\n"
"ds_write_addtid_b32 v136 offset:55296\n"
"ds_write_addtid_b32 v137 offset:55552\n"
"s_waitcnt lgkmcnt(3)\n"
"v_mfma_f32_16x16x32_bf16 v[202:205], v[8:11], v[218:221], 0\n"
"s_waitcnt lgkmcnt(2)\n"
"ds_read_b128 v[136:139], v5 offset:24576\n"
"v_cvt_pk_bf16_f32 v250, v250, v251\n"
"v_cvt_pk_bf16_f32 v251, v252, v253\n"
"v_mfma_f32_16x16x32_bf16 v[222:225], v[190:193], v[140:143], v[222:225]\n"
"v_cvt_pk_bf16_f32 v254, v156, v157\n"
"v_cvt_pk_bf16_f32 v252, v132, v133\n"
"v_mfma_f32_16x16x32_bf16 v[222:225], v[192:195], v[24:27], v[222:225]\n"
"v_cvt_pk_bf16_f32 v255, v158, v159\n"
"v_cvt_pk_bf16_f32 v253, v134, v135\n"
"v_exp_f32 v214, v214\n"
"v_exp_f32 v215, v215\n"
"v_mfma_f32_16x16x32_bf16 v[144:147], v[12:15], v[218:221], 0\n"
"ds_read_b128 v[132:135], v206 offset:2736\n"
"v_cvt_pk_bf16_f32 v214, v214, v215\n"
"v_mfma_f32_16x16x32_bf16 v[148:151], v[16:19], v[218:221], 0\n"
"s_waitcnt lgkmcnt(1)\n"
"v_exp_f32 v216, v216\n"
"v_exp_f32 v217, v217\n"
"v_mfma_f32_16x16x32_bf16 v[202:205], v[20:23], v[136:139], v[202:205]\n"
"ds_read_b128 v[218:221], v4 offset:26624\n"
"s_add_u32 s71, s50, 1008\n"
"v_mfma_f32_16x16x32_bf16 v[144:147], v[28:31], v[136:139], v[144:147]\n"
"ds_write_addtid_b32 v214 offset:55808\n"
"v_mfma_f32_16x16x32_bf16 v[148:151], v[32:35], v[136:139], v[148:151]\n"
"s_waitcnt lgkmcnt(2)\n"
"ds_read_b128 v[136:139], v207 offset:32\n"
"v_mfma_f32_16x16x32_bf16 v[132:135], v[196:199], v[140:143], v[132:135]\n"
"v_cvt_pk_bf16_f32 v215, v216, v217\n"
"ds_write_addtid_b32 v215 offset:56064\n"
"v_mfma_f32_16x16x32_bf16 v[132:135], v[198:201], v[24:27], v[132:135]\n"
"s_waitcnt lgkmcnt(3)\n"
"v_exp_f32 v224, v224\n"
"ds_read_b128 v[214:217], v5 offset:26624\n"
"v_mfma_f32_16x16x32_bf16 v[202:205], v[36:39], v[218:221], v[202:205]\n"
"v_exp_f32 v225, v225\n"
"v_mfma_f32_16x16x32_bf16 v[144:147], v[40:43], v[218:221], v[144:147]\n"
"v_cvt_pk_bf16_f32 v223, v224, v225\n"
"v_min_u32 v225, s71, v176\n"
"v_mfma_f32_16x16x32_bf16 v[148:151], v[44:47], v[218:221], v[148:151]\n"
"s_waitcnt lgkmcnt(0)\n"
"v_exp_f32 v132, v132\n"
"v_mfma_f32_16x16x32_bf16 v[202:205], v[48:51], v[214:217], v[202:205]\n"
"ds_read_b128 v[218:221], v4 offset:28672\n"
"v_mfma_f32_16x16x32_bf16 v[144:147], v[52:55], v[214:217], v[144:147]\n"
"v_exp_f32 v133, v133\n"
"v_mfma_f32_16x16x32_bf16 v[136:139], v[208:211], v[140:143], v[136:139]\n"
"ds_read_b128 v[206:209], v5 offset:28672\n"
"v_mfma_f32_16x16x32_bf16 v[136:139], v[210:213], v[24:27], v[136:139]\n"
"v_cvt_pk_bf16_f32 v132, v132, v133\n"
"v_mfma_f32_16x16x32_bf16 v[148:151], v[56:59], v[214:217], v[148:151]\n"
"s_waitcnt lgkmcnt(1)\n"
"ds_read_b128 v[210:213], v225 offset:1376\n"
"v_mfma_f32_16x16x32_bf16 v[202:205], v[60:63], v[218:221], v[202:205]\n"
"v_mfma_f32_16x16x32_bf16 v[144:147], v[64:67], v[218:221], v[144:147]\n"
"v_exp_f32 v134, v134\n"
"v_mfma_f32_16x16x32_bf16 v[148:151], v[68:71], v[218:221], v[148:151]\n"
"s_waitcnt lgkmcnt(1)\n"
"v_mfma_f32_16x16x32_bf16 v[202:205], v[72:75], v[206:209], v[202:205]\n"
"ds_write_addtid_b32 v223 offset:56576\n"
"s_waitcnt lgkmcnt(1)\n"
"v_mfma_f32_16x16x32_bf16 v[210:213], v[238:241], v[140:143], v[210:213]\n"
"v_exp_f32 v135, v135\n"
"ds_write_addtid_b32 v132 offset:56832\n"
"v_cvt_pk_bf16_f32 v133, v134, v135\n"
"v_exp_f32 v138, v138\n"
"v_mfma_f32_16x16x32_bf16 v[210:213], v[240:243], v[24:27], v[210:213]\n"
"ds_write_addtid_b32 v133 offset:57088\n"
"ds_read_b128 v[218:221], v225 offset:1392\n"
"v_mfma_f32_16x16x32_bf16 v[144:147], v[76:79], v[206:209], v[144:147]\n"
"v_mfma_f32_16x16x32_bf16 v[148:151], v[80:83], v[206:209], v[148:151]\n"
"ds_read_b128 v[214:217], v4 offset:30720\n"
"v_cvt_pk_bf16_f32 v202, v202, v203\n"
"v_cvt_pk_bf16_f32 v203, v204, v205\n"
"s_nop 3\n"
"v_cvt_pk_bf16_f32 v205, v146, v147\n"
"s_waitcnt lgkmcnt(2)\n"
"s_barrier\n"
"v_exp_f32 v139, v139\n"
"s_waitcnt lgkmcnt(0)\n"
"v_mfma_f32_16x16x32_bf16 v[132:135], v[84:87], v[214:217], 0\n"
"ds_read_b128 v[222:225], v5 offset:30720\n"
"v_cvt_pk_bf16_f32 v207, v150, v151\n"
"v_cvt_pk_bf16_f32 v204, v144, v145\n"
"v_min_u32 v176, s71, v177\n"
"v_cvt_pk_bf16_f32 v137, v138, v139\n"
"ds_write_addtid_b32 v137 offset:58112\n"
"v_mfma_f32_16x16x32_bf16 v[136:139], v[88:91], v[214:217], 0\n"
"ds_read_b128 v[144:147], v6 offset:5120\n"
"v_cvt_pk_bf16_f32 v206, v148, v149\n"
"v_mfma_f32_16x16x32_bf16 v[148:151], v[92:95], v[214:217], 0\n"
"ds_read_b128 v[214:217], v4 offset:32768\n"
"ds_read_b128 v[152:155], v176 offset:1408\n"
"v_mfma_f32_16x16x32_bf16 v[218:221], v[244:247], v[140:143], v[218:221]\n"
"v_exp_f32 v212, v212\n"
"v_mfma_f32_16x16x32_bf16 v[218:221], v[246:249], v[24:27], v[218:221]\n"
"s_waitcnt lgkmcnt(4)\n"
"v_mfma_f32_16x16x32_bf16 v[132:135], v[96:99], v[222:225], v[132:135]\n"
"v_exp_f32 v213, v213\n"
"s_nop 0\n"
"v_cvt_pk_bf16_f32 v211, v212, v213\n"
"v_mfma_f32_16x16x32_bf16 v[136:139], v[100:103], v[222:225], v[136:139]\n"
"s_waitcnt lgkmcnt(2)\n"
"ds_read_b128 v[156:159], v6 offset:6144\n"
"v_mfma_f32_16x16x32_bf16 v[148:151], v[144:147], v[222:225], v[148:151]\n"
"ds_read_b128 v[222:225], v6 offset:7168\n"
"ds_write_addtid_b32 v211 offset:58624\n"
"ds_read_b128 v[208:211], v5 offset:32768\n"
"s_waitcnt lgkmcnt(5)\n"
"v_mfma_f32_16x16x32_bf16 v[132:135], v[104:107], v[214:217], v[132:135]\n"
"v_min_u32 v176, s72, v177\n"
"v_mfma_f32_16x16x32_bf16 v[136:139], v[108:111], v[214:217], v[136:139]\n"
"s_waitcnt lgkmcnt(4)\n"
"v_exp_f32 v218, v218\n"
"v_mfma_f32_16x16x32_bf16 v[152:155], v[250:253], v[140:143], v[152:155]\n"
"ds_read_b128 v[144:147], v4 offset:34816\n"
"v_mfma_f32_16x16x32_bf16 v[152:155], v[252:255], v[24:27], v[152:155]\n"
"s_waitcnt lgkmcnt(4)\n"
"v_exp_f32 v219, v219\n"
"ds_read_b128 v[160:163], v176 offset:2752\n"
"v_mfma_f32_16x16x32_bf16 v[148:151], v[156:159], v[214:217], v[148:151]\n"
"s_waitcnt lgkmcnt(2)\n"
"ds_read_b128 v[212:215], v6 offset:8192\n"
"v_mfma_f32_16x16x32_bf16 v[132:135], v[112:115], v[208:211], v[132:135]\n"
"v_mfma_f32_16x16x32_bf16 v[136:139], v[116:119], v[208:211], v[136:139]\n"
"v_exp_f32 v220, v220\n"
"v_exp_f32 v221, v221\n"
"v_mfma_f32_16x16x32_bf16 v[148:151], v[222:225], v[208:211], v[148:151]\n"
"s_waitcnt lgkmcnt(2)\n"
"ds_read_b128 v[208:211], v5 offset:34816\n"
"v_cvt_pk_bf16_f32 v218, v218, v219\n"
"v_mfma_f32_16x16x32_bf16 v[132:135], v[0:3], v[144:147], v[132:135]\n"
"s_waitcnt lgkmcnt(2)\n"
"v_exp_f32 v152, v152\n"
"v_exp_f32 v153, v153\n"
"v_exp_f32 v154, v154\n"
"v_mfma_f32_16x16x32_bf16 v[160:163], v[202:205], v[140:143], v[160:163]\n"
"ds_write_addtid_b32 v218 offset:58880\n"
"v_cvt_pk_bf16_f32 v219, v220, v221\n"
"ds_write_addtid_b32 v219 offset:59136\n"
"v_cvt_pk_bf16_f32 v152, v152, v153\n"
"v_mfma_f32_16x16x32_bf16 v[160:163], v[204:207], v[24:27], v[160:163]\n"
"ds_read_b128 v[216:219], v6 offset:9216\n"
"v_mfma_f32_16x16x32_bf16 v[136:139], v[120:123], v[144:147], v[136:139]\n"
"s_waitcnt lgkmcnt(4)\n"
"v_exp_f32 v155, v155\n"
"v_mfma_f32_16x16x32_bf16 v[148:151], v[212:215], v[144:147], v[148:151]\n"
"s_waitcnt lgkmcnt(3)\n"
"v_bfe_u32 v225, v6, 2, 6\n"
"v_mfma_f32_16x16x32_bf16 v[132:135], v[124:127], v[208:211], v[132:135]\n"
"ds_read_b128 v[220:223], v4 offset:36864\n"
"v_mfma_f32_16x16x32_bf16 v[136:139], v[128:131], v[208:211], v[136:139]\n"
"s_waitcnt lgkmcnt(1)\n"
"v_mfma_f32_16x16x32_bf16 v[148:151], v[216:219], v[208:211], v[148:151]\n"
"ds_read_b128 v[212:215], v5 offset:36864\n"
"v_cvt_pk_bf16_f32 v153, v154, v155\n"
"v_exp_f32 v160, v160\n"
"v_exp_f32 v161, v161\n"
"v_exp_f32 v162, v162\n"
"s_waitcnt lgkmcnt(1)\n"
"v_mfma_f32_16x16x32_bf16 v[208:211], v[8:11], v[220:223], 0\n"
"ds_write_addtid_b32 v152 offset:34304\n"
"v_mul_f32 v132, s60, v132\n"
"v_mul_f32 v133, s60, v133\n"
"v_mul_f32 v136, s60, v136\n"
"v_mul_f32 v148, s60, v148\n"
"v_mfma_f32_16x16x32_bf16 v[140:143], v[12:15], v[220:223], 0\n"
"ds_read_b32 v225, v225 offset:1472\n"
"v_mul_f32 v135, s60, v135\n"
"v_mul_f32 v137, s60, v137\n"
"v_mfma_f32_16x16x32_bf16 v[144:147], v[16:19], v[220:223], 0\n"
"s_waitcnt lgkmcnt(2)\n"
"v_exp_f32 v163, v163\n"
"ds_read_b128 v[216:219], v4 offset:38912\n"
"v_mul_f32 v139, s60, v139\n"
"v_mfma_f32_16x16x32_bf16 v[208:211], v[20:23], v[212:215], v[208:211]\n"
"v_mul_f32 v134, s60, v134\n"
"v_cvt_pk_bf16_f32 v160, v160, v161\n"
"v_cvt_pk_bf16_f32 v161, v162, v163\n"
"ds_write_addtid_b32 v160 offset:57344\n"
"v_mul_f32 v149, s60, v149\n"
"v_mfma_f32_16x16x32_bf16 v[140:143], v[28:31], v[212:215], v[140:143]\n"
"v_cvt_pk_bf16_f32 v132, v132, v133\n"
"v_mul_f32 v138, s60, v138\n"
"ds_write_addtid_b32 v153 offset:34560\n"
"v_mul_f32 v150, s60, v150\n"
"v_mfma_f32_16x16x32_bf16 v[144:147], v[32:35], v[212:215], v[144:147]\n"
"s_waitcnt lgkmcnt(3)\n"
"v_cvt_pk_bf16_f32 v133, v134, v135\n"
"v_add_u32_sdwa v224, v225, v7 dst_sel:DWORD dst_unused:UNUSED_PAD src0_sel:WORD_0 src1_sel:DWORD\n"
"v_cvt_pk_bf16_f32 v134, v136, v137\n"
"s_add_u32 s68, s50, 4294966992\n"
"s_waitcnt lgkmcnt(2)\n"
"v_mfma_f32_16x16x32_bf16 v[208:211], v[36:39], v[216:219], v[208:211]\n"
"ds_read_b128 v[212:215], v5 offset:38912\n"
"ds_write_addtid_b32 v161 offset:57600\n"
"v_min_u32 v225, s68, v224\n"
"ds_read_b128 v[220:223], v225 offset:2688\n"
"v_cvt_pk_bf16_f32 v26, v148, v149\n"
"s_waitcnt lgkmcnt(1)\n"
"s_barrier\n"
"v_mfma_f32_16x16x32_bf16 v[140:143], v[40:43], v[216:219], v[140:143]\n"
"v_cvt_pk_bf16_f32 v135, v138, v139\n"
"v_mul_f32 v151, s60, v151\n"
"v_cvt_pk_bf16_f32 v27, v150, v151\n"
"ds_read_b128 v[136:139], v4 offset:40960\n"
"s_waitcnt lgkmcnt(1)\n"
"v_mfma_f32_16x16x32_bf16 v[220:223], v[178:181], v[132:135], v[220:223]\n"
"v_mfma_f32_16x16x32_bf16 v[220:223], v[180:183], v[24:27], v[220:223]\n"
"v_mfma_f32_16x16x32_bf16 v[144:147], v[44:47], v[216:219], v[144:147]\n"
"ds_read_b128 v[216:219], v5 offset:40960\n"
"v_mfma_f32_16x16x32_bf16 v[208:211], v[48:51], v[212:215], v[208:211]\n"
"ds_read_b128 v[148:151], v225 offset:2704\n"
"v_mfma_f32_16x16x32_bf16 v[140:143], v[52:55], v[212:215], v[140:143]\n"
"v_mfma_f32_16x16x32_bf16 v[144:147], v[56:59], v[212:215], v[144:147]\n"
"s_waitcnt lgkmcnt(2)\n"
"v_mfma_f32_16x16x32_bf16 v[208:211], v[60:63], v[136:139], v[208:211]\n"
"v_mfma_f32_16x16x32_bf16 v[140:143], v[64:67], v[136:139], v[140:143]\n"
"v_exp_f32 v220, v220\n"
"v_exp_f32 v221, v221\n"
"s_nop 0\n"
"v_cvt_pk_bf16_f32 v220, v220, v221\n"
"v_mfma_f32_16x16x32_bf16 v[144:147], v[68:71], v[136:139], v[144:147]\n"
"s_waitcnt lgkmcnt(1)\n"
"v_exp_f32 v222, v222\n"
"v_mfma_f32_16x16x32_bf16 v[208:211], v[72:75], v[216:219], v[208:211]\n"
"s_waitcnt lgkmcnt(0)\n"
"v_mfma_f32_16x16x32_bf16 v[148:151], v[184:187], v[132:135], v[148:151]\n"
"ds_read_b128 v[136:139], v4 offset:43008\n"
"ds_read_b128 v[152:155], v5 offset:43008\n"
"s_add_u32 s73, s50, 4294965952\n"
"v_mfma_f32_16x16x32_bf16 v[148:151], v[186:189], v[24:27], v[148:151]\n"
"v_mfma_f32_16x16x32_bf16 v[140:143], v[76:79], v[216:219], v[140:143]\n"
"ds_write_addtid_b32 v220 offset:53248\n"
"v_max_u32 v177, s73, v224\n"
"ds_read_b128 v[156:159], v177 offset:1344\n"
"v_mfma_f32_16x16x32_bf16 v[144:147], v[80:83], v[216:219], v[144:147]\n"
"s_waitcnt lgkmcnt(3)\n"
"v_exp_f32 v223, v223\n"
"v_mfma_f32_16x16x32_bf16 v[214:217], v[8:11], v[136:139], 0\n"
"v_cvt_pk_bf16_f32 v208, v208, v209\n"
"v_cvt_pk_bf16_f32 v209, v210, v211\n"
"v_cvt_pk_bf16_f32 v221, v222, v223\n"
"ds_write_addtid_b32 v221 offset:53504\n"
"v_mfma_f32_16x16x32_bf16 v[218:221], v[12:15], v[136:139], 0\n"
"v_exp_f32 v148, v148\n"
"v_cvt_pk_bf16_f32 v211, v142, v143\n"
"v_cvt_pk_bf16_f32 v210, v140, v141\n"
"v_mfma_f32_16x16x32_bf16 v[140:143], v[16:19], v[136:139], 0\n"
"s_waitcnt lgkmcnt(3)\n"
"ds_read_b128 v[136:139], v4 offset:45056\n"
"v_cvt_pk_bf16_f32 v213, v146, v147\n"
"v_cvt_pk_bf16_f32 v212, v144, v145\n"
"v_mfma_f32_16x16x32_bf16 v[214:217], v[20:23], v[152:155], v[214:217]\n"
"v_mfma_f32_16x16x32_bf16 v[218:221], v[28:31], v[152:155], v[218:221]\n"
"v_exp_f32 v149, v149\n"
"v_mfma_f32_16x16x32_bf16 v[140:143], v[32:35], v[152:155], v[140:143]\n"
"s_waitcnt lgkmcnt(2)\n"
"v_mfma_f32_16x16x32_bf16 v[156:159], v[226:229], v[132:135], v[156:159]\n"
"v_exp_f32 v150, v150\n"
"ds_read_b128 v[152:155], v4 offset:47104\n"
"v_exp_f32 v151, v151\n"
"v_cvt_pk_bf16_f32 v148, v148, v149\n"
"v_mfma_f32_16x16x32_bf16 v[156:159], v[228:231], v[24:27], v[156:159]\n"
"ds_read_b128 v[160:163], v177 offset:1360\n"
"ds_write_addtid_b32 v148 offset:53760\n"
"ds_read_b128 v[144:147], v5 offset:45056\n"
"s_waitcnt lgkmcnt(4)\n"
"v_mfma_f32_16x16x32_bf16 v[214:217], v[36:39], v[136:139], v[214:217]\n"
"s_nop 2\n"
"v_exp_f32 v156, v156\n"
"v_mfma_f32_16x16x32_bf16 v[218:221], v[40:43], v[136:139], v[218:221]\n"
"v_mfma_f32_16x16x32_bf16 v[140:143], v[44:47], v[136:139], v[140:143]\n"
"s_waitcnt lgkmcnt(0)\n"
"ds_read_b128 v[136:139], v5 offset:47104\n"
"v_mfma_f32_16x16x32_bf16 v[214:217], v[48:51], v[144:147], v[214:217]\n"
"v_cvt_pk_bf16_f32 v149, v150, v151\n"
"v_mfma_f32_16x16x32_bf16 v[218:221], v[52:55], v[144:147], v[218:221]\n"
"ds_write_addtid_b32 v149 offset:54016\n"
"v_mfma_f32_16x16x32_bf16 v[140:143], v[56:59], v[144:147], v[140:143]\n"
"v_exp_f32 v157, v157\n"
"v_mfma_f32_16x16x32_bf16 v[214:217], v[60:63], v[152:155], v[214:217]\n"
"v_cvt_pk_bf16_f32 v156, v156, v157\n"
"v_mfma_f32_16x16x32_bf16 v[160:163], v[232:235], v[132:135], v[160:163]\n"
"ds_read_b128 v[148:151], v177 offset:1376\n"
"v_mfma_f32_16x16x32_bf16 v[160:163], v[234:237], v[24:27], v[160:163]\n"
"v_mfma_f32_16x16x32_bf16 v[218:221], v[64:67], v[152:155], v[218:221]\n"
"v_mfma_f32_16x16x32_bf16 v[140:143], v[68:71], v[152:155], v[140:143]\n"
"ds_write_addtid_b32 v156 offset:54272\n"
"s_waitcnt lgkmcnt(3)\n"
"v_mfma_f32_16x16x32_bf16 v[214:217], v[72:75], v[136:139], v[214:217]\n"
"v_exp_f32 v158, v158\n"
"v_mfma_f32_16x16x32_bf16 v[218:221], v[76:79], v[136:139], v[218:221]\n"
"v_exp_f32 v159, v159\n"
"ds_read_b128 v[144:147], v4 offset:55296\n"
"v_cvt_pk_bf16_f32 v157, v158, v159\n"
"ds_write_addtid_b32 v157 offset:54528\n"
"s_nop 1\n"
"v_cvt_pk_bf16_f32 v214, v214, v215\n"
"v_cvt_pk_bf16_f32 v215, v216, v217\n"
"v_mfma_f32_16x16x32_bf16 v[140:143], v[80:83], v[136:139], v[140:143]\n"
"s_waitcnt lgkmcnt(1)\n"
"v_mfma_f32_16x16x32_bf16 v[136:139], v[84:87], v[144:147], 0\n"
"v_cvt_pk_bf16_f32 v216, v218, v219\n"
"s_nop 4\n"
"v_cvt_pk_bf16_f32 v219, v142, v143\n"
"v_cvt_pk_bf16_f32 v217, v220, v221\n"
"v_mfma_f32_16x16x32_bf16 v[156:159], v[88:91], v[144:147], 0\n"
"ds_read_b128 v[152:155], v5 offset:55296\n"
"v_cvt_pk_bf16_f32 v218, v140, v141\n"
"v_mfma_f32_16x16x32_bf16 v[140:143], v[92:95], v[144:147], 0\n"
"s_waitcnt lgkmcnt(1)\n"
"s_barrier\n"
"ds_read_b128 v[164:167], v6 offset:5120\n"
"v_mfma_f32_16x16x32_bf16 v[148:151], v[238:241], v[132:135], v[148:151]\n"
"ds_read_b128 v[220:223], v4 offset:57344\n"
"v_mfma_f32_16x16x32_bf16 v[148:151], v[240:243], v[24:27], v[148:151]\n"
"s_waitcnt lgkmcnt(2)\n"
"ds_read_b128 v[144:147], v6 offset:6144\n"
"v_mfma_f32_16x16x32_bf16 v[136:139], v[96:99], v[152:155], v[136:139]\n"
"v_exp_f32 v160, v160\n"
"v_exp_f32 v161, v161\n"
"ds_read_b128 v[168:171], v5 offset:57344\n"
"v_exp_f32 v162, v162\n"
"v_mfma_f32_16x16x32_bf16 v[156:159], v[100:103], v[152:155], v[156:159]\n"
"s_waitcnt lgkmcnt(3)\n"
"v_mfma_f32_16x16x32_bf16 v[140:143], v[164:167], v[152:155], v[140:143]\n"
"s_waitcnt lgkmcnt(2)\n"
"v_cvt_pk_bf16_f32 v160, v160, v161\n"
"v_mfma_f32_16x16x32_bf16 v[136:139], v[104:107], v[220:223], v[136:139]\n"
"ds_read_b128 v[152:155], v225 offset:2720\n"
"v_mfma_f32_16x16x32_bf16 v[156:159], v[108:111], v[220:223], v[156:159]\n"
"s_waitcnt lgkmcnt(2)\n"
"v_exp_f32 v163, v163\n"
"v_mfma_f32_16x16x32_bf16 v[140:143], v[144:147], v[220:223], v[140:143]\n"
"s_waitcnt lgkmcnt(1)\n"
"v_cvt_pk_bf16_f32 v161, v162, v163\n"
"v_mfma_f32_16x16x32_bf16 v[136:139], v[112:115], v[168:171], v[136:139]\n"
"ds_read_b128 v[220:223], v6 offset:7168\n"
"v_mfma_f32_16x16x32_bf16 v[156:159], v[116:119], v[168:171], v[156:159]\n"
"ds_read_b128 v[144:147], v4 offset:59392\n"
"ds_write_addtid_b32 v160 offset:54784\n"
"ds_write_addtid_b32 v161 offset:55040\n"
"s_waitcnt lgkmcnt(4)\n"
"v_mfma_f32_16x16x32_bf16 v[152:155], v[190:193], v[132:135], v[152:155]\n"
"ds_read_b128 v[160:163], v6 offset:8192\n"
"v_mfma_f32_16x16x32_bf16 v[152:155], v[192:195], v[24:27], v[152:155]\n"
"s_waitcnt lgkmcnt(4)\n"
"v_exp_f32 v148, v148\n"
"v_mfma_f32_16x16x32_bf16 v[140:143], v[220:223], v[168:171], v[140:143]\n"
"v_exp_f32 v149, v149\n"
"v_exp_f32 v150, v150\n"
"ds_read_b128 v[220:223], v6 offset:9216\n"
"v_cvt_pk_bf16_f32 v148, v148, v149\n"
"ds_write_addtid_b32 v148 offset:57856\n"
"s_waitcnt lgkmcnt(5)\n"
"v_mfma_f32_16x16x32_bf16 v[136:139], v[0:3], v[144:147], v[136:139]\n"
"v_bfe_u32 v225, v6, 2, 6\n"
"v_mfma_f32_16x16x32_bf16 v[156:159], v[120:123], v[144:147], v[156:159]\n"
"s_waitcnt lgkmcnt(2)\n"
"ds_read_b128 v[132:135], v5 offset:59392\n"
"ds_read_b128 v[164:167], v4 offset:49152\n"
"v_mfma_f32_16x16x32_bf16 v[140:143], v[160:163], v[144:147], v[140:143]\n"
"s_waitcnt lgkmcnt(1)\n"
"v_mfma_f32_16x16x32_bf16 v[136:139], v[124:127], v[132:135], v[136:139]\n"
"v_exp_f32 v151, v151\n"
"v_exp_f32 v152, v152\n"
"v_mfma_f32_16x16x32_bf16 v[156:159], v[128:131], v[132:135], v[156:159]\n"
"ds_read_b32 v225, v225 offset:1536\n"
"s_add_u32 s70, s50, 4294964608\n"
"v_mfma_f32_16x16x32_bf16 v[140:143], v[220:223], v[132:135], v[140:143]\n"
"s_nop 1\n"
"v_mul_f32 v136, s60, v136\n"
"s_waitcnt lgkmcnt(1)\n"
"v_mfma_f32_16x16x32_bf16 v[220:223], v[8:11], v[164:167], 0\n"
"v_mul_f32 v138, s60, v138\n"
"v_cvt_pk_bf16_f32 v149, v150, v151\n"
"ds_read_b128 v[144:147], v5 offset:49152\n"
"v_mul_f32 v156, s60, v156\n"
"v_mfma_f32_16x16x32_bf16 v[132:135], v[12:15], v[164:167], 0\n"
"v_exp_f32 v153, v153\n"
"v_exp_f32 v154, v154\n"
"v_mul_f32 v142, s60, v142\n"
"v_cvt_pk_bf16_f32 v152, v152, v153\n"
"ds_write_addtid_b32 v152 offset:57344\n"
"v_mfma_f32_16x16x32_bf16 v[160:163], v[16:19], v[164:167], 0\n"
"s_waitcnt lgkmcnt(1)\n"
"v_mfma_f32_16x16x32_bf16 v[220:223], v[20:23], v[144:147], v[220:223]\n"
"ds_write_addtid_b32 v149 offset:58112\n"
"ds_read_b128 v[148:151], v4 offset:51200\n"
"v_mfma_f32_16x16x32_bf16 v[132:135], v[28:31], v[144:147], v[132:135]\n"
"v_exp_f32 v155, v155\n"
"v_mul_f32 v139, s60, v139\n"
"v_mul_f32 v159, s60, v159\n"
"v_mul_f32 v141, s60, v141\n"
"v_mul_f32 v137, s60, v137\n"
"v_cvt_pk_bf16_f32 v136, v136, v137\n"
"v_mul_f32 v158, s60, v158\n"
"v_mul_f32 v157, s60, v157\n"
"v_mfma_f32_16x16x32_bf16 v[160:163], v[32:35], v[144:147], v[160:163]\n"
"s_waitcnt lgkmcnt(0)\n"
"ds_read_b128 v[144:147], v5 offset:51200\n"
"v_mfma_f32_16x16x32_bf16 v[220:223], v[36:39], v[148:151], v[220:223]\n"
"v_mfma_f32_16x16x32_bf16 v[132:135], v[40:43], v[148:151], v[132:135]\n"
"v_cvt_pk_bf16_f32 v137, v138, v139\n"
"v_cvt_pk_bf16_f32 v138, v156, v157\n"
"v_add_u32_sdwa v177, v225, v7 dst_sel:DWORD dst_unused:UNUSED_PAD src0_sel:WORD_0 src1_sel:DWORD\n"
"v_mul_f32 v143, s60, v143\n"
"v_cvt_pk_bf16_f32 v153, v154, v155\n"
"v_mul_f32 v140, s60, v140\n"
"s_add_u32 s71, s50, 4294965648\n"
"v_max_u32 v176, s70, v177\n"
"ds_write_addtid_b32 v153 offset:57600\n"
"v_cvt_pk_bf16_f32 v139, v158, v159\n"
"ds_read_b128 v[152:155], v176 offset:2688\n"
"s_waitcnt lgkmcnt(1)\n"
"s_barrier\n"
"s_waitcnt lgkmcnt(0)\n"
"v_mfma_f32_16x16x32_bf16 v[152:155], v[178:181], v[136:139], v[152:155]\n"
"v_cvt_pk_bf16_f32 v27, v142, v143\n"
"v_cvt_pk_bf16_f32 v26, v140, v141\n"
"s_nop 1\n"
"v_mfma_f32_16x16x32_bf16 v[152:155], v[180:183], v[24:27], v[152:155]\n"
"ds_read_b128 v[140:143], v176 offset:2704\n"
"v_mfma_f32_16x16x32_bf16 v[160:163], v[44:47], v[148:151], v[160:163]\n"
"ds_read_b128 v[148:151], v4 offset:53248\n"
"v_mfma_f32_16x16x32_bf16 v[220:223], v[48:51], v[144:147], v[220:223]\n"
"v_mfma_f32_16x16x32_bf16 v[132:135], v[52:55], v[144:147], v[132:135]\n"
"v_mfma_f32_16x16x32_bf16 v[160:163], v[56:59], v[144:147], v[160:163]\n"
"s_waitcnt lgkmcnt(1)\n"
"v_min_u32 v175, s71, v177\n"
"ds_read_b128 v[144:147], v175 offset:4032\n"
"v_mfma_f32_16x16x32_bf16 v[140:143], v[184:187], v[136:139], v[140:143]\n"
"v_mfma_f32_16x16x32_bf16 v[140:143], v[186:189], v[24:27], v[140:143]\n"
"s_waitcnt lgkmcnt(1)\n"
"v_exp_f32 v152, v152\n"
"v_exp_f32 v153, v153\n"
"v_mfma_f32_16x16x32_bf16 v[220:223], v[60:63], v[148:151], v[220:223]\n"
"ds_read_b128 v[156:159], v5 offset:53248\n"
"v_mfma_f32_16x16x32_bf16 v[132:135], v[64:67], v[148:151], v[132:135]\n"
"v_exp_f32 v154, v154\n"
"v_exp_f32 v155, v155\n"
"v_mfma_f32_16x16x32_bf16 v[160:163], v[68:71], v[148:151], v[160:163]\n"
"s_waitcnt lgkmcnt(1)\n"
"ds_read_b128 v[148:151], v175 offset:4048\n"
"v_mfma_f32_16x16x32_bf16 v[144:147], v[208:211], v[136:139], v[144:147]\n"
"v_cvt_pk_bf16_f32 v152, v152, v153\n"
"v_cvt_pk_bf16_f32 v153, v154, v155\n"
"ds_write_addtid_b32 v152 offset:55296\n"
"ds_write_addtid_b32 v153 offset:55552\n"
"v_mfma_f32_16x16x32_bf16 v[144:147], v[210:213], v[24:27], v[144:147]\n"
"s_waitcnt lgkmcnt(3)\n"
"v_mfma_f32_16x16x32_bf16 v[220:223], v[72:75], v[156:159], v[220:223]\n"
"v_exp_f32 v140, v140\n"
"v_mfma_f32_16x16x32_bf16 v[132:135], v[76:79], v[156:159], v[132:135]\n"
"v_exp_f32 v141, v141\n"
"v_exp_f32 v142, v142\n"
"ds_read_b128 v[152:155], v4 offset:30720\n"
"v_mfma_f32_16x16x32_bf16 v[160:163], v[80:83], v[156:159], v[160:163]\n"
"v_cvt_pk_bf16_f32 v140, v140, v141\n"
"v_exp_f32 v143, v143\n"
"ds_write_addtid_b32 v140 offset:55808\n"
"v_cvt_pk_bf16_f32 v141, v142, v143\n"
"ds_write_addtid_b32 v141 offset:56064\n"
"s_add_u32 s69, s50, 4294965952\n"
"v_cvt_pk_bf16_f32 v220, v220, v221\n"
"v_cvt_pk_bf16_f32 v221, v222, v223\n"
"s_waitcnt lgkmcnt(5)\n"
"v_mfma_f32_16x16x32_bf16 v[148:151], v[214:217], v[136:139], v[148:151]\n"
"v_cvt_pk_bf16_f32 v225, v162, v163\n"
"v_mfma_f32_16x16x32_bf16 v[148:151], v[216:219], v[24:27], v[148:151]\n"
"s_waitcnt lgkmcnt(2)\n"
"ds_read_b128 v[140:143], v176 offset:2720\n"
"v_cvt_pk_bf16_f32 v223, v134, v135\n"
"v_cvt_pk_bf16_f32 v222, v132, v133\n"
"v_mfma_f32_16x16x32_bf16 v[132:135], v[84:87], v[152:155], 0\n"
"ds_read_b128 v[156:159], v5 offset:30720\n"
"v_cvt_pk_bf16_f32 v224, v160, v161\n"
"v_mfma_f32_16x16x32_bf16 v[160:163], v[88:91], v[152:155], 0\n"
"v_exp_f32 v144, v144\n"
"v_exp_f32 v145, v145\n"
"v_exp_f32 v146, v146\n"
"v_exp_f32 v147, v147\n"
"v_mfma_f32_16x16x32_bf16 v[164:167], v[92:95], v[152:155], 0\n"
"s_waitcnt lgkmcnt(1)\n"
"v_cvt_pk_bf16_f32 v144, v144, v145\n"
"v_mfma_f32_16x16x32_bf16 v[140:143], v[190:193], v[136:139], v[140:143]\n"
"v_exp_f32 v148, v148\n"
"v_max_u32 v174, s69, v177\n"
"ds_read_b128 v[168:171], v174 offset:1344\n"
"ds_write_addtid_b32 v144 offset:56320\n"
"ds_read_b128 v[152:155], v6 offset:5120\n"
"v_mfma_f32_16x16x32_bf16 v[140:143], v[192:195], v[24:27], v[140:143]\n"
"s_waitcnt lgkmcnt(3)\n"
"v_exp_f32 v149, v149\n"
"v_mfma_f32_16x16x32_bf16 v[132:135], v[96:99], v[156:159], v[132:135]\n"
"v_exp_f32 v150, v150\n"
"v_cvt_pk_bf16_f32 v145, v146, v147\n"
"v_mfma_f32_16x16x32_bf16 v[160:163], v[100:103], v[156:159], v[160:163]\n"
"ds_write_addtid_b32 v145 offset:56576\n"
"ds_read_b128 v[144:147], v4 offset:32768\n"
"v_exp_f32 v151, v151\n"
"s_waitcnt lgkmcnt(2)\n"
"v_mfma_f32_16x16x32_bf16 v[164:167], v[152:155], v[156:159], v[164:167]\n"
"ds_read_b128 v[156:159], v174 offset:1360\n"
"v_mfma_f32_16x16x32_bf16 v[168:171], v[226:229], v[136:139], v[168:171]\n"
"ds_read_b128 v[152:155], v6 offset:6144\n"
"v_mfma_f32_16x16x32_bf16 v[168:171], v[228:231], v[24:27], v[168:171]\n"
"s_waitcnt lgkmcnt(2)\n"
"v_cvt_pk_bf16_f32 v148, v148, v149\n"
"v_cvt_pk_bf16_f32 v149, v150, v151\n"
"v_mfma_f32_16x16x32_bf16 v[132:135], v[104:107], v[144:147], v[132:135]\n"
"v_mfma_f32_16x16x32_bf16 v[160:163], v[108:111], v[144:147], v[160:163]\n"
"v_exp_f32 v140, v140\n"
"ds_write_addtid_b32 v148 offset:56832\n"
"s_waitcnt lgkmcnt(1)\n"
"v_mfma_f32_16x16x32_bf16 v[164:167], v[152:155], v[144:147], v[164:167]\n"
"ds_read_b128 v[226:229], v5 offset:32768\n"
"v_mfma_f32_16x16x32_bf16 v[156:159], v[232:235], v[136:139], v[156:159]\n"
"v_exp_f32 v141, v141\n"
"s_nop 0\n"
"v_cvt_pk_bf16_f32 v140, v140, v141\n"
"ds_write_addtid_b32 v140 offset:34304\n"
"ds_write_addtid_b32 v149 offset:57088\n"
"v_mfma_f32_16x16x32_bf16 v[156:159], v[234:237], v[24:27], v[156:159]\n"
"s_waitcnt lgkmcnt(2)\n"
"v_mfma_f32_16x16x32_bf16 v[132:135], v[112:115], v[226:229], v[132:135]\n"
"v_exp_f32 v168, v168\n"
"ds_read_b128 v[144:147], v6 offset:7168\n"
"v_mfma_f32_16x16x32_bf16 v[160:163], v[116:119], v[226:229], v[160:163]\n"
"s_waitcnt lgkmcnt(0)\n"
"ds_read_b128 v[148:151], v174 offset:1376\n"
"v_mfma_f32_16x16x32_bf16 v[164:167], v[144:147], v[226:229], v[164:167]\n"
"v_exp_f32 v169, v169\n"
"v_exp_f32 v170, v170\n"
"v_exp_f32 v171, v171\n"
"s_barrier\n"
"ds_read_b128 v[140:143], v4 offset:34816\n"
"ds_read_b128 v[226:229], v6 offset:8192\n"
"v_cvt_pk_bf16_f32 v168, v168, v169\n"
"v_exp_f32 v156, v156\n"
"ds_write_addtid_b32 v168 offset:57856\n"
"s_waitcnt lgkmcnt(3)\n"
"v_mfma_f32_16x16x32_bf16 v[148:151], v[238:241], v[136:139], v[148:151]\n"
"ds_read_b128 v[144:147], v175 offset:4064\n"
"v_mfma_f32_16x16x32_bf16 v[148:151], v[240:243], v[24:27], v[148:151]\n"
"s_waitcnt lgkmcnt(3)\n"
"v_exp_f32 v157, v157\n"
"v_mfma_f32_16x16x32_bf16 v[132:135], v[0:3], v[140:143], v[132:135]\n"
"ds_read_b128 v[152:155], v5 offset:34816\n"
"v_cvt_pk_bf16_f32 v169, v170, v171\n"
"v_exp_f32 v158, v158\n"
"v_exp_f32 v159, v159\n"
"ds_write_addtid_b32 v169 offset:58112\n"
"v_mfma_f32_16x16x32_bf16 v[160:163], v[120:123], v[140:143], v[160:163]\n"
"s_add_u32 s73, s50, 4294966976\n"
"v_cvt_pk_bf16_f32 v156, v156, v157\n"
"s_waitcnt lgkmcnt(4)\n"
"v_mfma_f32_16x16x32_bf16 v[164:167], v[226:229], v[140:143], v[164:167]\n"
"s_waitcnt lgkmcnt(2)\n"
"ds_read_b128 v[226:229], v6 offset:9216\n"
"v_mfma_f32_16x16x32_bf16 v[144:147], v[220:223], v[136:139], v[144:147]\n"
"ds_read_b128 v[136:139], v4 offset:24576\n"
"v_mfma_f32_16x16x32_bf16 v[144:147], v[222:225], v[24:27], v[144:147]\n"
"ds_write_addtid_b32 v156 offset:58368\n"
"v_cvt_pk_bf16_f32 v157, v158, v159\n"
"ds_read_b128 v[140:143], v5 offset:24576\n"
"s_waitcnt lgkmcnt(5)\n"
"v_mfma_f32_16x16x32_bf16 v[132:135], v[124:127], v[152:155], v[132:135]\n"
"v_mfma_f32_16x16x32_bf16 v[160:163], v[128:131], v[152:155], v[160:163]\n"
"v_exp_f32 v148, v148\n"
"v_exp_f32 v149, v149\n"
"s_nop 0\n"
"v_cvt_pk_bf16_f32 v148, v148, v149\n"
"v_bfe_u32 v177, v6, 2, 6\n"
"ds_write_addtid_b32 v157 offset:58624\n"
"s_waitcnt lgkmcnt(4)\n"
"v_mfma_f32_16x16x32_bf16 v[164:167], v[226:229], v[152:155], v[164:167]\n"
"s_waitcnt lgkmcnt(3)\n"
"v_mul_f32 v132, s60, v132\n"
"v_mfma_f32_16x16x32_bf16 v[226:229], v[8:11], v[136:139], 0\n"
"ds_read_b32 v177, v177 offset:1600\n"
"v_mul_f32 v163, s60, v163\n"
"v_mfma_f32_16x16x32_bf16 v[152:155], v[12:15], v[136:139], 0\n"
"ds_write_addtid_b32 v148 offset:58880\n"
"v_mul_f32 v162, s60, v162\n"
"v_mul_f32 v133, s60, v133\n"
"v_mfma_f32_16x16x32_bf16 v[148:151], v[16:19], v[136:139], 0\n"
"s_waitcnt lgkmcnt(3)\n"
"v_exp_f32 v144, v144\n"
"v_exp_f32 v145, v145\n"
"v_mfma_f32_16x16x32_bf16 v[226:229], v[20:23], v[140:143], v[226:229]\n"
"ds_read_b128 v[136:139], v4 offset:26624\n"
"v_mul_f32 v160, s60, v160\n"
"v_mfma_f32_16x16x32_bf16 v[152:155], v[28:31], v[140:143], v[152:155]\n"
"v_cvt_pk_bf16_f32 v132, v132, v133\n"
"v_mul_f32 v161, s60, v161\n"
"v_cvt_pk_bf16_f32 v144, v144, v145\n"
"v_mul_f32 v164, s60, v164\n"
"s_waitcnt lgkmcnt(2)\n"
"v_add_u32_sdwa v176, v177, v7 dst_sel:DWORD dst_unused:UNUSED_PAD src0_sel:WORD_0 src1_sel:DWORD\n"
"v_mul_f32 v135, s60, v135\n"
"v_mfma_f32_16x16x32_bf16 v[148:151], v[32:35], v[140:143], v[148:151]\n"
"ds_write_addtid_b32 v144 offset:57344\n"
"v_mul_f32 v134, s60, v134\n"
"v_cvt_pk_bf16_f32 v133, v134, v135\n"
"v_cvt_pk_bf16_f32 v134, v160, v161\n"
"v_cvt_pk_bf16_f32 v135, v162, v163\n"
"s_waitcnt lgkmcnt(1)\n"
"v_mfma_f32_16x16x32_bf16 v[226:229], v[36:39], v[136:139], v[226:229]\n"
"v_min_u32 v177, s73, v176\n"
"ds_read_b128 v[156:159], v177 offset:2704\n"
"ds_read_b128 v[140:143], v5 offset:26624\n"
"v_mul_f32 v167, s60, v167\n"
"v_mul_f32 v166, s60, v166\n"
"v_mfma_f32_16x16x32_bf16 v[152:155], v[40:43], v[136:139], v[152:155]\n"
"s_waitcnt lgkmcnt(2)\n"
"s_barrier\n"
"v_mul_f32 v165, s60, v165\n"
"v_cvt_pk_bf16_f32 v26, v164, v165\n"
"v_cvt_pk_bf16_f32 v27, v166, v167\n"
"s_waitcnt lgkmcnt(1)\n"
"v_mfma_f32_16x16x32_bf16 v[156:159], v[184:187], v[132:135], v[156:159]\n"
"v_mfma_f32_16x16x32_bf16 v[156:159], v[186:189], v[24:27], v[156:159]\n"
"ds_read_b128 v[144:147], v177 offset:2720\n"
"v_mfma_f32_16x16x32_bf16 v[148:151], v[44:47], v[136:139], v[148:151]\n"
"s_waitcnt lgkmcnt(1)\n"
"ds_read_b128 v[136:139], v4 offset:28672\n"
"v_mfma_f32_16x16x32_bf16 v[226:229], v[48:51], v[140:143], v[226:229]\n"
"v_mfma_f32_16x16x32_bf16 v[152:155], v[52:55], v[140:143], v[152:155]\n"
"v_mfma_f32_16x16x32_bf16 v[148:151], v[56:59], v[140:143], v[148:151]\n"
"s_waitcnt lgkmcnt(1)\n"
"v_mfma_f32_16x16x32_bf16 v[144:147], v[190:193], v[132:135], v[144:147]\n"
"s_add_u32 s68, s50, 4294965632\n"
"v_exp_f32 v156, v156\n"
"v_min_u32 v175, s68, v176\n"
"ds_read_b128 v[140:143], v175 offset:4048\n"
"v_mfma_f32_16x16x32_bf16 v[144:147], v[192:195], v[24:27], v[144:147]\n"
"s_waitcnt lgkmcnt(1)\n"
"v_exp_f32 v157, v157\n"
"v_exp_f32 v158, v158\n"
"v_mfma_f32_16x16x32_bf16 v[226:229], v[60:63], v[136:139], v[226:229]\n"
"ds_read_b128 v[160:163], v5 offset:28672\n"
"v_mfma_f32_16x16x32_bf16 v[152:155], v[64:67], v[136:139], v[152:155]\n"
"v_cvt_pk_bf16_f32 v156, v156, v157\n"
"ds_write_addtid_b32 v156 offset:53248\n"
"v_mfma_f32_16x16x32_bf16 v[148:151], v[68:71], v[136:139], v[148:151]\n"
"s_waitcnt lgkmcnt(2)\n"
"v_exp_f32 v159, v159\n"
"v_mfma_f32_16x16x32_bf16 v[140:143], v[214:217], v[132:135], v[140:143]\n"
"ds_read_b128 v[136:139], v175 offset:4064\n"
"v_cvt_pk_bf16_f32 v157, v158, v159\n"
"v_mfma_f32_16x16x32_bf16 v[140:143], v[216:219], v[24:27], v[140:143]\n"
"s_waitcnt lgkmcnt(2)\n"
"v_exp_f32 v144, v144\n"
"v_mfma_f32_16x16x32_bf16 v[226:229], v[72:75], v[160:163], v[226:229]\n"
"ds_write_addtid_b32 v157 offset:53504\n"
"ds_read_b128 v[156:159], v4 offset:18432\n"
"v_mfma_f32_16x16x32_bf16 v[152:155], v[76:79], v[160:163], v[152:155]\n"
"s_add_u32 s72, s50, 4294965936\n"
"v_exp_f32 v145, v145\n"
"v_exp_f32 v146, v146\n"
"v_exp_f32 v147, v147\n"
"v_mfma_f32_16x16x32_bf16 v[148:151], v[80:83], v[160:163], v[148:151]\n"
"v_cvt_pk_bf16_f32 v226, v226, v227\n"
"v_cvt_pk_bf16_f32 v144, v144, v145\n"
"ds_write_addtid_b32 v144 offset:53760\n"
"v_cvt_pk_bf16_f32 v145, v146, v147\n"
"ds_write_addtid_b32 v145 offset:54016\n"
"v_cvt_pk_bf16_f32 v227, v228, v229\n"
"s_waitcnt lgkmcnt(4)\n"
"v_mfma_f32_16x16x32_bf16 v[136:139], v[220:223], v[132:135], v[136:139]\n"
"v_cvt_pk_bf16_f32 v229, v154, v155\n"
"v_cvt_pk_bf16_f32 v228, v152, v153\n"
"v_cvt_pk_bf16_f32 v230, v148, v149\n"
"v_mfma_f32_16x16x32_bf16 v[136:139], v[222:225], v[24:27], v[136:139]\n"
"s_waitcnt lgkmcnt(2)\n"
"ds_read_b128 v[144:147], v177 offset:2736\n"
"v_cvt_pk_bf16_f32 v231, v150, v151\n"
"v_mfma_f32_16x16x32_bf16 v[148:151], v[84:87], v[156:159], 0\n"
"v_exp_f32 v140, v140\n"
"v_mfma_f32_16x16x32_bf16 v[160:163], v[88:91], v[156:159], 0\n"
"v_exp_f32 v141, v141\n"
"ds_read_b128 v[152:155], v5 offset:18432\n"
"v_mfma_f32_16x16x32_bf16 v[164:167], v[92:95], v[156:159], 0\n"
"v_cvt_pk_bf16_f32 v140, v140, v141\n"
"v_exp_f32 v142, v142\n"
"ds_write_addtid_b32 v140 offset:54272\n"
"v_max_u32 v174, s72, v176\n"
"s_waitcnt lgkmcnt(2)\n"
"v_mfma_f32_16x16x32_bf16 v[144:147], v[196:199], v[132:135], v[144:147]\n"
"ds_read_b128 v[156:159], v6 offset:5120\n"
"v_mfma_f32_16x16x32_bf16 v[144:147], v[198:201], v[24:27], v[144:147]\n"
"v_exp_f32 v143, v143\n"
"s_nop 0\n"
"v_cvt_pk_bf16_f32 v141, v142, v143\n"
"ds_write_addtid_b32 v141 offset:54528\n"
"ds_read_b128 v[168:171], v174 offset:1360\n"
"ds_read_b128 v[140:143], v4 offset:20480\n"
"s_waitcnt lgkmcnt(5)\n"
"v_mfma_f32_16x16x32_bf16 v[148:151], v[96:99], v[152:155], v[148:151]\n"
"v_exp_f32 v136, v136\n"
"v_mfma_f32_16x16x32_bf16 v[160:163], v[100:103], v[152:155], v[160:163]\n"
"s_waitcnt lgkmcnt(3)\n"
"v_exp_f32 v137, v137\n"
"v_exp_f32 v138, v138\n"
"v_mfma_f32_16x16x32_bf16 v[164:167], v[156:159], v[152:155], v[164:167]\n"
"v_exp_f32 v139, v139\n"
"v_cvt_pk_bf16_f32 v136, v136, v137\n"
"ds_write_addtid_b32 v136 offset:54784\n"
"ds_read_b128 v[152:155], v6 offset:6144\n"
"s_waitcnt lgkmcnt(3)\n"
"v_mfma_f32_16x16x32_bf16 v[168:171], v[232:235], v[132:135], v[168:171]\n"
"v_cvt_pk_bf16_f32 v137, v138, v139\n"
"ds_read_b128 v[156:159], v174 offset:1376\n"
"v_mfma_f32_16x16x32_bf16 v[168:171], v[234:237], v[24:27], v[168:171]\n"
"s_waitcnt lgkmcnt(3)\n"
"v_exp_f32 v144, v144\n"
"v_exp_f32 v145, v145\n"
"v_mfma_f32_16x16x32_bf16 v[148:151], v[104:107], v[140:143], v[148:151]\n"
"ds_write_addtid_b32 v137 offset:55040\n"
"v_exp_f32 v146, v146\n"
"v_mfma_f32_16x16x32_bf16 v[160:163], v[108:111], v[140:143], v[160:163]\n"
"s_waitcnt lgkmcnt(2)\n"
"ds_read_b128 v[232:235], v5 offset:20480\n"
"v_mfma_f32_16x16x32_bf16 v[164:167], v[152:155], v[140:143], v[164:167]\n"
"s_waitcnt lgkmcnt(2)\n"
"ds_read_b128 v[136:139], v6 offset:7168\n"
"v_cvt_pk_bf16_f32 v144, v144, v145\n"
"v_mfma_f32_16x16x32_bf16 v[156:159], v[238:241], v[132:135], v[156:159]\n"
"ds_write_addtid_b32 v144 offset:33792\n"
"v_exp_f32 v147, v147\n"
"s_nop 0\n"
"v_cvt_pk_bf16_f32 v145, v146, v147\n"
"v_mfma_f32_16x16x32_bf16 v[156:159], v[240:243], v[24:27], v[156:159]\n"
"ds_write_addtid_b32 v145 offset:34048\n"
"s_waitcnt lgkmcnt(3)\n"
"v_mfma_f32_16x16x32_bf16 v[148:151], v[112:115], v[232:235], v[148:151]\n"
"v_exp_f32 v168, v168\n"
"ds_read_b128 v[144:147], v4 offset:22528\n"
"ds_read_b128 v[152:155], v5 offset:22528\n"
"v_mfma_f32_16x16x32_bf16 v[160:163], v[116:119], v[232:235], v[160:163]\n"
"s_waitcnt lgkmcnt(4)\n"
"v_exp_f32 v169, v169\n"
"v_exp_f32 v170, v170\n"
"v_mfma_f32_16x16x32_bf16 v[164:167], v[136:139], v[232:235], v[164:167]\n"
"s_waitcnt lgkmcnt(2)\n"
"ds_read_b128 v[140:143], v174 offset:1392\n"
"ds_read_b128 v[232:235], v6 offset:8192\n"
"s_barrier\n"
"ds_read_b128 v[136:139], v175 offset:4080\n"
"v_exp_f32 v171, v171\n"
"v_cvt_pk_bf16_f32 v168, v168, v169\n"
"v_cvt_pk_bf16_f32 v169, v170, v171\n"
"s_waitcnt lgkmcnt(2)\n"
"v_mfma_f32_16x16x32_bf16 v[140:143], v[244:247], v[132:135], v[140:143]\n"
"v_mfma_f32_16x16x32_bf16 v[140:143], v[246:249], v[24:27], v[140:143]\n"
"ds_write_addtid_b32 v168 offset:57856\n"
"ds_write_addtid_b32 v169 offset:58112\n"
"v_mfma_f32_16x16x32_bf16 v[148:151], v[0:3], v[144:147], v[148:151]\n"
"v_exp_f32 v156, v156\n"
"v_exp_f32 v157, v157\n"
"v_mfma_f32_16x16x32_bf16 v[160:163], v[120:123], v[144:147], v[160:163]\n"
"s_waitcnt lgkmcnt(3)\n"
"v_exp_f32 v158, v158\n"
"v_mfma_f32_16x16x32_bf16 v[164:167], v[232:235], v[144:147], v[164:167]\n"
"s_waitcnt lgkmcnt(2)\n"
"v_exp_f32 v159, v159\n"
"ds_read_b128 v[232:235], v6 offset:9216\n"
"v_cvt_pk_bf16_f32 v156, v156, v157\n"
"v_mfma_f32_16x16x32_bf16 v[136:139], v[226:229], v[132:135], v[136:139]\n"
"ds_read_b128 v[132:135], v4 offset:12288\n"
"ds_write_addtid_b32 v156 offset:58368\n"
"v_mfma_f32_16x16x32_bf16 v[136:139], v[228:231], v[24:27], v[136:139]\n"
"v_cvt_pk_bf16_f32 v157, v158, v159\n"
"ds_write_addtid_b32 v157 offset:58624\n"
"v_bfe_u32 v177, v6, 2, 6\n"
"ds_read_b32 v177, v177 offset:1664\n"
"v_mfma_f32_16x16x32_bf16 v[148:151], v[124:127], v[152:155], v[148:151]\n"
"v_exp_f32 v140, v140\n"
"v_exp_f32 v141, v141\n"
"v_exp_f32 v142, v142\n"
"v_cvt_pk_bf16_f32 v140, v140, v141\n"
"v_mfma_f32_16x16x32_bf16 v[160:163], v[128:131], v[152:155], v[160:163]\n"
"s_waitcnt lgkmcnt(4)\n"
"ds_read_b128 v[144:147], v5 offset:12288\n"
"v_exp_f32 v143, v143\n"
"v_mfma_f32_16x16x32_bf16 v[164:167], v[232:235], v[152:155], v[164:167]\n"
"s_waitcnt lgkmcnt(1)\n"
"v_cvt_pk_bf16_f32 v141, v142, v143\n"
"v_add_u32_sdwa v176, v177, v7 dst_sel:DWORD dst_unused:UNUSED_PAD src0_sel:WORD_0 src1_sel:DWORD\n"
"v_mfma_f32_16x16x32_bf16 v[232:235], v[8:11], v[132:135], 0\n"
"ds_write_addtid_b32 v140 offset:58880\n"
"v_mfma_f32_16x16x32_bf16 v[152:155], v[12:15], v[132:135], 0\n"
"s_add_u32 s69, s50, 4294965920\n"
"v_mul_f32 v148, s60, v148\n"
"v_max_u32 v175, s69, v176\n"
"ds_write_addtid_b32 v141 offset:59136\n"
"v_mfma_f32_16x16x32_bf16 v[140:143], v[16:19], v[132:135], 0\n"
"s_waitcnt lgkmcnt(2)\n"
"ds_read_b128 v[156:159], v175 offset:1392\n"
"ds_read_b128 v[132:135], v4 offset:14336\n"
"v_mfma_f32_16x16x32_bf16 v[232:235], v[20:23], v[144:147], v[232:235]\n"
"v_mul_f32 v149, s60, v149\n"
"v_mul_f32 v151, s60, v151\n"
"v_exp_f32 v136, v136\n"
"v_exp_f32 v137, v137\n"
"s_nop 0\n"
"v_cvt_pk_bf16_f32 v136, v136, v137\n"
"v_cvt_pk_bf16_f32 v148, v148, v149\n"
"v_exp_f32 v138, v138\n"
"v_mfma_f32_16x16x32_bf16 v[152:155], v[28:31], v[144:147], v[152:155]\n"
"ds_write_addtid_b32 v136 offset:57344\n"
"v_mul_f32 v162, s60, v162\n"
"v_exp_f32 v139, v139\n"
"v_mfma_f32_16x16x32_bf16 v[140:143], v[32:35], v[144:147], v[140:143]\n"
"s_waitcnt lgkmcnt(1)\n"
"v_mul_f32 v166, s60, v166\n"
"v_cvt_pk_bf16_f32 v137, v138, v139\n"
"v_mfma_f32_16x16x32_bf16 v[232:235], v[36:39], v[132:135], v[232:235]\n"
"ds_write_addtid_b32 v137 offset:57600\n"
"v_mul_f32 v150, s60, v150\n"
"v_mul_f32 v161, s60, v161\n"
"v_mul_f32 v163, s60, v163\n"
"v_mfma_f32_16x16x32_bf16 v[152:155], v[40:43], v[132:135], v[152:155]\n"
"ds_read_b128 v[136:139], v5 offset:14336\n"
"v_mul_f32 v167, s60, v167\n"
"v_mul_f32 v160, s60, v160\n"
"v_cvt_pk_bf16_f32 v149, v150, v151\n"
"v_cvt_pk_bf16_f32 v151, v162, v163\n"
"v_add_u32_sdwa v177, sext(v177), v176 dst_sel:DWORD dst_unused:UNUSED_PAD src0_sel:WORD_1 src1_sel:DWORD\n"
"v_cvt_pk_bf16_f32 v150, v160, v161\n"
"v_mul_f32 v165, s60, v165\n"
"v_mul_f32 v164, s60, v164\n"
"v_cvt_pk_bf16_f32 v27, v166, v167\n"
"v_mfma_f32_16x16x32_bf16 v[156:159], v[244:247], v[148:151], v[156:159]\n"
"s_waitcnt lgkmcnt(1)\n"
"s_barrier\n"
"v_max_u32 v174, s69, v177\n"
"v_cvt_pk_bf16_f32 v26, v164, v165\n"
"s_add_u32 s70, s50, 4294966960\n"
"s_nop 0\n"
"v_mfma_f32_16x16x32_bf16 v[156:159], v[246:249], v[24:27], v[156:159]\n"
"ds_read_b128 v[244:247], v174 offset:1408\n"
"v_mfma_f32_16x16x32_bf16 v[140:143], v[44:47], v[132:135], v[140:143]\n"
"s_waitcnt lgkmcnt(1)\n"
"ds_read_b128 v[132:135], v4 offset:16384\n"
"v_mfma_f32_16x16x32_bf16 v[232:235], v[48:51], v[136:139], v[232:235]\n"
"v_min_u32 v174, s70, v176\n"
"v_mfma_f32_16x16x32_bf16 v[152:155], v[52:55], v[136:139], v[152:155]\n"
"v_mfma_f32_16x16x32_bf16 v[140:143], v[56:59], v[136:139], v[140:143]\n"
"s_waitcnt lgkmcnt(1)\n"
"v_mfma_f32_16x16x32_bf16 v[244:247], v[250:253], v[148:151], v[244:247]\n"
"ds_read_b128 v[248:251], v174 offset:2720\n"
"v_mfma_f32_16x16x32_bf16 v[244:247], v[252:255], v[24:27], v[244:247]\n"
"s_waitcnt lgkmcnt(1)\n"
"v_exp_f32 v156, v156\n"
"v_exp_f32 v157, v157\n"
"v_mfma_f32_16x16x32_bf16 v[232:235], v[60:63], v[132:135], v[232:235]\n"
"ds_read_b128 v[252:255], v5 offset:16384\n"
"ds_read_b128 v[136:139], v4 offset:36864\n"
"v_mfma_f32_16x16x32_bf16 v[152:155], v[64:67], v[132:135], v[152:155]\n"
"v_cvt_pk_bf16_f32 v156, v156, v157\n"
"ds_write_addtid_b32 v156 offset:55296\n"
"v_mfma_f32_16x16x32_bf16 v[140:143], v[68:71], v[132:135], v[140:143]\n"
"s_waitcnt lgkmcnt(3)\n"
"v_exp_f32 v158, v158\n"
"v_mfma_f32_16x16x32_bf16 v[248:251], v[190:193], v[148:151], v[248:251]\n"
"ds_read_b128 v[132:135], v174 offset:2736\n"
"v_mfma_f32_16x16x32_bf16 v[248:251], v[192:195], v[24:27], v[248:251]\n"
"s_waitcnt lgkmcnt(3)\n"
"v_exp_f32 v159, v159\n"
"v_exp_f32 v244, v244\n"
"v_mfma_f32_16x16x32_bf16 v[232:235], v[72:75], v[252:255], v[232:235]\n"
"v_cvt_pk_bf16_f32 v157, v158, v159\n"
"v_mfma_f32_16x16x32_bf16 v[152:155], v[76:79], v[252:255], v[152:155]\n"
"v_exp_f32 v245, v245\n"
"v_exp_f32 v246, v246\n"
"ds_write_addtid_b32 v157 offset:55552\n"
"v_mfma_f32_16x16x32_bf16 v[140:143], v[80:83], v[252:255], v[140:143]\n"
"v_exp_f32 v247, v247\n"
"v_cvt_pk_bf16_f32 v244, v244, v245\n"
"v_cvt_pk_bf16_f32 v232, v232, v233\n"
"v_cvt_pk_bf16_f32 v245, v246, v247\n"
"v_min_u32 v174, s70, v177\n"
"ds_write_addtid_b32 v244 offset:55808\n"
"s_waitcnt lgkmcnt(2)\n"
"v_mfma_f32_16x16x32_bf16 v[132:135], v[196:199], v[148:151], v[132:135]\n"
"ds_write_addtid_b32 v245 offset:56064\n"
"v_cvt_pk_bf16_f32 v233, v234, v235\n"
"v_mfma_f32_16x16x32_bf16 v[132:135], v[198:201], v[24:27], v[132:135]\n"
"ds_read_b128 v[244:247], v174 offset:2752\n"
"v_cvt_pk_bf16_f32 v234, v152, v153\n"
"v_cvt_pk_bf16_f32 v237, v142, v143\n"
"v_cvt_pk_bf16_f32 v236, v140, v141\n"
"v_mfma_f32_16x16x32_bf16 v[252:255], v[84:87], v[136:139], 0\n"
"v_exp_f32 v250, v250\n"
"v_cvt_pk_bf16_f32 v235, v154, v155\n"
"v_mfma_f32_16x16x32_bf16 v[144:147], v[88:91], v[136:139], 0\n"
"v_exp_f32 v251, v251\n"
"v_exp_f32 v132, v132\n"
"ds_read_b128 v[140:143], v5 offset:36864\n"
"v_mfma_f32_16x16x32_bf16 v[152:155], v[92:95], v[136:139], 0\n"
"s_waitcnt lgkmcnt(1)\n"
"ds_read_b128 v[136:139], v6 offset:5120\n"
"v_mfma_f32_16x16x32_bf16 v[244:247], v[202:205], v[148:151], v[244:247]\n"
"v_cvt_pk_bf16_f32 v249, v250, v251\n"
"ds_write_addtid_b32 v249 offset:56576\n"
"s_add_u32 s71, s50, 4294965616\n"
"ds_read_b128 v[248:251], v175 offset:1376\n"
"v_mfma_f32_16x16x32_bf16 v[244:247], v[204:207], v[24:27], v[244:247]\n"
"s_waitcnt lgkmcnt(3)\n"
"v_mfma_f32_16x16x32_bf16 v[252:255], v[96:99], v[140:143], v[252:255]\n"
"v_exp_f32 v133, v133\n"
"ds_read_b128 v[156:159], v4 offset:38912\n"
"v_mfma_f32_16x16x32_bf16 v[144:147], v[100:103], v[140:143], v[144:147]\n"
"v_cvt_pk_bf16_f32 v132, v132, v133\n"
"ds_write_addtid_b32 v132 offset:56832\n"
"s_waitcnt lgkmcnt(4)\n"
"v_mfma_f32_16x16x32_bf16 v[152:155], v[136:139], v[140:143], v[152:155]\n"
"s_waitcnt lgkmcnt(2)\n"
"ds_read_b128 v[136:139], v6 offset:6144\n"
"v_min_u32 v132, s71, v176\n"
"ds_read_b128 v[140:143], v132 offset:4064\n"
"v_mfma_f32_16x16x32_bf16 v[248:251], v[238:241], v[148:151], v[248:251]\n"
"v_exp_f32 v134, v134\n"
"v_exp_f32 v135, v135\n"
"v_exp_f32 v244, v244\n"
"v_exp_f32 v245, v245\n"
"v_cvt_pk_bf16_f32 v133, v134, v135\n"
"ds_write_addtid_b32 v133 offset:57088\n"
"v_mfma_f32_16x16x32_bf16 v[248:251], v[240:243], v[24:27], v[248:251]\n"
"s_waitcnt lgkmcnt(4)\n"
"v_mfma_f32_16x16x32_bf16 v[252:255], v[104:107], v[156:159], v[252:255]\n"
"ds_read_b128 v[238:241], v5 offset:38912\n"
"v_mfma_f32_16x16x32_bf16 v[144:147], v[108:111], v[156:159], v[144:147]\n"
"s_waitcnt lgkmcnt(3)\n"
"v_cvt_pk_bf16_f32 v244, v244, v245\n"
"v_mfma_f32_16x16x32_bf16 v[152:155], v[136:139], v[156:159], v[152:155]\n"
"s_waitcnt lgkmcnt(2)\n"
"ds_read_b128 v[156:159], v132 offset:4080\n"
"v_mfma_f32_16x16x32_bf16 v[140:143], v[220:223], v[148:151], v[140:143]\n"
"v_exp_f32 v246, v246\n"
"v_exp_f32 v247, v247\n"
"v_mfma_f32_16x16x32_bf16 v[140:143], v[222:225], v[24:27], v[140:143]\n"
"s_waitcnt lgkmcnt(1)\n"
"ds_read_b128 v[136:139], v6 offset:7168\n"
"v_cvt_pk_bf16_f32 v245, v246, v247\n"
"v_mfma_f32_16x16x32_bf16 v[252:255], v[112:115], v[238:241], v[252:255]\n"
"ds_write_addtid_b32 v244 offset:34304\n"
"ds_write_addtid_b32 v245 offset:34560\n"
"v_min_u32 v176, s71, v177\n"
"v_mfma_f32_16x16x32_bf16 v[144:147], v[116:119], v[238:241], v[144:147]\n"
"s_waitcnt lgkmcnt(2)\n"
"v_mfma_f32_16x16x32_bf16 v[152:155], v[136:139], v[238:241], v[152:155]\n"
"v_exp_f32 v250, v250\n"
"v_exp_f32 v251, v251\n"
"ds_read_b128 v[242:245], v4 offset:40960\n"
"s_add_u32 s72, s50, 4294964592\n"
"v_cvt_pk_bf16_f32 v249, v250, v251\n"
"ds_read_b128 v[238:241], v6 offset:8192\n"
"v_bfe_u32 v177, v6, 2, 6\n"
"s_waitcnt lgkmcnt(2)\n"
"s_barrier\n"
"ds_write_addtid_b32 v249 offset:59136\n"
"v_mfma_f32_16x16x32_bf16 v[156:159], v[226:229], v[148:151], v[156:159]\n"
"ds_read_b128 v[246:249], v176 offset:4096\n"
"v_mfma_f32_16x16x32_bf16 v[156:159], v[228:231], v[24:27], v[156:159]\n"
"s_waitcnt lgkmcnt(3)\n"
"ds_read_b128 v[132:135], v5 offset:40960\n"
"v_mfma_f32_16x16x32_bf16 v[252:255], v[0:3], v[242:245], v[252:255]\n"
"v_mfma_f32_16x16x32_bf16 v[144:147], v[120:123], v[242:245], v[144:147]\n"
"s_waitcnt lgkmcnt(3)\n"
"v_exp_f32 v142, v142\n"
"v_exp_f32 v143, v143\n"
"v_mfma_f32_16x16x32_bf16 v[152:155], v[238:241], v[242:245], v[152:155]\n"
"s_waitcnt lgkmcnt(1)\n"
"ds_read_b128 v[238:241], v6 offset:9216\n"
"v_mfma_f32_16x16x32_bf16 v[246:249], v[232:235], v[148:151], v[246:249]\n"
"ds_read_b32 v177, v177 offset:1728\n"
"v_cvt_pk_bf16_f32 v141, v142, v143\n"
"v_exp_f32 v156, v156\n"
"ds_read_b128 v[242:245], v4 offset:43008\n"
"v_mfma_f32_16x16x32_bf16 v[246:249], v[234:237], v[24:27], v[246:249]\n"
"s_waitcnt lgkmcnt(3)\n"
"v_exp_f32 v157, v157\n"
"v_exp_f32 v158, v158\n"
"v_mfma_f32_16x16x32_bf16 v[252:255], v[124:127], v[132:135], v[252:255]\n"
"v_cvt_pk_bf16_f32 v156, v156, v157\n"
"v_mfma_f32_16x16x32_bf16 v[144:147], v[128:131], v[132:135], v[144:147]\n"
"v_exp_f32 v159, v159\n"
"ds_write_addtid_b32 v141 offset:57600\n"
"s_waitcnt lgkmcnt(3)\n"
"v_mfma_f32_16x16x32_bf16 v[152:155], v[238:241], v[132:135], v[152:155]\n"
"s_waitcnt lgkmcnt(2)\n"
"ds_read_b128 v[136:139], v5 offset:43008\n"
"v_add_u32_sdwa v176, v177, v7 dst_sel:DWORD dst_unused:UNUSED_PAD src0_sel:WORD_0 src1_sel:DWORD\n"
"v_mul_f32 v254, s60, v254\n"
"s_waitcnt lgkmcnt(2)\n"
"v_mfma_f32_16x16x32_bf16 v[238:241], v[8:11], v[242:245], 0\n"
"ds_write_addtid_b32 v156 offset:57856\n"
"v_cvt_pk_bf16_f32 v157, v158, v159\n"
"v_mul_f32 v253, s60, v253\n"
"v_mfma_f32_16x16x32_bf16 v[132:135], v[12:15], v[242:245], 0\n"
"v_mfma_f32_16x16x32_bf16 v[140:143], v[16:19], v[242:245], 0\n"
"ds_write_addtid_b32 v157 offset:58112\n"
"v_max_u32 v251, s72, v176\n"
"ds_read_b128 v[148:151], v251 offset:2704\n"
"v_mul_f32 v147, s60, v147\n"
"s_waitcnt lgkmcnt(3)\n"
"v_mfma_f32_16x16x32_bf16 v[238:241], v[20:23], v[136:139], v[238:241]\n"
"v_exp_f32 v246, v246\n"
"v_exp_f32 v247, v247\n"
"ds_read_b128 v[242:245], v4 offset:45056\n"
"v_mul_f32 v146, s60, v146\n"
"v_mfma_f32_16x16x32_bf16 v[132:135], v[28:31], v[136:139], v[132:135]\n"
"v_exp_f32 v248, v248\n"
"v_mul_f32 v252, s60, v252\n"
"v_mul_f32 v153, s60, v153\n"
"v_exp_f32 v249, v249\n"
"v_mfma_f32_16x16x32_bf16 v[140:143], v[32:35], v[136:139], v[140:143]\n"
"ds_read_b128 v[136:139], v5 offset:45056\n"
"v_mul_f32 v144, s60, v144\n"
"v_cvt_pk_bf16_f32 v252, v252, v253\n"
"v_cvt_pk_bf16_f32 v246, v246, v247\n"
"v_cvt_pk_bf16_f32 v247, v248, v249\n"
"v_mul_f32 v255, s60, v255\n"
"v_mul_f32 v154, s60, v154\n"
"v_mul_f32 v155, s60, v155\n"
"s_waitcnt lgkmcnt(1)\n"
"v_mfma_f32_16x16x32_bf16 v[238:241], v[36:39], v[242:245], v[238:241]\n"
"ds_write_addtid_b32 v246 offset:58368\n"
"v_mul_f32 v145, s60, v145\n"
"ds_write_addtid_b32 v247 offset:58624\n"
"v_cvt_pk_bf16_f32 v253, v254, v255\n"
"v_mfma_f32_16x16x32_bf16 v[132:135], v[40:43], v[242:245], v[132:135]\n"
"v_cvt_pk_bf16_f32 v254, v144, v145\n"
"v_cvt_pk_bf16_f32 v27, v154, v155\n"
"v_cvt_pk_bf16_f32 v255, v146, v147\n"
"v_mul_f32 v152, s60, v152\n"
"v_cvt_pk_bf16_f32 v26, v152, v153\n"
"v_mfma_f32_16x16x32_bf16 v[148:151], v[184:187], v[252:255], v[148:151]\n"
"ds_read_b128 v[246:249], v4 offset:47104\n"
"v_mfma_f32_16x16x32_bf16 v[148:151], v[186:189], v[24:27], v[148:151]\n"
"s_waitcnt lgkmcnt(1)\n"
"s_barrier\n"
"v_mfma_f32_16x16x32_bf16 v[140:143], v[44:47], v[242:245], v[140:143]\n"
"ds_read_b128 v[144:147], v251 offset:2720\n"
"v_add_u32_sdwa v177, sext(v177), v176 dst_sel:DWORD dst_unused:UNUSED_PAD src0_sel:WORD_1 src1_sel:DWORD\n"
"v_mfma_f32_16x16x32_bf16 v[238:241], v[48:51], v[136:139], v[238:241]\n"
"v_mfma_f32_16x16x32_bf16 v[132:135], v[52:55], v[136:139], v[132:135]\n"
"v_mfma_f32_16x16x32_bf16 v[140:143], v[56:59], v[136:139], v[140:143]\n"
"s_waitcnt lgkmcnt(1)\n"
"v_mfma_f32_16x16x32_bf16 v[238:241], v[60:63], v[246:249], v[238:241]\n"
"ds_read_b128 v[242:245], v5 offset:47104\n"
"v_mfma_f32_16x16x32_bf16 v[132:135], v[64:67], v[246:249], v[132:135]\n"
"s_waitcnt lgkmcnt(1)\n"
"ds_read_b128 v[136:139], v4 offset:49152\n"
"v_mfma_f32_16x16x32_bf16 v[144:147], v[190:193], v[252:255], v[144:147]\n"
"v_mfma_f32_16x16x32_bf16 v[144:147], v[192:195], v[24:27], v[144:147]\n"
"v_exp_f32 v150, v150\n"
"v_exp_f32 v151, v151\n"
"v_mfma_f32_16x16x32_bf16 v[140:143], v[68:71], v[246:249], v[140:143]\n"
"s_waitcnt lgkmcnt(1)\n"
"v_cvt_pk_bf16_f32 v149, v150, v151\n"
"ds_read_b128 v[152:155], v251 offset:2736\n"
"v_mfma_f32_16x16x32_bf16 v[238:241], v[72:75], v[242:245], v[238:241]\n"
"s_nop 0\n"
"v_exp_f32 v144, v144\n"
"ds_write_addtid_b32 v149 offset:53504\n"
"v_mfma_f32_16x16x32_bf16 v[132:135], v[76:79], v[242:245], v[132:135]\n"
"v_exp_f32 v145, v145\n"
"v_mfma_f32_16x16x32_bf16 v[140:143], v[80:83], v[242:245], v[140:143]\n"
"s_waitcnt lgkmcnt(2)\n"
"ds_read_b128 v[248:251], v5 offset:49152\n"
"v_mfma_f32_16x16x32_bf16 v[244:247], v[8:11], v[136:139], 0\n"
"v_cvt_pk_bf16_f32 v238, v238, v239\n"
"v_cvt_pk_bf16_f32 v239, v240, v241\n"
"s_nop 0\n"
"v_cvt_pk_bf16_f32 v240, v132, v133\n"
"v_cvt_pk_bf16_f32 v241, v134, v135\n"
"v_mfma_f32_16x16x32_bf16 v[148:151], v[12:15], v[136:139], 0\n"
"s_waitcnt lgkmcnt(2)\n"
"ds_read_b128 v[132:135], v4 offset:51200\n"
"v_mfma_f32_16x16x32_bf16 v[152:155], v[196:199], v[252:255], v[152:155]\n"
"v_cvt_pk_bf16_f32 v243, v142, v143\n"
"v_cvt_pk_bf16_f32 v242, v140, v141\n"
"v_mfma_f32_16x16x32_bf16 v[152:155], v[198:201], v[24:27], v[152:155]\n"
"v_exp_f32 v146, v146\n"
"v_mfma_f32_16x16x32_bf16 v[140:143], v[16:19], v[136:139], 0\n"
"s_waitcnt lgkmcnt(1)\n"
"v_max_u32 v175, s72, v177\n"
"v_mfma_f32_16x16x32_bf16 v[244:247], v[20:23], v[248:251], v[244:247]\n"
"v_cvt_pk_bf16_f32 v144, v144, v145\n"
"v_mfma_f32_16x16x32_bf16 v[148:151], v[28:31], v[248:251], v[148:151]\n"
"v_exp_f32 v147, v147\n"
"v_exp_f32 v152, v152\n"
"v_cvt_pk_bf16_f32 v145, v146, v147\n"
"v_mfma_f32_16x16x32_bf16 v[140:143], v[32:35], v[248:251], v[140:143]\n"
"s_waitcnt lgkmcnt(0)\n"
"ds_read_b128 v[136:139], v175 offset:2752\n"
"v_mfma_f32_16x16x32_bf16 v[244:247], v[36:39], v[132:135], v[244:247]\n"
"s_add_u32 s73, s50, 4294965632\n"
"ds_write_addtid_b32 v144 offset:53760\n"
"ds_write_addtid_b32 v145 offset:54016\n"
"v_mfma_f32_16x16x32_bf16 v[148:151], v[40:43], v[132:135], v[148:151]\n"
"s_waitcnt lgkmcnt(2)\n"
"ds_read_b128 v[144:147], v4 offset:53248\n"
"ds_read_b128 v[248:251], v5 offset:51200\n"
"v_mfma_f32_16x16x32_bf16 v[136:139], v[202:205], v[252:255], v[136:139]\n"
"v_exp_f32 v153, v153\n"
"v_exp_f32 v154, v154\n"
"v_mfma_f32_16x16x32_bf16 v[136:139], v[204:207], v[24:27], v[136:139]\n"
"v_cvt_pk_bf16_f32 v152, v152, v153\n"
"v_mfma_f32_16x16x32_bf16 v[140:143], v[44:47], v[132:135], v[140:143]\n"
"s_waitcnt lgkmcnt(0)\n"
"v_min_u32 v175, s73, v176\n"
"v_mfma_f32_16x16x32_bf16 v[244:247], v[48:51], v[248:251], v[244:247]\n"
"ds_read_b128 v[156:159], v175 offset:4048\n"
"v_mfma_f32_16x16x32_bf16 v[148:151], v[52:55], v[248:251], v[148:151]\n"
"ds_write_addtid_b32 v152 offset:54272\n"
"v_mfma_f32_16x16x32_bf16 v[140:143], v[56:59], v[248:251], v[140:143]\n"
"v_exp_f32 v155, v155\n"
"v_mfma_f32_16x16x32_bf16 v[244:247], v[60:63], v[144:147], v[244:247]\n"
"s_waitcnt lgkmcnt(1)\n"
"ds_read_b128 v[248:251], v5 offset:53248\n"
"v_cvt_pk_bf16_f32 v153, v154, v155\n"
"v_mfma_f32_16x16x32_bf16 v[156:159], v[214:217], v[252:255], v[156:159]\n"
"v_exp_f32 v136, v136\n"
"v_mfma_f32_16x16x32_bf16 v[156:159], v[216:219], v[24:27], v[156:159]\n"
"ds_write_addtid_b32 v153 offset:54528\n"
"ds_read_b128 v[132:135], v4 offset:55296\n"
"v_mfma_f32_16x16x32_bf16 v[148:151], v[64:67], v[144:147], v[148:151]\n"
"v_exp_f32 v137, v137\n"
"v_mfma_f32_16x16x32_bf16 v[140:143], v[68:71], v[144:147], v[140:143]\n"
"s_waitcnt lgkmcnt(2)\n"
"ds_read_b128 v[152:155], v175 offset:4064\n"
"ds_read_b128 v[144:147], v5 offset:55296\n"
"v_cvt_pk_bf16_f32 v136, v136, v137\n"
"v_mfma_f32_16x16x32_bf16 v[244:247], v[72:75], v[248:251], v[244:247]\n"
"v_exp_f32 v138, v138\n"
"v_exp_f32 v139, v139\n"
"v_mfma_f32_16x16x32_bf16 v[148:151], v[76:79], v[248:251], v[148:151]\n"
"ds_write_addtid_b32 v136 offset:54784\n"
"v_cvt_pk_bf16_f32 v137, v138, v139\n"
"ds_write_addtid_b32 v137 offset:55040\n"
"v_mfma_f32_16x16x32_bf16 v[140:143], v[80:83], v[248:251], v[140:143]\n"
"v_exp_f32 v158, v158\n"
"v_cvt_pk_bf16_f32 v244, v244, v245\n"
"v_cvt_pk_bf16_f32 v245, v246, v247\n"
"ds_read_b128 v[160:163], v6 offset:5120\n"
"s_waitcnt lgkmcnt(5)\n"
"v_mfma_f32_16x16x32_bf16 v[136:139], v[84:87], v[132:135], 0\n"
"s_waitcnt lgkmcnt(1)\n"
"s_barrier\n"
"v_mfma_f32_16x16x32_bf16 v[152:155], v[220:223], v[252:255], v[152:155]\n"
"v_cvt_pk_bf16_f32 v247, v150, v151\n"
"v_mfma_f32_16x16x32_bf16 v[152:155], v[222:225], v[24:27], v[152:155]\n"
"v_cvt_pk_bf16_f32 v246, v148, v149\n"
"v_cvt_pk_bf16_f32 v248, v140, v141\n"
"ds_read_b128 v[164:167], v175 offset:4080\n"
"v_mfma_f32_16x16x32_bf16 v[148:151], v[88:91], v[132:135], 0\n"
"v_mfma_f32_16x16x32_bf16 v[168:171], v[92:95], v[132:135], 0\n"
"ds_read_b128 v[132:135], v6 offset:6144\n"
"v_mfma_f32_16x16x32_bf16 v[136:139], v[96:99], v[144:147], v[136:139]\n"
"v_mfma_f32_16x16x32_bf16 v[148:151], v[100:103], v[144:147], v[148:151]\n"
"s_waitcnt lgkmcnt(2)\n"
"v_cvt_pk_bf16_f32 v249, v142, v143\n"
"ds_read_b128 v[140:143], v4 offset:57344\n"
"v_mfma_f32_16x16x32_bf16 v[168:171], v[160:163], v[144:147], v[168:171]\n"
"ds_read_b128 v[144:147], v5 offset:57344\n"
"v_min_u32 v176, s73, v177\n"
"v_exp_f32 v159, v159\n"
"v_exp_f32 v152, v152\n"
"v_exp_f32 v153, v153\n"
"v_cvt_pk_bf16_f32 v157, v158, v159\n"
"ds_write_addtid_b32 v157 offset:57600\n"
"s_waitcnt lgkmcnt(2)\n"
"v_mfma_f32_16x16x32_bf16 v[136:139], v[104:107], v[140:143], v[136:139]\n"
"ds_read_b128 v[160:163], v4 offset:59392\n"
"v_mfma_f32_16x16x32_bf16 v[164:167], v[226:229], v[252:255], v[164:167]\n"
"v_mfma_f32_16x16x32_bf16 v[164:167], v[228:231], v[24:27], v[164:167]\n"
"ds_read_b128 v[156:159], v6 offset:7168\n"
"v_mfma_f32_16x16x32_bf16 v[148:151], v[108:111], v[140:143], v[148:151]\n"
"ds_read_b128 v[172:175], v176 offset:4096\n"
"v_mfma_f32_16x16x32_bf16 v[168:171], v[132:135], v[140:143], v[168:171]\n"
"s_waitcnt lgkmcnt(4)\n"
"v_exp_f32 v154, v154\n"
"v_exp_f32 v155, v155\n"
"v_mfma_f32_16x16x32_bf16 v[136:139], v[112:115], v[144:147], v[136:139]\n"
"ds_read_b128 v[132:135], v6 offset:8192\n"
"v_mfma_f32_16x16x32_bf16 v[148:151], v[116:119], v[144:147], v[148:151]\n"
"s_waitcnt lgkmcnt(2)\n"
"v_mfma_f32_16x16x32_bf16 v[168:171], v[156:159], v[144:147], v[168:171]\n"
"s_add_u32 s68, s50, 4294964608\n"
"v_exp_f32 v164, v164\n"
"ds_read_b128 v[140:143], v6 offset:9216\n"
"v_mfma_f32_16x16x32_bf16 v[136:139], v[0:3], v[160:163], v[136:139]\n"
"s_waitcnt lgkmcnt(2)\n"
"v_mfma_f32_16x16x32_bf16 v[172:175], v[232:235], v[252:255], v[172:175]\n"
"ds_read_b128 v[250:253], v5 offset:59392\n"
"v_cvt_pk_bf16_f32 v152, v152, v153\n"
"ds_read_b128 v[144:147], v4 offset:24576\n"
"v_mfma_f32_16x16x32_bf16 v[172:175], v[234:237], v[24:27], v[172:175]\n"
"v_exp_f32 v165, v165\n"
"v_cvt_pk_bf16_f32 v153, v154, v155\n"
"ds_write_addtid_b32 v152 offset:57856\n"
"v_mfma_f32_16x16x32_bf16 v[148:151], v[120:123], v[160:163], v[148:151]\n"
"ds_write_addtid_b32 v153 offset:58112\n"
"v_cvt_pk_bf16_f32 v164, v164, v165\n"
"s_waitcnt lgkmcnt(5)\n"
"v_mfma_f32_16x16x32_bf16 v[168:171], v[132:135], v[160:163], v[168:171]\n"
"s_waitcnt lgkmcnt(3)\n"
"v_bfe_u32 v177, v6, 2, 6\n"
"v_mfma_f32_16x16x32_bf16 v[136:139], v[124:127], v[250:253], v[136:139]\n"
"ds_write_addtid_b32 v164 offset:58368\n"
"v_mfma_f32_16x16x32_bf16 v[148:151], v[128:131], v[250:253], v[148:151]\n"
"v_exp_f32 v166, v166\n"
"v_exp_f32 v167, v167\n"
"v_mfma_f32_16x16x32_bf16 v[168:171], v[140:143], v[250:253], v[168:171]\n"
"s_waitcnt lgkmcnt(3)\n"
"v_mfma_f32_16x16x32_bf16 v[250:253], v[8:11], v[144:147], 0\n"
"ds_read_b128 v[132:135], v5 offset:24576\n"
"v_mul_f32 v139, s60, v139\n"
"v_mul_f32 v136, s60, v136\n"
"ds_read_b32 v177, v177 offset:1792\n"
"v_mfma_f32_16x16x32_bf16 v[140:143], v[12:15], v[144:147], 0\n"
"v_exp_f32 v172, v172\n"
"v_cvt_pk_bf16_f32 v165, v166, v167\n"
"v_mfma_f32_16x16x32_bf16 v[152:155], v[16:19], v[144:147], 0\n"
"v_exp_f32 v173, v173\n"
"v_exp_f32 v174, v174\n"
"v_exp_f32 v175, v175\n"
"v_mul_f32 v137, s60, v137\n"
"v_cvt_pk_bf16_f32 v136, v136, v137\n"
"ds_write_addtid_b32 v165 offset:58624\n"
"v_mul_f32 v170, s60, v170\n"
"v_mul_f32 v151, s60, v151\n"
"ds_read_b128 v[156:159], v5 offset:26624\n"
"v_mul_f32 v168, s60, v168\n"
"s_waitcnt lgkmcnt(2)\n"
"v_add_u32_sdwa v255, v177, v7 dst_sel:DWORD dst_unused:UNUSED_PAD src0_sel:WORD_0 src1_sel:DWORD\n"
"ds_read_b128 v[144:147], v4 offset:26624\n"
"v_cvt_pk_bf16_f32 v172, v172, v173\n"
"v_cvt_pk_bf16_f32 v173, v174, v175\n"
"v_mfma_f32_16x16x32_bf16 v[250:253], v[20:23], v[132:135], v[250:253]\n"
"ds_write_addtid_b32 v172 offset:58880\n"
"v_max_u32 v177, s68, v255\n"
"ds_write_addtid_b32 v173 offset:59136\n"
"v_mul_f32 v138, s60, v138\n"
"v_mfma_f32_16x16x32_bf16 v[140:143], v[28:31], v[132:135], v[140:143]\n"
"v_mfma_f32_16x16x32_bf16 v[152:155], v[32:35], v[132:135], v[152:155]\n"
"s_waitcnt lgkmcnt(2)\n"
"v_mfma_f32_16x16x32_bf16 v[250:253], v[36:39], v[144:147], v[250:253]\n"
"v_mul_f32 v150, s60, v150\n"
"ds_read_b128 v[132:135], v177 offset:2688\n"
"v_mul_f32 v148, s60, v148\n"
"v_mfma_f32_16x16x32_bf16 v[140:143], v[40:43], v[144:147], v[140:143]\n"
"s_waitcnt lgkmcnt(1)\n"
"v_cvt_pk_bf16_f32 v137, v138, v139\n"
"s_barrier\n"
"v_cvt_pk_bf16_f32 v139, v150, v151\n"
"v_mul_f32 v171, s60, v171\n"
"v_mul_f32 v149, s60, v149\n"
"v_cvt_pk_bf16_f32 v138, v148, v149\n"
"v_cvt_pk_bf16_f32 v27, v170, v171\n"
"s_waitcnt lgkmcnt(0)\n"
"v_mfma_f32_16x16x32_bf16 v[132:135], v[178:181], v[136:139], v[132:135]\n"
"v_mul_f32 v169, s60, v169\n"
"v_cvt_pk_bf16_f32 v26, v168, v169\n"
"s_nop 1\n"
"v_mfma_f32_16x16x32_bf16 v[132:135], v[180:183], v[24:27], v[132:135]\n"
"s_add_u32 s69, s50, 4294963264\n"
"v_max_u32 v176, s69, v255\n"
"ds_read_b128 v[178:181], v176 offset:4032\n"
"v_mfma_f32_16x16x32_bf16 v[152:155], v[44:47], v[144:147], v[152:155]\n"
"ds_read_b128 v[144:147], v4 offset:28672\n"
"v_mfma_f32_16x16x32_bf16 v[250:253], v[48:51], v[156:159], v[250:253]\n"
"ds_read_b128 v[148:151], v177 offset:2704\n"
"v_mfma_f32_16x16x32_bf16 v[140:143], v[52:55], v[156:159], v[140:143]\n"
"v_mfma_f32_16x16x32_bf16 v[152:155], v[56:59], v[156:159], v[152:155]\n"
"s_waitcnt lgkmcnt(2)\n"
"v_mfma_f32_16x16x32_bf16 v[178:181], v[208:211], v[136:139], v[178:181]\n"
"v_exp_f32 v132, v132\n"
"v_mfma_f32_16x16x32_bf16 v[178:181], v[210:213], v[24:27], v[178:181]\n"
"s_waitcnt lgkmcnt(1)\n"
"ds_read_b128 v[208:211], v5 offset:28672\n"
"v_mfma_f32_16x16x32_bf16 v[250:253], v[60:63], v[144:147], v[250:253]\n"
"v_mfma_f32_16x16x32_bf16 v[140:143], v[64:67], v[144:147], v[140:143]\n"
"v_exp_f32 v133, v133\n"
"v_exp_f32 v134, v134\n"
"v_exp_f32 v135, v135\n"
"v_mfma_f32_16x16x32_bf16 v[152:155], v[68:71], v[144:147], v[152:155]\n"
"s_waitcnt lgkmcnt(1)\n"
"v_cvt_pk_bf16_f32 v132, v132, v133\n"
"v_cvt_pk_bf16_f32 v133, v134, v135\n"
"v_mfma_f32_16x16x32_bf16 v[148:151], v[184:187], v[136:139], v[148:151]\n"
"ds_read_b128 v[144:147], v4 offset:30720\n"
"ds_write_addtid_b32 v132 offset:55296\n"
"ds_write_addtid_b32 v133 offset:55552\n"
"v_mfma_f32_16x16x32_bf16 v[148:151], v[186:189], v[24:27], v[148:151]\n"
"s_waitcnt lgkmcnt(3)\n"
"ds_read_b128 v[132:135], v176 offset:4048\n"
"v_mfma_f32_16x16x32_bf16 v[250:253], v[72:75], v[208:211], v[250:253]\n"
"s_add_u32 s70, s50, 4294964304\n"
"v_min_u32 v213, s70, v255\n"
"v_mfma_f32_16x16x32_bf16 v[140:143], v[76:79], v[208:211], v[140:143]\n"
"v_exp_f32 v178, v178\n"
"v_exp_f32 v179, v179\n"
"v_mfma_f32_16x16x32_bf16 v[152:155], v[80:83], v[208:211], v[152:155]\n"
"v_exp_f32 v180, v180\n"
"v_cvt_pk_bf16_f32 v178, v178, v179\n"
"ds_write_addtid_b32 v178 offset:56320\n"
"v_cvt_pk_bf16_f32 v250, v250, v251\n"
"s_mov_b64 exec, s[52:53]\n"
"v_add_u32 v213, 1040, v213\n"
"s_mov_b64 exec, -1\n"
"v_exp_f32 v181, v181\n"
"v_cvt_pk_bf16_f32 v254, v152, v153\n"
"v_cvt_pk_bf16_f32 v255, v154, v155\n"
"v_cvt_pk_bf16_f32 v179, v180, v181\n"
"s_waitcnt lgkmcnt(1)\n"
"v_mfma_f32_16x16x32_bf16 v[132:135], v[214:217], v[136:139], v[132:135]\n"
"ds_write_addtid_b32 v179 offset:56576\n"
"v_cvt_pk_bf16_f32 v251, v252, v253\n"
"v_cvt_pk_bf16_f32 v252, v140, v141\n"
"v_cvt_pk_bf16_f32 v253, v142, v143\n"
"v_mfma_f32_16x16x32_bf16 v[132:135], v[216:219], v[24:27], v[132:135]\n"
"ds_read_b128 v[178:181], v213 offset:5376\n"
"ds_read_b128 v[140:143], v5 offset:30720\n"
"v_mfma_f32_16x16x32_bf16 v[208:211], v[84:87], v[144:147], 0\n"
"v_exp_f32 v148, v148\n"
"v_mfma_f32_16x16x32_bf16 v[152:155], v[88:91], v[144:147], 0\n"
"v_exp_f32 v149, v149\n"
"s_nop 0\n"
"v_cvt_pk_bf16_f32 v148, v148, v149\n"
"v_mfma_f32_16x16x32_bf16 v[156:159], v[92:95], v[144:147], 0\n"
"v_exp_f32 v150, v150\n"
"ds_read_b128 v[144:147], v6 offset:5120\n"
"ds_write_addtid_b32 v148 offset:55808\n"
"s_waitcnt lgkmcnt(3)\n"
"v_mfma_f32_16x16x32_bf16 v[178:181], v[238:241], v[136:139], v[178:181]\n"
"v_mfma_f32_16x16x32_bf16 v[178:181], v[240:243], v[24:27], v[178:181]\n"
"v_exp_f32 v151, v151\n"
"s_nop 0\n"
"v_cvt_pk_bf16_f32 v149, v150, v151\n"
"v_exp_f32 v132, v132\n"
"s_waitcnt lgkmcnt(2)\n"
"v_mfma_f32_16x16x32_bf16 v[208:211], v[96:99], v[140:143], v[208:211]\n"
"ds_read_b128 v[238:241], v4 offset:32768\n"
"ds_read_b128 v[160:163], v177 offset:2720\n"
"ds_write_addtid_b32 v149 offset:56064\n"
"v_mfma_f32_16x16x32_bf16 v[152:155], v[100:103], v[140:143], v[152:155]\n"
"s_waitcnt lgkmcnt(4)\n"
"v_exp_f32 v133, v133\n"
"v_mfma_f32_16x16x32_bf16 v[156:159], v[144:147], v[140:143], v[156:159]\n"
"s_waitcnt lgkmcnt(1)\n"
"ds_read_b128 v[144:147], v176 offset:4064\n"
"v_mfma_f32_16x16x32_bf16 v[160:163], v[190:193], v[136:139], v[160:163]\n"
"ds_read_b128 v[140:143], v6 offset:6144\n"
"v_mfma_f32_16x16x32_bf16 v[160:163], v[192:195], v[24:27], v[160:163]\n"
"v_exp_f32 v134, v134\n"
"v_exp_f32 v135, v135\n"
"v_cvt_pk_bf16_f32 v132, v132, v133\n"
"v_cvt_pk_bf16_f32 v133, v134, v135\n"
"v_mfma_f32_16x16x32_bf16 v[208:211], v[104:107], v[238:241], v[208:211]\n"
"ds_write_addtid_b32 v132 offset:56832\n"
"ds_write_addtid_b32 v133 offset:57088\n"
"v_mfma_f32_16x16x32_bf16 v[152:155], v[108:111], v[238:241], v[152:155]\n"
"s_waitcnt lgkmcnt(2)\n"
"v_mfma_f32_16x16x32_bf16 v[156:159], v[140:143], v[238:241], v[156:159]\n"
"v_exp_f32 v178, v178\n"
"v_exp_f32 v179, v179\n"
"ds_read_b128 v[238:241], v6 offset:7168\n"
"v_mfma_f32_16x16x32_bf16 v[144:147], v[220:223], v[136:139], v[144:147]\n"
"v_exp_f32 v180, v180\n"
"v_exp_f32 v181, v181\n"
"v_cvt_pk_bf16_f32 v178, v178, v179\n"
"v_cvt_pk_bf16_f32 v179, v180, v181\n"
"ds_read_b128 v[132:135], v5 offset:32768\n"
"s_waitcnt lgkmcnt(2)\n"
"s_barrier\n"
"v_mfma_f32_16x16x32_bf16 v[144:147], v[222:225], v[24:27], v[144:147]\n"
"s_waitcnt lgkmcnt(0)\n"
"ds_read_b128 v[140:143], v213 offset:5392\n"
"v_mfma_f32_16x16x32_bf16 v[208:211], v[112:115], v[132:135], v[208:211]\n"
"ds_write_addtid_b32 v178 offset:57344\n"
"ds_write_addtid_b32 v179 offset:57600\n"
"v_mfma_f32_16x16x32_bf16 v[152:155], v[116:119], v[132:135], v[152:155]\n"
"v_exp_f32 v160, v160\n"
"ds_read_b128 v[178:181], v4 offset:34816\n"
"v_mfma_f32_16x16x32_bf16 v[156:159], v[238:241], v[132:135], v[156:159]\n"
"s_waitcnt lgkmcnt(3)\n"
"v_exp_f32 v161, v161\n"
"v_mfma_f32_16x16x32_bf16 v[140:143], v[244:247], v[136:139], v[140:143]\n"
"ds_read_b128 v[238:241], v6 offset:8192\n"
"v_cvt_pk_bf16_f32 v160, v160, v161\n"
"ds_write_addtid_b32 v160 offset:58368\n"
"ds_read_b128 v[132:135], v213 offset:5408\n"
"v_mfma_f32_16x16x32_bf16 v[140:143], v[246:249], v[24:27], v[140:143]\n"
"s_waitcnt lgkmcnt(3)\n"
"v_mfma_f32_16x16x32_bf16 v[208:211], v[0:3], v[178:181], v[208:211]\n"
"ds_read_b128 v[148:151], v5 offset:34816\n"
"v_bfe_u32 v243, v6, 2, 6\n"
"v_mfma_f32_16x16x32_bf16 v[152:155], v[120:123], v[178:181], v[152:155]\n"
"s_waitcnt lgkmcnt(3)\n"
"v_exp_f32 v144, v144\n"
"v_exp_f32 v145, v145\n"
"v_mfma_f32_16x16x32_bf16 v[156:159], v[238:241], v[178:181], v[156:159]\n"
"s_waitcnt lgkmcnt(1)\n"
"ds_read_b128 v[178:181], v6 offset:9216\n"
"v_cvt_pk_bf16_f32 v144, v144, v145\n"
"v_mfma_f32_16x16x32_bf16 v[132:135], v[250:253], v[136:139], v[132:135]\n"
"v_exp_f32 v140, v140\n"
"ds_read_b128 v[238:241], v4 offset:12288\n"
"v_mfma_f32_16x16x32_bf16 v[132:135], v[252:255], v[24:27], v[132:135]\n"
"s_waitcnt lgkmcnt(2)\n"
"ds_read_b32 v243, v243 offset:1856\n"
"v_mfma_f32_16x16x32_bf16 v[208:211], v[124:127], v[148:151], v[208:211]\n"
"v_exp_f32 v141, v141\n"
"v_mfma_f32_16x16x32_bf16 v[152:155], v[128:131], v[148:151], v[152:155]\n"
"s_waitcnt lgkmcnt(2)\n"
"ds_read_b128 v[136:139], v5 offset:12288\n"
"v_cvt_pk_bf16_f32 v140, v140, v141\n"
"v_exp_f32 v142, v142\n"
"v_mfma_f32_16x16x32_bf16 v[156:159], v[178:181], v[148:151], v[156:159]\n"
"ds_write_addtid_b32 v144 offset:58880\n"
"s_waitcnt lgkmcnt(3)\n"
"v_mfma_f32_16x16x32_bf16 v[178:181], v[8:11], v[238:241], 0\n"
"v_exp_f32 v143, v143\n"
"ds_write_addtid_b32 v140 offset:57856\n"
"v_cvt_pk_bf16_f32 v141, v142, v143\n"
"v_mfma_f32_16x16x32_bf16 v[144:147], v[12:15], v[238:241], 0\n"
"ds_write_addtid_b32 v141 offset:58112\n"
"v_exp_f32 v132, v132\n"
"v_mfma_f32_16x16x32_bf16 v[140:143], v[16:19], v[238:241], 0\n"
"s_waitcnt lgkmcnt(3)\n"
"ds_read_b128 v[238:241], v4 offset:14336\n"
"v_mfma_f32_16x16x32_bf16 v[178:181], v[20:23], v[136:139], v[178:181]\n"
"v_add_u32_sdwa v213, v243, v7 dst_sel:DWORD dst_unused:UNUSED_PAD src0_sel:WORD_0 src1_sel:DWORD\n"
"v_mul_f32 v208, s60, v208\n"
"v_mul_f32 v209, s60, v209\n"
"v_cvt_pk_bf16_f32 v208, v208, v209\n"
"v_mfma_f32_16x16x32_bf16 v[144:147], v[28:31], v[136:139], v[144:147]\n"
"v_exp_f32 v133, v133\n"
"s_add_u32 s71, s50, 4294964592\n"
"v_max_u32 v212, s71, v213\n"
"v_mul_f32 v159, s60, v159\n"
"v_mfma_f32_16x16x32_bf16 v[140:143], v[32:35], v[136:139], v[140:143]\n"
"ds_read_b128 v[148:151], v212 offset:2720\n"
"v_mul_f32 v211, s60, v211\n"
"v_cvt_pk_bf16_f32 v132, v132, v133\n"
"v_mul_f32 v210, s60, v210\n"
"v_mul_f32 v154, s60, v154\n"
"v_mul_f32 v155, s60, v155\n"
"v_mul_f32 v158, s60, v158\n"
"v_cvt_pk_bf16_f32 v27, v158, v159\n"
"v_cvt_pk_bf16_f32 v209, v210, v211\n"
"ds_write_addtid_b32 v132 offset:34304\n"
"s_waitcnt lgkmcnt(2)\n"
"v_mfma_f32_16x16x32_bf16 v[178:181], v[36:39], v[238:241], v[178:181]\n"
"v_mul_f32 v157, s60, v157\n"
"v_cvt_pk_bf16_f32 v211, v154, v155\n"
"s_add_u32 s72, s50, 4294965632\n"
"ds_read_b128 v[132:135], v5 offset:14336\n"
"v_mul_f32 v152, s60, v152\n"
"v_mul_f32 v156, s60, v156\n"
"v_mul_f32 v153, s60, v153\n"
"v_mfma_f32_16x16x32_bf16 v[144:147], v[40:43], v[238:241], v[144:147]\n"
"s_waitcnt lgkmcnt(1)\n"
"v_cvt_pk_bf16_f32 v210, v152, v153\n"
"s_barrier\n"
"v_min_u32 v177, s72, v213\n"
"v_cvt_pk_bf16_f32 v26, v156, v157\n"
"v_mfma_f32_16x16x32_bf16 v[148:151], v[190:193], v[208:211], v[148:151]\n"
"s_nop 0\n"
"v_mfma_f32_16x16x32_bf16 v[148:151], v[192:195], v[24:27], v[148:151]\n"
"ds_read_b128 v[136:139], v212 offset:2736\n"
"v_mfma_f32_16x16x32_bf16 v[140:143], v[44:47], v[238:241], v[140:143]\n"
"s_waitcnt lgkmcnt(1)\n"
"ds_read_b128 v[238:241], v4 offset:16384\n"
"v_mfma_f32_16x16x32_bf16 v[178:181], v[48:51], v[132:135], v[178:181]\n"
"v_mfma_f32_16x16x32_bf16 v[144:147], v[52:55], v[132:135], v[144:147]\n"
"v_mfma_f32_16x16x32_bf16 v[140:143], v[56:59], v[132:135], v[140:143]\n"
"s_nop 0\n"
"v_exp_f32 v148, v148\n"
"s_waitcnt lgkmcnt(1)\n"
"v_mfma_f32_16x16x32_bf16 v[136:139], v[196:199], v[208:211], v[136:139]\n"
"v_exp_f32 v149, v149\n"
"ds_read_b128 v[132:135], v177 offset:4048\n"
"v_cvt_pk_bf16_f32 v148, v148, v149\n"
"v_mfma_f32_16x16x32_bf16 v[136:139], v[198:201], v[24:27], v[136:139]\n"
"s_waitcnt lgkmcnt(1)\n"
"v_mfma_f32_16x16x32_bf16 v[178:181], v[60:63], v[238:241], v[178:181]\n"
"v_exp_f32 v150, v150\n"
"v_mfma_f32_16x16x32_bf16 v[144:147], v[64:67], v[238:241], v[144:147]\n"
"ds_write_addtid_b32 v148 offset:53248\n"
"ds_read_b128 v[152:155], v5 offset:16384\n"
"v_mfma_f32_16x16x32_bf16 v[140:143], v[68:71], v[238:241], v[140:143]\n"
"s_waitcnt lgkmcnt(2)\n"
"v_exp_f32 v151, v151\n"
"v_mfma_f32_16x16x32_bf16 v[132:135], v[214:217], v[208:211], v[132:135]\n"
"ds_read_b128 v[238:241], v177 offset:4064\n"
"v_cvt_pk_bf16_f32 v149, v150, v151\n"
"v_mfma_f32_16x16x32_bf16 v[132:135], v[216:219], v[24:27], v[132:135]\n"
"s_waitcnt lgkmcnt(1)\n"
"v_mfma_f32_16x16x32_bf16 v[178:181], v[72:75], v[152:155], v[178:181]\n"
"v_exp_f32 v136, v136\n"
"v_exp_f32 v137, v137\n"
"v_mfma_f32_16x16x32_bf16 v[144:147], v[76:79], v[152:155], v[144:147]\n"
"ds_write_addtid_b32 v149 offset:53504\n"
"v_cvt_pk_bf16_f32 v136, v136, v137\n"
"v_exp_f32 v138, v138\n"
"v_exp_f32 v139, v139\n"
"v_mfma_f32_16x16x32_bf16 v[140:143], v[80:83], v[152:155], v[140:143]\n"
"ds_write_addtid_b32 v136 offset:53760\n"
"ds_read_b128 v[148:151], v4 offset:18432\n"
"v_cvt_pk_bf16_f32 v137, v138, v139\n"
"ds_write_addtid_b32 v137 offset:54016\n"
"v_cvt_pk_bf16_f32 v178, v178, v179\n"
"s_nop 2\n"
"v_cvt_pk_bf16_f32 v183, v142, v143\n"
"ds_read_b128 v[136:139], v177 offset:4080\n"
"v_cvt_pk_bf16_f32 v182, v140, v141\n"
"s_waitcnt lgkmcnt(5)\n"
"v_mfma_f32_16x16x32_bf16 v[238:241], v[220:223], v[208:211], v[238:241]\n"
"v_cvt_pk_bf16_f32 v179, v180, v181\n"
"v_cvt_pk_bf16_f32 v180, v144, v145\n"
"v_cvt_pk_bf16_f32 v181, v146, v147\n"
"v_mfma_f32_16x16x32_bf16 v[238:241], v[222:225], v[24:27], v[238:241]\n"
"s_waitcnt lgkmcnt(2)\n"
"v_exp_f32 v132, v132\n"
"v_mfma_f32_16x16x32_bf16 v[140:143], v[84:87], v[148:151], 0\n"
"v_exp_f32 v133, v133\n"
"v_mfma_f32_16x16x32_bf16 v[152:155], v[88:91], v[148:151], 0\n"
"s_add_u32 s73, s50, 4294964288\n"
"ds_read_b128 v[144:147], v5 offset:18432\n"
"v_cvt_pk_bf16_f32 v132, v132, v133\n"
"v_mfma_f32_16x16x32_bf16 v[156:159], v[92:95], v[148:151], 0\n"
"s_waitcnt lgkmcnt(1)\n"
"v_exp_f32 v134, v134\n"
"v_exp_f32 v135, v135\n"
"v_mfma_f32_16x16x32_bf16 v[136:139], v[226:229], v[208:211], v[136:139]\n"
"ds_write_addtid_b32 v132 offset:54272\n"
"ds_read_b128 v[148:151], v6 offset:5120\n"
"ds_read_b128 v[160:163], v212 offset:2704\n"
"v_cvt_pk_bf16_f32 v133, v134, v135\n"
"v_mfma_f32_16x16x32_bf16 v[136:139], v[228:231], v[24:27], v[136:139]\n"
"s_waitcnt lgkmcnt(3)\n"
"v_exp_f32 v238, v238\n"
"v_mfma_f32_16x16x32_bf16 v[140:143], v[96:99], v[144:147], v[140:143]\n"
"ds_write_addtid_b32 v133 offset:54528\n"
"v_min_u32 v212, s73, v213\n"
"ds_read_b128 v[132:135], v4 offset:20480\n"
"v_mfma_f32_16x16x32_bf16 v[152:155], v[100:103], v[144:147], v[152:155]\n"
"s_waitcnt lgkmcnt(3)\n"
"v_exp_f32 v239, v239\n"
"v_exp_f32 v240, v240\n"
"v_mfma_f32_16x16x32_bf16 v[156:159], v[148:151], v[144:147], v[156:159]\n"
"s_mov_b64 exec, s[52:53]\n"
"v_add_u32 v212, 1040, v212\n"
"s_mov_b64 exec, -1\n"
"ds_read_b128 v[144:147], v6 offset:6144\n"
"s_waitcnt lgkmcnt(3)\n"
"v_mfma_f32_16x16x32_bf16 v[160:163], v[184:187], v[208:211], v[160:163]\n"
"v_exp_f32 v241, v241\n"
"ds_read_b128 v[148:151], v212 offset:5392\n"
"v_mfma_f32_16x16x32_bf16 v[160:163], v[186:189], v[24:27], v[160:163]\n"
"s_waitcnt lgkmcnt(2)\n"
"ds_read_b128 v[184:187], v5 offset:20480\n"
"v_cvt_pk_bf16_f32 v238, v238, v239\n"
"v_exp_f32 v136, v136\n"
"v_mfma_f32_16x16x32_bf16 v[140:143], v[104:107], v[132:135], v[140:143]\n"
"ds_write_addtid_b32 v238 offset:54784\n"
"v_mfma_f32_16x16x32_bf16 v[152:155], v[108:111], v[132:135], v[152:155]\n"
"s_waitcnt lgkmcnt(3)\n"
"v_exp_f32 v137, v137\n"
"v_mfma_f32_16x16x32_bf16 v[156:159], v[144:147], v[132:135], v[156:159]\n"
"s_waitcnt lgkmcnt(2)\n"
"v_exp_f32 v138, v138\n"
"v_cvt_pk_bf16_f32 v239, v240, v241\n"
"v_cvt_pk_bf16_f32 v136, v136, v137\n"
"v_mfma_f32_16x16x32_bf16 v[148:151], v[244:247], v[208:211], v[148:151]\n"
"ds_write_addtid_b32 v136 offset:33792\n"
"ds_write_addtid_b32 v239 offset:55040\n"
"v_mfma_f32_16x16x32_bf16 v[148:151], v[246:249], v[24:27], v[148:151]\n"
"v_exp_f32 v139, v139\n"
"v_exp_f32 v160, v160\n"
"ds_read_b128 v[238:241], v6 offset:7168\n"
"v_cvt_pk_bf16_f32 v137, v138, v139\n"
"ds_write_addtid_b32 v137 offset:34048\n"
"s_waitcnt lgkmcnt(5)\n"
"v_mfma_f32_16x16x32_bf16 v[140:143], v[112:115], v[184:187], v[140:143]\n"
"v_exp_f32 v161, v161\n"
"s_nop 0\n"
"v_cvt_pk_bf16_f32 v160, v160, v161\n"
"ds_read_b128 v[132:135], v212 offset:5408\n"
"v_mfma_f32_16x16x32_bf16 v[152:155], v[116:119], v[184:187], v[152:155]\n"
"s_waitcnt lgkmcnt(2)\n"
"v_exp_f32 v162, v162\n"
"v_mfma_f32_16x16x32_bf16 v[156:159], v[238:241], v[184:187], v[156:159]\n"
"s_waitcnt lgkmcnt(1)\n"
"s_barrier\n"
"ds_read_b128 v[184:187], v6 offset:8192\n"
"ds_read_b128 v[136:139], v4 offset:22528\n"
"s_waitcnt lgkmcnt(2)\n"
"v_mfma_f32_16x16x32_bf16 v[132:135], v[250:253], v[208:211], v[132:135]\n"
"v_exp_f32 v163, v163\n"
"ds_read_b128 v[238:241], v212 offset:5424\n"
"v_cvt_pk_bf16_f32 v161, v162, v163\n"
"v_exp_f32 v148, v148\n"
"v_exp_f32 v149, v149\n"
"v_mfma_f32_16x16x32_bf16 v[132:135], v[252:255], v[24:27], v[132:135]\n"
"ds_write_addtid_b32 v160 offset:57856\n"
"s_waitcnt lgkmcnt(2)\n"
"v_mfma_f32_16x16x32_bf16 v[140:143], v[0:3], v[136:139], v[140:143]\n"
"ds_read_b128 v[144:147], v5 offset:22528\n"
"v_cvt_pk_bf16_f32 v148, v148, v149\n"
"v_mfma_f32_16x16x32_bf16 v[152:155], v[120:123], v[136:139], v[152:155]\n"
"v_exp_f32 v150, v150\n"
"ds_write_addtid_b32 v161 offset:58112\n"
"v_exp_f32 v151, v151\n"
"v_mfma_f32_16x16x32_bf16 v[156:159], v[184:187], v[136:139], v[156:159]\n"
"s_waitcnt lgkmcnt(3)\n"
"ds_read_b128 v[184:187], v6 offset:9216\n"
"v_cvt_pk_bf16_f32 v149, v150, v151\n"
"v_mfma_f32_16x16x32_bf16 v[238:241], v[178:181], v[208:211], v[238:241]\n"
"v_exp_f32 v132, v132\n"
"ds_read_b128 v[136:139], v5 offset:36864\n"
"ds_read_b128 v[208:211], v4 offset:36864\n"
"v_exp_f32 v133, v133\n"
"v_mfma_f32_16x16x32_bf16 v[238:241], v[180:183], v[24:27], v[238:241]\n"
"ds_write_addtid_b32 v148 offset:58368\n"
"s_waitcnt lgkmcnt(5)\n"
"v_mfma_f32_16x16x32_bf16 v[140:143], v[124:127], v[144:147], v[140:143]\n"
"v_exp_f32 v134, v134\n"
"v_mfma_f32_16x16x32_bf16 v[152:155], v[128:131], v[144:147], v[152:155]\n"
"ds_write_addtid_b32 v149 offset:58624\n"
"v_bfe_u32 v213, v6, 2, 6\n"
"ds_read_b32 v213, v213 offset:1920\n"
"s_waitcnt lgkmcnt(5)\n"
"v_mfma_f32_16x16x32_bf16 v[156:159], v[184:187], v[144:147], v[156:159]\n"
"s_waitcnt lgkmcnt(3)\n"
"v_exp_f32 v135, v135\n"
"v_mfma_f32_16x16x32_bf16 v[184:187], v[8:11], v[208:211], 0\n"
"v_cvt_pk_bf16_f32 v132, v132, v133\n"
"v_cvt_pk_bf16_f32 v133, v134, v135\n"
"ds_write_addtid_b32 v132 offset:58880\n"
"s_add_u32 s68, s50, 4294964576\n"
"v_mfma_f32_16x16x32_bf16 v[8:11], v[12:15], v[208:211], 0\n"
"ds_write_addtid_b32 v133 offset:59136\n"
"v_mul_f32 v140, s60, v140\n"
"s_waitcnt lgkmcnt(2)\n"
"v_add_u32_sdwa v212, v213, v7 dst_sel:DWORD dst_unused:UNUSED_PAD src0_sel:WORD_0 src1_sel:DWORD\n"
"v_exp_f32 v238, v238\n"
"v_mfma_f32_16x16x32_bf16 v[12:15], v[16:19], v[208:211], 0\n"
"ds_read_b128 v[208:211], v4 offset:38912\n"
"v_max_u32 v177, s68, v212\n"
"v_mfma_f32_16x16x32_bf16 v[184:187], v[20:23], v[136:139], v[184:187]\n"
"v_exp_f32 v239, v239\n"
"ds_read_b128 v[16:19], v177 offset:2720\n"
"v_mul_f32 v141, s60, v141\n"
"v_mfma_f32_16x16x32_bf16 v[8:11], v[28:31], v[136:139], v[8:11]\n"
"v_exp_f32 v240, v240\n"
"v_exp_f32 v241, v241\n"
"v_mfma_f32_16x16x32_bf16 v[12:15], v[32:35], v[136:139], v[12:15]\n"
"s_waitcnt lgkmcnt(1)\n"
"v_cvt_pk_bf16_f32 v238, v238, v239\n"
"v_cvt_pk_bf16_f32 v239, v240, v241\n"
"v_mfma_f32_16x16x32_bf16 v[184:187], v[36:39], v[208:211], v[184:187]\n"
"ds_write_addtid_b32 v238 offset:57344\n"
"v_mul_f32 v143, s60, v143\n"
"v_mfma_f32_16x16x32_bf16 v[8:11], v[40:43], v[208:211], v[8:11]\n"
"ds_write_addtid_b32 v239 offset:57600\n"
"v_mul_f32 v157, s60, v157\n"
"ds_read_b128 v[238:241], v5 offset:38912\n"
"v_mul_f32 v155, s60, v155\n"
"v_mul_f32 v156, s60, v156\n"
"v_cvt_pk_bf16_f32 v26, v156, v157\n"
"v_mul_f32 v152, s60, v152\n"
"v_cvt_pk_bf16_f32 v140, v140, v141\n"
"v_mul_f32 v142, s60, v142\n"
"v_mul_f32 v153, s60, v153\n"
"v_cvt_pk_bf16_f32 v141, v142, v143\n"
"v_mul_f32 v154, s60, v154\n"
"v_cvt_pk_bf16_f32 v143, v154, v155\n"
"v_cvt_pk_bf16_f32 v142, v152, v153\n"
"s_waitcnt lgkmcnt(1)\n"
"s_barrier\n"
"v_mfma_f32_16x16x32_bf16 v[16:19], v[190:193], v[140:143], v[16:19]\n"
"s_add_u32 s69, s50, 4294963232\n"
"v_mul_f32 v159, s60, v159\n"
"v_mul_f32 v158, s60, v158\n"
"v_cvt_pk_bf16_f32 v27, v158, v159\n"
"v_add_u32_sdwa v213, sext(v213), v212 dst_sel:DWORD dst_unused:UNUSED_PAD src0_sel:WORD_1 src1_sel:DWORD\n"
"s_nop 0\n"
"v_mfma_f32_16x16x32_bf16 v[16:19], v[192:195], v[24:27], v[16:19]\n"
"ds_read_b128 v[188:191], v177 offset:2736\n"
"v_max_u32 v177, s68, v213\n"
"v_mfma_f32_16x16x32_bf16 v[12:15], v[44:47], v[208:211], v[12:15]\n"
"s_waitcnt lgkmcnt(1)\n"
"v_mfma_f32_16x16x32_bf16 v[184:187], v[48:51], v[238:241], v[184:187]\n"
"ds_read_b128 v[192:195], v4 offset:40960\n"
"v_mfma_f32_16x16x32_bf16 v[8:11], v[52:55], v[238:241], v[8:11]\n"
"v_mfma_f32_16x16x32_bf16 v[12:15], v[56:59], v[238:241], v[12:15]\n"
"s_waitcnt lgkmcnt(1)\n"
"v_mfma_f32_16x16x32_bf16 v[188:191], v[196:199], v[140:143], v[188:191]\n"
"s_add_u32 s70, s50, 4294964272\n"
"ds_read_b128 v[208:211], v177 offset:2752\n"
"v_exp_f32 v18, v18\n"
"v_mfma_f32_16x16x32_bf16 v[188:191], v[198:201], v[24:27], v[188:191]\n"
"s_waitcnt lgkmcnt(1)\n"
"ds_read_b128 v[196:199], v5 offset:40960\n"
"v_mfma_f32_16x16x32_bf16 v[184:187], v[60:63], v[192:195], v[184:187]\n"
"v_exp_f32 v19, v19\n"
"v_mfma_f32_16x16x32_bf16 v[8:11], v[64:67], v[192:195], v[8:11]\n"
"v_cvt_pk_bf16_f32 v17, v18, v19\n"
"ds_write_addtid_b32 v17 offset:55552\n"
"v_mfma_f32_16x16x32_bf16 v[12:15], v[68:71], v[192:195], v[12:15]\n"
"s_waitcnt lgkmcnt(2)\n"
"v_max_u32 v239, s69, v213\n"
"v_mfma_f32_16x16x32_bf16 v[208:211], v[202:205], v[140:143], v[208:211]\n"
"v_exp_f32 v188, v188\n"
"v_mfma_f32_16x16x32_bf16 v[208:211], v[204:207], v[24:27], v[208:211]\n"
"s_waitcnt lgkmcnt(1)\n"
"v_min_u32 v177, s70, v212\n"
"ds_read_b128 v[192:195], v239 offset:4096\n"
"v_mfma_f32_16x16x32_bf16 v[184:187], v[72:75], v[196:199], v[184:187]\n"
"v_exp_f32 v189, v189\n"
"v_exp_f32 v190, v190\n"
"v_mfma_f32_16x16x32_bf16 v[8:11], v[76:79], v[196:199], v[8:11]\n"
"ds_read_b128 v[200:203], v4 offset:43008\n"
"v_cvt_pk_bf16_f32 v188, v188, v189\n"
"ds_write_addtid_b32 v188 offset:55808\n"
"v_exp_f32 v191, v191\n"
"v_mfma_f32_16x16x32_bf16 v[12:15], v[80:83], v[196:199], v[12:15]\n"
"v_cvt_pk_bf16_f32 v184, v184, v185\n"
"v_cvt_pk_bf16_f32 v185, v186, v187\n"
"v_cvt_pk_bf16_f32 v189, v190, v191\n"
"ds_write_addtid_b32 v189 offset:56064\n"
"v_cvt_pk_bf16_f32 v187, v10, v11\n"
"v_min_u32 v176, s70, v213\n"
"s_waitcnt lgkmcnt(3)\n"
"v_mfma_f32_16x16x32_bf16 v[192:195], v[232:235], v[140:143], v[192:195]\n"
"s_mov_b64 exec, s[52:53]\n"
"v_add_u32 v177, 1040, v177\n"
"v_add_u32 v176, 1040, v176\n"
"s_mov_b64 exec, -1\n"
"ds_read_b128 v[196:199], v177 offset:5408\n"
"v_mfma_f32_16x16x32_bf16 v[192:195], v[234:237], v[24:27], v[192:195]\n"
"s_waitcnt lgkmcnt(3)\n"
"ds_read_b128 v[232:235], v5 offset:43008\n"
"v_cvt_pk_bf16_f32 v189, v14, v15\n"
"v_cvt_pk_bf16_f32 v188, v12, v13\n"
"v_mfma_f32_16x16x32_bf16 v[204:207], v[84:87], v[200:203], 0\n"
"v_exp_f32 v208, v208\n"
"v_mfma_f32_16x16x32_bf16 v[236:239], v[88:91], v[200:203], 0\n"
"v_exp_f32 v209, v209\n"
"v_mfma_f32_16x16x32_bf16 v[240:243], v[92:95], v[200:203], 0\n"
"s_waitcnt lgkmcnt(1)\n"
"v_exp_f32 v210, v210\n"
"v_exp_f32 v211, v211\n"
"v_cvt_pk_bf16_f32 v208, v208, v209\n"
"v_mfma_f32_16x16x32_bf16 v[196:199], v[250:253], v[140:143], v[196:199]\n"
"v_exp_f32 v192, v192\n"
"ds_read_b128 v[200:203], v6 offset:5120\n"
"v_cvt_pk_bf16_f32 v209, v210, v211\n"
"v_mfma_f32_16x16x32_bf16 v[196:199], v[252:255], v[24:27], v[196:199]\n"
"s_waitcnt lgkmcnt(1)\n"
"v_exp_f32 v193, v193\n"
"v_cvt_pk_bf16_f32 v186, v8, v9\n"
"v_mfma_f32_16x16x32_bf16 v[204:207], v[96:99], v[232:235], v[204:207]\n"
"v_cvt_pk_bf16_f32 v192, v192, v193\n"
"ds_write_addtid_b32 v208 offset:56320\n"
"ds_write_addtid_b32 v209 offset:56576\n"
"ds_write_addtid_b32 v192 offset:56832\n"
"v_mfma_f32_16x16x32_bf16 v[236:239], v[100:103], v[232:235], v[236:239]\n"
"v_exp_f32 v194, v194\n"
"v_exp_f32 v195, v195\n"
"s_nop 0\n"
"v_cvt_pk_bf16_f32 v193, v194, v195\n"
"v_max_u32 v213, s69, v212\n"
"ds_read_b128 v[8:11], v213 offset:4064\n"
"v_exp_f32 v198, v198\n"
"ds_read_b128 v[208:211], v4 offset:45056\n"
"s_waitcnt lgkmcnt(5)\n"
"v_mfma_f32_16x16x32_bf16 v[240:243], v[200:203], v[232:235], v[240:243]\n"
"ds_write_addtid_b32 v193 offset:57088\n"
"s_waitcnt lgkmcnt(2)\n"
"v_mfma_f32_16x16x32_bf16 v[8:11], v[220:223], v[140:143], v[8:11]\n"
"ds_read_b128 v[200:203], v6 offset:6144\n"
"v_mfma_f32_16x16x32_bf16 v[8:11], v[222:225], v[24:27], v[8:11]\n"
"s_waitcnt lgkmcnt(2)\n"
"ds_read_b128 v[232:235], v213 offset:4080\n"
"v_mfma_f32_16x16x32_bf16 v[204:207], v[104:107], v[208:211], v[204:207]\n"
"s_add_u32 s71, s50, 4294963248\n"
"ds_read_b128 v[190:193], v5 offset:45056\n"
"v_mfma_f32_16x16x32_bf16 v[236:239], v[108:111], v[208:211], v[236:239]\n"
"s_waitcnt lgkmcnt(2)\n"
"v_exp_f32 v199, v199\n"
"v_mfma_f32_16x16x32_bf16 v[240:243], v[200:203], v[208:211], v[240:243]\n"
"s_waitcnt lgkmcnt(1)\n"
"v_exp_f32 v10, v10\n"
"v_mfma_f32_16x16x32_bf16 v[232:235], v[226:229], v[140:143], v[232:235]\n"
"ds_read_b128 v[208:211], v177 offset:5424\n"
"v_mfma_f32_16x16x32_bf16 v[232:235], v[228:231], v[24:27], v[232:235]\n"
"v_cvt_pk_bf16_f32 v197, v198, v199\n"
"s_add_u32 s72, s50, 4294964288\n"
"ds_write_addtid_b32 v197 offset:34560\n"
"s_waitcnt lgkmcnt(2)\n"
"v_mfma_f32_16x16x32_bf16 v[204:207], v[112:115], v[190:193], v[204:207]\n"
"ds_read_b128 v[200:203], v6 offset:7168\n"
"ds_read_b128 v[194:197], v4 offset:47104\n"
"v_bfe_u32 v4, v6, 2, 6\n"
"v_mfma_f32_16x16x32_bf16 v[236:239], v[116:119], v[190:193], v[236:239]\n"
"s_waitcnt lgkmcnt(1)\n"
"v_mfma_f32_16x16x32_bf16 v[240:243], v[200:203], v[190:193], v[240:243]\n"
"v_exp_f32 v11, v11\n"
"v_exp_f32 v232, v232\n"
"v_mfma_f32_16x16x32_bf16 v[208:211], v[178:181], v[140:143], v[208:211]\n"
"ds_read_b128 v[190:193], v6 offset:8192\n"
"ds_read_b32 v4, v4 offset:1984\n"
"v_exp_f32 v233, v233\n"
"s_barrier\n"
"v_mfma_f32_16x16x32_bf16 v[208:211], v[180:183], v[24:27], v[208:211]\n"
"s_waitcnt lgkmcnt(0)\n"
"v_add_u32_sdwa v213, v4, v7 dst_sel:DWORD dst_unused:UNUSED_PAD src0_sel:WORD_0 src1_sel:DWORD\n"
"ds_read_b128 v[198:201], v176 offset:5440\n"
"v_cvt_pk_bf16_f32 v232, v232, v233\n"
"v_mfma_f32_16x16x32_bf16 v[204:207], v[0:3], v[194:197], v[204:207]\n"
"ds_read_b128 v[0:3], v5 offset:47104\n"
"v_mfma_f32_16x16x32_bf16 v[236:239], v[120:123], v[194:197], v[236:239]\n"
"v_exp_f32 v234, v234\n"
"v_mfma_f32_16x16x32_bf16 v[240:243], v[190:193], v[194:197], v[240:243]\n"
"ds_read_b128 v[190:193], v6 offset:9216\n"
"v_max_u32 v203, s71, v213\n"
"ds_write_addtid_b32 v232 offset:58880\n"
"v_cvt_pk_bf16_f32 v9, v10, v11\n"
"ds_write_addtid_b32 v9 offset:58624\n"
"v_exp_f32 v235, v235\n"
"v_exp_f32 v208, v208\n"
"v_cvt_pk_bf16_f32 v233, v234, v235\n"
"s_waitcnt lgkmcnt(4)\n"
"v_mfma_f32_16x16x32_bf16 v[198:201], v[184:187], v[140:143], v[198:201]\n"
"ds_write_addtid_b32 v233 offset:59136\n"
"v_exp_f32 v209, v209\n"
"v_mfma_f32_16x16x32_bf16 v[198:201], v[186:189], v[24:27], v[198:201]\n"
"s_waitcnt lgkmcnt(4)\n"
"ds_read_b128 v[184:187], v203 offset:4048\n"
"v_mfma_f32_16x16x32_bf16 v[204:207], v[124:127], v[0:3], v[204:207]\n"
"v_cvt_pk_bf16_f32 v208, v208, v209\n"
"v_min_u32 v177, s72, v213\n"
"s_mov_b64 exec, s[52:53]\n"
"v_add_u32 v177, 1040, v177\n"
"s_mov_b64 exec, -1\n"
"v_mfma_f32_16x16x32_bf16 v[236:239], v[128:131], v[0:3], v[236:239]\n"
"s_waitcnt lgkmcnt(4)\n"
"v_exp_f32 v210, v210\n"
"v_exp_f32 v211, v211\n"
"v_mul_f32 v205, s60, v205\n"
"v_mfma_f32_16x16x32_bf16 v[240:243], v[190:193], v[0:3], v[240:243]\n"
"ds_read_b128 v[194:197], v203 offset:4064\n"
"v_mul_f32 v207, s60, v207\n"
"v_mul_f32 v204, s60, v204\n"
"v_cvt_pk_bf16_f32 v204, v204, v205\n"
"ds_read_b128 v[188:191], v177 offset:5392\n"
"v_mul_f32 v236, s60, v236\n"
"v_mul_f32 v206, s60, v206\n"
"v_cvt_pk_bf16_f32 v205, v206, v207\n"
"v_mul_f32 v241, s60, v241\n"
"v_mul_f32 v242, s60, v242\n"
"v_mul_f32 v240, s60, v240\n"
"v_cvt_pk_bf16_f32 v26, v240, v241\n"
"v_mul_f32 v243, s60, v243\n"
"v_mul_f32 v237, s60, v237\n"
"v_cvt_pk_bf16_f32 v206, v236, v237\n"
"v_mul_f32 v239, s60, v239\n"
"v_cvt_pk_bf16_f32 v27, v242, v243\n"
"v_mul_f32 v238, s60, v238\n"
"v_cvt_pk_bf16_f32 v207, v238, v239\n"
"v_exp_f32 v198, v198\n"
"s_waitcnt lgkmcnt(2)\n"
"v_mfma_f32_16x16x32_bf16 v[184:187], v[214:217], v[204:207], v[184:187]\n"
"ds_write_addtid_b32 v208 offset:57344\n"
"v_cvt_pk_bf16_f32 v209, v210, v211\n"
"ds_write_addtid_b32 v209 offset:57600\n"
"v_exp_f32 v199, v199\n"
"ds_read_b128 v[212:215], v177 offset:5408\n"
"v_exp_f32 v200, v200\n"
"v_mfma_f32_16x16x32_bf16 v[184:187], v[216:219], v[24:27], v[184:187]\n"
"s_waitcnt lgkmcnt(4)\n"
"ds_read_b128 v[208:211], v203 offset:4080\n"
"v_cvt_pk_bf16_f32 v198, v198, v199\n"
"v_mfma_f32_16x16x32_bf16 v[194:197], v[220:223], v[204:207], v[194:197]\n"
"v_exp_f32 v201, v201\n"
"v_mfma_f32_16x16x32_bf16 v[194:197], v[222:225], v[24:27], v[194:197]\n"
"ds_write_addtid_b32 v198 offset:57856\n"
"v_cvt_pk_bf16_f32 v199, v200, v201\n"
"ds_write_addtid_b32 v199 offset:58112\n"
"s_waitcnt lgkmcnt(6)\n"
"v_mfma_f32_16x16x32_bf16 v[188:191], v[244:247], v[204:207], v[188:191]\n"
"v_exp_f32 v184, v184\n"
"v_exp_f32 v185, v185\n"
"ds_read_b128 v[198:201], v177 offset:5424\n"
"v_mfma_f32_16x16x32_bf16 v[188:191], v[246:249], v[24:27], v[188:191]\n"
"s_waitcnt lgkmcnt(1)\n"
"s_barrier\n"
"v_mfma_f32_16x16x32_bf16 v[208:211], v[226:229], v[204:207], v[208:211]\n"
"v_exp_f32 v186, v186\n"
"v_exp_f32 v187, v187\n"
"v_cvt_pk_bf16_f32 v184, v184, v185\n"
"v_mfma_f32_16x16x32_bf16 v[208:211], v[228:231], v[24:27], v[208:211]\n"
"ds_write_addtid_b32 v184 offset:53248\n"
"v_exp_f32 v194, v194\n"
"v_exp_f32 v195, v195\n"
"v_cvt_pk_bf16_f32 v185, v186, v187\n"
"v_cvt_pk_bf16_f32 v194, v194, v195\n"
"ds_write_addtid_b32 v185 offset:53504\n"
"v_exp_f32 v196, v196\n"
"v_exp_f32 v197, v197\n"
"s_nop 0\n"
"v_cvt_pk_bf16_f32 v195, v196, v197\n"
"v_mfma_f32_16x16x32_bf16 v[212:215], v[250:253], v[204:207], v[212:215]\n"
"v_exp_f32 v188, v188\n"
"ds_write_addtid_b32 v194 offset:53760\n"
"ds_write_addtid_b32 v195 offset:54016\n"
"v_mfma_f32_16x16x32_bf16 v[212:215], v[252:255], v[24:27], v[212:215]\n"
"s_waitcnt lgkmcnt(4)\n"
"v_exp_f32 v189, v189\n"
"v_mfma_f32_16x16x32_bf16 v[198:201], v[178:181], v[204:207], v[198:201]\n"
"v_exp_f32 v190, v190\n"
"v_exp_f32 v191, v191\n"
"v_exp_f32 v208, v208\n"
"v_exp_f32 v209, v209\n"
"v_exp_f32 v210, v210\n"
"v_cvt_pk_bf16_f32 v208, v208, v209\n"
"v_mfma_f32_16x16x32_bf16 v[198:201], v[180:183], v[24:27], v[198:201]\n"
"ds_write_addtid_b32 v208 offset:4096\n"
"v_cvt_pk_bf16_f32 v188, v188, v189\n"
"v_exp_f32 v211, v211\n"
"v_exp_f32 v212, v212\n"
"ds_write_addtid_b32 v188 offset:54272\n"
"v_exp_f32 v213, v213\n"
"v_exp_f32 v214, v214\n"
"v_exp_f32 v215, v215\n"
"v_exp_f32 v198, v198\n"
"v_cvt_pk_bf16_f32 v189, v190, v191\n"
"v_cvt_pk_bf16_f32 v212, v212, v213\n"
"v_cvt_pk_bf16_f32 v213, v214, v215\n"
"ds_write_addtid_b32 v212 offset:54784\n"
"ds_write_addtid_b32 v213 offset:55040\n"
"v_exp_f32 v199, v199\n"
"ds_write_addtid_b32 v189 offset:54528\n"
"v_exp_f32 v200, v200\n"
"v_exp_f32 v201, v201\n"
"v_cvt_pk_bf16_f32 v198, v198, v199\n"
"v_cvt_pk_bf16_f32 v199, v200, v201\n"
"ds_write_addtid_b32 v198 offset:4608\n"
"ds_write_addtid_b32 v199 offset:4864\n"
"v_cvt_pk_bf16_f32 v209, v210, v211\n"
"ds_write_addtid_b32 v209 offset:4352\n"
"s_waitcnt lgkmcnt(0)\n"
"s_barrier\n"
"s_barrier\n"
"s_barrier\n"
    ,
"s_add_u32 s50, s54, 53248\n"
"s_lshr_b32 s41, s40, 1\n"
"s_sub_u32 s55, 0, s41\n"
"s_mul_i32 s51, s41, 4096\n"
"s_lshl_b32 s41, s40, 10\n"
"s_add_u32 s51, s51, s41\n"
"s_lshl_b32 s65, s40, 2\n"
"s_add_u32 s65, s65, 32192\n"
"s_andn2_b32 s41, s45, 7\n"
"s_add_u32 s56, s56, s41\n"
"s_addc_u32 s57, s57, 0\n"
"s_add_u32 s66, s66, s41\n"
"s_addc_u32 s67, s67, 0\n"
"s_mul_i32 s68, s40, 1552\n"
"s_add_u32 s68, s68, 57344\n"
"s_setprio 0\n"
"s_movk_i32 s40, 768\n"
"s_mov_b64 s[62:63], 0\n"
"s_movk_i32 s64, 0x207\n"
"s_add_u32 s58, s56, 128\n"
"s_addc_u32 s59, s57, 0\n"
"s_add_u32 s60, s56, 256\n"
"s_addc_u32 s61, s57, 0\n"
"v_subrev_u32 v179, 24576, v10\n"
"s_waitcnt vmcnt(0)\n"
"s_barrier\n"
"s_add_u32 s42, s46, 110976\n"
"s_addc_u32 s43, s47, 0\n"
"s_add_u32 m0, s50, 27648\n"
"s_nop 0\n"
"global_load_lds_dwordx4 v0, s[42:43]\n"
"s_add_u32 m0, s50, 28672\n"
"s_nop 0\n"
"global_load_lds_dwordx4 v1, s[42:43]\n"
"s_add_u32 m0, s50, 29696\n"
"s_nop 0\n"
"global_load_lds_dwordx4 v2, s[42:43]\n"
"s_add_u32 s42, s46, 147840\n"
"s_addc_u32 s43, s47, 0\n"
"s_add_u32 m0, s50, 49152\n"
"s_nop 0\n"
"global_load_lds_dwordx4 v0, s[42:43]\n"
"s_add_u32 m0, s50, 50176\n"
"s_nop 0\n"
"global_load_lds_dwordx4 v1, s[42:43]\n"
"s_add_u32 m0, s50, 51200\n"
"s_nop 0\n"
"global_load_lds_dwordx4 v2, s[42:43]\n"
"s_add_u32 s42, s46, 184704\n"
"s_addc_u32 s43, s47, 0\n"
"s_add_u32 m0, s50, 52224\n"
"s_nop 0\n"
"global_load_lds_dwordx4 v0, s[42:43]\n"
"s_add_u32 m0, s50, 53248\n"
"s_nop 0\n"
"global_load_lds_dwordx4 v1, s[42:43]\n"
"s_add_u32 m0, s50, 54272\n"
"s_nop 0\n"
"global_load_lds_dwordx4 v2, s[42:43]\n"
"s_add_u32 s42, s46, 768\n"
"s_addc_u32 s43, s47, 0\n"
"s_add_u32 m0, s50, 0\n"
"s_nop 0\n"
"global_load_lds_dwordx4 v0, s[42:43]\n"
"s_add_u32 m0, s50, 1024\n"
"s_nop 0\n"
"global_load_lds_dwordx4 v1, s[42:43]\n"
"s_add_u32 m0, s50, 2048\n"
"s_nop 0\n"
"global_load_lds_dwordx4 v2, s[42:43]\n"
"s_waitcnt vmcnt(9)\n"
"s_barrier\n"
"s_add_u32 s42, s46, 37632\n"
"s_addc_u32 s43, s47, 0\n"
"s_add_u32 m0, s50, 3072\n"
"s_nop 0\n"
"global_load_lds_dwordx4 v0, s[42:43]\n"
"s_add_u32 m0, s50, 4096\n"
"s_nop 0\n"
"global_load_lds_dwordx4 v1, s[42:43]\n"
"s_add_u32 m0, s50, 5120\n"
"s_nop 0\n"
"global_load_lds_dwordx4 v2, s[42:43]\n"
"s_add_u32 s42, s46, 74496\n"
"s_addc_u32 s43, s47, 0\n"
"s_add_u32 m0, s50, 4294918144\n"
"s_nop 0\n"
"global_load_lds_dwordx4 v0, s[42:43]\n"
"s_add_u32 m0, s50, 4294919168\n"
"s_nop 0\n"
"global_load_lds_dwordx4 v1, s[42:43]\n"
"s_add_u32 m0, s50, 4294920192\n"
"s_nop 0\n"
"global_load_lds_dwordx4 v2, s[42:43]\n"
"s_waitcnt vmcnt(9)\n"
"s_barrier\n"
"s_add_u32 s42, s46, 111360\n"
"s_addc_u32 s43, s47, 0\n"
"s_add_u32 m0, s50, 4294921216\n"
"s_nop 0\n"
"global_load_lds_dwordx4 v0, s[42:43]\n"
"s_add_u32 m0, s50, 4294922240\n"
"s_nop 0\n"
"global_load_lds_dwordx4 v1, s[42:43]\n"
"s_add_u32 m0, s50, 4294923264\n"
"s_nop 0\n"
"global_load_lds_dwordx4 v2, s[42:43]\n"
"s_barrier\n"
"s_add_u32 s42, s46, 148224\n"
"s_addc_u32 s43, s47, 0\n"
"s_add_u32 m0, s50, 24576\n"
"s_nop 0\n"
"global_load_lds_dwordx4 v0, s[42:43]\n"
"s_add_u32 m0, s50, 25600\n"
"s_nop 0\n"
"global_load_lds_dwordx4 v1, s[42:43]\n"
"s_add_u32 m0, s50, 26624\n"
"s_nop 0\n"
"global_load_lds_dwordx4 v2, s[42:43]\n"
"s_add_u32 s42, s46, 185088\n"
"s_addc_u32 s43, s47, 0\n"
"s_add_u32 m0, s50, 27648\n"
"s_nop 0\n"
"global_load_lds_dwordx4 v0, s[42:43]\n"
"ds_read_b128 v[180:183], v12 offset:24576\n"
"s_add_u32 m0, s50, 28672\n"
"s_nop 0\n"
"global_load_lds_dwordx4 v1, s[42:43]\n"
"s_add_u32 m0, s50, 29696\n"
"s_nop 0\n"
"global_load_lds_dwordx4 v2, s[42:43]\n"
"s_mov_b32 m0, 3584\n"
"s_bfm_b64 exec, 6, 0\n"
"global_load_lds_dword v22, s[46:47]\n"
"s_mov_b64 exec, 31\n"
"global_load_lds_dword v23, s[48:49]\n"
"s_mov_b64 exec, -1\n"
"s_waitcnt vmcnt(17)\n"
"ds_read_b64_tr_b16 v[26:27], v10 offset:29504\n"
"ds_read_b64_tr_b16 v[24:25], v10 offset:28736\n"
"ds_read_u16 v23, v21 offset:1152\n"
"ds_read_b64_tr_b16 v[34:35], v10 offset:29472\n"
"s_andn2_b32 s41, 576, s55\n"
"v_add_u32 v178, s41, v15\n"
"ds_read_u16 v178, v178 offset:224\n"
"ds_read_b128 v[184:187], v12 offset:30720\n"
"ds_read_b64_tr_b16 v[28:29], v10 offset:28672\n"
"s_waitcnt lgkmcnt(2)\n"
"v_mad_u32_u24 v178, v178, s40, v16\n"
"s_add_u32 m0, s51, 102400\n"
"s_nop 0\n"
"global_load_lds_dwordx4 v178, s[56:57]\n"
"ds_read_b64_tr_b16 v[32:33], v10 offset:28704\n"
"ds_read_b128 v[188:191], v12 offset:36864\n"
"s_add_u32 m0, s51, 104448\n"
"s_nop 0\n"
"global_load_lds_dwordx4 v178, s[58:59]\n"
"s_add_u32 m0, s51, 106496\n"
"s_nop 0\n"
"global_load_lds_dwordx4 v178, s[60:61]\n"
"s_waitcnt vmcnt(17)\n"
"ds_read_b64_tr_b16 v[38:39], v10 offset:32576\n"
"s_and_b32 s41, 32, s55\n"
"v_add_u32 v178, s41, v15\n"
"ds_read_u16 v178, v178 offset:352\n"
"ds_read_b64_tr_b16 v[30:31], v10 offset:29440\n"
"ds_read_u16 v22, v21 offset:1184\n"
"ds_read_b64_tr_b16 v[44:45], v10 offset:31776\n"
"ds_read_b128 v[192:195], v13 offset:24576\n"
"ds_read_b64_tr_b16 v[46:47], v10 offset:32544\n"
"s_waitcnt lgkmcnt(5)\n"
"v_mad_u32_u24 v178, v178, s40, v16\n"
"s_waitcnt lgkmcnt(3)\n"
"v_mad_u32_u24 v176, v22, s40, v18\n"
"s_add_u32 m0, s51, 114688\n"
"s_nop 0\n"
"global_load_lds_dwordx4 v178, s[56:57]\n"
"ds_read_b64_tr_b16 v[42:43], v10 offset:32512\n"
"v_mad_u32_u24 v22, v22, s40, v19\n"
"s_add_u32 m0, s51, 116736\n"
"s_nop 0\n"
"global_load_lds_dwordx4 v178, s[58:59]\n"
"s_add_u32 m0, s51, 118784\n"
"s_nop 0\n"
"global_load_lds_dwordx4 v178, s[60:61]\n"
"v_mfma_f32_16x16x32_bf16 v[196:199], v[180:183], v[28:31], 0\n"
"v_mfma_f32_16x16x32_bf16 v[200:203], v[180:183], v[32:35], 0\n"
"v_mad_u32_u24 v177, v23, s40, v18\n"
"v_mfma_f32_16x16x32_bf16 v[204:207], v[180:183], v[24:27], 0\n"
"ds_read_b128 v[180:183], v13 offset:30720\n"
"v_mfma_f32_16x16x32_bf16 v[212:215], v[184:187], v[28:31], 0\n"
"s_waitcnt vmcnt(17)\n"
"ds_read_b64_tr_b16 v[40:41], v10 offset:31744\n"
"v_mfma_f32_16x16x32_bf16 v[216:219], v[184:187], v[32:35], 0\n"
"ds_read_b64_tr_b16 v[58:59], v179 offset:4928\n"
"ds_read_b64_tr_b16 v[36:37], v10 offset:31808\n"
"v_mfma_f32_16x16x32_bf16 v[220:223], v[184:187], v[24:27], 0\n"
"v_mfma_f32_16x16x32_bf16 v[224:227], v[188:191], v[28:31], 0\n"
"ds_read_b128 v[184:187], v13 offset:36864\n"
"v_mad_u32_u24 v23, v23, s40, v19\n"
"v_mfma_f32_16x16x32_bf16 v[228:231], v[188:191], v[32:35], 0\n"
"ds_read_b64_tr_b16 v[50:51], v179 offset:4864\n"
"v_mfma_f32_16x16x32_bf16 v[232:235], v[188:191], v[24:27], 0\n"
"s_waitcnt lgkmcnt(4)\n"
"ds_read_b128 v[188:191], v12 offset:26624\n"
"v_mfma_f32_16x16x32_bf16 v[196:199], v[192:195], v[40:43], v[196:199]\n"
"ds_read_b64_tr_b16 v[48:49], v179 offset:4096\n"
"v_mfma_f32_16x16x32_bf16 v[200:203], v[192:195], v[44:47], v[200:203]\n"
"s_waitcnt vmcnt(14)\n"
"ds_read_b64_tr_b16 v[60:61], v179 offset:7168\n"
"s_waitcnt lgkmcnt(5)\n"
"v_mfma_f32_16x16x32_bf16 v[204:207], v[192:195], v[36:39], v[204:207]\n"
"ds_read_b64_tr_b16 v[68:69], v179 offset:7232\n"
"ds_read_b64_tr_b16 v[52:53], v179 offset:4128\n"
"v_mfma_f32_16x16x32_bf16 v[212:215], v[180:183], v[40:43], v[212:215]\n"
"ds_read_b64_tr_b16 v[54:55], v179 offset:4896\n"
"v_mfma_f32_16x16x32_bf16 v[216:219], v[180:183], v[44:47], v[216:219]\n"
"ds_read_b64_tr_b16 v[56:57], v179 offset:4160\n"
"v_mfma_f32_16x16x32_bf16 v[220:223], v[180:183], v[36:39], v[220:223]\n"
"s_waitcnt lgkmcnt(8)\n"
"v_mfma_f32_16x16x32_bf16 v[224:227], v[184:187], v[40:43], v[224:227]\n"
"ds_read_b64_tr_b16 v[64:65], v179 offset:7200\n"
"v_mfma_f32_16x16x32_bf16 v[228:231], v[184:187], v[44:47], v[228:231]\n"
"ds_read_b128 v[192:195], v12 offset:32768\n"
"v_mfma_f32_16x16x32_bf16 v[232:235], v[184:187], v[36:39], v[232:235]\n"
"s_waitcnt lgkmcnt(7)\n"
"ds_read_b128 v[180:183], v12 offset:38912\n"
"v_mfma_f32_16x16x32_bf16 v[196:199], v[188:191], v[48:51], v[196:199]\n"
"s_waitcnt lgkmcnt(4)\n"
"ds_read_b64_tr_b16 v[70:71], v179 offset:8000\n"
"v_mfma_f32_16x16x32_bf16 v[200:203], v[188:191], v[52:55], v[200:203]\n"
"s_waitcnt lgkmcnt(4)\n"
"ds_read_b64_tr_b16 v[62:63], v179 offset:7936\n"
"v_mfma_f32_16x16x32_bf16 v[204:207], v[188:191], v[56:59], v[204:207]\n"
"s_waitcnt lgkmcnt(3)\n"
"ds_read_b128 v[188:191], v13 offset:32768\n"
"v_mfma_f32_16x16x32_bf16 v[212:215], v[192:195], v[48:51], v[212:215]\n"
"ds_read_b64_tr_b16 v[66:67], v179 offset:7968\n"
"s_waitcnt vmcnt(11)\n"
"v_mfma_f32_16x16x32_bf16 v[216:219], v[192:195], v[52:55], v[216:219]\n"
"ds_read_b128 v[184:187], v13 offset:26624\n"
"v_mfma_f32_16x16x32_bf16 v[220:223], v[192:195], v[56:59], v[220:223]\n"
"s_waitcnt lgkmcnt(5)\n"
"ds_read_b64_tr_b16 v[82:83], v10 offset:54080\n"
"v_mfma_f32_16x16x32_bf16 v[224:227], v[180:183], v[48:51], v[224:227]\n"
"ds_read_b64_tr_b16 v[80:81], v10 offset:53312\n"
"v_mfma_f32_16x16x32_bf16 v[228:231], v[180:183], v[52:55], v[228:231]\n"
"ds_read_b128 v[192:195], v13 offset:38912\n"
"v_mfma_f32_16x16x32_bf16 v[232:235], v[180:183], v[56:59], v[232:235]\n"
"s_waitcnt lgkmcnt(3)\n"
"ds_read_b64_tr_b16 v[78:79], v10 offset:54048\n"
"v_mfma_f32_16x16x32_bf16 v[196:199], v[184:187], v[60:63], v[196:199]\n"
"ds_read_b64_tr_b16 v[74:75], v10 offset:54016\n"
"v_mfma_f32_16x16x32_bf16 v[200:203], v[184:187], v[64:67], v[200:203]\n"
"ds_read_b128 v[178:181], v12 offset:28672\n"
"v_mfma_f32_16x16x32_bf16 v[204:207], v[184:187], v[68:71], v[204:207]\n"
"ds_read_b128 v[182:185], v12 offset:34816\n"
"ds_read_b64_tr_b16 v[76:77], v10 offset:53280\n"
"v_mfma_f32_16x16x32_bf16 v[212:215], v[188:191], v[60:63], v[212:215]\n"
"s_barrier\n"
"v_mfma_f32_16x16x32_bf16 v[216:219], v[188:191], v[64:67], v[216:219]\n"
"ds_read_b64_tr_b16 v[72:73], v10 offset:53248\n"
"v_mfma_f32_16x16x32_bf16 v[220:223], v[188:191], v[68:71], v[220:223]\n"
"s_waitcnt lgkmcnt(6)\n"
"ds_read_b128 v[186:189], v12 offset:40960\n"
"v_mfma_f32_16x16x32_bf16 v[224:227], v[192:195], v[60:63], v[224:227]\n"
"s_waitcnt vmcnt(8)\n"
"ds_read_b64_tr_b16 v[92:93], v10 offset:56384\n"
"v_mfma_f32_16x16x32_bf16 v[228:231], v[192:195], v[64:67], v[228:231]\n"
"ds_read_b64_tr_b16 v[84:85], v10 offset:56320\n"
"v_mfma_f32_16x16x32_bf16 v[232:235], v[192:195], v[68:71], v[232:235]\n"
"s_waitcnt lgkmcnt(3)\n"
"ds_read_b64_tr_b16 v[94:95], v10 offset:57152\n"
"v_mfma_f32_16x16x32_bf16 v[196:199], v[178:181], v[72:75], v[196:199]\n"
"ds_read_b64_tr_b16 v[86:87], v10 offset:57088\n"
"v_mfma_f32_16x16x32_bf16 v[200:203], v[178:181], v[76:79], v[200:203]\n"
"ds_read_b64_tr_b16 v[88:89], v10 offset:56352\n"
"v_mfma_f32_16x16x32_bf16 v[204:207], v[178:181], v[80:83], v[204:207]\n"
"ds_read_b64_tr_b16 v[90:91], v10 offset:57120\n"
"v_mfma_f32_16x16x32_bf16 v[212:215], v[182:185], v[72:75], v[212:215]\n"
"v_mfma_f32_16x16x32_bf16 v[216:219], v[182:185], v[76:79], v[216:219]\n"
"ds_read_b128 v[190:193], v13 offset:28672\n"
"v_mfma_f32_16x16x32_bf16 v[220:223], v[182:185], v[80:83], v[220:223]\n"
"s_waitcnt lgkmcnt(7)\n"
"ds_read_b128 v[178:181], v13 offset:34816\n"
"v_mfma_f32_16x16x32_bf16 v[224:227], v[186:189], v[72:75], v[224:227]\n"
"v_mfma_f32_16x16x32_bf16 v[228:231], v[186:189], v[76:79], v[228:231]\n"
"v_mfma_f32_16x16x32_bf16 v[232:235], v[186:189], v[80:83], v[232:235]\n"
"s_waitcnt lgkmcnt(1)\n"
"v_mfma_f32_16x16x32_bf16 v[196:199], v[190:193], v[84:87], v[196:199]\n"
"ds_read_b128 v[238:241], v13 offset:40960\n"
"v_mfma_f32_16x16x32_bf16 v[200:203], v[190:193], v[88:91], v[200:203]\n"
"s_barrier\n"
"v_mfma_f32_16x16x32_bf16 v[204:207], v[190:193], v[92:95], v[204:207]\n"
"s_waitcnt lgkmcnt(1)\n"
"s_setprio 2\n"
"v_mfma_f32_16x16x32_bf16 v[212:215], v[178:181], v[84:87], v[212:215]\n"
"ds_read_b128 v[242:245], v12 offset:43008\n"
"v_mfma_f32_16x16x32_bf16 v[216:219], v[178:181], v[88:91], v[216:219]\n"
"s_nop 0\n"
"v_cvt_pk_bf16_f32 v210, v200, v201\n"
"v_mfma_f32_16x16x32_bf16 v[220:223], v[178:181], v[92:95], v[220:223]\n"
"s_waitcnt lgkmcnt(1)\n"
"v_cvt_pk_bf16_f32 v237, v206, v207\n"
"v_mfma_f32_16x16x32_bf16 v[224:227], v[238:241], v[84:87], v[224:227]\n"
"v_cvt_pk_bf16_f32 v211, v202, v203\n"
"v_cvt_pk_bf16_f32 v187, v214, v215\n"
"v_mfma_f32_16x16x32_bf16 v[228:231], v[238:241], v[88:91], v[228:231]\n"
"ds_read_b128 v[188:191], v13 offset:43008\n"
"v_cvt_pk_bf16_f32 v185, v198, v199\n"
"v_mfma_f32_16x16x32_bf16 v[232:235], v[238:241], v[92:95], v[232:235]\n"
"s_waitcnt lgkmcnt(1)\n"
"v_cvt_pk_bf16_f32 v184, v196, v197\n"
"v_cvt_pk_bf16_f32 v236, v204, v205\n"
"v_mfma_f32_16x16x32_bf16 v[200:203], v[242:245], v[28:31], 0\n"
"v_cvt_pk_bf16_f32 v186, v212, v213\n"
"v_cvt_pk_bf16_f32 v212, v216, v217\n"
"v_mfma_f32_16x16x32_bf16 v[204:207], v[242:245], v[32:35], 0\n"
"ds_read_b128 v[178:181], v12 offset:45056\n"
"v_cvt_pk_bf16_f32 v195, v226, v227\n"
"v_mfma_f32_16x16x32_bf16 v[214:217], v[242:245], v[24:27], 0\n"
"s_waitcnt lgkmcnt(1)\n"
"ds_read_b128 v[196:199], v13 offset:45056\n"
"v_mfma_f32_16x16x32_bf16 v[200:203], v[188:191], v[40:43], v[200:203]\n"
"v_cvt_pk_bf16_f32 v238, v220, v221\n"
"v_cvt_pk_bf16_f32 v194, v224, v225\n"
"v_mfma_f32_16x16x32_bf16 v[204:207], v[188:191], v[44:47], v[204:207]\n"
"v_cvt_pk_bf16_f32 v220, v228, v229\n"
"v_mfma_f32_16x16x32_bf16 v[214:217], v[188:191], v[36:39], v[214:217]\n"
"s_waitcnt lgkmcnt(1)\n"
"ds_read_b128 v[188:191], v13 offset:47104\n"
"v_mfma_f32_16x16x32_bf16 v[200:203], v[178:181], v[48:51], v[200:203]\n"
"v_cvt_pk_bf16_f32 v239, v222, v223\n"
"v_cvt_pk_bf16_f32 v247, v234, v235\n"
"v_mfma_f32_16x16x32_bf16 v[204:207], v[178:181], v[52:55], v[204:207]\n"
"ds_read_b128 v[240:243], v12 offset:47104\n"
"v_mfma_f32_16x16x32_bf16 v[214:217], v[178:181], v[56:59], v[214:217]\n"
"s_waitcnt lgkmcnt(2)\n"
"v_mfma_f32_16x16x32_bf16 v[200:203], v[196:199], v[60:63], v[200:203]\n"
"v_cvt_pk_bf16_f32 v213, v218, v219\n"
"s_setprio 0\n"
"v_mfma_f32_16x16x32_bf16 v[204:207], v[196:199], v[64:67], v[204:207]\n"
"ds_read_b128 v[178:181], v12 offset:49152\n"
"v_mfma_f32_16x16x32_bf16 v[214:217], v[196:199], v[68:71], v[214:217]\n"
"s_waitcnt lgkmcnt(1)\n"
"v_cvt_pk_bf16_f32 v246, v232, v233\n"
"v_mfma_f32_16x16x32_bf16 v[200:203], v[240:243], v[72:75], v[200:203]\n"
"v_cvt_pk_bf16_f32 v221, v230, v231\n"
"v_mfma_f32_16x16x32_bf16 v[204:207], v[240:243], v[76:79], v[204:207]\n"
"v_mfma_f32_16x16x32_bf16 v[214:217], v[240:243], v[80:83], v[214:217]\n"
"v_mfma_f32_16x16x32_bf16 v[200:203], v[188:191], v[84:87], v[200:203]\n"
"ds_read_b128 v[224:227], v13 offset:49152\n"
"v_mfma_f32_16x16x32_bf16 v[204:207], v[188:191], v[88:91], v[204:207]\n"
"ds_read_b128 v[228:231], v12 offset:51200\n"
"v_mfma_f32_16x16x32_bf16 v[214:217], v[188:191], v[92:95], v[214:217]\n"
"s_waitcnt lgkmcnt(2)\n"
"v_mfma_f32_16x16x32_bf16 v[232:235], v[178:181], v[28:31], 0\n"
"s_nop 1\n"
"v_cvt_pk_bf16_f32 v196, v200, v201\n"
"v_mfma_f32_16x16x32_bf16 v[240:243], v[178:181], v[32:35], 0\n"
"ds_read_b128 v[188:191], v13 offset:51200\n"
"v_cvt_pk_bf16_f32 v197, v202, v203\n"
"v_mfma_f32_16x16x32_bf16 v[250:253], v[178:181], v[24:27], 0\n"
"s_waitcnt lgkmcnt(2)\n"
"v_mfma_f32_16x16x32_bf16 v[232:235], v[224:227], v[40:43], v[232:235]\n"
"v_cvt_pk_bf16_f32 v249, v216, v217\n"
"v_mfma_f32_16x16x32_bf16 v[240:243], v[224:227], v[44:47], v[240:243]\n"
"v_cvt_pk_bf16_f32 v222, v204, v205\n"
"v_mfma_f32_16x16x32_bf16 v[250:253], v[224:227], v[36:39], v[250:253]\n"
"s_waitcnt lgkmcnt(1)\n"
"v_mfma_f32_16x16x32_bf16 v[232:235], v[228:231], v[48:51], v[232:235]\n"
"v_cvt_pk_bf16_f32 v223, v206, v207\n"
"v_mfma_f32_16x16x32_bf16 v[240:243], v[228:231], v[52:55], v[240:243]\n"
"ds_read_b128 v[178:181], v12 offset:53248\n"
"v_mfma_f32_16x16x32_bf16 v[250:253], v[228:231], v[56:59], v[250:253]\n"
"s_waitcnt lgkmcnt(1)\n"
"v_mfma_f32_16x16x32_bf16 v[232:235], v[188:191], v[60:63], v[232:235]\n"
"ds_read_b128 v[198:201], v13 offset:53248\n"
"v_mfma_f32_16x16x32_bf16 v[240:243], v[188:191], v[64:67], v[240:243]\n"
"v_cvt_pk_bf16_f32 v248, v214, v215\n"
"v_mfma_f32_16x16x32_bf16 v[250:253], v[188:191], v[68:71], v[250:253]\n"
"s_waitcnt lgkmcnt(1)\n"
"v_mfma_f32_16x16x32_bf16 v[232:235], v[178:181], v[72:75], v[232:235]\n"
"v_mfma_f32_16x16x32_bf16 v[240:243], v[178:181], v[76:79], v[240:243]\n"
"ds_read_b128 v[202:205], v12 offset:55296\n"
"v_mfma_f32_16x16x32_bf16 v[250:253], v[178:181], v[80:83], v[250:253]\n"
"s_waitcnt lgkmcnt(1)\n"
"v_mfma_f32_16x16x32_bf16 v[232:235], v[198:201], v[84:87], v[232:235]\n"
"v_mfma_f32_16x16x32_bf16 v[240:243], v[198:201], v[88:91], v[240:243]\n"
"ds_read_b128 v[190:193], v13 offset:55296\n"
"v_mfma_f32_16x16x32_bf16 v[250:253], v[198:201], v[92:95], v[250:253]\n"
"s_waitcnt lgkmcnt(1)\n"
"v_mfma_f32_16x16x32_bf16 v[206:209], v[202:205], v[28:31], 0\n"
"ds_read_b128 v[178:181], v12 offset:57344\n"
"v_mfma_f32_16x16x32_bf16 v[216:219], v[202:205], v[32:35], 0\n"
"ds_read_b128 v[198:201], v13 offset:57344\n"
"v_mfma_f32_16x16x32_bf16 v[224:227], v[202:205], v[24:27], 0\n"
"s_waitcnt lgkmcnt(2)\n"
"ds_read_b128 v[202:205], v12 offset:59392\n"
"v_mfma_f32_16x16x32_bf16 v[206:209], v[190:193], v[40:43], v[206:209]\n"
"v_mfma_f32_16x16x32_bf16 v[216:219], v[190:193], v[44:47], v[216:219]\n"
"v_cvt_pk_bf16_f32 v188, v232, v233\n"
"v_mfma_f32_16x16x32_bf16 v[224:227], v[190:193], v[36:39], v[224:227]\n"
"s_waitcnt lgkmcnt(2)\n"
"ds_read_b128 v[190:193], v13 offset:59392\n"
"v_mfma_f32_16x16x32_bf16 v[206:209], v[178:181], v[48:51], v[206:209]\n"
"v_cvt_pk_bf16_f32 v215, v242, v243\n"
"v_mfma_f32_16x16x32_bf16 v[216:219], v[178:181], v[52:55], v[216:219]\n"
"v_cvt_pk_bf16_f32 v214, v240, v241\n"
"v_mfma_f32_16x16x32_bf16 v[224:227], v[178:181], v[56:59], v[224:227]\n"
"s_waitcnt lgkmcnt(2)\n"
"v_mfma_f32_16x16x32_bf16 v[206:209], v[198:201], v[60:63], v[206:209]\n"
"v_mfma_f32_16x16x32_bf16 v[216:219], v[198:201], v[64:67], v[216:219]\n"
"v_cvt_pk_bf16_f32 v241, v252, v253\n"
"v_mfma_f32_16x16x32_bf16 v[224:227], v[198:201], v[68:71], v[224:227]\n"
"s_waitcnt lgkmcnt(0)\n"
"s_waitcnt vmcnt(0)\n"
"s_barrier\n"
"v_mfma_f32_16x16x32_bf16 v[206:209], v[202:205], v[72:75], v[206:209]\n"
"v_cvt_pk_bf16_f32 v189, v234, v235\n"
"v_mfma_f32_16x16x32_bf16 v[216:219], v[202:205], v[76:79], v[216:219]\n"
"v_cvt_pk_bf16_f32 v240, v250, v251\n"
"v_mfma_f32_16x16x32_bf16 v[224:227], v[202:205], v[80:83], v[224:227]\n"
"s_setprio 2\n"
"v_mfma_f32_16x16x32_bf16 v[206:209], v[190:193], v[84:87], v[206:209]\n"
"ds_read_b128 v[180:183], v12 offset:6144\n"
"s_and_b32 s41, 160, s55\n"
"v_add_u32 v2, s41, v15\n"
"v_mfma_f32_16x16x32_bf16 v[216:219], v[190:193], v[88:91], v[216:219]\n"
"ds_read_b128 v[198:201], v12 offset:8192\n"
"ds_read_u16 v2, v2 offset:96\n"
"v_mfma_f32_16x16x32_bf16 v[224:227], v[190:193], v[92:95], v[224:227]\n"
"ds_read_b128 v[190:193], v13 offset:6144\n"
"v_cvt_pk_bf16_f32 v178, v206, v207\n"
"v_cvt_pk_bf16_f32 v179, v208, v209\n"
"s_nop 1\n"
"v_cvt_pk_bf16_f32 v205, v218, v219\n"
"v_cvt_pk_bf16_f32 v204, v216, v217\n"
"s_waitcnt lgkmcnt(1)\n"
"v_mad_u32_u24 v2, v2, s40, v16\n"
"v_cvt_pk_bf16_f32 v230, v224, v225\n"
"v_cvt_pk_bf16_f32 v231, v226, v227\n"
"v_mfma_f32_16x16x32_bf16 v[206:209], v[180:183], v[28:31], 0\n"
"v_mfma_f32_16x16x32_bf16 v[216:219], v[180:183], v[32:35], 0\n"
"s_add_u32 m0, s51, 126976\n"
"s_nop 0\n"
"global_load_lds_dwordx4 v2, s[56:57]\n"
"v_mfma_f32_16x16x32_bf16 v[232:235], v[180:183], v[24:27], 0\n"
"s_waitcnt lgkmcnt(0)\n"
"v_mfma_f32_16x16x32_bf16 v[206:209], v[190:193], v[40:43], v[206:209]\n"
"s_add_u32 m0, s51, 129024\n"
"s_nop 0\n"
"global_load_lds_dwordx4 v2, s[58:59]\n"
"s_add_u32 m0, s51, 131072\n"
"s_nop 0\n"
"global_load_lds_dwordx4 v2, s[60:61]\n"
"v_mfma_f32_16x16x32_bf16 v[216:219], v[190:193], v[44:47], v[216:219]\n"
"s_and_b32 s41, 32, s55\n"
"v_add_u32 v2, s41, v15\n"
"v_mfma_f32_16x16x32_bf16 v[232:235], v[190:193], v[36:39], v[232:235]\n"
"ds_read_b128 v[180:183], v13 offset:8192\n"
"v_mfma_f32_16x16x32_bf16 v[206:209], v[198:201], v[48:51], v[206:209]\n"
"ds_read_b128 v[190:193], v12 offset:10240\n"
"ds_read_u16 v2, v2 offset:832\n"
"v_mfma_f32_16x16x32_bf16 v[216:219], v[198:201], v[52:55], v[216:219]\n"
"v_mfma_f32_16x16x32_bf16 v[232:235], v[198:201], v[56:59], v[232:235]\n"
"s_waitcnt lgkmcnt(2)\n"
"v_mfma_f32_16x16x32_bf16 v[206:209], v[180:183], v[60:63], v[206:209]\n"
"s_waitcnt lgkmcnt(0)\n"
"v_mad_u32_u24 v2, v2, s40, v16\n"
"v_mfma_f32_16x16x32_bf16 v[216:219], v[180:183], v[64:67], v[216:219]\n"
"v_mfma_f32_16x16x32_bf16 v[232:235], v[180:183], v[68:71], v[232:235]\n"
"v_mfma_f32_16x16x32_bf16 v[206:209], v[190:193], v[72:75], v[206:209]\n"
"ds_read_b128 v[198:201], v13 offset:10240\n"
"s_add_u32 m0, s51, 139264\n"
"s_nop 0\n"
"global_load_lds_dwordx4 v2, s[56:57]\n"
"v_mfma_f32_16x16x32_bf16 v[216:219], v[190:193], v[76:79], v[216:219]\n"
"v_mfma_f32_16x16x32_bf16 v[232:235], v[190:193], v[80:83], v[232:235]\n"
"s_add_u32 m0, s51, 141312\n"
"s_nop 0\n"
"global_load_lds_dwordx4 v2, s[58:59]\n"
"s_waitcnt lgkmcnt(0)\n"
"v_mfma_f32_16x16x32_bf16 v[206:209], v[198:201], v[84:87], v[206:209]\n"
"s_add_u32 m0, s51, 143360\n"
"s_nop 0\n"
"global_load_lds_dwordx4 v2, s[60:61]\n"
"ds_read_b128 v[180:183], v12 offset:12288\n"
"v_mfma_f32_16x16x32_bf16 v[216:219], v[198:201], v[88:91], v[216:219]\n"
"s_andn2_b32 s41, 288, s55\n"
"v_add_u32 v2, s41, v15\n"
"ds_read_b128 v[190:193], v13 offset:12288\n"
"v_mfma_f32_16x16x32_bf16 v[232:235], v[198:201], v[92:95], v[232:235]\n"
"s_waitcnt lgkmcnt(1)\n"
"ds_read_u16 v2, v2 offset:128\n"
"v_mfma_f32_16x16x32_bf16 v[200:203], v[180:183], v[28:31], 0\n"
"ds_read_b128 v[226:229], v12 offset:14336\n"
"s_setprio 0\n"
"v_mfma_f32_16x16x32_bf16 v[242:245], v[180:183], v[32:35], 0\n"
"v_cvt_pk_bf16_f32 v225, v218, v219\n"
"v_mfma_f32_16x16x32_bf16 v[252:255], v[180:183], v[24:27], 0\n"
"s_waitcnt lgkmcnt(2)\n"
"v_mfma_f32_16x16x32_bf16 v[200:203], v[190:193], v[40:43], v[200:203]\n"
"v_cvt_pk_bf16_f32 v198, v206, v207\n"
"v_mfma_f32_16x16x32_bf16 v[242:245], v[190:193], v[44:47], v[242:245]\n"
"v_mfma_f32_16x16x32_bf16 v[252:255], v[190:193], v[36:39], v[252:255]\n"
"s_waitcnt lgkmcnt(1)\n"
"v_mad_u32_u24 v2, v2, s40, v16\n"
"s_add_u32 m0, s51, 151552\n"
"s_nop 0\n"
"global_load_lds_dwordx4 v2, s[56:57]\n"
"s_add_u32 m0, s51, 153600\n"
"s_nop 0\n"
"global_load_lds_dwordx4 v2, s[58:59]\n"
"s_add_u32 m0, s51, 155648\n"
"s_nop 0\n"
"global_load_lds_dwordx4 v2, s[60:61]\n"
"s_waitcnt lgkmcnt(0)\n"
"v_mfma_f32_16x16x32_bf16 v[200:203], v[226:229], v[48:51], v[200:203]\n"
"ds_read_b128 v[180:183], v13 offset:14336\n"
"v_cvt_pk_bf16_f32 v251, v234, v235\n"
"s_barrier\n"
"v_mfma_f32_16x16x32_bf16 v[242:245], v[226:229], v[52:55], v[242:245]\n"
"v_cvt_pk_bf16_f32 v250, v232, v233\n"
"v_mfma_f32_16x16x32_bf16 v[252:255], v[226:229], v[56:59], v[252:255]\n"
"v_cvt_pk_bf16_f32 v224, v216, v217\n"
"s_add_u32 s42, s48, 0\n"
"s_addc_u32 s43, s49, 0\n"
"s_add_u32 m0, s50, 24576\n"
"s_nop 0\n"
"global_load_lds_dwordx4 v3, s[42:43]\n"
"s_waitcnt lgkmcnt(0)\n"
"v_mfma_f32_16x16x32_bf16 v[200:203], v[180:183], v[60:63], v[200:203]\n"
"ds_read_b128 v[190:193], v12 offset:16384\n"
"v_mfma_f32_16x16x32_bf16 v[242:245], v[180:183], v[64:67], v[242:245]\n"
"v_cvt_pk_bf16_f32 v199, v208, v209\n"
"ds_read_b128 v[206:209], v13 offset:16384\n"
"v_mfma_f32_16x16x32_bf16 v[252:255], v[180:183], v[68:71], v[252:255]\n"
"s_add_u32 m0, s50, 25600\n"
"s_nop 0\n"
"global_load_lds_dwordx4 v4, s[42:43]\n"
"s_add_u32 m0, s50, 26624\n"
"s_nop 0\n"
"global_load_lds_dwordx4 v5, s[42:43]\n"
"s_waitcnt lgkmcnt(1)\n"
"v_mfma_f32_16x16x32_bf16 v[200:203], v[190:193], v[72:75], v[200:203]\n"
"ds_read_b128 v[180:183], v12 offset:18432\n"
"ds_read_b128 v[216:219], v12 offset:20480\n"
"v_mfma_f32_16x16x32_bf16 v[242:245], v[190:193], v[76:79], v[242:245]\n"
"v_mfma_f32_16x16x32_bf16 v[252:255], v[190:193], v[80:83], v[252:255]\n"
"s_waitcnt lgkmcnt(2)\n"
"ds_read_b128 v[190:193], v13 offset:18432\n"
"v_mfma_f32_16x16x32_bf16 v[200:203], v[206:209], v[84:87], v[200:203]\n"
"v_mfma_f32_16x16x32_bf16 v[242:245], v[206:209], v[88:91], v[242:245]\n"
"v_mfma_f32_16x16x32_bf16 v[252:255], v[206:209], v[92:95], v[252:255]\n"
"s_waitcnt lgkmcnt(2)\n"
"v_mfma_f32_16x16x32_bf16 v[226:229], v[180:183], v[28:31], 0\n"
"s_nop 4\n"
"v_cvt_pk_bf16_f32 v207, v244, v245\n"
"v_mfma_f32_16x16x32_bf16 v[112:115], v[180:183], v[32:35], 0\n"
"v_cvt_pk_bf16_f32 v206, v242, v243\n"
"v_mfma_f32_16x16x32_bf16 v[116:119], v[180:183], v[24:27], 0\n"
"s_waitcnt lgkmcnt(0)\n"
"v_cvt_pk_bf16_f32 v180, v200, v201\n"
"v_mfma_f32_16x16x32_bf16 v[226:229], v[190:193], v[40:43], v[226:229]\n"
"v_cvt_pk_bf16_f32 v181, v202, v203\n"
"v_mfma_f32_16x16x32_bf16 v[112:115], v[190:193], v[44:47], v[112:115]\n"
"s_add_u32 s42, s48, 18432\n"
"s_addc_u32 s43, s49, 0\n"
"s_add_u32 m0, s50, 27648\n"
"s_nop 0\n"
"global_load_lds_dwordx4 v3, s[42:43]\n"
"v_cvt_pk_bf16_f32 v232, v252, v253\n"
"v_mfma_f32_16x16x32_bf16 v[116:119], v[190:193], v[36:39], v[116:119]\n"
"v_mfma_f32_16x16x32_bf16 v[226:229], v[216:219], v[48:51], v[226:229]\n"
"ds_read_b128 v[104:107], v13 offset:20480\n"
"s_add_u32 m0, s50, 28672\n"
"s_nop 0\n"
"global_load_lds_dwordx4 v4, s[42:43]\n"
"v_mfma_f32_16x16x32_bf16 v[112:115], v[216:219], v[52:55], v[112:115]\n"
"v_cvt_pk_bf16_f32 v233, v254, v255\n"
"s_add_u32 m0, s50, 29696\n"
"s_nop 0\n"
"global_load_lds_dwordx4 v5, s[42:43]\n"
"ds_read_b128 v[190:193], v12 offset:22528\n"
"v_mfma_f32_16x16x32_bf16 v[116:119], v[216:219], v[56:59], v[116:119]\n"
"s_waitcnt lgkmcnt(1)\n"
"v_mfma_f32_16x16x32_bf16 v[226:229], v[104:107], v[60:63], v[226:229]\n"
"s_waitcnt vmcnt(9)\n"
"s_barrier\n"
"s_waitcnt vmcnt(3)\n"
"ds_read_b64_tr_b16 v[254:255], v10 offset:54080\n"
"v_mfma_f32_16x16x32_bf16 v[112:115], v[104:107], v[64:67], v[112:115]\n"
"ds_read_b64_tr_b16 v[252:253], v10 offset:53312\n"
"ds_read_b64_tr_b16 v[98:99], v11 offset:54016\n"
"ds_read_b64_tr_b16 v[100:101], v11 offset:53256\n"
"v_mfma_f32_16x16x32_bf16 v[116:119], v[104:107], v[68:71], v[116:119]\n"
"s_waitcnt lgkmcnt(4)\n"
"ds_read_b64_tr_b16 v[102:103], v11 offset:54024\n"
"v_mov_b32 v219, 0x3f803f80\n"
"v_mfma_f32_16x16x32_bf16 v[226:229], v[190:193], v[72:75], v[226:229]\n"
"ds_read_b64_tr_b16 v[96:97], v11 offset:53248\n"
"v_mfma_f32_16x16x32_bf16 v[112:115], v[190:193], v[76:79], v[112:115]\n"
"ds_read_b128 v[200:203], v13 offset:22528\n"
"s_waitcnt vmcnt(0)\n"
"ds_read_b64_tr_b16 v[108:109], v11 offset:56328\n"
"ds_read_b64_tr_b16 v[104:105], v11 offset:56320\n"
"v_mov_b32 v217, 0x3f803f80\n"
"v_mov_b32 v218, 0x3f803f80\n"
"v_mfma_f32_16x16x32_bf16 v[116:119], v[190:193], v[80:83], v[116:119]\n"
"s_waitcnt lgkmcnt(3)\n"
"s_add_u32 s42, s48, 36864\n"
"s_addc_u32 s43, s49, 0\n"
"s_add_u32 m0, s50, 24576\n"
"s_nop 0\n"
"global_load_lds_dwordx4 v3, s[42:43]\n"
"s_waitcnt lgkmcnt(2)\n"
"v_mfma_f32_16x16x32_bf16 v[226:229], v[200:203], v[84:87], v[226:229]\n"
"s_nop 7\n"
"v_cvt_pk_bf16_f32 v183, v228, v229\n"
"v_mov_b32 v216, 0x3f803f80\n"
"v_mov_b32 v193, 0\n"
"v_cvt_pk_bf16_f32 v182, v226, v227\n"
"ds_read_b64_tr_b16 v[110:111], v11 offset:57096\n"
"s_add_u32 m0, s50, 25600\n"
"s_nop 0\n"
"global_load_lds_dwordx4 v4, s[42:43]\n"
"ds_read_b64_tr_b16 v[138:139], v10 offset:57152\n"
"v_mfma_f32_16x16x32_bf16 v[112:115], v[200:203], v[88:91], v[112:115]\n"
"ds_read_b64_tr_b16 v[106:107], v11 offset:57088\n"
"ds_read_b64_tr_b16 v[136:137], v10 offset:56384\n"
"v_mfma_f32_16x16x32_bf16 v[116:119], v[200:203], v[92:95], v[116:119]\n"
"s_nop 4\n"
"v_cvt_pk_bf16_f32 v208, v112, v113\n"
"s_add_u32 m0, s50, 26624\n"
"s_nop 0\n"
"global_load_lds_dwordx4 v5, s[42:43]\n"
"v_cvt_pk_bf16_f32 v209, v114, v115\n"
"s_waitcnt lgkmcnt(0)\n"
"s_add_u32 s42, s48, 55296\n"
"s_addc_u32 s43, s49, 0\n"
"s_add_u32 m0, s50, 27648\n"
"s_nop 0\n"
"global_load_lds_dwordx4 v3, s[42:43]\n"
"s_add_u32 m0, s50, 28672\n"
"s_nop 0\n"
"global_load_lds_dwordx4 v4, s[42:43]\n"
"s_add_u32 m0, s50, 29696\n"
"s_nop 0\n"
"global_load_lds_dwordx4 v5, s[42:43]\n"
"s_barrier\n"
"ds_read2st64_b32 v[190:191], v9 offset0:112 offset1:113\n"
"s_waitcnt vmcnt(3)\n"
"ds_read_b64_tr_b16 v[114:115], v11 offset:54024\n"
"ds_read_b64_tr_b16 v[2:3], v11 offset:54016\n"
"ds_read_b32 v192, v9 offset:29184\n"
"ds_read_b64_tr_b16 v[112:113], v11 offset:53256\n"
"v_cvt_pk_bf16_f32 v235, v118, v119\n"
"ds_read_b64_tr_b16 v[146:147], v10 offset:54080\n"
"ds_read2st64_b32 v[202:203], v9 offset0:118 offset1:119\n"
"s_waitcnt lgkmcnt(3)\n"
"v_mfma_f32_16x16x32_bf16 v[226:229], v[216:219], v[190:193], 0\n"
"ds_read2st64_b32 v[200:201], v9 offset0:116 offset1:117\n"
"v_mfma_f32_16x16x32_bf16 v[140:143], v[186:189], v[190:193], 0\n"
"ds_read_b64_tr_b16 v[144:145], v10 offset:53312\n"
"v_cvt_pk_bf16_f32 v234, v116, v117\n"
"v_mfma_f32_16x16x32_bf16 v[242:245], v[212:215], v[190:193], 0\n"
"ds_read_b64_tr_b16 v[0:1], v11 offset:53248\n"
"v_mfma_f32_16x16x32_bf16 v[124:127], v[238:241], v[190:193], 0\n"
"s_waitcnt lgkmcnt(2)\n"
"ds_read2st64_b32 v[192:193], v9 offset0:130 offset1:131\n"
"v_mfma_f32_16x16x32_bf16 v[226:229], v[216:219], v[200:203], v[226:229]\n"
"v_mfma_f32_16x16x32_bf16 v[140:143], v[194:197], v[200:203], v[140:143]\n"
"ds_read2st64_b32 v[190:191], v9 offset0:128 offset1:129\n"
"s_waitcnt vmcnt(0)\n"
"ds_read_b64_tr_b16 v[118:119], v11 offset:57088\n"
"ds_read_b32 v130, v9 offset:9216\n"
"v_mfma_f32_16x16x32_bf16 v[242:245], v[220:223], v[200:203], v[242:245]\n"
"ds_read_b64_tr_b16 v[116:117], v11 offset:56320\n"
"ds_read_b64_tr_b16 v[120:121], v11 offset:56328\n"
"v_mfma_f32_16x16x32_bf16 v[124:127], v[246:249], v[200:203], v[124:127]\n"
"s_waitcnt lgkmcnt(6)\n"
"s_add_u32 s42, s48, 12288\n"
"s_addc_u32 s43, s49, 0\n"
"s_add_u32 m0, s50, 24576\n"
"s_nop 0\n"
"global_load_lds_dwordx4 v6, s[42:43]\n"
"v_mov_b32 v131, 0\n"
"ds_read_b32 v200, v9 offset:33792\n"
"s_waitcnt lgkmcnt(5)\n"
"v_mfma_f32_16x16x32_bf16 v[226:229], v[216:219], v[190:193], v[226:229]\n"
"s_setprio 2\n"
"v_mov_b32 v201, 0\n"
"ds_read2st64_b32 v[202:203], v9 offset0:134 offset1:135\n"
"s_add_u32 m0, s50, 25600\n"
"s_nop 0\n"
"global_load_lds_dwordx4 v7, s[42:43]\n"
"ds_read_b64_tr_b16 v[150:151], v10 offset:57152\n"
"v_mfma_f32_16x16x32_bf16 v[140:143], v[178:181], v[190:193], v[140:143]\n"
"v_mfma_f32_16x16x32_bf16 v[242:245], v[204:207], v[190:193], v[242:245]\n"
"v_mfma_f32_16x16x32_bf16 v[124:127], v[230:233], v[190:193], v[124:127]\n"
"ds_read_b64_tr_b16 v[148:149], v10 offset:56384\n"
"v_mov_b32 v128, 0\n"
"s_add_u32 m0, s50, 26624\n"
"s_nop 0\n"
"global_load_lds_dwordx4 v8, s[42:43]\n"
"v_mov_b32 v129, 0\n"
"s_waitcnt lgkmcnt(2)\n"
"v_mfma_f32_16x16x32_bf16 v[226:229], v[216:219], v[200:203], v[226:229]\n"
"v_mfma_f32_16x16x32_bf16 v[226:229], v[216:219], v[128:131], v[226:229]\n"
"ds_read_b64_tr_b16 v[122:123], v11 offset:57096\n"
"s_nop 6\n"
"v_rcp_f32 v227, v226\n"
"v_mfma_f32_16x16x32_bf16 v[124:127], v[234:237], v[200:203], v[124:127]\n"
"ds_read_b128 v[190:193], v12 offset:24576\n"
"v_add_u32 v226, 0xd4800000, v226\n"
"v_mfma_f32_16x16x32_bf16 v[124:127], v[248:251], v[128:131], v[124:127]\n"
"s_waitcnt lgkmcnt(1)\n"
"s_add_u32 s42, s48, 49152\n"
"s_addc_u32 s43, s49, 0\n"
"s_add_u32 m0, s50, 27648\n"
"s_nop 0\n"
"global_load_lds_dwordx4 v6, s[42:43]\n"
"v_mfma_f32_16x16x32_bf16 v[140:143], v[182:185], v[200:203], v[140:143]\n"
"s_nop 0\n"
"v_mul_f32 v127, v127, v227\n"
"v_mul_f32 v126, v126, v227\n"
"v_mfma_f32_16x16x32_bf16 v[140:143], v[196:199], v[128:131], v[140:143]\n"
"v_cmp_lt_u32 vcc, 0x28000000, v226\n"
"v_mul_f32 v125, v125, v227\n"
"v_mfma_f32_16x16x32_bf16 v[242:245], v[208:211], v[200:203], v[242:245]\n"
"ds_read_b128 v[200:203], v12 offset:26624\n"
"v_mfma_f32_16x16x32_bf16 v[242:245], v[222:225], v[128:131], v[242:245]\n"
"s_waitcnt lgkmcnt(1)\n"
"ds_read_b128 v[216:219], v13 offset:24576\n"
"v_mfma_f32_16x16x32_bf16 v[128:131], v[190:193], v[28:31], 0\n"
"v_mul_f32 v141, v141, v227\n"
"v_mul_f32 v124, v124, v227\n"
"s_or_b64 s[62:63], s[62:63], vcc\n"
"s_add_u32 m0, s50, 28672\n"
"s_nop 0\n"
"global_load_lds_dwordx4 v7, s[42:43]\n"
"v_cvt_pk_bf16_f32 v236, v124, v125\n"
"s_add_u32 m0, s50, 29696\n"
"s_nop 0\n"
"global_load_lds_dwordx4 v8, s[42:43]\n"
"v_cvt_pk_bf16_f32 v237, v126, v127\n"
"v_mfma_f32_16x16x32_bf16 v[132:135], v[190:193], v[32:35], 0\n"
"s_andn2_b32 s41, 608, s55\n"
"v_add_u32 v8, s41, v15\n"
"v_mul_f32 v143, v143, v227\n"
"v_mfma_f32_16x16x32_bf16 v[152:155], v[190:193], v[24:27], 0\n"
"s_waitcnt lgkmcnt(0)\n"
"ds_read_u16 v8, v8 offset:288\n"
"v_mul_f32 v140, v140, v227\n"
"v_mfma_f32_16x16x32_bf16 v[128:131], v[216:219], v[40:43], v[128:131]\n"
"ds_read_b128 v[190:193], v13 offset:26624\n"
"v_mul_f32 v142, v142, v227\n"
"v_mfma_f32_16x16x32_bf16 v[132:135], v[216:219], v[44:47], v[132:135]\n"
"v_cvt_pk_bf16_f32 v140, v140, v141\n"
"v_mfma_f32_16x16x32_bf16 v[152:155], v[216:219], v[36:39], v[152:155]\n"
"ds_read_b128 v[216:219], v12 offset:28672\n"
"v_mul_f32 v243, v243, v227\n"
"v_mfma_f32_16x16x32_bf16 v[128:131], v[200:203], v[48:51], v[128:131]\n"
"s_waitcnt lgkmcnt(2)\n"
"v_mad_u32_u24 v8, v8, s40, v16\n"
"v_mul_f32 v242, v242, v227\n"
"v_mfma_f32_16x16x32_bf16 v[132:135], v[200:203], v[52:55], v[132:135]\n"
"v_mul_f32 v244, v244, v227\n"
"v_mul_f32 v245, v245, v227\n"
"v_mfma_f32_16x16x32_bf16 v[152:155], v[200:203], v[56:59], v[152:155]\n"
"s_waitcnt lgkmcnt(1)\n"
"ds_read_b128 v[200:203], v13 offset:28672\n"
"v_cvt_pk_bf16_f32 v141, v142, v143\n"
"v_mfma_f32_16x16x32_bf16 v[128:131], v[190:193], v[60:63], v[128:131]\n"
"s_add_u32 m0, s51, 114688\n"
"s_nop 0\n"
"global_load_lds_dwordx4 v8, s[56:57]\n"
"v_mfma_f32_16x16x32_bf16 v[132:135], v[190:193], v[64:67], v[132:135]\n"
"v_cvt_pk_bf16_f32 v143, v244, v245\n"
"v_mfma_f32_16x16x32_bf16 v[152:155], v[190:193], v[68:71], v[152:155]\n"
"s_waitcnt lgkmcnt(1)\n"
"s_setprio 0\n"
"ds_read_b128 v[190:193], v12 offset:30720\n"
"v_mfma_f32_16x16x32_bf16 v[128:131], v[216:219], v[72:75], v[128:131]\n"
"v_mfma_f32_16x16x32_bf16 v[132:135], v[216:219], v[76:79], v[132:135]\n"
"v_mfma_f32_16x16x32_bf16 v[152:155], v[216:219], v[80:83], v[152:155]\n"
"s_waitcnt lgkmcnt(1)\n"
"v_mfma_f32_16x16x32_bf16 v[128:131], v[200:203], v[84:87], v[128:131]\n"
"v_cvt_pk_bf16_f32 v142, v242, v243\n"
"v_mfma_f32_16x16x32_bf16 v[132:135], v[200:203], v[88:91], v[132:135]\n"
"s_add_u32 m0, s51, 116736\n"
"s_nop 0\n"
"global_load_lds_dwordx4 v8, s[58:59]\n"
"v_mfma_f32_16x16x32_bf16 v[152:155], v[200:203], v[92:95], v[152:155]\n"
"s_waitcnt lgkmcnt(0)\n"
"ds_read_b128 v[200:203], v12 offset:32768\n"
"v_mfma_f32_16x16x32_bf16 v[156:159], v[190:193], v[28:31], 0\n"
"ds_read_b128 v[226:229], v13 offset:30720\n"
"v_mfma_f32_16x16x32_bf16 v[160:163], v[190:193], v[32:35], 0\n"
"s_add_u32 m0, s51, 118784\n"
"s_nop 0\n"
"global_load_lds_dwordx4 v8, s[60:61]\n"
"v_mfma_f32_16x16x32_bf16 v[164:167], v[190:193], v[24:27], 0\n"
"s_waitcnt vmcnt(21)\n"
"s_barrier\n"
"s_waitcnt vmcnt(6)\n"
"ds_read_b64_tr_b16 v[6:7], v11 offset:54016\n"
"v_cvt_pk_bf16_f32 v242, v152, v153\n"
"ds_read_b128 v[168:171], v13 offset:32768\n"
"s_waitcnt lgkmcnt(2)\n"
"v_mfma_f32_16x16x32_bf16 v[156:159], v[226:229], v[40:43], v[156:159]\n"
"v_cvt_pk_bf16_f32 v243, v154, v155\n"
"v_cvt_pk_bf16_f32 v190, v128, v129\n"
"ds_read_b64_tr_b16 v[4:5], v11 offset:53248\n"
"v_cvt_pk_bf16_f32 v216, v132, v133\n"
"v_mfma_f32_16x16x32_bf16 v[160:163], v[226:229], v[44:47], v[160:163]\n"
"s_mov_b32 m0, s54\n"
"v_mfma_f32_16x16x32_bf16 v[164:167], v[226:229], v[36:39], v[164:167]\n"
"v_cvt_pk_bf16_f32 v191, v130, v131\n"
"ds_read_b64_tr_b16 v[226:227], v10 offset:53312\n"
"ds_read_b64_tr_b16 v[126:127], v11 offset:54024\n"
"v_mfma_f32_16x16x32_bf16 v[156:159], v[200:203], v[48:51], v[156:159]\n"
"v_cvt_pk_bf16_f32 v217, v134, v135\n"
"ds_read_b64_tr_b16 v[124:125], v11 offset:53256\n"
"ds_read_b64_tr_b16 v[228:229], v10 offset:54080\n"
"v_mfma_f32_16x16x32_bf16 v[160:163], v[200:203], v[52:55], v[160:163]\n"
"s_waitcnt vmcnt(3)\n"
"ds_read_b64_tr_b16 v[132:133], v11 offset:56328\n"
"ds_read_b64_tr_b16 v[128:129], v11 offset:56320\n"
"ds_read_b64_tr_b16 v[152:153], v10 offset:56384\n"
"v_mfma_f32_16x16x32_bf16 v[164:167], v[200:203], v[56:59], v[164:167]\n"
"s_waitcnt lgkmcnt(8)\n"
"ds_read_b64_tr_b16 v[154:155], v10 offset:57152\n"
"ds_read_b64_tr_b16 v[134:135], v11 offset:57096\n"
"v_mfma_f32_16x16x32_bf16 v[156:159], v[168:171], v[60:63], v[156:159]\n"
"s_and_b32 s41, 480, s55\n"
"v_add_u32 v8, s41, v15\n"
"ds_read_b64_tr_b16 v[130:131], v11 offset:57088\n"
"ds_write_addtid_b32 v236 offset:33280\n"
"v_mfma_f32_16x16x32_bf16 v[160:163], v[168:171], v[64:67], v[160:163]\n"
"ds_read_b128 v[200:203], v12 offset:34816\n"
"ds_write_addtid_b32 v141 offset:32512\n"
"v_mfma_f32_16x16x32_bf16 v[164:167], v[168:171], v[68:71], v[164:167]\n"
"ds_write_addtid_b32 v140 offset:32256\n"
"ds_read_u16 v8, v8 offset:448\n"
"ds_write_addtid_b32 v237 offset:33536\n"
"ds_write_addtid_b32 v143 offset:33024\n"
"s_waitcnt lgkmcnt(7)\n"
"ds_write_b128 v17, v[252:255] offset:53248\n"
"ds_read_b128 v[252:255], v13 offset:34816\n"
"s_waitcnt lgkmcnt(7)\n"
"v_mfma_f32_16x16x32_bf16 v[156:159], v[200:203], v[72:75], v[156:159]\n"
"ds_write_addtid_b32 v142 offset:32768\n"
"ds_write_b128 v17, v[136:139] offset:54272\n"
"v_mfma_f32_16x16x32_bf16 v[160:163], v[200:203], v[76:79], v[160:163]\n"
"ds_write_b128 v17, v[144:147] offset:55296\n"
"v_mfma_f32_16x16x32_bf16 v[164:167], v[200:203], v[80:83], v[164:167]\n"
"s_waitcnt lgkmcnt(3)\n"
"v_mov_b32 v137, 0x3f803f80\n"
"v_mov_b32 v136, 0x3f803f80\n"
"v_mfma_f32_16x16x32_bf16 v[156:159], v[252:255], v[84:87], v[156:159]\n"
"s_waitcnt lgkmcnt(2)\n"
"s_waitcnt vmcnt(0)\n"
"s_barrier\n"
"v_mfma_f32_16x16x32_bf16 v[160:163], v[252:255], v[88:91], v[160:163]\n"
"ds_read2st64_b32 v[140:141], v9 offset0:120 offset1:121\n"
"v_mfma_f32_16x16x32_bf16 v[164:167], v[252:255], v[92:95], v[164:167]\n"
"ds_write_b128 v17, v[148:151] offset:56320\n"
"v_mov_b32 v138, 0x3f803f80\n"
"v_mad_u32_u24 v8, v8, s40, v16\n"
"s_nop 4\n"
"v_cvt_pk_bf16_f32 v252, v164, v165\n"
"v_mov_b32 v139, 0x3f803f80\n"
"ds_write_b128 v17, v[226:229] offset:57344\n"
"v_cvt_pk_bf16_f32 v226, v160, v161\n"
"v_cvt_pk_bf16_f32 v200, v156, v157\n"
"ds_read2st64_b32 v[150:151], v9 offset0:128 offset1:129\n"
"s_add_u32 m0, s51, 126976\n"
"s_nop 0\n"
"global_load_lds_dwordx4 v8, s[56:57]\n"
"ds_read2st64_b32 v[144:145], v9 offset0:124 offset1:125\n"
"ds_read2st64_b32 v[142:143], v9 offset0:122 offset1:123\n"
"v_cvt_pk_bf16_f32 v201, v158, v159\n"
"ds_write_b128 v17, v[152:155] offset:58368\n"
"s_waitcnt lgkmcnt(1)\n"
"v_mfma_f32_16x16x32_bf16 v[156:159], v[136:139], v[140:143], 0\n"
"ds_read2st64_b32 v[146:147], v9 offset0:126 offset1:127\n"
"v_cvt_pk_bf16_f32 v227, v162, v163\n"
"v_mfma_f32_16x16x32_bf16 v[160:163], v[186:189], v[140:143], 0\n"
"s_add_u32 m0, s51, 129024\n"
"s_nop 0\n"
"global_load_lds_dwordx4 v8, s[58:59]\n"
"ds_read2st64_b32 v[152:153], v9 offset0:130 offset1:131\n"
"v_cvt_pk_bf16_f32 v253, v166, v167\n"
"v_mfma_f32_16x16x32_bf16 v[164:167], v[212:215], v[140:143], 0\n"
"ds_read2st64_b32 v[172:173], v14 offset0:150 offset1:151\n"
"v_mfma_f32_16x16x32_bf16 v[168:171], v[238:241], v[140:143], 0\n"
"s_waitcnt lgkmcnt(2)\n"
"s_setprio 2\n"
"v_mfma_f32_16x16x32_bf16 v[156:159], v[136:139], v[144:147], v[156:159]\n"
"s_waitcnt lgkmcnt(1)\n"
"v_mov_b32 v155, 0\n"
"v_mov_b32 v154, 0\n"
"v_mfma_f32_16x16x32_bf16 v[156:159], v[136:139], v[150:153], v[156:159]\n"
"v_mov_b32 v148, 0\n"
"v_mfma_f32_16x16x32_bf16 v[168:171], v[250:253], v[144:147], v[168:171]\n"
"v_mov_b32 v149, 0\n"
"s_add_u32 m0, s51, 131072\n"
"s_nop 0\n"
"global_load_lds_dwordx4 v8, s[60:61]\n"
"s_and_b32 s41, 32, s55\n"
"v_add_u32 v255, s41, v15\n"
"v_mfma_f32_16x16x32_bf16 v[168:171], v[240:243], v[148:151], v[168:171]\n"
"ds_read_b128 v[140:143], v17 offset:53248\n"
"ds_read2st64_b32 v[138:139], v14 offset0:128 offset1:129\n"
"v_mfma_f32_16x16x32_bf16 v[168:171], v[248:251], v[152:155], v[168:171]\n"
"v_mfma_f32_16x16x32_bf16 v[160:163], v[198:201], v[144:147], v[160:163]\n"
"v_rcp_f32 v8, v156\n"
"v_mfma_f32_16x16x32_bf16 v[160:163], v[188:191], v[148:151], v[160:163]\n"
"ds_read2st64_b32 v[136:137], v14 offset0:126 offset1:127\n"
"v_mfma_f32_16x16x32_bf16 v[160:163], v[196:199], v[152:155], v[160:163]\n"
"v_add_u32 v156, 0xd4800000, v156\n"
"v_mfma_f32_16x16x32_bf16 v[164:167], v[224:227], v[144:147], v[164:167]\n"
"v_cmp_lt_u32 vcc, 0x28000000, v156\n"
"v_mul_f32 v169, v169, v8\n"
"v_mfma_f32_16x16x32_bf16 v[164:167], v[214:217], v[148:151], v[164:167]\n"
"ds_read2st64_b32 v[174:175], v14 offset0:152 offset1:153\n"
"v_mul_f32 v168, v168, v8\n"
"ds_read_b128 v[148:151], v17 offset:54272\n"
"ds_read_u16 v255, v255 offset:480\n"
"v_mfma_f32_16x16x32_bf16 v[164:167], v[222:225], v[152:155], v[164:167]\n"
"s_waitcnt lgkmcnt(3)\n"
"v_mul_f32 v161, v161, v8\n"
"v_cvt_pk_bf16_f32 v244, v168, v169\n"
"v_mfma_f32_16x16x32_bf16 v[144:147], v[96:99], v[136:139], 0\n"
"ds_read2st64_b32 v[158:159], v14 offset0:176 offset1:177\n"
"v_mfma_f32_16x16x32_bf16 v[152:155], v[100:103], v[136:139], 0\n"
"s_or_b64 s[62:63], s[62:63], vcc\n"
"ds_read2st64_b32 v[156:157], v14 offset0:174 offset1:175\n"
"v_mul_f32 v171, v171, v8\n"
"v_mul_f32 v170, v170, v8\n"
"v_cvt_pk_bf16_f32 v245, v170, v171\n"
"v_mfma_f32_16x16x32_bf16 v[168:171], v[140:143], v[136:139], 0\n"
"s_waitcnt lgkmcnt(4)\n"
"ds_read2st64_b32 v[140:141], v14 offset0:198 offset1:199\n"
"v_mul_f32 v160, v160, v8\n"
"v_mfma_f32_16x16x32_bf16 v[144:147], v[104:107], v[172:175], v[144:147]\n"
"s_waitcnt lgkmcnt(3)\n"
"v_mul_f32 v165, v165, v8\n"
"v_mad_u32_u24 v255, v255, s40, v16\n"
"v_mfma_f32_16x16x32_bf16 v[152:155], v[108:111], v[172:175], v[152:155]\n"
"v_mul_f32 v162, v162, v8\n"
"s_add_u32 m0, s51, 139264\n"
"s_nop 0\n"
"global_load_lds_dwordx4 v255, s[56:57]\n"
"v_mfma_f32_16x16x32_bf16 v[168:171], v[148:151], v[172:175], v[168:171]\n"
"s_waitcnt lgkmcnt(1)\n"
"ds_read2st64_b32 v[142:143], v14 offset0:200 offset1:201\n"
"v_mul_f32 v164, v164, v8\n"
"v_mfma_f32_16x16x32_bf16 v[144:147], v[0:3], v[156:159], v[144:147]\n"
"ds_read_b128 v[148:151], v17 offset:56320\n"
"ds_read_b128 v[136:139], v17 offset:55296\n"
"v_cvt_pk_bf16_f32 v160, v160, v161\n"
"ds_read2st64_b32 v[172:173], v14 offset0:130 offset1:131\n"
"ds_read2st64_b32 v[174:175], v14 offset0:154 offset1:155\n"
"v_mfma_f32_16x16x32_bf16 v[152:155], v[112:115], v[156:159], v[152:155]\n"
"s_setprio 0\n"
"v_mul_f32 v166, v166, v8\n"
"s_add_u32 m0, s51, 141312\n"
"s_nop 0\n"
"global_load_lds_dwordx4 v255, s[58:59]\n"
"v_mul_f32 v163, v163, v8\n"
"v_mul_f32 v167, v167, v8\n"
"v_cvt_pk_bf16_f32 v161, v162, v163\n"
"v_cvt_pk_bf16_f32 v162, v164, v165\n"
"s_waitcnt lgkmcnt(2)\n"
"v_mfma_f32_16x16x32_bf16 v[168:171], v[136:139], v[156:159], v[168:171]\n"
"ds_read2st64_b32 v[158:159], v14 offset0:202 offset1:203\n"
"v_mfma_f32_16x16x32_bf16 v[144:147], v[116:119], v[140:143], v[144:147]\n"
"ds_read_b128 v[136:139], v17 offset:57344\n"
"v_mfma_f32_16x16x32_bf16 v[152:155], v[120:123], v[140:143], v[152:155]\n"
"ds_read2st64_b32 v[156:157], v14 offset0:178 offset1:179\n"
"v_mfma_f32_16x16x32_bf16 v[168:171], v[148:151], v[140:143], v[168:171]\n"
"s_waitcnt lgkmcnt(3)\n"
"ds_read_b128 v[140:143], v17 offset:58368\n"
"v_mfma_f32_16x16x32_bf16 v[144:147], v[4:7], v[172:175], v[144:147]\n"
"v_cvt_pk_bf16_f32 v163, v166, v167\n"
"v_mfma_f32_16x16x32_bf16 v[152:155], v[124:127], v[172:175], v[152:155]\n"
"s_waitcnt lgkmcnt(2)\n"
"v_mfma_f32_16x16x32_bf16 v[168:171], v[136:139], v[172:175], v[168:171]\n"
"s_waitcnt lgkmcnt(1)\n"
"ds_read_b128 v[148:151], v12 offset:49152\n"
"v_mfma_f32_16x16x32_bf16 v[144:147], v[128:131], v[156:159], v[144:147]\n"
"s_add_u32 m0, s51, 143360\n"
"s_nop 0\n"
"global_load_lds_dwordx4 v255, s[60:61]\n"
"v_mfma_f32_16x16x32_bf16 v[152:155], v[132:135], v[156:159], v[152:155]\n"
"s_barrier\n"
"s_waitcnt lgkmcnt(1)\n"
"v_mfma_f32_16x16x32_bf16 v[168:171], v[140:143], v[156:159], v[168:171]\n"
"ds_read_b128 v[140:143], v12 offset:51200\n"
"s_mov_b32 m0, s54\n"
"v_cmp_class_f32_e64 vcc, v144, s64\n"
"ds_read_b128 v[136:139], v13 offset:49152\n"
"s_waitcnt lgkmcnt(2)\n"
"v_mfma_f32_16x16x32_bf16 v[156:159], v[148:151], v[28:31], 0\n"
"v_cvt_pk_bf16_f32 v144, v144, v145\n"
"v_cvt_pk_bf16_f32 v145, v146, v147\n"
"ds_write_addtid_b32 v244 offset:33280\n"
"v_mfma_f32_16x16x32_bf16 v[164:167], v[148:151], v[32:35], 0\n"
"v_cvt_pk_bf16_f32 v146, v152, v153\n"
"v_mfma_f32_16x16x32_bf16 v[172:175], v[148:151], v[24:27], 0\n"
"ds_write_addtid_b32 v245 offset:33536\n"
"s_waitcnt lgkmcnt(2)\n"
"v_mfma_f32_16x16x32_bf16 v[156:159], v[136:139], v[40:43], v[156:159]\n"
"ds_read_b128 v[148:151], v13 offset:51200\n"
"v_mfma_f32_16x16x32_bf16 v[164:167], v[136:139], v[44:47], v[164:167]\n"
"v_mfma_f32_16x16x32_bf16 v[172:175], v[136:139], v[36:39], v[172:175]\n"
"ds_write_addtid_b32 v161 offset:32512\n"
"v_mfma_f32_16x16x32_bf16 v[156:159], v[140:143], v[48:51], v[156:159]\n"
"ds_write_addtid_b32 v163 offset:33024\n"
"s_or_b64 s[62:63], s[62:63], vcc\n"
"v_mfma_f32_16x16x32_bf16 v[164:167], v[140:143], v[52:55], v[164:167]\n"
"v_cvt_pk_bf16_f32 v168, v168, v169\n"
"ds_read_b128 v[136:139], v12 offset:53248\n"
"v_mfma_f32_16x16x32_bf16 v[172:175], v[140:143], v[56:59], v[172:175]\n"
"ds_read_b128 v[140:143], v13 offset:53248\n"
"ds_write_addtid_b32 v162 offset:32768\n"
"s_waitcnt lgkmcnt(5)\n"
"v_mfma_f32_16x16x32_bf16 v[156:159], v[148:151], v[60:63], v[156:159]\n"
"ds_write_addtid_b32 v160 offset:32256\n"
"v_mfma_f32_16x16x32_bf16 v[164:167], v[148:151], v[64:67], v[164:167]\n"
"s_mov_b32 m0, s68\n"
"s_nop 0\n"
"ds_write_addtid_b32 v146 offset:45568\n"
"v_mfma_f32_16x16x32_bf16 v[172:175], v[148:151], v[68:71], v[172:175]\n"
"s_waitcnt lgkmcnt(4)\n"
"v_cvt_pk_bf16_f32 v147, v154, v155\n"
"v_cvt_pk_bf16_f32 v169, v170, v171\n"
"v_mfma_f32_16x16x32_bf16 v[156:159], v[136:139], v[72:75], v[156:159]\n"
"ds_read_b128 v[148:151], v12 offset:55296\n"
"v_mfma_f32_16x16x32_bf16 v[164:167], v[136:139], v[76:79], v[164:167]\n"
"ds_write_addtid_b32 v145 offset:45312\n"
"v_mfma_f32_16x16x32_bf16 v[172:175], v[136:139], v[80:83], v[172:175]\n"
"s_waitcnt lgkmcnt(5)\n"
"ds_read_b128 v[136:139], v13 offset:55296\n"
"v_mfma_f32_16x16x32_bf16 v[156:159], v[140:143], v[84:87], v[156:159]\n"
"s_setprio 2\n"
"ds_write_addtid_b32 v169 offset:46336\n"
"v_mfma_f32_16x16x32_bf16 v[164:167], v[140:143], v[88:91], v[164:167]\n"
"ds_write_addtid_b32 v168 offset:46080\n"
"v_mfma_f32_16x16x32_bf16 v[172:175], v[140:143], v[92:95], v[172:175]\n"
"ds_write_addtid_b32 v147 offset:45824\n"
"ds_write_addtid_b32 v144 offset:45056\n"
"s_waitcnt lgkmcnt(6)\n"
"v_mfma_f32_16x16x32_bf16 v[144:147], v[148:151], v[28:31], 0\n"
"ds_read_b128 v[140:143], v12 offset:57344\n"
"v_cvt_pk_bf16_f32 v185, v158, v159\n"
"v_mfma_f32_16x16x32_bf16 v[152:155], v[148:151], v[32:35], 0\n"
"v_cvt_pk_bf16_f32 v184, v156, v157\n"
"v_mfma_f32_16x16x32_bf16 v[156:159], v[148:151], v[24:27], 0\n"
"s_waitcnt lgkmcnt(5)\n"
"ds_read_b128 v[148:151], v13 offset:57344\n"
"v_cvt_pk_bf16_f32 v210, v164, v165\n"
"v_cvt_pk_bf16_f32 v211, v166, v167\n"
"v_mfma_f32_16x16x32_bf16 v[144:147], v[136:139], v[40:43], v[144:147]\n"
"v_cvt_pk_bf16_f32 v236, v172, v173\n"
"s_setprio 0\n"
"v_mfma_f32_16x16x32_bf16 v[152:155], v[136:139], v[44:47], v[152:155]\n"
"v_mfma_f32_16x16x32_bf16 v[156:159], v[136:139], v[36:39], v[156:159]\n"
"s_waitcnt lgkmcnt(1)\n"
"ds_read_b128 v[136:139], v12 offset:59392\n"
"v_mfma_f32_16x16x32_bf16 v[144:147], v[140:143], v[48:51], v[144:147]\n"
"v_cvt_pk_bf16_f32 v237, v174, v175\n"
"v_mfma_f32_16x16x32_bf16 v[152:155], v[140:143], v[52:55], v[152:155]\n"
"v_mfma_f32_16x16x32_bf16 v[156:159], v[140:143], v[56:59], v[156:159]\n"
"s_waitcnt lgkmcnt(1)\n"
"ds_read_b128 v[140:143], v13 offset:59392\n"
"v_mfma_f32_16x16x32_bf16 v[144:147], v[148:151], v[60:63], v[144:147]\n"
"v_mfma_f32_16x16x32_bf16 v[152:155], v[148:151], v[64:67], v[152:155]\n"
"v_mfma_f32_16x16x32_bf16 v[156:159], v[148:151], v[68:71], v[156:159]\n"
"s_waitcnt lgkmcnt(1)\n"
"v_mov_b32 v151, 0x3f803f80\n"
"v_mfma_f32_16x16x32_bf16 v[144:147], v[136:139], v[72:75], v[144:147]\n"
"v_mov_b32 v150, 0x3f803f80\n"
"v_mfma_f32_16x16x32_bf16 v[152:155], v[136:139], v[76:79], v[152:155]\n"
"v_mov_b32 v149, 0x3f803f80\n"
"v_mfma_f32_16x16x32_bf16 v[156:159], v[136:139], v[80:83], v[156:159]\n"
"s_waitcnt lgkmcnt(0)\n"
"v_mfma_f32_16x16x32_bf16 v[144:147], v[140:143], v[84:87], v[144:147]\n"
"s_waitcnt vmcnt(3)\n"
"s_barrier\n"
"v_mfma_f32_16x16x32_bf16 v[152:155], v[140:143], v[88:91], v[152:155]\n"
"ds_read2st64_b32 v[136:137], v9 offset0:112 offset1:113\n"
"v_mfma_f32_16x16x32_bf16 v[156:159], v[140:143], v[92:95], v[156:159]\n"
"ds_read2st64_b32 v[138:139], v9 offset0:114 offset1:115\n"
"ds_read2st64_b32 v[162:163], v20 offset0:2 offset1:3\n"
"s_nop 0\n"
"v_cvt_pk_bf16_f32 v193, v146, v147\n"
"s_nop 3\n"
"v_cvt_pk_bf16_f32 v245, v158, v159\n"
"ds_read_u16 v255, v21 offset:800\n"
"v_mov_b32 v148, 0x3f803f80\n"
"s_and_b32 s41, 416, s55\n"
"v_add_u32 v8, s41, v15\n"
"v_cvt_pk_bf16_f32 v244, v156, v157\n"
"ds_read2st64_b32 v[160:161], v20 offset0:0 offset1:1\n"
"ds_read_u16 v8, v8 offset:544\n"
"v_cvt_pk_bf16_f32 v192, v144, v145\n"
"s_waitcnt lgkmcnt(4)\n"
"v_mfma_f32_16x16x32_bf16 v[144:147], v[148:151], v[136:139], 0\n"
"ds_read2st64_b32 v[140:141], v9 offset0:116 offset1:117\n"
"v_cvt_pk_bf16_f32 v218, v152, v153\n"
"v_cvt_pk_bf16_f32 v219, v154, v155\n"
"v_mfma_f32_16x16x32_bf16 v[152:155], v[180:183], v[136:139], 0\n"
"ds_read2st64_b32 v[142:143], v9 offset0:118 offset1:119\n"
"v_mfma_f32_16x16x32_bf16 v[156:159], v[206:209], v[136:139], 0\n"
"s_waitcnt lgkmcnt(2)\n"
"v_mad_u32_u24 v8, v8, s40, v16\n"
"v_mfma_f32_16x16x32_bf16 v[164:167], v[232:235], v[136:139], 0\n"
"ds_read2st64_b32 v[136:137], v9 offset0:128 offset1:129\n"
"s_add_u32 m0, s51, 151552\n"
"s_nop 0\n"
"global_load_lds_dwordx4 v8, s[56:57]\n"
"s_waitcnt lgkmcnt(1)\n"
"v_mfma_f32_16x16x32_bf16 v[144:147], v[148:151], v[140:143], v[144:147]\n"
"ds_read2st64_b32 v[228:229], v20 offset0:4 offset1:5\n"
"v_mfma_f32_16x16x32_bf16 v[152:155], v[188:191], v[140:143], v[152:155]\n"
"ds_read2st64_b32 v[138:139], v9 offset0:130 offset1:131\n"
"v_mfma_f32_16x16x32_bf16 v[156:159], v[214:217], v[140:143], v[156:159]\n"
"v_mad_u32_u24 v254, v255, s40, v18\n"
"s_setprio 2\n"
"v_mfma_f32_16x16x32_bf16 v[164:167], v[240:243], v[140:143], v[164:167]\n"
"global_store_dwordx4 v254, v[160:163], s[66:67] sc1\n"
"v_mad_u32_u24 v255, v255, s40, v19\n"
"ds_read2st64_b32 v[140:141], v9 offset0:132 offset1:133\n"
"s_waitcnt lgkmcnt(1)\n"
"v_mfma_f32_16x16x32_bf16 v[144:147], v[148:151], v[136:139], v[144:147]\n"
"v_mov_b32 v160, 0\n"
"s_add_u32 m0, s51, 153600\n"
"s_nop 0\n"
"global_load_lds_dwordx4 v8, s[58:59]\n"
"ds_read2st64_b32 v[142:143], v9 offset0:134 offset1:135\n"
"v_mfma_f32_16x16x32_bf16 v[152:155], v[184:187], v[136:139], v[152:155]\n"
"ds_read2st64_b32 v[162:163], v9 offset0:36 offset1:37\n"
"v_mov_b32 v161, 0\n"
"v_mfma_f32_16x16x32_bf16 v[156:159], v[210:213], v[136:139], v[156:159]\n"
"global_store_dwordx2 v255, v[228:229], s[66:67] offset:256 sc1\n"
"v_mfma_f32_16x16x32_bf16 v[164:167], v[236:239], v[136:139], v[164:167]\n"
"s_waitcnt lgkmcnt(1)\n"
"ds_read2st64_b32 v[138:139], v14 offset0:128 offset1:129\n"
"v_mfma_f32_16x16x32_bf16 v[144:147], v[148:151], v[140:143], v[144:147]\n"
"s_waitcnt lgkmcnt(1)\n"
"v_mfma_f32_16x16x32_bf16 v[144:147], v[148:151], v[160:163], v[144:147]\n"
"v_mfma_f32_16x16x32_bf16 v[164:167], v[248:251], v[140:143], v[164:167]\n"
"ds_read2st64_b32 v[136:137], v14 offset0:126 offset1:127\n"
"s_add_u32 m0, s51, 155648\n"
"s_nop 0\n"
"global_load_lds_dwordx4 v8, s[60:61]\n"
"v_mfma_f32_16x16x32_bf16 v[164:167], v[250:253], v[160:163], v[164:167]\n"
"ds_read_b128 v[148:151], v17 offset:53248\n"
"v_mfma_f32_16x16x32_bf16 v[152:155], v[196:199], v[140:143], v[152:155]\n"
"v_rcp_f32 v8, v144\n"
"v_mfma_f32_16x16x32_bf16 v[152:155], v[198:201], v[160:163], v[152:155]\n"
"ds_read2st64_b32 v[170:171], v14 offset0:176 offset1:177\n"
"v_add_u32 v144, 0xd4800000, v144\n"
"v_mfma_f32_16x16x32_bf16 v[156:159], v[222:225], v[140:143], v[156:159]\n"
"s_nop 0\n"
"v_mul_f32 v166, v166, v8\n"
"v_mul_f32 v167, v167, v8\n"
"ds_read2st64_b32 v[140:141], v14 offset0:150 offset1:151\n"
"ds_read2st64_b32 v[142:143], v14 offset0:152 offset1:153\n"
"v_mfma_f32_16x16x32_bf16 v[156:159], v[224:227], v[160:163], v[156:159]\n"
"s_waitcnt lgkmcnt(4)\n"
"v_mul_f32 v165, v165, v8\n"
"v_cmp_lt_u32 vcc, 0x28000000, v144\n"
"v_mfma_f32_16x16x32_bf16 v[144:147], v[96:99], v[136:139], 0\n"
"v_mul_f32 v164, v164, v8\n"
"v_cvt_pk_bf16_f32 v254, v164, v165\n"
"ds_read_b128 v[160:163], v17 offset:54272\n"
"v_cvt_pk_bf16_f32 v255, v166, v167\n"
"v_mfma_f32_16x16x32_bf16 v[164:167], v[100:103], v[136:139], 0\n"
"s_waitcnt lgkmcnt(4)\n"
"ds_read2st64_b32 v[168:169], v14 offset0:174 offset1:175\n"
"v_mul_f32 v152, v152, v8\n"
"v_mfma_f32_16x16x32_bf16 v[172:175], v[148:151], v[136:139], 0\n"
"s_waitcnt lgkmcnt(2)\n"
"ds_read_b128 v[136:139], v17 offset:55296\n"
"v_mul_f32 v154, v154, v8\n"
"v_mfma_f32_16x16x32_bf16 v[144:147], v[104:107], v[140:143], v[144:147]\n"
"v_mul_f32 v153, v153, v8\n"
"v_mul_f32 v155, v155, v8\n"
"ds_read2st64_b32 v[148:149], v14 offset0:198 offset1:199\n"
"v_mul_f32 v159, v159, v8\n"
"v_mfma_f32_16x16x32_bf16 v[164:167], v[108:111], v[140:143], v[164:167]\n"
"s_waitcnt lgkmcnt(3)\n"
"ds_read2st64_b32 v[150:151], v14 offset0:200 offset1:201\n"
"v_mul_f32 v156, v156, v8\n"
"v_mfma_f32_16x16x32_bf16 v[172:175], v[160:163], v[140:143], v[172:175]\n"
"s_waitcnt lgkmcnt(3)\n"
"ds_read_b128 v[140:143], v17 offset:56320\n"
"v_cvt_pk_bf16_f32 v152, v152, v153\n"
"v_cvt_pk_bf16_f32 v153, v154, v155\n"
"v_mfma_f32_16x16x32_bf16 v[144:147], v[0:3], v[168:171], v[144:147]\n"
"v_mul_f32 v157, v157, v8\n"
"ds_read2st64_b32 v[162:163], v14 offset0:202 offset1:203\n"
"v_cvt_pk_bf16_f32 v154, v156, v157\n"
"v_mfma_f32_16x16x32_bf16 v[164:167], v[112:115], v[168:171], v[164:167]\n"
"s_or_b64 s[62:63], s[62:63], vcc\n"
"v_mul_f32 v158, v158, v8\n"
"v_cvt_pk_bf16_f32 v155, v158, v159\n"
"ds_read2st64_b32 v[158:159], v14 offset0:154 offset1:155\n"
"s_waitcnt lgkmcnt(5)\n"
"v_mfma_f32_16x16x32_bf16 v[172:175], v[136:139], v[168:171], v[172:175]\n"
"s_setprio 0\n"
"ds_read_b128 v[136:139], v17 offset:57344\n"
"ds_read2st64_b32 v[156:157], v14 offset0:130 offset1:131\n"
"ds_read2st64_b32 v[160:161], v14 offset0:178 offset1:179\n"
"s_waitcnt lgkmcnt(6)\n"
"v_mfma_f32_16x16x32_bf16 v[144:147], v[116:119], v[148:151], v[144:147]\n"
"v_mfma_f32_16x16x32_bf16 v[164:167], v[120:123], v[148:151], v[164:167]\n"
"s_waitcnt lgkmcnt(5)\n"
"v_mfma_f32_16x16x32_bf16 v[172:175], v[140:143], v[148:151], v[172:175]\n"
"s_waitcnt lgkmcnt(1)\n"
"ds_read_b128 v[140:143], v17 offset:58368\n"
"v_mfma_f32_16x16x32_bf16 v[144:147], v[4:7], v[156:159], v[144:147]\n"
"ds_read_b128 v[148:151], v12 offset:18432\n"
"v_mfma_f32_16x16x32_bf16 v[164:167], v[124:127], v[156:159], v[164:167]\n"
"s_mov_b32 m0, s54\n"
"v_mfma_f32_16x16x32_bf16 v[172:175], v[136:139], v[156:159], v[172:175]\n"
"s_waitcnt lgkmcnt(2)\n"
"v_mfma_f32_16x16x32_bf16 v[144:147], v[128:131], v[160:163], v[144:147]\n"
"s_waitcnt vmcnt(5)\n"
"s_barrier\n"
"v_mfma_f32_16x16x32_bf16 v[164:167], v[132:135], v[160:163], v[164:167]\n"
"s_waitcnt lgkmcnt(1)\n"
"ds_read_b128 v[156:159], v12 offset:20480\n"
"v_mfma_f32_16x16x32_bf16 v[172:175], v[140:143], v[160:163], v[172:175]\n"
"ds_write_addtid_b32 v255 offset:33536\n"
"s_nop 0\n"
"v_cmp_class_f32_e64 vcc, v144, s64\n"
"s_waitcnt lgkmcnt(2)\n"
"v_mfma_f32_16x16x32_bf16 v[140:143], v[148:151], v[28:31], 0\n"
"ds_write_addtid_b32 v153 offset:32512\n"
"ds_read_b128 v[136:139], v13 offset:18432\n"
"v_mfma_f32_16x16x32_bf16 v[160:163], v[148:151], v[32:35], 0\n"
"v_cvt_pk_bf16_f32 v144, v144, v145\n"
"v_cvt_pk_bf16_f32 v145, v146, v147\n"
"v_mfma_f32_16x16x32_bf16 v[168:171], v[148:151], v[24:27], 0\n"
"ds_write_addtid_b32 v254 offset:33280\n"
"v_cvt_pk_bf16_f32 v172, v172, v173\n"
"s_waitcnt lgkmcnt(1)\n"
"v_mfma_f32_16x16x32_bf16 v[140:143], v[136:139], v[40:43], v[140:143]\n"
"v_cvt_pk_bf16_f32 v173, v174, v175\n"
"v_cvt_pk_bf16_f32 v146, v164, v165\n"
"v_mfma_f32_16x16x32_bf16 v[160:163], v[136:139], v[44:47], v[160:163]\n"
"v_cvt_pk_bf16_f32 v147, v166, v167\n"
"v_mfma_f32_16x16x32_bf16 v[168:171], v[136:139], v[36:39], v[168:171]\n"
"ds_read_b128 v[136:139], v12 offset:22528\n"
"ds_read_b128 v[148:151], v13 offset:20480\n"
"v_mfma_f32_16x16x32_bf16 v[140:143], v[156:159], v[48:51], v[140:143]\n"
"ds_write_addtid_b32 v154 offset:32768\n"
"v_mfma_f32_16x16x32_bf16 v[160:163], v[156:159], v[52:55], v[160:163]\n"
"ds_write_addtid_b32 v152 offset:32256\n"
"v_mfma_f32_16x16x32_bf16 v[168:171], v[156:159], v[56:59], v[168:171]\n"
"ds_write_addtid_b32 v155 offset:33024\n"
"s_waitcnt lgkmcnt(3)\n"
"v_mfma_f32_16x16x32_bf16 v[140:143], v[148:151], v[60:63], v[140:143]\n"
"s_mov_b32 m0, s68\n"
"ds_read_b128 v[152:155], v13 offset:22528\n"
"ds_write_addtid_b32 v144 offset:45056\n"
"v_mfma_f32_16x16x32_bf16 v[160:163], v[148:151], v[64:67], v[160:163]\n"
"v_mfma_f32_16x16x32_bf16 v[168:171], v[148:151], v[68:71], v[168:171]\n"
"ds_write_addtid_b32 v172 offset:46080\n"
"v_mfma_f32_16x16x32_bf16 v[140:143], v[136:139], v[72:75], v[140:143]\n"
"ds_read_b128 v[148:151], v12 offset:24576\n"
"s_or_b64 s[62:63], s[62:63], vcc\n"
"v_mfma_f32_16x16x32_bf16 v[160:163], v[136:139], v[76:79], v[160:163]\n"
"ds_write_addtid_b32 v145 offset:45312\n"
"v_mfma_f32_16x16x32_bf16 v[168:171], v[136:139], v[80:83], v[168:171]\n"
"ds_write_addtid_b32 v147 offset:45824\n"
"s_waitcnt lgkmcnt(5)\n"
"v_mfma_f32_16x16x32_bf16 v[140:143], v[152:155], v[84:87], v[140:143]\n"
"ds_write_addtid_b32 v146 offset:45568\n"
"v_mfma_f32_16x16x32_bf16 v[160:163], v[152:155], v[88:91], v[160:163]\n"
"ds_write_addtid_b32 v173 offset:46336\n"
"v_mfma_f32_16x16x32_bf16 v[168:171], v[152:155], v[92:95], v[168:171]\n"
"s_waitcnt lgkmcnt(4)\n"
"ds_read_b128 v[152:155], v12 offset:26624\n"
"ds_read_b128 v[136:139], v13 offset:24576\n"
"v_mfma_f32_16x16x32_bf16 v[144:147], v[148:151], v[28:31], 0\n"
"v_cvt_pk_bf16_f32 v203, v142, v143\n"
"v_mfma_f32_16x16x32_bf16 v[156:159], v[148:151], v[32:35], 0\n"
"v_mfma_f32_16x16x32_bf16 v[164:167], v[148:151], v[24:27], 0\n"
"s_waitcnt lgkmcnt(0)\n"
"v_cvt_pk_bf16_f32 v202, v140, v141\n"
"v_mfma_f32_16x16x32_bf16 v[144:147], v[136:139], v[40:43], v[144:147]\n"
"ds_read_b128 v[140:143], v13 offset:26624\n"
"v_mfma_f32_16x16x32_bf16 v[156:159], v[136:139], v[44:47], v[156:159]\n"
"v_mfma_f32_16x16x32_bf16 v[164:167], v[136:139], v[36:39], v[164:167]\n"
"ds_read_b128 v[136:139], v12 offset:28672\n"
"v_mfma_f32_16x16x32_bf16 v[144:147], v[152:155], v[48:51], v[144:147]\n"
"v_cvt_pk_bf16_f32 v255, v170, v171\n"
"v_mfma_f32_16x16x32_bf16 v[156:159], v[152:155], v[52:55], v[156:159]\n"
"v_mfma_f32_16x16x32_bf16 v[164:167], v[152:155], v[56:59], v[164:167]\n"
"s_waitcnt lgkmcnt(1)\n"
"v_mfma_f32_16x16x32_bf16 v[144:147], v[140:143], v[60:63], v[144:147]\n"
"v_cvt_pk_bf16_f32 v229, v162, v163\n"
"v_mfma_f32_16x16x32_bf16 v[156:159], v[140:143], v[64:67], v[156:159]\n"
"ds_read_b128 v[148:151], v13 offset:28672\n"
"v_cvt_pk_bf16_f32 v228, v160, v161\n"
"v_mfma_f32_16x16x32_bf16 v[164:167], v[140:143], v[68:71], v[164:167]\n"
"s_waitcnt lgkmcnt(1)\n"
"v_mfma_f32_16x16x32_bf16 v[144:147], v[136:139], v[72:75], v[144:147]\n"
"v_mov_b32 v143, 0x3f803f80\n"
"v_cvt_pk_bf16_f32 v254, v168, v169\n"
"v_mfma_f32_16x16x32_bf16 v[156:159], v[136:139], v[76:79], v[156:159]\n"
"v_mov_b32 v140, 0x3f803f80\n"
"v_mov_b32 v142, 0x3f803f80\n"
"v_mfma_f32_16x16x32_bf16 v[164:167], v[136:139], v[80:83], v[164:167]\n"
"s_waitcnt lgkmcnt(0)\n"
"v_mov_b32 v141, 0x3f803f80\n"
"v_mfma_f32_16x16x32_bf16 v[144:147], v[148:151], v[84:87], v[144:147]\n"
"s_waitcnt vmcnt(0)\n"
"s_barrier\n"
"ds_read2st64_b32 v[136:137], v9 offset0:120 offset1:121\n"
"v_mfma_f32_16x16x32_bf16 v[156:159], v[148:151], v[88:91], v[156:159]\n"
"ds_read2st64_b32 v[138:139], v9 offset0:122 offset1:123\n"
"v_mfma_f32_16x16x32_bf16 v[164:167], v[148:151], v[92:95], v[164:167]\n"
"s_and_b32 s41, 416, s55\n"
"v_add_u32 v8, s41, v15\n"
"v_cvt_pk_bf16_f32 v186, v144, v145\n"
"ds_read2st64_b32 v[150:151], v9 offset0:126 offset1:127\n"
"s_nop 1\n"
"v_cvt_pk_bf16_f32 v212, v156, v157\n"
"v_cvt_pk_bf16_f32 v187, v146, v147\n"
"ds_read_b32 v149, v9 offset:32000\n"
"ds_read_u16 v8, v8 offset:576\n"
"v_cvt_pk_bf16_f32 v239, v166, v167\n"
"v_cvt_pk_bf16_f32 v213, v158, v159\n"
"v_cvt_pk_bf16_f32 v238, v164, v165\n"
"s_waitcnt lgkmcnt(3)\n"
"v_mfma_f32_16x16x32_bf16 v[144:147], v[140:143], v[136:139], 0\n"
"v_mfma_f32_16x16x32_bf16 v[152:155], v[190:193], v[136:139], 0\n"
"s_waitcnt lgkmcnt(0)\n"
"v_mad_u32_u24 v8, v8, s40, v16\n"
"ds_read_u16 v193, v21 offset:832\n"
"v_mov_b32 v148, 0\n"
"v_mfma_f32_16x16x32_bf16 v[156:159], v[216:219], v[136:139], 0\n"
"ds_read2st64_b32 v[216:217], v20 offset0:0 offset1:1\n"
"ds_read2st64_b32 v[218:219], v20 offset0:2 offset1:3\n"
"v_mfma_f32_16x16x32_bf16 v[160:163], v[242:245], v[136:139], 0\n"
"s_setprio 2\n"
"v_mov_b32 v244, 0\n"
"ds_read_b32 v245, v9 offset:33536\n"
"s_add_u32 m0, s51, 126976\n"
"s_nop 0\n"
"global_load_lds_dwordx4 v8, s[56:57]\n"
"v_mfma_f32_16x16x32_bf16 v[144:147], v[140:143], v[148:151], v[144:147]\n"
"v_mfma_f32_16x16x32_bf16 v[152:155], v[182:185], v[148:151], v[152:155]\n"
"ds_read2st64_b32 v[242:243], v9 offset0:128 offset1:129\n"
"v_mfma_f32_16x16x32_bf16 v[156:159], v[208:211], v[148:151], v[156:159]\n"
"s_waitcnt lgkmcnt(4)\n"
"v_mad_u32_u24 v192, v193, s40, v18\n"
"v_mad_u32_u24 v193, v193, s40, v19\n"
"ds_read2st64_b32 v[190:191], v9 offset0:38 offset1:39\n"
"s_waitcnt lgkmcnt(3)\n"
"global_store_dwordx4 v192, v[216:219], s[66:67] sc1\n"
"v_mfma_f32_16x16x32_bf16 v[160:163], v[234:237], v[148:151], v[160:163]\n"
"s_waitcnt lgkmcnt(1)\n"
"v_mfma_f32_16x16x32_bf16 v[144:147], v[140:143], v[242:245], v[144:147]\n"
"ds_read_b32 v137, v9 offset:34048\n"
"ds_read2st64_b32 v[138:139], v9 offset0:134 offset1:135\n"
"v_mfma_f32_16x16x32_bf16 v[152:155], v[186:189], v[242:245], v[152:155]\n"
"ds_read2st64_b32 v[10:11], v20 offset0:4 offset1:5\n"
"v_mov_b32 v188, 0\n"
"v_mov_b32 v189, 0\n"
"v_mfma_f32_16x16x32_bf16 v[156:159], v[212:215], v[242:245], v[156:159]\n"
"ds_read2st64_b32 v[214:215], v14 offset0:126 offset1:127\n"
"v_mov_b32 v136, 0\n"
"v_mfma_f32_16x16x32_bf16 v[160:163], v[238:241], v[242:245], v[160:163]\n"
"ds_read_b128 v[240:243], v17 offset:53248\n"
"s_add_u32 m0, s51, 129024\n"
"s_nop 0\n"
"global_load_lds_dwordx4 v8, s[58:59]\n"
"s_waitcnt lgkmcnt(3)\n"
"v_mfma_f32_16x16x32_bf16 v[144:147], v[140:143], v[136:139], v[144:147]\n"
"s_waitcnt lgkmcnt(2)\n"
"global_store_dwordx2 v193, v[10:11], s[66:67] offset:256 sc1\n"
"v_mfma_f32_16x16x32_bf16 v[144:147], v[140:143], v[188:191], v[144:147]\n"
"v_mfma_f32_16x16x32_bf16 v[160:163], v[250:253], v[136:139], v[160:163]\n"
"s_add_u32 m0, s51, 131072\n"
"s_nop 0\n"
"global_load_lds_dwordx4 v8, s[60:61]\n"
"s_and_b32 s41, 416, s55\n"
"v_add_u32 v245, s41, v15\n"
"v_mfma_f32_16x16x32_bf16 v[160:163], v[252:255], v[188:191], v[160:163]\n"
"v_mfma_f32_16x16x32_bf16 v[152:155], v[198:201], v[136:139], v[152:155]\n"
"v_mfma_f32_16x16x32_bf16 v[152:155], v[200:203], v[188:191], v[152:155]\n"
"v_rcp_f32 v8, v144\n"
"ds_read2st64_b32 v[216:217], v14 offset0:128 offset1:129\n"
"v_add_u32 v144, 0xd4800000, v144\n"
"v_mfma_f32_16x16x32_bf16 v[156:159], v[224:227], v[136:139], v[156:159]\n"
"ds_read2st64_b32 v[136:137], v14 offset0:150 offset1:151\n"
"v_cmp_lt_u32 vcc, 0x28000000, v144\n"
"v_mfma_f32_16x16x32_bf16 v[156:159], v[226:229], v[188:191], v[156:159]\n"
"s_nop 0\n"
"v_mul_f32 v153, v153, v8\n"
"v_mul_f32 v162, v162, v8\n"
"v_mul_f32 v163, v163, v8\n"
"v_cvt_pk_bf16_f32 v193, v162, v163\n"
"ds_read_b128 v[144:147], v17 offset:54272\n"
"s_waitcnt lgkmcnt(2)\n"
"v_mfma_f32_16x16x32_bf16 v[188:191], v[96:99], v[214:217], 0\n"
"v_mul_f32 v160, v160, v8\n"
"v_mul_f32 v159, v159, v8\n"
"v_mfma_f32_16x16x32_bf16 v[140:143], v[100:103], v[214:217], 0\n"
"v_mul_f32 v161, v161, v8\n"
"v_cvt_pk_bf16_f32 v192, v160, v161\n"
"ds_read2st64_b32 v[138:139], v14 offset0:152 offset1:153\n"
"v_mul_f32 v157, v157, v8\n"
"v_mfma_f32_16x16x32_bf16 v[148:151], v[240:243], v[214:217], 0\n"
"v_mul_f32 v155, v155, v8\n"
"v_mul_f32 v152, v152, v8\n"
"ds_read2st64_b32 v[214:215], v14 offset0:174 offset1:175\n"
"v_cvt_pk_bf16_f32 v152, v152, v153\n"
"s_setprio 0\n"
"v_mul_f32 v156, v156, v8\n"
"s_or_b64 s[62:63], s[62:63], vcc\n"
"v_mul_f32 v158, v158, v8\n"
"v_mul_f32 v154, v154, v8\n"
"ds_read2st64_b32 v[216:217], v14 offset0:176 offset1:177\n"
"ds_read_b128 v[240:243], v17 offset:55296\n"
"ds_read_u16 v245, v245 offset:608\n"
"s_waitcnt lgkmcnt(4)\n"
"v_mfma_f32_16x16x32_bf16 v[188:191], v[104:107], v[136:139], v[188:191]\n"
"v_cvt_pk_bf16_f32 v153, v154, v155\n"
"v_mfma_f32_16x16x32_bf16 v[140:143], v[108:111], v[136:139], v[140:143]\n"
"v_cvt_pk_bf16_f32 v155, v158, v159\n"
"v_cvt_pk_bf16_f32 v154, v156, v157\n"
"v_mfma_f32_16x16x32_bf16 v[148:151], v[144:147], v[136:139], v[148:151]\n"
"s_waitcnt lgkmcnt(2)\n"
"ds_read2st64_b32 v[138:139], v14 offset0:200 offset1:201\n"
"ds_read2st64_b32 v[136:137], v14 offset0:198 offset1:199\n"
"ds_read2st64_b32 v[158:159], v14 offset0:154 offset1:155\n"
"v_mfma_f32_16x16x32_bf16 v[188:191], v[0:3], v[214:217], v[188:191]\n"
"ds_read2st64_b32 v[156:157], v14 offset0:130 offset1:131\n"
"v_mfma_f32_16x16x32_bf16 v[140:143], v[112:115], v[214:217], v[140:143]\n"
"s_waitcnt lgkmcnt(4)\n"
"ds_read_b128 v[144:147], v17 offset:56320\n"
"v_mad_u32_u24 v245, v245, s40, v16\n"
"v_mfma_f32_16x16x32_bf16 v[148:151], v[240:243], v[214:217], v[148:151]\n"
"s_waitcnt lgkmcnt(3)\n"
"ds_read_b128 v[214:217], v17 offset:57344\n"
"ds_read2st64_b32 v[242:243], v14 offset0:202 offset1:203\n"
"v_mfma_f32_16x16x32_bf16 v[188:191], v[116:119], v[136:139], v[188:191]\n"
"s_add_u32 m0, s51, 114688\n"
"s_nop 0\n"
"global_load_lds_dwordx4 v245, s[56:57]\n"
"v_mfma_f32_16x16x32_bf16 v[140:143], v[120:123], v[136:139], v[140:143]\n"
"s_waitcnt lgkmcnt(2)\n"
"ds_read2st64_b32 v[240:241], v14 offset0:178 offset1:179\n"
"v_mfma_f32_16x16x32_bf16 v[148:151], v[144:147], v[136:139], v[148:151]\n"
"ds_read_b128 v[136:139], v17 offset:58368\n"
"v_mfma_f32_16x16x32_bf16 v[188:191], v[4:7], v[156:159], v[188:191]\n"
"v_mfma_f32_16x16x32_bf16 v[140:143], v[124:127], v[156:159], v[140:143]\n"
"s_waitcnt lgkmcnt(3)\n"
"ds_read_b128 v[144:147], v12 offset:36864\n"
"v_mfma_f32_16x16x32_bf16 v[148:151], v[214:217], v[156:159], v[148:151]\n"
"s_waitcnt lgkmcnt(2)\n"
"ds_read_b128 v[156:159], v12 offset:38912\n"
"v_mfma_f32_16x16x32_bf16 v[188:191], v[128:131], v[240:243], v[188:191]\n"
"s_add_u32 m0, s51, 116736\n"
"s_nop 0\n"
"global_load_lds_dwordx4 v245, s[58:59]\n"
"v_mfma_f32_16x16x32_bf16 v[140:143], v[132:135], v[240:243], v[140:143]\n"
"s_barrier\n"
"s_nop 2\n"
"v_cmp_class_f32_e64 vcc, v188, s64\n"
"v_cvt_pk_bf16_f32 v188, v188, v189\n"
"v_cvt_pk_bf16_f32 v189, v190, v191\n"
"s_waitcnt lgkmcnt(2)\n"
"v_mfma_f32_16x16x32_bf16 v[148:151], v[136:139], v[240:243], v[148:151]\n"
"s_waitcnt lgkmcnt(1)\n"
"v_mfma_f32_16x16x32_bf16 v[240:243], v[144:147], v[28:31], 0\n"
"v_cvt_pk_bf16_f32 v191, v142, v143\n"
"s_add_u32 m0, s51, 118784\n"
"s_nop 0\n"
"global_load_lds_dwordx4 v245, s[60:61]\n"
"ds_read_b128 v[214:217], v13 offset:36864\n"
"v_cvt_pk_bf16_f32 v190, v140, v141\n"
"v_cvt_pk_bf16_f32 v148, v148, v149\n"
"v_mfma_f32_16x16x32_bf16 v[136:139], v[144:147], v[32:35], 0\n"
"s_mov_b32 m0, s54\n"
"v_mfma_f32_16x16x32_bf16 v[160:163], v[144:147], v[24:27], 0\n"
"ds_write_addtid_b32 v155 offset:33024\n"
"ds_read_b128 v[144:147], v13 offset:38912\n"
"s_waitcnt lgkmcnt(2)\n"
"v_mfma_f32_16x16x32_bf16 v[240:243], v[214:217], v[40:43], v[240:243]\n"
"ds_write_addtid_b32 v193 offset:33536\n"
"v_mfma_f32_16x16x32_bf16 v[136:139], v[214:217], v[44:47], v[136:139]\n"
"v_mfma_f32_16x16x32_bf16 v[160:163], v[214:217], v[36:39], v[160:163]\n"
"v_cvt_pk_bf16_f32 v149, v150, v151\n"
"ds_read_b128 v[214:217], v12 offset:40960\n"
"v_mfma_f32_16x16x32_bf16 v[240:243], v[156:159], v[48:51], v[240:243]\n"
"ds_write_addtid_b32 v154 offset:32768\n"
"v_mfma_f32_16x16x32_bf16 v[136:139], v[156:159], v[52:55], v[136:139]\n"
"ds_write_addtid_b32 v153 offset:32512\n"
"v_mfma_f32_16x16x32_bf16 v[160:163], v[156:159], v[56:59], v[160:163]\n"
"ds_write_addtid_b32 v192 offset:33280\n"
"ds_read_b128 v[140:143], v13 offset:40960\n"
"s_waitcnt lgkmcnt(6)\n"
"v_mfma_f32_16x16x32_bf16 v[240:243], v[144:147], v[60:63], v[240:243]\n"
"ds_write_addtid_b32 v152 offset:32256\n"
"s_mov_b32 m0, s68\n"
"s_nop 0\n"
"ds_write_addtid_b32 v190 offset:45568\n"
"v_mfma_f32_16x16x32_bf16 v[136:139], v[144:147], v[64:67], v[136:139]\n"
"s_or_b64 s[62:63], s[62:63], vcc\n"
"ds_write_addtid_b32 v191 offset:45824\n"
"v_mfma_f32_16x16x32_bf16 v[160:163], v[144:147], v[68:71], v[160:163]\n"
"s_waitcnt lgkmcnt(7)\n"
"ds_read_b128 v[190:193], v13 offset:43008\n"
"v_mfma_f32_16x16x32_bf16 v[240:243], v[214:217], v[72:75], v[240:243]\n"
"ds_read_b128 v[144:147], v12 offset:43008\n"
"v_mfma_f32_16x16x32_bf16 v[136:139], v[214:217], v[76:79], v[136:139]\n"
"ds_write_addtid_b32 v189 offset:45312\n"
"v_mfma_f32_16x16x32_bf16 v[160:163], v[214:217], v[80:83], v[160:163]\n"
"s_waitcnt lgkmcnt(6)\n"
"v_mfma_f32_16x16x32_bf16 v[240:243], v[140:143], v[84:87], v[240:243]\n"
"ds_write_addtid_b32 v188 offset:45056\n"
"v_mfma_f32_16x16x32_bf16 v[136:139], v[140:143], v[88:91], v[136:139]\n"
"ds_write_addtid_b32 v149 offset:46336\n"
"v_mfma_f32_16x16x32_bf16 v[160:163], v[140:143], v[92:95], v[160:163]\n"
"s_waitcnt lgkmcnt(3)\n"
"ds_read_b128 v[140:143], v12 offset:45056\n"
"s_setprio 2\n"
"v_mfma_f32_16x16x32_bf16 v[216:219], v[144:147], v[28:31], 0\n"
"ds_write_addtid_b32 v148 offset:46080\n"
"v_mfma_f32_16x16x32_bf16 v[148:151], v[144:147], v[32:35], 0\n"
"v_cvt_pk_bf16_f32 v214, v136, v137\n"
"v_mfma_f32_16x16x32_bf16 v[152:155], v[144:147], v[24:27], 0\n"
"v_mfma_f32_16x16x32_bf16 v[216:219], v[190:193], v[40:43], v[216:219]\n"
"v_cvt_pk_bf16_f32 v189, v242, v243\n"
"v_cvt_pk_bf16_f32 v215, v138, v139\n"
"v_mfma_f32_16x16x32_bf16 v[148:151], v[190:193], v[44:47], v[148:151]\n"
"v_mfma_f32_16x16x32_bf16 v[152:155], v[190:193], v[36:39], v[152:155]\n"
"v_cvt_pk_bf16_f32 v188, v240, v241\n"
"v_cvt_pk_bf16_f32 v241, v162, v163\n"
"s_setprio 0\n"
"ds_read_b128 v[242:245], v13 offset:45056\n"
"s_waitcnt lgkmcnt(2)\n"
"v_mfma_f32_16x16x32_bf16 v[216:219], v[140:143], v[48:51], v[216:219]\n"
"ds_read_b128 v[136:139], v13 offset:47104\n"
"v_mfma_f32_16x16x32_bf16 v[148:151], v[140:143], v[52:55], v[148:151]\n"
"ds_read_b128 v[190:193], v12 offset:47104\n"
"v_mfma_f32_16x16x32_bf16 v[152:155], v[140:143], v[56:59], v[152:155]\n"
"s_waitcnt lgkmcnt(2)\n"
"v_mfma_f32_16x16x32_bf16 v[216:219], v[242:245], v[60:63], v[216:219]\n"
"v_cvt_pk_bf16_f32 v240, v160, v161\n"
"v_mfma_f32_16x16x32_bf16 v[148:151], v[242:245], v[64:67], v[148:151]\n"
"v_mov_b32 v142, 0x3f803f80\n"
"v_mfma_f32_16x16x32_bf16 v[152:155], v[242:245], v[68:71], v[152:155]\n"
"s_waitcnt lgkmcnt(0)\n"
"v_mfma_f32_16x16x32_bf16 v[216:219], v[190:193], v[72:75], v[216:219]\n"
"v_mfma_f32_16x16x32_bf16 v[148:151], v[190:193], v[76:79], v[148:151]\n"
"v_mov_b32 v141, 0x3f803f80\n"
"v_mfma_f32_16x16x32_bf16 v[152:155], v[190:193], v[80:83], v[152:155]\n"
"v_mov_b32 v140, 0x3f803f80\n"
"v_mfma_f32_16x16x32_bf16 v[216:219], v[136:139], v[84:87], v[216:219]\n"
"s_waitcnt vmcnt(3)\n"
"s_barrier\n"
"v_mfma_f32_16x16x32_bf16 v[148:151], v[136:139], v[88:91], v[148:151]\n"
"ds_read2st64_b32 v[144:145], v9 offset0:112 offset1:113\n"
"ds_read2st64_b32 v[146:147], v9 offset0:114 offset1:115\n"
"v_mfma_f32_16x16x32_bf16 v[152:155], v[136:139], v[92:95], v[152:155]\n"
"ds_read2st64_b32 v[160:161], v9 offset0:130 offset1:131\n"
"s_andn2_b32 s41, 416, s55\n"
"v_add_u32 v8, s41, v15\n"
"v_cvt_pk_bf16_f32 v191, v218, v219\n"
"v_cvt_pk_bf16_f32 v190, v216, v217\n"
"v_cvt_pk_bf16_f32 v217, v150, v151\n"
"ds_read2st64_b32 v[136:137], v9 offset0:116 offset1:117\n"
"v_mov_b32 v143, 0x3f803f80\n"
"ds_read2st64_b32 v[168:169], v20 offset0:0 offset1:1\n"
"v_cvt_pk_bf16_f32 v216, v148, v149\n"
"s_waitcnt lgkmcnt(3)\n"
"v_mfma_f32_16x16x32_bf16 v[148:151], v[140:143], v[144:147], 0\n"
"ds_read2st64_b32 v[138:139], v9 offset0:118 offset1:119\n"
"v_cvt_pk_bf16_f32 v243, v154, v155\n"
"v_mfma_f32_16x16x32_bf16 v[164:167], v[178:181], v[144:147], 0\n"
"ds_read_u16 v8, v8 offset:640\n"
"v_cvt_pk_bf16_f32 v242, v152, v153\n"
"v_mfma_f32_16x16x32_bf16 v[152:155], v[204:207], v[144:147], 0\n"
"ds_read_u16 v245, v21 offset:864\n"
"v_mov_b32 v156, 0\n"
"ds_read2st64_b32 v[158:159], v9 offset0:128 offset1:129\n"
"v_mfma_f32_16x16x32_bf16 v[172:175], v[230:233], v[144:147], 0\n"
"s_waitcnt lgkmcnt(3)\n"
"v_mov_b32 v157, 0\n"
"v_mfma_f32_16x16x32_bf16 v[148:151], v[140:143], v[136:139], v[148:151]\n"
"s_waitcnt lgkmcnt(0)\n"
"ds_read2st64_b32 v[218:219], v20 offset0:4 offset1:5\n"
"ds_read2st64_b32 v[170:171], v20 offset0:2 offset1:3\n"
"v_mfma_f32_16x16x32_bf16 v[148:151], v[140:143], v[158:161], v[148:151]\n"
"v_mad_u32_u24 v8, v8, s40, v16\n"
"s_add_u32 m0, s51, 139264\n"
"s_nop 0\n"
"global_load_lds_dwordx4 v8, s[56:57]\n"
"v_mfma_f32_16x16x32_bf16 v[172:175], v[246:249], v[136:139], v[172:175]\n"
"v_mov_b32 v162, 0\n"
"v_mad_u32_u24 v244, v245, s40, v18\n"
"v_mov_b32 v163, 0\n"
"v_mfma_f32_16x16x32_bf16 v[172:175], v[232:235], v[156:159], v[172:175]\n"
"v_mad_u32_u24 v245, v245, s40, v19\n"
"v_mfma_f32_16x16x32_bf16 v[172:175], v[250:253], v[160:163], v[172:175]\n"
"s_waitcnt lgkmcnt(0)\n"
"ds_read2st64_b32 v[142:143], v14 offset0:128 offset1:129\n"
"global_store_dwordx4 v244, v[168:171], s[66:67] sc1\n"
"v_mfma_f32_16x16x32_bf16 v[164:167], v[194:197], v[136:139], v[164:167]\n"
"ds_read2st64_b32 v[144:145], v14 offset0:150 offset1:151\n"
"ds_read2st64_b32 v[140:141], v14 offset0:126 offset1:127\n"
"v_mfma_f32_16x16x32_bf16 v[164:167], v[180:183], v[156:159], v[164:167]\n"
"v_mfma_f32_16x16x32_bf16 v[164:167], v[198:201], v[160:163], v[164:167]\n"
"s_add_u32 m0, s51, 141312\n"
"s_nop 0\n"
"global_load_lds_dwordx4 v8, s[58:59]\n"
"global_store_dwordx2 v245, v[218:219], s[66:67] offset:256 sc1\n"
"v_mfma_f32_16x16x32_bf16 v[152:155], v[220:223], v[136:139], v[152:155]\n"
"s_setprio 2\n"
"ds_read_b128 v[136:139], v17 offset:53248\n"
"v_rcp_f32 v245, v148\n"
"v_add_u32 v148, 0xd4800000, v148\n"
"v_mul_f32 v164, v164, v245\n"
"v_mfma_f32_16x16x32_bf16 v[152:155], v[206:209], v[156:159], v[152:155]\n"
"v_mul_f32 v174, v174, v245\n"
"v_mul_f32 v172, v172, v245\n"
"v_mul_f32 v165, v165, v245\n"
"v_mul_f32 v173, v173, v245\n"
"v_mfma_f32_16x16x32_bf16 v[152:155], v[224:227], v[160:163], v[152:155]\n"
"s_waitcnt lgkmcnt(1)\n"
"ds_read_b128 v[156:159], v17 offset:54272\n"
"ds_read2st64_b32 v[146:147], v14 offset0:152 offset1:153\n"
"v_cmp_lt_u32 vcc, 0x28000000, v148\n"
"v_mfma_f32_16x16x32_bf16 v[148:151], v[96:99], v[140:143], 0\n"
"v_cvt_pk_bf16_f32 v218, v172, v173\n"
"s_add_u32 m0, s51, 143360\n"
"s_nop 0\n"
"global_load_lds_dwordx4 v8, s[60:61]\n"
"s_or_b64 s[62:63], s[62:63], vcc\n"
"v_mfma_f32_16x16x32_bf16 v[160:163], v[100:103], v[140:143], 0\n"
"s_waitcnt lgkmcnt(2)\n"
"ds_read2st64_b32 v[170:171], v14 offset0:176 offset1:177\n"
"v_mul_f32 v175, v175, v245\n"
"v_cvt_pk_bf16_f32 v219, v174, v175\n"
"v_mfma_f32_16x16x32_bf16 v[172:175], v[136:139], v[140:143], 0\n"
"s_waitcnt lgkmcnt(1)\n"
"ds_read2st64_b32 v[168:169], v14 offset0:174 offset1:175\n"
"v_mul_f32 v167, v167, v245\n"
"ds_read_b128 v[136:139], v17 offset:55296\n"
"v_mfma_f32_16x16x32_bf16 v[148:151], v[104:107], v[144:147], v[148:151]\n"
"v_cvt_pk_bf16_f32 v164, v164, v165\n"
"v_mul_f32 v166, v166, v245\n"
"ds_read2st64_b32 v[142:143], v14 offset0:200 offset1:201\n"
"ds_read2st64_b32 v[140:141], v14 offset0:198 offset1:199\n"
"v_mfma_f32_16x16x32_bf16 v[160:163], v[108:111], v[144:147], v[160:163]\n"
"v_mul_f32 v155, v155, v245\n"
"v_cvt_pk_bf16_f32 v165, v166, v167\n"
"s_setprio 0\n"
"v_mul_f32 v152, v152, v245\n"
"v_mfma_f32_16x16x32_bf16 v[172:175], v[156:159], v[144:147], v[172:175]\n"
"s_waitcnt lgkmcnt(3)\n"
"ds_read_b128 v[144:147], v17 offset:56320\n"
"v_mul_f32 v153, v153, v245\n"
"v_mfma_f32_16x16x32_bf16 v[148:151], v[0:3], v[168:171], v[148:151]\n"
"v_mul_f32 v154, v154, v245\n"
"v_cvt_pk_bf16_f32 v166, v152, v153\n"
"ds_read2st64_b32 v[152:153], v14 offset0:130 offset1:131\n"
"v_cvt_pk_bf16_f32 v167, v154, v155\n"
"ds_read2st64_b32 v[156:157], v14 offset0:178 offset1:179\n"
"v_mfma_f32_16x16x32_bf16 v[160:163], v[112:115], v[168:171], v[160:163]\n"
"s_waitcnt lgkmcnt(5)\n"
"ds_read2st64_b32 v[154:155], v14 offset0:154 offset1:155\n"
"v_mfma_f32_16x16x32_bf16 v[172:175], v[136:139], v[168:171], v[172:175]\n"
"s_waitcnt lgkmcnt(4)\n"
"ds_read_b128 v[136:139], v17 offset:57344\n"
"v_mfma_f32_16x16x32_bf16 v[148:151], v[116:119], v[140:143], v[148:151]\n"
"v_mfma_f32_16x16x32_bf16 v[160:163], v[120:123], v[140:143], v[160:163]\n"
"s_waitcnt lgkmcnt(4)\n"
"ds_read2st64_b32 v[158:159], v14 offset0:202 offset1:203\n"
"v_mfma_f32_16x16x32_bf16 v[172:175], v[144:147], v[140:143], v[172:175]\n"
"s_waitcnt lgkmcnt(2)\n"
"v_mfma_f32_16x16x32_bf16 v[148:151], v[4:7], v[152:155], v[148:151]\n"
"ds_read_b128 v[140:143], v17 offset:58368\n"
"s_mov_b32 m0, s54\n"
"v_mfma_f32_16x16x32_bf16 v[160:163], v[124:127], v[152:155], v[160:163]\n"
"s_waitcnt lgkmcnt(2)\n"
"ds_read_b128 v[144:147], v12 offset:49152\n"
"v_mfma_f32_16x16x32_bf16 v[172:175], v[136:139], v[152:155], v[172:175]\n"
"s_waitcnt lgkmcnt(2)\n"
"ds_read_b128 v[136:139], v13 offset:49152\n"
"v_mfma_f32_16x16x32_bf16 v[148:151], v[128:131], v[156:159], v[148:151]\n"
"v_mfma_f32_16x16x32_bf16 v[160:163], v[132:135], v[156:159], v[160:163]\n"
"s_barrier\n"
"s_waitcnt lgkmcnt(2)\n"
"v_mfma_f32_16x16x32_bf16 v[172:175], v[140:143], v[156:159], v[172:175]\n"
"s_waitcnt lgkmcnt(1)\n"
"v_mfma_f32_16x16x32_bf16 v[140:143], v[144:147], v[28:31], 0\n"
"ds_read_b128 v[152:155], v12 offset:51200\n"
"v_mfma_f32_16x16x32_bf16 v[156:159], v[144:147], v[32:35], 0\n"
"ds_write_addtid_b32 v219 offset:33536\n"
"v_mfma_f32_16x16x32_bf16 v[168:171], v[144:147], v[24:27], 0\n"
"ds_write_addtid_b32 v218 offset:33280\n"
"s_waitcnt lgkmcnt(3)\n"
"v_mfma_f32_16x16x32_bf16 v[140:143], v[136:139], v[40:43], v[140:143]\n"
"v_mfma_f32_16x16x32_bf16 v[156:159], v[136:139], v[44:47], v[156:159]\n"
"v_cmp_class_f32_e64 vcc, v148, s64\n"
"v_cvt_pk_bf16_f32 v148, v148, v149\n"
"v_cvt_pk_bf16_f32 v172, v172, v173\n"
"v_cvt_pk_bf16_f32 v173, v174, v175\n"
"v_cvt_pk_bf16_f32 v149, v150, v151\n"
"v_cvt_pk_bf16_f32 v151, v162, v163\n"
"v_cvt_pk_bf16_f32 v150, v160, v161\n"
"v_mfma_f32_16x16x32_bf16 v[168:171], v[136:139], v[36:39], v[168:171]\n"
"s_waitcnt lgkmcnt(2)\n"
"ds_read_b128 v[144:147], v13 offset:51200\n"
"ds_read_b128 v[136:139], v12 offset:53248\n"
"v_mfma_f32_16x16x32_bf16 v[140:143], v[152:155], v[48:51], v[140:143]\n"
"ds_write_addtid_b32 v167 offset:33024\n"
"v_mfma_f32_16x16x32_bf16 v[156:159], v[152:155], v[52:55], v[156:159]\n"
"ds_write_addtid_b32 v165 offset:32512\n"
"v_mfma_f32_16x16x32_bf16 v[168:171], v[152:155], v[56:59], v[168:171]\n"
"ds_write_addtid_b32 v166 offset:32768\n"
"s_waitcnt lgkmcnt(4)\n"
"v_mfma_f32_16x16x32_bf16 v[140:143], v[144:147], v[60:63], v[140:143]\n"
"ds_write_addtid_b32 v164 offset:32256\n"
"s_mov_b32 m0, s68\n"
"s_or_b64 s[62:63], s[62:63], vcc\n"
"ds_write_addtid_b32 v173 offset:46336\n"
"v_mfma_f32_16x16x32_bf16 v[156:159], v[144:147], v[64:67], v[156:159]\n"
"ds_write_addtid_b32 v149 offset:45312\n"
"ds_read_b128 v[152:155], v13 offset:53248\n"
"v_mfma_f32_16x16x32_bf16 v[168:171], v[144:147], v[68:71], v[168:171]\n"
"ds_write_addtid_b32 v172 offset:46080\n"
"s_waitcnt lgkmcnt(8)\n"
"v_mfma_f32_16x16x32_bf16 v[140:143], v[136:139], v[72:75], v[140:143]\n"
"ds_write_addtid_b32 v150 offset:45568\n"
"v_mfma_f32_16x16x32_bf16 v[156:159], v[136:139], v[76:79], v[156:159]\n"
"ds_write_addtid_b32 v148 offset:45056\n"
"v_mfma_f32_16x16x32_bf16 v[168:171], v[136:139], v[80:83], v[168:171]\n"
"ds_write_addtid_b32 v151 offset:45824\n"
"s_waitcnt lgkmcnt(4)\n"
"v_mfma_f32_16x16x32_bf16 v[140:143], v[152:155], v[84:87], v[140:143]\n"
"v_mov_b32 v145, 0x3f803f80\n"
"v_mov_b32 v144, 0x3f803f80\n"
"v_mfma_f32_16x16x32_bf16 v[156:159], v[152:155], v[88:91], v[156:159]\n"
"v_mfma_f32_16x16x32_bf16 v[168:171], v[152:155], v[92:95], v[168:171]\n"
"s_waitcnt lgkmcnt(0)\n"
"s_waitcnt vmcnt(5)\n"
"s_barrier\n"
"ds_read2st64_b32 v[148:149], v9 offset0:124 offset1:125\n"
"ds_read2st64_b32 v[136:137], v9 offset0:120 offset1:121\n"
"ds_read2st64_b32 v[138:139], v9 offset0:122 offset1:123\n"
"v_mov_b32 v146, 0x3f803f80\n"
"v_cvt_pk_bf16_f32 v218, v156, v157\n"
"v_cvt_pk_bf16_f32 v193, v142, v143\n"
"s_and_b32 s41, 416, s55\n"
"v_add_u32 v11, s41, v15\n"
"v_mov_b32 v147, 0x3f803f80\n"
"v_cvt_pk_bf16_f32 v244, v168, v169\n"
"ds_read2st64_b32 v[150:151], v9 offset0:126 offset1:127\n"
"ds_read_u16 v11, v11 offset:672\n"
"v_cvt_pk_bf16_f32 v192, v140, v141\n"
"s_waitcnt lgkmcnt(2)\n"
"v_mfma_f32_16x16x32_bf16 v[140:143], v[144:147], v[136:139], 0\n"
"v_cvt_pk_bf16_f32 v219, v158, v159\n"
"v_mfma_f32_16x16x32_bf16 v[152:155], v[178:181], v[136:139], 0\n"
"ds_read_b32 v164, v9 offset:32768\n"
"v_mfma_f32_16x16x32_bf16 v[156:159], v[204:207], v[136:139], 0\n"
"v_cvt_pk_bf16_f32 v245, v170, v171\n"
"v_mfma_f32_16x16x32_bf16 v[160:163], v[230:233], v[136:139], 0\n"
"s_waitcnt lgkmcnt(2)\n"
"ds_read_u16 v8, v21 offset:896\n"
"ds_read2st64_b32 v[136:137], v20 offset0:0 offset1:1\n"
"ds_read2st64_b32 v[138:139], v20 offset0:2 offset1:3\n"
"v_mfma_f32_16x16x32_bf16 v[140:143], v[144:147], v[148:151], v[140:143]\n"
"v_mfma_f32_16x16x32_bf16 v[152:155], v[188:191], v[148:151], v[152:155]\n"
"ds_read2st64_b32 v[166:167], v9 offset0:130 offset1:131\n"
"v_mfma_f32_16x16x32_bf16 v[156:159], v[214:217], v[148:151], v[156:159]\n"
"v_mov_b32 v165, 0\n"
"v_mfma_f32_16x16x32_bf16 v[160:163], v[240:243], v[148:151], v[160:163]\n"
"s_waitcnt lgkmcnt(3)\n"
"ds_read2st64_b32 v[148:149], v9 offset0:132 offset1:133\n"
"v_mad_u32_u24 v10, v8, s40, v18\n"
"v_mad_u32_u24 v8, v8, s40, v19\n"
"s_waitcnt lgkmcnt(2)\n"
"global_store_dwordx4 v10, v[136:139], s[66:67] sc1\n"
"ds_read2st64_b32 v[174:175], v20 offset0:4 offset1:5\n"
"ds_read_b32 v150, v9 offset:34304\n"
"ds_read_b32 v138, v9 offset:9728\n"
"s_waitcnt lgkmcnt(4)\n"
"v_mfma_f32_16x16x32_bf16 v[140:143], v[144:147], v[164:167], v[140:143]\n"
"v_mad_u32_u24 v11, v11, s40, v16\n"
"v_mov_b32 v136, 0\n"
"v_mfma_f32_16x16x32_bf16 v[152:155], v[192:195], v[164:167], v[152:155]\n"
"v_mov_b32 v151, 0\n"
"v_mfma_f32_16x16x32_bf16 v[156:159], v[218:221], v[164:167], v[156:159]\n"
"s_add_u32 m0, s51, 151552\n"
"s_nop 0\n"
"global_load_lds_dwordx4 v11, s[56:57]\n"
"s_waitcnt lgkmcnt(2)\n"
"global_store_dwordx2 v8, v[174:175], s[66:67] offset:256 sc1\n"
"v_mfma_f32_16x16x32_bf16 v[160:163], v[244:247], v[164:167], v[160:163]\n"
"s_waitcnt lgkmcnt(1)\n"
"s_setprio 2\n"
"ds_read2st64_b32 v[168:169], v14 offset0:150 offset1:151\n"
"v_mfma_f32_16x16x32_bf16 v[140:143], v[144:147], v[148:151], v[140:143]\n"
"s_waitcnt lgkmcnt(1)\n"
"v_mov_b32 v137, 0\n"
"v_mov_b32 v139, 0\n"
"s_nop 1\n"
"v_mfma_f32_16x16x32_bf16 v[140:143], v[144:147], v[136:139], v[140:143]\n"
"ds_read2st64_b32 v[146:147], v14 offset0:128 offset1:129\n"
"ds_read_b128 v[164:167], v17 offset:53248\n"
"v_mfma_f32_16x16x32_bf16 v[160:163], v[248:251], v[148:151], v[160:163]\n"
"ds_read2st64_b32 v[144:145], v14 offset0:126 offset1:127\n"
"v_mfma_f32_16x16x32_bf16 v[160:163], v[232:235], v[136:139], v[160:163]\n"
"s_add_u32 m0, s51, 153600\n"
"s_nop 0\n"
"global_load_lds_dwordx4 v11, s[58:59]\n"
"v_mfma_f32_16x16x32_bf16 v[152:155], v[196:199], v[148:151], v[152:155]\n"
"v_rcp_f32 v8, v140\n"
"v_mfma_f32_16x16x32_bf16 v[152:155], v[180:183], v[136:139], v[152:155]\n"
"v_add_u32 v140, 0xd4800000, v140\n"
"v_cmp_lt_u32 vcc, 0x28000000, v140\n"
"v_mfma_f32_16x16x32_bf16 v[156:159], v[222:225], v[148:151], v[156:159]\n"
"v_mul_f32 v161, v161, v8\n"
"ds_read2st64_b32 v[170:171], v14 offset0:152 offset1:153\n"
"v_mfma_f32_16x16x32_bf16 v[156:159], v[206:209], v[136:139], v[156:159]\n"
"s_waitcnt lgkmcnt(1)\n"
"ds_read_b128 v[136:139], v17 offset:54272\n"
"v_mul_f32 v160, v160, v8\n"
"v_cvt_pk_bf16_f32 v246, v160, v161\n"
"v_mfma_f32_16x16x32_bf16 v[140:143], v[96:99], v[144:147], 0\n"
"s_add_u32 m0, s51, 155648\n"
"s_nop 0\n"
"global_load_lds_dwordx4 v11, s[60:61]\n"
"v_mfma_f32_16x16x32_bf16 v[148:151], v[100:103], v[144:147], 0\n"
"ds_read2st64_b32 v[160:161], v14 offset0:174 offset1:175\n"
"v_mul_f32 v163, v163, v8\n"
"s_or_b64 s[62:63], s[62:63], vcc\n"
"v_mul_f32 v162, v162, v8\n"
"v_cvt_pk_bf16_f32 v247, v162, v163\n"
"v_mfma_f32_16x16x32_bf16 v[172:175], v[164:167], v[144:147], 0\n"
"s_waitcnt lgkmcnt(2)\n"
"ds_read_b128 v[144:147], v17 offset:55296\n"
"ds_read2st64_b32 v[162:163], v14 offset0:176 offset1:177\n"
"v_mfma_f32_16x16x32_bf16 v[140:143], v[104:107], v[168:171], v[140:143]\n"
"ds_read2st64_b32 v[166:167], v14 offset0:200 offset1:201\n"
"v_mul_f32 v152, v152, v8\n"
"v_mul_f32 v153, v153, v8\n"
"v_mul_f32 v157, v157, v8\n"
"v_mfma_f32_16x16x32_bf16 v[148:151], v[108:111], v[168:171], v[148:151]\n"
"s_waitcnt lgkmcnt(4)\n"
"ds_read2st64_b32 v[164:165], v14 offset0:198 offset1:199\n"
"v_mul_f32 v154, v154, v8\n"
"v_mfma_f32_16x16x32_bf16 v[172:175], v[136:139], v[168:171], v[172:175]\n"
"s_waitcnt lgkmcnt(2)\n"
"v_mul_f32 v155, v155, v8\n"
"v_cvt_pk_bf16_f32 v152, v152, v153\n"
"v_cvt_pk_bf16_f32 v153, v154, v155\n"
"v_mfma_f32_16x16x32_bf16 v[140:143], v[0:3], v[160:163], v[140:143]\n"
"ds_read_b128 v[136:139], v17 offset:56320\n"
"v_mul_f32 v156, v156, v8\n"
"v_mfma_f32_16x16x32_bf16 v[148:151], v[112:115], v[160:163], v[148:151]\n"
"v_cvt_pk_bf16_f32 v154, v156, v157\n"
"v_mul_f32 v159, v159, v8\n"
"ds_read2st64_b32 v[156:157], v14 offset0:130 offset1:131\n"
"v_mfma_f32_16x16x32_bf16 v[172:175], v[144:147], v[160:163], v[172:175]\n"
"s_waitcnt lgkmcnt(2)\n"
"v_mul_f32 v158, v158, v8\n"
"v_cvt_pk_bf16_f32 v155, v158, v159\n"
"v_mfma_f32_16x16x32_bf16 v[140:143], v[116:119], v[164:167], v[140:143]\n"
"ds_read2st64_b32 v[158:159], v14 offset0:154 offset1:155\n"
"v_mfma_f32_16x16x32_bf16 v[148:151], v[120:123], v[164:167], v[148:151]\n"
"s_waitcnt lgkmcnt(2)\n"
"ds_read2st64_b32 v[162:163], v14 offset0:202 offset1:203\n"
"ds_read_b128 v[144:147], v17 offset:57344\n"
"ds_read2st64_b32 v[160:161], v14 offset0:178 offset1:179\n"
"v_mfma_f32_16x16x32_bf16 v[172:175], v[136:139], v[164:167], v[172:175]\n"
"s_waitcnt lgkmcnt(3)\n"
"s_setprio 0\n"
"v_mfma_f32_16x16x32_bf16 v[140:143], v[4:7], v[156:159], v[140:143]\n"
"s_mov_b32 m0, s54\n"
"ds_read_b128 v[164:167], v12 offset:24576\n"
"v_mfma_f32_16x16x32_bf16 v[148:151], v[124:127], v[156:159], v[148:151]\n"
"s_waitcnt lgkmcnt(2)\n"
"ds_read_b128 v[136:139], v17 offset:58368\n"
"v_mfma_f32_16x16x32_bf16 v[172:175], v[144:147], v[156:159], v[172:175]\n"
"s_waitcnt lgkmcnt(2)\n"
"v_mfma_f32_16x16x32_bf16 v[140:143], v[128:131], v[160:163], v[140:143]\n"
"s_barrier\n"
"v_mfma_f32_16x16x32_bf16 v[148:151], v[132:135], v[160:163], v[148:151]\n"
"s_waitcnt lgkmcnt(0)\n"
"v_mfma_f32_16x16x32_bf16 v[172:175], v[136:139], v[160:163], v[172:175]\n"
"ds_read_b128 v[156:159], v12 offset:26624\n"
"v_mfma_f32_16x16x32_bf16 v[136:139], v[164:167], v[28:31], 0\n"
"ds_read_b128 v[144:147], v13 offset:24576\n"
"s_nop 0\n"
"v_cmp_class_f32_e64 vcc, v140, s64\n"
"v_cvt_pk_bf16_f32 v140, v140, v141\n"
"v_cvt_pk_bf16_f32 v141, v142, v143\n"
"v_cvt_pk_bf16_f32 v142, v148, v149\n"
"ds_write_addtid_b32 v246 offset:33280\n"
"v_mfma_f32_16x16x32_bf16 v[160:163], v[164:167], v[32:35], 0\n"
"v_mfma_f32_16x16x32_bf16 v[168:171], v[164:167], v[24:27], 0\n"
"v_cvt_pk_bf16_f32 v143, v150, v151\n"
"ds_write_addtid_b32 v154 offset:32768\n"
"s_waitcnt lgkmcnt(2)\n"
"v_mfma_f32_16x16x32_bf16 v[136:139], v[144:147], v[40:43], v[136:139]\n"
"v_cvt_pk_bf16_f32 v172, v172, v173\n"
"v_mfma_f32_16x16x32_bf16 v[160:163], v[144:147], v[44:47], v[160:163]\n"
"ds_write_addtid_b32 v155 offset:33024\n"
"v_mfma_f32_16x16x32_bf16 v[168:171], v[144:147], v[36:39], v[168:171]\n"
"ds_read_b128 v[144:147], v12 offset:28672\n"
"v_mfma_f32_16x16x32_bf16 v[136:139], v[156:159], v[48:51], v[136:139]\n"
"ds_read_b128 v[164:167], v13 offset:26624\n"
"v_mfma_f32_16x16x32_bf16 v[160:163], v[156:159], v[52:55], v[160:163]\n"
"ds_write_addtid_b32 v153 offset:32512\n"
"ds_write_addtid_b32 v152 offset:32256\n"
"ds_write_addtid_b32 v247 offset:33536\n"
"v_cvt_pk_bf16_f32 v173, v174, v175\n"
"v_mfma_f32_16x16x32_bf16 v[168:171], v[156:159], v[56:59], v[168:171]\n"
"s_mov_b32 m0, s68\n"
"s_nop 0\n"
"ds_write_addtid_b32 v172 offset:46080\n"
"ds_write_addtid_b32 v173 offset:46336\n"
"s_waitcnt lgkmcnt(5)\n"
"v_mfma_f32_16x16x32_bf16 v[136:139], v[164:167], v[60:63], v[136:139]\n"
"ds_write_addtid_b32 v143 offset:45824\n"
"v_mfma_f32_16x16x32_bf16 v[160:163], v[164:167], v[64:67], v[160:163]\n"
"v_mfma_f32_16x16x32_bf16 v[168:171], v[164:167], v[68:71], v[168:171]\n"
"ds_read_b128 v[148:151], v13 offset:28672\n"
"v_mfma_f32_16x16x32_bf16 v[136:139], v[144:147], v[72:75], v[136:139]\n"
"ds_write_addtid_b32 v142 offset:45568\n"
"ds_write_addtid_b32 v140 offset:45056\n"
"v_mfma_f32_16x16x32_bf16 v[160:163], v[144:147], v[76:79], v[160:163]\n"
"ds_write_addtid_b32 v141 offset:45312\n"
"v_mov_b32 v140, 0x3f803f80\n"
"s_or_b64 s[62:63], s[62:63], vcc\n"
"v_mfma_f32_16x16x32_bf16 v[168:171], v[144:147], v[80:83], v[168:171]\n"
"s_waitcnt lgkmcnt(3)\n"
"v_mfma_f32_16x16x32_bf16 v[136:139], v[148:151], v[84:87], v[136:139]\n"
"v_mfma_f32_16x16x32_bf16 v[160:163], v[148:151], v[88:91], v[160:163]\n"
"v_mfma_f32_16x16x32_bf16 v[168:171], v[148:151], v[92:95], v[168:171]\n"
"s_waitcnt lgkmcnt(0)\n"
"s_waitcnt vmcnt(5)\n"
"s_barrier\n"
"ds_read2st64_b32 v[146:147], v9 offset0:114 offset1:115\n"
"ds_read2st64_b32 v[144:145], v9 offset0:112 offset1:113\n"
"v_mov_b32 v142, 0x3f803f80\n"
"s_nop 0\n"
"v_cvt_pk_bf16_f32 v221, v162, v163\n"
"v_cvt_pk_bf16_f32 v195, v138, v139\n"
"v_mov_b32 v143, 0x3f803f80\n"
"v_cvt_pk_bf16_f32 v194, v136, v137\n"
"ds_read2st64_b32 v[150:151], v9 offset0:118 offset1:119\n"
"v_cvt_pk_bf16_f32 v247, v170, v171\n"
"ds_read_u16 v8, v21 offset:928\n"
"v_mov_b32 v141, 0x3f803f80\n"
"v_cvt_pk_bf16_f32 v220, v160, v161\n"
"s_waitcnt lgkmcnt(2)\n"
"v_mfma_f32_16x16x32_bf16 v[136:139], v[140:143], v[144:147], 0\n"
"ds_read2st64_b32 v[148:149], v9 offset0:116 offset1:117\n"
"v_mfma_f32_16x16x32_bf16 v[152:155], v[180:183], v[144:147], 0\n"
"ds_read2st64_b32 v[166:167], v9 offset0:130 offset1:131\n"
"v_mfma_f32_16x16x32_bf16 v[156:159], v[206:209], v[144:147], 0\n"
"s_and_b32 s41, 416, s55\n"
"v_add_u32 v11, s41, v15\n"
"v_cvt_pk_bf16_f32 v246, v168, v169\n"
"v_mfma_f32_16x16x32_bf16 v[160:163], v[232:235], v[144:147], 0\n"
"s_waitcnt lgkmcnt(1)\n"
"ds_read2st64_b32 v[146:147], v20 offset0:2 offset1:3\n"
"v_mfma_f32_16x16x32_bf16 v[136:139], v[140:143], v[148:151], v[136:139]\n"
"ds_read2st64_b32 v[164:165], v9 offset0:128 offset1:129\n"
"v_mad_u32_u24 v10, v8, s40, v18\n"
"v_mfma_f32_16x16x32_bf16 v[152:155], v[190:193], v[148:151], v[152:155]\n"
"ds_read2st64_b32 v[144:145], v20 offset0:0 offset1:1\n"
"ds_read_u16 v11, v11 offset:704\n"
"v_mfma_f32_16x16x32_bf16 v[156:159], v[216:219], v[148:151], v[156:159]\n"
"ds_read2st64_b32 v[174:175], v20 offset0:4 offset1:5\n"
"v_mad_u32_u24 v8, v8, s40, v19\n"
"v_mfma_f32_16x16x32_bf16 v[160:163], v[242:245], v[148:151], v[160:163]\n"
"s_waitcnt lgkmcnt(2)\n"
"global_store_dwordx4 v10, v[144:147], s[66:67] sc1\n"
"v_mfma_f32_16x16x32_bf16 v[136:139], v[140:143], v[164:167], v[136:139]\n"
"ds_read2st64_b32 v[148:149], v9 offset0:132 offset1:133\n"
"ds_read2st64_b32 v[146:147], v9 offset0:36 offset1:37\n"
"v_mfma_f32_16x16x32_bf16 v[152:155], v[194:197], v[164:167], v[152:155]\n"
"s_waitcnt lgkmcnt(3)\n"
"v_mad_u32_u24 v11, v11, s40, v16\n"
"s_add_u32 m0, s51, 126976\n"
"s_nop 0\n"
"global_load_lds_dwordx4 v11, s[56:57]\n"
"ds_read2st64_b32 v[150:151], v9 offset0:134 offset1:135\n"
"v_mfma_f32_16x16x32_bf16 v[156:159], v[220:223], v[164:167], v[156:159]\n"
"s_waitcnt lgkmcnt(3)\n"
"v_mov_b32 v144, 0\n"
"v_mov_b32 v145, 0\n"
"global_store_dwordx2 v8, v[174:175], s[66:67] offset:256 sc1\n"
"v_mfma_f32_16x16x32_bf16 v[160:163], v[246:249], v[164:167], v[160:163]\n"
"s_waitcnt lgkmcnt(0)\n"
"v_mfma_f32_16x16x32_bf16 v[136:139], v[140:143], v[148:151], v[136:139]\n"
"v_mfma_f32_16x16x32_bf16 v[136:139], v[140:143], v[144:147], v[136:139]\n"
"s_add_u32 m0, s51, 129024\n"
"s_nop 0\n"
"global_load_lds_dwordx4 v11, s[58:59]\n"
"ds_read_b128 v[164:167], v17 offset:53248\n"
"v_mfma_f32_16x16x32_bf16 v[160:163], v[250:253], v[148:151], v[160:163]\n"
"s_setprio 2\n"
"ds_read2st64_b32 v[142:143], v14 offset0:128 offset1:129\n"
"v_mfma_f32_16x16x32_bf16 v[160:163], v[234:237], v[144:147], v[160:163]\n"
"v_rcp_f32 v8, v136\n"
"v_mfma_f32_16x16x32_bf16 v[152:155], v[198:201], v[148:151], v[152:155]\n"
"ds_read2st64_b32 v[140:141], v14 offset0:126 offset1:127\n"
"v_mfma_f32_16x16x32_bf16 v[152:155], v[182:185], v[144:147], v[152:155]\n"
"ds_read2st64_b32 v[168:169], v14 offset0:150 offset1:151\n"
"v_add_u32 v136, 0xd4800000, v136\n"
"v_mfma_f32_16x16x32_bf16 v[156:159], v[224:227], v[148:151], v[156:159]\n"
"ds_read2st64_b32 v[170:171], v14 offset0:152 offset1:153\n"
"v_mul_f32 v161, v161, v8\n"
"v_mul_f32 v160, v160, v8\n"
"v_mul_f32 v163, v163, v8\n"
"v_mul_f32 v162, v162, v8\n"
"v_cmp_lt_u32 vcc, 0x28000000, v136\n"
"v_mfma_f32_16x16x32_bf16 v[156:159], v[208:211], v[144:147], v[156:159]\n"
"s_waitcnt lgkmcnt(2)\n"
"ds_read_b128 v[136:139], v17 offset:54272\n"
"v_cvt_pk_bf16_f32 v248, v160, v161\n"
"v_mfma_f32_16x16x32_bf16 v[144:147], v[96:99], v[140:143], 0\n"
"s_add_u32 m0, s51, 131072\n"
"s_nop 0\n"
"global_load_lds_dwordx4 v11, s[60:61]\n"
"v_mfma_f32_16x16x32_bf16 v[148:151], v[100:103], v[140:143], 0\n"
"v_mul_f32 v156, v156, v8\n"
"v_mul_f32 v153, v153, v8\n"
"ds_read2st64_b32 v[160:161], v14 offset0:174 offset1:175\n"
"v_cvt_pk_bf16_f32 v249, v162, v163\n"
"v_mfma_f32_16x16x32_bf16 v[172:175], v[164:167], v[140:143], 0\n"
"s_waitcnt lgkmcnt(2)\n"
"ds_read2st64_b32 v[162:163], v14 offset0:176 offset1:177\n"
"v_mfma_f32_16x16x32_bf16 v[144:147], v[104:107], v[168:171], v[144:147]\n"
"ds_read_b128 v[140:143], v17 offset:55296\n"
"v_mul_f32 v152, v152, v8\n"
"v_mul_f32 v154, v154, v8\n"
"v_mul_f32 v158, v158, v8\n"
"v_mfma_f32_16x16x32_bf16 v[148:151], v[108:111], v[168:171], v[148:151]\n"
"s_waitcnt lgkmcnt(3)\n"
"ds_read2st64_b32 v[166:167], v14 offset0:200 offset1:201\n"
"ds_read2st64_b32 v[164:165], v14 offset0:198 offset1:199\n"
"v_mfma_f32_16x16x32_bf16 v[172:175], v[136:139], v[168:171], v[172:175]\n"
"s_waitcnt lgkmcnt(3)\n"
"ds_read_b128 v[136:139], v17 offset:56320\n"
"v_cvt_pk_bf16_f32 v152, v152, v153\n"
"v_mfma_f32_16x16x32_bf16 v[144:147], v[0:3], v[160:163], v[144:147]\n"
"v_mul_f32 v155, v155, v8\n"
"v_cvt_pk_bf16_f32 v153, v154, v155\n"
"s_or_b64 s[62:63], s[62:63], vcc\n"
"v_mul_f32 v157, v157, v8\n"
"v_cvt_pk_bf16_f32 v154, v156, v157\n"
"v_mfma_f32_16x16x32_bf16 v[148:151], v[112:115], v[160:163], v[148:151]\n"
"s_waitcnt lgkmcnt(3)\n"
"ds_read2st64_b32 v[156:157], v14 offset0:130 offset1:131\n"
"v_mul_f32 v159, v159, v8\n"
"v_cvt_pk_bf16_f32 v155, v158, v159\n"
"v_mfma_f32_16x16x32_bf16 v[172:175], v[140:143], v[160:163], v[172:175]\n"
"s_waitcnt lgkmcnt(2)\n"
"s_setprio 0\n"
"ds_read2st64_b32 v[158:159], v14 offset0:154 offset1:155\n"
"ds_read_b128 v[140:143], v17 offset:57344\n"
"v_mfma_f32_16x16x32_bf16 v[144:147], v[116:119], v[164:167], v[144:147]\n"
"ds_read2st64_b32 v[162:163], v14 offset0:202 offset1:203\n"
"ds_read2st64_b32 v[160:161], v14 offset0:178 offset1:179\n"
"v_mfma_f32_16x16x32_bf16 v[148:151], v[120:123], v[164:167], v[148:151]\n"
"s_waitcnt lgkmcnt(5)\n"
"v_mfma_f32_16x16x32_bf16 v[172:175], v[136:139], v[164:167], v[172:175]\n"
"s_waitcnt lgkmcnt(3)\n"
"ds_read_b128 v[136:139], v17 offset:58368\n"
"v_mfma_f32_16x16x32_bf16 v[144:147], v[4:7], v[156:159], v[144:147]\n"
"v_mfma_f32_16x16x32_bf16 v[148:151], v[124:127], v[156:159], v[148:151]\n"
"s_waitcnt lgkmcnt(3)\n"
"v_mfma_f32_16x16x32_bf16 v[172:175], v[140:143], v[156:159], v[172:175]\n"
"s_waitcnt lgkmcnt(1)\n"
"ds_read_b128 v[164:167], v12 offset:12288\n"
"v_mfma_f32_16x16x32_bf16 v[144:147], v[128:131], v[160:163], v[144:147]\n"
"ds_read_b128 v[156:159], v12 offset:14336\n"
"s_mov_b32 m0, s54\n"
"v_mfma_f32_16x16x32_bf16 v[148:151], v[132:135], v[160:163], v[148:151]\n"
"s_barrier\n"
"s_nop 3\n"
"v_cmp_class_f32_e64 vcc, v144, s64\n"
"v_cvt_pk_bf16_f32 v144, v144, v145\n"
"ds_read_b128 v[140:143], v13 offset:12288\n"
"s_waitcnt lgkmcnt(3)\n"
"v_mfma_f32_16x16x32_bf16 v[172:175], v[136:139], v[160:163], v[172:175]\n"
"ds_write_addtid_b32 v248 offset:33280\n"
"v_cvt_pk_bf16_f32 v145, v146, v147\n"
"v_cvt_pk_bf16_f32 v147, v150, v151\n"
"s_waitcnt lgkmcnt(3)\n"
"v_mfma_f32_16x16x32_bf16 v[136:139], v[164:167], v[28:31], 0\n"
"v_mfma_f32_16x16x32_bf16 v[160:163], v[164:167], v[32:35], 0\n"
"v_mfma_f32_16x16x32_bf16 v[168:171], v[164:167], v[24:27], 0\n"
"ds_read_b128 v[164:167], v13 offset:14336\n"
"ds_write_addtid_b32 v249 offset:33536\n"
"ds_write_addtid_b32 v152 offset:32256\n"
"s_waitcnt lgkmcnt(4)\n"
"v_mfma_f32_16x16x32_bf16 v[136:139], v[140:143], v[40:43], v[136:139]\n"
"v_cvt_pk_bf16_f32 v146, v148, v149\n"
"v_mfma_f32_16x16x32_bf16 v[160:163], v[140:143], v[44:47], v[160:163]\n"
"v_cvt_pk_bf16_f32 v172, v172, v173\n"
"v_mfma_f32_16x16x32_bf16 v[168:171], v[140:143], v[36:39], v[168:171]\n"
"ds_write_addtid_b32 v153 offset:32512\n"
"s_or_b64 s[62:63], s[62:63], vcc\n"
"ds_read_b128 v[140:143], v12 offset:16384\n"
"v_mfma_f32_16x16x32_bf16 v[136:139], v[156:159], v[48:51], v[136:139]\n"
"v_cvt_pk_bf16_f32 v173, v174, v175\n"
"v_mfma_f32_16x16x32_bf16 v[160:163], v[156:159], v[52:55], v[160:163]\n"
"ds_read_b128 v[148:151], v13 offset:16384\n"
"ds_write_addtid_b32 v154 offset:32768\n"
"v_mfma_f32_16x16x32_bf16 v[168:171], v[156:159], v[56:59], v[168:171]\n"
"ds_write_addtid_b32 v155 offset:33024\n"
"s_waitcnt lgkmcnt(7)\n"
"v_mfma_f32_16x16x32_bf16 v[136:139], v[164:167], v[60:63], v[136:139]\n"
"s_mov_b32 m0, s68\n"
"s_nop 0\n"
"ds_write_addtid_b32 v172 offset:46080\n"
"v_mfma_f32_16x16x32_bf16 v[160:163], v[164:167], v[64:67], v[160:163]\n"
"ds_write_addtid_b32 v146 offset:45568\n"
"v_mfma_f32_16x16x32_bf16 v[168:171], v[164:167], v[68:71], v[168:171]\n"
"ds_write_addtid_b32 v145 offset:45312\n"
"s_waitcnt lgkmcnt(6)\n"
"v_mfma_f32_16x16x32_bf16 v[136:139], v[140:143], v[72:75], v[136:139]\n"
"ds_write_addtid_b32 v173 offset:46336\n"
"v_mfma_f32_16x16x32_bf16 v[160:163], v[140:143], v[76:79], v[160:163]\n"
"v_mfma_f32_16x16x32_bf16 v[168:171], v[140:143], v[80:83], v[168:171]\n"
"ds_write_addtid_b32 v147 offset:45824\n"
"s_waitcnt lgkmcnt(7)\n"
"v_mfma_f32_16x16x32_bf16 v[136:139], v[148:151], v[84:87], v[136:139]\n"
"ds_write_addtid_b32 v144 offset:45056\n"
"v_mfma_f32_16x16x32_bf16 v[160:163], v[148:151], v[88:91], v[160:163]\n"
"v_mfma_f32_16x16x32_bf16 v[168:171], v[148:151], v[92:95], v[168:171]\n"
"s_nop 4\n"
"v_cvt_pk_bf16_f32 v196, v136, v137\n"
"s_waitcnt lgkmcnt(0)\n"
"s_waitcnt vmcnt(5)\n"
"s_barrier\n"
"v_cvt_pk_bf16_f32 v222, v160, v161\n"
"ds_read2st64_b32 v[146:147], v9 offset0:122 offset1:123\n"
"v_mov_b32 v142, 0x3f803f80\n"
"ds_read2st64_b32 v[144:145], v9 offset0:120 offset1:121\n"
"v_mov_b32 v143, 0x3f803f80\n"
"v_mov_b32 v141, 0x3f803f80\n"
"v_mov_b32 v140, 0x3f803f80\n"
"v_cvt_pk_bf16_f32 v223, v162, v163\n"
"v_cvt_pk_bf16_f32 v197, v138, v139\n"
"v_mov_b32 v148, 0\n"
"ds_read_b32 v149, v9 offset:32000\n"
"ds_read2st64_b32 v[150:151], v9 offset0:126 offset1:127\n"
"s_waitcnt lgkmcnt(2)\n"
"v_mfma_f32_16x16x32_bf16 v[136:139], v[140:143], v[144:147], 0\n"
"v_mfma_f32_16x16x32_bf16 v[152:155], v[200:203], v[144:147], 0\n"
"ds_read_u16 v8, v21 offset:960\n"
"s_setprio 2\n"
"v_mfma_f32_16x16x32_bf16 v[200:203], v[226:229], v[144:147], 0\n"
"s_and_b32 s41, 416, s55\n"
"v_add_u32 v229, s41, v15\n"
"v_cvt_pk_bf16_f32 v249, v170, v171\n"
"v_mfma_f32_16x16x32_bf16 v[156:159], v[252:255], v[144:147], 0\n"
"s_waitcnt lgkmcnt(1)\n"
"ds_read2st64_b32 v[252:253], v20 offset0:0 offset1:1\n"
"ds_read2st64_b32 v[254:255], v20 offset0:2 offset1:3\n"
"v_mfma_f32_16x16x32_bf16 v[136:139], v[140:143], v[148:151], v[136:139]\n"
"v_mov_b32 v144, 0\n"
"ds_read_b32 v145, v9 offset:33024\n"
"v_mfma_f32_16x16x32_bf16 v[152:155], v[182:185], v[148:151], v[152:155]\n"
"ds_read_u16 v229, v229 offset:736\n"
"v_cvt_pk_bf16_f32 v248, v168, v169\n"
"v_mfma_f32_16x16x32_bf16 v[200:203], v[208:211], v[148:151], v[200:203]\n"
"s_waitcnt lgkmcnt(4)\n"
"ds_read2st64_b32 v[146:147], v9 offset0:130 offset1:131\n"
"v_mad_u32_u24 v228, v8, s40, v18\n"
"s_waitcnt lgkmcnt(3)\n"
"global_store_dwordx4 v228, v[252:255], s[66:67] sc1\n"
"v_mad_u32_u24 v8, v8, s40, v19\n"
"ds_read2st64_b32 v[226:227], v20 offset0:4 offset1:5\n"
"v_mfma_f32_16x16x32_bf16 v[156:159], v[234:237], v[148:151], v[156:159]\n"
"s_waitcnt lgkmcnt(2)\n"
"v_mad_u32_u24 v229, v229, s40, v16\n"
"s_waitcnt lgkmcnt(1)\n"
"v_mfma_f32_16x16x32_bf16 v[136:139], v[140:143], v[144:147], v[136:139]\n"
"ds_read2st64_b32 v[254:255], v9 offset0:38 offset1:39\n"
"s_add_u32 m0, s51, 114688\n"
"s_nop 0\n"
"global_load_lds_dwordx4 v229, s[56:57]\n"
"ds_read2st64_b32 v[148:149], v9 offset0:132 offset1:133\n"
"ds_read_b32 v151, v9 offset:34560\n"
"v_mfma_f32_16x16x32_bf16 v[152:155], v[192:195], v[144:147], v[152:155]\n"
"v_mov_b32 v253, 0\n"
"v_mov_b32 v150, 0\n"
"v_mfma_f32_16x16x32_bf16 v[200:203], v[218:221], v[144:147], v[200:203]\n"
"s_waitcnt lgkmcnt(3)\n"
"global_store_dwordx2 v8, v[226:227], s[66:67] offset:256 sc1\n"
"v_mfma_f32_16x16x32_bf16 v[156:159], v[244:247], v[144:147], v[156:159]\n"
"s_waitcnt lgkmcnt(0)\n"
"ds_read_b128 v[144:147], v17 offset:53248\n"
"v_mov_b32 v252, 0\n"
"v_mfma_f32_16x16x32_bf16 v[136:139], v[140:143], v[148:151], v[136:139]\n"
"s_nop 0\n"
"v_mfma_f32_16x16x32_bf16 v[136:139], v[140:143], v[252:255], v[136:139]\n"
"ds_read2st64_b32 v[140:141], v14 offset0:126 offset1:127\n"
"s_add_u32 m0, s51, 116736\n"
"s_nop 0\n"
"global_load_lds_dwordx4 v229, s[58:59]\n"
"ds_read2st64_b32 v[142:143], v14 offset0:128 offset1:129\n"
"v_mfma_f32_16x16x32_bf16 v[156:159], v[248:251], v[148:151], v[156:159]\n"
"ds_read2st64_b32 v[160:161], v14 offset0:150 offset1:151\n"
"v_mfma_f32_16x16x32_bf16 v[156:159], v[236:239], v[252:255], v[156:159]\n"
"ds_read2st64_b32 v[162:163], v14 offset0:152 offset1:153\n"
"v_mfma_f32_16x16x32_bf16 v[152:155], v[196:199], v[148:151], v[152:155]\n"
"v_rcp_f32 v8, v136\n"
"v_add_u32 v136, 0xd4800000, v136\n"
"v_cmp_lt_u32 vcc, 0x28000000, v136\n"
"v_mfma_f32_16x16x32_bf16 v[152:155], v[184:187], v[252:255], v[152:155]\n"
"v_mfma_f32_16x16x32_bf16 v[200:203], v[222:225], v[148:151], v[200:203]\n"
"ds_read2st64_b32 v[148:149], v14 offset0:174 offset1:175\n"
"ds_read_b128 v[224:227], v17 offset:54272\n"
"v_mul_f32 v156, v156, v8\n"
"v_mfma_f32_16x16x32_bf16 v[200:203], v[210:213], v[252:255], v[200:203]\n"
"s_or_b64 s[62:63], s[62:63], vcc\n"
"v_mul_f32 v157, v157, v8\n"
"ds_read2st64_b32 v[150:151], v14 offset0:176 offset1:177\n"
"s_waitcnt lgkmcnt(5)\n"
"v_mfma_f32_16x16x32_bf16 v[252:255], v[96:99], v[140:143], 0\n"
"v_cvt_pk_bf16_f32 v250, v156, v157\n"
"s_add_u32 m0, s51, 118784\n"
"s_nop 0\n"
"global_load_lds_dwordx4 v229, s[60:61]\n"
"v_mul_f32 v158, v158, v8\n"
"v_mfma_f32_16x16x32_bf16 v[136:139], v[100:103], v[140:143], 0\n"
"v_mul_f32 v159, v159, v8\n"
"v_cvt_pk_bf16_f32 v251, v158, v159\n"
"v_mfma_f32_16x16x32_bf16 v[156:159], v[144:147], v[140:143], 0\n"
"s_waitcnt lgkmcnt(3)\n"
"ds_read2st64_b32 v[144:145], v14 offset0:198 offset1:199\n"
"v_mul_f32 v152, v152, v8\n"
"v_mfma_f32_16x16x32_bf16 v[252:255], v[104:107], v[160:163], v[252:255]\n"
"v_mul_f32 v201, v201, v8\n"
"v_mul_f32 v153, v153, v8\n"
"ds_read_b128 v[140:143], v17 offset:55296\n"
"v_mul_f32 v154, v154, v8\n"
"v_mfma_f32_16x16x32_bf16 v[136:139], v[108:111], v[160:163], v[136:139]\n"
"s_waitcnt lgkmcnt(3)\n"
"ds_read2st64_b32 v[146:147], v14 offset0:200 offset1:201\n"
"v_cvt_pk_bf16_f32 v152, v152, v153\n"
"v_mfma_f32_16x16x32_bf16 v[156:159], v[224:227], v[160:163], v[156:159]\n"
"s_waitcnt lgkmcnt(3)\n"
"v_mul_f32 v155, v155, v8\n"
"v_cvt_pk_bf16_f32 v153, v154, v155\n"
"v_mfma_f32_16x16x32_bf16 v[252:255], v[0:3], v[148:151], v[252:255]\n"
"ds_read2st64_b32 v[224:225], v14 offset0:130 offset1:131\n"
"v_mul_f32 v200, v200, v8\n"
"v_cvt_pk_bf16_f32 v154, v200, v201\n"
"v_mul_f32 v203, v203, v8\n"
"v_mfma_f32_16x16x32_bf16 v[136:139], v[112:115], v[148:151], v[136:139]\n"
"s_waitcnt lgkmcnt(2)\n"
"ds_read_b128 v[198:201], v17 offset:56320\n"
"ds_read2st64_b32 v[226:227], v14 offset0:154 offset1:155\n"
"v_mfma_f32_16x16x32_bf16 v[156:159], v[140:143], v[148:151], v[156:159]\n"
"s_waitcnt lgkmcnt(3)\n"
"ds_read_b128 v[140:143], v17 offset:57344\n"
"v_mul_f32 v202, v202, v8\n"
"v_cvt_pk_bf16_f32 v155, v202, v203\n"
"v_mfma_f32_16x16x32_bf16 v[252:255], v[116:119], v[144:147], v[252:255]\n"
"s_setprio 0\n"
"ds_read2st64_b32 v[148:149], v14 offset0:178 offset1:179\n"
"v_mfma_f32_16x16x32_bf16 v[136:139], v[120:123], v[144:147], v[136:139]\n"
"s_waitcnt lgkmcnt(3)\n"
"ds_read2st64_b32 v[150:151], v14 offset0:202 offset1:203\n"
"v_mfma_f32_16x16x32_bf16 v[156:159], v[198:201], v[144:147], v[156:159]\n"
"s_waitcnt lgkmcnt(3)\n"
"ds_read_b128 v[144:147], v12 offset:43008\n"
"v_mfma_f32_16x16x32_bf16 v[252:255], v[4:7], v[224:227], v[252:255]\n"
"v_mfma_f32_16x16x32_bf16 v[136:139], v[124:127], v[224:227], v[136:139]\n"
"s_mov_b32 m0, s54\n"
"s_waitcnt lgkmcnt(3)\n"
"v_mfma_f32_16x16x32_bf16 v[156:159], v[140:143], v[224:227], v[156:159]\n"
"s_waitcnt lgkmcnt(1)\n"
"ds_read_b128 v[198:201], v17 offset:58368\n"
"v_mfma_f32_16x16x32_bf16 v[252:255], v[128:131], v[148:151], v[252:255]\n"
"v_mfma_f32_16x16x32_bf16 v[136:139], v[132:135], v[148:151], v[136:139]\n"
"s_waitcnt vmcnt(5)\n"
"s_barrier\n"
"ds_write_addtid_b32 v251 offset:33536\n"
"ds_read_b128 v[224:227], v13 offset:43008\n"
"s_waitcnt lgkmcnt(2)\n"
"v_mfma_f32_16x16x32_bf16 v[156:159], v[198:201], v[148:151], v[156:159]\n"
"s_nop 0\n"
"v_cmp_class_f32_e64 vcc, v252, s64\n"
"v_cvt_pk_bf16_f32 v252, v252, v253\n"
"v_cvt_pk_bf16_f32 v253, v254, v255\n"
"v_cvt_pk_bf16_f32 v255, v138, v139\n"
"ds_read_b128 v[140:143], v12 offset:45056\n"
"v_cvt_pk_bf16_f32 v254, v136, v137\n"
"v_mfma_f32_16x16x32_bf16 v[200:203], v[144:147], v[28:31], 0\n"
"v_mfma_f32_16x16x32_bf16 v[148:151], v[144:147], v[32:35], 0\n"
"ds_write_addtid_b32 v250 offset:33280\n"
"v_mfma_f32_16x16x32_bf16 v[160:163], v[144:147], v[24:27], 0\n"
"s_waitcnt lgkmcnt(2)\n"
"ds_read_b128 v[144:147], v13 offset:45056\n"
"v_mfma_f32_16x16x32_bf16 v[200:203], v[224:227], v[40:43], v[200:203]\n"
"v_mfma_f32_16x16x32_bf16 v[148:151], v[224:227], v[44:47], v[148:151]\n"
"ds_write_addtid_b32 v152 offset:32256\n"
"v_mfma_f32_16x16x32_bf16 v[160:163], v[224:227], v[36:39], v[160:163]\n"
"s_waitcnt lgkmcnt(3)\n"
"v_cvt_pk_bf16_f32 v156, v156, v157\n"
"ds_read_b128 v[136:139], v13 offset:47104\n"
"ds_read_b128 v[224:227], v12 offset:47104\n"
"v_mfma_f32_16x16x32_bf16 v[200:203], v[140:143], v[48:51], v[200:203]\n"
"ds_write_addtid_b32 v154 offset:32768\n"
"v_mfma_f32_16x16x32_bf16 v[148:151], v[140:143], v[52:55], v[148:151]\n"
"ds_write_addtid_b32 v155 offset:33024\n"
"v_mfma_f32_16x16x32_bf16 v[160:163], v[140:143], v[56:59], v[160:163]\n"
"s_waitcnt lgkmcnt(5)\n"
"v_mfma_f32_16x16x32_bf16 v[200:203], v[144:147], v[60:63], v[200:203]\n"
"ds_write_addtid_b32 v153 offset:32512\n"
"s_mov_b32 m0, s68\n"
"v_mfma_f32_16x16x32_bf16 v[148:151], v[144:147], v[64:67], v[148:151]\n"
"v_cvt_pk_bf16_f32 v157, v158, v159\n"
"v_mfma_f32_16x16x32_bf16 v[160:163], v[144:147], v[68:71], v[160:163]\n"
"s_or_b64 s[62:63], s[62:63], vcc\n"
"ds_write_addtid_b32 v252 offset:45056\n"
"s_waitcnt lgkmcnt(4)\n"
"v_mfma_f32_16x16x32_bf16 v[200:203], v[224:227], v[72:75], v[200:203]\n"
"ds_write_addtid_b32 v254 offset:45568\n"
"ds_write_addtid_b32 v253 offset:45312\n"
"v_mfma_f32_16x16x32_bf16 v[148:151], v[224:227], v[76:79], v[148:151]\n"
"ds_write_addtid_b32 v156 offset:46080\n"
"v_mfma_f32_16x16x32_bf16 v[160:163], v[224:227], v[80:83], v[160:163]\n"
"ds_read_b128 v[250:253], v12 offset:49152\n"
"v_mfma_f32_16x16x32_bf16 v[200:203], v[136:139], v[84:87], v[200:203]\n"
"ds_write_addtid_b32 v157 offset:46336\n"
"ds_read_b128 v[226:229], v13 offset:49152\n"
"v_mfma_f32_16x16x32_bf16 v[148:151], v[136:139], v[88:91], v[148:151]\n"
"ds_read_b128 v[140:143], v12 offset:51200\n"
"v_mfma_f32_16x16x32_bf16 v[160:163], v[136:139], v[92:95], v[160:163]\n"
"s_waitcnt lgkmcnt(3)\n"
"v_mfma_f32_16x16x32_bf16 v[136:139], v[250:253], v[28:31], 0\n"
"ds_write_addtid_b32 v255 offset:45824\n"
"v_mfma_f32_16x16x32_bf16 v[144:147], v[250:253], v[32:35], 0\n"
"v_mfma_f32_16x16x32_bf16 v[152:155], v[250:253], v[24:27], 0\n"
"s_waitcnt lgkmcnt(2)\n"
"ds_read_b128 v[252:255], v13 offset:51200\n"
"v_mfma_f32_16x16x32_bf16 v[136:139], v[226:229], v[40:43], v[136:139]\n"
"v_mfma_f32_16x16x32_bf16 v[144:147], v[226:229], v[44:47], v[144:147]\n"
"v_cvt_pk_bf16_f32 v250, v160, v161\n"
"v_mfma_f32_16x16x32_bf16 v[152:155], v[226:229], v[36:39], v[152:155]\n"
"s_waitcnt lgkmcnt(2)\n"
"v_cvt_pk_bf16_f32 v198, v200, v201\n"
"v_mfma_f32_16x16x32_bf16 v[136:139], v[140:143], v[48:51], v[136:139]\n"
"ds_read_b128 v[226:229], v13 offset:53248\n"
"v_cvt_pk_bf16_f32 v199, v202, v203\n"
"v_mfma_f32_16x16x32_bf16 v[144:147], v[140:143], v[52:55], v[144:147]\n"
"ds_read_b128 v[200:203], v12 offset:53248\n"
"v_mfma_f32_16x16x32_bf16 v[152:155], v[140:143], v[56:59], v[152:155]\n"
"s_waitcnt lgkmcnt(2)\n"
"v_mov_b32 v141, 0x3f803f80\n"
"v_mfma_f32_16x16x32_bf16 v[136:139], v[252:255], v[60:63], v[136:139]\n"
"v_cvt_pk_bf16_f32 v225, v150, v151\n"
"v_mfma_f32_16x16x32_bf16 v[144:147], v[252:255], v[64:67], v[144:147]\n"
"v_cvt_pk_bf16_f32 v224, v148, v149\n"
"v_mfma_f32_16x16x32_bf16 v[152:155], v[252:255], v[68:71], v[152:155]\n"
"s_waitcnt lgkmcnt(0)\n"
"v_cvt_pk_bf16_f32 v251, v162, v163\n"
"v_mov_b32 v140, 0x3f803f80\n"
"v_mfma_f32_16x16x32_bf16 v[136:139], v[200:203], v[72:75], v[136:139]\n"
"s_barrier\n"
"v_mfma_f32_16x16x32_bf16 v[144:147], v[200:203], v[76:79], v[144:147]\n"
"ds_read2st64_b32 v[150:151], v9 offset0:114 offset1:115\n"
"v_mfma_f32_16x16x32_bf16 v[152:155], v[200:203], v[80:83], v[152:155]\n"
"v_mfma_f32_16x16x32_bf16 v[136:139], v[226:229], v[84:87], v[136:139]\n"
"ds_read_b32 v149, v9 offset:28928\n"
"v_mov_b32 v142, 0x3f803f80\n"
"v_mfma_f32_16x16x32_bf16 v[144:147], v[226:229], v[88:91], v[144:147]\n"
"v_mov_b32 v143, 0x3f803f80\n"
"v_mfma_f32_16x16x32_bf16 v[152:155], v[226:229], v[92:95], v[152:155]\n"
"s_and_b32 s41, 416, s55\n"
"v_add_u32 v8, s41, v15\n"
"s_nop 0\n"
"v_cvt_pk_bf16_f32 v200, v136, v137\n"
"v_mov_b32 v148, 0\n"
"s_nop 0\n"
"v_cvt_pk_bf16_f32 v226, v144, v145\n"
"ds_read2st64_b32 v[136:137], v9 offset0:116 offset1:117\n"
"v_cvt_pk_bf16_f32 v201, v138, v139\n"
"ds_read_u16 v15, v21 offset:992\n"
"ds_read2st64_b32 v[138:139], v9 offset0:118 offset1:119\n"
"v_cvt_pk_bf16_f32 v253, v154, v155\n"
"v_cvt_pk_bf16_f32 v252, v152, v153\n"
"v_cvt_pk_bf16_f32 v227, v146, v147\n"
"ds_read_u16 v8, v8 offset:768\n"
"s_waitcnt lgkmcnt(4)\n"
"v_mfma_f32_16x16x32_bf16 v[144:147], v[140:143], v[148:151], 0\n"
"ds_read2st64_b32 v[168:169], v9 offset0:132 offset1:133\n"
"v_mfma_f32_16x16x32_bf16 v[152:155], v[180:183], v[148:151], 0\n"
"ds_read2st64_b32 v[160:161], v20 offset0:0 offset1:1\n"
"ds_read2st64_b32 v[162:163], v20 offset0:2 offset1:3\n"
"v_mfma_f32_16x16x32_bf16 v[156:159], v[206:209], v[148:151], 0\n"
"s_waitcnt lgkmcnt(3)\n"
"v_mad_u32_u24 v8, v8, s40, v16\n"
"v_mfma_f32_16x16x32_bf16 v[164:167], v[232:235], v[148:151], 0\n"
"s_add_u32 m0, s51, 139264\n"
"s_nop 0\n"
"global_load_lds_dwordx4 v8, s[56:57]\n"
"v_mfma_f32_16x16x32_bf16 v[144:147], v[140:143], v[136:139], v[144:147]\n"
"ds_read_b32 v149, v9 offset:33024\n"
"ds_read2st64_b32 v[150:151], v9 offset0:130 offset1:131\n"
"v_mfma_f32_16x16x32_bf16 v[152:155], v[184:187], v[136:139], v[152:155]\n"
"v_mov_b32 v148, 0\n"
"v_mfma_f32_16x16x32_bf16 v[156:159], v[210:213], v[136:139], v[156:159]\n"
"v_mad_u32_u24 v16, v15, s40, v18\n"
"v_mfma_f32_16x16x32_bf16 v[164:167], v[236:239], v[136:139], v[164:167]\n"
"s_waitcnt lgkmcnt(2)\n"
"ds_read2st64_b32 v[254:255], v20 offset0:4 offset1:5\n"
"ds_read2st64_b32 v[170:171], v9 offset0:134 offset1:135\n"
"global_store_dwordx4 v16, v[160:163], s[66:67] sc1\n"
"s_waitcnt lgkmcnt(2)\n"
"s_setprio 2\n"
"v_mfma_f32_16x16x32_bf16 v[144:147], v[140:143], v[148:151], v[144:147]\n"
"s_add_u32 m0, s51, 141312\n"
"s_nop 0\n"
"global_load_lds_dwordx4 v8, s[58:59]\n"
"s_waitcnt lgkmcnt(0)\n"
"v_mfma_f32_16x16x32_bf16 v[144:147], v[140:143], v[168:171], v[144:147]\n"
"ds_read_b128 v[140:143], v17 offset:53248\n"
"v_mfma_f32_16x16x32_bf16 v[164:167], v[242:245], v[148:151], v[164:167]\n"
"v_mad_u32_u24 v15, v15, s40, v19\n"
"v_mfma_f32_16x16x32_bf16 v[164:167], v[246:249], v[168:171], v[164:167]\n"
"s_setprio 0\n"
"ds_read2st64_b32 v[136:137], v14 offset0:126 offset1:127\n"
"ds_read2st64_b32 v[138:139], v14 offset0:128 offset1:129\n"
"global_store_dwordx2 v15, v[254:255], s[66:67] offset:256 sc1\n"
"v_mfma_f32_16x16x32_bf16 v[152:155], v[190:193], v[148:151], v[152:155]\n"
"s_setprio 2\n"
"v_mfma_f32_16x16x32_bf16 v[152:155], v[194:197], v[168:171], v[152:155]\n"
"v_rcp_f32 v16, v144\n"
"s_add_u32 m0, s51, 143360\n"
"s_nop 0\n"
"global_load_lds_dwordx4 v8, s[60:61]\n"
"v_mul_f32 v166, v166, v16\n"
"v_mfma_f32_16x16x32_bf16 v[156:159], v[216:219], v[148:151], v[156:159]\n"
"v_mul_f32 v165, v165, v16\n"
"v_mul_f32 v164, v164, v16\n"
"v_add_u32 v144, 0xd4800000, v144\n"
"v_cmp_lt_u32 vcc, 0x28000000, v144\n"
"v_mfma_f32_16x16x32_bf16 v[156:159], v[220:223], v[168:171], v[156:159]\n"
"s_waitcnt lgkmcnt(0)\n"
"ds_read2st64_b32 v[146:147], v14 offset0:152 offset1:153\n"
"v_cvt_pk_bf16_f32 v254, v164, v165\n"
"v_mfma_f32_16x16x32_bf16 v[148:151], v[96:99], v[136:139], 0\n"
"ds_read2st64_b32 v[144:145], v14 offset0:150 offset1:151\n"
"v_mul_f32 v167, v167, v16\n"
"ds_read_b128 v[168:171], v17 offset:54272\n"
"v_mul_f32 v153, v153, v16\n"
"v_mul_f32 v152, v152, v16\n"
"v_cvt_pk_bf16_f32 v152, v152, v153\n"
"v_mul_f32 v158, v158, v16\n"
"v_mfma_f32_16x16x32_bf16 v[160:163], v[100:103], v[136:139], 0\n"
"s_or_b64 s[62:63], s[62:63], vcc\n"
"v_mul_f32 v157, v157, v16\n"
"v_mul_f32 v156, v156, v16\n"
"v_mul_f32 v154, v154, v16\n"
"v_cvt_pk_bf16_f32 v255, v166, v167\n"
"v_mfma_f32_16x16x32_bf16 v[164:167], v[140:143], v[136:139], 0\n"
"s_waitcnt lgkmcnt(1)\n"
"ds_read_b128 v[140:143], v17 offset:55296\n"
"ds_read2st64_b32 v[136:137], v14 offset0:174 offset1:175\n"
"v_mfma_f32_16x16x32_bf16 v[148:151], v[104:107], v[144:147], v[148:151]\n"
"ds_read2st64_b32 v[138:139], v14 offset0:176 offset1:177\n"
"v_mul_f32 v155, v155, v16\n"
"v_cvt_pk_bf16_f32 v153, v154, v155\n"
"v_mfma_f32_16x16x32_bf16 v[160:163], v[108:111], v[144:147], v[160:163]\n"
"s_waitcnt lgkmcnt(3)\n"
"v_mul_f32 v159, v159, v16\n"
"v_cvt_pk_bf16_f32 v155, v158, v159\n"
"v_mfma_f32_16x16x32_bf16 v[164:167], v[168:171], v[144:147], v[164:167]\n"
"s_setprio 0\n"
"ds_read2st64_b32 v[146:147], v14 offset0:200 offset1:201\n"
"v_cvt_pk_bf16_f32 v154, v156, v157\n"
"ds_read2st64_b32 v[144:145], v14 offset0:198 offset1:199\n"
"s_waitcnt lgkmcnt(2)\n"
"v_mfma_f32_16x16x32_bf16 v[148:151], v[0:3], v[136:139], v[148:151]\n"
"ds_read2st64_b32 v[168:169], v14 offset0:130 offset1:131\n"
"ds_read_b128 v[156:159], v17 offset:56320\n"
"v_mfma_f32_16x16x32_bf16 v[160:163], v[112:115], v[136:139], v[160:163]\n"
"ds_read2st64_b32 v[170:171], v14 offset0:154 offset1:155\n"
"v_mfma_f32_16x16x32_bf16 v[164:167], v[140:143], v[136:139], v[164:167]\n"
"s_waitcnt lgkmcnt(3)\n"
"ds_read2st64_b32 v[140:141], v14 offset0:178 offset1:179\n"
"v_mfma_f32_16x16x32_bf16 v[148:151], v[116:119], v[144:147], v[148:151]\n"
"ds_read_b128 v[136:139], v17 offset:57344\n"
"ds_read2st64_b32 v[142:143], v14 offset0:202 offset1:203\n"
"v_mfma_f32_16x16x32_bf16 v[160:163], v[120:123], v[144:147], v[160:163]\n"
"s_waitcnt lgkmcnt(4)\n"
"v_mfma_f32_16x16x32_bf16 v[164:167], v[156:159], v[144:147], v[164:167]\n"
"s_waitcnt lgkmcnt(3)\n"
"ds_read_b128 v[144:147], v17 offset:58368\n"
"v_mfma_f32_16x16x32_bf16 v[148:151], v[4:7], v[168:171], v[148:151]\n"
"ds_read_b128 v[156:159], v12 offset:24576\n"
"v_mfma_f32_16x16x32_bf16 v[160:163], v[124:127], v[168:171], v[160:163]\n"
"s_waitcnt lgkmcnt(3)\n"
"v_mfma_f32_16x16x32_bf16 v[164:167], v[136:139], v[168:171], v[164:167]\n"
"s_waitcnt lgkmcnt(2)\n"
"ds_read_b128 v[136:139], v13 offset:24576\n"
"v_mfma_f32_16x16x32_bf16 v[148:151], v[128:131], v[140:143], v[148:151]\n"
"v_mfma_f32_16x16x32_bf16 v[160:163], v[132:135], v[140:143], v[160:163]\n"
"s_waitcnt vmcnt(5)\n"
"s_barrier\n"
"s_waitcnt lgkmcnt(2)\n"
"v_mfma_f32_16x16x32_bf16 v[164:167], v[144:147], v[140:143], v[164:167]\n"
"s_waitcnt lgkmcnt(1)\n"
"v_mfma_f32_16x16x32_bf16 v[140:143], v[156:159], v[28:31], 0\n"
"s_nop 0\n"
"v_cmp_class_f32_e64 vcc, v148, s64\n"
"v_cvt_pk_bf16_f32 v148, v148, v149\n"
"ds_read_b128 v[144:147], v12 offset:26624\n"
"v_mfma_f32_16x16x32_bf16 v[168:171], v[156:159], v[32:35], 0\n"
"v_cvt_pk_bf16_f32 v149, v150, v151\n"
"s_mov_b32 m0, s54\n"
"v_mfma_f32_16x16x32_bf16 v[172:175], v[156:159], v[24:27], 0\n"
"s_waitcnt lgkmcnt(1)\n"
"v_cvt_pk_bf16_f32 v164, v164, v165\n"
"v_cvt_pk_bf16_f32 v165, v166, v167\n"
"ds_read_b128 v[156:159], v13 offset:26624\n"
"v_mfma_f32_16x16x32_bf16 v[140:143], v[136:139], v[40:43], v[140:143]\n"
"ds_write_addtid_b32 v154 offset:32768\n"
"v_cvt_pk_bf16_f32 v150, v160, v161\n"
"ds_write_addtid_b32 v155 offset:33024\n"
"v_mfma_f32_16x16x32_bf16 v[168:171], v[136:139], v[44:47], v[168:171]\n"
"v_cvt_pk_bf16_f32 v151, v162, v163\n"
"ds_write_addtid_b32 v254 offset:33280\n"
"v_mfma_f32_16x16x32_bf16 v[172:175], v[136:139], v[36:39], v[172:175]\n"
"ds_write_addtid_b32 v152 offset:32256\n"
"s_waitcnt lgkmcnt(5)\n"
"v_mfma_f32_16x16x32_bf16 v[140:143], v[144:147], v[48:51], v[140:143]\n"
"ds_read_b128 v[136:139], v12 offset:28672\n"
"ds_write_addtid_b32 v153 offset:32512\n"
"v_mfma_f32_16x16x32_bf16 v[168:171], v[144:147], v[52:55], v[168:171]\n"
"ds_write_addtid_b32 v255 offset:33536\n"
"s_mov_b32 m0, s68\n"
"s_or_b64 s[62:63], s[62:63], vcc\n"
"ds_write_addtid_b32 v149 offset:45312\n"
"v_mfma_f32_16x16x32_bf16 v[172:175], v[144:147], v[56:59], v[172:175]\n"
"s_waitcnt lgkmcnt(8)\n"
"ds_read_b128 v[144:147], v13 offset:28672\n"
"v_mfma_f32_16x16x32_bf16 v[140:143], v[156:159], v[60:63], v[140:143]\n"
"ds_write_addtid_b32 v148 offset:45056\n"
"v_mfma_f32_16x16x32_bf16 v[168:171], v[156:159], v[64:67], v[168:171]\n"
"ds_write_addtid_b32 v150 offset:45568\n"
"v_mfma_f32_16x16x32_bf16 v[172:175], v[156:159], v[68:71], v[172:175]\n"
"ds_write_addtid_b32 v151 offset:45824\n"
"s_waitcnt lgkmcnt(7)\n"
"v_mfma_f32_16x16x32_bf16 v[140:143], v[136:139], v[72:75], v[140:143]\n"
"ds_write_addtid_b32 v164 offset:46080\n"
"v_mfma_f32_16x16x32_bf16 v[168:171], v[136:139], v[76:79], v[168:171]\n"
"ds_write_addtid_b32 v165 offset:46336\n"
"v_mfma_f32_16x16x32_bf16 v[172:175], v[136:139], v[80:83], v[172:175]\n"
"s_waitcnt lgkmcnt(5)\n"
"v_mov_b32 v151, 0x3f803f80\n"
"v_mfma_f32_16x16x32_bf16 v[140:143], v[144:147], v[84:87], v[140:143]\n"
"v_mov_b32 v149, 0x3f803f80\n"
"v_mfma_f32_16x16x32_bf16 v[168:171], v[144:147], v[88:91], v[168:171]\n"
"s_waitcnt lgkmcnt(0)\n"
"s_barrier\n"
"v_mfma_f32_16x16x32_bf16 v[172:175], v[144:147], v[92:95], v[172:175]\n"
"ds_read2st64_b32 v[138:139], v9 offset0:122 offset1:123\n"
"ds_read2st64_b32 v[136:137], v9 offset0:120 offset1:121\n"
"s_nop 2\n"
"v_cvt_pk_bf16_f32 v229, v170, v171\n"
"v_cvt_pk_bf16_f32 v228, v168, v169\n"
"v_mov_b32 v150, 0x3f803f80\n"
"v_cvt_pk_bf16_f32 v203, v142, v143\n"
"v_mov_b32 v148, 0x3f803f80\n"
"v_cvt_pk_bf16_f32 v202, v140, v141\n"
"ds_read2st64_b32 v[144:145], v9 offset0:124 offset1:125\n"
"ds_read2st64_b32 v[162:163], v20 offset0:2 offset1:3\n"
"ds_read2st64_b32 v[146:147], v9 offset0:126 offset1:127\n"
"s_waitcnt lgkmcnt(3)\n"
"v_mfma_f32_16x16x32_bf16 v[140:143], v[148:151], v[136:139], 0\n"
"ds_read_u16 v16, v21 offset:1024\n"
"v_mfma_f32_16x16x32_bf16 v[152:155], v[178:181], v[136:139], 0\n"
"v_cvt_pk_bf16_f32 v254, v172, v173\n"
"v_cvt_pk_bf16_f32 v255, v174, v175\n"
"ds_read2st64_b32 v[160:161], v20 offset0:0 offset1:1\n"
"v_mfma_f32_16x16x32_bf16 v[156:159], v[204:207], v[136:139], 0\n"
"v_mfma_f32_16x16x32_bf16 v[164:167], v[230:233], v[136:139], 0\n"
"s_waitcnt lgkmcnt(1)\n"
"ds_read2st64_b32 v[138:139], v9 offset0:130 offset1:131\n"
"ds_read2st64_b32 v[136:137], v9 offset0:128 offset1:129\n"
"ds_read2st64_b32 v[230:231], v20 offset0:4 offset1:5\n"
"v_mad_u32_u24 v15, v16, s40, v18\n"
"v_mfma_f32_16x16x32_bf16 v[140:143], v[148:151], v[144:147], v[140:143]\n"
"v_mov_b32 v168, 0\n"
"v_mfma_f32_16x16x32_bf16 v[152:155], v[188:191], v[144:147], v[152:155]\n"
"v_mfma_f32_16x16x32_bf16 v[156:159], v[214:217], v[144:147], v[156:159]\n"
"ds_read_b32 v170, v9 offset:33792\n"
"v_mfma_f32_16x16x32_bf16 v[164:167], v[240:243], v[144:147], v[164:167]\n"
"s_waitcnt lgkmcnt(2)\n"
"s_setprio 2\n"
"v_mfma_f32_16x16x32_bf16 v[140:143], v[148:151], v[136:139], v[140:143]\n"
"ds_read_b32 v146, v9 offset:9728\n"
"v_mov_b32 v144, 0\n"
"ds_read_b32 v172, v9 offset:34304\n"
"v_mov_b32 v145, 0\n"
"v_mfma_f32_16x16x32_bf16 v[152:155], v[198:201], v[136:139], v[152:155]\n"
"global_store_dwordx4 v15, v[160:163], s[66:67] sc1\n"
"v_mfma_f32_16x16x32_bf16 v[156:159], v[224:227], v[136:139], v[156:159]\n"
"v_mov_b32 v147, 0\n"
"v_mov_b32 v171, 0\n"
"v_mov_b32 v174, 0\n"
"v_mfma_f32_16x16x32_bf16 v[164:167], v[250:253], v[136:139], v[164:167]\n"
"s_waitcnt lgkmcnt(0)\n"
"v_mad_u32_u24 v16, v16, s40, v19\n"
"v_mov_b32 v173, 0\n"
"s_nop 1\n"
"v_mfma_f32_16x16x32_bf16 v[140:143], v[148:151], v[170:173], v[140:143]\n"
"global_store_dwordx2 v16, v[230:231], s[66:67] offset:256 sc1\n"
"v_mov_b32 v169, 0\n"
"v_mfma_f32_16x16x32_bf16 v[140:143], v[148:151], v[144:147], v[140:143]\n"
"s_nop 0\n"
"v_mfma_f32_16x16x32_bf16 v[164:167], v[232:235], v[168:171], v[164:167]\n"
"v_mov_b32 v175, 0\n"
"s_nop 1\n"
"v_mfma_f32_16x16x32_bf16 v[164:167], v[244:247], v[172:175], v[164:167]\n"
"v_mfma_f32_16x16x32_bf16 v[164:167], v[252:255], v[144:147], v[164:167]\n"
"s_nop 0\n"
"v_rcp_f32 v16, v140\n"
"v_mfma_f32_16x16x32_bf16 v[152:155], v[180:183], v[168:171], v[152:155]\n"
"ds_read2st64_b32 v[138:139], v14 offset0:128 offset1:129\n"
"v_mfma_f32_16x16x32_bf16 v[152:155], v[192:195], v[172:175], v[152:155]\n"
"ds_read2st64_b32 v[136:137], v14 offset0:126 offset1:127\n"
"v_add_u32 v140, 0xd4800000, v140\n"
"ds_read2st64_b32 v[160:161], v14 offset0:150 offset1:151\n"
"v_mul_f32 v166, v166, v16\n"
"v_mfma_f32_16x16x32_bf16 v[152:155], v[200:203], v[144:147], v[152:155]\n"
"v_cmp_lt_u32 vcc, 0x28000000, v140\n"
"v_mfma_f32_16x16x32_bf16 v[156:159], v[206:209], v[168:171], v[156:159]\n"
"v_mul_f32 v164, v164, v16\n"
"v_mul_f32 v165, v165, v16\n"
"ds_read_b128 v[140:143], v17 offset:54272\n"
"v_mfma_f32_16x16x32_bf16 v[156:159], v[218:221], v[172:175], v[156:159]\n"
"ds_read_b128 v[148:151], v17 offset:53248\n"
"v_cvt_pk_bf16_f32 v240, v164, v165\n"
"v_mfma_f32_16x16x32_bf16 v[156:159], v[226:229], v[144:147], v[156:159]\n"
"s_waitcnt lgkmcnt(3)\n"
"ds_read2st64_b32 v[162:163], v14 offset0:152 offset1:153\n"
"v_mfma_f32_16x16x32_bf16 v[144:147], v[96:99], v[136:139], 0\n"
"v_mul_f32 v167, v167, v16\n"
"v_mul_f32 v153, v153, v16\n"
"ds_read2st64_b32 v[170:171], v14 offset0:176 offset1:177\n"
"v_mfma_f32_16x16x32_bf16 v[172:175], v[100:103], v[136:139], 0\n"
"s_or_b64 s[62:63], s[62:63], vcc\n"
"v_mul_f32 v152, v152, v16\n"
"v_cvt_pk_bf16_f32 v241, v166, v167\n"
"s_waitcnt lgkmcnt(2)\n"
"v_mfma_f32_16x16x32_bf16 v[164:167], v[148:151], v[136:139], 0\n"
"s_waitcnt lgkmcnt(1)\n"
"ds_read2st64_b32 v[168:169], v14 offset0:174 offset1:175\n"
"v_mfma_f32_16x16x32_bf16 v[144:147], v[104:107], v[160:163], v[144:147]\n"
"v_mul_f32 v155, v155, v16\n"
"v_cvt_pk_bf16_f32 v152, v152, v153\n"
"ds_read2st64_b32 v[148:149], v14 offset0:198 offset1:199\n"
"ds_read_b128 v[136:139], v17 offset:55296\n"
"v_mfma_f32_16x16x32_bf16 v[172:175], v[108:111], v[160:163], v[172:175]\n"
"v_mul_f32 v154, v154, v16\n"
"v_mul_f32 v156, v156, v16\n"
"v_mfma_f32_16x16x32_bf16 v[164:167], v[140:143], v[160:163], v[164:167]\n"
"s_waitcnt lgkmcnt(2)\n"
"ds_read2st64_b32 v[150:151], v14 offset0:200 offset1:201\n"
"ds_read2st64_b32 v[160:161], v14 offset0:130 offset1:131\n"
"ds_read_b128 v[140:143], v17 offset:56320\n"
"v_mfma_f32_16x16x32_bf16 v[144:147], v[0:3], v[168:171], v[144:147]\n"
"v_mul_f32 v158, v158, v16\n"
"v_mul_f32 v157, v157, v16\n"
"v_mfma_f32_16x16x32_bf16 v[172:175], v[112:115], v[168:171], v[172:175]\n"
"s_waitcnt lgkmcnt(3)\n"
"v_cvt_pk_bf16_f32 v153, v154, v155\n"
"v_cvt_pk_bf16_f32 v154, v156, v157\n"
"v_mfma_f32_16x16x32_bf16 v[164:167], v[136:139], v[168:171], v[164:167]\n"
"s_waitcnt lgkmcnt(2)\n"
"ds_read_b128 v[136:139], v17 offset:57344\n"
"ds_read2st64_b32 v[162:163], v14 offset0:154 offset1:155\n"
"v_mfma_f32_16x16x32_bf16 v[144:147], v[116:119], v[148:151], v[144:147]\n"
"s_setprio 0\n"
"v_mul_f32 v159, v159, v16\n"
"v_cvt_pk_bf16_f32 v155, v158, v159\n"
"ds_read2st64_b32 v[158:159], v14 offset0:202 offset1:203\n"
"v_mfma_f32_16x16x32_bf16 v[172:175], v[120:123], v[148:151], v[172:175]\n"
"s_waitcnt lgkmcnt(3)\n"
"ds_read2st64_b32 v[156:157], v14 offset0:178 offset1:179\n"
"v_mfma_f32_16x16x32_bf16 v[164:167], v[140:143], v[148:151], v[164:167]\n"
"s_waitcnt lgkmcnt(2)\n"
"v_mfma_f32_16x16x32_bf16 v[144:147], v[4:7], v[160:163], v[144:147]\n"
"ds_read_b128 v[148:151], v12 offset:12288\n"
"v_mfma_f32_16x16x32_bf16 v[172:175], v[124:127], v[160:163], v[172:175]\n"
"ds_read_b128 v[140:143], v17 offset:58368\n"
"v_mfma_f32_16x16x32_bf16 v[164:167], v[136:139], v[160:163], v[164:167]\n"
"s_waitcnt lgkmcnt(2)\n"
"ds_read_b128 v[136:139], v13 offset:12288\n"
"v_mfma_f32_16x16x32_bf16 v[144:147], v[128:131], v[156:159], v[144:147]\n"
"s_waitcnt vmcnt(2)\n"
"s_barrier\n"
"v_mfma_f32_16x16x32_bf16 v[172:175], v[132:135], v[156:159], v[172:175]\n"
"s_waitcnt lgkmcnt(1)\n"
"v_mfma_f32_16x16x32_bf16 v[164:167], v[140:143], v[156:159], v[164:167]\n"
"ds_read_b128 v[156:159], v12 offset:14336\n"
"v_mfma_f32_16x16x32_bf16 v[140:143], v[148:151], v[28:31], 0\n"
"v_mfma_f32_16x16x32_bf16 v[160:163], v[148:151], v[32:35], 0\n"
"s_mov_b32 m0, s54\n"
"s_nop 0\n"
"ds_write_addtid_b32 v241 offset:33536\n"
"v_mfma_f32_16x16x32_bf16 v[168:171], v[148:151], v[24:27], 0\n"
"s_waitcnt lgkmcnt(2)\n"
"v_cmp_class_f32_e64 vcc, v144, s64\n"
"ds_read_b128 v[148:151], v13 offset:14336\n"
"v_mfma_f32_16x16x32_bf16 v[140:143], v[136:139], v[40:43], v[140:143]\n"
"ds_write_addtid_b32 v154 offset:32768\n"
"ds_write_addtid_b32 v240 offset:33280\n"
"v_mfma_f32_16x16x32_bf16 v[160:163], v[136:139], v[44:47], v[160:163]\n"
"v_cvt_pk_bf16_f32 v144, v144, v145\n"
"v_mfma_f32_16x16x32_bf16 v[168:171], v[136:139], v[36:39], v[168:171]\n"
"s_waitcnt lgkmcnt(4)\n"
"ds_read_b128 v[136:139], v12 offset:16384\n"
"v_cvt_pk_bf16_f32 v145, v146, v147\n"
"v_cvt_pk_bf16_f32 v146, v172, v173\n"
"v_mfma_f32_16x16x32_bf16 v[140:143], v[156:159], v[48:51], v[140:143]\n"
"ds_write_addtid_b32 v155 offset:33024\n"
"ds_write_addtid_b32 v153 offset:32512\n"
"v_mfma_f32_16x16x32_bf16 v[160:163], v[156:159], v[52:55], v[160:163]\n"
"v_cvt_pk_bf16_f32 v164, v164, v165\n"
"v_cvt_pk_bf16_f32 v165, v166, v167\n"
"v_mfma_f32_16x16x32_bf16 v[168:171], v[156:159], v[56:59], v[168:171]\n"
"ds_write_addtid_b32 v152 offset:32256\n"
"ds_read_b128 v[152:155], v13 offset:16384\n"
"s_waitcnt lgkmcnt(7)\n"
"v_mfma_f32_16x16x32_bf16 v[140:143], v[148:151], v[60:63], v[140:143]\n"
"s_mov_b32 m0, s68\n"
"s_nop 0\n"
"ds_write_addtid_b32 v146 offset:45568\n"
"v_mfma_f32_16x16x32_bf16 v[160:163], v[148:151], v[64:67], v[160:163]\n"
"ds_write_addtid_b32 v164 offset:46080\n"
"v_cvt_pk_bf16_f32 v147, v174, v175\n"
"ds_write_addtid_b32 v165 offset:46336\n"
"v_mfma_f32_16x16x32_bf16 v[168:171], v[148:151], v[68:71], v[168:171]\n"
"s_waitcnt lgkmcnt(7)\n"
"v_mfma_f32_16x16x32_bf16 v[140:143], v[136:139], v[72:75], v[140:143]\n"
"ds_write_addtid_b32 v144 offset:45056\n"
"v_mfma_f32_16x16x32_bf16 v[160:163], v[136:139], v[76:79], v[160:163]\n"
"ds_write_addtid_b32 v145 offset:45312\n"
"s_or_b64 s[62:63], s[62:63], vcc\n"
"v_mfma_f32_16x16x32_bf16 v[168:171], v[136:139], v[80:83], v[168:171]\n"
"ds_write_addtid_b32 v147 offset:45824\n"
"s_waitcnt lgkmcnt(6)\n"
"v_mfma_f32_16x16x32_bf16 v[140:143], v[152:155], v[84:87], v[140:143]\n"
"v_mfma_f32_16x16x32_bf16 v[160:163], v[152:155], v[88:91], v[160:163]\n"
"v_mfma_f32_16x16x32_bf16 v[168:171], v[152:155], v[92:95], v[168:171]\n"
"s_waitcnt lgkmcnt(0)\n"
"s_barrier\n"
"ds_read2st64_b32 v[144:145], v9 offset0:112 offset1:113\n"
"s_nop 3\n"
"v_cvt_pk_bf16_f32 v204, v160, v161\n"
"ds_read2st64_b32 v[146:147], v9 offset0:114 offset1:115\n"
"v_cvt_pk_bf16_f32 v178, v140, v141\n"
"v_mov_b32 v137, 0x3f803f80\n"
"v_mov_b32 v136, 0x3f803f80\n"
"ds_read2st64_b32 v[148:149], v9 offset0:116 offset1:117\n"
"v_cvt_pk_bf16_f32 v179, v142, v143\n"
"ds_read_u16 v16, v21 offset:1056\n"
"v_mov_b32 v138, 0x3f803f80\n"
"v_mov_b32 v139, 0x3f803f80\n"
"v_cvt_pk_bf16_f32 v231, v170, v171\n"
"ds_read2st64_b32 v[150:151], v9 offset0:118 offset1:119\n"
"ds_read2st64_b32 v[240:241], v20 offset0:4 offset1:5\n"
"s_waitcnt lgkmcnt(4)\n"
"v_mfma_f32_16x16x32_bf16 v[140:143], v[136:139], v[144:147], 0\n"
"v_cvt_pk_bf16_f32 v230, v168, v169\n"
"v_cvt_pk_bf16_f32 v205, v162, v163\n"
"ds_read2st64_b32 v[160:161], v20 offset0:0 offset1:1\n"
"v_mfma_f32_16x16x32_bf16 v[152:155], v[182:185], v[144:147], 0\n"
"v_mfma_f32_16x16x32_bf16 v[156:159], v[208:211], v[144:147], 0\n"
"ds_read2st64_b32 v[162:163], v20 offset0:2 offset1:3\n"
"v_mfma_f32_16x16x32_bf16 v[164:167], v[234:237], v[144:147], 0\n"
"s_waitcnt lgkmcnt(3)\n"
"ds_read2st64_b32 v[146:147], v9 offset0:130 offset1:131\n"
"ds_read2st64_b32 v[144:145], v9 offset0:128 offset1:129\n"
"v_mfma_f32_16x16x32_bf16 v[140:143], v[136:139], v[148:151], v[140:143]\n"
"v_mfma_f32_16x16x32_bf16 v[152:155], v[190:193], v[148:151], v[152:155]\n"
"v_mad_u32_u24 v15, v16, s40, v18\n"
"v_mfma_f32_16x16x32_bf16 v[156:159], v[216:219], v[148:151], v[156:159]\n"
"v_mad_u32_u24 v16, v16, s40, v19\n"
"v_mfma_f32_16x16x32_bf16 v[164:167], v[242:245], v[148:151], v[164:167]\n"
"s_waitcnt lgkmcnt(2)\n"
"global_store_dwordx4 v15, v[160:163], s[66:67] sc1\n"
"s_waitcnt lgkmcnt(0)\n"
"v_mfma_f32_16x16x32_bf16 v[140:143], v[136:139], v[144:147], v[140:143]\n"
"ds_read2st64_b32 v[148:149], v9 offset0:132 offset1:133\n"
"v_mfma_f32_16x16x32_bf16 v[152:155], v[178:181], v[144:147], v[152:155]\n"
"ds_read2st64_b32 v[150:151], v9 offset0:134 offset1:135\n"
"ds_read2st64_b32 v[162:163], v9 offset0:36 offset1:37\n"
"global_store_dwordx2 v16, v[240:241], s[66:67] offset:256 sc1\n"
"v_mfma_f32_16x16x32_bf16 v[156:159], v[204:207], v[144:147], v[156:159]\n"
"v_mov_b32 v160, 0\n"
"v_mov_b32 v161, 0\n"
"v_mfma_f32_16x16x32_bf16 v[164:167], v[230:233], v[144:147], v[164:167]\n"
"s_waitcnt lgkmcnt(1)\n"
"v_mfma_f32_16x16x32_bf16 v[140:143], v[136:139], v[148:151], v[140:143]\n"
"s_waitcnt lgkmcnt(0)\n"
"ds_read2st64_b32 v[170:171], v14 offset0:152 offset1:153\n"
"v_mfma_f32_16x16x32_bf16 v[140:143], v[136:139], v[160:163], v[140:143]\n"
"ds_read2st64_b32 v[138:139], v14 offset0:128 offset1:129\n"
"s_setprio 2\n"
"v_mfma_f32_16x16x32_bf16 v[164:167], v[252:255], v[148:151], v[164:167]\n"
"ds_read2st64_b32 v[136:137], v14 offset0:126 offset1:127\n"
"v_mfma_f32_16x16x32_bf16 v[164:167], v[244:247], v[160:163], v[164:167]\n"
"ds_read_b128 v[144:147], v17 offset:53248\n"
"v_mfma_f32_16x16x32_bf16 v[152:155], v[200:203], v[148:151], v[152:155]\n"
"s_nop 0\n"
"v_rcp_f32 v16, v140\n"
"v_mfma_f32_16x16x32_bf16 v[152:155], v[192:195], v[160:163], v[152:155]\n"
"ds_read2st64_b32 v[168:169], v14 offset0:150 offset1:151\n"
"v_mfma_f32_16x16x32_bf16 v[156:159], v[226:229], v[148:151], v[156:159]\n"
"s_nop 0\n"
"v_mul_f32 v167, v167, v16\n"
"v_add_u32 v140, 0xd4800000, v140\n"
"v_mfma_f32_16x16x32_bf16 v[156:159], v[218:221], v[160:163], v[156:159]\n"
"s_waitcnt lgkmcnt(2)\n"
"ds_read2st64_b32 v[162:163], v14 offset0:176 offset1:177\n"
"v_cmp_lt_u32 vcc, 0x28000000, v140\n"
"ds_read_b128 v[140:143], v17 offset:54272\n"
"v_mfma_f32_16x16x32_bf16 v[148:151], v[96:99], v[136:139], 0\n"
"v_mul_f32 v155, v155, v16\n"
"v_mul_f32 v165, v165, v16\n"
"v_mul_f32 v164, v164, v16\n"
"v_mul_f32 v166, v166, v16\n"
"ds_read2st64_b32 v[160:161], v14 offset0:174 offset1:175\n"
"v_mul_f32 v152, v152, v16\n"
"v_mfma_f32_16x16x32_bf16 v[172:175], v[100:103], v[136:139], 0\n"
"s_waitcnt lgkmcnt(4)\n"
"v_cvt_pk_bf16_f32 v241, v166, v167\n"
"v_cvt_pk_bf16_f32 v240, v164, v165\n"
"v_mfma_f32_16x16x32_bf16 v[164:167], v[144:147], v[136:139], 0\n"
"s_waitcnt lgkmcnt(3)\n"
"ds_read_b128 v[136:139], v17 offset:55296\n"
"v_mfma_f32_16x16x32_bf16 v[148:151], v[104:107], v[168:171], v[148:151]\n"
"v_mul_f32 v153, v153, v16\n"
"s_or_b64 s[62:63], s[62:63], vcc\n"
"v_mfma_f32_16x16x32_bf16 v[172:175], v[108:111], v[168:171], v[172:175]\n"
"s_waitcnt lgkmcnt(2)\n"
"ds_read2st64_b32 v[146:147], v14 offset0:200 offset1:201\n"
"v_mfma_f32_16x16x32_bf16 v[164:167], v[140:143], v[168:171], v[164:167]\n"
"s_waitcnt lgkmcnt(2)\n"
"ds_read2st64_b32 v[144:145], v14 offset0:198 offset1:199\n"
"v_mfma_f32_16x16x32_bf16 v[148:151], v[0:3], v[160:163], v[148:151]\n"
"ds_read2st64_b32 v[170:171], v14 offset0:154 offset1:155\n"
"v_cvt_pk_bf16_f32 v152, v152, v153\n"
"ds_read2st64_b32 v[168:169], v14 offset0:130 offset1:131\n"
"v_mfma_f32_16x16x32_bf16 v[172:175], v[112:115], v[160:163], v[172:175]\n"
"s_waitcnt lgkmcnt(4)\n"
"v_mul_f32 v159, v159, v16\n"
"v_mul_f32 v158, v158, v16\n"
"ds_read_b128 v[140:143], v17 offset:56320\n"
"v_mfma_f32_16x16x32_bf16 v[164:167], v[136:139], v[160:163], v[164:167]\n"
"s_waitcnt lgkmcnt(3)\n"
"v_mul_f32 v156, v156, v16\n"
"s_setprio 0\n"
"ds_read_b128 v[136:139], v17 offset:57344\n"
"v_mfma_f32_16x16x32_bf16 v[148:151], v[116:119], v[144:147], v[148:151]\n"
"v_mul_f32 v154, v154, v16\n"
"v_cvt_pk_bf16_f32 v153, v154, v155\n"
"v_mfma_f32_16x16x32_bf16 v[172:175], v[120:123], v[144:147], v[172:175]\n"
"s_waitcnt lgkmcnt(1)\n"
"v_mul_f32 v157, v157, v16\n"
"v_cvt_pk_bf16_f32 v154, v156, v157\n"
"v_cvt_pk_bf16_f32 v155, v158, v159\n"
"v_mfma_f32_16x16x32_bf16 v[164:167], v[140:143], v[144:147], v[164:167]\n"
"ds_read2st64_b32 v[158:159], v14 offset0:202 offset1:203\n"
"ds_read2st64_b32 v[156:157], v14 offset0:178 offset1:179\n"
"v_mfma_f32_16x16x32_bf16 v[148:151], v[4:7], v[168:171], v[148:151]\n"
"ds_read_b128 v[140:143], v17 offset:58368\n"
"v_mfma_f32_16x16x32_bf16 v[172:175], v[124:127], v[168:171], v[172:175]\n"
"s_waitcnt lgkmcnt(3)\n"
"v_mfma_f32_16x16x32_bf16 v[164:167], v[136:139], v[168:171], v[164:167]\n"
"s_waitcnt lgkmcnt(1)\n"
"v_mfma_f32_16x16x32_bf16 v[148:151], v[128:131], v[156:159], v[148:151]\n"
"ds_read_b128 v[144:147], v12 offset:36864\n"
"ds_read_b128 v[136:139], v13 offset:36864\n"
"v_mfma_f32_16x16x32_bf16 v[172:175], v[132:135], v[156:159], v[172:175]\n"
"s_barrier\n"
"s_mov_b32 m0, s54\n"
"s_waitcnt lgkmcnt(2)\n"
"v_mfma_f32_16x16x32_bf16 v[164:167], v[140:143], v[156:159], v[164:167]\n"
"s_nop 0\n"
"v_cmp_class_f32_e64 vcc, v148, s64\n"
"s_nop 5\n"
"v_cvt_pk_bf16_f32 v164, v164, v165\n"
"s_waitcnt lgkmcnt(1)\n"
"v_mfma_f32_16x16x32_bf16 v[140:143], v[144:147], v[28:31], 0\n"
"v_mfma_f32_16x16x32_bf16 v[156:159], v[144:147], v[32:35], 0\n"
"ds_write_addtid_b32 v153 offset:32512\n"
"v_mfma_f32_16x16x32_bf16 v[32:35], v[144:147], v[24:27], 0\n"
"s_waitcnt lgkmcnt(1)\n"
"ds_read_b128 v[24:27], v13 offset:38912\n"
"v_mfma_f32_16x16x32_bf16 v[140:143], v[136:139], v[40:43], v[140:143]\n"
"ds_read_b128 v[28:31], v12 offset:38912\n"
"v_cvt_pk_bf16_f32 v148, v148, v149\n"
"v_cvt_pk_bf16_f32 v149, v150, v151\n"
"v_cvt_pk_bf16_f32 v165, v166, v167\n"
"ds_write_addtid_b32 v240 offset:33280\n"
"v_mfma_f32_16x16x32_bf16 v[156:159], v[136:139], v[44:47], v[156:159]\n"
"s_or_b64 s[62:63], s[62:63], vcc\n"
"ds_write_addtid_b32 v155 offset:33024\n"
"v_cvt_pk_bf16_f32 v151, v174, v175\n"
"ds_write_addtid_b32 v154 offset:32768\n"
"v_mfma_f32_16x16x32_bf16 v[32:35], v[136:139], v[36:39], v[32:35]\n"
"ds_write_addtid_b32 v241 offset:33536\n"
"ds_write_addtid_b32 v152 offset:32256\n"
"s_mov_b32 m0, s68\n"
"s_waitcnt lgkmcnt(5)\n"
"v_mfma_f32_16x16x32_bf16 v[140:143], v[28:31], v[48:51], v[140:143]\n"
"ds_read_b128 v[36:39], v12 offset:40960\n"
"v_mfma_f32_16x16x32_bf16 v[156:159], v[28:31], v[52:55], v[156:159]\n"
"v_cvt_pk_bf16_f32 v150, v172, v173\n"
"v_mfma_f32_16x16x32_bf16 v[32:35], v[28:31], v[56:59], v[32:35]\n"
"ds_write_addtid_b32 v149 offset:45312\n"
"v_mfma_f32_16x16x32_bf16 v[140:143], v[24:27], v[60:63], v[140:143]\n"
"ds_write_addtid_b32 v151 offset:45824\n"
"v_mfma_f32_16x16x32_bf16 v[156:159], v[24:27], v[64:67], v[156:159]\n"
"ds_write_addtid_b32 v164 offset:46080\n"
"v_mfma_f32_16x16x32_bf16 v[32:35], v[24:27], v[68:71], v[32:35]\n"
"s_waitcnt lgkmcnt(3)\n"
"ds_read_b128 v[28:31], v13 offset:40960\n"
"v_mfma_f32_16x16x32_bf16 v[140:143], v[36:39], v[72:75], v[140:143]\n"
"v_mfma_f32_16x16x32_bf16 v[156:159], v[36:39], v[76:79], v[156:159]\n"
"ds_write_addtid_b32 v165 offset:46336\n"
"ds_write_addtid_b32 v148 offset:45056\n"
"ds_write_addtid_b32 v150 offset:45568\n"
"v_mov_b32 v25, 0x3f803f80\n"
"v_mfma_f32_16x16x32_bf16 v[32:35], v[36:39], v[80:83], v[32:35]\n"
"s_waitcnt lgkmcnt(3)\n"
"v_mov_b32 v24, 0x3f803f80\n"
"v_mfma_f32_16x16x32_bf16 v[140:143], v[28:31], v[84:87], v[140:143]\n"
"v_mfma_f32_16x16x32_bf16 v[156:159], v[28:31], v[88:91], v[156:159]\n"
"v_mov_b32 v26, 0x3f803f80\n"
"v_mfma_f32_16x16x32_bf16 v[32:35], v[28:31], v[92:95], v[32:35]\n"
"s_waitcnt lgkmcnt(0)\n"
"v_mov_b32 v36, 0\n"
"v_mov_b32 v27, 0x3f803f80\n"
"s_barrier\n"
"ds_read_b32 v37, v9 offset:30976\n"
"ds_read2st64_b32 v[38:39], v9 offset0:122 offset1:123\n"
"ds_read2st64_b32 v[30:31], v9 offset0:130 offset1:131\n"
"v_cvt_pk_bf16_f32 v206, v156, v157\n"
"s_setprio 2\n"
"ds_read2st64_b32 v[28:29], v9 offset0:128 offset1:129\n"
"ds_read_u16 v16, v21 offset:1088\n"
"v_cvt_pk_bf16_f32 v180, v140, v141\n"
"v_cvt_pk_bf16_f32 v232, v32, v33\n"
"v_cvt_pk_bf16_f32 v233, v34, v35\n"
"s_waitcnt lgkmcnt(3)\n"
"v_mfma_f32_16x16x32_bf16 v[32:35], v[24:27], v[36:39], 0\n"
"v_cvt_pk_bf16_f32 v207, v158, v159\n"
"v_cvt_pk_bf16_f32 v181, v142, v143\n"
"v_mfma_f32_16x16x32_bf16 v[40:43], v[182:185], v[36:39], 0\n"
"ds_read2st64_b32 v[44:45], v20 offset0:0 offset1:1\n"
"v_mfma_f32_16x16x32_bf16 v[48:51], v[208:211], v[36:39], 0\n"
"ds_read2st64_b32 v[46:47], v20 offset0:2 offset1:3\n"
"v_mfma_f32_16x16x32_bf16 v[52:55], v[234:237], v[36:39], 0\n"
"s_waitcnt lgkmcnt(3)\n"
"ds_read2st64_b32 v[38:39], v9 offset0:134 offset1:135\n"
"ds_read_b32 v37, v9 offset:34048\n"
"v_mfma_f32_16x16x32_bf16 v[32:35], v[24:27], v[28:31], v[32:35]\n"
"ds_read2st64_b32 v[240:241], v20 offset0:4 offset1:5\n"
"v_mfma_f32_16x16x32_bf16 v[40:43], v[178:181], v[28:31], v[40:43]\n"
"s_waitcnt lgkmcnt(5)\n"
"v_mov_b32 v36, 0\n"
"v_mad_u32_u24 v15, v16, s40, v18\n"
"ds_read2st64_b32 v[60:61], v9 offset0:126 offset1:127\n"
"v_mfma_f32_16x16x32_bf16 v[48:51], v[204:207], v[28:31], v[48:51]\n"
"s_waitcnt lgkmcnt(4)\n"
"global_store_dwordx4 v15, v[44:47], s[66:67] sc1\n"
"v_mfma_f32_16x16x32_bf16 v[52:55], v[230:233], v[28:31], v[52:55]\n"
"s_waitcnt lgkmcnt(2)\n"
"ds_read_b32 v31, v9 offset:9984\n"
"ds_read2st64_b32 v[58:59], v9 offset0:124 offset1:125\n"
"v_mfma_f32_16x16x32_bf16 v[32:35], v[24:27], v[36:39], v[32:35]\n"
"v_mad_u32_u24 v16, v16, s40, v19\n"
"v_mfma_f32_16x16x32_bf16 v[40:43], v[192:195], v[36:39], v[40:43]\n"
"v_mov_b32 v30, 0\n"
"v_mov_b32 v56, 0\n"
"v_mfma_f32_16x16x32_bf16 v[48:51], v[218:221], v[36:39], v[48:51]\n"
"v_mov_b32 v29, 0\n"
"v_mov_b32 v28, 0\n"
"v_mfma_f32_16x16x32_bf16 v[52:55], v[244:247], v[36:39], v[52:55]\n"
"s_waitcnt lgkmcnt(3)\n"
"global_store_dwordx2 v16, v[240:241], s[66:67] offset:256 sc1\n"
"s_waitcnt lgkmcnt(0)\n"
"v_mfma_f32_16x16x32_bf16 v[32:35], v[24:27], v[58:61], v[32:35]\n"
"ds_read2st64_b32 v[182:183], v14 offset0:126 offset1:127\n"
"v_mov_b32 v62, 0\n"
"v_mfma_f32_16x16x32_bf16 v[32:35], v[24:27], v[28:31], v[32:35]\n"
"v_mov_b32 v63, 0\n"
"v_mov_b32 v57, 0\n"
"s_nop 1\n"
"v_mfma_f32_16x16x32_bf16 v[52:55], v[236:239], v[56:59], v[52:55]\n"
"v_mfma_f32_16x16x32_bf16 v[52:55], v[248:251], v[60:63], v[52:55]\n"
"v_mfma_f32_16x16x32_bf16 v[52:55], v[252:255], v[28:31], v[52:55]\n"
"v_mfma_f32_16x16x32_bf16 v[40:43], v[184:187], v[56:59], v[40:43]\n"
"ds_read2st64_b32 v[184:185], v14 offset0:128 offset1:129\n"
"v_mfma_f32_16x16x32_bf16 v[40:43], v[196:199], v[60:63], v[40:43]\n"
"ds_read2st64_b32 v[196:197], v14 offset0:150 offset1:151\n"
"ds_read_b128 v[186:189], v17 offset:53248\n"
"v_mfma_f32_16x16x32_bf16 v[40:43], v[200:203], v[28:31], v[40:43]\n"
"v_rcp_f32 v16, v32\n"
"v_add_u32 v32, 0xd4800000, v32\n"
"v_mfma_f32_16x16x32_bf16 v[48:51], v[210:213], v[56:59], v[48:51]\n"
"ds_read2st64_b32 v[212:213], v14 offset0:174 offset1:175\n"
"ds_read_b128 v[208:211], v17 offset:54272\n"
"v_cmp_lt_u32 vcc, 0x28000000, v32\n"
"v_mfma_f32_16x16x32_bf16 v[48:51], v[222:225], v[60:63], v[48:51]\n"
"ds_read2st64_b32 v[198:199], v14 offset0:152 offset1:153\n"
"v_mul_f32 v40, v40, v16\n"
"v_mul_f32 v53, v53, v16\n"
"v_mfma_f32_16x16x32_bf16 v[48:51], v[226:229], v[28:31], v[48:51]\n"
"s_waitcnt lgkmcnt(5)\n"
"v_mul_f32 v52, v52, v16\n"
"v_mul_f32 v55, v55, v16\n"
"v_mfma_f32_16x16x32_bf16 v[222:225], v[96:99], v[182:185], 0\n"
"ds_read_b128 v[248:251], v17 offset:55296\n"
"ds_read2st64_b32 v[214:215], v14 offset0:176 offset1:177\n"
"v_mul_f32 v54, v54, v16\n"
"v_mfma_f32_16x16x32_bf16 v[234:237], v[100:103], v[182:185], 0\n"
"s_waitcnt lgkmcnt(5)\n"
"v_cvt_pk_bf16_f32 v241, v54, v55\n"
"v_mul_f32 v42, v42, v16\n"
"v_mfma_f32_16x16x32_bf16 v[24:27], v[186:189], v[182:185], 0\n"
"ds_read_b128 v[186:189], v17 offset:56320\n"
"ds_read2st64_b32 v[182:183], v14 offset0:198 offset1:199\n"
"v_cvt_pk_bf16_f32 v240, v52, v53\n"
"v_mul_f32 v41, v41, v16\n"
"s_waitcnt lgkmcnt(4)\n"
"v_mfma_f32_16x16x32_bf16 v[222:225], v[104:107], v[196:199], v[222:225]\n"
"ds_read2st64_b32 v[184:185], v14 offset0:200 offset1:201\n"
"v_mfma_f32_16x16x32_bf16 v[234:237], v[108:111], v[196:199], v[234:237]\n"
"s_or_b64 s[62:63], s[62:63], vcc\n"
"v_cvt_pk_bf16_f32 v40, v40, v41\n"
"v_mul_f32 v43, v43, v16\n"
"v_cvt_pk_bf16_f32 v41, v42, v43\n"
"v_mfma_f32_16x16x32_bf16 v[24:27], v[208:211], v[196:199], v[24:27]\n"
"s_waitcnt lgkmcnt(3)\n"
"ds_read2st64_b32 v[210:211], v14 offset0:202 offset1:203\n"
"ds_read2st64_b32 v[196:197], v14 offset0:130 offset1:131\n"
"v_mfma_f32_16x16x32_bf16 v[222:225], v[0:3], v[212:215], v[222:225]\n"
"ds_read2st64_b32 v[208:209], v14 offset0:178 offset1:179\n"
"v_mul_f32 v48, v48, v16\n"
"v_mfma_f32_16x16x32_bf16 v[234:237], v[112:115], v[212:215], v[234:237]\n"
"v_mul_f32 v51, v51, v16\n"
"v_mul_f32 v49, v49, v16\n"
"v_mfma_f32_16x16x32_bf16 v[24:27], v[248:251], v[212:215], v[24:27]\n"
"s_mov_b32 m0, s54\n"
"ds_read2st64_b32 v[198:199], v14 offset0:154 offset1:155\n"
"ds_write_addtid_b32 v40 offset:6144\n"
"v_mul_f32 v50, v50, v16\n"
"v_cvt_pk_bf16_f32 v42, v48, v49\n"
"s_waitcnt lgkmcnt(5)\n"
"v_mfma_f32_16x16x32_bf16 v[222:225], v[116:119], v[182:185], v[222:225]\n"
"s_setprio 0\n"
"ds_write_addtid_b32 v41 offset:6400\n"
"ds_write_addtid_b32 v42 offset:6656\n"
"ds_write_addtid_b32 v240 offset:7168\n"
"v_cvt_pk_bf16_f32 v43, v50, v51\n"
"v_mfma_f32_16x16x32_bf16 v[234:237], v[120:123], v[182:185], v[234:237]\n"
"ds_write_addtid_b32 v241 offset:7424\n"
"ds_write_addtid_b32 v43 offset:6912\n"
"v_mfma_f32_16x16x32_bf16 v[24:27], v[186:189], v[182:185], v[24:27]\n"
"s_waitcnt lgkmcnt(6)\n"
"v_mov_b32 v215, 0x3f803f80\n"
"v_mfma_f32_16x16x32_bf16 v[222:225], v[4:7], v[196:199], v[222:225]\n"
"v_mov_b32 v213, 0x3f803f80\n"
"v_mov_b32 v214, 0x3f803f80\n"
"ds_read_b128 v[182:185], v17 offset:57344\n"
"v_mfma_f32_16x16x32_bf16 v[234:237], v[124:127], v[196:199], v[234:237]\n"
"s_waitcnt lgkmcnt(1)\n"
"ds_read_b128 v[186:189], v17 offset:58368\n"
"s_barrier\n"
"ds_read2st64_b32 v[238:239], v9 offset0:112 offset1:113\n"
"s_waitcnt lgkmcnt(2)\n"
"v_mfma_f32_16x16x32_bf16 v[24:27], v[182:185], v[196:199], v[24:27]\n"
"ds_read2st64_b32 v[240:241], v9 offset0:114 offset1:115\n"
"v_subrev_u32 v16, 24576, v9\n"
"v_mfma_f32_16x16x32_bf16 v[222:225], v[128:131], v[208:211], v[222:225]\n"
"s_setprio 2\n"
"ds_read2st64_b32 v[30:31], v16 offset0:16 offset1:17\n"
"v_mov_b32 v212, 0x3f803f80\n"
"v_mfma_f32_16x16x32_bf16 v[234:237], v[132:135], v[208:211], v[234:237]\n"
"s_waitcnt lgkmcnt(3)\n"
"ds_read2st64_b32 v[184:185], v9 offset0:118 offset1:119\n"
"ds_read2st64_b32 v[182:183], v9 offset0:116 offset1:117\n"
"v_mfma_f32_16x16x32_bf16 v[24:27], v[186:189], v[208:211], v[24:27]\n"
"s_waitcnt lgkmcnt(3)\n"
"v_mfma_f32_16x16x32_bf16 v[186:189], v[212:215], v[238:241], 0\n"
"ds_read2st64_b32 v[32:33], v16 offset0:18 offset1:19\n"
"v_cmp_class_f32_e64 vcc, v222, s64\n"
"v_mfma_f32_16x16x32_bf16 v[196:199], v[190:193], v[238:241], 0\n"
"v_cvt_pk_bf16_f32 v222, v222, v223\n"
"v_cvt_pk_bf16_f32 v223, v224, v225\n"
"v_mfma_f32_16x16x32_bf16 v[208:211], v[216:219], v[238:241], 0\n"
"v_cvt_pk_bf16_f32 v224, v234, v235\n"
"v_cvt_pk_bf16_f32 v225, v236, v237\n"
"v_mfma_f32_16x16x32_bf16 v[234:237], v[242:245], v[238:241], 0\n"
"s_waitcnt lgkmcnt(1)\n"
"v_mov_b32 v29, 0\n"
"v_mov_b32 v28, 0\n"
"v_mfma_f32_16x16x32_bf16 v[186:189], v[212:215], v[182:185], v[186:189]\n"
"s_waitcnt lgkmcnt(0)\n"
"s_setprio 0\n"
"ds_read2st64_b32 v[190:191], v14 offset0:26 offset1:27\n"
"v_mfma_f32_16x16x32_bf16 v[186:189], v[212:215], v[30:33], v[186:189]\n"
"s_setprio 2\n"
"v_cvt_pk_bf16_f32 v24, v24, v25\n"
"v_mov_b32 v35, 0\n"
"v_mfma_f32_16x16x32_bf16 v[234:237], v[252:255], v[182:185], v[234:237]\n"
"v_cvt_pk_bf16_f32 v25, v26, v27\n"
"v_mov_b32 v34, 0\n"
"v_mfma_f32_16x16x32_bf16 v[234:237], v[244:247], v[28:31], v[234:237]\n"
"s_setprio 0\n"
"v_rcp_f32 v16, v186\n"
"v_mfma_f32_16x16x32_bf16 v[234:237], v[230:233], v[32:35], v[234:237]\n"
"v_add_u32 v186, 0xd4800000, v186\n"
"v_mfma_f32_16x16x32_bf16 v[196:199], v[200:203], v[182:185], v[196:199]\n"
"s_mov_b32 m0, s54\n"
"s_or_b64 s[62:63], s[62:63], vcc\n"
"ds_read2st64_b32 v[188:189], v14 offset0:24 offset1:25\n"
"v_cmp_lt_u32 vcc, 0x28000000, v186\n"
"v_mfma_f32_16x16x32_bf16 v[196:199], v[192:195], v[28:31], v[196:199]\n"
"ds_read_b128 v[192:195], v17 offset:53248\n"
"v_mul_f32 v235, v235, v16\n"
"v_mfma_f32_16x16x32_bf16 v[196:199], v[178:181], v[32:35], v[196:199]\n"
"v_mul_f32 v237, v237, v16\n"
"v_mul_f32 v234, v234, v16\n"
"v_cvt_pk_bf16_f32 v216, v234, v235\n"
"v_mfma_f32_16x16x32_bf16 v[208:211], v[226:229], v[182:185], v[208:211]\n"
"s_or_b64 s[62:63], s[62:63], vcc\n"
"ds_write_addtid_b32 v216 offset:33280\n"
"v_mul_f32 v236, v236, v16\n"
"v_cvt_pk_bf16_f32 v217, v236, v237\n"
"v_mfma_f32_16x16x32_bf16 v[208:211], v[218:221], v[28:31], v[208:211]\n"
"ds_write_addtid_b32 v217 offset:33536\n"
"v_mul_f32 v197, v197, v16\n"
"v_mfma_f32_16x16x32_bf16 v[208:211], v[204:207], v[32:35], v[208:211]\n"
"s_waitcnt lgkmcnt(3)\n"
"ds_read2st64_b32 v[180:181], v14 offset0:50 offset1:51\n"
"v_mul_f32 v196, v196, v16\n"
"v_cvt_pk_bf16_f32 v196, v196, v197\n"
"v_mfma_f32_16x16x32_bf16 v[182:185], v[96:99], v[188:191], 0\n"
"ds_read2st64_b32 v[178:179], v14 offset0:48 offset1:49\n"
"ds_write_addtid_b32 v196 offset:32256\n"
"v_mul_f32 v198, v198, v16\n"
"v_mul_f32 v199, v199, v16\n"
"v_mul_f32 v208, v208, v16\n"
"v_mul_f32 v210, v210, v16\n"
"v_cvt_pk_bf16_f32 v197, v198, v199\n"
"v_mul_f32 v211, v211, v16\n"
"v_cvt_pk_bf16_f32 v199, v210, v211\n"
"v_mfma_f32_16x16x32_bf16 v[200:203], v[100:103], v[188:191], 0\n"
"ds_write_addtid_b32 v199 offset:33024\n"
"ds_write_addtid_b32 v197 offset:32512\n"
"s_waitcnt lgkmcnt(7)\n"
"v_mfma_f32_16x16x32_bf16 v[204:207], v[192:195], v[188:191], 0\n"
"ds_read2st64_b32 v[192:193], v14 offset0:74 offset1:75\n"
"ds_read_b128 v[194:197], v17 offset:55296\n"
"ds_read2st64_b32 v[190:191], v14 offset0:72 offset1:73\n"
"ds_read_b128 v[186:189], v17 offset:54272\n"
"v_mul_f32 v209, v209, v16\n"
"v_cvt_pk_bf16_f32 v198, v208, v209\n"
"ds_write_addtid_b32 v198 offset:32768\n"
"s_waitcnt lgkmcnt(8)\n"
"v_mfma_f32_16x16x32_bf16 v[182:185], v[104:107], v[178:181], v[182:185]\n"
"s_mov_b32 m0, s68\n"
"s_nop 0\n"
"ds_write_addtid_b32 v24 offset:46080\n"
"ds_write_addtid_b32 v222 offset:45056\n"
"v_mfma_f32_16x16x32_bf16 v[200:203], v[108:111], v[178:181], v[200:203]\n"
"ds_read2st64_b32 v[208:209], v14 offset0:28 offset1:29\n"
"ds_write_addtid_b32 v225 offset:45824\n"
"s_waitcnt lgkmcnt(5)\n"
"v_mfma_f32_16x16x32_bf16 v[204:207], v[186:189], v[178:181], v[204:207]\n"
"ds_read2st64_b32 v[178:179], v14 offset0:96 offset1:97\n"
"ds_read2st64_b32 v[180:181], v14 offset0:98 offset1:99\n"
"ds_write_addtid_b32 v224 offset:45568\n"
"ds_read2st64_b32 v[210:211], v14 offset0:52 offset1:53\n"
"v_mfma_f32_16x16x32_bf16 v[182:185], v[0:3], v[190:193], v[182:185]\n"
"ds_read_b128 v[186:189], v17 offset:56320\n"
"ds_write_addtid_b32 v223 offset:45312\n"
"v_mfma_f32_16x16x32_bf16 v[200:203], v[112:115], v[190:193], v[200:203]\n"
"ds_read2st64_b32 v[214:215], v14 offset0:100 offset1:101\n"
"ds_write_addtid_b32 v25 offset:46336\n"
"v_mfma_f32_16x16x32_bf16 v[204:207], v[194:197], v[190:193], v[204:207]\n"
"s_waitcnt lgkmcnt(6)\n"
"v_mfma_f32_16x16x32_bf16 v[182:185], v[116:119], v[178:181], v[182:185]\n"
"ds_read2st64_b32 v[212:213], v14 offset0:76 offset1:77\n"
"v_mfma_f32_16x16x32_bf16 v[200:203], v[120:123], v[178:181], v[200:203]\n"
"s_waitcnt lgkmcnt(4)\n"
"v_mfma_f32_16x16x32_bf16 v[204:207], v[186:189], v[178:181], v[204:207]\n"
"ds_read_b128 v[194:197], v17 offset:58368\n"
"ds_read_b128 v[190:193], v17 offset:57344\n"
"v_mfma_f32_16x16x32_bf16 v[182:185], v[4:7], v[208:211], v[182:185]\n"
"s_waitcnt lgkmcnt(2)\n"
"ds_read_b128 v[186:189], v17 offset:53248\n"
"s_barrier\n"
"ds_read2st64_b32 v[178:179], v14 offset0:126 offset1:127\n"
"v_mfma_f32_16x16x32_bf16 v[200:203], v[124:127], v[208:211], v[200:203]\n"
"s_waitcnt lgkmcnt(2)\n"
"ds_read2st64_b32 v[180:181], v14 offset0:128 offset1:129\n"
"s_setprio 2\n"
"v_mfma_f32_16x16x32_bf16 v[204:207], v[190:193], v[208:211], v[204:207]\n"
"ds_read_u16 v16, v21 offset:1120\n"
"v_mfma_f32_16x16x32_bf16 v[182:185], v[128:131], v[212:215], v[182:185]\n"
"ds_read2st64_b32 v[192:193], v14 offset0:152 offset1:153\n"
"v_mfma_f32_16x16x32_bf16 v[200:203], v[132:135], v[212:215], v[200:203]\n"
"ds_read2st64_b32 v[190:191], v14 offset0:150 offset1:151\n"
"v_mfma_f32_16x16x32_bf16 v[204:207], v[194:197], v[212:215], v[204:207]\n"
"s_waitcnt lgkmcnt(3)\n"
"ds_read_b128 v[194:197], v17 offset:54272\n"
"ds_read2st64_b32 v[210:211], v14 offset0:176 offset1:177\n"
"v_mfma_f32_16x16x32_bf16 v[212:215], v[96:99], v[178:181], 0\n"
"ds_read2st64_b32 v[208:209], v14 offset0:174 offset1:175\n"
"v_cmp_class_f32_e64 vcc, v182, s64\n"
"v_mfma_f32_16x16x32_bf16 v[216:219], v[100:103], v[178:181], 0\n"
"s_waitcnt lgkmcnt(5)\n"
"v_mad_u32_u24 v21, v16, s40, v18\n"
"v_cvt_pk_bf16_f32 v182, v182, v183\n"
"v_mfma_f32_16x16x32_bf16 v[220:223], v[186:189], v[178:181], 0\n"
"s_waitcnt lgkmcnt(3)\n"
"ds_read2st64_b32 v[178:179], v20 offset0:0 offset1:1\n"
"v_cvt_pk_bf16_f32 v183, v184, v185\n"
"v_mfma_f32_16x16x32_bf16 v[212:215], v[104:107], v[190:193], v[212:215]\n"
"ds_read_b128 v[186:189], v17 offset:55296\n"
"v_cvt_pk_bf16_f32 v184, v200, v201\n"
"v_mfma_f32_16x16x32_bf16 v[216:219], v[108:111], v[190:193], v[216:219]\n"
"s_waitcnt lgkmcnt(4)\n"
"ds_read2st64_b32 v[180:181], v20 offset0:2 offset1:3\n"
"v_cvt_pk_bf16_f32 v185, v202, v203\n"
"v_mfma_f32_16x16x32_bf16 v[220:223], v[194:197], v[190:193], v[220:223]\n"
"s_waitcnt lgkmcnt(3)\n"
"ds_read2st64_b32 v[192:193], v14 offset0:200 offset1:201\n"
"v_mad_u32_u24 v16, v16, s40, v19\n"
"v_mfma_f32_16x16x32_bf16 v[212:215], v[0:3], v[208:211], v[212:215]\n"
"ds_read2st64_b32 v[190:191], v14 offset0:198 offset1:199\n"
"v_cvt_pk_bf16_f32 v204, v204, v205\n"
"v_mfma_f32_16x16x32_bf16 v[216:219], v[112:115], v[208:211], v[216:219]\n"
"s_mov_b32 m0, s68\n"
"ds_read_b128 v[196:199], v17 offset:56320\n"
"s_or_b64 s[62:63], s[62:63], vcc\n"
"ds_read2st64_b32 v[194:195], v20 offset0:4 offset1:5\n"
"s_waitcnt lgkmcnt(5)\n"
"v_mfma_f32_16x16x32_bf16 v[220:223], v[186:189], v[208:211], v[220:223]\n"
"s_waitcnt lgkmcnt(4)\n"
"ds_read2st64_b32 v[200:201], v14 offset0:130 offset1:131\n"
"ds_read2st64_b32 v[202:203], v14 offset0:154 offset1:155\n"
"global_store_dwordx4 v21, v[178:181], s[66:67] sc1\n"
"ds_read2st64_b32 v[186:187], v14 offset0:178 offset1:179\n"
"s_waitcnt lgkmcnt(5)\n"
"v_mfma_f32_16x16x32_bf16 v[212:215], v[116:119], v[190:193], v[212:215]\n"
"ds_read_b128 v[178:181], v17 offset:57344\n"
"v_cvt_pk_bf16_f32 v205, v206, v207\n"
"v_mfma_f32_16x16x32_bf16 v[216:219], v[120:123], v[190:193], v[216:219]\n"
"s_waitcnt lgkmcnt(5)\n"
"ds_read2st64_b32 v[188:189], v14 offset0:202 offset1:203\n"
"v_mfma_f32_16x16x32_bf16 v[220:223], v[196:199], v[190:193], v[220:223]\n"
"s_waitcnt lgkmcnt(5)\n"
"ds_read_b128 v[190:193], v17 offset:58368\n"
"global_store_dwordx2 v16, v[194:195], s[66:67] offset:256 sc1\n"
"s_waitcnt lgkmcnt(4)\n"
"v_mfma_f32_16x16x32_bf16 v[212:215], v[4:7], v[200:203], v[212:215]\n"
"ds_write_addtid_b32 v182 offset:51456\n"
"v_mfma_f32_16x16x32_bf16 v[216:219], v[124:127], v[200:203], v[216:219]\n"
"ds_write_addtid_b32 v184 offset:51968\n"
"s_waitcnt lgkmcnt(4)\n"
"v_mfma_f32_16x16x32_bf16 v[220:223], v[178:181], v[200:203], v[220:223]\n"
"ds_write_addtid_b32 v183 offset:51712\n"
"s_waitcnt lgkmcnt(4)\n"
"v_mfma_f32_16x16x32_bf16 v[212:215], v[128:131], v[186:189], v[212:215]\n"
"ds_write_addtid_b32 v185 offset:52224\n"
"ds_write_addtid_b32 v204 offset:52480\n"
"v_mfma_f32_16x16x32_bf16 v[216:219], v[132:135], v[186:189], v[216:219]\n"
"s_setprio 0\n"
"ds_write_addtid_b32 v205 offset:52736\n"
"s_waitcnt lgkmcnt(6)\n"
"v_mfma_f32_16x16x32_bf16 v[220:223], v[190:193], v[186:189], v[220:223]\n"
"s_nop 0\n"
"v_cmp_class_f32_e64 vcc, v212, s64\n"
"v_cvt_pk_bf16_f32 v212, v212, v213\n"
"v_cvt_pk_bf16_f32 v213, v214, v215\n"
"v_cvt_pk_bf16_f32 v214, v216, v217\n"
"v_cvt_pk_bf16_f32 v215, v218, v219\n"
"s_nop 0\n"
"s_or_b64 s[62:63], s[62:63], vcc\n"
"ds_write_addtid_b32 v214 offset:58368\n"
"ds_write_addtid_b32 v213 offset:58112\n"
"v_cvt_pk_bf16_f32 v220, v220, v221\n"
"v_cvt_pk_bf16_f32 v221, v222, v223\n"
"s_cmp_lg_u64 s[62:63], 0\n"
"s_cselect_b32 s41, 1, 0\n"
"v_mov_b32 v178, s65\n"
"v_mov_b32 v179, s41\n"
"ds_write_b32 v178, v179 offset:0\n"
"ds_write_addtid_b32 v212 offset:57856\n"
"ds_write_addtid_b32 v221 offset:59136\n"
"ds_write_addtid_b32 v215 offset:58624\n"
"ds_write_addtid_b32 v220 offset:58880\n"
"s_waitcnt lgkmcnt(0)\n"
"s_barrier\n"
"ds_read2st64_b32 v[180:181], v20 offset0:27 offset1:28\n"
"ds_read2st64_b32 v[178:179], v20 offset0:25 offset1:26\n"
"ds_read2st64_b32 v[182:183], v20 offset0:29 offset1:30\n"
"ds_read2st64_b32 v[184:185], v20 offset0:50 offset1:51\n"
"ds_read2st64_b32 v[254:255], v20 offset0:54 offset1:55\n"
"s_waitcnt lgkmcnt(3)\n"
"global_store_dwordx4 v177, v[178:181], s[66:67] sc1\n"
"s_waitcnt lgkmcnt(2)\n"
"global_store_dwordx2 v23, v[182:183], s[66:67] offset:256 sc1\n"
"ds_read2st64_b32 v[186:187], v20 offset0:52 offset1:53\n"
"s_waitcnt lgkmcnt(0)\n"
"global_store_dwordx4 v176, v[184:187], s[66:67] sc1\n"
"global_store_dwordx2 v22, v[254:255], s[66:67] offset:256 sc1\n"
    );
    PEND;
  }
  const u32x4 fl = *LP(const u32x4, FLAG);
  if (__builtin_expect(UNI(fl[0] | fl[1] | fl[2] | fl[3]) != 0, 0)) {
    asm volatile("s_waitcnt vmcnt(0) lgkmcnt(0)\n\ts_barrier" ::: "memory");
    __builtin_amdgcn_s_setprio(0);
    Regs R;
    int w2 = w;
    asm volatile("" : "+s"(w2), "+s"(G.t0), "+s"(G.sr), "+s"(G.sc));
    prologue(x, wqkv, wo, rpb, out, G, w2, R);
    BODY(
"s_add_u32 s50, s54, 28672\n"
"s_bitcmp1_b32 s45, 0\n"
"s_cselect_b32 s53, -1, 0\n"
"s_lshl_b32 s52, s53, 16\n"
"s_setprio 1\n"
"s_mov_b32 s60, 0x3e553b95\n"
"s_mov_b32 s61, 0x3fb8aa3b\n"
"v_add_u32 v27, 77824, v3\n"
"v_mov_b32 v24, 0\n"
"v_mov_b32 v25, 0\n"
"v_mov_b32 v178, 0xf1800000\n"
"v_mov_b32 v179, 0xf1800000\n"
"v_mov_b32 v180, 0xf1800000\n"
"v_mov_b32 v181, 0xf1800000\n"
"s_bfm_b64 exec, 18, 0\n"
"ds_write_b128 v9, v[178:181] offset:0\n"
"s_mov_b64 exec, -1\n"
"s_waitcnt vmcnt(5)\n"
"ds_read_u16 v13, v13 offset:0\n"
"ds_read_u16 v14, v14 offset:0\n"
"ds_read_u16 v15, v15 offset:0\n"
"ds_read_u16 v16, v16 offset:0\n"
"ds_read_u16 v17, v17 offset:0\n"
"ds_read_u16 v18, v18 offset:0\n"
"ds_read_u16 v12, v12 offset:0\n"
"ds_read_u16 v20, v20 offset:0\n"
"ds_read_u16 v21, v21 offset:0\n"
"ds_read_u16 v22, v22 offset:0\n"
"ds_read_u16 v23, v23 offset:0\n"
"s_waitcnt lgkmcnt(10)\n"
"v_lshlrev_b32 v13, 16, v13\n"
"s_waitcnt lgkmcnt(9)\n"
"v_lshlrev_b32 v14, 16, v14\n"
"v_mul_f32 v13, s61, v13\n"
"s_waitcnt lgkmcnt(8)\n"
"v_lshlrev_b32 v15, 16, v15\n"
"v_mul_f32 v14, s61, v14\n"
"s_waitcnt lgkmcnt(7)\n"
"v_lshlrev_b32 v16, 16, v16\n"
"v_mul_f32 v15, s61, v15\n"
"s_waitcnt lgkmcnt(6)\n"
"v_lshlrev_b32 v17, 16, v17\n"
"v_mul_f32 v16, s61, v16\n"
"s_waitcnt lgkmcnt(5)\n"
"v_lshlrev_b32 v18, 16, v18\n"
"v_mul_f32 v17, s61, v17\n"
"s_waitcnt lgkmcnt(4)\n"
"v_lshlrev_b32 v12, 16, v12\n"
"v_mul_f32 v18, s61, v18\n"
"s_waitcnt lgkmcnt(3)\n"
"v_lshlrev_b32 v20, 16, v20\n"
"v_mul_f32 v12, s61, v12\n"
"s_waitcnt lgkmcnt(2)\n"
"v_lshlrev_b32 v21, 16, v21\n"
"v_mul_f32 v20, s61, v20\n"
"s_waitcnt lgkmcnt(1)\n"
"v_lshlrev_b32 v22, 16, v22\n"
"v_mul_f32 v21, s61, v21\n"
"s_waitcnt lgkmcnt(0)\n"
"v_lshlrev_b32 v23, 16, v23\n"
"v_mul_f32 v22, s61, v22\n"
"v_mul_f32 v23, s61, v23\n"
"v_mov_b32 v178, v13\n"
"v_mov_b32 v179, v14\n"
"v_mov_b32 v180, v15\n"
"v_mov_b32 v181, v16\n"
"v_mov_b32 v182, v17\n"
"v_mov_b32 v183, v18\n"
"s_bfm_b64 exec, 40, 0\n"
"ds_write_b128 v8, v[180:183] offset:0\n"
"ds_write_b128 v8, v[14:17] offset:64\n"
"ds_write_b128 v8, v[178:181] offset:128\n"
"ds_write_b128 v8, v[12:15] offset:192\n"
"ds_write_b128 v8, v[20:23] offset:256\n"
"s_mov_b64 exec, -1\n"
"s_waitcnt lgkmcnt(0)\n"
"s_add_u32 s42, s46, 37248\n"
"s_addc_u32 s43, s47, 0\n"
"s_add_u32 m0, s50, 4294945792\n"
"s_nop 0\n"
"global_load_lds_dwordx4 v0, s[42:43]\n"
"s_add_u32 m0, s50, 4294946816\n"
"s_nop 0\n"
"global_load_lds_dwordx4 v1, s[42:43]\n"
"s_add_u32 m0, s50, 4294947840\n"
"s_nop 0\n"
"global_load_lds_dwordx4 v2, s[42:43]\n"
"s_add_u32 s42, s46, 74112\n"
"s_addc_u32 s43, s47, 0\n"
"s_add_u32 m0, s50, 49152\n"
"s_nop 0\n"
"global_load_lds_dwordx4 v0, s[42:43]\n"
"s_add_u32 m0, s50, 50176\n"
"s_nop 0\n"
"global_load_lds_dwordx4 v1, s[42:43]\n"
"s_add_u32 m0, s50, 51200\n"
"s_nop 0\n"
"global_load_lds_dwordx4 v2, s[42:43]\n"
"s_barrier\n"
"s_waitcnt vmcnt(6)\n"
"ds_read_b64_tr_b16 v[8:9], v3 offset:4096\n"
"ds_read_b64_tr_b16 v[10:11], v3 offset:4864\n"
"ds_read_b128 v[178:181], v4 offset:24576\n"
"ds_read_b64_tr_b16 v[12:13], v3 offset:4128\n"
"ds_read_b64_tr_b16 v[14:15], v3 offset:4896\n"
"ds_read_b64_tr_b16 v[16:17], v3 offset:4160\n"
"ds_read_b64_tr_b16 v[18:19], v3 offset:4928\n"
"ds_read_b128 v[182:185], v4 offset:30720\n"
"ds_read_b128 v[186:189], v4 offset:36864\n"
"s_waitcnt lgkmcnt(6)\n"
"v_mfma_f32_16x16x32_bf16 v[196:199], v[8:11], v[178:181], 0\n"
"s_waitcnt lgkmcnt(4)\n"
"v_mfma_f32_16x16x32_bf16 v[190:193], v[12:15], v[178:181], 0\n"
"s_waitcnt lgkmcnt(2)\n"
"v_mfma_f32_16x16x32_bf16 v[212:215], v[16:19], v[178:181], 0\n"
"s_waitcnt lgkmcnt(1)\n"
"ds_read_b128 v[216:219], v4 offset:43008\n"
"v_mfma_f32_16x16x32_bf16 v[202:205], v[8:11], v[182:185], 0\n"
"v_mfma_f32_16x16x32_bf16 v[220:223], v[12:15], v[182:185], 0\n"
"v_mfma_f32_16x16x32_bf16 v[236:239], v[16:19], v[182:185], 0\n"
"s_waitcnt lgkmcnt(1)\n"
"ds_read_b128 v[240:243], v4 offset:49152\n"
"v_mfma_f32_16x16x32_bf16 v[226:229], v[8:11], v[186:189], 0\n"
"v_mfma_f32_16x16x32_bf16 v[244:247], v[12:15], v[186:189], 0\n"
"v_mfma_f32_16x16x32_bf16 v[248:251], v[16:19], v[186:189], 0\n"
"s_waitcnt lgkmcnt(1)\n"
"ds_read_b128 v[252:255], v4 offset:55296\n"
"v_mfma_f32_16x16x32_bf16 v[232:235], v[8:11], v[216:219], 0\n"
"v_mfma_f32_16x16x32_bf16 v[182:185], v[12:15], v[216:219], 0\n"
"v_mfma_f32_16x16x32_bf16 v[84:87], v[16:19], v[216:219], 0\n"
"s_waitcnt vmcnt(3)\n"
"ds_read_b64_tr_b16 v[20:21], v3 offset:7168\n"
"s_waitcnt lgkmcnt(2)\n"
"v_mfma_f32_16x16x32_bf16 v[208:211], v[8:11], v[240:243], 0\n"
"ds_read_b64_tr_b16 v[22:23], v3 offset:7936\n"
"v_mfma_f32_16x16x32_bf16 v[186:189], v[12:15], v[240:243], 0\n"
"ds_read_b128 v[36:39], v5 offset:24576\n"
"v_mfma_f32_16x16x32_bf16 v[216:219], v[16:19], v[240:243], 0\n"
"s_waitcnt lgkmcnt(3)\n"
"ds_read_b64_tr_b16 v[28:29], v3 offset:7200\n"
"v_mfma_f32_16x16x32_bf16 v[178:181], v[8:11], v[252:255], 0\n"
"ds_read_b64_tr_b16 v[30:31], v3 offset:7968\n"
"v_mfma_f32_16x16x32_bf16 v[88:91], v[12:15], v[252:255], 0\n"
"ds_read_b64_tr_b16 v[32:33], v3 offset:7232\n"
"ds_read_b64_tr_b16 v[34:35], v3 offset:8000\n"
"v_mfma_f32_16x16x32_bf16 v[240:243], v[16:19], v[252:255], 0\n"
"s_waitcnt lgkmcnt(4)\n"
"ds_read_b128 v[40:43], v5 offset:30720\n"
"ds_read_b128 v[44:47], v5 offset:36864\n"
"v_mfma_f32_16x16x32_bf16 v[196:199], v[20:23], v[36:39], v[196:199]\n"
"s_waitcnt lgkmcnt(4)\n"
"v_mfma_f32_16x16x32_bf16 v[190:193], v[28:31], v[36:39], v[190:193]\n"
"s_waitcnt lgkmcnt(2)\n"
"v_mfma_f32_16x16x32_bf16 v[212:215], v[32:35], v[36:39], v[212:215]\n"
"s_waitcnt lgkmcnt(1)\n"
"ds_read_b128 v[48:51], v5 offset:43008\n"
"v_mfma_f32_16x16x32_bf16 v[202:205], v[20:23], v[40:43], v[202:205]\n"
"v_mfma_f32_16x16x32_bf16 v[220:223], v[28:31], v[40:43], v[220:223]\n"
"v_mfma_f32_16x16x32_bf16 v[236:239], v[32:35], v[40:43], v[236:239]\n"
"s_waitcnt lgkmcnt(1)\n"
"ds_read_b128 v[252:255], v5 offset:49152\n"
"v_mfma_f32_16x16x32_bf16 v[226:229], v[20:23], v[44:47], v[226:229]\n"
"v_mfma_f32_16x16x32_bf16 v[244:247], v[28:31], v[44:47], v[244:247]\n"
"v_mfma_f32_16x16x32_bf16 v[248:251], v[32:35], v[44:47], v[248:251]\n"
"s_waitcnt lgkmcnt(1)\n"
"ds_read_b128 v[52:55], v5 offset:55296\n"
"v_mfma_f32_16x16x32_bf16 v[232:235], v[20:23], v[48:51], v[232:235]\n"
"v_mfma_f32_16x16x32_bf16 v[182:185], v[28:31], v[48:51], v[182:185]\n"
"v_mfma_f32_16x16x32_bf16 v[84:87], v[32:35], v[48:51], v[84:87]\n"
"s_waitcnt vmcnt(0)\n"
"ds_read_b64_tr_b16 v[36:37], v27 offset:0\n"
"s_waitcnt lgkmcnt(2)\n"
"v_mfma_f32_16x16x32_bf16 v[208:211], v[20:23], v[252:255], v[208:211]\n"
"ds_read_b64_tr_b16 v[38:39], v27 offset:768\n"
"v_mfma_f32_16x16x32_bf16 v[186:189], v[28:31], v[252:255], v[186:189]\n"
"ds_read_b128 v[56:59], v4 offset:26624\n"
"v_mfma_f32_16x16x32_bf16 v[216:219], v[32:35], v[252:255], v[216:219]\n"
"s_waitcnt lgkmcnt(3)\n"
"ds_read_b64_tr_b16 v[40:41], v27 offset:32\n"
"v_mfma_f32_16x16x32_bf16 v[178:181], v[20:23], v[52:55], v[178:181]\n"
"ds_read_b64_tr_b16 v[42:43], v27 offset:800\n"
"v_mfma_f32_16x16x32_bf16 v[88:91], v[28:31], v[52:55], v[88:91]\n"
"ds_read_b64_tr_b16 v[44:45], v27 offset:64\n"
"ds_read_b64_tr_b16 v[46:47], v27 offset:832\n"
"v_mfma_f32_16x16x32_bf16 v[240:243], v[32:35], v[52:55], v[240:243]\n"
"s_waitcnt lgkmcnt(4)\n"
"ds_read_b128 v[252:255], v4 offset:32768\n"
"ds_read_b128 v[60:63], v4 offset:38912\n"
"v_mfma_f32_16x16x32_bf16 v[196:199], v[36:39], v[56:59], v[196:199]\n"
"s_waitcnt lgkmcnt(4)\n"
"v_mfma_f32_16x16x32_bf16 v[190:193], v[40:43], v[56:59], v[190:193]\n"
"s_waitcnt lgkmcnt(2)\n"
"v_mfma_f32_16x16x32_bf16 v[212:215], v[44:47], v[56:59], v[212:215]\n"
"s_waitcnt lgkmcnt(1)\n"
"ds_read_b128 v[64:67], v4 offset:45056\n"
"v_mfma_f32_16x16x32_bf16 v[202:205], v[36:39], v[252:255], v[202:205]\n"
"v_mfma_f32_16x16x32_bf16 v[220:223], v[40:43], v[252:255], v[220:223]\n"
"v_mfma_f32_16x16x32_bf16 v[236:239], v[44:47], v[252:255], v[236:239]\n"
"s_waitcnt lgkmcnt(1)\n"
"ds_read_b128 v[68:71], v4 offset:51200\n"
"v_mfma_f32_16x16x32_bf16 v[226:229], v[36:39], v[60:63], v[226:229]\n"
"v_mfma_f32_16x16x32_bf16 v[244:247], v[40:43], v[60:63], v[244:247]\n"
"v_mfma_f32_16x16x32_bf16 v[248:251], v[44:47], v[60:63], v[248:251]\n"
"s_waitcnt lgkmcnt(1)\n"
"ds_read_b128 v[72:75], v4 offset:57344\n"
"v_mfma_f32_16x16x32_bf16 v[232:235], v[36:39], v[64:67], v[232:235]\n"
"v_mfma_f32_16x16x32_bf16 v[182:185], v[40:43], v[64:67], v[182:185]\n"
"v_mfma_f32_16x16x32_bf16 v[84:87], v[44:47], v[64:67], v[84:87]\n"
"s_barrier\n"
"ds_read_b64_tr_b16 v[48:49], v27 offset:3072\n"
"s_waitcnt lgkmcnt(2)\n"
"v_mfma_f32_16x16x32_bf16 v[208:211], v[36:39], v[68:71], v[208:211]\n"
"ds_read_b64_tr_b16 v[50:51], v27 offset:3840\n"
"v_mfma_f32_16x16x32_bf16 v[186:189], v[40:43], v[68:71], v[186:189]\n"
"ds_read_b128 v[252:255], v5 offset:26624\n"
"v_mfma_f32_16x16x32_bf16 v[216:219], v[44:47], v[68:71], v[216:219]\n"
"s_waitcnt lgkmcnt(3)\n"
"ds_read_b64_tr_b16 v[52:53], v27 offset:3104\n"
"v_mfma_f32_16x16x32_bf16 v[178:181], v[36:39], v[72:75], v[178:181]\n"
"ds_read_b64_tr_b16 v[54:55], v27 offset:3872\n"
"v_mfma_f32_16x16x32_bf16 v[88:91], v[40:43], v[72:75], v[88:91]\n"
"ds_read_b64_tr_b16 v[56:57], v27 offset:3136\n"
"ds_read_b64_tr_b16 v[58:59], v27 offset:3904\n"
"v_mfma_f32_16x16x32_bf16 v[240:243], v[44:47], v[72:75], v[240:243]\n"
"s_waitcnt lgkmcnt(4)\n"
"ds_read_b128 v[60:63], v5 offset:32768\n"
"ds_read_b128 v[64:67], v5 offset:38912\n"
"v_mfma_f32_16x16x32_bf16 v[196:199], v[48:51], v[252:255], v[196:199]\n"
"s_waitcnt lgkmcnt(4)\n"
"v_mfma_f32_16x16x32_bf16 v[190:193], v[52:55], v[252:255], v[190:193]\n"
"s_waitcnt lgkmcnt(2)\n"
"v_mfma_f32_16x16x32_bf16 v[212:215], v[56:59], v[252:255], v[212:215]\n"
"s_waitcnt lgkmcnt(1)\n"
"ds_read_b128 v[68:71], v5 offset:45056\n"
"v_mfma_f32_16x16x32_bf16 v[202:205], v[48:51], v[60:63], v[202:205]\n"
"v_mfma_f32_16x16x32_bf16 v[220:223], v[52:55], v[60:63], v[220:223]\n"
"v_mfma_f32_16x16x32_bf16 v[236:239], v[56:59], v[60:63], v[236:239]\n"
"s_waitcnt lgkmcnt(1)\n"
"ds_read_b128 v[72:75], v5 offset:51200\n"
"v_mfma_f32_16x16x32_bf16 v[226:229], v[48:51], v[64:67], v[226:229]\n"
"v_mfma_f32_16x16x32_bf16 v[244:247], v[52:55], v[64:67], v[244:247]\n"
"v_mfma_f32_16x16x32_bf16 v[248:251], v[56:59], v[64:67], v[248:251]\n"
"s_waitcnt lgkmcnt(1)\n"
"ds_read_b128 v[252:255], v5 offset:57344\n"
"v_mfma_f32_16x16x32_bf16 v[232:235], v[48:51], v[68:71], v[232:235]\n"
"v_mfma_f32_16x16x32_bf16 v[182:185], v[52:55], v[68:71], v[182:185]\n"
"v_mfma_f32_16x16x32_bf16 v[84:87], v[56:59], v[68:71], v[84:87]\n"
"s_barrier\n"
"ds_read_b64_tr_b16 v[60:61], v27 offset:24576\n"
"s_waitcnt lgkmcnt(2)\n"
"v_mfma_f32_16x16x32_bf16 v[208:211], v[48:51], v[72:75], v[208:211]\n"
"ds_read_b64_tr_b16 v[62:63], v27 offset:25344\n"
"v_mfma_f32_16x16x32_bf16 v[186:189], v[52:55], v[72:75], v[186:189]\n"
"ds_read_b128 v[92:95], v4 offset:28672\n"
"v_mfma_f32_16x16x32_bf16 v[216:219], v[56:59], v[72:75], v[216:219]\n"
"s_waitcnt lgkmcnt(3)\n"
"ds_read_b64_tr_b16 v[64:65], v27 offset:24608\n"
"v_mfma_f32_16x16x32_bf16 v[178:181], v[48:51], v[252:255], v[178:181]\n"
"ds_read_b64_tr_b16 v[66:67], v27 offset:25376\n"
"v_mfma_f32_16x16x32_bf16 v[88:91], v[52:55], v[252:255], v[88:91]\n"
"ds_read_b64_tr_b16 v[68:69], v27 offset:24640\n"
"ds_read_b64_tr_b16 v[70:71], v27 offset:25408\n"
"v_mfma_f32_16x16x32_bf16 v[240:243], v[56:59], v[252:255], v[240:243]\n"
"s_waitcnt lgkmcnt(4)\n"
"ds_read_b128 v[96:99], v4 offset:34816\n"
"ds_read_b128 v[100:103], v4 offset:40960\n"
"v_mfma_f32_16x16x32_bf16 v[196:199], v[60:63], v[92:95], v[196:199]\n"
"s_waitcnt lgkmcnt(4)\n"
"ds_read_b64_tr_b16 v[72:73], v27 offset:27648\n"
"ds_read_b64_tr_b16 v[74:75], v27 offset:28416\n"
"ds_read_b64_tr_b16 v[76:77], v27 offset:27680\n"
"v_mfma_f32_16x16x32_bf16 v[190:193], v[64:67], v[92:95], v[190:193]\n"
"s_waitcnt lgkmcnt(5)\n"
"ds_read_b64_tr_b16 v[78:79], v27 offset:28448\n"
"ds_read_b64_tr_b16 v[80:81], v27 offset:27712\n"
"v_mfma_f32_16x16x32_bf16 v[212:215], v[68:71], v[92:95], v[212:215]\n"
"s_waitcnt lgkmcnt(6)\n"
"ds_read_b64_tr_b16 v[82:83], v27 offset:28480\n"
"v_mfma_f32_16x16x32_bf16 v[202:205], v[60:63], v[96:99], v[202:205]\n"
"ds_read_b128 v[252:255], v4 offset:47104\n"
"v_mfma_f32_16x16x32_bf16 v[220:223], v[64:67], v[96:99], v[220:223]\n"
"v_mfma_f32_16x16x32_bf16 v[236:239], v[68:71], v[96:99], v[236:239]\n"
"s_waitcnt lgkmcnt(7)\n"
"ds_read_b128 v[104:107], v5 offset:28672\n"
"v_mfma_f32_16x16x32_bf16 v[226:229], v[60:63], v[100:103], v[226:229]\n"
"ds_read_b128 v[92:95], v5 offset:34816\n"
"v_mfma_f32_16x16x32_bf16 v[244:247], v[64:67], v[100:103], v[244:247]\n"
"s_waitcnt lgkmcnt(3)\n"
"ds_read_b128 v[108:111], v5 offset:40960\n"
"s_barrier\n"
"ds_read_b128 v[96:99], v5 offset:47104\n"
"v_mfma_f32_16x16x32_bf16 v[248:251], v[68:71], v[100:103], v[248:251]\n"
"s_waitcnt lgkmcnt(4)\n"
"ds_read_b128 v[112:115], v4 offset:53248\n"
"ds_read_b128 v[116:119], v5 offset:53248\n"
"v_mfma_f32_16x16x32_bf16 v[232:235], v[60:63], v[252:255], v[232:235]\n"
"ds_read_b128 v[120:123], v4 offset:59392\n"
"ds_read_b128 v[124:127], v5 offset:59392\n"
"v_mfma_f32_16x16x32_bf16 v[182:185], v[64:67], v[252:255], v[182:185]\n"
"v_mfma_f32_16x16x32_bf16 v[84:87], v[68:71], v[252:255], v[84:87]\n"
"s_waitcnt lgkmcnt(3)\n"
"v_mfma_f32_16x16x32_bf16 v[208:211], v[60:63], v[112:115], v[208:211]\n"
"v_mfma_f32_16x16x32_bf16 v[186:189], v[64:67], v[112:115], v[186:189]\n"
"v_mfma_f32_16x16x32_bf16 v[216:219], v[68:71], v[112:115], v[216:219]\n"
"s_waitcnt lgkmcnt(1)\n"
"v_mfma_f32_16x16x32_bf16 v[178:181], v[60:63], v[120:123], v[178:181]\n"
"v_mfma_f32_16x16x32_bf16 v[88:91], v[64:67], v[120:123], v[88:91]\n"
"v_mfma_f32_16x16x32_bf16 v[240:243], v[68:71], v[120:123], v[240:243]\n"
"v_mfma_f32_16x16x32_bf16 v[196:199], v[72:75], v[104:107], v[196:199]\n"
"v_mfma_f32_16x16x32_bf16 v[190:193], v[76:79], v[104:107], v[190:193]\n"
"v_mfma_f32_16x16x32_bf16 v[212:215], v[80:83], v[104:107], v[212:215]\n"
"v_mfma_f32_16x16x32_bf16 v[202:205], v[72:75], v[92:95], v[202:205]\n"
"v_mfma_f32_16x16x32_bf16 v[220:223], v[76:79], v[92:95], v[220:223]\n"
"v_mfma_f32_16x16x32_bf16 v[236:239], v[80:83], v[92:95], v[236:239]\n"
"v_mfma_f32_16x16x32_bf16 v[226:229], v[72:75], v[108:111], v[226:229]\n"
"v_mfma_f32_16x16x32_bf16 v[244:247], v[76:79], v[108:111], v[244:247]\n"
"v_mfma_f32_16x16x32_bf16 v[248:251], v[80:83], v[108:111], v[248:251]\n"
"v_cvt_pk_bf16_f32 v196, v196, v197\n"
"v_cvt_pk_bf16_f32 v197, v198, v199\n"
"v_cvt_pk_bf16_f32 v198, v190, v191\n"
"v_cvt_pk_bf16_f32 v199, v192, v193\n"
"v_cvt_pk_bf16_f32 v200, v212, v213\n"
"v_cvt_pk_bf16_f32 v201, v214, v215\n"
"v_cvt_pk_bf16_f32 v202, v202, v203\n"
"v_cvt_pk_bf16_f32 v203, v204, v205\n"
"v_cvt_pk_bf16_f32 v204, v220, v221\n"
"v_cvt_pk_bf16_f32 v205, v222, v223\n"
"v_cvt_pk_bf16_f32 v206, v236, v237\n"
"v_cvt_pk_bf16_f32 v207, v238, v239\n"
"v_cvt_pk_bf16_f32 v226, v226, v227\n"
"v_cvt_pk_bf16_f32 v227, v228, v229\n"
"v_mfma_f32_16x16x32_bf16 v[232:235], v[72:75], v[96:99], v[232:235]\n"
"v_cvt_pk_bf16_f32 v228, v244, v245\n"
"v_cvt_pk_bf16_f32 v229, v246, v247\n"
"v_mfma_f32_16x16x32_bf16 v[182:185], v[76:79], v[96:99], v[182:185]\n"
"v_cvt_pk_bf16_f32 v230, v248, v249\n"
"v_cvt_pk_bf16_f32 v231, v250, v251\n"
"v_mfma_f32_16x16x32_bf16 v[84:87], v[80:83], v[96:99], v[84:87]\n"
"v_mfma_f32_16x16x32_bf16 v[208:211], v[72:75], v[116:119], v[208:211]\n"
"v_mfma_f32_16x16x32_bf16 v[186:189], v[76:79], v[116:119], v[186:189]\n"
"v_cvt_pk_bf16_f32 v232, v232, v233\n"
"v_cvt_pk_bf16_f32 v233, v234, v235\n"
"v_mfma_f32_16x16x32_bf16 v[216:219], v[80:83], v[116:119], v[216:219]\n"
"s_waitcnt lgkmcnt(0)\n"
"v_cvt_pk_bf16_f32 v234, v182, v183\n"
"v_cvt_pk_bf16_f32 v235, v184, v185\n"
"v_mfma_f32_16x16x32_bf16 v[178:181], v[72:75], v[124:127], v[178:181]\n"
"v_cvt_pk_bf16_f32 v236, v84, v85\n"
"v_cvt_pk_bf16_f32 v237, v86, v87\n"
"v_cvt_pk_bf16_f32 v208, v208, v209\n"
"v_cvt_pk_bf16_f32 v209, v210, v211\n"
"v_cvt_pk_bf16_f32 v210, v186, v187\n"
"v_cvt_pk_bf16_f32 v211, v188, v189\n"
"v_mfma_f32_16x16x32_bf16 v[88:91], v[76:79], v[124:127], v[88:91]\n"
"v_cvt_pk_bf16_f32 v212, v216, v217\n"
"v_cvt_pk_bf16_f32 v213, v218, v219\n"
"v_mfma_f32_16x16x32_bf16 v[240:243], v[80:83], v[124:127], v[240:243]\n"
"s_barrier\n"
"s_add_u32 s42, s46, 0\n"
"s_addc_u32 s43, s47, 0\n"
"s_add_u32 m0, s50, 24576\n"
"s_nop 0\n"
"global_load_lds_dwordx4 v0, s[42:43]\n"
"s_add_u32 m0, s50, 25600\n"
"s_nop 0\n"
"global_load_lds_dwordx4 v1, s[42:43]\n"
"s_add_u32 m0, s50, 26624\n"
"s_nop 0\n"
"global_load_lds_dwordx4 v2, s[42:43]\n"
"s_barrier\n"
"s_add_u32 s42, s46, 36864\n"
"s_addc_u32 s43, s47, 0\n"
"s_add_u32 m0, s50, 27648\n"
"s_nop 0\n"
"global_load_lds_dwordx4 v0, s[42:43]\n"
"s_add_u32 m0, s50, 28672\n"
"s_nop 0\n"
"global_load_lds_dwordx4 v1, s[42:43]\n"
"s_add_u32 m0, s50, 29696\n"
"s_nop 0\n"
"global_load_lds_dwordx4 v2, s[42:43]\n"
"s_add_u32 s42, s46, 73728\n"
"s_addc_u32 s43, s47, 0\n"
"s_add_u32 m0, s50, 4294942720\n"
"s_nop 0\n"
"global_load_lds_dwordx4 v0, s[42:43]\n"
"s_add_u32 m0, s50, 4294943744\n"
"s_nop 0\n"
"global_load_lds_dwordx4 v1, s[42:43]\n"
"s_add_u32 m0, s50, 4294944768\n"
"s_nop 0\n"
"global_load_lds_dwordx4 v2, s[42:43]\n"
"s_add_u32 s42, s46, 110592\n"
"s_addc_u32 s43, s47, 0\n"
"s_add_u32 m0, s50, 4294945792\n"
"s_nop 0\n"
"global_load_lds_dwordx4 v0, s[42:43]\n"
"s_add_u32 m0, s50, 4294946816\n"
"s_nop 0\n"
"global_load_lds_dwordx4 v1, s[42:43]\n"
"s_add_u32 m0, s50, 4294947840\n"
"s_nop 0\n"
"global_load_lds_dwordx4 v2, s[42:43]\n"
"s_add_u32 s42, s46, 147456\n"
"s_addc_u32 s43, s47, 0\n"
"s_add_u32 m0, s50, 49152\n"
"s_nop 0\n"
"global_load_lds_dwordx4 v0, s[42:43]\n"
"s_add_u32 m0, s50, 50176\n"
"s_nop 0\n"
"global_load_lds_dwordx4 v1, s[42:43]\n"
"s_add_u32 m0, s50, 51200\n"
"s_nop 0\n"
"global_load_lds_dwordx4 v2, s[42:43]\n"
"s_add_u32 s42, s46, 184320\n"
"s_addc_u32 s43, s47, 0\n"
"s_add_u32 m0, s50, 52224\n"
"s_nop 0\n"
"global_load_lds_dwordx4 v0, s[42:43]\n"
"s_add_u32 m0, s50, 53248\n"
"s_nop 0\n"
"global_load_lds_dwordx4 v1, s[42:43]\n"
"s_add_u32 m0, s50, 54272\n"
"s_nop 0\n"
"global_load_lds_dwordx4 v2, s[42:43]\n"
"s_barrier\n"
"ds_read_b128 v[184:187], v4 offset:6144\n"
"s_waitcnt vmcnt(15)\n"
"ds_read_b128 v[188:191], v5 offset:6144\n"
"s_waitcnt vmcnt(12)\n"
"v_cvt_pk_bf16_f32 v178, v178, v179\n"
"v_cvt_pk_bf16_f32 v179, v180, v181\n"
"v_cvt_pk_bf16_f32 v180, v88, v89\n"
"v_cvt_pk_bf16_f32 v181, v90, v91\n"
"v_cvt_pk_bf16_f32 v182, v240, v241\n"
"v_cvt_pk_bf16_f32 v183, v242, v243\n"
"ds_read_b64_tr_b16 v[84:85], v3 offset:53248\n"
"s_sub_u32 m0, s50, 28672\n"
"ds_read_b64_tr_b16 v[86:87], v3 offset:54016\n"
"ds_read_b64_tr_b16 v[88:89], v3 offset:53280\n"
"ds_read_b64_tr_b16 v[90:91], v3 offset:54048\n"
"s_waitcnt lgkmcnt(5)\n"
"v_mfma_f32_16x16x32_bf16 v[238:241], v[8:11], v[184:187], 0\n"
"ds_read_b64_tr_b16 v[92:93], v3 offset:53312\n"
"v_mfma_f32_16x16x32_bf16 v[192:195], v[12:15], v[184:187], 0\n"
"ds_read_b128 v[214:217], v4 offset:8192\n"
"v_mfma_f32_16x16x32_bf16 v[218:221], v[16:19], v[184:187], 0\n"
"s_waitcnt lgkmcnt(6)\n"
"ds_read_b64_tr_b16 v[94:95], v3 offset:54080\n"
"v_mfma_f32_16x16x32_bf16 v[238:241], v[20:23], v[188:191], v[238:241]\n"
"s_waitcnt vmcnt(9)\n"
"ds_read_b64_tr_b16 v[96:97], v3 offset:56320\n"
"v_mfma_f32_16x16x32_bf16 v[192:195], v[28:31], v[188:191], v[192:195]\n"
"ds_read_b64_tr_b16 v[98:99], v3 offset:57088\n"
"ds_read_b64_tr_b16 v[100:101], v3 offset:56352\n"
"v_mfma_f32_16x16x32_bf16 v[218:221], v[32:35], v[188:191], v[218:221]\n"
"s_waitcnt lgkmcnt(4)\n"
"ds_read_b64_tr_b16 v[102:103], v3 offset:57120\n"
"ds_read_b64_tr_b16 v[222:223], v3 offset:56384\n"
"v_mfma_f32_16x16x32_bf16 v[238:241], v[36:39], v[214:217], v[238:241]\n"
"ds_read_b64_tr_b16 v[224:225], v3 offset:57152\n"
"ds_read_b128 v[184:187], v5 offset:8192\n"
"v_mfma_f32_16x16x32_bf16 v[192:195], v[40:43], v[214:217], v[192:195]\n"
"s_waitcnt vmcnt(6)\n"
"ds_read_b64_tr_b16 v[104:105], v3 offset:4096\n"
"v_mfma_f32_16x16x32_bf16 v[218:221], v[44:47], v[214:217], v[218:221]\n"
"ds_read_b64_tr_b16 v[106:107], v3 offset:4864\n"
"ds_read_b64_tr_b16 v[108:109], v3 offset:4128\n"
"ds_read_b64_tr_b16 v[110:111], v3 offset:4896\n"
"ds_read_b64_tr_b16 v[244:245], v3 offset:4160\n"
"ds_read_b64_tr_b16 v[246:247], v3 offset:4928\n"
"ds_read_b128 v[188:191], v4 offset:10240\n"
"ds_read_b64_tr_b16 v[112:113], v3 offset:7168\n"
"s_waitcnt lgkmcnt(8)\n"
"v_mfma_f32_16x16x32_bf16 v[238:241], v[48:51], v[184:187], v[238:241]\n"
"ds_read_b64_tr_b16 v[114:115], v3 offset:7936\n"
"v_mfma_f32_16x16x32_bf16 v[192:195], v[52:55], v[184:187], v[192:195]\n"
"ds_read_b128 v[214:217], v5 offset:10240\n"
"v_mfma_f32_16x16x32_bf16 v[218:221], v[56:59], v[184:187], v[218:221]\n"
"s_waitcnt vmcnt(3)\n"
"ds_read_b64_tr_b16 v[116:117], v3 offset:7200\n"
"ds_read_b64_tr_b16 v[118:119], v3 offset:7968\n"
"ds_read_b64_tr_b16 v[248:249], v3 offset:7232\n"
"ds_read_b64_tr_b16 v[250:251], v3 offset:8000\n"
"s_waitcnt lgkmcnt(7)\n"
"v_mfma_f32_16x16x32_bf16 v[238:241], v[60:63], v[188:191], v[238:241]\n"
"ds_read_b64_tr_b16 v[0:1], v27 offset:0\n"
"v_mfma_f32_16x16x32_bf16 v[192:195], v[64:67], v[188:191], v[192:195]\n"
"ds_read_b128 v[184:187], v4 offset:0\n"
"v_mfma_f32_16x16x32_bf16 v[218:221], v[68:71], v[188:191], v[218:221]\n"
"s_waitcnt lgkmcnt(6)\n"
"ds_read_b64_tr_b16 v[2:3], v27 offset:768\n"
"v_mfma_f32_16x16x32_bf16 v[238:241], v[72:75], v[214:217], v[238:241]\n"
"s_waitcnt vmcnt(0)\n"
"ds_read_b64_tr_b16 v[120:121], v27 offset:32\n"
"v_mfma_f32_16x16x32_bf16 v[192:195], v[76:79], v[214:217], v[192:195]\n"
"ds_read_b64_tr_b16 v[122:123], v27 offset:800\n"
"ds_read_b64_tr_b16 v[252:253], v27 offset:64\n"
"v_mfma_f32_16x16x32_bf16 v[218:221], v[80:83], v[214:217], v[218:221]\n"
"ds_read_b64_tr_b16 v[254:255], v27 offset:832\n"
"ds_read_b128 v[188:191], v5 offset:0\n"
"v_cvt_pk_bf16_f32 v238, v238, v239\n"
"v_cvt_pk_bf16_f32 v239, v240, v241\n"
"s_waitcnt lgkmcnt(6)\n"
"v_mfma_f32_16x16x32_bf16 v[132:135], v[84:87], v[184:187], 0\n"
"ds_read_b64_tr_b16 v[124:125], v27 offset:3072\n"
"v_cvt_pk_bf16_f32 v240, v192, v193\n"
"v_cvt_pk_bf16_f32 v241, v194, v195\n"
"v_mfma_f32_16x16x32_bf16 v[214:217], v[88:91], v[184:187], 0\n"
"ds_read_b64_tr_b16 v[126:127], v27 offset:3840\n"
"v_cvt_pk_bf16_f32 v242, v218, v219\n"
"v_cvt_pk_bf16_f32 v243, v220, v221\n"
"v_mfma_f32_16x16x32_bf16 v[136:139], v[92:95], v[184:187], 0\n"
"s_waitcnt lgkmcnt(2)\n"
"ds_read_b64_tr_b16 v[128:129], v27 offset:3104\n"
"ds_read_b64_tr_b16 v[130:131], v27 offset:3872\n"
"ds_read_b64_tr_b16 v[140:141], v27 offset:3136\n"
"v_mfma_f32_16x16x32_bf16 v[132:135], v[96:99], v[188:191], v[132:135]\n"
"ds_read_b64_tr_b16 v[142:143], v27 offset:3904\n"
"ds_read_b128 v[192:195], v4 offset:2048\n"
"v_mfma_f32_16x16x32_bf16 v[214:217], v[100:103], v[188:191], v[214:217]\n"
"ds_read_b128 v[184:187], v5 offset:2048\n"
"v_mfma_f32_16x16x32_bf16 v[136:139], v[222:225], v[188:191], v[136:139]\n"
"s_waitcnt lgkmcnt(2)\n"
"s_barrier\n"
"s_waitcnt lgkmcnt(1)\n"
"v_mfma_f32_16x16x32_bf16 v[132:135], v[104:107], v[192:195], v[132:135]\n"
"v_mfma_f32_16x16x32_bf16 v[214:217], v[108:111], v[192:195], v[214:217]\n"
"ds_read_b128 v[218:221], v4 offset:4096\n"
"v_mfma_f32_16x16x32_bf16 v[136:139], v[244:247], v[192:195], v[136:139]\n"
"s_waitcnt lgkmcnt(1)\n"
"v_mfma_f32_16x16x32_bf16 v[132:135], v[112:115], v[184:187], v[132:135]\n"
"v_mfma_f32_16x16x32_bf16 v[214:217], v[116:119], v[184:187], v[214:217]\n"
"ds_read_b128 v[188:191], v5 offset:4096\n"
"v_mfma_f32_16x16x32_bf16 v[136:139], v[248:251], v[184:187], v[136:139]\n"
"s_waitcnt lgkmcnt(1)\n"
"v_mfma_f32_16x16x32_bf16 v[132:135], v[0:3], v[218:221], v[132:135]\n"
"v_mfma_f32_16x16x32_bf16 v[214:217], v[120:123], v[218:221], v[214:217]\n"
"ds_read_b128 v[192:195], v4 offset:12288\n"
"v_mfma_f32_16x16x32_bf16 v[136:139], v[252:255], v[218:221], v[136:139]\n"
"s_waitcnt lgkmcnt(1)\n"
"v_mfma_f32_16x16x32_bf16 v[132:135], v[124:127], v[188:191], v[132:135]\n"
"v_mfma_f32_16x16x32_bf16 v[214:217], v[128:131], v[188:191], v[214:217]\n"
"ds_read_b128 v[144:147], v5 offset:12288\n"
"v_mfma_f32_16x16x32_bf16 v[136:139], v[140:143], v[188:191], v[136:139]\n"
"s_waitcnt lgkmcnt(1)\n"
"v_mfma_f32_16x16x32_bf16 v[184:187], v[8:11], v[192:195], 0\n"
"v_mfma_f32_16x16x32_bf16 v[148:151], v[12:15], v[192:195], 0\n"
"ds_read_b128 v[218:221], v4 offset:14336\n"
"v_mfma_f32_16x16x32_bf16 v[152:155], v[16:19], v[192:195], 0\n"
"s_waitcnt lgkmcnt(1)\n"
"v_mfma_f32_16x16x32_bf16 v[184:187], v[20:23], v[144:147], v[184:187]\n"
"v_mul_f32 v132, s60, v132\n"
"v_mul_f32 v133, s60, v133\n"
"v_mfma_f32_16x16x32_bf16 v[148:151], v[28:31], v[144:147], v[148:151]\n"
"ds_read_b128 v[188:191], v5 offset:14336\n"
"v_cvt_pk_bf16_f32 v132, v132, v133\n"
"v_mfma_f32_16x16x32_bf16 v[152:155], v[32:35], v[144:147], v[152:155]\n"
"v_mul_f32 v134, s60, v134\n"
"v_mul_f32 v135, s60, v135\n"
"v_cvt_pk_bf16_f32 v133, v134, v135\n"
"v_mul_f32 v214, s60, v214\n"
"s_waitcnt lgkmcnt(1)\n"
"v_mfma_f32_16x16x32_bf16 v[184:187], v[36:39], v[218:221], v[184:187]\n"
"v_mul_f32 v215, s60, v215\n"
"v_mul_f32 v216, s60, v216\n"
"v_mfma_f32_16x16x32_bf16 v[148:151], v[40:43], v[218:221], v[148:151]\n"
"ds_read_b128 v[144:147], v4 offset:16384\n"
"v_mul_f32 v217, s60, v217\n"
"v_mfma_f32_16x16x32_bf16 v[152:155], v[44:47], v[218:221], v[152:155]\n"
"s_waitcnt lgkmcnt(1)\n"
"v_cvt_pk_bf16_f32 v134, v214, v215\n"
"v_cvt_pk_bf16_f32 v135, v216, v217\n"
"v_mfma_f32_16x16x32_bf16 v[184:187], v[48:51], v[188:191], v[184:187]\n"
"ds_write_b128 v6, v[222:225] offset:5120\n"
"v_mfma_f32_16x16x32_bf16 v[148:151], v[52:55], v[188:191], v[148:151]\n"
"ds_read_b128 v[156:159], v5 offset:16384\n"
"v_mul_f32 v136, s60, v136\n"
"v_mfma_f32_16x16x32_bf16 v[152:155], v[56:59], v[188:191], v[152:155]\n"
"s_waitcnt lgkmcnt(2)\n"
"v_mul_f32 v137, s60, v137\n"
"v_mul_f32 v138, s60, v138\n"
"v_mfma_f32_16x16x32_bf16 v[184:187], v[60:63], v[144:147], v[184:187]\n"
"v_mul_f32 v139, s60, v139\n"
"v_cvt_pk_bf16_f32 v26, v136, v137\n"
"v_mfma_f32_16x16x32_bf16 v[148:151], v[64:67], v[144:147], v[148:151]\n"
"ds_read_b128 v[214:217], v4 offset:18432\n"
"v_cvt_pk_bf16_f32 v27, v138, v139\n"
"v_mfma_f32_16x16x32_bf16 v[152:155], v[68:71], v[144:147], v[152:155]\n"
"s_waitcnt lgkmcnt(1)\n"
"v_mfma_f32_16x16x32_bf16 v[184:187], v[72:75], v[156:159], v[184:187]\n"
"v_mfma_f32_16x16x32_bf16 v[148:151], v[76:79], v[156:159], v[148:151]\n"
"ds_read_b128 v[218:221], v5 offset:18432\n"
"v_mfma_f32_16x16x32_bf16 v[152:155], v[80:83], v[156:159], v[152:155]\n"
"s_waitcnt lgkmcnt(1)\n"
"v_mfma_f32_16x16x32_bf16 v[190:193], v[8:11], v[214:217], 0\n"
"v_mfma_f32_16x16x32_bf16 v[136:139], v[12:15], v[214:217], 0\n"
"ds_read_b128 v[144:147], v4 offset:20480\n"
"v_mfma_f32_16x16x32_bf16 v[160:163], v[16:19], v[214:217], 0\n"
"s_waitcnt lgkmcnt(1)\n"
"v_mfma_f32_16x16x32_bf16 v[190:193], v[20:23], v[218:221], v[190:193]\n"
"ds_write_b128 v6, v[244:247] offset:6144\n"
"v_cvt_pk_bf16_f32 v184, v184, v185\n"
"v_cvt_pk_bf16_f32 v185, v186, v187\n"
"v_cvt_pk_bf16_f32 v186, v148, v149\n"
"v_cvt_pk_bf16_f32 v187, v150, v151\n"
"v_cvt_pk_bf16_f32 v188, v152, v153\n"
"v_cvt_pk_bf16_f32 v189, v154, v155\n"
"v_mfma_f32_16x16x32_bf16 v[136:139], v[28:31], v[218:221], v[136:139]\n"
"ds_read_b128 v[214:217], v5 offset:20480\n"
"v_bfe_u32 v195, v6, 2, 6\n"
"v_mfma_f32_16x16x32_bf16 v[160:163], v[32:35], v[218:221], v[160:163]\n"
"s_waitcnt lgkmcnt(2)\n"
"ds_read_b32 v195, v195 offset:1216\n"
"v_mfma_f32_16x16x32_bf16 v[190:193], v[36:39], v[144:147], v[190:193]\n"
"s_add_u32 s68, s50, 0\n"
"v_mfma_f32_16x16x32_bf16 v[136:139], v[40:43], v[144:147], v[136:139]\n"
"ds_read_b128 v[148:151], v4 offset:22528\n"
"v_mfma_f32_16x16x32_bf16 v[160:163], v[44:47], v[144:147], v[160:163]\n"
"s_waitcnt lgkmcnt(2)\n"
"v_mfma_f32_16x16x32_bf16 v[190:193], v[48:51], v[214:217], v[190:193]\n"
"v_mfma_f32_16x16x32_bf16 v[136:139], v[52:55], v[214:217], v[136:139]\n"
"s_add_u32 s69, s50, 1040\n"
"ds_read_b128 v[218:221], v5 offset:22528\n"
"s_waitcnt lgkmcnt(2)\n"
"v_add_u32_sdwa v177, v195, v7 dst_sel:DWORD dst_unused:UNUSED_PAD src0_sel:WORD_0 src1_sel:DWORD\n"
"v_mfma_f32_16x16x32_bf16 v[160:163], v[56:59], v[214:217], v[160:163]\n"
"s_waitcnt lgkmcnt(1)\n"
"v_max_u32 v176, s68, v177\n"
"v_mfma_f32_16x16x32_bf16 v[190:193], v[60:63], v[148:151], v[190:193]\n"
"v_mfma_f32_16x16x32_bf16 v[136:139], v[64:67], v[148:151], v[136:139]\n"
"ds_read_b128 v[144:147], v176 offset:16\n"
"v_min_u32 v175, s69, v177\n"
"v_mfma_f32_16x16x32_bf16 v[160:163], v[68:71], v[148:151], v[160:163]\n"
"s_waitcnt lgkmcnt(1)\n"
"v_mfma_f32_16x16x32_bf16 v[190:193], v[72:75], v[218:221], v[190:193]\n"
"ds_read_b128 v[214:217], v176 offset:32\n"
"v_mfma_f32_16x16x32_bf16 v[136:139], v[76:79], v[218:221], v[136:139]\n"
"s_add_u32 s70, s50, 4294966992\n"
"ds_write_b128 v6, v[248:251] offset:7168\n"
"v_mfma_f32_16x16x32_bf16 v[160:163], v[80:83], v[218:221], v[160:163]\n"
"s_waitcnt lgkmcnt(2)\n"
"ds_read_b128 v[148:151], v175 offset:1344\n"
"v_mfma_f32_16x16x32_bf16 v[144:147], v[202:205], v[132:135], v[144:147]\n"
"v_mfma_f32_16x16x32_bf16 v[144:147], v[204:207], v[24:27], v[144:147]\n"
"ds_read_b128 v[152:155], v175 offset:1360\n"
"v_cvt_pk_bf16_f32 v190, v190, v191\n"
"v_cvt_pk_bf16_f32 v191, v192, v193\n"
"v_cvt_pk_bf16_f32 v192, v136, v137\n"
"v_cvt_pk_bf16_f32 v193, v138, v139\n"
"v_cvt_pk_bf16_f32 v194, v160, v161\n"
"v_cvt_pk_bf16_f32 v195, v162, v163\n"
"s_waitcnt lgkmcnt(3)\n"
"v_mfma_f32_16x16x32_bf16 v[214:217], v[208:211], v[132:135], v[214:217]\n"
"v_max3_f32 v220, v144, v145, v146\n"
"v_max_f32 v220, v220, v147\n"
"v_mfma_f32_16x16x32_bf16 v[214:217], v[210:213], v[24:27], v[214:217]\n"
"s_waitcnt lgkmcnt(1)\n"
"ds_read_b128 v[136:139], v176 offset:0\n"
"v_min_u32 v174, s70, v177\n"
"v_mfma_f32_16x16x32_bf16 v[148:151], v[226:229], v[132:135], v[148:151]\n"
"v_mfma_f32_16x16x32_bf16 v[148:151], v[228:231], v[24:27], v[148:151]\n"
"s_waitcnt lgkmcnt(1)\n"
"ds_read_b128 v[156:159], v174 offset:2688\n"
"s_nop 0\n"
"v_max3_f32 v220, v220, v214, v215\n"
"v_max3_f32 v220, v220, v216, v217\n"
"v_mfma_f32_16x16x32_bf16 v[152:155], v[232:235], v[132:135], v[152:155]\n"
"v_mfma_f32_16x16x32_bf16 v[152:155], v[234:237], v[24:27], v[152:155]\n"
"s_waitcnt lgkmcnt(1)\n"
"ds_read_b128 v[144:147], v175 offset:1376\n"
"v_max3_f32 v220, v220, v148, v149\n"
"v_max3_f32 v220, v220, v150, v151\n"
"v_mfma_f32_16x16x32_bf16 v[136:139], v[196:199], v[132:135], v[136:139]\n"
"v_mfma_f32_16x16x32_bf16 v[136:139], v[198:201], v[24:27], v[136:139]\n"
"ds_read_b128 v[214:217], v174 offset:2704\n"
"ds_write_b128 v6, v[252:255] offset:8192\n"
"v_max3_f32 v220, v220, v152, v153\n"
"v_max3_f32 v220, v220, v154, v155\n"
"s_waitcnt lgkmcnt(3)\n"
"v_mfma_f32_16x16x32_bf16 v[156:159], v[178:181], v[132:135], v[156:159]\n"
"v_mfma_f32_16x16x32_bf16 v[156:159], v[180:183], v[24:27], v[156:159]\n"
"s_waitcnt lgkmcnt(2)\n"
"ds_read_b128 v[148:151], v174 offset:2720\n"
"v_max3_f32 v220, v220, v136, v137\n"
"v_max3_f32 v220, v220, v138, v139\n"
"v_mfma_f32_16x16x32_bf16 v[144:147], v[238:241], v[132:135], v[144:147]\n"
"v_mfma_f32_16x16x32_bf16 v[144:147], v[240:243], v[24:27], v[144:147]\n"
"s_waitcnt lgkmcnt(2)\n"
"ds_read_b128 v[152:155], v176 offset:16\n"
"v_max3_f32 v220, v220, v156, v157\n"
"v_max3_f32 v220, v220, v158, v159\n"
"v_mfma_f32_16x16x32_bf16 v[214:217], v[184:187], v[132:135], v[214:217]\n"
"v_mfma_f32_16x16x32_bf16 v[214:217], v[186:189], v[24:27], v[214:217]\n"
"s_waitcnt lgkmcnt(1)\n"
"ds_read_b128 v[136:139], v176 offset:32\n"
"v_max3_f32 v220, v220, v144, v145\n"
"v_max3_f32 v220, v220, v146, v147\n"
"v_mfma_f32_16x16x32_bf16 v[148:151], v[190:193], v[132:135], v[148:151]\n"
"v_mfma_f32_16x16x32_bf16 v[148:151], v[192:195], v[24:27], v[148:151]\n"
"s_waitcnt lgkmcnt(1)\n"
"ds_read_b128 v[156:159], v175 offset:1344\n"
"v_max3_f32 v220, v220, v214, v215\n"
"v_max3_f32 v220, v220, v216, v217\n"
"v_mfma_f32_16x16x32_bf16 v[152:155], v[202:205], v[132:135], v[152:155]\n"
"v_mfma_f32_16x16x32_bf16 v[152:155], v[204:207], v[24:27], v[152:155]\n"
"ds_read_b128 v[144:147], v175 offset:1360\n"
"s_nop 0\n"
"v_max3_f32 v220, v220, v148, v149\n"
"v_max3_f32 v220, v220, v150, v151\n"
"v_mov_b32 v221, v220\n"
"s_nop 1\n"
"v_permlane16_swap_b32 v220, v221\n"
"v_max_f32 v220, v220, v221\n"
"v_mov_b32 v221, v220\n"
"s_nop 1\n"
"v_permlane32_swap_b32 v220, v221\n"
"v_max_f32 v220, v220, v221\n"
"v_sub_f32 v152, v152, v220\n"
"v_exp_f32 v152, v152\n"
"v_sub_f32 v153, v153, v220\n"
"v_exp_f32 v153, v153\n"
"v_sub_f32 v154, v154, v220\n"
"v_exp_f32 v154, v154\n"
"v_sub_f32 v155, v155, v220\n"
"v_exp_f32 v155, v155\n"
"s_waitcnt lgkmcnt(2)\n"
"v_mfma_f32_16x16x32_bf16 v[136:139], v[208:211], v[132:135], v[136:139]\n"
"v_cvt_pk_bf16_f32 v152, v152, v153\n"
"v_cvt_pk_bf16_f32 v153, v154, v155\n"
"v_mfma_f32_16x16x32_bf16 v[136:139], v[210:213], v[24:27], v[136:139]\n"
"ds_write_addtid_b32 v152 offset:53248\n"
"ds_read_b128 v[214:217], v176 offset:0\n"
"ds_write_addtid_b32 v153 offset:53504\n"
"s_waitcnt lgkmcnt(4)\n"
"v_mfma_f32_16x16x32_bf16 v[156:159], v[226:229], v[132:135], v[156:159]\n"
"ds_write_b128 v6, v[140:143] offset:9216\n"
"v_mfma_f32_16x16x32_bf16 v[156:159], v[228:231], v[24:27], v[156:159]\n"
"ds_read_b128 v[148:151], v174 offset:2688\n"
"v_sub_f32 v136, v136, v220\n"
"v_exp_f32 v136, v136\n"
"v_sub_f32 v137, v137, v220\n"
"v_exp_f32 v137, v137\n"
"s_nop 0\n"
"v_cvt_pk_bf16_f32 v136, v136, v137\n"
"ds_write_addtid_b32 v136 offset:53760\n"
"s_waitcnt lgkmcnt(6)\n"
"v_mfma_f32_16x16x32_bf16 v[144:147], v[232:235], v[132:135], v[144:147]\n"
"v_sub_f32 v156, v156, v220\n"
"v_exp_f32 v156, v156\n"
"v_sub_f32 v157, v157, v220\n"
"v_exp_f32 v157, v157\n"
"v_sub_f32 v158, v158, v220\n"
"v_exp_f32 v158, v158\n"
"v_sub_f32 v159, v159, v220\n"
"v_exp_f32 v159, v159\n"
"v_cvt_pk_bf16_f32 v156, v156, v157\n"
"v_cvt_pk_bf16_f32 v157, v158, v159\n"
"v_mfma_f32_16x16x32_bf16 v[144:147], v[234:237], v[24:27], v[144:147]\n"
"ds_write_addtid_b32 v156 offset:54272\n"
"ds_write_addtid_b32 v157 offset:54528\n"
"s_waitcnt lgkmcnt(6)\n"
"v_mfma_f32_16x16x32_bf16 v[214:217], v[196:199], v[132:135], v[214:217]\n"
"ds_read_b128 v[136:139], v175 offset:1376\n"
"v_mfma_f32_16x16x32_bf16 v[214:217], v[198:201], v[24:27], v[214:217]\n"
"ds_read_b128 v[152:155], v174 offset:2704\n"
"s_nop 0\n"
"v_sub_f32 v144, v144, v220\n"
"v_exp_f32 v144, v144\n"
"v_sub_f32 v145, v145, v220\n"
"v_exp_f32 v145, v145\n"
"v_sub_f32 v146, v146, v220\n"
"v_exp_f32 v146, v146\n"
"v_sub_f32 v147, v147, v220\n"
"v_exp_f32 v147, v147\n"
"v_cvt_pk_bf16_f32 v144, v144, v145\n"
"v_sub_f32 v214, v214, v220\n"
"v_exp_f32 v214, v214\n"
"v_cvt_pk_bf16_f32 v145, v146, v147\n"
"v_sub_f32 v215, v215, v220\n"
"v_exp_f32 v215, v215\n"
"v_sub_f32 v216, v216, v220\n"
"v_exp_f32 v216, v216\n"
"v_sub_f32 v217, v217, v220\n"
"v_exp_f32 v217, v217\n"
"v_cvt_pk_bf16_f32 v214, v214, v215\n"
"v_cvt_pk_bf16_f32 v215, v216, v217\n"
"ds_write_addtid_b32 v144 offset:54784\n"
"ds_write_addtid_b32 v145 offset:55040\n"
"s_waitcnt lgkmcnt(7)\n"
"v_mfma_f32_16x16x32_bf16 v[148:151], v[178:181], v[132:135], v[148:151]\n"
"v_mfma_f32_16x16x32_bf16 v[148:151], v[180:183], v[24:27], v[148:151]\n"
"s_waitcnt lgkmcnt(0)\n"
"ds_read_b128 v[196:199], v174 offset:2720\n"
"s_barrier\n"
"ds_write_addtid_b32 v214 offset:58880\n"
"ds_write_addtid_b32 v215 offset:59136\n"
"v_mfma_f32_16x16x32_bf16 v[136:139], v[238:241], v[132:135], v[136:139]\n"
"v_mfma_f32_16x16x32_bf16 v[136:139], v[240:243], v[24:27], v[136:139]\n"
"ds_read_b128 v[216:219], v4 offset:24576\n"
"v_sub_f32 v148, v148, v220\n"
"v_exp_f32 v148, v148\n"
"v_sub_f32 v149, v149, v220\n"
"v_exp_f32 v149, v149\n"
"v_sub_f32 v150, v150, v220\n"
"v_exp_f32 v150, v150\n"
"v_sub_f32 v151, v151, v220\n"
"v_exp_f32 v151, v151\n"
"v_cvt_pk_bf16_f32 v148, v148, v149\n"
"v_cvt_pk_bf16_f32 v149, v150, v151\n"
"v_sub_f32 v136, v136, v220\n"
"v_exp_f32 v136, v136\n"
"v_sub_f32 v137, v137, v220\n"
"v_exp_f32 v137, v137\n"
"ds_write_addtid_b32 v148 offset:57344\n"
"v_cvt_pk_bf16_f32 v136, v136, v137\n"
"ds_write_addtid_b32 v149 offset:57600\n"
"v_mfma_f32_16x16x32_bf16 v[152:155], v[184:187], v[132:135], v[152:155]\n"
"ds_write_addtid_b32 v136 offset:33792\n"
"v_mfma_f32_16x16x32_bf16 v[152:155], v[186:189], v[24:27], v[152:155]\n"
"s_waitcnt lgkmcnt(6)\n"
"v_mfma_f32_16x16x32_bf16 v[196:199], v[190:193], v[132:135], v[196:199]\n"
"ds_read_b128 v[144:147], v5 offset:24576\n"
"v_mfma_f32_16x16x32_bf16 v[196:199], v[192:195], v[24:27], v[196:199]\n"
"s_waitcnt lgkmcnt(4)\n"
"v_mfma_f32_16x16x32_bf16 v[148:151], v[84:87], v[216:219], 0\n"
"v_mfma_f32_16x16x32_bf16 v[156:159], v[88:91], v[216:219], 0\n"
"ds_read_b128 v[136:139], v4 offset:26624\n"
"v_mfma_f32_16x16x32_bf16 v[160:163], v[92:95], v[216:219], 0\n"
"s_waitcnt lgkmcnt(1)\n"
"v_mfma_f32_16x16x32_bf16 v[148:151], v[96:99], v[144:147], v[148:151]\n"
"v_sub_f32 v152, v152, v220\n"
"v_exp_f32 v152, v152\n"
"v_sub_f32 v153, v153, v220\n"
"v_exp_f32 v153, v153\n"
"v_sub_f32 v154, v154, v220\n"
"v_exp_f32 v154, v154\n"
"v_sub_f32 v155, v155, v220\n"
"v_exp_f32 v155, v155\n"
"v_cvt_pk_bf16_f32 v152, v152, v153\n"
"v_cvt_pk_bf16_f32 v153, v154, v155\n"
"v_mfma_f32_16x16x32_bf16 v[156:159], v[100:103], v[144:147], v[156:159]\n"
"ds_write_addtid_b32 v152 offset:57856\n"
"ds_write_addtid_b32 v153 offset:58112\n"
"v_mfma_f32_16x16x32_bf16 v[160:163], v[222:225], v[144:147], v[160:163]\n"
"s_waitcnt lgkmcnt(2)\n"
"ds_read_b128 v[198:201], v5 offset:26624\n"
"v_mfma_f32_16x16x32_bf16 v[148:151], v[104:107], v[136:139], v[148:151]\n"
"v_sub_f32 v196, v196, v220\n"
"v_exp_f32 v196, v196\n"
"v_mfma_f32_16x16x32_bf16 v[156:159], v[108:111], v[136:139], v[156:159]\n"
"ds_read_b128 v[132:135], v4 offset:28672\n"
"v_sub_f32 v197, v197, v220\n"
"v_exp_f32 v197, v197\n"
"v_mfma_f32_16x16x32_bf16 v[160:163], v[244:247], v[136:139], v[160:163]\n"
"v_cvt_pk_bf16_f32 v196, v196, v197\n"
"ds_write_addtid_b32 v196 offset:58368\n"
"s_waitcnt lgkmcnt(2)\n"
"v_mfma_f32_16x16x32_bf16 v[148:151], v[112:115], v[198:201], v[148:151]\n"
"v_mfma_f32_16x16x32_bf16 v[156:159], v[116:119], v[198:201], v[156:159]\n"
"ds_read_b128 v[218:221], v5 offset:28672\n"
"v_mfma_f32_16x16x32_bf16 v[160:163], v[248:251], v[198:201], v[160:163]\n"
"s_waitcnt lgkmcnt(2)\n"
"v_mfma_f32_16x16x32_bf16 v[148:151], v[0:3], v[132:135], v[148:151]\n"
"s_waitcnt lgkmcnt(1)\n"
"s_barrier\n"
"v_mfma_f32_16x16x32_bf16 v[156:159], v[120:123], v[132:135], v[156:159]\n"
"ds_read_b128 v[222:225], v4 offset:36864\n"
"v_mfma_f32_16x16x32_bf16 v[160:163], v[252:255], v[132:135], v[160:163]\n"
"s_waitcnt lgkmcnt(1)\n"
"v_mfma_f32_16x16x32_bf16 v[148:151], v[124:127], v[218:221], v[148:151]\n"
"v_mfma_f32_16x16x32_bf16 v[156:159], v[128:131], v[218:221], v[156:159]\n"
"ds_read_b128 v[196:199], v5 offset:36864\n"
"v_mfma_f32_16x16x32_bf16 v[160:163], v[140:143], v[218:221], v[160:163]\n"
"s_waitcnt lgkmcnt(1)\n"
"v_mfma_f32_16x16x32_bf16 v[214:217], v[8:11], v[222:225], 0\n"
"v_mfma_f32_16x16x32_bf16 v[248:251], v[12:15], v[222:225], 0\n"
"ds_read_b128 v[244:247], v4 offset:38912\n"
"v_mfma_f32_16x16x32_bf16 v[252:255], v[16:19], v[222:225], 0\n"
"s_waitcnt lgkmcnt(1)\n"
"v_mfma_f32_16x16x32_bf16 v[214:217], v[20:23], v[196:199], v[214:217]\n"
"v_mul_f32 v148, s60, v148\n"
"v_mul_f32 v149, s60, v149\n"
"v_mfma_f32_16x16x32_bf16 v[248:251], v[28:31], v[196:199], v[248:251]\n"
"ds_read_b128 v[218:221], v5 offset:38912\n"
"v_cvt_pk_bf16_f32 v148, v148, v149\n"
"v_mfma_f32_16x16x32_bf16 v[252:255], v[32:35], v[196:199], v[252:255]\n"
"s_waitcnt lgkmcnt(1)\n"
"v_mul_f32 v150, s60, v150\n"
"v_mul_f32 v151, s60, v151\n"
"v_mfma_f32_16x16x32_bf16 v[214:217], v[36:39], v[244:247], v[214:217]\n"
"v_cvt_pk_bf16_f32 v149, v150, v151\n"
"v_mul_f32 v156, s60, v156\n"
"v_mfma_f32_16x16x32_bf16 v[248:251], v[40:43], v[244:247], v[248:251]\n"
"ds_read_b128 v[196:199], v4 offset:40960\n"
"v_mul_f32 v157, s60, v157\n"
"v_mfma_f32_16x16x32_bf16 v[252:255], v[44:47], v[244:247], v[252:255]\n"
"v_mul_f32 v158, s60, v158\n"
"v_mul_f32 v159, s60, v159\n"
"v_cvt_pk_bf16_f32 v150, v156, v157\n"
"v_cvt_pk_bf16_f32 v151, v158, v159\n"
"s_waitcnt lgkmcnt(1)\n"
"v_mfma_f32_16x16x32_bf16 v[214:217], v[48:51], v[218:221], v[214:217]\n"
"v_mul_f32 v160, s60, v160\n"
"v_mul_f32 v161, s60, v161\n"
"v_mfma_f32_16x16x32_bf16 v[248:251], v[52:55], v[218:221], v[248:251]\n"
"ds_read_b128 v[222:225], v5 offset:40960\n"
"v_mul_f32 v162, s60, v162\n"
"v_mfma_f32_16x16x32_bf16 v[252:255], v[56:59], v[218:221], v[252:255]\n"
"s_waitcnt lgkmcnt(1)\n"
"v_mul_f32 v163, s60, v163\n"
"v_cvt_pk_bf16_f32 v26, v160, v161\n"
"v_mfma_f32_16x16x32_bf16 v[214:217], v[60:63], v[196:199], v[214:217]\n"
"v_cvt_pk_bf16_f32 v27, v162, v163\n"
"v_mfma_f32_16x16x32_bf16 v[248:251], v[64:67], v[196:199], v[248:251]\n"
"ds_read_b128 v[132:135], v4 offset:43008\n"
"v_mfma_f32_16x16x32_bf16 v[252:255], v[68:71], v[196:199], v[252:255]\n"
"s_waitcnt lgkmcnt(1)\n"
"v_mfma_f32_16x16x32_bf16 v[214:217], v[72:75], v[222:225], v[214:217]\n"
"v_mfma_f32_16x16x32_bf16 v[248:251], v[76:79], v[222:225], v[248:251]\n"
"ds_read_b128 v[136:139], v5 offset:43008\n"
"v_mfma_f32_16x16x32_bf16 v[252:255], v[80:83], v[222:225], v[252:255]\n"
"s_waitcnt lgkmcnt(1)\n"
"v_mfma_f32_16x16x32_bf16 v[244:247], v[8:11], v[132:135], 0\n"
"v_mfma_f32_16x16x32_bf16 v[140:143], v[12:15], v[132:135], 0\n"
"ds_read_b128 v[196:199], v4 offset:45056\n"
"v_mfma_f32_16x16x32_bf16 v[144:147], v[16:19], v[132:135], 0\n"
"s_waitcnt lgkmcnt(1)\n"
"v_mfma_f32_16x16x32_bf16 v[244:247], v[20:23], v[136:139], v[244:247]\n"
"v_cvt_pk_bf16_f32 v214, v214, v215\n"
"v_cvt_pk_bf16_f32 v215, v216, v217\n"
"v_cvt_pk_bf16_f32 v216, v248, v249\n"
"v_cvt_pk_bf16_f32 v217, v250, v251\n"
"v_cvt_pk_bf16_f32 v218, v252, v253\n"
"v_cvt_pk_bf16_f32 v219, v254, v255\n"
"v_mfma_f32_16x16x32_bf16 v[140:143], v[28:31], v[136:139], v[140:143]\n"
"ds_read_b128 v[220:223], v5 offset:45056\n"
"v_bfe_u32 v225, v6, 2, 6\n"
"v_mfma_f32_16x16x32_bf16 v[144:147], v[32:35], v[136:139], v[144:147]\n"
"s_waitcnt lgkmcnt(1)\n"
"ds_read_b32 v225, v225 offset:1280\n"
"v_mfma_f32_16x16x32_bf16 v[244:247], v[36:39], v[196:199], v[244:247]\n"
"s_add_u32 s71, s50, 4294967280\n"
"v_mfma_f32_16x16x32_bf16 v[140:143], v[40:43], v[196:199], v[140:143]\n"
"ds_read_b128 v[248:251], v4 offset:47104\n"
"v_mfma_f32_16x16x32_bf16 v[144:147], v[44:47], v[196:199], v[144:147]\n"
"s_waitcnt lgkmcnt(2)\n"
"v_mfma_f32_16x16x32_bf16 v[244:247], v[48:51], v[220:223], v[244:247]\n"
"v_mfma_f32_16x16x32_bf16 v[140:143], v[52:55], v[220:223], v[140:143]\n"
"s_add_u32 s72, s50, 1024\n"
"ds_read_b128 v[252:255], v5 offset:47104\n"
"s_waitcnt lgkmcnt(2)\n"
"v_add_u32_sdwa v224, v225, v7 dst_sel:DWORD dst_unused:UNUSED_PAD src0_sel:WORD_0 src1_sel:DWORD\n"
"v_mfma_f32_16x16x32_bf16 v[144:147], v[56:59], v[220:223], v[144:147]\n"
"s_waitcnt lgkmcnt(1)\n"
"v_max_u32 v201, s71, v224\n"
"v_mfma_f32_16x16x32_bf16 v[244:247], v[60:63], v[248:251], v[244:247]\n"
"v_mfma_f32_16x16x32_bf16 v[140:143], v[64:67], v[248:251], v[140:143]\n"
"ds_read_b128 v[196:199], v201 offset:16\n"
"v_min_u32 v200, s72, v224\n"
"v_mfma_f32_16x16x32_bf16 v[144:147], v[68:71], v[248:251], v[144:147]\n"
"s_waitcnt lgkmcnt(1)\n"
"v_mfma_f32_16x16x32_bf16 v[244:247], v[72:75], v[252:255], v[244:247]\n"
"ds_read_b128 v[220:223], v201 offset:32\n"
"v_mfma_f32_16x16x32_bf16 v[140:143], v[76:79], v[252:255], v[140:143]\n"
"v_mfma_f32_16x16x32_bf16 v[144:147], v[80:83], v[252:255], v[144:147]\n"
"s_waitcnt lgkmcnt(1)\n"
"ds_read_b128 v[132:135], v200 offset:1376\n"
"v_mfma_f32_16x16x32_bf16 v[196:199], v[202:205], v[148:151], v[196:199]\n"
"v_mfma_f32_16x16x32_bf16 v[196:199], v[204:207], v[24:27], v[196:199]\n"
"s_waitcnt lgkmcnt(1)\n"
"ds_read_b128 v[136:139], v200 offset:1360\n"
"v_mfma_f32_16x16x32_bf16 v[220:223], v[208:211], v[148:151], v[220:223]\n"
"v_cvt_pk_bf16_f32 v244, v244, v245\n"
"v_cvt_pk_bf16_f32 v245, v246, v247\n"
"v_cvt_pk_bf16_f32 v246, v140, v141\n"
"v_cvt_pk_bf16_f32 v247, v142, v143\n"
"v_cvt_pk_bf16_f32 v248, v144, v145\n"
"v_cvt_pk_bf16_f32 v249, v146, v147\n"
"v_max3_f32 v250, v196, v197, v198\n"
"v_max_f32 v250, v250, v199\n"
"v_mfma_f32_16x16x32_bf16 v[220:223], v[210:213], v[24:27], v[220:223]\n"
"s_waitcnt lgkmcnt(1)\n"
"ds_read_b128 v[252:255], v201 offset:48\n"
"v_mfma_f32_16x16x32_bf16 v[132:135], v[238:241], v[148:151], v[132:135]\n"
"v_mfma_f32_16x16x32_bf16 v[132:135], v[240:243], v[24:27], v[132:135]\n"
"s_waitcnt lgkmcnt(1)\n"
"ds_read_b128 v[140:143], v200 offset:1392\n"
"s_nop 1\n"
"v_max3_f32 v250, v250, v220, v221\n"
"v_max3_f32 v250, v250, v222, v223\n"
"v_mfma_f32_16x16x32_bf16 v[136:139], v[232:235], v[148:151], v[136:139]\n"
"v_mfma_f32_16x16x32_bf16 v[136:139], v[234:237], v[24:27], v[136:139]\n"
"s_waitcnt lgkmcnt(1)\n"
"ds_read_b128 v[196:199], v201 offset:16\n"
"v_max3_f32 v250, v250, v132, v133\n"
"v_max3_f32 v250, v250, v134, v135\n"
"v_mfma_f32_16x16x32_bf16 v[252:255], v[214:217], v[148:151], v[252:255]\n"
"v_mfma_f32_16x16x32_bf16 v[252:255], v[216:219], v[24:27], v[252:255]\n"
"s_waitcnt lgkmcnt(1)\n"
"ds_read_b128 v[220:223], v201 offset:32\n"
"v_max3_f32 v250, v250, v136, v137\n"
"v_max3_f32 v250, v250, v138, v139\n"
"v_mfma_f32_16x16x32_bf16 v[140:143], v[244:247], v[148:151], v[140:143]\n"
"v_mfma_f32_16x16x32_bf16 v[140:143], v[246:249], v[24:27], v[140:143]\n"
"s_waitcnt lgkmcnt(1)\n"
"ds_read_b128 v[132:135], v200 offset:1376\n"
"v_max3_f32 v250, v250, v252, v253\n"
"v_max3_f32 v250, v250, v254, v255\n"
"v_mfma_f32_16x16x32_bf16 v[196:199], v[202:205], v[148:151], v[196:199]\n"
"v_mfma_f32_16x16x32_bf16 v[196:199], v[204:207], v[24:27], v[196:199]\n"
"ds_read_b128 v[136:139], v200 offset:1360\n"
"s_nop 0\n"
"v_max3_f32 v250, v250, v140, v141\n"
"v_max3_f32 v250, v250, v142, v143\n"
"v_mov_b32 v251, v250\n"
"s_nop 1\n"
"v_permlane16_swap_b32 v250, v251\n"
"v_max_f32 v250, v250, v251\n"
"v_mov_b32 v251, v250\n"
"s_nop 1\n"
"v_permlane32_swap_b32 v250, v251\n"
"v_max_f32 v250, v250, v251\n"
"v_sub_f32 v196, v196, v250\n"
"v_exp_f32 v196, v196\n"
"v_sub_f32 v197, v197, v250\n"
"v_exp_f32 v197, v197\n"
"v_sub_f32 v198, v198, v250\n"
"v_exp_f32 v198, v198\n"
"v_sub_f32 v199, v199, v250\n"
"v_exp_f32 v199, v199\n"
"s_waitcnt lgkmcnt(2)\n"
"v_mfma_f32_16x16x32_bf16 v[220:223], v[208:211], v[148:151], v[220:223]\n"
"v_cvt_pk_bf16_f32 v196, v196, v197\n"
"v_cvt_pk_bf16_f32 v197, v198, v199\n"
"v_mfma_f32_16x16x32_bf16 v[220:223], v[210:213], v[24:27], v[220:223]\n"
"ds_write_addtid_b32 v196 offset:55296\n"
"ds_read_b128 v[252:255], v201 offset:48\n"
"ds_write_addtid_b32 v197 offset:55552\n"
"s_waitcnt lgkmcnt(4)\n"
"v_mfma_f32_16x16x32_bf16 v[132:135], v[238:241], v[148:151], v[132:135]\n"
"v_mfma_f32_16x16x32_bf16 v[132:135], v[240:243], v[24:27], v[132:135]\n"
"ds_read_b128 v[140:143], v200 offset:1392\n"
"s_nop 0\n"
"v_sub_f32 v220, v220, v250\n"
"v_exp_f32 v220, v220\n"
"v_sub_f32 v221, v221, v250\n"
"v_exp_f32 v221, v221\n"
"v_sub_f32 v222, v222, v250\n"
"v_exp_f32 v222, v222\n"
"v_sub_f32 v223, v223, v250\n"
"v_exp_f32 v223, v223\n"
"v_sub_f32 v132, v132, v250\n"
"v_exp_f32 v132, v132\n"
"v_sub_f32 v133, v133, v250\n"
"v_exp_f32 v133, v133\n"
"v_sub_f32 v134, v134, v250\n"
"v_exp_f32 v134, v134\n"
"v_sub_f32 v135, v135, v250\n"
"v_exp_f32 v135, v135\n"
"v_cvt_pk_bf16_f32 v220, v220, v221\n"
"v_cvt_pk_bf16_f32 v132, v132, v133\n"
"v_cvt_pk_bf16_f32 v221, v222, v223\n"
"v_cvt_pk_bf16_f32 v133, v134, v135\n"
"ds_write_addtid_b32 v220 offset:55808\n"
"ds_write_addtid_b32 v132 offset:56320\n"
"ds_write_addtid_b32 v221 offset:56064\n"
"s_waitcnt lgkmcnt(7)\n"
"v_mfma_f32_16x16x32_bf16 v[136:139], v[232:235], v[148:151], v[136:139]\n"
"ds_write_addtid_b32 v133 offset:56576\n"
"v_mfma_f32_16x16x32_bf16 v[136:139], v[234:237], v[24:27], v[136:139]\n"
"s_waitcnt lgkmcnt(6)\n"
"ds_read_b128 v[196:199], v4 offset:30720\n"
"v_mfma_f32_16x16x32_bf16 v[252:255], v[214:217], v[148:151], v[252:255]\n"
"v_mfma_f32_16x16x32_bf16 v[252:255], v[216:219], v[24:27], v[252:255]\n"
"s_waitcnt lgkmcnt(1)\n"
"s_barrier\n"
"s_nop 1\n"
"v_sub_f32 v136, v136, v250\n"
"v_exp_f32 v136, v136\n"
"v_sub_f32 v137, v137, v250\n"
"v_exp_f32 v137, v137\n"
"v_sub_f32 v138, v138, v250\n"
"v_exp_f32 v138, v138\n"
"v_sub_f32 v139, v139, v250\n"
"v_exp_f32 v139, v139\n"
"v_cvt_pk_bf16_f32 v136, v136, v137\n"
"v_cvt_pk_bf16_f32 v137, v138, v139\n"
"v_sub_f32 v252, v252, v250\n"
"v_exp_f32 v252, v252\n"
"v_sub_f32 v253, v253, v250\n"
"v_exp_f32 v253, v253\n"
"v_sub_f32 v254, v254, v250\n"
"v_exp_f32 v254, v254\n"
"v_sub_f32 v255, v255, v250\n"
"v_exp_f32 v255, v255\n"
"v_cvt_pk_bf16_f32 v252, v252, v253\n"
"v_cvt_pk_bf16_f32 v253, v254, v255\n"
"ds_write_addtid_b32 v136 offset:57856\n"
"ds_write_addtid_b32 v137 offset:58112\n"
"v_mfma_f32_16x16x32_bf16 v[140:143], v[244:247], v[148:151], v[140:143]\n"
"ds_read_b128 v[220:223], v5 offset:30720\n"
"v_mfma_f32_16x16x32_bf16 v[140:143], v[246:249], v[24:27], v[140:143]\n"
"ds_write_addtid_b32 v252 offset:57344\n"
"s_waitcnt lgkmcnt(4)\n"
"v_mfma_f32_16x16x32_bf16 v[132:135], v[84:87], v[196:199], 0\n"
"ds_read_b128 v[144:147], v6 offset:5120\n"
"v_mfma_f32_16x16x32_bf16 v[136:139], v[88:91], v[196:199], 0\n"
"ds_read_b128 v[148:151], v4 offset:32768\n"
"v_mfma_f32_16x16x32_bf16 v[152:155], v[92:95], v[196:199], 0\n"
"ds_write_addtid_b32 v253 offset:57600\n"
"s_waitcnt lgkmcnt(4)\n"
"v_mfma_f32_16x16x32_bf16 v[132:135], v[96:99], v[220:223], v[132:135]\n"
"ds_read_b128 v[156:159], v6 offset:6144\n"
"v_mfma_f32_16x16x32_bf16 v[136:139], v[100:103], v[220:223], v[136:139]\n"
"s_waitcnt lgkmcnt(3)\n"
"ds_read_b128 v[160:163], v5 offset:32768\n"
"v_mfma_f32_16x16x32_bf16 v[152:155], v[144:147], v[220:223], v[152:155]\n"
"s_waitcnt lgkmcnt(3)\n"
"v_sub_f32 v140, v140, v250\n"
"v_exp_f32 v140, v140\n"
"v_mfma_f32_16x16x32_bf16 v[132:135], v[104:107], v[148:151], v[132:135]\n"
"ds_read_b128 v[196:199], v6 offset:7168\n"
"v_sub_f32 v141, v141, v250\n"
"v_exp_f32 v141, v141\n"
"v_sub_f32 v142, v142, v250\n"
"v_exp_f32 v142, v142\n"
"v_mfma_f32_16x16x32_bf16 v[136:139], v[108:111], v[148:151], v[136:139]\n"
"ds_read_b128 v[220:223], v4 offset:34816\n"
"v_cvt_pk_bf16_f32 v140, v140, v141\n"
"v_sub_f32 v143, v143, v250\n"
"v_exp_f32 v143, v143\n"
"s_waitcnt lgkmcnt(3)\n"
"v_mfma_f32_16x16x32_bf16 v[152:155], v[156:159], v[148:151], v[152:155]\n"
"ds_write_addtid_b32 v140 offset:56832\n"
"v_cvt_pk_bf16_f32 v141, v142, v143\n"
"s_waitcnt lgkmcnt(3)\n"
"v_mfma_f32_16x16x32_bf16 v[132:135], v[112:115], v[160:163], v[132:135]\n"
"ds_write_addtid_b32 v141 offset:57088\n"
"v_mfma_f32_16x16x32_bf16 v[136:139], v[116:119], v[160:163], v[136:139]\n"
"s_waitcnt lgkmcnt(3)\n"
"ds_read_b128 v[250:253], v6 offset:8192\n"
"v_mfma_f32_16x16x32_bf16 v[152:155], v[196:199], v[160:163], v[152:155]\n"
"s_waitcnt lgkmcnt(3)\n"
"ds_read_b128 v[144:147], v5 offset:34816\n"
"v_mfma_f32_16x16x32_bf16 v[132:135], v[0:3], v[220:223], v[132:135]\n"
"ds_read_b128 v[148:151], v6 offset:9216\n"
"v_mfma_f32_16x16x32_bf16 v[136:139], v[120:123], v[220:223], v[136:139]\n"
"s_waitcnt lgkmcnt(3)\n"
"ds_read_b128 v[140:143], v4 offset:49152\n"
"s_barrier\n"
"s_waitcnt lgkmcnt(3)\n"
"v_mfma_f32_16x16x32_bf16 v[152:155], v[250:253], v[220:223], v[152:155]\n"
"s_waitcnt lgkmcnt(2)\n"
"v_mfma_f32_16x16x32_bf16 v[132:135], v[124:127], v[144:147], v[132:135]\n"
"v_mfma_f32_16x16x32_bf16 v[136:139], v[128:131], v[144:147], v[136:139]\n"
"s_waitcnt lgkmcnt(1)\n"
"ds_read_b128 v[156:159], v5 offset:49152\n"
"v_mfma_f32_16x16x32_bf16 v[152:155], v[148:151], v[144:147], v[152:155]\n"
"s_waitcnt lgkmcnt(1)\n"
"v_mfma_f32_16x16x32_bf16 v[196:199], v[8:11], v[140:143], 0\n"
"v_mfma_f32_16x16x32_bf16 v[160:163], v[12:15], v[140:143], 0\n"
"ds_read_b128 v[220:223], v4 offset:51200\n"
"v_mfma_f32_16x16x32_bf16 v[250:253], v[16:19], v[140:143], 0\n"
"s_waitcnt lgkmcnt(1)\n"
"v_mfma_f32_16x16x32_bf16 v[196:199], v[20:23], v[156:159], v[196:199]\n"
"v_mul_f32 v132, s60, v132\n"
"v_mul_f32 v133, s60, v133\n"
"v_mfma_f32_16x16x32_bf16 v[160:163], v[28:31], v[156:159], v[160:163]\n"
"ds_read_b128 v[144:147], v5 offset:51200\n"
"v_cvt_pk_bf16_f32 v132, v132, v133\n"
"v_mfma_f32_16x16x32_bf16 v[250:253], v[32:35], v[156:159], v[250:253]\n"
"s_waitcnt lgkmcnt(1)\n"
"v_mul_f32 v134, s60, v134\n"
"v_mul_f32 v135, s60, v135\n"
"v_mfma_f32_16x16x32_bf16 v[196:199], v[36:39], v[220:223], v[196:199]\n"
"v_cvt_pk_bf16_f32 v133, v134, v135\n"
"v_mul_f32 v136, s60, v136\n"
"v_mfma_f32_16x16x32_bf16 v[160:163], v[40:43], v[220:223], v[160:163]\n"
"ds_read_b128 v[140:143], v4 offset:53248\n"
"v_mul_f32 v137, s60, v137\n"
"v_mfma_f32_16x16x32_bf16 v[250:253], v[44:47], v[220:223], v[250:253]\n"
"v_mul_f32 v138, s60, v138\n"
"v_mul_f32 v139, s60, v139\n"
"v_cvt_pk_bf16_f32 v134, v136, v137\n"
"v_cvt_pk_bf16_f32 v135, v138, v139\n"
"s_waitcnt lgkmcnt(1)\n"
"v_mfma_f32_16x16x32_bf16 v[196:199], v[48:51], v[144:147], v[196:199]\n"
"v_mul_f32 v152, s60, v152\n"
"v_mul_f32 v153, s60, v153\n"
"v_mfma_f32_16x16x32_bf16 v[160:163], v[52:55], v[144:147], v[160:163]\n"
"ds_read_b128 v[148:151], v5 offset:53248\n"
"v_mul_f32 v154, s60, v154\n"
"v_mfma_f32_16x16x32_bf16 v[250:253], v[56:59], v[144:147], v[250:253]\n"
"s_waitcnt lgkmcnt(1)\n"
"v_mul_f32 v155, s60, v155\n"
"v_cvt_pk_bf16_f32 v26, v152, v153\n"
"v_mfma_f32_16x16x32_bf16 v[196:199], v[60:63], v[140:143], v[196:199]\n"
"v_cvt_pk_bf16_f32 v27, v154, v155\n"
"v_mfma_f32_16x16x32_bf16 v[160:163], v[64:67], v[140:143], v[160:163]\n"
"ds_read_b128 v[136:139], v4 offset:55296\n"
"v_mfma_f32_16x16x32_bf16 v[250:253], v[68:71], v[140:143], v[250:253]\n"
"s_waitcnt lgkmcnt(1)\n"
"v_mfma_f32_16x16x32_bf16 v[196:199], v[72:75], v[148:151], v[196:199]\n"
"v_mfma_f32_16x16x32_bf16 v[160:163], v[76:79], v[148:151], v[160:163]\n"
"ds_read_b128 v[144:147], v5 offset:55296\n"
"v_mfma_f32_16x16x32_bf16 v[250:253], v[80:83], v[148:151], v[250:253]\n"
"s_waitcnt lgkmcnt(1)\n"
"v_mfma_f32_16x16x32_bf16 v[220:223], v[8:11], v[136:139], 0\n"
"v_mfma_f32_16x16x32_bf16 v[152:155], v[12:15], v[136:139], 0\n"
"ds_read_b128 v[140:143], v4 offset:57344\n"
"v_mfma_f32_16x16x32_bf16 v[156:159], v[16:19], v[136:139], 0\n"
"s_waitcnt lgkmcnt(1)\n"
"v_mfma_f32_16x16x32_bf16 v[220:223], v[20:23], v[144:147], v[220:223]\n"
"v_cvt_pk_bf16_f32 v196, v196, v197\n"
"v_cvt_pk_bf16_f32 v197, v198, v199\n"
"v_cvt_pk_bf16_f32 v198, v160, v161\n"
"v_cvt_pk_bf16_f32 v199, v162, v163\n"
"v_cvt_pk_bf16_f32 v200, v250, v251\n"
"v_cvt_pk_bf16_f32 v201, v252, v253\n"
"v_mfma_f32_16x16x32_bf16 v[152:155], v[28:31], v[144:147], v[152:155]\n"
"ds_read_b128 v[136:139], v5 offset:57344\n"
"v_bfe_u32 v225, v6, 2, 6\n"
"v_mfma_f32_16x16x32_bf16 v[156:159], v[32:35], v[144:147], v[156:159]\n"
"s_waitcnt lgkmcnt(1)\n"
"ds_read_b32 v225, v225 offset:1344\n"
"v_mfma_f32_16x16x32_bf16 v[220:223], v[36:39], v[140:143], v[220:223]\n"
"v_mfma_f32_16x16x32_bf16 v[152:155], v[40:43], v[140:143], v[152:155]\n"
"s_add_u32 s69, s50, 4294966976\n"
"ds_read_b128 v[250:253], v4 offset:59392\n"
"v_mfma_f32_16x16x32_bf16 v[156:159], v[44:47], v[140:143], v[156:159]\n"
"s_waitcnt lgkmcnt(2)\n"
"v_mfma_f32_16x16x32_bf16 v[220:223], v[48:51], v[136:139], v[220:223]\n"
"v_mfma_f32_16x16x32_bf16 v[152:155], v[52:55], v[136:139], v[152:155]\n"
"s_waitcnt lgkmcnt(1)\n"
"ds_read_b128 v[144:147], v5 offset:59392\n"
"v_add_u32_sdwa v224, v225, v7 dst_sel:DWORD dst_unused:UNUSED_PAD src0_sel:WORD_0 src1_sel:DWORD\n"
"v_mfma_f32_16x16x32_bf16 v[156:159], v[56:59], v[136:139], v[156:159]\n"
"s_add_u32 s73, s50, 4294967280\n"
"v_min_u32 v177, s69, v224\n"
"s_waitcnt lgkmcnt(1)\n"
"v_mfma_f32_16x16x32_bf16 v[220:223], v[60:63], v[250:253], v[220:223]\n"
"v_mfma_f32_16x16x32_bf16 v[152:155], v[64:67], v[250:253], v[152:155]\n"
"ds_read_b128 v[140:143], v177 offset:2704\n"
"v_max_u32 v176, s73, v224\n"
"v_mfma_f32_16x16x32_bf16 v[156:159], v[68:71], v[250:253], v[156:159]\n"
"s_waitcnt lgkmcnt(1)\n"
"v_mfma_f32_16x16x32_bf16 v[220:223], v[72:75], v[144:147], v[220:223]\n"
"s_add_u32 s68, s50, 4294965936\n"
"ds_read_b128 v[136:139], v177 offset:2720\n"
"v_mfma_f32_16x16x32_bf16 v[152:155], v[76:79], v[144:147], v[152:155]\n"
"v_mfma_f32_16x16x32_bf16 v[156:159], v[80:83], v[144:147], v[156:159]\n"
"s_waitcnt lgkmcnt(1)\n"
"ds_read_b128 v[148:151], v176 offset:32\n"
"v_max_u32 v251, s68, v224\n"
"v_mfma_f32_16x16x32_bf16 v[140:143], v[184:187], v[132:135], v[140:143]\n"
"v_mfma_f32_16x16x32_bf16 v[140:143], v[186:189], v[24:27], v[140:143]\n"
"ds_read_b128 v[252:255], v176 offset:48\n"
"v_cvt_pk_bf16_f32 v220, v220, v221\n"
"v_cvt_pk_bf16_f32 v221, v222, v223\n"
"v_cvt_pk_bf16_f32 v222, v152, v153\n"
"v_cvt_pk_bf16_f32 v223, v154, v155\n"
"v_cvt_pk_bf16_f32 v224, v156, v157\n"
"v_cvt_pk_bf16_f32 v225, v158, v159\n"
"s_waitcnt lgkmcnt(2)\n"
"v_mfma_f32_16x16x32_bf16 v[136:139], v[190:193], v[132:135], v[136:139]\n"
"v_max3_f32 v174, v140, v141, v142\n"
"v_max_f32 v174, v174, v143\n"
"v_mfma_f32_16x16x32_bf16 v[136:139], v[192:195], v[24:27], v[136:139]\n"
"s_waitcnt lgkmcnt(1)\n"
"ds_read_b128 v[144:147], v251 offset:1392\n"
"v_mfma_f32_16x16x32_bf16 v[148:151], v[208:211], v[132:135], v[148:151]\n"
"v_mfma_f32_16x16x32_bf16 v[148:151], v[210:213], v[24:27], v[148:151]\n"
"s_waitcnt lgkmcnt(1)\n"
"ds_read_b128 v[152:155], v176 offset:16\n"
"s_nop 1\n"
"v_max3_f32 v174, v174, v136, v137\n"
"v_max3_f32 v174, v174, v138, v139\n"
"v_mfma_f32_16x16x32_bf16 v[252:255], v[214:217], v[132:135], v[252:255]\n"
"v_mfma_f32_16x16x32_bf16 v[252:255], v[216:219], v[24:27], v[252:255]\n"
"s_waitcnt lgkmcnt(1)\n"
"ds_read_b128 v[140:143], v251 offset:1360\n"
"v_max3_f32 v174, v174, v148, v149\n"
"v_max3_f32 v174, v174, v150, v151\n"
"v_mfma_f32_16x16x32_bf16 v[144:147], v[244:247], v[132:135], v[144:147]\n"
"v_mfma_f32_16x16x32_bf16 v[144:147], v[246:249], v[24:27], v[144:147]\n"
"s_waitcnt lgkmcnt(1)\n"
"ds_read_b128 v[136:139], v251 offset:1376\n"
"v_max3_f32 v174, v174, v252, v253\n"
"v_max3_f32 v174, v174, v254, v255\n"
"v_mfma_f32_16x16x32_bf16 v[152:155], v[202:205], v[132:135], v[152:155]\n"
"v_mfma_f32_16x16x32_bf16 v[152:155], v[204:207], v[24:27], v[152:155]\n"
"s_waitcnt lgkmcnt(1)\n"
"ds_read_b128 v[148:151], v177 offset:2736\n"
"v_max3_f32 v174, v174, v144, v145\n"
"v_max3_f32 v174, v174, v146, v147\n"
"v_mfma_f32_16x16x32_bf16 v[140:143], v[232:235], v[132:135], v[140:143]\n"
"v_mfma_f32_16x16x32_bf16 v[140:143], v[234:237], v[24:27], v[140:143]\n"
"s_waitcnt lgkmcnt(1)\n"
"ds_read_b128 v[252:255], v177 offset:2704\n"
"v_max3_f32 v174, v174, v152, v153\n"
"v_max3_f32 v174, v174, v154, v155\n"
"v_mfma_f32_16x16x32_bf16 v[136:139], v[238:241], v[132:135], v[136:139]\n"
"v_mfma_f32_16x16x32_bf16 v[136:139], v[240:243], v[24:27], v[136:139]\n"
"s_waitcnt lgkmcnt(1)\n"
"ds_read_b128 v[144:147], v177 offset:2720\n"
"v_max3_f32 v174, v174, v140, v141\n"
"v_max3_f32 v174, v174, v142, v143\n"
"v_mfma_f32_16x16x32_bf16 v[148:151], v[196:199], v[132:135], v[148:151]\n"
"v_mfma_f32_16x16x32_bf16 v[148:151], v[198:201], v[24:27], v[148:151]\n"
"s_waitcnt lgkmcnt(1)\n"
"ds_read_b128 v[152:155], v176 offset:32\n"
"v_max3_f32 v174, v174, v136, v137\n"
"v_max3_f32 v174, v174, v138, v139\n"
"v_mfma_f32_16x16x32_bf16 v[252:255], v[184:187], v[132:135], v[252:255]\n"
"v_mfma_f32_16x16x32_bf16 v[252:255], v[186:189], v[24:27], v[252:255]\n"
"ds_read_b128 v[140:143], v176 offset:48\n"
"s_nop 0\n"
"v_max3_f32 v174, v174, v148, v149\n"
"v_max3_f32 v174, v174, v150, v151\n"
"v_mov_b32 v175, v174\n"
"s_nop 1\n"
"v_permlane16_swap_b32 v174, v175\n"
"v_max_f32 v174, v174, v175\n"
"v_mov_b32 v175, v174\n"
"s_nop 1\n"
"v_permlane32_swap_b32 v174, v175\n"
"v_max_f32 v174, v174, v175\n"
"v_sub_f32 v252, v252, v174\n"
"v_exp_f32 v252, v252\n"
"v_sub_f32 v253, v253, v174\n"
"v_exp_f32 v253, v253\n"
"v_sub_f32 v254, v254, v174\n"
"v_exp_f32 v254, v254\n"
"v_sub_f32 v255, v255, v174\n"
"v_exp_f32 v255, v255\n"
"s_waitcnt lgkmcnt(2)\n"
"v_mfma_f32_16x16x32_bf16 v[144:147], v[190:193], v[132:135], v[144:147]\n"
"v_cvt_pk_bf16_f32 v252, v252, v253\n"
"v_cvt_pk_bf16_f32 v253, v254, v255\n"
"v_mfma_f32_16x16x32_bf16 v[144:147], v[192:195], v[24:27], v[144:147]\n"
"ds_write_addtid_b32 v252 offset:53248\n"
"ds_read_b128 v[136:139], v251 offset:1392\n"
"ds_write_addtid_b32 v253 offset:53504\n"
"s_waitcnt lgkmcnt(4)\n"
"v_mfma_f32_16x16x32_bf16 v[152:155], v[208:211], v[132:135], v[152:155]\n"
"v_mfma_f32_16x16x32_bf16 v[152:155], v[210:213], v[24:27], v[152:155]\n"
"ds_read_b128 v[148:151], v176 offset:16\n"
"s_nop 0\n"
"v_sub_f32 v144, v144, v174\n"
"v_exp_f32 v144, v144\n"
"v_sub_f32 v145, v145, v174\n"
"v_exp_f32 v145, v145\n"
"v_sub_f32 v146, v146, v174\n"
"v_exp_f32 v146, v146\n"
"v_sub_f32 v147, v147, v174\n"
"v_exp_f32 v147, v147\n"
"v_sub_f32 v152, v152, v174\n"
"v_exp_f32 v152, v152\n"
"v_sub_f32 v153, v153, v174\n"
"v_exp_f32 v153, v153\n"
"v_sub_f32 v154, v154, v174\n"
"v_exp_f32 v154, v154\n"
"v_sub_f32 v155, v155, v174\n"
"v_exp_f32 v155, v155\n"
"v_cvt_pk_bf16_f32 v144, v144, v145\n"
"v_cvt_pk_bf16_f32 v145, v146, v147\n"
"v_cvt_pk_bf16_f32 v152, v152, v153\n"
"v_cvt_pk_bf16_f32 v153, v154, v155\n"
"ds_write_addtid_b32 v144 offset:53760\n"
"ds_write_addtid_b32 v145 offset:54016\n"
"s_waitcnt lgkmcnt(6)\n"
"v_mfma_f32_16x16x32_bf16 v[140:143], v[214:217], v[132:135], v[140:143]\n"
"ds_write_addtid_b32 v152 offset:54272\n"
"v_mfma_f32_16x16x32_bf16 v[140:143], v[216:219], v[24:27], v[140:143]\n"
"ds_read_b128 v[252:255], v251 offset:1360\n"
"ds_write_addtid_b32 v153 offset:54528\n"
"s_waitcnt lgkmcnt(7)\n"
"v_mfma_f32_16x16x32_bf16 v[136:139], v[244:247], v[132:135], v[136:139]\n"
"v_mfma_f32_16x16x32_bf16 v[136:139], v[246:249], v[24:27], v[136:139]\n"
"ds_read_b128 v[144:147], v251 offset:1376\n"
"s_nop 1\n"
"v_sub_f32 v140, v140, v174\n"
"v_exp_f32 v140, v140\n"
"v_sub_f32 v141, v141, v174\n"
"v_exp_f32 v141, v141\n"
"v_sub_f32 v142, v142, v174\n"
"v_exp_f32 v142, v142\n"
"v_sub_f32 v143, v143, v174\n"
"v_exp_f32 v143, v143\n"
"v_sub_f32 v136, v136, v174\n"
"v_exp_f32 v136, v136\n"
"v_sub_f32 v137, v137, v174\n"
"v_exp_f32 v137, v137\n"
"v_sub_f32 v138, v138, v174\n"
"v_exp_f32 v138, v138\n"
"v_sub_f32 v139, v139, v174\n"
"v_exp_f32 v139, v139\n"
"v_cvt_pk_bf16_f32 v140, v140, v141\n"
"v_cvt_pk_bf16_f32 v136, v136, v137\n"
"v_cvt_pk_bf16_f32 v141, v142, v143\n"
"v_cvt_pk_bf16_f32 v137, v138, v139\n"
"ds_write_addtid_b32 v140 offset:54784\n"
"ds_write_addtid_b32 v136 offset:33792\n"
"ds_write_addtid_b32 v141 offset:55040\n"
"s_waitcnt lgkmcnt(9)\n"
"v_mfma_f32_16x16x32_bf16 v[148:151], v[202:205], v[132:135], v[148:151]\n"
"ds_write_addtid_b32 v137 offset:34048\n"
"v_mfma_f32_16x16x32_bf16 v[148:151], v[204:207], v[24:27], v[148:151]\n"
"s_waitcnt lgkmcnt(6)\n"
"ds_read_b128 v[152:155], v177 offset:2736\n"
"v_mfma_f32_16x16x32_bf16 v[252:255], v[232:235], v[132:135], v[252:255]\n"
"v_mfma_f32_16x16x32_bf16 v[252:255], v[234:237], v[24:27], v[252:255]\n"
"s_waitcnt lgkmcnt(1)\n"
"ds_read_b128 v[140:143], v4 offset:12288\n"
"s_barrier\n"
"s_nop 0\n"
"v_sub_f32 v148, v148, v174\n"
"v_exp_f32 v148, v148\n"
"v_sub_f32 v149, v149, v174\n"
"v_exp_f32 v149, v149\n"
"v_sub_f32 v150, v150, v174\n"
"v_exp_f32 v150, v150\n"
"v_sub_f32 v151, v151, v174\n"
"v_exp_f32 v151, v151\n"
"v_sub_f32 v252, v252, v174\n"
"v_exp_f32 v252, v252\n"
"v_sub_f32 v253, v253, v174\n"
"v_exp_f32 v253, v253\n"
"v_cvt_pk_bf16_f32 v148, v148, v149\n"
"v_cvt_pk_bf16_f32 v149, v150, v151\n"
"v_sub_f32 v254, v254, v174\n"
"v_exp_f32 v254, v254\n"
"v_sub_f32 v255, v255, v174\n"
"v_exp_f32 v255, v255\n"
"v_cvt_pk_bf16_f32 v252, v252, v253\n"
"v_cvt_pk_bf16_f32 v253, v254, v255\n"
"ds_write_addtid_b32 v148 offset:57856\n"
"ds_write_addtid_b32 v149 offset:58112\n"
"v_mfma_f32_16x16x32_bf16 v[144:147], v[238:241], v[132:135], v[144:147]\n"
"ds_write_addtid_b32 v252 offset:58368\n"
"v_mfma_f32_16x16x32_bf16 v[144:147], v[240:243], v[24:27], v[144:147]\n"
"ds_write_addtid_b32 v253 offset:58624\n"
"s_waitcnt lgkmcnt(5)\n"
"v_mfma_f32_16x16x32_bf16 v[152:155], v[196:199], v[132:135], v[152:155]\n"
"ds_read_b128 v[202:205], v5 offset:12288\n"
"v_mfma_f32_16x16x32_bf16 v[152:155], v[198:201], v[24:27], v[152:155]\n"
"s_waitcnt lgkmcnt(5)\n"
"v_mfma_f32_16x16x32_bf16 v[136:139], v[84:87], v[140:143], 0\n"
"ds_read_b128 v[148:151], v6 offset:5120\n"
"v_mfma_f32_16x16x32_bf16 v[156:159], v[88:91], v[140:143], 0\n"
"ds_read_b128 v[250:253], v4 offset:14336\n"
"v_sub_f32 v144, v144, v174\n"
"v_exp_f32 v144, v144\n"
"v_sub_f32 v145, v145, v174\n"
"v_exp_f32 v145, v145\n"
"v_sub_f32 v146, v146, v174\n"
"v_exp_f32 v146, v146\n"
"v_sub_f32 v147, v147, v174\n"
"v_exp_f32 v147, v147\n"
"v_mfma_f32_16x16x32_bf16 v[132:135], v[92:95], v[140:143], 0\n"
"s_waitcnt lgkmcnt(2)\n"
"v_cvt_pk_bf16_f32 v144, v144, v145\n"
"v_cvt_pk_bf16_f32 v145, v146, v147\n"
"v_mfma_f32_16x16x32_bf16 v[136:139], v[96:99], v[202:205], v[136:139]\n"
"ds_write_addtid_b32 v144 offset:58880\n"
"v_mfma_f32_16x16x32_bf16 v[156:159], v[100:103], v[202:205], v[156:159]\n"
"ds_write_addtid_b32 v145 offset:59136\n"
"s_waitcnt lgkmcnt(3)\n"
"v_mfma_f32_16x16x32_bf16 v[132:135], v[148:151], v[202:205], v[132:135]\n"
"s_waitcnt lgkmcnt(2)\n"
"ds_read_b128 v[140:143], v6 offset:6144\n"
"v_sub_f32 v152, v152, v174\n"
"v_exp_f32 v152, v152\n"
"v_mfma_f32_16x16x32_bf16 v[136:139], v[104:107], v[250:253], v[136:139]\n"
"ds_read_b128 v[160:163], v5 offset:14336\n"
"v_sub_f32 v153, v153, v174\n"
"v_exp_f32 v153, v153\n"
"v_mfma_f32_16x16x32_bf16 v[156:159], v[108:111], v[250:253], v[156:159]\n"
"ds_read_b128 v[202:205], v6 offset:7168\n"
"v_sub_f32 v154, v154, v174\n"
"v_exp_f32 v154, v154\n"
"v_sub_f32 v155, v155, v174\n"
"v_exp_f32 v155, v155\n"
"v_cvt_pk_bf16_f32 v152, v152, v153\n"
"s_waitcnt lgkmcnt(2)\n"
"v_mfma_f32_16x16x32_bf16 v[132:135], v[140:143], v[250:253], v[132:135]\n"
"s_waitcnt lgkmcnt(1)\n"
"ds_read_b128 v[144:147], v4 offset:16384\n"
"v_cvt_pk_bf16_f32 v153, v154, v155\n"
"v_mfma_f32_16x16x32_bf16 v[136:139], v[112:115], v[160:163], v[136:139]\n"
"ds_write_addtid_b32 v152 offset:57344\n"
"v_mfma_f32_16x16x32_bf16 v[156:159], v[116:119], v[160:163], v[156:159]\n"
"ds_write_addtid_b32 v153 offset:57600\n"
"s_waitcnt lgkmcnt(3)\n"
"v_mfma_f32_16x16x32_bf16 v[132:135], v[202:205], v[160:163], v[132:135]\n"
"s_waitcnt lgkmcnt(2)\n"
"ds_read_b128 v[140:143], v6 offset:8192\n"
"ds_read_b128 v[148:151], v5 offset:16384\n"
"v_mfma_f32_16x16x32_bf16 v[136:139], v[0:3], v[144:147], v[136:139]\n"
"ds_read_b128 v[164:167], v6 offset:9216\n"
"v_mfma_f32_16x16x32_bf16 v[156:159], v[120:123], v[144:147], v[156:159]\n"
"s_waitcnt lgkmcnt(3)\n"
"s_barrier\n"
"s_waitcnt lgkmcnt(2)\n"
"v_mfma_f32_16x16x32_bf16 v[132:135], v[140:143], v[144:147], v[132:135]\n"
"s_waitcnt lgkmcnt(1)\n"
"ds_read_b128 v[202:205], v4 offset:18432\n"
"v_mfma_f32_16x16x32_bf16 v[136:139], v[124:127], v[148:151], v[136:139]\n"
"v_mfma_f32_16x16x32_bf16 v[156:159], v[128:131], v[148:151], v[156:159]\n"
"s_waitcnt lgkmcnt(1)\n"
"v_mfma_f32_16x16x32_bf16 v[132:135], v[164:167], v[148:151], v[132:135]\n"
"s_waitcnt lgkmcnt(0)\n"
"ds_read_b128 v[152:155], v5 offset:18432\n"
"v_mfma_f32_16x16x32_bf16 v[250:253], v[8:11], v[202:205], 0\n"
"v_mfma_f32_16x16x32_bf16 v[140:143], v[12:15], v[202:205], 0\n"
"v_mfma_f32_16x16x32_bf16 v[144:147], v[16:19], v[202:205], 0\n"
"s_waitcnt lgkmcnt(0)\n"
"ds_read_b128 v[160:163], v4 offset:20480\n"
"v_mfma_f32_16x16x32_bf16 v[250:253], v[20:23], v[152:155], v[250:253]\n"
"v_mul_f32 v136, s60, v136\n"
"v_mul_f32 v137, s60, v137\n"
"v_mfma_f32_16x16x32_bf16 v[140:143], v[28:31], v[152:155], v[140:143]\n"
"v_cvt_pk_bf16_f32 v136, v136, v137\n"
"v_mul_f32 v138, s60, v138\n"
"v_mfma_f32_16x16x32_bf16 v[144:147], v[32:35], v[152:155], v[144:147]\n"
"ds_read_b128 v[202:205], v5 offset:20480\n"
"v_mul_f32 v139, s60, v139\n"
"v_cvt_pk_bf16_f32 v137, v138, v139\n"
"v_mul_f32 v156, s60, v156\n"
"v_mul_f32 v157, s60, v157\n"
"v_mul_f32 v158, s60, v158\n"
"v_mul_f32 v159, s60, v159\n"
"v_cvt_pk_bf16_f32 v138, v156, v157\n"
"s_waitcnt lgkmcnt(1)\n"
"v_mfma_f32_16x16x32_bf16 v[250:253], v[36:39], v[160:163], v[250:253]\n"
"v_cvt_pk_bf16_f32 v139, v158, v159\n"
"v_mul_f32 v132, s60, v132\n"
"v_mfma_f32_16x16x32_bf16 v[140:143], v[40:43], v[160:163], v[140:143]\n"
"v_mul_f32 v133, s60, v133\n"
"v_mul_f32 v134, s60, v134\n"
"v_mfma_f32_16x16x32_bf16 v[144:147], v[44:47], v[160:163], v[144:147]\n"
"s_waitcnt lgkmcnt(0)\n"
"ds_read_b128 v[148:151], v4 offset:22528\n"
"v_mul_f32 v135, s60, v135\n"
"v_mfma_f32_16x16x32_bf16 v[250:253], v[48:51], v[202:205], v[250:253]\n"
"v_cvt_pk_bf16_f32 v26, v132, v133\n"
"v_cvt_pk_bf16_f32 v27, v134, v135\n"
"v_mfma_f32_16x16x32_bf16 v[140:143], v[52:55], v[202:205], v[140:143]\n"
"v_mfma_f32_16x16x32_bf16 v[144:147], v[56:59], v[202:205], v[144:147]\n"
"s_waitcnt lgkmcnt(0)\n"
"ds_read_b128 v[152:155], v5 offset:22528\n"
"v_mfma_f32_16x16x32_bf16 v[250:253], v[60:63], v[148:151], v[250:253]\n"
"v_mfma_f32_16x16x32_bf16 v[140:143], v[64:67], v[148:151], v[140:143]\n"
"v_mfma_f32_16x16x32_bf16 v[144:147], v[68:71], v[148:151], v[144:147]\n"
"s_waitcnt lgkmcnt(0)\n"
"ds_read_b128 v[132:135], v4 offset:36864\n"
"v_mfma_f32_16x16x32_bf16 v[250:253], v[72:75], v[152:155], v[250:253]\n"
"v_mfma_f32_16x16x32_bf16 v[140:143], v[76:79], v[152:155], v[140:143]\n"
"v_mfma_f32_16x16x32_bf16 v[144:147], v[80:83], v[152:155], v[144:147]\n"
"s_waitcnt lgkmcnt(0)\n"
"ds_read_b128 v[156:159], v5 offset:36864\n"
"v_mfma_f32_16x16x32_bf16 v[202:205], v[8:11], v[132:135], 0\n"
"v_mfma_f32_16x16x32_bf16 v[148:151], v[12:15], v[132:135], 0\n"
"v_mfma_f32_16x16x32_bf16 v[160:163], v[16:19], v[132:135], 0\n"
"s_waitcnt lgkmcnt(0)\n"
"ds_read_b128 v[164:167], v4 offset:38912\n"
"v_mfma_f32_16x16x32_bf16 v[202:205], v[20:23], v[156:159], v[202:205]\n"
"v_cvt_pk_bf16_f32 v250, v250, v251\n"
"v_cvt_pk_bf16_f32 v251, v252, v253\n"
"v_cvt_pk_bf16_f32 v252, v140, v141\n"
"v_cvt_pk_bf16_f32 v253, v142, v143\n"
"v_cvt_pk_bf16_f32 v254, v144, v145\n"
"v_cvt_pk_bf16_f32 v255, v146, v147\n"
"v_mfma_f32_16x16x32_bf16 v[148:151], v[28:31], v[156:159], v[148:151]\n"
"v_mfma_f32_16x16x32_bf16 v[160:163], v[32:35], v[156:159], v[160:163]\n"
"s_waitcnt lgkmcnt(0)\n"
"ds_read_b128 v[132:135], v5 offset:38912\n"
"v_mfma_f32_16x16x32_bf16 v[202:205], v[36:39], v[164:167], v[202:205]\n"
"v_bfe_u32 v177, v6, 2, 6\n"
"v_mfma_f32_16x16x32_bf16 v[148:151], v[40:43], v[164:167], v[148:151]\n"
"ds_read_b32 v177, v177 offset:1408\n"
"v_mfma_f32_16x16x32_bf16 v[160:163], v[44:47], v[164:167], v[160:163]\n"
"s_add_u32 s70, s50, 4294967264\n"
"ds_read_b128 v[140:143], v4 offset:40960\n"
"s_waitcnt lgkmcnt(2)\n"
"v_mfma_f32_16x16x32_bf16 v[202:205], v[48:51], v[132:135], v[202:205]\n"
"v_mfma_f32_16x16x32_bf16 v[148:151], v[52:55], v[132:135], v[148:151]\n"
"v_mfma_f32_16x16x32_bf16 v[160:163], v[56:59], v[132:135], v[160:163]\n"
"s_waitcnt lgkmcnt(1)\n"
"ds_read_b128 v[144:147], v5 offset:40960\n"
"v_add_u32_sdwa v176, v177, v7 dst_sel:DWORD dst_unused:UNUSED_PAD src0_sel:WORD_0 src1_sel:DWORD\n"
"s_nop 0\n"
"v_max_u32 v175, s70, v176\n"
"v_add_u32_sdwa v177, sext(v177), v176 dst_sel:DWORD dst_unused:UNUSED_PAD src0_sel:WORD_1 src1_sel:DWORD\n"
"s_waitcnt lgkmcnt(1)\n"
"v_mfma_f32_16x16x32_bf16 v[202:205], v[60:63], v[140:143], v[202:205]\n"
"s_add_u32 s72, s50, 4294966960\n"
"v_max_u32 v174, s70, v177\n"
"v_mfma_f32_16x16x32_bf16 v[148:151], v[64:67], v[140:143], v[148:151]\n"
"v_mfma_f32_16x16x32_bf16 v[160:163], v[68:71], v[140:143], v[160:163]\n"
"s_waitcnt lgkmcnt(0)\n"
"ds_read_b128 v[132:135], v175 offset:48\n"
"v_min_u32 v173, s72, v176\n"
"v_mfma_f32_16x16x32_bf16 v[202:205], v[72:75], v[144:147], v[202:205]\n"
"v_mfma_f32_16x16x32_bf16 v[148:151], v[76:79], v[144:147], v[148:151]\n"
"ds_read_b128 v[152:155], v174 offset:64\n"
"v_mfma_f32_16x16x32_bf16 v[160:163], v[80:83], v[144:147], v[160:163]\n"
"s_waitcnt lgkmcnt(1)\n"
"v_mfma_f32_16x16x32_bf16 v[132:135], v[214:217], v[136:139], v[132:135]\n"
"s_add_u32 s71, s50, 1008\n"
"ds_read_b128 v[140:143], v173 offset:2720\n"
"v_mfma_f32_16x16x32_bf16 v[132:135], v[216:219], v[24:27], v[132:135]\n"
"s_waitcnt lgkmcnt(1)\n"
"v_mfma_f32_16x16x32_bf16 v[152:155], v[220:223], v[136:139], v[152:155]\n"
"ds_read_b128 v[156:159], v173 offset:2736\n"
"v_cvt_pk_bf16_f32 v202, v202, v203\n"
"v_cvt_pk_bf16_f32 v203, v204, v205\n"
"v_cvt_pk_bf16_f32 v204, v148, v149\n"
"v_cvt_pk_bf16_f32 v205, v150, v151\n"
"v_cvt_pk_bf16_f32 v206, v160, v161\n"
"v_cvt_pk_bf16_f32 v207, v162, v163\n"
"v_mfma_f32_16x16x32_bf16 v[152:155], v[222:225], v[24:27], v[152:155]\n"
"s_waitcnt lgkmcnt(1)\n"
"v_max3_f32 v170, v132, v133, v134\n"
"v_max_f32 v170, v170, v135\n"
"v_mfma_f32_16x16x32_bf16 v[140:143], v[190:193], v[136:139], v[140:143]\n"
"ds_read_b128 v[144:147], v175 offset:32\n"
"v_min_u32 v169, s71, v176\n"
"v_mfma_f32_16x16x32_bf16 v[140:143], v[192:195], v[24:27], v[140:143]\n"
"s_waitcnt lgkmcnt(1)\n"
"v_max3_f32 v170, v170, v152, v153\n"
"v_max3_f32 v170, v170, v154, v155\n"
"v_mfma_f32_16x16x32_bf16 v[156:159], v[196:199], v[136:139], v[156:159]\n"
"ds_read_b128 v[132:135], v169 offset:1376\n"
"v_min_u32 v168, s71, v177\n"
"v_mfma_f32_16x16x32_bf16 v[156:159], v[198:201], v[24:27], v[156:159]\n"
"s_waitcnt lgkmcnt(1)\n"
"v_max3_f32 v170, v170, v140, v141\n"
"v_max3_f32 v170, v170, v142, v143\n"
"v_mfma_f32_16x16x32_bf16 v[144:147], v[208:211], v[136:139], v[144:147]\n"
"ds_read_b128 v[148:151], v169 offset:1392\n"
"v_min_u32 v176, s72, v177\n"
"v_mfma_f32_16x16x32_bf16 v[144:147], v[210:213], v[24:27], v[144:147]\n"
"s_waitcnt lgkmcnt(1)\n"
"v_max3_f32 v170, v170, v156, v157\n"
"v_max3_f32 v170, v170, v158, v159\n"
"v_mfma_f32_16x16x32_bf16 v[132:135], v[238:241], v[136:139], v[132:135]\n"
"ds_read_b128 v[140:143], v168 offset:1408\n"
"v_mfma_f32_16x16x32_bf16 v[132:135], v[240:243], v[24:27], v[132:135]\n"
"s_waitcnt lgkmcnt(1)\n"
"s_nop 0\n"
"v_max3_f32 v170, v170, v144, v145\n"
"v_max3_f32 v170, v170, v146, v147\n"
"v_mfma_f32_16x16x32_bf16 v[148:151], v[244:247], v[136:139], v[148:151]\n"
"ds_read_b128 v[152:155], v176 offset:2752\n"
"v_mfma_f32_16x16x32_bf16 v[148:151], v[246:249], v[24:27], v[148:151]\n"
"s_waitcnt lgkmcnt(1)\n"
"v_max3_f32 v170, v170, v132, v133\n"
"v_max3_f32 v170, v170, v134, v135\n"
"v_mfma_f32_16x16x32_bf16 v[140:143], v[250:253], v[136:139], v[140:143]\n"
"ds_read_b128 v[144:147], v175 offset:48\n"
"v_mfma_f32_16x16x32_bf16 v[140:143], v[252:255], v[24:27], v[140:143]\n"
"s_waitcnt lgkmcnt(1)\n"
"s_nop 0\n"
"v_max3_f32 v170, v170, v148, v149\n"
"v_max3_f32 v170, v170, v150, v151\n"
"v_mfma_f32_16x16x32_bf16 v[152:155], v[202:205], v[136:139], v[152:155]\n"
"ds_read_b128 v[132:135], v174 offset:64\n"
"v_mfma_f32_16x16x32_bf16 v[152:155], v[204:207], v[24:27], v[152:155]\n"
"s_waitcnt lgkmcnt(1)\n"
"v_max3_f32 v170, v170, v140, v141\n"
"v_max3_f32 v170, v170, v142, v143\n"
"v_mfma_f32_16x16x32_bf16 v[144:147], v[214:217], v[136:139], v[144:147]\n"
"ds_read_b128 v[148:151], v173 offset:2720\n"
"v_mfma_f32_16x16x32_bf16 v[144:147], v[216:219], v[24:27], v[144:147]\n"
"s_nop 1\n"
"v_max3_f32 v170, v170, v152, v153\n"
"v_max3_f32 v170, v170, v154, v155\n"
"v_mov_b32 v171, v170\n"
"s_nop 1\n"
"v_permlane16_swap_b32 v170, v171\n"
"v_max_f32 v170, v170, v171\n"
"v_mov_b32 v171, v170\n"
"s_nop 1\n"
"v_permlane32_swap_b32 v170, v171\n"
"v_max_f32 v170, v170, v171\n"
"v_sub_f32 v144, v144, v170\n"
"v_exp_f32 v144, v144\n"
"v_sub_f32 v145, v145, v170\n"
"v_exp_f32 v145, v145\n"
"v_sub_f32 v146, v146, v170\n"
"v_exp_f32 v146, v146\n"
"v_sub_f32 v147, v147, v170\n"
"v_exp_f32 v147, v147\n"
"s_waitcnt lgkmcnt(1)\n"
"v_mfma_f32_16x16x32_bf16 v[132:135], v[220:223], v[136:139], v[132:135]\n"
"ds_read_b128 v[214:217], v173 offset:2736\n"
"v_cvt_pk_bf16_f32 v144, v144, v145\n"
"v_mfma_f32_16x16x32_bf16 v[132:135], v[222:225], v[24:27], v[132:135]\n"
"ds_write_addtid_b32 v144 offset:55296\n"
"v_cvt_pk_bf16_f32 v145, v146, v147\n"
"ds_write_addtid_b32 v145 offset:55552\n"
"s_waitcnt lgkmcnt(3)\n"
"v_mfma_f32_16x16x32_bf16 v[148:151], v[190:193], v[136:139], v[148:151]\n"
"ds_read_b128 v[218:221], v175 offset:32\n"
"v_mfma_f32_16x16x32_bf16 v[148:151], v[192:195], v[24:27], v[148:151]\n"
"s_nop 0\n"
"v_sub_f32 v132, v132, v170\n"
"v_exp_f32 v132, v132\n"
"v_sub_f32 v133, v133, v170\n"
"v_exp_f32 v133, v133\n"
"v_sub_f32 v134, v134, v170\n"
"v_exp_f32 v134, v134\n"
"v_sub_f32 v135, v135, v170\n"
"v_exp_f32 v135, v135\n"
"v_sub_f32 v150, v150, v170\n"
"v_exp_f32 v150, v150\n"
"v_sub_f32 v151, v151, v170\n"
"v_exp_f32 v151, v151\n"
"v_cvt_pk_bf16_f32 v132, v132, v133\n"
"v_cvt_pk_bf16_f32 v133, v134, v135\n"
"v_cvt_pk_bf16_f32 v149, v150, v151\n"
"ds_write_addtid_b32 v132 offset:55808\n"
"ds_write_addtid_b32 v133 offset:56064\n"
"s_waitcnt lgkmcnt(5)\n"
"v_mfma_f32_16x16x32_bf16 v[214:217], v[196:199], v[136:139], v[214:217]\n"
"ds_read_b128 v[222:225], v169 offset:1376\n"
"v_mfma_f32_16x16x32_bf16 v[214:217], v[198:201], v[24:27], v[214:217]\n"
"ds_write_addtid_b32 v149 offset:56576\n"
"s_waitcnt lgkmcnt(4)\n"
"v_mfma_f32_16x16x32_bf16 v[218:221], v[208:211], v[136:139], v[218:221]\n"
"ds_read_b128 v[140:143], v169 offset:1392\n"
"v_mfma_f32_16x16x32_bf16 v[218:221], v[210:213], v[24:27], v[218:221]\n"
"s_nop 2\n"
"v_sub_f32 v214, v214, v170\n"
"v_exp_f32 v214, v214\n"
"v_sub_f32 v215, v215, v170\n"
"v_exp_f32 v215, v215\n"
"v_sub_f32 v216, v216, v170\n"
"v_exp_f32 v216, v216\n"
"v_sub_f32 v217, v217, v170\n"
"v_exp_f32 v217, v217\n"
"v_cvt_pk_bf16_f32 v214, v214, v215\n"
"v_cvt_pk_bf16_f32 v215, v216, v217\n"
"v_sub_f32 v220, v220, v170\n"
"v_exp_f32 v220, v220\n"
"v_sub_f32 v221, v221, v170\n"
"v_exp_f32 v221, v221\n"
"ds_write_addtid_b32 v214 offset:56832\n"
"v_cvt_pk_bf16_f32 v219, v220, v221\n"
"ds_write_addtid_b32 v215 offset:57088\n"
"s_waitcnt lgkmcnt(4)\n"
"v_mfma_f32_16x16x32_bf16 v[222:225], v[238:241], v[136:139], v[222:225]\n"
"ds_read_b128 v[208:211], v168 offset:1408\n"
"v_mfma_f32_16x16x32_bf16 v[222:225], v[240:243], v[24:27], v[222:225]\n"
"s_waitcnt lgkmcnt(1)\n"
"s_barrier\n"
"ds_write_addtid_b32 v219 offset:58112\n"
"v_mfma_f32_16x16x32_bf16 v[140:143], v[244:247], v[136:139], v[140:143]\n"
"ds_read_b128 v[212:215], v176 offset:2752\n"
"v_mfma_f32_16x16x32_bf16 v[140:143], v[246:249], v[24:27], v[140:143]\n"
"s_nop 1\n"
"v_sub_f32 v224, v224, v170\n"
"v_exp_f32 v224, v224\n"
"v_sub_f32 v225, v225, v170\n"
"v_exp_f32 v225, v225\n"
"s_nop 0\n"
"v_cvt_pk_bf16_f32 v223, v224, v225\n"
"ds_write_addtid_b32 v223 offset:58624\n"
"s_waitcnt lgkmcnt(3)\n"
"v_mfma_f32_16x16x32_bf16 v[208:211], v[250:253], v[136:139], v[208:211]\n"
"ds_read_b128 v[216:219], v4 offset:43008\n"
"v_sub_f32 v140, v140, v170\n"
"v_exp_f32 v140, v140\n"
"v_sub_f32 v141, v141, v170\n"
"v_exp_f32 v141, v141\n"
"v_sub_f32 v142, v142, v170\n"
"v_exp_f32 v142, v142\n"
"v_sub_f32 v143, v143, v170\n"
"v_exp_f32 v143, v143\n"
"v_mfma_f32_16x16x32_bf16 v[208:211], v[252:255], v[24:27], v[208:211]\n"
"v_cvt_pk_bf16_f32 v140, v140, v141\n"
"v_cvt_pk_bf16_f32 v141, v142, v143\n"
"ds_write_addtid_b32 v140 offset:58880\n"
"ds_write_addtid_b32 v141 offset:59136\n"
"s_waitcnt lgkmcnt(4)\n"
"v_mfma_f32_16x16x32_bf16 v[212:215], v[202:205], v[136:139], v[212:215]\n"
"v_mfma_f32_16x16x32_bf16 v[212:215], v[204:207], v[24:27], v[212:215]\n"
"s_waitcnt lgkmcnt(2)\n"
"ds_read_b128 v[220:223], v5 offset:43008\n"
"v_mfma_f32_16x16x32_bf16 v[132:135], v[84:87], v[216:219], 0\n"
"v_sub_f32 v208, v208, v170\n"
"v_exp_f32 v208, v208\n"
"v_sub_f32 v209, v209, v170\n"
"v_exp_f32 v209, v209\n"
"v_mfma_f32_16x16x32_bf16 v[136:139], v[88:91], v[216:219], 0\n"
"ds_read_b128 v[140:143], v6 offset:5120\n"
"v_cvt_pk_bf16_f32 v208, v208, v209\n"
"v_sub_f32 v210, v210, v170\n"
"v_exp_f32 v210, v210\n"
"v_sub_f32 v211, v211, v170\n"
"v_exp_f32 v211, v211\n"
"v_mfma_f32_16x16x32_bf16 v[144:147], v[92:95], v[216:219], 0\n"
"s_waitcnt lgkmcnt(1)\n"
"ds_read_b128 v[148:151], v4 offset:45056\n"
"v_cvt_pk_bf16_f32 v209, v210, v211\n"
"v_mfma_f32_16x16x32_bf16 v[132:135], v[96:99], v[220:223], v[132:135]\n"
"ds_write_addtid_b32 v208 offset:34304\n"
"v_mfma_f32_16x16x32_bf16 v[136:139], v[100:103], v[220:223], v[136:139]\n"
"ds_write_addtid_b32 v209 offset:34560\n"
"s_waitcnt lgkmcnt(3)\n"
"v_mfma_f32_16x16x32_bf16 v[144:147], v[140:143], v[220:223], v[144:147]\n"
"ds_read_b128 v[216:219], v6 offset:6144\n"
"v_sub_f32 v212, v212, v170\n"
"v_exp_f32 v212, v212\n"
"v_sub_f32 v213, v213, v170\n"
"v_exp_f32 v213, v213\n"
"s_waitcnt lgkmcnt(3)\n"
"v_mfma_f32_16x16x32_bf16 v[132:135], v[104:107], v[148:151], v[132:135]\n"
"ds_read_b128 v[208:211], v5 offset:45056\n"
"v_cvt_pk_bf16_f32 v212, v212, v213\n"
"v_mfma_f32_16x16x32_bf16 v[136:139], v[108:111], v[148:151], v[136:139]\n"
"ds_read_b128 v[220:223], v6 offset:7168\n"
"v_sub_f32 v214, v214, v170\n"
"v_exp_f32 v214, v214\n"
"v_sub_f32 v215, v215, v170\n"
"v_exp_f32 v215, v215\n"
"s_waitcnt lgkmcnt(2)\n"
"v_mfma_f32_16x16x32_bf16 v[144:147], v[216:219], v[148:151], v[144:147]\n"
"ds_read_b128 v[140:143], v4 offset:47104\n"
"v_cvt_pk_bf16_f32 v213, v214, v215\n"
"ds_write_addtid_b32 v212 offset:57344\n"
"ds_write_addtid_b32 v213 offset:57600\n"
"s_waitcnt lgkmcnt(4)\n"
"v_mfma_f32_16x16x32_bf16 v[132:135], v[112:115], v[208:211], v[132:135]\n"
"v_mfma_f32_16x16x32_bf16 v[136:139], v[116:119], v[208:211], v[136:139]\n"
"s_waitcnt lgkmcnt(3)\n"
"ds_read_b128 v[216:219], v6 offset:8192\n"
"v_mfma_f32_16x16x32_bf16 v[144:147], v[220:223], v[208:211], v[144:147]\n"
"s_waitcnt lgkmcnt(3)\n"
"ds_read_b128 v[148:151], v5 offset:47104\n"
"v_mfma_f32_16x16x32_bf16 v[132:135], v[0:3], v[140:143], v[132:135]\n"
"s_waitcnt lgkmcnt(2)\n"
"s_barrier\n"
"v_mfma_f32_16x16x32_bf16 v[136:139], v[120:123], v[140:143], v[136:139]\n"
"s_waitcnt lgkmcnt(1)\n"
"ds_read_b128 v[212:215], v6 offset:9216\n"
"v_mfma_f32_16x16x32_bf16 v[144:147], v[216:219], v[140:143], v[144:147]\n"
"s_waitcnt lgkmcnt(1)\n"
"ds_read_b128 v[220:223], v4 offset:24576\n"
"v_mfma_f32_16x16x32_bf16 v[132:135], v[124:127], v[148:151], v[132:135]\n"
"v_mfma_f32_16x16x32_bf16 v[136:139], v[128:131], v[148:151], v[136:139]\n"
"s_waitcnt lgkmcnt(1)\n"
"v_mfma_f32_16x16x32_bf16 v[144:147], v[212:215], v[148:151], v[144:147]\n"
"s_waitcnt lgkmcnt(0)\n"
"ds_read_b128 v[152:155], v5 offset:24576\n"
"v_mfma_f32_16x16x32_bf16 v[208:211], v[8:11], v[220:223], 0\n"
"v_mfma_f32_16x16x32_bf16 v[140:143], v[12:15], v[220:223], 0\n"
"v_mfma_f32_16x16x32_bf16 v[156:159], v[16:19], v[220:223], 0\n"
"s_waitcnt lgkmcnt(0)\n"
"ds_read_b128 v[216:219], v4 offset:26624\n"
"v_mfma_f32_16x16x32_bf16 v[208:211], v[20:23], v[152:155], v[208:211]\n"
"v_mul_f32 v132, s60, v132\n"
"v_mul_f32 v133, s60, v133\n"
"v_mfma_f32_16x16x32_bf16 v[140:143], v[28:31], v[152:155], v[140:143]\n"
"v_cvt_pk_bf16_f32 v132, v132, v133\n"
"v_mul_f32 v134, s60, v134\n"
"v_mfma_f32_16x16x32_bf16 v[156:159], v[32:35], v[152:155], v[156:159]\n"
"ds_read_b128 v[212:215], v5 offset:26624\n"
"v_mul_f32 v135, s60, v135\n"
"v_cvt_pk_bf16_f32 v133, v134, v135\n"
"v_mul_f32 v136, s60, v136\n"
"v_mul_f32 v137, s60, v137\n"
"v_mul_f32 v138, s60, v138\n"
"v_mul_f32 v139, s60, v139\n"
"s_waitcnt lgkmcnt(1)\n"
"v_mfma_f32_16x16x32_bf16 v[208:211], v[36:39], v[216:219], v[208:211]\n"
"v_cvt_pk_bf16_f32 v134, v136, v137\n"
"v_cvt_pk_bf16_f32 v135, v138, v139\n"
"v_mfma_f32_16x16x32_bf16 v[140:143], v[40:43], v[216:219], v[140:143]\n"
"v_mul_f32 v144, s60, v144\n"
"v_mul_f32 v145, s60, v145\n"
"v_mfma_f32_16x16x32_bf16 v[156:159], v[44:47], v[216:219], v[156:159]\n"
"s_waitcnt lgkmcnt(0)\n"
"ds_read_b128 v[220:223], v4 offset:28672\n"
"v_mul_f32 v146, s60, v146\n"
"v_mul_f32 v147, s60, v147\n"
"v_mfma_f32_16x16x32_bf16 v[208:211], v[48:51], v[212:215], v[208:211]\n"
"v_cvt_pk_bf16_f32 v26, v144, v145\n"
"v_cvt_pk_bf16_f32 v27, v146, v147\n"
"v_mfma_f32_16x16x32_bf16 v[140:143], v[52:55], v[212:215], v[140:143]\n"
"v_mfma_f32_16x16x32_bf16 v[156:159], v[56:59], v[212:215], v[156:159]\n"
"s_waitcnt lgkmcnt(0)\n"
"ds_read_b128 v[136:139], v5 offset:28672\n"
"v_mfma_f32_16x16x32_bf16 v[208:211], v[60:63], v[220:223], v[208:211]\n"
"v_mfma_f32_16x16x32_bf16 v[140:143], v[64:67], v[220:223], v[140:143]\n"
"v_mfma_f32_16x16x32_bf16 v[156:159], v[68:71], v[220:223], v[156:159]\n"
"s_waitcnt lgkmcnt(0)\n"
"ds_read_b128 v[144:147], v4 offset:30720\n"
"v_mfma_f32_16x16x32_bf16 v[208:211], v[72:75], v[136:139], v[208:211]\n"
"v_mfma_f32_16x16x32_bf16 v[140:143], v[76:79], v[136:139], v[140:143]\n"
"v_mfma_f32_16x16x32_bf16 v[156:159], v[80:83], v[136:139], v[156:159]\n"
"s_waitcnt lgkmcnt(0)\n"
"ds_read_b128 v[148:151], v5 offset:30720\n"
"v_mfma_f32_16x16x32_bf16 v[214:217], v[8:11], v[144:147], 0\n"
"v_mfma_f32_16x16x32_bf16 v[220:223], v[12:15], v[144:147], 0\n"
"v_mfma_f32_16x16x32_bf16 v[152:155], v[16:19], v[144:147], 0\n"
"s_waitcnt lgkmcnt(0)\n"
"ds_read_b128 v[160:163], v4 offset:32768\n"
"v_mfma_f32_16x16x32_bf16 v[214:217], v[20:23], v[148:151], v[214:217]\n"
"v_cvt_pk_bf16_f32 v208, v208, v209\n"
"v_cvt_pk_bf16_f32 v209, v210, v211\n"
"v_cvt_pk_bf16_f32 v210, v140, v141\n"
"v_cvt_pk_bf16_f32 v211, v142, v143\n"
"v_cvt_pk_bf16_f32 v212, v156, v157\n"
"v_cvt_pk_bf16_f32 v213, v158, v159\n"
"v_mfma_f32_16x16x32_bf16 v[220:223], v[28:31], v[148:151], v[220:223]\n"
"v_mfma_f32_16x16x32_bf16 v[152:155], v[32:35], v[148:151], v[152:155]\n"
"s_waitcnt lgkmcnt(0)\n"
"ds_read_b128 v[136:139], v5 offset:32768\n"
"v_mfma_f32_16x16x32_bf16 v[214:217], v[36:39], v[160:163], v[214:217]\n"
"v_bfe_u32 v225, v6, 2, 6\n"
"v_mfma_f32_16x16x32_bf16 v[220:223], v[40:43], v[160:163], v[220:223]\n"
"ds_read_b32 v225, v225 offset:1472\n"
"v_mfma_f32_16x16x32_bf16 v[152:155], v[44:47], v[160:163], v[152:155]\n"
"s_add_u32 s68, s50, 4294966992\n"
"ds_read_b128 v[140:143], v4 offset:34816\n"
"s_waitcnt lgkmcnt(2)\n"
"v_mfma_f32_16x16x32_bf16 v[214:217], v[48:51], v[136:139], v[214:217]\n"
"v_mfma_f32_16x16x32_bf16 v[220:223], v[52:55], v[136:139], v[220:223]\n"
"v_mfma_f32_16x16x32_bf16 v[152:155], v[56:59], v[136:139], v[152:155]\n"
"s_waitcnt lgkmcnt(1)\n"
"ds_read_b128 v[144:147], v5 offset:34816\n"
"v_add_u32_sdwa v224, v225, v7 dst_sel:DWORD dst_unused:UNUSED_PAD src0_sel:WORD_0 src1_sel:DWORD\n"
"s_nop 0\n"
"v_min_u32 v177, s68, v224\n"
"s_waitcnt lgkmcnt(1)\n"
"v_mfma_f32_16x16x32_bf16 v[214:217], v[60:63], v[140:143], v[214:217]\n"
"s_add_u32 s73, s50, 4294965952\n"
"v_mfma_f32_16x16x32_bf16 v[220:223], v[64:67], v[140:143], v[220:223]\n"
"v_mfma_f32_16x16x32_bf16 v[152:155], v[68:71], v[140:143], v[152:155]\n"
"s_waitcnt lgkmcnt(0)\n"
"ds_read_b128 v[136:139], v177 offset:2688\n"
"v_max_u32 v176, s73, v224\n"
"v_mfma_f32_16x16x32_bf16 v[214:217], v[72:75], v[144:147], v[214:217]\n"
"v_mfma_f32_16x16x32_bf16 v[220:223], v[76:79], v[144:147], v[220:223]\n"
"ds_read_b128 v[148:151], v177 offset:2704\n"
"v_mfma_f32_16x16x32_bf16 v[152:155], v[80:83], v[144:147], v[152:155]\n"
"s_waitcnt lgkmcnt(1)\n"
"v_mfma_f32_16x16x32_bf16 v[136:139], v[178:181], v[132:135], v[136:139]\n"
"ds_read_b128 v[140:143], v176 offset:1344\n"
"v_mfma_f32_16x16x32_bf16 v[136:139], v[180:183], v[24:27], v[136:139]\n"
"s_waitcnt lgkmcnt(1)\n"
"v_mfma_f32_16x16x32_bf16 v[148:151], v[184:187], v[132:135], v[148:151]\n"
"ds_read_b128 v[156:159], v176 offset:1360\n"
"v_cvt_pk_bf16_f32 v214, v214, v215\n"
"v_cvt_pk_bf16_f32 v215, v216, v217\n"
"v_cvt_pk_bf16_f32 v216, v220, v221\n"
"v_cvt_pk_bf16_f32 v217, v222, v223\n"
"v_cvt_pk_bf16_f32 v218, v152, v153\n"
"v_cvt_pk_bf16_f32 v219, v154, v155\n"
"v_mfma_f32_16x16x32_bf16 v[148:151], v[186:189], v[24:27], v[148:151]\n"
"s_waitcnt lgkmcnt(1)\n"
"v_max3_f32 v224, v136, v137, v138\n"
"v_max_f32 v224, v224, v139\n"
"v_mfma_f32_16x16x32_bf16 v[140:143], v[226:229], v[132:135], v[140:143]\n"
"ds_read_b128 v[220:223], v176 offset:1376\n"
"v_mfma_f32_16x16x32_bf16 v[140:143], v[228:231], v[24:27], v[140:143]\n"
"s_waitcnt lgkmcnt(1)\n"
"s_nop 0\n"
"v_max3_f32 v224, v224, v148, v149\n"
"v_max3_f32 v224, v224, v150, v151\n"
"v_mfma_f32_16x16x32_bf16 v[156:159], v[232:235], v[132:135], v[156:159]\n"
"ds_read_b128 v[136:139], v177 offset:2720\n"
"v_mfma_f32_16x16x32_bf16 v[156:159], v[234:237], v[24:27], v[156:159]\n"
"s_waitcnt lgkmcnt(1)\n"
"v_max3_f32 v224, v224, v140, v141\n"
"v_max3_f32 v224, v224, v142, v143\n"
"v_mfma_f32_16x16x32_bf16 v[220:223], v[238:241], v[132:135], v[220:223]\n"
"ds_read_b128 v[144:147], v177 offset:2688\n"
"v_mfma_f32_16x16x32_bf16 v[220:223], v[240:243], v[24:27], v[220:223]\n"
"s_waitcnt lgkmcnt(1)\n"
"s_nop 0\n"
"v_max3_f32 v224, v224, v156, v157\n"
"v_max3_f32 v224, v224, v158, v159\n"
"v_mfma_f32_16x16x32_bf16 v[136:139], v[190:193], v[132:135], v[136:139]\n"
"ds_read_b128 v[140:143], v177 offset:2704\n"
"v_mfma_f32_16x16x32_bf16 v[136:139], v[192:195], v[24:27], v[136:139]\n"
"s_waitcnt lgkmcnt(1)\n"
"v_max3_f32 v224, v224, v220, v221\n"
"v_max3_f32 v224, v224, v222, v223\n"
"v_mfma_f32_16x16x32_bf16 v[144:147], v[178:181], v[132:135], v[144:147]\n"
"ds_read_b128 v[148:151], v176 offset:1344\n"
"v_mfma_f32_16x16x32_bf16 v[144:147], v[180:183], v[24:27], v[144:147]\n"
"s_nop 1\n"
"v_max3_f32 v224, v224, v136, v137\n"
"v_max3_f32 v224, v224, v138, v139\n"
"v_mov_b32 v225, v224\n"
"s_nop 1\n"
"v_permlane16_swap_b32 v224, v225\n"
"v_max_f32 v224, v224, v225\n"
"v_mov_b32 v225, v224\n"
"s_nop 1\n"
"v_permlane32_swap_b32 v224, v225\n"
"v_max_f32 v224, v224, v225\n"
"v_sub_f32 v144, v144, v224\n"
"v_exp_f32 v144, v144\n"
"v_sub_f32 v145, v145, v224\n"
"v_exp_f32 v145, v145\n"
"v_sub_f32 v146, v146, v224\n"
"v_exp_f32 v146, v146\n"
"v_sub_f32 v147, v147, v224\n"
"v_exp_f32 v147, v147\n"
"s_waitcnt lgkmcnt(1)\n"
"v_mfma_f32_16x16x32_bf16 v[140:143], v[184:187], v[132:135], v[140:143]\n"
"ds_read_b128 v[220:223], v176 offset:1360\n"
"v_cvt_pk_bf16_f32 v144, v144, v145\n"
"v_mfma_f32_16x16x32_bf16 v[140:143], v[186:189], v[24:27], v[140:143]\n"
"ds_write_addtid_b32 v144 offset:53248\n"
"v_cvt_pk_bf16_f32 v145, v146, v147\n"
"ds_write_addtid_b32 v145 offset:53504\n"
"s_waitcnt lgkmcnt(3)\n"
"v_mfma_f32_16x16x32_bf16 v[148:151], v[226:229], v[132:135], v[148:151]\n"
"ds_read_b128 v[136:139], v176 offset:1376\n"
"v_mfma_f32_16x16x32_bf16 v[148:151], v[228:231], v[24:27], v[148:151]\n"
"s_nop 0\n"
"v_sub_f32 v140, v140, v224\n"
"v_exp_f32 v140, v140\n"
"v_sub_f32 v141, v141, v224\n"
"v_exp_f32 v141, v141\n"
"v_sub_f32 v142, v142, v224\n"
"v_exp_f32 v142, v142\n"
"v_sub_f32 v143, v143, v224\n"
"v_exp_f32 v143, v143\n"
"v_sub_f32 v148, v148, v224\n"
"v_exp_f32 v148, v148\n"
"v_sub_f32 v149, v149, v224\n"
"v_exp_f32 v149, v149\n"
"v_sub_f32 v150, v150, v224\n"
"v_exp_f32 v150, v150\n"
"v_sub_f32 v151, v151, v224\n"
"v_exp_f32 v151, v151\n"
"v_cvt_pk_bf16_f32 v140, v140, v141\n"
"v_cvt_pk_bf16_f32 v148, v148, v149\n"
"v_cvt_pk_bf16_f32 v141, v142, v143\n"
"v_cvt_pk_bf16_f32 v149, v150, v151\n"
"ds_write_addtid_b32 v140 offset:53760\n"
"ds_write_addtid_b32 v148 offset:54272\n"
"ds_write_addtid_b32 v141 offset:54016\n"
"s_waitcnt lgkmcnt(6)\n"
"v_mfma_f32_16x16x32_bf16 v[220:223], v[232:235], v[132:135], v[220:223]\n"
"ds_write_addtid_b32 v149 offset:54528\n"
"v_mfma_f32_16x16x32_bf16 v[220:223], v[234:237], v[24:27], v[220:223]\n"
"s_waitcnt lgkmcnt(4)\n"
"ds_read_b128 v[144:147], v177 offset:2720\n"
"v_mfma_f32_16x16x32_bf16 v[136:139], v[238:241], v[132:135], v[136:139]\n"
"ds_read_b128 v[152:155], v4 offset:55296\n"
"v_mfma_f32_16x16x32_bf16 v[136:139], v[240:243], v[24:27], v[136:139]\n"
"s_waitcnt lgkmcnt(2)\n"
"s_barrier\n"
"s_nop 0\n"
"v_sub_f32 v220, v220, v224\n"
"v_exp_f32 v220, v220\n"
"v_sub_f32 v221, v221, v224\n"
"v_exp_f32 v221, v221\n"
"v_sub_f32 v222, v222, v224\n"
"v_exp_f32 v222, v222\n"
"v_sub_f32 v223, v223, v224\n"
"v_exp_f32 v223, v223\n"
"v_cvt_pk_bf16_f32 v220, v220, v221\n"
"v_cvt_pk_bf16_f32 v221, v222, v223\n"
"v_sub_f32 v136, v136, v224\n"
"v_exp_f32 v136, v136\n"
"v_sub_f32 v137, v137, v224\n"
"v_exp_f32 v137, v137\n"
"v_sub_f32 v138, v138, v224\n"
"v_exp_f32 v138, v138\n"
"v_sub_f32 v139, v139, v224\n"
"v_exp_f32 v139, v139\n"
"v_cvt_pk_bf16_f32 v136, v136, v137\n"
"v_cvt_pk_bf16_f32 v137, v138, v139\n"
"ds_write_addtid_b32 v220 offset:54784\n"
"ds_write_addtid_b32 v221 offset:55040\n"
"s_waitcnt lgkmcnt(3)\n"
"v_mfma_f32_16x16x32_bf16 v[144:147], v[190:193], v[132:135], v[144:147]\n"
"ds_write_addtid_b32 v136 offset:57856\n"
"v_mfma_f32_16x16x32_bf16 v[144:147], v[192:195], v[24:27], v[144:147]\n"
"s_waitcnt lgkmcnt(3)\n"
"ds_read_b128 v[140:143], v5 offset:55296\n"
"v_mfma_f32_16x16x32_bf16 v[148:151], v[84:87], v[152:155], 0\n"
"ds_write_addtid_b32 v137 offset:58112\n"
"v_mfma_f32_16x16x32_bf16 v[156:159], v[88:91], v[152:155], 0\n"
"ds_read_b128 v[220:223], v6 offset:5120\n"
"v_mfma_f32_16x16x32_bf16 v[132:135], v[92:95], v[152:155], 0\n"
"s_waitcnt lgkmcnt(2)\n"
"ds_read_b128 v[160:163], v4 offset:57344\n"
"v_mfma_f32_16x16x32_bf16 v[148:151], v[96:99], v[140:143], v[148:151]\n"
"v_sub_f32 v144, v144, v224\n"
"v_exp_f32 v144, v144\n"
"v_mfma_f32_16x16x32_bf16 v[156:159], v[100:103], v[140:143], v[156:159]\n"
"s_waitcnt lgkmcnt(1)\n"
"ds_read_b128 v[136:139], v6 offset:6144\n"
"v_mfma_f32_16x16x32_bf16 v[132:135], v[220:223], v[140:143], v[132:135]\n"
"s_waitcnt lgkmcnt(1)\n"
"ds_read_b128 v[152:155], v5 offset:57344\n"
"v_mfma_f32_16x16x32_bf16 v[148:151], v[104:107], v[160:163], v[148:151]\n"
"v_sub_f32 v145, v145, v224\n"
"v_exp_f32 v145, v145\n"
"v_mfma_f32_16x16x32_bf16 v[156:159], v[108:111], v[160:163], v[156:159]\n"
"ds_read_b128 v[164:167], v6 offset:7168\n"
"v_sub_f32 v146, v146, v224\n"
"v_exp_f32 v146, v146\n"
"v_sub_f32 v147, v147, v224\n"
"v_exp_f32 v147, v147\n"
"s_waitcnt lgkmcnt(2)\n"
"v_mfma_f32_16x16x32_bf16 v[132:135], v[136:139], v[160:163], v[132:135]\n"
"s_waitcnt lgkmcnt(1)\n"
"ds_read_b128 v[140:143], v4 offset:59392\n"
"v_cvt_pk_bf16_f32 v144, v144, v145\n"
"v_cvt_pk_bf16_f32 v145, v146, v147\n"
"v_mfma_f32_16x16x32_bf16 v[148:151], v[112:115], v[152:155], v[148:151]\n"
"ds_write_addtid_b32 v144 offset:57344\n"
"v_mfma_f32_16x16x32_bf16 v[156:159], v[116:119], v[152:155], v[156:159]\n"
"ds_write_addtid_b32 v145 offset:57600\n"
"s_waitcnt lgkmcnt(3)\n"
"v_mfma_f32_16x16x32_bf16 v[132:135], v[164:167], v[152:155], v[132:135]\n"
"s_waitcnt lgkmcnt(2)\n"
"ds_read_b128 v[136:139], v6 offset:8192\n"
"ds_read_b128 v[160:163], v5 offset:59392\n"
"v_mfma_f32_16x16x32_bf16 v[148:151], v[0:3], v[140:143], v[148:151]\n"
"v_mfma_f32_16x16x32_bf16 v[156:159], v[120:123], v[140:143], v[156:159]\n"
"s_waitcnt lgkmcnt(2)\n"
"s_barrier\n"
"ds_read_b128 v[144:147], v6 offset:9216\n"
"s_waitcnt lgkmcnt(2)\n"
"v_mfma_f32_16x16x32_bf16 v[132:135], v[136:139], v[140:143], v[132:135]\n"
"s_waitcnt lgkmcnt(1)\n"
"v_mfma_f32_16x16x32_bf16 v[148:151], v[124:127], v[160:163], v[148:151]\n"
"ds_read_b128 v[152:155], v4 offset:49152\n"
"v_mfma_f32_16x16x32_bf16 v[156:159], v[128:131], v[160:163], v[156:159]\n"
"s_waitcnt lgkmcnt(1)\n"
"v_mfma_f32_16x16x32_bf16 v[132:135], v[144:147], v[160:163], v[132:135]\n"
"s_waitcnt lgkmcnt(0)\n"
"v_mfma_f32_16x16x32_bf16 v[220:223], v[8:11], v[152:155], 0\n"
"ds_read_b128 v[164:167], v5 offset:49152\n"
"v_mfma_f32_16x16x32_bf16 v[136:139], v[12:15], v[152:155], 0\n"
"v_mfma_f32_16x16x32_bf16 v[140:143], v[16:19], v[152:155], 0\n"
"s_waitcnt lgkmcnt(0)\n"
"v_mfma_f32_16x16x32_bf16 v[220:223], v[20:23], v[164:167], v[220:223]\n"
"ds_read_b128 v[168:171], v4 offset:51200\n"
"v_mul_f32 v148, s60, v148\n"
"v_mfma_f32_16x16x32_bf16 v[136:139], v[28:31], v[164:167], v[136:139]\n"
"v_mul_f32 v149, s60, v149\n"
"v_cvt_pk_bf16_f32 v148, v148, v149\n"
"v_mfma_f32_16x16x32_bf16 v[140:143], v[32:35], v[164:167], v[140:143]\n"
"v_mul_f32 v150, s60, v150\n"
"v_mul_f32 v151, s60, v151\n"
"v_cvt_pk_bf16_f32 v149, v150, v151\n"
"v_mul_f32 v156, s60, v156\n"
"v_mul_f32 v157, s60, v157\n"
"v_mul_f32 v158, s60, v158\n"
"v_mul_f32 v159, s60, v159\n"
"v_cvt_pk_bf16_f32 v150, v156, v157\n"
"v_cvt_pk_bf16_f32 v151, v158, v159\n"
"v_mul_f32 v132, s60, v132\n"
"v_mul_f32 v133, s60, v133\n"
"v_mul_f32 v134, s60, v134\n"
"s_waitcnt lgkmcnt(0)\n"
"v_mfma_f32_16x16x32_bf16 v[220:223], v[36:39], v[168:171], v[220:223]\n"
"ds_read_b128 v[144:147], v5 offset:51200\n"
"v_mul_f32 v135, s60, v135\n"
"v_mfma_f32_16x16x32_bf16 v[136:139], v[40:43], v[168:171], v[136:139]\n"
"v_cvt_pk_bf16_f32 v26, v132, v133\n"
"v_cvt_pk_bf16_f32 v27, v134, v135\n"
"v_mfma_f32_16x16x32_bf16 v[140:143], v[44:47], v[168:171], v[140:143]\n"
"s_waitcnt lgkmcnt(0)\n"
"v_bfe_u32 v225, v6, 2, 6\n"
"ds_read_b32 v225, v225 offset:1536\n"
"v_mfma_f32_16x16x32_bf16 v[220:223], v[48:51], v[144:147], v[220:223]\n"
"ds_read_b128 v[152:155], v4 offset:53248\n"
"v_mfma_f32_16x16x32_bf16 v[136:139], v[52:55], v[144:147], v[136:139]\n"
"s_add_u32 s70, s50, 4294964608\n"
"v_mfma_f32_16x16x32_bf16 v[140:143], v[56:59], v[144:147], v[140:143]\n"
"s_waitcnt lgkmcnt(1)\n"
"v_add_u32_sdwa v177, v225, v7 dst_sel:DWORD dst_unused:UNUSED_PAD src0_sel:WORD_0 src1_sel:DWORD\n"
"s_waitcnt lgkmcnt(0)\n"
"v_mfma_f32_16x16x32_bf16 v[220:223], v[60:63], v[152:155], v[220:223]\n"
"ds_read_b128 v[132:135], v5 offset:53248\n"
"v_max_u32 v176, s70, v177\n"
"v_mfma_f32_16x16x32_bf16 v[136:139], v[64:67], v[152:155], v[136:139]\n"
"v_mfma_f32_16x16x32_bf16 v[140:143], v[68:71], v[152:155], v[140:143]\n"
"s_add_u32 s71, s50, 4294965648\n"
"s_waitcnt lgkmcnt(0)\n"
"v_mfma_f32_16x16x32_bf16 v[220:223], v[72:75], v[132:135], v[220:223]\n"
"ds_read_b128 v[144:147], v176 offset:2688\n"
"v_mfma_f32_16x16x32_bf16 v[136:139], v[76:79], v[132:135], v[136:139]\n"
"v_min_u32 v175, s71, v177\n"
"v_mfma_f32_16x16x32_bf16 v[140:143], v[80:83], v[132:135], v[140:143]\n"
"s_waitcnt lgkmcnt(0)\n"
"ds_read_b128 v[152:155], v176 offset:2704\n"
"v_mfma_f32_16x16x32_bf16 v[144:147], v[178:181], v[148:151], v[144:147]\n"
"v_mfma_f32_16x16x32_bf16 v[144:147], v[180:183], v[24:27], v[144:147]\n"
"ds_read_b128 v[156:159], v175 offset:4032\n"
"v_cvt_pk_bf16_f32 v220, v220, v221\n"
"v_cvt_pk_bf16_f32 v221, v222, v223\n"
"v_cvt_pk_bf16_f32 v222, v136, v137\n"
"v_cvt_pk_bf16_f32 v223, v138, v139\n"
"v_cvt_pk_bf16_f32 v224, v140, v141\n"
"v_cvt_pk_bf16_f32 v225, v142, v143\n"
"s_waitcnt lgkmcnt(1)\n"
"v_mfma_f32_16x16x32_bf16 v[152:155], v[184:187], v[148:151], v[152:155]\n"
"s_add_u32 s69, s50, 4294965952\n"
"v_max3_f32 v172, v144, v145, v146\n"
"v_max_f32 v172, v172, v147\n"
"v_mfma_f32_16x16x32_bf16 v[152:155], v[186:189], v[24:27], v[152:155]\n"
"s_waitcnt lgkmcnt(0)\n"
"ds_read_b128 v[132:135], v175 offset:4048\n"
"v_mfma_f32_16x16x32_bf16 v[156:159], v[208:211], v[148:151], v[156:159]\n"
"v_max_u32 v174, s69, v177\n"
"v_mfma_f32_16x16x32_bf16 v[156:159], v[210:213], v[24:27], v[156:159]\n"
"s_waitcnt lgkmcnt(0)\n"
"ds_read_b128 v[136:139], v176 offset:2720\n"
"s_nop 0\n"
"v_max3_f32 v172, v172, v152, v153\n"
"v_max3_f32 v172, v172, v154, v155\n"
"v_mfma_f32_16x16x32_bf16 v[132:135], v[214:217], v[148:151], v[132:135]\n"
"v_mfma_f32_16x16x32_bf16 v[132:135], v[216:219], v[24:27], v[132:135]\n"
"s_waitcnt lgkmcnt(0)\n"
"ds_read_b128 v[140:143], v174 offset:1344\n"
"v_max3_f32 v172, v172, v156, v157\n"
"v_max3_f32 v172, v172, v158, v159\n"
"v_mfma_f32_16x16x32_bf16 v[136:139], v[190:193], v[148:151], v[136:139]\n"
"v_mfma_f32_16x16x32_bf16 v[136:139], v[192:195], v[24:27], v[136:139]\n"
"s_waitcnt lgkmcnt(0)\n"
"ds_read_b128 v[144:147], v174 offset:1360\n"
"v_max3_f32 v172, v172, v132, v133\n"
"v_max3_f32 v172, v172, v134, v135\n"
"v_mfma_f32_16x16x32_bf16 v[140:143], v[226:229], v[148:151], v[140:143]\n"
"v_mfma_f32_16x16x32_bf16 v[140:143], v[228:231], v[24:27], v[140:143]\n"
"s_waitcnt lgkmcnt(0)\n"
"ds_read_b128 v[152:155], v174 offset:1376\n"
"v_max3_f32 v172, v172, v136, v137\n"
"v_max3_f32 v172, v172, v138, v139\n"
"v_mfma_f32_16x16x32_bf16 v[144:147], v[232:235], v[148:151], v[144:147]\n"
"v_mfma_f32_16x16x32_bf16 v[144:147], v[234:237], v[24:27], v[144:147]\n"
"s_waitcnt lgkmcnt(0)\n"
"ds_read_b128 v[132:135], v175 offset:4064\n"
"v_max3_f32 v172, v172, v140, v141\n"
"v_max3_f32 v172, v172, v142, v143\n"
"v_mfma_f32_16x16x32_bf16 v[152:155], v[238:241], v[148:151], v[152:155]\n"
"v_mfma_f32_16x16x32_bf16 v[152:155], v[240:243], v[24:27], v[152:155]\n"
"s_waitcnt lgkmcnt(0)\n"
"ds_read_b128 v[136:139], v176 offset:2688\n"
"v_max3_f32 v172, v172, v144, v145\n"
"v_max3_f32 v172, v172, v146, v147\n"
"v_mfma_f32_16x16x32_bf16 v[132:135], v[220:223], v[148:151], v[132:135]\n"
"v_mfma_f32_16x16x32_bf16 v[132:135], v[222:225], v[24:27], v[132:135]\n"
"s_waitcnt lgkmcnt(0)\n"
"ds_read_b128 v[140:143], v176 offset:2704\n"
"v_max3_f32 v172, v172, v152, v153\n"
"v_max3_f32 v172, v172, v154, v155\n"
"v_mfma_f32_16x16x32_bf16 v[136:139], v[178:181], v[148:151], v[136:139]\n"
"v_mfma_f32_16x16x32_bf16 v[136:139], v[180:183], v[24:27], v[136:139]\n"
"ds_read_b128 v[144:147], v175 offset:4032\n"
"s_nop 0\n"
"v_max3_f32 v172, v172, v132, v133\n"
"v_max3_f32 v172, v172, v134, v135\n"
"v_mov_b32 v173, v172\n"
"s_nop 1\n"
"v_permlane16_swap_b32 v172, v173\n"
"v_max_f32 v172, v172, v173\n"
"v_mov_b32 v173, v172\n"
"s_nop 1\n"
"v_permlane32_swap_b32 v172, v173\n"
"v_max_f32 v172, v172, v173\n"
"v_sub_f32 v136, v136, v172\n"
"v_exp_f32 v136, v136\n"
"v_sub_f32 v137, v137, v172\n"
"v_exp_f32 v137, v137\n"
"v_sub_f32 v138, v138, v172\n"
"v_exp_f32 v138, v138\n"
"v_sub_f32 v139, v139, v172\n"
"v_exp_f32 v139, v139\n"
"s_waitcnt lgkmcnt(1)\n"
"v_mfma_f32_16x16x32_bf16 v[140:143], v[184:187], v[148:151], v[140:143]\n"
"v_cvt_pk_bf16_f32 v136, v136, v137\n"
"v_cvt_pk_bf16_f32 v137, v138, v139\n"
"v_mfma_f32_16x16x32_bf16 v[140:143], v[186:189], v[24:27], v[140:143]\n"
"ds_read_b128 v[132:135], v175 offset:4048\n"
"ds_write_addtid_b32 v136 offset:55296\n"
"ds_write_addtid_b32 v137 offset:55552\n"
"s_waitcnt lgkmcnt(3)\n"
"v_mfma_f32_16x16x32_bf16 v[144:147], v[208:211], v[148:151], v[144:147]\n"
"v_mfma_f32_16x16x32_bf16 v[144:147], v[210:213], v[24:27], v[144:147]\n"
"ds_read_b128 v[152:155], v176 offset:2720\n"
"s_nop 0\n"
"v_sub_f32 v140, v140, v172\n"
"v_exp_f32 v140, v140\n"
"v_sub_f32 v141, v141, v172\n"
"v_exp_f32 v141, v141\n"
"v_sub_f32 v142, v142, v172\n"
"v_exp_f32 v142, v142\n"
"v_sub_f32 v143, v143, v172\n"
"v_exp_f32 v143, v143\n"
"v_sub_f32 v144, v144, v172\n"
"v_exp_f32 v144, v144\n"
"v_sub_f32 v145, v145, v172\n"
"v_exp_f32 v145, v145\n"
"v_cvt_pk_bf16_f32 v140, v140, v141\n"
"v_sub_f32 v146, v146, v172\n"
"v_exp_f32 v146, v146\n"
"v_cvt_pk_bf16_f32 v141, v142, v143\n"
"v_sub_f32 v147, v147, v172\n"
"v_exp_f32 v147, v147\n"
"v_cvt_pk_bf16_f32 v144, v144, v145\n"
"v_cvt_pk_bf16_f32 v145, v146, v147\n"
"ds_write_addtid_b32 v140 offset:55808\n"
"ds_write_addtid_b32 v141 offset:56064\n"
"s_waitcnt lgkmcnt(5)\n"
"v_mfma_f32_16x16x32_bf16 v[132:135], v[214:217], v[148:151], v[132:135]\n"
"ds_write_addtid_b32 v144 offset:56320\n"
"v_mfma_f32_16x16x32_bf16 v[132:135], v[216:219], v[24:27], v[132:135]\n"
"ds_read_b128 v[136:139], v174 offset:1344\n"
"ds_write_addtid_b32 v145 offset:56576\n"
"s_waitcnt lgkmcnt(5)\n"
"v_mfma_f32_16x16x32_bf16 v[152:155], v[190:193], v[148:151], v[152:155]\n"
"v_mfma_f32_16x16x32_bf16 v[152:155], v[192:195], v[24:27], v[152:155]\n"
"ds_read_b128 v[140:143], v174 offset:1360\n"
"s_nop 1\n"
"v_sub_f32 v132, v132, v172\n"
"v_exp_f32 v132, v132\n"
"v_sub_f32 v133, v133, v172\n"
"v_exp_f32 v133, v133\n"
"v_sub_f32 v134, v134, v172\n"
"v_exp_f32 v134, v134\n"
"v_sub_f32 v135, v135, v172\n"
"v_exp_f32 v135, v135\n"
"v_sub_f32 v152, v152, v172\n"
"v_exp_f32 v152, v152\n"
"v_cvt_pk_bf16_f32 v132, v132, v133\n"
"v_cvt_pk_bf16_f32 v133, v134, v135\n"
"v_sub_f32 v153, v153, v172\n"
"v_exp_f32 v153, v153\n"
"ds_write_addtid_b32 v132 offset:56832\n"
"v_cvt_pk_bf16_f32 v152, v152, v153\n"
"ds_write_addtid_b32 v133 offset:57088\n"
"s_waitcnt lgkmcnt(4)\n"
"v_mfma_f32_16x16x32_bf16 v[136:139], v[226:229], v[148:151], v[136:139]\n"
"ds_write_addtid_b32 v152 offset:34304\n"
"v_mfma_f32_16x16x32_bf16 v[136:139], v[228:231], v[24:27], v[136:139]\n"
"s_waitcnt lgkmcnt(3)\n"
"ds_read_b128 v[144:147], v174 offset:1376\n"
"v_mfma_f32_16x16x32_bf16 v[140:143], v[232:235], v[148:151], v[140:143]\n"
"v_mfma_f32_16x16x32_bf16 v[140:143], v[234:237], v[24:27], v[140:143]\n"
"s_waitcnt lgkmcnt(1)\n"
"ds_read_b128 v[132:135], v175 offset:4064\n"
"s_barrier\n"
"s_nop 0\n"
"v_sub_f32 v136, v136, v172\n"
"v_exp_f32 v136, v136\n"
"v_sub_f32 v137, v137, v172\n"
"v_exp_f32 v137, v137\n"
"v_sub_f32 v138, v138, v172\n"
"v_exp_f32 v138, v138\n"
"v_sub_f32 v139, v139, v172\n"
"v_exp_f32 v139, v139\n"
"v_sub_f32 v140, v140, v172\n"
"v_exp_f32 v140, v140\n"
"v_cvt_pk_bf16_f32 v136, v136, v137\n"
"v_cvt_pk_bf16_f32 v137, v138, v139\n"
"v_sub_f32 v141, v141, v172\n"
"v_exp_f32 v141, v141\n"
"v_sub_f32 v142, v142, v172\n"
"v_exp_f32 v142, v142\n"
"v_sub_f32 v143, v143, v172\n"
"v_exp_f32 v143, v143\n"
"v_cvt_pk_bf16_f32 v140, v140, v141\n"
"v_cvt_pk_bf16_f32 v141, v142, v143\n"
"ds_write_addtid_b32 v136 offset:57856\n"
"ds_write_addtid_b32 v137 offset:58112\n"
"s_waitcnt lgkmcnt(3)\n"
"v_mfma_f32_16x16x32_bf16 v[144:147], v[238:241], v[148:151], v[144:147]\n"
"ds_write_addtid_b32 v140 offset:58368\n"
"v_mfma_f32_16x16x32_bf16 v[144:147], v[240:243], v[24:27], v[144:147]\n"
"ds_read_b128 v[226:229], v4 offset:18432\n"
"ds_write_addtid_b32 v141 offset:58624\n"
"s_waitcnt lgkmcnt(5)\n"
"v_mfma_f32_16x16x32_bf16 v[132:135], v[220:223], v[148:151], v[132:135]\n"
"v_mfma_f32_16x16x32_bf16 v[132:135], v[222:225], v[24:27], v[132:135]\n"
"s_waitcnt lgkmcnt(1)\n"
"v_mfma_f32_16x16x32_bf16 v[136:139], v[84:87], v[226:229], 0\n"
"ds_read_b128 v[152:155], v5 offset:18432\n"
"v_mfma_f32_16x16x32_bf16 v[156:159], v[88:91], v[226:229], 0\n"
"v_mfma_f32_16x16x32_bf16 v[160:163], v[92:95], v[226:229], 0\n"
"ds_read_b128 v[140:143], v6 offset:5120\n"
"v_sub_f32 v144, v144, v172\n"
"v_exp_f32 v144, v144\n"
"v_sub_f32 v145, v145, v172\n"
"v_exp_f32 v145, v145\n"
"v_sub_f32 v132, v132, v172\n"
"v_exp_f32 v132, v132\n"
"v_cvt_pk_bf16_f32 v144, v144, v145\n"
"s_waitcnt lgkmcnt(1)\n"
"v_mfma_f32_16x16x32_bf16 v[136:139], v[96:99], v[152:155], v[136:139]\n"
"ds_read_b128 v[226:229], v4 offset:20480\n"
"v_mfma_f32_16x16x32_bf16 v[156:159], v[100:103], v[152:155], v[156:159]\n"
"ds_write_addtid_b32 v144 offset:58880\n"
"s_waitcnt lgkmcnt(2)\n"
"v_mfma_f32_16x16x32_bf16 v[160:163], v[140:143], v[152:155], v[160:163]\n"
"s_waitcnt lgkmcnt(1)\n"
"ds_read_b128 v[148:151], v6 offset:6144\n"
"v_sub_f32 v133, v133, v172\n"
"v_exp_f32 v133, v133\n"
"v_mfma_f32_16x16x32_bf16 v[136:139], v[104:107], v[226:229], v[136:139]\n"
"ds_read_b128 v[164:167], v5 offset:20480\n"
"v_cvt_pk_bf16_f32 v132, v132, v133\n"
"v_mfma_f32_16x16x32_bf16 v[156:159], v[108:111], v[226:229], v[156:159]\n"
"ds_write_addtid_b32 v132 offset:57344\n"
"s_waitcnt lgkmcnt(2)\n"
"v_mfma_f32_16x16x32_bf16 v[160:163], v[148:151], v[226:229], v[160:163]\n"
"s_waitcnt lgkmcnt(1)\n"
"ds_read_b128 v[140:143], v6 offset:7168\n"
"v_mfma_f32_16x16x32_bf16 v[136:139], v[112:115], v[164:167], v[136:139]\n"
"s_waitcnt lgkmcnt(1)\n"
"s_barrier\n"
"ds_read_b128 v[144:147], v4 offset:22528\n"
"v_mfma_f32_16x16x32_bf16 v[156:159], v[116:119], v[164:167], v[156:159]\n"
"s_waitcnt lgkmcnt(1)\n"
"v_mfma_f32_16x16x32_bf16 v[160:163], v[140:143], v[164:167], v[160:163]\n"
"s_waitcnt lgkmcnt(0)\n"
"ds_read_b128 v[132:135], v6 offset:8192\n"
"v_mfma_f32_16x16x32_bf16 v[136:139], v[0:3], v[144:147], v[136:139]\n"
"ds_read_b128 v[148:151], v5 offset:22528\n"
"v_mfma_f32_16x16x32_bf16 v[156:159], v[120:123], v[144:147], v[156:159]\n"
"s_waitcnt lgkmcnt(1)\n"
"v_mfma_f32_16x16x32_bf16 v[160:163], v[132:135], v[144:147], v[160:163]\n"
"s_waitcnt lgkmcnt(0)\n"
"ds_read_b128 v[152:155], v6 offset:9216\n"
"v_mfma_f32_16x16x32_bf16 v[136:139], v[124:127], v[148:151], v[136:139]\n"
"ds_read_b128 v[140:143], v4 offset:12288\n"
"v_mfma_f32_16x16x32_bf16 v[156:159], v[128:131], v[148:151], v[156:159]\n"
"s_waitcnt lgkmcnt(1)\n"
"v_mfma_f32_16x16x32_bf16 v[160:163], v[152:155], v[148:151], v[160:163]\n"
"s_waitcnt lgkmcnt(0)\n"
"v_mfma_f32_16x16x32_bf16 v[226:229], v[8:11], v[140:143], 0\n"
"ds_read_b128 v[132:135], v5 offset:12288\n"
"v_mfma_f32_16x16x32_bf16 v[144:147], v[12:15], v[140:143], 0\n"
"v_mfma_f32_16x16x32_bf16 v[164:167], v[16:19], v[140:143], 0\n"
"s_waitcnt lgkmcnt(0)\n"
"v_mfma_f32_16x16x32_bf16 v[226:229], v[20:23], v[132:135], v[226:229]\n"
"ds_read_b128 v[168:171], v4 offset:14336\n"
"v_mul_f32 v136, s60, v136\n"
"v_mfma_f32_16x16x32_bf16 v[144:147], v[28:31], v[132:135], v[144:147]\n"
"v_mul_f32 v137, s60, v137\n"
"v_cvt_pk_bf16_f32 v136, v136, v137\n"
"v_mfma_f32_16x16x32_bf16 v[164:167], v[32:35], v[132:135], v[164:167]\n"
"v_mul_f32 v138, s60, v138\n"
"v_mul_f32 v139, s60, v139\n"
"v_cvt_pk_bf16_f32 v137, v138, v139\n"
"v_mul_f32 v156, s60, v156\n"
"v_mul_f32 v157, s60, v157\n"
"v_mul_f32 v158, s60, v158\n"
"v_mul_f32 v159, s60, v159\n"
"v_cvt_pk_bf16_f32 v138, v156, v157\n"
"v_cvt_pk_bf16_f32 v139, v158, v159\n"
"v_mul_f32 v160, s60, v160\n"
"v_mul_f32 v161, s60, v161\n"
"v_mul_f32 v162, s60, v162\n"
"s_waitcnt lgkmcnt(0)\n"
"v_mfma_f32_16x16x32_bf16 v[226:229], v[36:39], v[168:171], v[226:229]\n"
"ds_read_b128 v[132:135], v5 offset:14336\n"
"v_mul_f32 v163, s60, v163\n"
"v_mfma_f32_16x16x32_bf16 v[144:147], v[40:43], v[168:171], v[144:147]\n"
"v_cvt_pk_bf16_f32 v26, v160, v161\n"
"v_cvt_pk_bf16_f32 v27, v162, v163\n"
"v_mfma_f32_16x16x32_bf16 v[164:167], v[44:47], v[168:171], v[164:167]\n"
"s_waitcnt lgkmcnt(0)\n"
"v_bfe_u32 v231, v6, 2, 6\n"
"ds_read_b32 v231, v231 offset:1600\n"
"v_mfma_f32_16x16x32_bf16 v[226:229], v[48:51], v[132:135], v[226:229]\n"
"ds_read_b128 v[140:143], v4 offset:16384\n"
"v_mfma_f32_16x16x32_bf16 v[144:147], v[52:55], v[132:135], v[144:147]\n"
"s_add_u32 s73, s50, 4294966976\n"
"v_mfma_f32_16x16x32_bf16 v[164:167], v[56:59], v[132:135], v[164:167]\n"
"s_waitcnt lgkmcnt(1)\n"
"v_add_u32_sdwa v177, v231, v7 dst_sel:DWORD dst_unused:UNUSED_PAD src0_sel:WORD_0 src1_sel:DWORD\n"
"s_waitcnt lgkmcnt(0)\n"
"v_mfma_f32_16x16x32_bf16 v[226:229], v[60:63], v[140:143], v[226:229]\n"
"ds_read_b128 v[148:151], v5 offset:16384\n"
"v_min_u32 v176, s73, v177\n"
"v_mfma_f32_16x16x32_bf16 v[144:147], v[64:67], v[140:143], v[144:147]\n"
"v_mfma_f32_16x16x32_bf16 v[164:167], v[68:71], v[140:143], v[164:167]\n"
"s_add_u32 s68, s50, 4294965632\n"
"s_waitcnt lgkmcnt(0)\n"
"v_mfma_f32_16x16x32_bf16 v[226:229], v[72:75], v[148:151], v[226:229]\n"
"ds_read_b128 v[132:135], v176 offset:2704\n"
"v_mfma_f32_16x16x32_bf16 v[144:147], v[76:79], v[148:151], v[144:147]\n"
"v_min_u32 v175, s68, v177\n"
"v_mfma_f32_16x16x32_bf16 v[164:167], v[80:83], v[148:151], v[164:167]\n"
"s_waitcnt lgkmcnt(0)\n"
"ds_read_b128 v[140:143], v176 offset:2720\n"
"v_mfma_f32_16x16x32_bf16 v[132:135], v[184:187], v[136:139], v[132:135]\n"
"v_mfma_f32_16x16x32_bf16 v[132:135], v[186:189], v[24:27], v[132:135]\n"
"ds_read_b128 v[152:155], v175 offset:4048\n"
"v_cvt_pk_bf16_f32 v226, v226, v227\n"
"v_cvt_pk_bf16_f32 v227, v228, v229\n"
"v_cvt_pk_bf16_f32 v228, v144, v145\n"
"v_cvt_pk_bf16_f32 v229, v146, v147\n"
"v_cvt_pk_bf16_f32 v230, v164, v165\n"
"v_cvt_pk_bf16_f32 v231, v166, v167\n"
"s_waitcnt lgkmcnt(1)\n"
"v_mfma_f32_16x16x32_bf16 v[140:143], v[190:193], v[136:139], v[140:143]\n"
"s_add_u32 s72, s50, 4294965936\n"
"v_max3_f32 v172, v132, v133, v134\n"
"v_max_f32 v172, v172, v135\n"
"v_mfma_f32_16x16x32_bf16 v[140:143], v[192:195], v[24:27], v[140:143]\n"
"s_waitcnt lgkmcnt(0)\n"
"ds_read_b128 v[144:147], v175 offset:4064\n"
"v_mfma_f32_16x16x32_bf16 v[152:155], v[214:217], v[136:139], v[152:155]\n"
"v_max_u32 v174, s72, v177\n"
"v_mfma_f32_16x16x32_bf16 v[152:155], v[216:219], v[24:27], v[152:155]\n"
"s_waitcnt lgkmcnt(0)\n"
"ds_read_b128 v[148:151], v176 offset:2736\n"
"s_nop 0\n"
"v_max3_f32 v172, v172, v140, v141\n"
"v_max3_f32 v172, v172, v142, v143\n"
"v_mfma_f32_16x16x32_bf16 v[144:147], v[220:223], v[136:139], v[144:147]\n"
"v_mfma_f32_16x16x32_bf16 v[144:147], v[222:225], v[24:27], v[144:147]\n"
"s_waitcnt lgkmcnt(0)\n"
"ds_read_b128 v[132:135], v174 offset:1360\n"
"v_max3_f32 v172, v172, v152, v153\n"
"v_max3_f32 v172, v172, v154, v155\n"
"v_mfma_f32_16x16x32_bf16 v[148:151], v[196:199], v[136:139], v[148:151]\n"
"v_mfma_f32_16x16x32_bf16 v[148:151], v[198:201], v[24:27], v[148:151]\n"
"s_waitcnt lgkmcnt(0)\n"
"ds_read_b128 v[140:143], v174 offset:1376\n"
"v_max3_f32 v172, v172, v144, v145\n"
"v_max3_f32 v172, v172, v146, v147\n"
"v_mfma_f32_16x16x32_bf16 v[132:135], v[232:235], v[136:139], v[132:135]\n"
"v_mfma_f32_16x16x32_bf16 v[132:135], v[234:237], v[24:27], v[132:135]\n"
"s_waitcnt lgkmcnt(0)\n"
"ds_read_b128 v[152:155], v174 offset:1392\n"
"v_max3_f32 v172, v172, v148, v149\n"
"v_max3_f32 v172, v172, v150, v151\n"
"v_mfma_f32_16x16x32_bf16 v[140:143], v[238:241], v[136:139], v[140:143]\n"
"v_mfma_f32_16x16x32_bf16 v[140:143], v[240:243], v[24:27], v[140:143]\n"
"s_waitcnt lgkmcnt(0)\n"
"ds_read_b128 v[144:147], v175 offset:4080\n"
"v_max3_f32 v172, v172, v132, v133\n"
"v_max3_f32 v172, v172, v134, v135\n"
"v_mfma_f32_16x16x32_bf16 v[152:155], v[244:247], v[136:139], v[152:155]\n"
"v_mfma_f32_16x16x32_bf16 v[152:155], v[246:249], v[24:27], v[152:155]\n"
"s_waitcnt lgkmcnt(0)\n"
"ds_read_b128 v[148:151], v176 offset:2704\n"
"v_max3_f32 v172, v172, v140, v141\n"
"v_max3_f32 v172, v172, v142, v143\n"
"v_mfma_f32_16x16x32_bf16 v[144:147], v[226:229], v[136:139], v[144:147]\n"
"v_mfma_f32_16x16x32_bf16 v[144:147], v[228:231], v[24:27], v[144:147]\n"
"s_waitcnt lgkmcnt(0)\n"
"ds_read_b128 v[132:135], v176 offset:2720\n"
"v_max3_f32 v172, v172, v152, v153\n"
"v_max3_f32 v172, v172, v154, v155\n"
"v_mfma_f32_16x16x32_bf16 v[148:151], v[184:187], v[136:139], v[148:151]\n"
"v_mfma_f32_16x16x32_bf16 v[148:151], v[186:189], v[24:27], v[148:151]\n"
"ds_read_b128 v[140:143], v175 offset:4048\n"
"s_nop 0\n"
"v_max3_f32 v172, v172, v144, v145\n"
"v_max3_f32 v172, v172, v146, v147\n"
"v_mov_b32 v173, v172\n"
"s_nop 1\n"
"v_permlane16_swap_b32 v172, v173\n"
"v_max_f32 v172, v172, v173\n"
"v_mov_b32 v173, v172\n"
"s_nop 1\n"
"v_permlane32_swap_b32 v172, v173\n"
"v_max_f32 v172, v172, v173\n"
"v_sub_f32 v148, v148, v172\n"
"v_exp_f32 v148, v148\n"
"v_sub_f32 v149, v149, v172\n"
"v_exp_f32 v149, v149\n"
"v_sub_f32 v150, v150, v172\n"
"v_exp_f32 v150, v150\n"
"v_sub_f32 v151, v151, v172\n"
"v_exp_f32 v151, v151\n"
"s_waitcnt lgkmcnt(1)\n"
"v_mfma_f32_16x16x32_bf16 v[132:135], v[190:193], v[136:139], v[132:135]\n"
"v_cvt_pk_bf16_f32 v148, v148, v149\n"
"v_cvt_pk_bf16_f32 v149, v150, v151\n"
"v_mfma_f32_16x16x32_bf16 v[132:135], v[192:195], v[24:27], v[132:135]\n"
"ds_read_b128 v[144:147], v175 offset:4064\n"
"ds_write_addtid_b32 v148 offset:53248\n"
"ds_write_addtid_b32 v149 offset:53504\n"
"s_waitcnt lgkmcnt(3)\n"
"v_mfma_f32_16x16x32_bf16 v[140:143], v[214:217], v[136:139], v[140:143]\n"
"v_mfma_f32_16x16x32_bf16 v[140:143], v[216:219], v[24:27], v[140:143]\n"
"ds_read_b128 v[152:155], v176 offset:2736\n"
"s_nop 0\n"
"v_sub_f32 v132, v132, v172\n"
"v_exp_f32 v132, v132\n"
"v_sub_f32 v133, v133, v172\n"
"v_exp_f32 v133, v133\n"
"v_sub_f32 v134, v134, v172\n"
"v_exp_f32 v134, v134\n"
"v_sub_f32 v135, v135, v172\n"
"v_exp_f32 v135, v135\n"
"v_sub_f32 v140, v140, v172\n"
"v_exp_f32 v140, v140\n"
"v_sub_f32 v141, v141, v172\n"
"v_exp_f32 v141, v141\n"
"v_sub_f32 v142, v142, v172\n"
"v_exp_f32 v142, v142\n"
"v_sub_f32 v143, v143, v172\n"
"v_exp_f32 v143, v143\n"
"v_cvt_pk_bf16_f32 v132, v132, v133\n"
"v_cvt_pk_bf16_f32 v133, v134, v135\n"
"v_cvt_pk_bf16_f32 v140, v140, v141\n"
"v_cvt_pk_bf16_f32 v141, v142, v143\n"
"ds_write_addtid_b32 v132 offset:53760\n"
"ds_write_addtid_b32 v133 offset:54016\n"
"s_waitcnt lgkmcnt(5)\n"
"v_mfma_f32_16x16x32_bf16 v[144:147], v[220:223], v[136:139], v[144:147]\n"
"ds_write_addtid_b32 v140 offset:54272\n"
"v_mfma_f32_16x16x32_bf16 v[144:147], v[222:225], v[24:27], v[144:147]\n"
"ds_read_b128 v[148:151], v174 offset:1360\n"
"ds_write_addtid_b32 v141 offset:54528\n"
"s_waitcnt lgkmcnt(5)\n"
"v_mfma_f32_16x16x32_bf16 v[152:155], v[196:199], v[136:139], v[152:155]\n"
"v_mfma_f32_16x16x32_bf16 v[152:155], v[198:201], v[24:27], v[152:155]\n"
"ds_read_b128 v[132:135], v174 offset:1376\n"
"s_nop 1\n"
"v_sub_f32 v144, v144, v172\n"
"v_exp_f32 v144, v144\n"
"v_sub_f32 v145, v145, v172\n"
"v_exp_f32 v145, v145\n"
"v_sub_f32 v146, v146, v172\n"
"v_exp_f32 v146, v146\n"
"v_sub_f32 v147, v147, v172\n"
"v_exp_f32 v147, v147\n"
"v_sub_f32 v152, v152, v172\n"
"v_exp_f32 v152, v152\n"
"v_sub_f32 v153, v153, v172\n"
"v_exp_f32 v153, v153\n"
"v_sub_f32 v154, v154, v172\n"
"v_exp_f32 v154, v154\n"
"v_cvt_pk_bf16_f32 v144, v144, v145\n"
"v_cvt_pk_bf16_f32 v145, v146, v147\n"
"v_sub_f32 v155, v155, v172\n"
"v_exp_f32 v155, v155\n"
"v_cvt_pk_bf16_f32 v152, v152, v153\n"
"v_cvt_pk_bf16_f32 v153, v154, v155\n"
"ds_write_addtid_b32 v144 offset:54784\n"
"ds_write_addtid_b32 v145 offset:55040\n"
"s_waitcnt lgkmcnt(4)\n"
"v_mfma_f32_16x16x32_bf16 v[148:151], v[232:235], v[136:139], v[148:151]\n"
"ds_write_addtid_b32 v152 offset:33792\n"
"v_mfma_f32_16x16x32_bf16 v[148:151], v[234:237], v[24:27], v[148:151]\n"
"ds_write_addtid_b32 v153 offset:34048\n"
"s_waitcnt lgkmcnt(4)\n"
"v_mfma_f32_16x16x32_bf16 v[132:135], v[238:241], v[136:139], v[132:135]\n"
"ds_read_b128 v[140:143], v174 offset:1392\n"
"v_mfma_f32_16x16x32_bf16 v[132:135], v[240:243], v[24:27], v[132:135]\n"
"s_waitcnt lgkmcnt(1)\n"
"ds_read_b128 v[144:147], v175 offset:4080\n"
"s_barrier\n"
"v_sub_f32 v148, v148, v172\n"
"v_exp_f32 v148, v148\n"
"v_sub_f32 v149, v149, v172\n"
"v_exp_f32 v149, v149\n"
"v_sub_f32 v150, v150, v172\n"
"v_exp_f32 v150, v150\n"
"v_sub_f32 v151, v151, v172\n"
"v_exp_f32 v151, v151\n"
"v_sub_f32 v132, v132, v172\n"
"v_exp_f32 v132, v132\n"
"v_cvt_pk_bf16_f32 v148, v148, v149\n"
"v_cvt_pk_bf16_f32 v149, v150, v151\n"
"v_sub_f32 v133, v133, v172\n"
"v_exp_f32 v133, v133\n"
"v_sub_f32 v134, v134, v172\n"
"v_exp_f32 v134, v134\n"
"v_sub_f32 v135, v135, v172\n"
"v_exp_f32 v135, v135\n"
"v_cvt_pk_bf16_f32 v132, v132, v133\n"
"v_cvt_pk_bf16_f32 v133, v134, v135\n"
"ds_write_addtid_b32 v148 offset:57856\n"
"ds_write_addtid_b32 v149 offset:58112\n"
"s_waitcnt lgkmcnt(3)\n"
"v_mfma_f32_16x16x32_bf16 v[140:143], v[244:247], v[136:139], v[140:143]\n"
"ds_write_addtid_b32 v132 offset:58368\n"
"v_mfma_f32_16x16x32_bf16 v[140:143], v[246:249], v[24:27], v[140:143]\n"
"ds_read_b128 v[232:235], v4 offset:43008\n"
"ds_write_addtid_b32 v133 offset:58624\n"
"s_waitcnt lgkmcnt(5)\n"
"v_mfma_f32_16x16x32_bf16 v[144:147], v[226:229], v[136:139], v[144:147]\n"
"v_mfma_f32_16x16x32_bf16 v[144:147], v[228:231], v[24:27], v[144:147]\n"
"s_waitcnt lgkmcnt(1)\n"
"v_mfma_f32_16x16x32_bf16 v[148:151], v[84:87], v[232:235], 0\n"
"ds_read_b128 v[152:155], v5 offset:43008\n"
"v_mfma_f32_16x16x32_bf16 v[156:159], v[88:91], v[232:235], 0\n"
"v_mfma_f32_16x16x32_bf16 v[160:163], v[92:95], v[232:235], 0\n"
"ds_read_b128 v[132:135], v6 offset:5120\n"
"v_sub_f32 v140, v140, v172\n"
"v_exp_f32 v140, v140\n"
"v_sub_f32 v141, v141, v172\n"
"v_exp_f32 v141, v141\n"
"v_sub_f32 v142, v142, v172\n"
"v_exp_f32 v142, v142\n"
"v_cvt_pk_bf16_f32 v140, v140, v141\n"
"v_sub_f32 v143, v143, v172\n"
"v_exp_f32 v143, v143\n"
"s_waitcnt lgkmcnt(1)\n"
"v_mfma_f32_16x16x32_bf16 v[148:151], v[96:99], v[152:155], v[148:151]\n"
"ds_write_addtid_b32 v140 offset:58880\n"
"v_cvt_pk_bf16_f32 v141, v142, v143\n"
"v_mfma_f32_16x16x32_bf16 v[156:159], v[100:103], v[152:155], v[156:159]\n"
"ds_read_b128 v[232:235], v4 offset:45056\n"
"ds_write_addtid_b32 v141 offset:59136\n"
"s_waitcnt lgkmcnt(3)\n"
"v_mfma_f32_16x16x32_bf16 v[160:163], v[132:135], v[152:155], v[160:163]\n"
"ds_read_b128 v[136:139], v6 offset:6144\n"
"v_sub_f32 v144, v144, v172\n"
"v_exp_f32 v144, v144\n"
"v_sub_f32 v145, v145, v172\n"
"v_exp_f32 v145, v145\n"
"v_sub_f32 v146, v146, v172\n"
"v_exp_f32 v146, v146\n"
"v_sub_f32 v147, v147, v172\n"
"v_exp_f32 v147, v147\n"
"v_cvt_pk_bf16_f32 v144, v144, v145\n"
"v_cvt_pk_bf16_f32 v145, v146, v147\n"
"s_waitcnt lgkmcnt(2)\n"
"v_mfma_f32_16x16x32_bf16 v[148:151], v[104:107], v[232:235], v[148:151]\n"
"ds_read_b128 v[132:135], v5 offset:45056\n"
"v_mfma_f32_16x16x32_bf16 v[156:159], v[108:111], v[232:235], v[156:159]\n"
"ds_write_addtid_b32 v144 offset:57344\n"
"s_waitcnt lgkmcnt(2)\n"
"v_mfma_f32_16x16x32_bf16 v[160:163], v[136:139], v[232:235], v[160:163]\n"
"ds_read_b128 v[140:143], v6 offset:7168\n"
"ds_write_addtid_b32 v145 offset:57600\n"
"s_waitcnt lgkmcnt(3)\n"
"v_mfma_f32_16x16x32_bf16 v[148:151], v[112:115], v[132:135], v[148:151]\n"
"ds_read_b128 v[152:155], v4 offset:47104\n"
"v_mfma_f32_16x16x32_bf16 v[156:159], v[116:119], v[132:135], v[156:159]\n"
"s_waitcnt lgkmcnt(1)\n"
"s_barrier\n"
"v_mfma_f32_16x16x32_bf16 v[160:163], v[140:143], v[132:135], v[160:163]\n"
"s_waitcnt lgkmcnt(0)\n"
"ds_read_b128 v[136:139], v6 offset:8192\n"
"v_mfma_f32_16x16x32_bf16 v[148:151], v[0:3], v[152:155], v[148:151]\n"
"ds_read_b128 v[144:147], v5 offset:47104\n"
"v_mfma_f32_16x16x32_bf16 v[156:159], v[120:123], v[152:155], v[156:159]\n"
"s_waitcnt lgkmcnt(1)\n"
"v_mfma_f32_16x16x32_bf16 v[160:163], v[136:139], v[152:155], v[160:163]\n"
"s_waitcnt lgkmcnt(0)\n"
"ds_read_b128 v[164:167], v6 offset:9216\n"
"v_mfma_f32_16x16x32_bf16 v[148:151], v[124:127], v[144:147], v[148:151]\n"
"ds_read_b128 v[132:135], v4 offset:36864\n"
"v_mfma_f32_16x16x32_bf16 v[156:159], v[128:131], v[144:147], v[156:159]\n"
"s_waitcnt lgkmcnt(1)\n"
"v_mfma_f32_16x16x32_bf16 v[160:163], v[164:167], v[144:147], v[160:163]\n"
"s_waitcnt lgkmcnt(0)\n"
"v_mfma_f32_16x16x32_bf16 v[232:235], v[8:11], v[132:135], 0\n"
"ds_read_b128 v[136:139], v5 offset:36864\n"
"v_mfma_f32_16x16x32_bf16 v[140:143], v[12:15], v[132:135], 0\n"
"v_mfma_f32_16x16x32_bf16 v[152:155], v[16:19], v[132:135], 0\n"
"s_waitcnt lgkmcnt(0)\n"
"v_mfma_f32_16x16x32_bf16 v[232:235], v[20:23], v[136:139], v[232:235]\n"
"ds_read_b128 v[168:171], v4 offset:38912\n"
"v_mul_f32 v148, s60, v148\n"
"v_mfma_f32_16x16x32_bf16 v[140:143], v[28:31], v[136:139], v[140:143]\n"
"v_mul_f32 v149, s60, v149\n"
"v_cvt_pk_bf16_f32 v148, v148, v149\n"
"v_mfma_f32_16x16x32_bf16 v[152:155], v[32:35], v[136:139], v[152:155]\n"
"v_mul_f32 v150, s60, v150\n"
"v_mul_f32 v151, s60, v151\n"
"v_cvt_pk_bf16_f32 v149, v150, v151\n"
"v_mul_f32 v156, s60, v156\n"
"v_mul_f32 v157, s60, v157\n"
"v_mul_f32 v158, s60, v158\n"
"v_mul_f32 v159, s60, v159\n"
"v_cvt_pk_bf16_f32 v150, v156, v157\n"
"v_cvt_pk_bf16_f32 v151, v158, v159\n"
"v_mul_f32 v160, s60, v160\n"
"v_mul_f32 v161, s60, v161\n"
"v_mul_f32 v162, s60, v162\n"
"s_waitcnt lgkmcnt(0)\n"
"v_mfma_f32_16x16x32_bf16 v[232:235], v[36:39], v[168:171], v[232:235]\n"
"ds_read_b128 v[132:135], v5 offset:38912\n"
"v_mul_f32 v163, s60, v163\n"
"v_mfma_f32_16x16x32_bf16 v[140:143], v[40:43], v[168:171], v[140:143]\n"
"v_cvt_pk_bf16_f32 v26, v160, v161\n"
"v_cvt_pk_bf16_f32 v27, v162, v163\n"
"v_mfma_f32_16x16x32_bf16 v[152:155], v[44:47], v[168:171], v[152:155]\n"
"s_waitcnt lgkmcnt(0)\n"
"v_bfe_u32 v177, v6, 2, 6\n"
"ds_read_b32 v177, v177 offset:1664\n"
"v_mfma_f32_16x16x32_bf16 v[232:235], v[48:51], v[132:135], v[232:235]\n"
"ds_read_b128 v[136:139], v4 offset:40960\n"
"v_mfma_f32_16x16x32_bf16 v[140:143], v[52:55], v[132:135], v[140:143]\n"
"s_add_u32 s69, s50, 4294965920\n"
"v_mfma_f32_16x16x32_bf16 v[152:155], v[56:59], v[132:135], v[152:155]\n"
"s_waitcnt lgkmcnt(1)\n"
"v_add_u32_sdwa v176, v177, v7 dst_sel:DWORD dst_unused:UNUSED_PAD src0_sel:WORD_0 src1_sel:DWORD\n"
"s_nop 0\n"
"v_add_u32_sdwa v177, sext(v177), v176 dst_sel:DWORD dst_unused:UNUSED_PAD src0_sel:WORD_1 src1_sel:DWORD\n"
"s_waitcnt lgkmcnt(0)\n"
"v_mfma_f32_16x16x32_bf16 v[232:235], v[60:63], v[136:139], v[232:235]\n"
"ds_read_b128 v[144:147], v5 offset:40960\n"
"v_max_u32 v175, s69, v176\n"
"v_mfma_f32_16x16x32_bf16 v[140:143], v[64:67], v[136:139], v[140:143]\n"
"v_mfma_f32_16x16x32_bf16 v[152:155], v[68:71], v[136:139], v[152:155]\n"
"s_add_u32 s70, s50, 4294966960\n"
"v_max_u32 v174, s69, v177\n"
"s_waitcnt lgkmcnt(0)\n"
"v_mfma_f32_16x16x32_bf16 v[232:235], v[72:75], v[144:147], v[232:235]\n"
"ds_read_b128 v[132:135], v175 offset:1392\n"
"v_mfma_f32_16x16x32_bf16 v[140:143], v[76:79], v[144:147], v[140:143]\n"
"v_min_u32 v173, s70, v176\n"
"v_mfma_f32_16x16x32_bf16 v[152:155], v[80:83], v[144:147], v[152:155]\n"
"s_waitcnt lgkmcnt(0)\n"
"ds_read_b128 v[136:139], v174 offset:1408\n"
"v_mfma_f32_16x16x32_bf16 v[132:135], v[244:247], v[148:151], v[132:135]\n"
"v_mfma_f32_16x16x32_bf16 v[132:135], v[246:249], v[24:27], v[132:135]\n"
"ds_read_b128 v[156:159], v173 offset:2720\n"
"v_cvt_pk_bf16_f32 v232, v232, v233\n"
"v_cvt_pk_bf16_f32 v233, v234, v235\n"
"v_cvt_pk_bf16_f32 v234, v140, v141\n"
"v_cvt_pk_bf16_f32 v235, v142, v143\n"
"v_cvt_pk_bf16_f32 v236, v152, v153\n"
"v_cvt_pk_bf16_f32 v237, v154, v155\n"
"s_waitcnt lgkmcnt(1)\n"
"v_mfma_f32_16x16x32_bf16 v[136:139], v[250:253], v[148:151], v[136:139]\n"
"v_max3_f32 v170, v132, v133, v134\n"
"v_max_f32 v170, v170, v135\n"
"v_mfma_f32_16x16x32_bf16 v[136:139], v[252:255], v[24:27], v[136:139]\n"
"s_waitcnt lgkmcnt(0)\n"
"ds_read_b128 v[140:143], v173 offset:2736\n"
"v_min_u32 v172, s70, v177\n"
"v_mfma_f32_16x16x32_bf16 v[156:159], v[190:193], v[148:151], v[156:159]\n"
"s_add_u32 s71, s50, 4294965616\n"
"v_mfma_f32_16x16x32_bf16 v[156:159], v[192:195], v[24:27], v[156:159]\n"
"s_waitcnt lgkmcnt(0)\n"
"ds_read_b128 v[132:135], v172 offset:2752\n"
"v_max3_f32 v170, v170, v136, v137\n"
"v_max3_f32 v170, v170, v138, v139\n"
"v_mfma_f32_16x16x32_bf16 v[140:143], v[196:199], v[148:151], v[140:143]\n"
"v_min_u32 v169, s71, v176\n"
"v_mfma_f32_16x16x32_bf16 v[140:143], v[198:201], v[24:27], v[140:143]\n"
"s_waitcnt lgkmcnt(0)\n"
"ds_read_b128 v[144:147], v175 offset:1376\n"
"v_max3_f32 v170, v170, v156, v157\n"
"v_max3_f32 v170, v170, v158, v159\n"
"v_mfma_f32_16x16x32_bf16 v[132:135], v[202:205], v[148:151], v[132:135]\n"
"v_mfma_f32_16x16x32_bf16 v[132:135], v[204:207], v[24:27], v[132:135]\n"
"s_waitcnt lgkmcnt(0)\n"
"ds_read_b128 v[136:139], v169 offset:4064\n"
"v_max3_f32 v170, v170, v140, v141\n"
"v_max3_f32 v170, v170, v142, v143\n"
"v_mfma_f32_16x16x32_bf16 v[144:147], v[238:241], v[148:151], v[144:147]\n"
"v_min_u32 v168, s71, v177\n"
"v_mfma_f32_16x16x32_bf16 v[144:147], v[240:243], v[24:27], v[144:147]\n"
"s_waitcnt lgkmcnt(0)\n"
"ds_read_b128 v[152:155], v169 offset:4080\n"
"v_max3_f32 v170, v170, v132, v133\n"
"v_max3_f32 v170, v170, v134, v135\n"
"v_mfma_f32_16x16x32_bf16 v[136:139], v[220:223], v[148:151], v[136:139]\n"
"v_mfma_f32_16x16x32_bf16 v[136:139], v[222:225], v[24:27], v[136:139]\n"
"s_waitcnt lgkmcnt(0)\n"
"ds_read_b128 v[140:143], v168 offset:4096\n"
"v_max3_f32 v170, v170, v144, v145\n"
"v_max3_f32 v170, v170, v146, v147\n"
"v_mfma_f32_16x16x32_bf16 v[152:155], v[226:229], v[148:151], v[152:155]\n"
"v_mfma_f32_16x16x32_bf16 v[152:155], v[228:231], v[24:27], v[152:155]\n"
"s_waitcnt lgkmcnt(0)\n"
"ds_read_b128 v[132:135], v175 offset:1392\n"
"v_max3_f32 v170, v170, v136, v137\n"
"v_max3_f32 v170, v170, v138, v139\n"
"v_mfma_f32_16x16x32_bf16 v[140:143], v[232:235], v[148:151], v[140:143]\n"
"v_mfma_f32_16x16x32_bf16 v[140:143], v[234:237], v[24:27], v[140:143]\n"
"s_waitcnt lgkmcnt(0)\n"
"ds_read_b128 v[144:147], v174 offset:1408\n"
"v_max3_f32 v170, v170, v152, v153\n"
"v_max3_f32 v170, v170, v154, v155\n"
"v_mfma_f32_16x16x32_bf16 v[132:135], v[244:247], v[148:151], v[132:135]\n"
"v_mfma_f32_16x16x32_bf16 v[132:135], v[246:249], v[24:27], v[132:135]\n"
"ds_read_b128 v[136:139], v173 offset:2720\n"
"s_nop 0\n"
"v_max3_f32 v170, v170, v140, v141\n"
"v_max3_f32 v170, v170, v142, v143\n"
"v_mov_b32 v171, v170\n"
"s_nop 1\n"
"v_permlane16_swap_b32 v170, v171\n"
"v_max_f32 v170, v170, v171\n"
"v_mov_b32 v171, v170\n"
"s_nop 1\n"
"v_permlane32_swap_b32 v170, v171\n"
"v_max_f32 v170, v170, v171\n"
"v_sub_f32 v132, v132, v170\n"
"v_exp_f32 v132, v132\n"
"v_sub_f32 v133, v133, v170\n"
"v_exp_f32 v133, v133\n"
"v_sub_f32 v134, v134, v170\n"
"v_exp_f32 v134, v134\n"
"v_sub_f32 v135, v135, v170\n"
"v_exp_f32 v135, v135\n"
"s_waitcnt lgkmcnt(1)\n"
"v_mfma_f32_16x16x32_bf16 v[144:147], v[250:253], v[148:151], v[144:147]\n"
"v_cvt_pk_bf16_f32 v132, v132, v133\n"
"v_cvt_pk_bf16_f32 v133, v134, v135\n"
"v_mfma_f32_16x16x32_bf16 v[144:147], v[252:255], v[24:27], v[144:147]\n"
"ds_read_b128 v[244:247], v173 offset:2736\n"
"ds_write_addtid_b32 v132 offset:55296\n"
"ds_write_addtid_b32 v133 offset:55552\n"
"s_waitcnt lgkmcnt(3)\n"
"v_mfma_f32_16x16x32_bf16 v[136:139], v[190:193], v[148:151], v[136:139]\n"
"v_mfma_f32_16x16x32_bf16 v[136:139], v[192:195], v[24:27], v[136:139]\n"
"ds_read_b128 v[248:251], v172 offset:2752\n"
"s_nop 0\n"
"v_sub_f32 v144, v144, v170\n"
"v_exp_f32 v144, v144\n"
"v_sub_f32 v145, v145, v170\n"
"v_exp_f32 v145, v145\n"
"v_sub_f32 v146, v146, v170\n"
"v_exp_f32 v146, v146\n"
"v_sub_f32 v147, v147, v170\n"
"v_exp_f32 v147, v147\n"
"v_sub_f32 v138, v138, v170\n"
"v_exp_f32 v138, v138\n"
"v_sub_f32 v139, v139, v170\n"
"v_exp_f32 v139, v139\n"
"v_cvt_pk_bf16_f32 v144, v144, v145\n"
"v_cvt_pk_bf16_f32 v145, v146, v147\n"
"v_cvt_pk_bf16_f32 v137, v138, v139\n"
"ds_write_addtid_b32 v144 offset:55808\n"
"ds_write_addtid_b32 v145 offset:56064\n"
"s_waitcnt lgkmcnt(5)\n"
"v_mfma_f32_16x16x32_bf16 v[244:247], v[196:199], v[148:151], v[244:247]\n"
"ds_write_addtid_b32 v137 offset:56576\n"
"v_mfma_f32_16x16x32_bf16 v[244:247], v[198:201], v[24:27], v[244:247]\n"
"s_waitcnt lgkmcnt(3)\n"
"ds_read_b128 v[252:255], v175 offset:1376\n"
"v_mfma_f32_16x16x32_bf16 v[248:251], v[202:205], v[148:151], v[248:251]\n"
"v_mfma_f32_16x16x32_bf16 v[248:251], v[204:207], v[24:27], v[248:251]\n"
"ds_read_b128 v[132:135], v169 offset:4064\n"
"s_nop 2\n"
"v_sub_f32 v244, v244, v170\n"
"v_exp_f32 v244, v244\n"
"v_sub_f32 v245, v245, v170\n"
"v_exp_f32 v245, v245\n"
"v_sub_f32 v246, v246, v170\n"
"v_exp_f32 v246, v246\n"
"v_sub_f32 v247, v247, v170\n"
"v_exp_f32 v247, v247\n"
"v_sub_f32 v248, v248, v170\n"
"v_exp_f32 v248, v248\n"
"v_sub_f32 v249, v249, v170\n"
"v_exp_f32 v249, v249\n"
"v_cvt_pk_bf16_f32 v244, v244, v245\n"
"v_sub_f32 v250, v250, v170\n"
"v_exp_f32 v250, v250\n"
"v_cvt_pk_bf16_f32 v245, v246, v247\n"
"v_sub_f32 v251, v251, v170\n"
"v_exp_f32 v251, v251\n"
"v_cvt_pk_bf16_f32 v248, v248, v249\n"
"v_cvt_pk_bf16_f32 v249, v250, v251\n"
"ds_write_addtid_b32 v244 offset:56832\n"
"ds_write_addtid_b32 v245 offset:57088\n"
"s_waitcnt lgkmcnt(3)\n"
"v_mfma_f32_16x16x32_bf16 v[252:255], v[238:241], v[148:151], v[252:255]\n"
"ds_write_addtid_b32 v248 offset:34304\n"
"v_mfma_f32_16x16x32_bf16 v[252:255], v[240:243], v[24:27], v[252:255]\n"
"ds_read_b128 v[136:139], v169 offset:4080\n"
"ds_write_addtid_b32 v249 offset:34560\n"
"s_waitcnt lgkmcnt(5)\n"
"v_mfma_f32_16x16x32_bf16 v[132:135], v[220:223], v[148:151], v[132:135]\n"
"v_mfma_f32_16x16x32_bf16 v[132:135], v[222:225], v[24:27], v[132:135]\n"
"s_waitcnt lgkmcnt(0)\n"
"ds_read_b128 v[244:247], v168 offset:4096\n"
"s_barrier\n"
"v_sub_f32 v254, v254, v170\n"
"v_exp_f32 v254, v254\n"
"v_sub_f32 v255, v255, v170\n"
"v_exp_f32 v255, v255\n"
"s_nop 0\n"
"v_cvt_pk_bf16_f32 v253, v254, v255\n"
"ds_write_addtid_b32 v253 offset:59136\n"
"v_mfma_f32_16x16x32_bf16 v[136:139], v[226:229], v[148:151], v[136:139]\n"
"v_sub_f32 v134, v134, v170\n"
"v_exp_f32 v134, v134\n"
"v_sub_f32 v135, v135, v170\n"
"v_exp_f32 v135, v135\n"
"v_mfma_f32_16x16x32_bf16 v[136:139], v[228:231], v[24:27], v[136:139]\n"
"ds_read_b128 v[238:241], v4 offset:24576\n"
"v_cvt_pk_bf16_f32 v133, v134, v135\n"
"ds_write_addtid_b32 v133 offset:57600\n"
"s_waitcnt lgkmcnt(3)\n"
"v_mfma_f32_16x16x32_bf16 v[244:247], v[232:235], v[148:151], v[244:247]\n"
"v_mfma_f32_16x16x32_bf16 v[244:247], v[234:237], v[24:27], v[244:247]\n"
"s_waitcnt lgkmcnt(1)\n"
"v_mfma_f32_16x16x32_bf16 v[250:253], v[84:87], v[238:241], 0\n"
"ds_read_b128 v[140:143], v5 offset:24576\n"
"v_mfma_f32_16x16x32_bf16 v[144:147], v[88:91], v[238:241], 0\n"
"v_sub_f32 v136, v136, v170\n"
"v_exp_f32 v136, v136\n"
"v_sub_f32 v137, v137, v170\n"
"v_exp_f32 v137, v137\n"
"v_sub_f32 v138, v138, v170\n"
"v_exp_f32 v138, v138\n"
"v_mfma_f32_16x16x32_bf16 v[132:135], v[92:95], v[238:241], 0\n"
"ds_read_b128 v[148:151], v6 offset:5120\n"
"v_sub_f32 v139, v139, v170\n"
"v_exp_f32 v139, v139\n"
"v_cvt_pk_bf16_f32 v136, v136, v137\n"
"v_cvt_pk_bf16_f32 v137, v138, v139\n"
"s_waitcnt lgkmcnt(1)\n"
"v_mfma_f32_16x16x32_bf16 v[250:253], v[96:99], v[140:143], v[250:253]\n"
"ds_write_addtid_b32 v136 offset:57856\n"
"v_mfma_f32_16x16x32_bf16 v[144:147], v[100:103], v[140:143], v[144:147]\n"
"ds_read_b128 v[238:241], v4 offset:26624\n"
"v_sub_f32 v244, v244, v170\n"
"v_exp_f32 v244, v244\n"
"v_sub_f32 v245, v245, v170\n"
"v_exp_f32 v245, v245\n"
"s_waitcnt lgkmcnt(2)\n"
"v_mfma_f32_16x16x32_bf16 v[132:135], v[148:151], v[140:143], v[132:135]\n"
"ds_write_addtid_b32 v137 offset:58112\n"
"v_sub_f32 v246, v246, v170\n"
"v_exp_f32 v246, v246\n"
"v_sub_f32 v247, v247, v170\n"
"v_exp_f32 v247, v247\n"
"v_cvt_pk_bf16_f32 v244, v244, v245\n"
"v_cvt_pk_bf16_f32 v245, v246, v247\n"
"s_waitcnt lgkmcnt(1)\n"
"v_mfma_f32_16x16x32_bf16 v[250:253], v[104:107], v[238:241], v[250:253]\n"
"ds_read_b128 v[136:139], v6 offset:6144\n"
"v_mfma_f32_16x16x32_bf16 v[144:147], v[108:111], v[238:241], v[144:147]\n"
"ds_read_b128 v[140:143], v5 offset:26624\n"
"ds_write_addtid_b32 v244 offset:58368\n"
"ds_write_addtid_b32 v245 offset:58624\n"
"s_waitcnt lgkmcnt(3)\n"
"v_mfma_f32_16x16x32_bf16 v[132:135], v[136:139], v[238:241], v[132:135]\n"
"s_waitcnt lgkmcnt(2)\n"
"ds_read_b128 v[246:249], v6 offset:7168\n"
"v_mfma_f32_16x16x32_bf16 v[250:253], v[112:115], v[140:143], v[250:253]\n"
"s_waitcnt lgkmcnt(1)\n"
"s_barrier\n"
"ds_read_b128 v[148:151], v4 offset:28672\n"
"v_mfma_f32_16x16x32_bf16 v[144:147], v[116:119], v[140:143], v[144:147]\n"
"s_waitcnt lgkmcnt(1)\n"
"v_mfma_f32_16x16x32_bf16 v[132:135], v[246:249], v[140:143], v[132:135]\n"
"s_waitcnt lgkmcnt(0)\n"
"ds_read_b128 v[238:241], v6 offset:8192\n"
"v_mfma_f32_16x16x32_bf16 v[250:253], v[0:3], v[148:151], v[250:253]\n"
"ds_read_b128 v[242:245], v5 offset:28672\n"
"v_mfma_f32_16x16x32_bf16 v[144:147], v[120:123], v[148:151], v[144:147]\n"
"s_waitcnt lgkmcnt(1)\n"
"v_mfma_f32_16x16x32_bf16 v[132:135], v[238:241], v[148:151], v[132:135]\n"
"s_waitcnt lgkmcnt(0)\n"
"ds_read_b128 v[136:139], v6 offset:9216\n"
"v_mfma_f32_16x16x32_bf16 v[250:253], v[124:127], v[242:245], v[250:253]\n"
"ds_read_b128 v[246:249], v4 offset:30720\n"
"v_mfma_f32_16x16x32_bf16 v[144:147], v[128:131], v[242:245], v[144:147]\n"
"s_waitcnt lgkmcnt(1)\n"
"v_mfma_f32_16x16x32_bf16 v[132:135], v[136:139], v[242:245], v[132:135]\n"
"s_waitcnt lgkmcnt(0)\n"
"ds_read_b128 v[140:143], v5 offset:30720\n"
"v_mfma_f32_16x16x32_bf16 v[238:241], v[8:11], v[246:249], 0\n"
"v_mfma_f32_16x16x32_bf16 v[148:151], v[12:15], v[246:249], 0\n"
"v_mfma_f32_16x16x32_bf16 v[152:155], v[16:19], v[246:249], 0\n"
"ds_read_b128 v[156:159], v4 offset:32768\n"
"v_mul_f32 v250, s60, v250\n"
"v_mul_f32 v251, s60, v251\n"
"v_cvt_pk_bf16_f32 v250, v250, v251\n"
"v_mul_f32 v252, s60, v252\n"
"v_mul_f32 v253, s60, v253\n"
"v_cvt_pk_bf16_f32 v251, v252, v253\n"
"v_mul_f32 v144, s60, v144\n"
"s_waitcnt lgkmcnt(1)\n"
"v_mfma_f32_16x16x32_bf16 v[238:241], v[20:23], v[140:143], v[238:241]\n"
"v_mul_f32 v145, s60, v145\n"
"v_mul_f32 v146, s60, v146\n"
"v_mfma_f32_16x16x32_bf16 v[148:151], v[28:31], v[140:143], v[148:151]\n"
"v_mul_f32 v147, s60, v147\n"
"v_cvt_pk_bf16_f32 v252, v144, v145\n"
"v_mfma_f32_16x16x32_bf16 v[152:155], v[32:35], v[140:143], v[152:155]\n"
"s_waitcnt lgkmcnt(0)\n"
"ds_read_b128 v[242:245], v5 offset:32768\n"
"v_cvt_pk_bf16_f32 v253, v146, v147\n"
"v_mfma_f32_16x16x32_bf16 v[238:241], v[36:39], v[156:159], v[238:241]\n"
"v_mul_f32 v132, s60, v132\n"
"v_mul_f32 v133, s60, v133\n"
"v_mfma_f32_16x16x32_bf16 v[148:151], v[40:43], v[156:159], v[148:151]\n"
"v_mul_f32 v134, s60, v134\n"
"v_mul_f32 v135, s60, v135\n"
"v_mfma_f32_16x16x32_bf16 v[152:155], v[44:47], v[156:159], v[152:155]\n"
"s_waitcnt lgkmcnt(0)\n"
"ds_read_b128 v[136:139], v4 offset:34816\n"
"v_cvt_pk_bf16_f32 v26, v132, v133\n"
"v_cvt_pk_bf16_f32 v27, v134, v135\n"
"v_mfma_f32_16x16x32_bf16 v[238:241], v[48:51], v[242:245], v[238:241]\n"
"v_mfma_f32_16x16x32_bf16 v[148:151], v[52:55], v[242:245], v[148:151]\n"
"v_mfma_f32_16x16x32_bf16 v[152:155], v[56:59], v[242:245], v[152:155]\n"
"s_waitcnt lgkmcnt(0)\n"
"ds_read_b128 v[140:143], v5 offset:34816\n"
"v_mfma_f32_16x16x32_bf16 v[238:241], v[60:63], v[136:139], v[238:241]\n"
"v_mfma_f32_16x16x32_bf16 v[148:151], v[64:67], v[136:139], v[148:151]\n"
"v_mfma_f32_16x16x32_bf16 v[152:155], v[68:71], v[136:139], v[152:155]\n"
"s_waitcnt lgkmcnt(0)\n"
"ds_read_b128 v[132:135], v4 offset:49152\n"
"v_mfma_f32_16x16x32_bf16 v[238:241], v[72:75], v[140:143], v[238:241]\n"
"v_mfma_f32_16x16x32_bf16 v[148:151], v[76:79], v[140:143], v[148:151]\n"
"v_mfma_f32_16x16x32_bf16 v[152:155], v[80:83], v[140:143], v[152:155]\n"
"s_waitcnt lgkmcnt(0)\n"
"ds_read_b128 v[144:147], v5 offset:49152\n"
"v_mfma_f32_16x16x32_bf16 v[244:247], v[8:11], v[132:135], 0\n"
"v_mfma_f32_16x16x32_bf16 v[136:139], v[12:15], v[132:135], 0\n"
"v_mfma_f32_16x16x32_bf16 v[156:159], v[16:19], v[132:135], 0\n"
"s_waitcnt lgkmcnt(0)\n"
"ds_read_b128 v[160:163], v4 offset:51200\n"
"v_mfma_f32_16x16x32_bf16 v[244:247], v[20:23], v[144:147], v[244:247]\n"
"v_cvt_pk_bf16_f32 v238, v238, v239\n"
"v_cvt_pk_bf16_f32 v239, v240, v241\n"
"v_cvt_pk_bf16_f32 v240, v148, v149\n"
"v_cvt_pk_bf16_f32 v241, v150, v151\n"
"v_cvt_pk_bf16_f32 v242, v152, v153\n"
"v_cvt_pk_bf16_f32 v243, v154, v155\n"
"v_mfma_f32_16x16x32_bf16 v[136:139], v[28:31], v[144:147], v[136:139]\n"
"v_mfma_f32_16x16x32_bf16 v[156:159], v[32:35], v[144:147], v[156:159]\n"
"s_waitcnt lgkmcnt(0)\n"
"ds_read_b128 v[132:135], v5 offset:51200\n"
"v_mfma_f32_16x16x32_bf16 v[244:247], v[36:39], v[160:163], v[244:247]\n"
"v_bfe_u32 v255, v6, 2, 6\n"
"v_mfma_f32_16x16x32_bf16 v[136:139], v[40:43], v[160:163], v[136:139]\n"
"ds_read_b32 v255, v255 offset:1728\n"
"v_mfma_f32_16x16x32_bf16 v[156:159], v[44:47], v[160:163], v[156:159]\n"
"s_add_u32 s72, s50, 4294964592\n"
"ds_read_b128 v[140:143], v4 offset:53248\n"
"s_waitcnt lgkmcnt(2)\n"
"v_mfma_f32_16x16x32_bf16 v[244:247], v[48:51], v[132:135], v[244:247]\n"
"v_mfma_f32_16x16x32_bf16 v[136:139], v[52:55], v[132:135], v[136:139]\n"
"v_mfma_f32_16x16x32_bf16 v[156:159], v[56:59], v[132:135], v[156:159]\n"
"s_waitcnt lgkmcnt(1)\n"
"ds_read_b128 v[144:147], v5 offset:53248\n"
"v_add_u32_sdwa v254, v255, v7 dst_sel:DWORD dst_unused:UNUSED_PAD src0_sel:WORD_0 src1_sel:DWORD\n"
"s_waitcnt lgkmcnt(1)\n"
"v_mfma_f32_16x16x32_bf16 v[244:247], v[60:63], v[140:143], v[244:247]\n"
"v_max_u32 v177, s72, v254\n"
"v_add_u32_sdwa v255, sext(v255), v254 dst_sel:DWORD dst_unused:UNUSED_PAD src0_sel:WORD_1 src1_sel:DWORD\n"
"v_mfma_f32_16x16x32_bf16 v[136:139], v[64:67], v[140:143], v[136:139]\n"
"v_mfma_f32_16x16x32_bf16 v[156:159], v[68:71], v[140:143], v[156:159]\n"
"s_waitcnt lgkmcnt(0)\n"
"ds_read_b128 v[132:135], v177 offset:2704\n"
"v_mfma_f32_16x16x32_bf16 v[244:247], v[72:75], v[144:147], v[244:247]\n"
"v_mfma_f32_16x16x32_bf16 v[136:139], v[76:79], v[144:147], v[136:139]\n"
"s_add_u32 s73, s50, 4294965632\n"
"ds_read_b128 v[148:151], v177 offset:2720\n"
"v_max_u32 v176, s72, v255\n"
"v_mfma_f32_16x16x32_bf16 v[156:159], v[80:83], v[144:147], v[156:159]\n"
"s_waitcnt lgkmcnt(1)\n"
"v_mfma_f32_16x16x32_bf16 v[132:135], v[184:187], v[250:253], v[132:135]\n"
"ds_read_b128 v[140:143], v177 offset:2736\n"
"v_min_u32 v175, s73, v254\n"
"v_cvt_pk_bf16_f32 v244, v244, v245\n"
"v_cvt_pk_bf16_f32 v245, v246, v247\n"
"v_cvt_pk_bf16_f32 v246, v136, v137\n"
"v_cvt_pk_bf16_f32 v247, v138, v139\n"
"v_mfma_f32_16x16x32_bf16 v[132:135], v[186:189], v[24:27], v[132:135]\n"
"s_waitcnt lgkmcnt(1)\n"
"v_cvt_pk_bf16_f32 v248, v156, v157\n"
"v_cvt_pk_bf16_f32 v249, v158, v159\n"
"v_mfma_f32_16x16x32_bf16 v[148:151], v[190:193], v[250:253], v[148:151]\n"
"ds_read_b128 v[144:147], v176 offset:2752\n"
"v_mfma_f32_16x16x32_bf16 v[148:151], v[192:195], v[24:27], v[148:151]\n"
"s_waitcnt lgkmcnt(1)\n"
"s_nop 0\n"
"v_max3_f32 v254, v132, v133, v134\n"
"v_max_f32 v254, v254, v135\n"
"v_mfma_f32_16x16x32_bf16 v[140:143], v[196:199], v[250:253], v[140:143]\n"
"ds_read_b128 v[136:139], v175 offset:4048\n"
"v_mfma_f32_16x16x32_bf16 v[140:143], v[198:201], v[24:27], v[140:143]\n"
"s_waitcnt lgkmcnt(1)\n"
"v_max3_f32 v254, v254, v148, v149\n"
"v_max3_f32 v254, v254, v150, v151\n"
"v_mfma_f32_16x16x32_bf16 v[144:147], v[202:205], v[250:253], v[144:147]\n"
"ds_read_b128 v[132:135], v175 offset:4064\n"
"v_min_u32 v174, s73, v255\n"
"v_mfma_f32_16x16x32_bf16 v[144:147], v[204:207], v[24:27], v[144:147]\n"
"s_waitcnt lgkmcnt(1)\n"
"v_max3_f32 v254, v254, v140, v141\n"
"v_max3_f32 v254, v254, v142, v143\n"
"v_mfma_f32_16x16x32_bf16 v[136:139], v[214:217], v[250:253], v[136:139]\n"
"ds_read_b128 v[148:151], v175 offset:4080\n"
"v_mfma_f32_16x16x32_bf16 v[136:139], v[216:219], v[24:27], v[136:139]\n"
"s_waitcnt lgkmcnt(1)\n"
"s_nop 0\n"
"v_max3_f32 v254, v254, v144, v145\n"
"v_max3_f32 v254, v254, v146, v147\n"
"v_mfma_f32_16x16x32_bf16 v[132:135], v[220:223], v[250:253], v[132:135]\n"
"ds_read_b128 v[140:143], v174 offset:4096\n"
"v_mfma_f32_16x16x32_bf16 v[132:135], v[222:225], v[24:27], v[132:135]\n"
"s_waitcnt lgkmcnt(1)\n"
"v_max3_f32 v254, v254, v136, v137\n"
"v_max3_f32 v254, v254, v138, v139\n"
"v_mfma_f32_16x16x32_bf16 v[148:151], v[226:229], v[250:253], v[148:151]\n"
"ds_read_b128 v[144:147], v177 offset:2704\n"
"v_mfma_f32_16x16x32_bf16 v[148:151], v[228:231], v[24:27], v[148:151]\n"
"s_waitcnt lgkmcnt(1)\n"
"s_nop 0\n"
"v_max3_f32 v254, v254, v132, v133\n"
"v_max3_f32 v254, v254, v134, v135\n"
"v_mfma_f32_16x16x32_bf16 v[140:143], v[232:235], v[250:253], v[140:143]\n"
"ds_read_b128 v[136:139], v177 offset:2720\n"
"v_mfma_f32_16x16x32_bf16 v[140:143], v[234:237], v[24:27], v[140:143]\n"
"s_waitcnt lgkmcnt(1)\n"
"v_max3_f32 v254, v254, v148, v149\n"
"v_max3_f32 v254, v254, v150, v151\n"
"v_mfma_f32_16x16x32_bf16 v[144:147], v[184:187], v[250:253], v[144:147]\n"
"ds_read_b128 v[132:135], v177 offset:2736\n"
"v_mfma_f32_16x16x32_bf16 v[144:147], v[186:189], v[24:27], v[144:147]\n"
"s_nop 1\n"
"v_max3_f32 v254, v254, v140, v141\n"
"v_max3_f32 v254, v254, v142, v143\n"
"v_mov_b32 v255, v254\n"
"s_nop 1\n"
"v_permlane16_swap_b32 v254, v255\n"
"v_max_f32 v254, v254, v255\n"
"v_mov_b32 v255, v254\n"
"s_nop 1\n"
"v_permlane32_swap_b32 v254, v255\n"
"v_max_f32 v254, v254, v255\n"
"v_sub_f32 v146, v146, v254\n"
"v_exp_f32 v146, v146\n"
"v_sub_f32 v147, v147, v254\n"
"v_exp_f32 v147, v147\n"
"s_waitcnt lgkmcnt(1)\n"
"v_mfma_f32_16x16x32_bf16 v[136:139], v[190:193], v[250:253], v[136:139]\n"
"ds_read_b128 v[140:143], v176 offset:2752\n"
"v_cvt_pk_bf16_f32 v145, v146, v147\n"
"v_mfma_f32_16x16x32_bf16 v[136:139], v[192:195], v[24:27], v[136:139]\n"
"ds_write_addtid_b32 v145 offset:53504\n"
"s_waitcnt lgkmcnt(2)\n"
"v_mfma_f32_16x16x32_bf16 v[132:135], v[196:199], v[250:253], v[132:135]\n"
"ds_read_b128 v[148:151], v175 offset:4048\n"
"v_mfma_f32_16x16x32_bf16 v[132:135], v[198:201], v[24:27], v[132:135]\n"
"s_nop 2\n"
"v_sub_f32 v136, v136, v254\n"
"v_exp_f32 v136, v136\n"
"v_sub_f32 v137, v137, v254\n"
"v_exp_f32 v137, v137\n"
"v_sub_f32 v138, v138, v254\n"
"v_exp_f32 v138, v138\n"
"v_sub_f32 v139, v139, v254\n"
"v_exp_f32 v139, v139\n"
"v_sub_f32 v132, v132, v254\n"
"v_exp_f32 v132, v132\n"
"v_sub_f32 v133, v133, v254\n"
"v_exp_f32 v133, v133\n"
"v_sub_f32 v134, v134, v254\n"
"v_exp_f32 v134, v134\n"
"v_sub_f32 v135, v135, v254\n"
"v_exp_f32 v135, v135\n"
"v_cvt_pk_bf16_f32 v136, v136, v137\n"
"v_cvt_pk_bf16_f32 v137, v138, v139\n"
"v_cvt_pk_bf16_f32 v132, v132, v133\n"
"v_cvt_pk_bf16_f32 v133, v134, v135\n"
"ds_write_addtid_b32 v136 offset:53760\n"
"ds_write_addtid_b32 v137 offset:54016\n"
"s_waitcnt lgkmcnt(4)\n"
"v_mfma_f32_16x16x32_bf16 v[140:143], v[202:205], v[250:253], v[140:143]\n"
"ds_read_b128 v[144:147], v175 offset:4064\n"
"v_mfma_f32_16x16x32_bf16 v[140:143], v[204:207], v[24:27], v[140:143]\n"
"ds_write_addtid_b32 v132 offset:54272\n"
"ds_write_addtid_b32 v133 offset:54528\n"
"s_waitcnt lgkmcnt(5)\n"
"v_mfma_f32_16x16x32_bf16 v[148:151], v[214:217], v[250:253], v[148:151]\n"
"ds_read_b128 v[136:139], v175 offset:4080\n"
"v_mfma_f32_16x16x32_bf16 v[148:151], v[216:219], v[24:27], v[148:151]\n"
"s_nop 1\n"
"v_sub_f32 v140, v140, v254\n"
"v_exp_f32 v140, v140\n"
"v_sub_f32 v141, v141, v254\n"
"v_exp_f32 v141, v141\n"
"v_sub_f32 v142, v142, v254\n"
"v_exp_f32 v142, v142\n"
"v_sub_f32 v143, v143, v254\n"
"v_exp_f32 v143, v143\n"
"v_cvt_pk_bf16_f32 v140, v140, v141\n"
"v_cvt_pk_bf16_f32 v141, v142, v143\n"
"v_sub_f32 v150, v150, v254\n"
"v_exp_f32 v150, v150\n"
"v_sub_f32 v151, v151, v254\n"
"v_exp_f32 v151, v151\n"
"ds_write_addtid_b32 v140 offset:54784\n"
"v_cvt_pk_bf16_f32 v149, v150, v151\n"
"ds_write_addtid_b32 v141 offset:55040\n"
"s_waitcnt lgkmcnt(5)\n"
"v_mfma_f32_16x16x32_bf16 v[144:147], v[220:223], v[250:253], v[144:147]\n"
"ds_read_b128 v[132:135], v174 offset:4096\n"
"v_mfma_f32_16x16x32_bf16 v[144:147], v[222:225], v[24:27], v[144:147]\n"
"s_waitcnt lgkmcnt(1)\n"
"s_barrier\n"
"ds_write_addtid_b32 v149 offset:57600\n"
"v_mfma_f32_16x16x32_bf16 v[136:139], v[226:229], v[250:253], v[136:139]\n"
"ds_read_b128 v[140:143], v4 offset:55296\n"
"v_mfma_f32_16x16x32_bf16 v[136:139], v[228:231], v[24:27], v[136:139]\n"
"s_nop 1\n"
"v_sub_f32 v144, v144, v254\n"
"v_exp_f32 v144, v144\n"
"v_sub_f32 v145, v145, v254\n"
"v_exp_f32 v145, v145\n"
"v_sub_f32 v146, v146, v254\n"
"v_exp_f32 v146, v146\n"
"v_sub_f32 v147, v147, v254\n"
"v_exp_f32 v147, v147\n"
"v_cvt_pk_bf16_f32 v144, v144, v145\n"
"v_cvt_pk_bf16_f32 v145, v146, v147\n"
"v_sub_f32 v136, v136, v254\n"
"v_exp_f32 v136, v136\n"
"v_sub_f32 v137, v137, v254\n"
"v_exp_f32 v137, v137\n"
"v_sub_f32 v138, v138, v254\n"
"v_exp_f32 v138, v138\n"
"v_sub_f32 v139, v139, v254\n"
"v_exp_f32 v139, v139\n"
"v_cvt_pk_bf16_f32 v136, v136, v137\n"
"v_cvt_pk_bf16_f32 v137, v138, v139\n"
"ds_write_addtid_b32 v144 offset:57856\n"
"ds_write_addtid_b32 v145 offset:58112\n"
"s_waitcnt lgkmcnt(4)\n"
"v_mfma_f32_16x16x32_bf16 v[132:135], v[232:235], v[250:253], v[132:135]\n"
"ds_write_addtid_b32 v136 offset:58368\n"
"v_mfma_f32_16x16x32_bf16 v[132:135], v[234:237], v[24:27], v[132:135]\n"
"s_waitcnt lgkmcnt(3)\n"
"ds_read_b128 v[148:151], v5 offset:55296\n"
"v_mfma_f32_16x16x32_bf16 v[152:155], v[84:87], v[140:143], 0\n"
"ds_write_addtid_b32 v137 offset:58624\n"
"v_mfma_f32_16x16x32_bf16 v[144:147], v[88:91], v[140:143], 0\n"
"ds_read_b128 v[250:253], v6 offset:5120\n"
"v_mfma_f32_16x16x32_bf16 v[156:159], v[92:95], v[140:143], 0\n"
"s_waitcnt lgkmcnt(2)\n"
"ds_read_b128 v[160:163], v4 offset:57344\n"
"v_mfma_f32_16x16x32_bf16 v[152:155], v[96:99], v[148:151], v[152:155]\n"
"v_sub_f32 v132, v132, v254\n"
"v_exp_f32 v132, v132\n"
"v_mfma_f32_16x16x32_bf16 v[144:147], v[100:103], v[148:151], v[144:147]\n"
"s_waitcnt lgkmcnt(1)\n"
"ds_read_b128 v[136:139], v6 offset:6144\n"
"v_mfma_f32_16x16x32_bf16 v[156:159], v[250:253], v[148:151], v[156:159]\n"
"s_waitcnt lgkmcnt(1)\n"
"ds_read_b128 v[140:143], v5 offset:57344\n"
"v_mfma_f32_16x16x32_bf16 v[152:155], v[104:107], v[160:163], v[152:155]\n"
"v_sub_f32 v133, v133, v254\n"
"v_exp_f32 v133, v133\n"
"v_mfma_f32_16x16x32_bf16 v[144:147], v[108:111], v[160:163], v[144:147]\n"
"ds_read_b128 v[164:167], v6 offset:7168\n"
"v_sub_f32 v134, v134, v254\n"
"v_exp_f32 v134, v134\n"
"v_sub_f32 v135, v135, v254\n"
"v_exp_f32 v135, v135\n"
"s_waitcnt lgkmcnt(2)\n"
"v_mfma_f32_16x16x32_bf16 v[156:159], v[136:139], v[160:163], v[156:159]\n"
"s_waitcnt lgkmcnt(1)\n"
"ds_read_b128 v[148:151], v4 offset:59392\n"
"v_cvt_pk_bf16_f32 v132, v132, v133\n"
"v_cvt_pk_bf16_f32 v133, v134, v135\n"
"v_mfma_f32_16x16x32_bf16 v[152:155], v[112:115], v[140:143], v[152:155]\n"
"ds_write_addtid_b32 v132 offset:58880\n"
"v_mfma_f32_16x16x32_bf16 v[144:147], v[116:119], v[140:143], v[144:147]\n"
"ds_write_addtid_b32 v133 offset:59136\n"
"s_waitcnt lgkmcnt(3)\n"
"v_mfma_f32_16x16x32_bf16 v[156:159], v[164:167], v[140:143], v[156:159]\n"
"s_waitcnt lgkmcnt(2)\n"
"ds_read_b128 v[136:139], v6 offset:8192\n"
"ds_read_b128 v[160:163], v5 offset:59392\n"
"v_mfma_f32_16x16x32_bf16 v[152:155], v[0:3], v[148:151], v[152:155]\n"
"v_mfma_f32_16x16x32_bf16 v[144:147], v[120:123], v[148:151], v[144:147]\n"
"s_waitcnt lgkmcnt(2)\n"
"s_barrier\n"
"ds_read_b128 v[132:135], v6 offset:9216\n"
"s_waitcnt lgkmcnt(2)\n"
"v_mfma_f32_16x16x32_bf16 v[156:159], v[136:139], v[148:151], v[156:159]\n"
"s_waitcnt lgkmcnt(1)\n"
"v_mfma_f32_16x16x32_bf16 v[152:155], v[124:127], v[160:163], v[152:155]\n"
"ds_read_b128 v[140:143], v4 offset:12288\n"
"v_mfma_f32_16x16x32_bf16 v[144:147], v[128:131], v[160:163], v[144:147]\n"
"s_waitcnt lgkmcnt(1)\n"
"v_mfma_f32_16x16x32_bf16 v[156:159], v[132:135], v[160:163], v[156:159]\n"
"s_waitcnt lgkmcnt(0)\n"
"v_mfma_f32_16x16x32_bf16 v[250:253], v[8:11], v[140:143], 0\n"
"ds_read_b128 v[164:167], v5 offset:12288\n"
"v_mfma_f32_16x16x32_bf16 v[136:139], v[12:15], v[140:143], 0\n"
"v_mfma_f32_16x16x32_bf16 v[148:151], v[16:19], v[140:143], 0\n"
"s_waitcnt lgkmcnt(0)\n"
"v_mfma_f32_16x16x32_bf16 v[250:253], v[20:23], v[164:167], v[250:253]\n"
"ds_read_b128 v[168:171], v4 offset:14336\n"
"v_mul_f32 v152, s60, v152\n"
"v_mfma_f32_16x16x32_bf16 v[136:139], v[28:31], v[164:167], v[136:139]\n"
"v_mul_f32 v153, s60, v153\n"
"v_cvt_pk_bf16_f32 v152, v152, v153\n"
"v_mfma_f32_16x16x32_bf16 v[148:151], v[32:35], v[164:167], v[148:151]\n"
"v_mul_f32 v154, s60, v154\n"
"v_mul_f32 v155, s60, v155\n"
"v_cvt_pk_bf16_f32 v153, v154, v155\n"
"v_mul_f32 v144, s60, v144\n"
"v_mul_f32 v145, s60, v145\n"
"v_mul_f32 v146, s60, v146\n"
"v_mul_f32 v147, s60, v147\n"
"v_cvt_pk_bf16_f32 v154, v144, v145\n"
"v_cvt_pk_bf16_f32 v155, v146, v147\n"
"v_mul_f32 v156, s60, v156\n"
"v_mul_f32 v157, s60, v157\n"
"v_mul_f32 v158, s60, v158\n"
"s_waitcnt lgkmcnt(0)\n"
"v_mfma_f32_16x16x32_bf16 v[250:253], v[36:39], v[168:171], v[250:253]\n"
"ds_read_b128 v[132:135], v5 offset:14336\n"
"v_mul_f32 v159, s60, v159\n"
"v_mfma_f32_16x16x32_bf16 v[136:139], v[40:43], v[168:171], v[136:139]\n"
"v_cvt_pk_bf16_f32 v26, v156, v157\n"
"v_cvt_pk_bf16_f32 v27, v158, v159\n"
"v_mfma_f32_16x16x32_bf16 v[148:151], v[44:47], v[168:171], v[148:151]\n"
"s_waitcnt lgkmcnt(0)\n"
"v_bfe_u32 v255, v6, 2, 6\n"
"ds_read_b32 v255, v255 offset:1792\n"
"v_mfma_f32_16x16x32_bf16 v[250:253], v[48:51], v[132:135], v[250:253]\n"
"ds_read_b128 v[140:143], v4 offset:16384\n"
"v_mfma_f32_16x16x32_bf16 v[136:139], v[52:55], v[132:135], v[136:139]\n"
"s_add_u32 s68, s50, 4294964608\n"
"v_mfma_f32_16x16x32_bf16 v[148:151], v[56:59], v[132:135], v[148:151]\n"
"s_waitcnt lgkmcnt(1)\n"
"v_add_u32_sdwa v177, v255, v7 dst_sel:DWORD dst_unused:UNUSED_PAD src0_sel:WORD_0 src1_sel:DWORD\n"
"s_waitcnt lgkmcnt(0)\n"
"v_mfma_f32_16x16x32_bf16 v[250:253], v[60:63], v[140:143], v[250:253]\n"
"s_add_u32 s69, s50, 4294963264\n"
"ds_read_b128 v[144:147], v5 offset:16384\n"
"v_max_u32 v176, s68, v177\n"
"v_mfma_f32_16x16x32_bf16 v[136:139], v[64:67], v[140:143], v[136:139]\n"
"v_mfma_f32_16x16x32_bf16 v[148:151], v[68:71], v[140:143], v[148:151]\n"
"s_waitcnt lgkmcnt(0)\n"
"v_max_u32 v175, s69, v177\n"
"v_mfma_f32_16x16x32_bf16 v[250:253], v[72:75], v[144:147], v[250:253]\n"
"ds_read_b128 v[132:135], v176 offset:2688\n"
"v_mfma_f32_16x16x32_bf16 v[136:139], v[76:79], v[144:147], v[136:139]\n"
"v_mfma_f32_16x16x32_bf16 v[148:151], v[80:83], v[144:147], v[148:151]\n"
"s_add_u32 s70, s50, 4294964304\n"
"ds_read_b128 v[140:143], v175 offset:4032\n"
"s_waitcnt lgkmcnt(1)\n"
"v_mfma_f32_16x16x32_bf16 v[132:135], v[178:181], v[152:155], v[132:135]\n"
"v_mfma_f32_16x16x32_bf16 v[132:135], v[180:183], v[24:27], v[132:135]\n"
"ds_read_b128 v[156:159], v176 offset:2704\n"
"v_cvt_pk_bf16_f32 v250, v250, v251\n"
"v_cvt_pk_bf16_f32 v251, v252, v253\n"
"v_cvt_pk_bf16_f32 v252, v136, v137\n"
"v_cvt_pk_bf16_f32 v253, v138, v139\n"
"v_cvt_pk_bf16_f32 v254, v148, v149\n"
"v_cvt_pk_bf16_f32 v255, v150, v151\n"
"s_waitcnt lgkmcnt(1)\n"
"v_mfma_f32_16x16x32_bf16 v[140:143], v[208:211], v[152:155], v[140:143]\n"
"v_max3_f32 v172, v132, v133, v134\n"
"v_max_f32 v172, v172, v135\n"
"v_mfma_f32_16x16x32_bf16 v[140:143], v[210:213], v[24:27], v[140:143]\n"
"ds_read_b128 v[136:139], v175 offset:4048\n"
"v_min_u32 v174, s70, v177\n"
"s_mov_b64 exec, s[52:53]\n"
"v_add_u32 v174, 1040, v174\n"
"s_mov_b64 exec, -1\n"
"s_waitcnt lgkmcnt(1)\n"
"v_mfma_f32_16x16x32_bf16 v[156:159], v[184:187], v[152:155], v[156:159]\n"
"v_mfma_f32_16x16x32_bf16 v[156:159], v[186:189], v[24:27], v[156:159]\n"
"s_waitcnt lgkmcnt(0)\n"
"ds_read_b128 v[132:135], v174 offset:5376\n"
"v_max3_f32 v172, v172, v140, v141\n"
"v_max3_f32 v172, v172, v142, v143\n"
"v_mfma_f32_16x16x32_bf16 v[136:139], v[214:217], v[152:155], v[136:139]\n"
"v_mfma_f32_16x16x32_bf16 v[136:139], v[216:219], v[24:27], v[136:139]\n"
"s_waitcnt lgkmcnt(0)\n"
"ds_read_b128 v[144:147], v176 offset:2720\n"
"v_max3_f32 v172, v172, v156, v157\n"
"v_max3_f32 v172, v172, v158, v159\n"
"v_mfma_f32_16x16x32_bf16 v[132:135], v[238:241], v[152:155], v[132:135]\n"
"v_mfma_f32_16x16x32_bf16 v[132:135], v[240:243], v[24:27], v[132:135]\n"
"s_waitcnt lgkmcnt(0)\n"
"ds_read_b128 v[140:143], v175 offset:4064\n"
"v_max3_f32 v172, v172, v136, v137\n"
"v_max3_f32 v172, v172, v138, v139\n"
"v_mfma_f32_16x16x32_bf16 v[144:147], v[190:193], v[152:155], v[144:147]\n"
"v_mfma_f32_16x16x32_bf16 v[144:147], v[192:195], v[24:27], v[144:147]\n"
"s_waitcnt lgkmcnt(0)\n"
"ds_read_b128 v[148:151], v174 offset:5392\n"
"v_max3_f32 v172, v172, v132, v133\n"
"v_max3_f32 v172, v172, v134, v135\n"
"v_mfma_f32_16x16x32_bf16 v[140:143], v[220:223], v[152:155], v[140:143]\n"
"v_mfma_f32_16x16x32_bf16 v[140:143], v[222:225], v[24:27], v[140:143]\n"
"s_waitcnt lgkmcnt(0)\n"
"ds_read_b128 v[136:139], v174 offset:5408\n"
"v_max3_f32 v172, v172, v144, v145\n"
"v_max3_f32 v172, v172, v146, v147\n"
"v_mfma_f32_16x16x32_bf16 v[148:151], v[244:247], v[152:155], v[148:151]\n"
"v_mfma_f32_16x16x32_bf16 v[148:151], v[246:249], v[24:27], v[148:151]\n"
"s_waitcnt lgkmcnt(0)\n"
"ds_read_b128 v[132:135], v176 offset:2688\n"
"v_max3_f32 v172, v172, v140, v141\n"
"v_max3_f32 v172, v172, v142, v143\n"
"v_mfma_f32_16x16x32_bf16 v[136:139], v[250:253], v[152:155], v[136:139]\n"
"v_mfma_f32_16x16x32_bf16 v[136:139], v[252:255], v[24:27], v[136:139]\n"
"s_waitcnt lgkmcnt(0)\n"
"ds_read_b128 v[144:147], v175 offset:4032\n"
"v_max3_f32 v172, v172, v148, v149\n"
"v_max3_f32 v172, v172, v150, v151\n"
"v_mfma_f32_16x16x32_bf16 v[132:135], v[178:181], v[152:155], v[132:135]\n"
"v_mfma_f32_16x16x32_bf16 v[132:135], v[180:183], v[24:27], v[132:135]\n"
"ds_read_b128 v[140:143], v176 offset:2704\n"
"s_nop 0\n"
"v_max3_f32 v172, v172, v136, v137\n"
"v_max3_f32 v172, v172, v138, v139\n"
"v_mov_b32 v173, v172\n"
"s_nop 1\n"
"v_permlane16_swap_b32 v172, v173\n"
"v_max_f32 v172, v172, v173\n"
"v_mov_b32 v173, v172\n"
"s_nop 1\n"
"v_permlane32_swap_b32 v172, v173\n"
"v_max_f32 v172, v172, v173\n"
"v_sub_f32 v132, v132, v172\n"
"v_exp_f32 v132, v132\n"
"v_sub_f32 v133, v133, v172\n"
"v_exp_f32 v133, v133\n"
"v_sub_f32 v134, v134, v172\n"
"v_exp_f32 v134, v134\n"
"v_sub_f32 v135, v135, v172\n"
"v_exp_f32 v135, v135\n"
"s_waitcnt lgkmcnt(1)\n"
"v_mfma_f32_16x16x32_bf16 v[144:147], v[208:211], v[152:155], v[144:147]\n"
"v_cvt_pk_bf16_f32 v132, v132, v133\n"
"v_cvt_pk_bf16_f32 v133, v134, v135\n"
"v_mfma_f32_16x16x32_bf16 v[144:147], v[210:213], v[24:27], v[144:147]\n"
"ds_read_b128 v[178:181], v175 offset:4048\n"
"ds_write_addtid_b32 v132 offset:55296\n"
"ds_write_addtid_b32 v133 offset:55552\n"
"s_waitcnt lgkmcnt(3)\n"
"v_mfma_f32_16x16x32_bf16 v[140:143], v[184:187], v[152:155], v[140:143]\n"
"v_mfma_f32_16x16x32_bf16 v[140:143], v[186:189], v[24:27], v[140:143]\n"
"ds_read_b128 v[136:139], v174 offset:5376\n"
"s_nop 0\n"
"v_sub_f32 v144, v144, v172\n"
"v_exp_f32 v144, v144\n"
"v_sub_f32 v145, v145, v172\n"
"v_exp_f32 v145, v145\n"
"v_sub_f32 v146, v146, v172\n"
"v_exp_f32 v146, v146\n"
"v_sub_f32 v147, v147, v172\n"
"v_exp_f32 v147, v147\n"
"v_sub_f32 v140, v140, v172\n"
"v_exp_f32 v140, v140\n"
"v_sub_f32 v141, v141, v172\n"
"v_exp_f32 v141, v141\n"
"v_sub_f32 v142, v142, v172\n"
"v_exp_f32 v142, v142\n"
"v_cvt_pk_bf16_f32 v144, v144, v145\n"
"v_sub_f32 v143, v143, v172\n"
"v_exp_f32 v143, v143\n"
"v_cvt_pk_bf16_f32 v145, v146, v147\n"
"v_cvt_pk_bf16_f32 v140, v140, v141\n"
"v_cvt_pk_bf16_f32 v141, v142, v143\n"
"ds_write_addtid_b32 v144 offset:56320\n"
"ds_write_addtid_b32 v145 offset:56576\n"
"s_waitcnt lgkmcnt(5)\n"
"v_mfma_f32_16x16x32_bf16 v[178:181], v[214:217], v[152:155], v[178:181]\n"
"ds_write_addtid_b32 v140 offset:55808\n"
"v_mfma_f32_16x16x32_bf16 v[178:181], v[216:219], v[24:27], v[178:181]\n"
"ds_read_b128 v[208:211], v176 offset:2720\n"
"ds_write_addtid_b32 v141 offset:56064\n"
"s_waitcnt lgkmcnt(5)\n"
"v_mfma_f32_16x16x32_bf16 v[136:139], v[238:241], v[152:155], v[136:139]\n"
"v_mfma_f32_16x16x32_bf16 v[136:139], v[240:243], v[24:27], v[136:139]\n"
"ds_read_b128 v[132:135], v175 offset:4064\n"
"s_nop 1\n"
"v_sub_f32 v178, v178, v172\n"
"v_exp_f32 v178, v178\n"
"v_sub_f32 v179, v179, v172\n"
"v_exp_f32 v179, v179\n"
"v_sub_f32 v180, v180, v172\n"
"v_exp_f32 v180, v180\n"
"v_sub_f32 v181, v181, v172\n"
"v_exp_f32 v181, v181\n"
"v_cvt_pk_bf16_f32 v178, v178, v179\n"
"v_cvt_pk_bf16_f32 v179, v180, v181\n"
"v_sub_f32 v136, v136, v172\n"
"v_exp_f32 v136, v136\n"
"v_sub_f32 v137, v137, v172\n"
"v_exp_f32 v137, v137\n"
"v_sub_f32 v138, v138, v172\n"
"v_exp_f32 v138, v138\n"
"v_sub_f32 v139, v139, v172\n"
"v_exp_f32 v139, v139\n"
"v_cvt_pk_bf16_f32 v136, v136, v137\n"
"v_cvt_pk_bf16_f32 v137, v138, v139\n"
"ds_write_addtid_b32 v178 offset:56832\n"
"ds_write_addtid_b32 v179 offset:57088\n"
"s_waitcnt lgkmcnt(4)\n"
"v_mfma_f32_16x16x32_bf16 v[208:211], v[190:193], v[152:155], v[208:211]\n"
"v_mfma_f32_16x16x32_bf16 v[208:211], v[192:195], v[24:27], v[208:211]\n"
"s_waitcnt lgkmcnt(0)\n"
"ds_read_b128 v[180:183], v174 offset:5392\n"
"s_barrier\n"
"ds_write_addtid_b32 v136 offset:57344\n"
"ds_write_addtid_b32 v137 offset:57600\n"
"v_mfma_f32_16x16x32_bf16 v[132:135], v[220:223], v[152:155], v[132:135]\n"
"v_mfma_f32_16x16x32_bf16 v[132:135], v[222:225], v[24:27], v[132:135]\n"
"s_nop 0\n"
"ds_read_b128 v[210:213], v174 offset:5408\n"
"v_sub_f32 v208, v208, v172\n"
"v_exp_f32 v208, v208\n"
"v_sub_f32 v209, v209, v172\n"
"v_exp_f32 v209, v209\n"
"s_nop 0\n"
"v_cvt_pk_bf16_f32 v208, v208, v209\n"
"ds_write_addtid_b32 v208 offset:58368\n"
"s_waitcnt lgkmcnt(4)\n"
"v_mfma_f32_16x16x32_bf16 v[180:183], v[244:247], v[152:155], v[180:183]\n"
"v_sub_f32 v132, v132, v172\n"
"v_exp_f32 v132, v132\n"
"v_sub_f32 v133, v133, v172\n"
"v_exp_f32 v133, v133\n"
"v_mfma_f32_16x16x32_bf16 v[180:183], v[246:249], v[24:27], v[180:183]\n"
"ds_read_b128 v[238:241], v4 offset:18432\n"
"v_cvt_pk_bf16_f32 v132, v132, v133\n"
"ds_write_addtid_b32 v132 offset:58880\n"
"s_waitcnt lgkmcnt(3)\n"
"v_mfma_f32_16x16x32_bf16 v[210:213], v[250:253], v[152:155], v[210:213]\n"
"v_mfma_f32_16x16x32_bf16 v[210:213], v[252:255], v[24:27], v[210:213]\n"
"s_waitcnt lgkmcnt(1)\n"
"v_mfma_f32_16x16x32_bf16 v[136:139], v[84:87], v[238:241], 0\n"
"ds_read_b128 v[140:143], v5 offset:18432\n"
"v_mfma_f32_16x16x32_bf16 v[144:147], v[88:91], v[238:241], 0\n"
"v_sub_f32 v180, v180, v172\n"
"v_exp_f32 v180, v180\n"
"v_sub_f32 v181, v181, v172\n"
"v_exp_f32 v181, v181\n"
"v_sub_f32 v182, v182, v172\n"
"v_exp_f32 v182, v182\n"
"v_mfma_f32_16x16x32_bf16 v[132:135], v[92:95], v[238:241], 0\n"
"ds_read_b128 v[148:151], v6 offset:5120\n"
"v_sub_f32 v183, v183, v172\n"
"v_exp_f32 v183, v183\n"
"v_cvt_pk_bf16_f32 v180, v180, v181\n"
"v_cvt_pk_bf16_f32 v181, v182, v183\n"
"s_waitcnt lgkmcnt(1)\n"
"v_mfma_f32_16x16x32_bf16 v[136:139], v[96:99], v[140:143], v[136:139]\n"
"ds_write_addtid_b32 v180 offset:57856\n"
"v_mfma_f32_16x16x32_bf16 v[144:147], v[100:103], v[140:143], v[144:147]\n"
"ds_read_b128 v[238:241], v4 offset:20480\n"
"v_sub_f32 v210, v210, v172\n"
"v_exp_f32 v210, v210\n"
"v_sub_f32 v211, v211, v172\n"
"v_exp_f32 v211, v211\n"
"s_waitcnt lgkmcnt(2)\n"
"v_mfma_f32_16x16x32_bf16 v[132:135], v[148:151], v[140:143], v[132:135]\n"
"ds_write_addtid_b32 v181 offset:58112\n"
"v_cvt_pk_bf16_f32 v210, v210, v211\n"
"ds_read_b128 v[152:155], v6 offset:6144\n"
"ds_write_addtid_b32 v210 offset:34304\n"
"s_waitcnt lgkmcnt(3)\n"
"v_mfma_f32_16x16x32_bf16 v[136:139], v[104:107], v[238:241], v[136:139]\n"
"ds_read_b128 v[156:159], v5 offset:20480\n"
"v_mfma_f32_16x16x32_bf16 v[144:147], v[108:111], v[238:241], v[144:147]\n"
"s_waitcnt lgkmcnt(1)\n"
"s_barrier\n"
"v_mfma_f32_16x16x32_bf16 v[132:135], v[152:155], v[238:241], v[132:135]\n"
"s_waitcnt lgkmcnt(0)\n"
"ds_read_b128 v[178:181], v6 offset:7168\n"
"v_mfma_f32_16x16x32_bf16 v[136:139], v[112:115], v[156:159], v[136:139]\n"
"ds_read_b128 v[208:211], v4 offset:22528\n"
"v_mfma_f32_16x16x32_bf16 v[144:147], v[116:119], v[156:159], v[144:147]\n"
"s_waitcnt lgkmcnt(1)\n"
"v_mfma_f32_16x16x32_bf16 v[132:135], v[178:181], v[156:159], v[132:135]\n"
"s_waitcnt lgkmcnt(0)\n"
"ds_read_b128 v[140:143], v6 offset:8192\n"
"v_mfma_f32_16x16x32_bf16 v[136:139], v[0:3], v[208:211], v[136:139]\n"
"ds_read_b128 v[238:241], v5 offset:22528\n"
"v_mfma_f32_16x16x32_bf16 v[144:147], v[120:123], v[208:211], v[144:147]\n"
"s_waitcnt lgkmcnt(1)\n"
"v_mfma_f32_16x16x32_bf16 v[132:135], v[140:143], v[208:211], v[132:135]\n"
"s_waitcnt lgkmcnt(0)\n"
"ds_read_b128 v[148:151], v6 offset:9216\n"
"ds_read_b128 v[152:155], v4 offset:36864\n"
"v_mfma_f32_16x16x32_bf16 v[136:139], v[124:127], v[238:241], v[136:139]\n"
"v_mfma_f32_16x16x32_bf16 v[144:147], v[128:131], v[238:241], v[144:147]\n"
"s_waitcnt lgkmcnt(1)\n"
"v_mfma_f32_16x16x32_bf16 v[132:135], v[148:151], v[238:241], v[132:135]\n"
"s_waitcnt lgkmcnt(0)\n"
"ds_read_b128 v[156:159], v5 offset:36864\n"
"v_mfma_f32_16x16x32_bf16 v[178:181], v[8:11], v[152:155], 0\n"
"v_mfma_f32_16x16x32_bf16 v[208:211], v[12:15], v[152:155], 0\n"
"v_mfma_f32_16x16x32_bf16 v[140:143], v[16:19], v[152:155], 0\n"
"s_waitcnt lgkmcnt(0)\n"
"ds_read_b128 v[160:163], v4 offset:38912\n"
"v_mfma_f32_16x16x32_bf16 v[178:181], v[20:23], v[156:159], v[178:181]\n"
"v_mul_f32 v136, s60, v136\n"
"v_mul_f32 v137, s60, v137\n"
"v_mfma_f32_16x16x32_bf16 v[208:211], v[28:31], v[156:159], v[208:211]\n"
"v_cvt_pk_bf16_f32 v136, v136, v137\n"
"v_mul_f32 v138, s60, v138\n"
"v_mfma_f32_16x16x32_bf16 v[140:143], v[32:35], v[156:159], v[140:143]\n"
"s_waitcnt lgkmcnt(0)\n"
"ds_read_b128 v[238:241], v5 offset:38912\n"
"v_mul_f32 v139, s60, v139\n"
"v_mfma_f32_16x16x32_bf16 v[178:181], v[36:39], v[160:163], v[178:181]\n"
"v_cvt_pk_bf16_f32 v137, v138, v139\n"
"v_mul_f32 v144, s60, v144\n"
"v_mfma_f32_16x16x32_bf16 v[208:211], v[40:43], v[160:163], v[208:211]\n"
"v_bfe_u32 v243, v6, 2, 6\n"
"v_mul_f32 v145, s60, v145\n"
"v_mfma_f32_16x16x32_bf16 v[140:143], v[44:47], v[160:163], v[140:143]\n"
"ds_read_b128 v[148:151], v4 offset:40960\n"
"v_mul_f32 v146, s60, v146\n"
"v_mul_f32 v147, s60, v147\n"
"v_cvt_pk_bf16_f32 v138, v144, v145\n"
"v_cvt_pk_bf16_f32 v139, v146, v147\n"
"v_mul_f32 v132, s60, v132\n"
"v_mul_f32 v133, s60, v133\n"
"v_mul_f32 v134, s60, v134\n"
"s_waitcnt lgkmcnt(1)\n"
"v_mfma_f32_16x16x32_bf16 v[178:181], v[48:51], v[238:241], v[178:181]\n"
"s_add_u32 s71, s50, 4294964592\n"
"ds_read_b32 v243, v243 offset:1856\n"
"v_mul_f32 v135, s60, v135\n"
"v_mfma_f32_16x16x32_bf16 v[208:211], v[52:55], v[238:241], v[208:211]\n"
"v_cvt_pk_bf16_f32 v26, v132, v133\n"
"v_cvt_pk_bf16_f32 v27, v134, v135\n"
"v_mfma_f32_16x16x32_bf16 v[140:143], v[56:59], v[238:241], v[140:143]\n"
"s_waitcnt lgkmcnt(1)\n"
"ds_read_b128 v[144:147], v5 offset:40960\n"
"v_mfma_f32_16x16x32_bf16 v[178:181], v[60:63], v[148:151], v[178:181]\n"
"v_mfma_f32_16x16x32_bf16 v[208:211], v[64:67], v[148:151], v[208:211]\n"
"s_add_u32 s72, s50, 4294965632\n"
"v_mfma_f32_16x16x32_bf16 v[140:143], v[68:71], v[148:151], v[140:143]\n"
"s_waitcnt lgkmcnt(1)\n"
"v_add_u32_sdwa v213, v243, v7 dst_sel:DWORD dst_unused:UNUSED_PAD src0_sel:WORD_0 src1_sel:DWORD\n"
"s_nop 0\n"
"v_max_u32 v212, s71, v213\n"
"ds_read_b128 v[238:241], v212 offset:2720\n"
"s_waitcnt lgkmcnt(1)\n"
"v_mfma_f32_16x16x32_bf16 v[178:181], v[72:75], v[144:147], v[178:181]\n"
"v_min_u32 v177, s72, v213\n"
"v_mfma_f32_16x16x32_bf16 v[208:211], v[76:79], v[144:147], v[208:211]\n"
"ds_read_b128 v[132:135], v212 offset:2736\n"
"v_mfma_f32_16x16x32_bf16 v[140:143], v[80:83], v[144:147], v[140:143]\n"
"s_waitcnt lgkmcnt(1)\n"
"v_mfma_f32_16x16x32_bf16 v[238:241], v[190:193], v[136:139], v[238:241]\n"
"ds_read_b128 v[148:151], v177 offset:4048\n"
"v_mfma_f32_16x16x32_bf16 v[238:241], v[192:195], v[24:27], v[238:241]\n"
"s_waitcnt lgkmcnt(1)\n"
"v_mfma_f32_16x16x32_bf16 v[132:135], v[196:199], v[136:139], v[132:135]\n"
"s_add_u32 s73, s50, 4294964288\n"
"ds_read_b128 v[152:155], v177 offset:4064\n"
"v_cvt_pk_bf16_f32 v178, v178, v179\n"
"v_cvt_pk_bf16_f32 v179, v180, v181\n"
"v_cvt_pk_bf16_f32 v180, v208, v209\n"
"v_cvt_pk_bf16_f32 v181, v210, v211\n"
"v_cvt_pk_bf16_f32 v182, v140, v141\n"
"v_cvt_pk_bf16_f32 v183, v142, v143\n"
"v_mfma_f32_16x16x32_bf16 v[132:135], v[198:201], v[24:27], v[132:135]\n"
"s_waitcnt lgkmcnt(1)\n"
"v_max3_f32 v242, v238, v239, v240\n"
"v_max_f32 v242, v242, v241\n"
"v_mfma_f32_16x16x32_bf16 v[148:151], v[214:217], v[136:139], v[148:151]\n"
"ds_read_b128 v[208:211], v177 offset:4080\n"
"v_min_u32 v176, s73, v213\n"
"v_mfma_f32_16x16x32_bf16 v[148:151], v[216:219], v[24:27], v[148:151]\n"
"s_mov_b64 exec, s[52:53]\n"
"v_add_u32 v176, 1040, v176\n"
"s_mov_b64 exec, -1\n"
"v_max3_f32 v242, v242, v132, v133\n"
"v_max3_f32 v242, v242, v134, v135\n"
"s_waitcnt lgkmcnt(1)\n"
"v_mfma_f32_16x16x32_bf16 v[152:155], v[220:223], v[136:139], v[152:155]\n"
"ds_read_b128 v[238:241], v212 offset:2704\n"
"v_mfma_f32_16x16x32_bf16 v[152:155], v[222:225], v[24:27], v[152:155]\n"
"s_waitcnt lgkmcnt(1)\n"
"v_max3_f32 v242, v242, v148, v149\n"
"v_max3_f32 v242, v242, v150, v151\n"
"v_mfma_f32_16x16x32_bf16 v[208:211], v[226:229], v[136:139], v[208:211]\n"
"ds_read_b128 v[132:135], v176 offset:5392\n"
"v_mfma_f32_16x16x32_bf16 v[208:211], v[228:231], v[24:27], v[208:211]\n"
"s_waitcnt lgkmcnt(1)\n"
"s_nop 0\n"
"v_max3_f32 v242, v242, v152, v153\n"
"v_max3_f32 v242, v242, v154, v155\n"
"v_mfma_f32_16x16x32_bf16 v[238:241], v[184:187], v[136:139], v[238:241]\n"
"ds_read_b128 v[140:143], v176 offset:5408\n"
"v_mfma_f32_16x16x32_bf16 v[238:241], v[186:189], v[24:27], v[238:241]\n"
"s_waitcnt lgkmcnt(1)\n"
"v_max3_f32 v242, v242, v208, v209\n"
"v_max3_f32 v242, v242, v210, v211\n"
"v_mfma_f32_16x16x32_bf16 v[132:135], v[244:247], v[136:139], v[132:135]\n"
"ds_read_b128 v[144:147], v176 offset:5424\n"
"v_mfma_f32_16x16x32_bf16 v[132:135], v[246:249], v[24:27], v[132:135]\n"
"s_waitcnt lgkmcnt(1)\n"
"s_nop 0\n"
"v_max3_f32 v242, v242, v238, v239\n"
"v_max3_f32 v242, v242, v240, v241\n"
"v_mfma_f32_16x16x32_bf16 v[140:143], v[250:253], v[136:139], v[140:143]\n"
"ds_read_b128 v[208:211], v212 offset:2720\n"
"v_mfma_f32_16x16x32_bf16 v[140:143], v[252:255], v[24:27], v[140:143]\n"
"s_waitcnt lgkmcnt(1)\n"
"v_max3_f32 v242, v242, v132, v133\n"
"v_max3_f32 v242, v242, v134, v135\n"
"v_mfma_f32_16x16x32_bf16 v[144:147], v[178:181], v[136:139], v[144:147]\n"
"ds_read_b128 v[238:241], v212 offset:2736\n"
"v_mfma_f32_16x16x32_bf16 v[144:147], v[180:183], v[24:27], v[144:147]\n"
"s_waitcnt lgkmcnt(1)\n"
"s_nop 0\n"
"v_max3_f32 v242, v242, v140, v141\n"
"v_max3_f32 v242, v242, v142, v143\n"
"v_mfma_f32_16x16x32_bf16 v[208:211], v[190:193], v[136:139], v[208:211]\n"
"ds_read_b128 v[132:135], v177 offset:4048\n"
"v_mfma_f32_16x16x32_bf16 v[208:211], v[192:195], v[24:27], v[208:211]\n"
"s_nop 0\n"
"v_max3_f32 v242, v242, v144, v145\n"
"v_max3_f32 v242, v242, v146, v147\n"
"v_mov_b32 v243, v242\n"
"s_nop 1\n"
"v_permlane16_swap_b32 v242, v243\n"
"v_max_f32 v242, v242, v243\n"
"v_mov_b32 v243, v242\n"
"s_nop 1\n"
"v_permlane32_swap_b32 v242, v243\n"
"v_max_f32 v242, v242, v243\n"
"v_sub_f32 v208, v208, v242\n"
"v_exp_f32 v208, v208\n"
"v_sub_f32 v209, v209, v242\n"
"v_exp_f32 v209, v209\n"
"v_sub_f32 v210, v210, v242\n"
"v_exp_f32 v210, v210\n"
"v_sub_f32 v211, v211, v242\n"
"v_exp_f32 v211, v211\n"
"s_waitcnt lgkmcnt(1)\n"
"v_mfma_f32_16x16x32_bf16 v[238:241], v[196:199], v[136:139], v[238:241]\n"
"ds_read_b128 v[140:143], v177 offset:4064\n"
"v_cvt_pk_bf16_f32 v208, v208, v209\n"
"v_mfma_f32_16x16x32_bf16 v[238:241], v[198:201], v[24:27], v[238:241]\n"
"ds_write_addtid_b32 v208 offset:53248\n"
"v_cvt_pk_bf16_f32 v209, v210, v211\n"
"ds_write_addtid_b32 v209 offset:53504\n"
"s_waitcnt lgkmcnt(3)\n"
"v_mfma_f32_16x16x32_bf16 v[132:135], v[214:217], v[136:139], v[132:135]\n"
"ds_read_b128 v[144:147], v177 offset:4080\n"
"v_mfma_f32_16x16x32_bf16 v[132:135], v[216:219], v[24:27], v[132:135]\n"
"s_nop 0\n"
"v_sub_f32 v238, v238, v242\n"
"v_exp_f32 v238, v238\n"
"v_sub_f32 v239, v239, v242\n"
"v_exp_f32 v239, v239\n"
"v_sub_f32 v240, v240, v242\n"
"v_exp_f32 v240, v240\n"
"v_sub_f32 v241, v241, v242\n"
"v_exp_f32 v241, v241\n"
"v_sub_f32 v132, v132, v242\n"
"v_exp_f32 v132, v132\n"
"v_sub_f32 v133, v133, v242\n"
"v_exp_f32 v133, v133\n"
"v_sub_f32 v134, v134, v242\n"
"v_exp_f32 v134, v134\n"
"v_sub_f32 v135, v135, v242\n"
"v_exp_f32 v135, v135\n"
"v_cvt_pk_bf16_f32 v238, v238, v239\n"
"v_cvt_pk_bf16_f32 v239, v240, v241\n"
"v_cvt_pk_bf16_f32 v132, v132, v133\n"
"v_cvt_pk_bf16_f32 v133, v134, v135\n"
"ds_write_addtid_b32 v238 offset:53760\n"
"ds_write_addtid_b32 v239 offset:54016\n"
"s_waitcnt lgkmcnt(5)\n"
"v_mfma_f32_16x16x32_bf16 v[140:143], v[220:223], v[136:139], v[140:143]\n"
"ds_read_b128 v[208:211], v212 offset:2704\n"
"v_mfma_f32_16x16x32_bf16 v[140:143], v[222:225], v[24:27], v[140:143]\n"
"ds_write_addtid_b32 v132 offset:54272\n"
"ds_write_addtid_b32 v133 offset:54528\n"
"s_waitcnt lgkmcnt(5)\n"
"v_mfma_f32_16x16x32_bf16 v[144:147], v[226:229], v[136:139], v[144:147]\n"
"ds_read_b128 v[238:241], v176 offset:5392\n"
"v_mfma_f32_16x16x32_bf16 v[144:147], v[228:231], v[24:27], v[144:147]\n"
"s_nop 1\n"
"v_sub_f32 v140, v140, v242\n"
"v_exp_f32 v140, v140\n"
"v_sub_f32 v141, v141, v242\n"
"v_exp_f32 v141, v141\n"
"v_sub_f32 v142, v142, v242\n"
"v_exp_f32 v142, v142\n"
"v_sub_f32 v143, v143, v242\n"
"v_exp_f32 v143, v143\n"
"v_sub_f32 v144, v144, v242\n"
"v_exp_f32 v144, v144\n"
"v_sub_f32 v145, v145, v242\n"
"v_exp_f32 v145, v145\n"
"v_sub_f32 v146, v146, v242\n"
"v_exp_f32 v146, v146\n"
"v_cvt_pk_bf16_f32 v140, v140, v141\n"
"v_sub_f32 v147, v147, v242\n"
"v_exp_f32 v147, v147\n"
"v_cvt_pk_bf16_f32 v141, v142, v143\n"
"v_cvt_pk_bf16_f32 v144, v144, v145\n"
"v_cvt_pk_bf16_f32 v145, v146, v147\n"
"ds_write_addtid_b32 v140 offset:54784\n"
"ds_write_addtid_b32 v141 offset:55040\n"
"s_waitcnt lgkmcnt(5)\n"
"v_mfma_f32_16x16x32_bf16 v[208:211], v[184:187], v[136:139], v[208:211]\n"
"ds_write_addtid_b32 v144 offset:33792\n"
"v_mfma_f32_16x16x32_bf16 v[208:211], v[186:189], v[24:27], v[208:211]\n"
"ds_write_addtid_b32 v145 offset:34048\n"
"ds_read_b128 v[132:135], v176 offset:5408\n"
"s_waitcnt lgkmcnt(5)\n"
"v_mfma_f32_16x16x32_bf16 v[238:241], v[244:247], v[136:139], v[238:241]\n"
"ds_read_b128 v[140:143], v176 offset:5424\n"
"v_mfma_f32_16x16x32_bf16 v[238:241], v[246:249], v[24:27], v[238:241]\n"
"s_waitcnt lgkmcnt(2)\n"
"s_barrier\n"
"v_sub_f32 v208, v208, v242\n"
"v_exp_f32 v208, v208\n"
"v_sub_f32 v209, v209, v242\n"
"v_exp_f32 v209, v209\n"
"v_sub_f32 v210, v210, v242\n"
"v_exp_f32 v210, v210\n"
"v_sub_f32 v211, v211, v242\n"
"v_exp_f32 v211, v211\n"
"v_sub_f32 v238, v238, v242\n"
"v_exp_f32 v238, v238\n"
"v_cvt_pk_bf16_f32 v208, v208, v209\n"
"v_sub_f32 v239, v239, v242\n"
"v_exp_f32 v239, v239\n"
"v_cvt_pk_bf16_f32 v209, v210, v211\n"
"v_sub_f32 v240, v240, v242\n"
"v_exp_f32 v240, v240\n"
"v_sub_f32 v241, v241, v242\n"
"v_exp_f32 v241, v241\n"
"v_cvt_pk_bf16_f32 v238, v238, v239\n"
"v_cvt_pk_bf16_f32 v239, v240, v241\n"
"ds_write_addtid_b32 v208 offset:57856\n"
"ds_write_addtid_b32 v209 offset:58112\n"
"s_waitcnt lgkmcnt(3)\n"
"v_mfma_f32_16x16x32_bf16 v[132:135], v[250:253], v[136:139], v[132:135]\n"
"ds_read_b128 v[184:187], v4 offset:43008\n"
"v_mfma_f32_16x16x32_bf16 v[132:135], v[252:255], v[24:27], v[132:135]\n"
"ds_write_addtid_b32 v238 offset:58368\n"
"ds_write_addtid_b32 v239 offset:58624\n"
"s_waitcnt lgkmcnt(5)\n"
"v_mfma_f32_16x16x32_bf16 v[140:143], v[178:181], v[136:139], v[140:143]\n"
"v_mfma_f32_16x16x32_bf16 v[140:143], v[180:183], v[24:27], v[140:143]\n"
"s_waitcnt lgkmcnt(2)\n"
"ds_read_b128 v[208:211], v5 offset:43008\n"
"v_mfma_f32_16x16x32_bf16 v[144:147], v[84:87], v[184:187], 0\n"
"v_mfma_f32_16x16x32_bf16 v[148:151], v[88:91], v[184:187], 0\n"
"ds_read_b128 v[152:155], v6 offset:5120\n"
"v_sub_f32 v132, v132, v242\n"
"v_exp_f32 v132, v132\n"
"v_sub_f32 v133, v133, v242\n"
"v_exp_f32 v133, v133\n"
"v_sub_f32 v134, v134, v242\n"
"v_exp_f32 v134, v134\n"
"v_mfma_f32_16x16x32_bf16 v[238:241], v[92:95], v[184:187], 0\n"
"ds_read_b128 v[136:139], v4 offset:45056\n"
"v_cvt_pk_bf16_f32 v132, v132, v133\n"
"v_sub_f32 v135, v135, v242\n"
"v_exp_f32 v135, v135\n"
"s_waitcnt lgkmcnt(2)\n"
"v_mfma_f32_16x16x32_bf16 v[144:147], v[96:99], v[208:211], v[144:147]\n"
"ds_write_addtid_b32 v132 offset:58880\n"
"v_cvt_pk_bf16_f32 v133, v134, v135\n"
"v_mfma_f32_16x16x32_bf16 v[148:151], v[100:103], v[208:211], v[148:151]\n"
"ds_write_addtid_b32 v133 offset:59136\n"
"s_waitcnt lgkmcnt(3)\n"
"v_mfma_f32_16x16x32_bf16 v[238:241], v[152:155], v[208:211], v[238:241]\n"
"s_waitcnt lgkmcnt(2)\n"
"ds_read_b128 v[184:187], v6 offset:6144\n"
"v_sub_f32 v140, v140, v242\n"
"v_exp_f32 v140, v140\n"
"v_mfma_f32_16x16x32_bf16 v[144:147], v[104:107], v[136:139], v[144:147]\n"
"ds_read_b128 v[156:159], v5 offset:45056\n"
"v_sub_f32 v141, v141, v242\n"
"v_exp_f32 v141, v141\n"
"v_mfma_f32_16x16x32_bf16 v[148:151], v[108:111], v[136:139], v[148:151]\n"
"ds_read_b128 v[208:211], v6 offset:7168\n"
"v_sub_f32 v142, v142, v242\n"
"v_exp_f32 v142, v142\n"
"v_sub_f32 v143, v143, v242\n"
"v_exp_f32 v143, v143\n"
"s_waitcnt lgkmcnt(2)\n"
"v_mfma_f32_16x16x32_bf16 v[238:241], v[184:187], v[136:139], v[238:241]\n"
"s_waitcnt lgkmcnt(1)\n"
"ds_read_b128 v[132:135], v4 offset:47104\n"
"v_cvt_pk_bf16_f32 v140, v140, v141\n"
"v_cvt_pk_bf16_f32 v141, v142, v143\n"
"v_mfma_f32_16x16x32_bf16 v[144:147], v[112:115], v[156:159], v[144:147]\n"
"ds_write_addtid_b32 v140 offset:57344\n"
"v_mfma_f32_16x16x32_bf16 v[148:151], v[116:119], v[156:159], v[148:151]\n"
"ds_write_addtid_b32 v141 offset:57600\n"
"s_waitcnt lgkmcnt(3)\n"
"v_mfma_f32_16x16x32_bf16 v[238:241], v[208:211], v[156:159], v[238:241]\n"
"s_waitcnt lgkmcnt(2)\n"
"ds_read_b128 v[136:139], v6 offset:8192\n"
"ds_read_b128 v[152:155], v5 offset:47104\n"
"v_mfma_f32_16x16x32_bf16 v[144:147], v[0:3], v[132:135], v[144:147]\n"
"v_mfma_f32_16x16x32_bf16 v[148:151], v[120:123], v[132:135], v[148:151]\n"
"s_waitcnt lgkmcnt(2)\n"
"ds_read_b128 v[160:163], v6 offset:9216\n"
"s_barrier\n"
"s_waitcnt lgkmcnt(2)\n"
"v_mfma_f32_16x16x32_bf16 v[238:241], v[136:139], v[132:135], v[238:241]\n"
"s_waitcnt lgkmcnt(1)\n"
"ds_read_b128 v[208:211], v4 offset:24576\n"
"v_mfma_f32_16x16x32_bf16 v[144:147], v[124:127], v[152:155], v[144:147]\n"
"v_mfma_f32_16x16x32_bf16 v[148:151], v[128:131], v[152:155], v[148:151]\n"
"s_waitcnt lgkmcnt(1)\n"
"v_mfma_f32_16x16x32_bf16 v[238:241], v[160:163], v[152:155], v[238:241]\n"
"s_waitcnt lgkmcnt(0)\n"
"ds_read_b128 v[140:143], v5 offset:24576\n"
"v_mfma_f32_16x16x32_bf16 v[184:187], v[8:11], v[208:211], 0\n"
"v_mfma_f32_16x16x32_bf16 v[132:135], v[12:15], v[208:211], 0\n"
"v_mfma_f32_16x16x32_bf16 v[136:139], v[16:19], v[208:211], 0\n"
"s_waitcnt lgkmcnt(0)\n"
"ds_read_b128 v[156:159], v4 offset:26624\n"
"v_mfma_f32_16x16x32_bf16 v[184:187], v[20:23], v[140:143], v[184:187]\n"
"v_mul_f32 v144, s60, v144\n"
"v_mul_f32 v145, s60, v145\n"
"v_mfma_f32_16x16x32_bf16 v[132:135], v[28:31], v[140:143], v[132:135]\n"
"v_cvt_pk_bf16_f32 v144, v144, v145\n"
"v_mul_f32 v146, s60, v146\n"
"v_mfma_f32_16x16x32_bf16 v[136:139], v[32:35], v[140:143], v[136:139]\n"
"ds_read_b128 v[208:211], v5 offset:26624\n"
"v_mul_f32 v147, s60, v147\n"
"v_cvt_pk_bf16_f32 v145, v146, v147\n"
"v_mul_f32 v148, s60, v148\n"
"v_mul_f32 v149, s60, v149\n"
"v_mul_f32 v150, s60, v150\n"
"v_mul_f32 v151, s60, v151\n"
"v_cvt_pk_bf16_f32 v146, v148, v149\n"
"s_waitcnt lgkmcnt(1)\n"
"v_mfma_f32_16x16x32_bf16 v[184:187], v[36:39], v[156:159], v[184:187]\n"
"v_cvt_pk_bf16_f32 v147, v150, v151\n"
"v_mul_f32 v238, s60, v238\n"
"v_mfma_f32_16x16x32_bf16 v[132:135], v[40:43], v[156:159], v[132:135]\n"
"v_bfe_u32 v213, v6, 2, 6\n"
"v_mul_f32 v239, s60, v239\n"
"v_mfma_f32_16x16x32_bf16 v[136:139], v[44:47], v[156:159], v[136:139]\n"
"s_waitcnt lgkmcnt(0)\n"
"ds_read_b128 v[8:11], v4 offset:28672\n"
"v_mul_f32 v240, s60, v240\n"
"v_mfma_f32_16x16x32_bf16 v[184:187], v[48:51], v[208:211], v[184:187]\n"
"s_add_u32 s68, s50, 4294964576\n"
"ds_read_b32 v213, v213 offset:1920\n"
"v_mul_f32 v241, s60, v241\n"
"v_mfma_f32_16x16x32_bf16 v[132:135], v[52:55], v[208:211], v[132:135]\n"
"v_cvt_pk_bf16_f32 v26, v238, v239\n"
"v_cvt_pk_bf16_f32 v27, v240, v241\n"
"v_mfma_f32_16x16x32_bf16 v[136:139], v[56:59], v[208:211], v[136:139]\n"
"s_waitcnt lgkmcnt(1)\n"
"ds_read_b128 v[12:15], v5 offset:28672\n"
"v_mfma_f32_16x16x32_bf16 v[184:187], v[60:63], v[8:11], v[184:187]\n"
"v_mfma_f32_16x16x32_bf16 v[132:135], v[64:67], v[8:11], v[132:135]\n"
"s_waitcnt lgkmcnt(1)\n"
"v_add_u32_sdwa v212, v213, v7 dst_sel:DWORD dst_unused:UNUSED_PAD src0_sel:WORD_0 src1_sel:DWORD\n"
"s_nop 0\n"
"v_max_u32 v239, s68, v212\n"
"v_mfma_f32_16x16x32_bf16 v[136:139], v[68:71], v[8:11], v[136:139]\n"
"s_waitcnt lgkmcnt(0)\n"
"ds_read_b128 v[208:211], v239 offset:2720\n"
"v_add_u32_sdwa v213, sext(v213), v212 dst_sel:DWORD dst_unused:UNUSED_PAD src0_sel:WORD_1 src1_sel:DWORD\n"
"v_mfma_f32_16x16x32_bf16 v[184:187], v[72:75], v[12:15], v[184:187]\n"
"s_add_u32 s69, s50, 4294963232\n"
"v_max_u32 v238, s68, v213\n"
"v_mfma_f32_16x16x32_bf16 v[132:135], v[76:79], v[12:15], v[132:135]\n"
"s_add_u32 s70, s50, 4294964272\n"
"ds_read_b128 v[240:243], v239 offset:2736\n"
"v_mfma_f32_16x16x32_bf16 v[136:139], v[80:83], v[12:15], v[136:139]\n"
"s_waitcnt lgkmcnt(1)\n"
"v_max_u32 v177, s69, v213\n"
"v_mfma_f32_16x16x32_bf16 v[208:211], v[190:193], v[144:147], v[208:211]\n"
"ds_read_b128 v[8:11], v238 offset:2752\n"
"v_cvt_pk_bf16_f32 v184, v184, v185\n"
"v_cvt_pk_bf16_f32 v185, v186, v187\n"
"v_cvt_pk_bf16_f32 v186, v132, v133\n"
"v_cvt_pk_bf16_f32 v187, v134, v135\n"
"v_mfma_f32_16x16x32_bf16 v[208:211], v[192:195], v[24:27], v[208:211]\n"
"s_waitcnt lgkmcnt(1)\n"
"v_cvt_pk_bf16_f32 v188, v136, v137\n"
"v_cvt_pk_bf16_f32 v189, v138, v139\n"
"v_mfma_f32_16x16x32_bf16 v[240:243], v[196:199], v[144:147], v[240:243]\n"
"ds_read_b128 v[12:15], v177 offset:4096\n"
"v_min_u32 v176, s70, v212\n"
"v_mfma_f32_16x16x32_bf16 v[240:243], v[198:201], v[24:27], v[240:243]\n"
"v_min_u32 v175, s70, v213\n"
"s_mov_b64 exec, s[52:53]\n"
"v_add_u32 v176, 1040, v176\n"
"v_add_u32 v175, 1040, v175\n"
"s_mov_b64 exec, -1\n"
"v_max3_f32 v172, v208, v209, v210\n"
"v_max_f32 v172, v172, v211\n"
"s_waitcnt lgkmcnt(1)\n"
"v_mfma_f32_16x16x32_bf16 v[8:11], v[202:205], v[144:147], v[8:11]\n"
"ds_read_b128 v[16:19], v176 offset:5408\n"
"v_max_u32 v174, s69, v212\n"
"v_mfma_f32_16x16x32_bf16 v[8:11], v[204:207], v[24:27], v[8:11]\n"
"s_waitcnt lgkmcnt(1)\n"
"v_max3_f32 v172, v172, v240, v241\n"
"v_max3_f32 v172, v172, v242, v243\n"
"v_mfma_f32_16x16x32_bf16 v[12:15], v[232:235], v[144:147], v[12:15]\n"
"ds_read_b128 v[208:211], v174 offset:4064\n"
"v_mfma_f32_16x16x32_bf16 v[12:15], v[234:237], v[24:27], v[12:15]\n"
"s_waitcnt lgkmcnt(1)\n"
"s_nop 0\n"
"v_max3_f32 v172, v172, v8, v9\n"
"v_max3_f32 v172, v172, v10, v11\n"
"v_mfma_f32_16x16x32_bf16 v[16:19], v[250:253], v[144:147], v[16:19]\n"
"ds_read_b128 v[240:243], v174 offset:4080\n"
"v_mfma_f32_16x16x32_bf16 v[16:19], v[252:255], v[24:27], v[16:19]\n"
"s_waitcnt lgkmcnt(1)\n"
"v_max3_f32 v172, v172, v12, v13\n"
"v_max3_f32 v172, v172, v14, v15\n"
"v_mfma_f32_16x16x32_bf16 v[208:211], v[220:223], v[144:147], v[208:211]\n"
"ds_read_b128 v[8:11], v176 offset:5424\n"
"v_mfma_f32_16x16x32_bf16 v[208:211], v[222:225], v[24:27], v[208:211]\n"
"s_waitcnt lgkmcnt(1)\n"
"s_nop 0\n"
"v_max3_f32 v172, v172, v16, v17\n"
"v_max3_f32 v172, v172, v18, v19\n"
"v_mfma_f32_16x16x32_bf16 v[240:243], v[226:229], v[144:147], v[240:243]\n"
"ds_read_b128 v[12:15], v175 offset:5440\n"
"v_mfma_f32_16x16x32_bf16 v[240:243], v[228:231], v[24:27], v[240:243]\n"
"s_waitcnt lgkmcnt(1)\n"
"v_max3_f32 v172, v172, v208, v209\n"
"v_max3_f32 v172, v172, v210, v211\n"
"v_mfma_f32_16x16x32_bf16 v[8:11], v[178:181], v[144:147], v[8:11]\n"
"ds_read_b128 v[16:19], v239 offset:2720\n"
"v_mfma_f32_16x16x32_bf16 v[8:11], v[180:183], v[24:27], v[8:11]\n"
"s_waitcnt lgkmcnt(1)\n"
"s_nop 0\n"
"v_max3_f32 v172, v172, v240, v241\n"
"v_max3_f32 v172, v172, v242, v243\n"
"v_mfma_f32_16x16x32_bf16 v[12:15], v[184:187], v[144:147], v[12:15]\n"
"ds_read_b128 v[208:211], v239 offset:2736\n"
"v_mfma_f32_16x16x32_bf16 v[12:15], v[186:189], v[24:27], v[12:15]\n"
"s_waitcnt lgkmcnt(1)\n"
"v_max3_f32 v172, v172, v8, v9\n"
"v_max3_f32 v172, v172, v10, v11\n"
"v_mfma_f32_16x16x32_bf16 v[16:19], v[190:193], v[144:147], v[16:19]\n"
"ds_read_b128 v[240:243], v238 offset:2752\n"
"v_mfma_f32_16x16x32_bf16 v[16:19], v[192:195], v[24:27], v[16:19]\n"
"s_nop 1\n"
"v_max3_f32 v172, v172, v12, v13\n"
"v_max3_f32 v172, v172, v14, v15\n"
"v_mov_b32 v173, v172\n"
"s_nop 1\n"
"v_permlane16_swap_b32 v172, v173\n"
"v_max_f32 v172, v172, v173\n"
"v_mov_b32 v173, v172\n"
"s_nop 1\n"
"v_permlane32_swap_b32 v172, v173\n"
"v_max_f32 v172, v172, v173\n"
"v_sub_f32 v18, v18, v172\n"
"v_exp_f32 v18, v18\n"
"v_sub_f32 v19, v19, v172\n"
"v_exp_f32 v19, v19\n"
"s_waitcnt lgkmcnt(1)\n"
"v_mfma_f32_16x16x32_bf16 v[208:211], v[196:199], v[144:147], v[208:211]\n"
"ds_read_b128 v[190:193], v177 offset:4096\n"
"v_cvt_pk_bf16_f32 v17, v18, v19\n"
"v_mfma_f32_16x16x32_bf16 v[208:211], v[198:201], v[24:27], v[208:211]\n"
"ds_write_addtid_b32 v17 offset:55552\n"
"s_waitcnt lgkmcnt(2)\n"
"v_mfma_f32_16x16x32_bf16 v[240:243], v[202:205], v[144:147], v[240:243]\n"
"ds_read_b128 v[8:11], v176 offset:5408\n"
"v_mfma_f32_16x16x32_bf16 v[240:243], v[204:207], v[24:27], v[240:243]\n"
"s_nop 2\n"
"v_sub_f32 v208, v208, v172\n"
"v_exp_f32 v208, v208\n"
"v_sub_f32 v209, v209, v172\n"
"v_exp_f32 v209, v209\n"
"v_sub_f32 v210, v210, v172\n"
"v_exp_f32 v210, v210\n"
"v_sub_f32 v211, v211, v172\n"
"v_exp_f32 v211, v211\n"
"v_sub_f32 v240, v240, v172\n"
"v_exp_f32 v240, v240\n"
"v_sub_f32 v241, v241, v172\n"
"v_exp_f32 v241, v241\n"
"v_sub_f32 v242, v242, v172\n"
"v_exp_f32 v242, v242\n"
"v_cvt_pk_bf16_f32 v208, v208, v209\n"
"v_cvt_pk_bf16_f32 v209, v210, v211\n"
"v_sub_f32 v243, v243, v172\n"
"v_exp_f32 v243, v243\n"
"v_cvt_pk_bf16_f32 v240, v240, v241\n"
"v_cvt_pk_bf16_f32 v241, v242, v243\n"
"ds_write_addtid_b32 v208 offset:55808\n"
"ds_write_addtid_b32 v209 offset:56064\n"
"s_waitcnt lgkmcnt(4)\n"
"v_mfma_f32_16x16x32_bf16 v[190:193], v[232:235], v[144:147], v[190:193]\n"
"ds_read_b128 v[194:197], v174 offset:4064\n"
"v_mfma_f32_16x16x32_bf16 v[190:193], v[234:237], v[24:27], v[190:193]\n"
"ds_write_addtid_b32 v240 offset:56320\n"
"ds_write_addtid_b32 v241 offset:56576\n"
"s_waitcnt lgkmcnt(5)\n"
"v_mfma_f32_16x16x32_bf16 v[8:11], v[250:253], v[144:147], v[8:11]\n"
"ds_read_b128 v[198:201], v174 offset:4080\n"
"v_mfma_f32_16x16x32_bf16 v[8:11], v[252:255], v[24:27], v[8:11]\n"
"s_nop 1\n"
"v_sub_f32 v190, v190, v172\n"
"v_exp_f32 v190, v190\n"
"v_sub_f32 v191, v191, v172\n"
"v_exp_f32 v191, v191\n"
"v_sub_f32 v192, v192, v172\n"
"v_exp_f32 v192, v192\n"
"v_sub_f32 v193, v193, v172\n"
"v_exp_f32 v193, v193\n"
"v_sub_f32 v10, v10, v172\n"
"v_exp_f32 v10, v10\n"
"v_cvt_pk_bf16_f32 v190, v190, v191\n"
"v_cvt_pk_bf16_f32 v191, v192, v193\n"
"v_sub_f32 v11, v11, v172\n"
"v_exp_f32 v11, v11\n"
"ds_write_addtid_b32 v190 offset:56832\n"
"v_cvt_pk_bf16_f32 v9, v10, v11\n"
"ds_write_addtid_b32 v191 offset:57088\n"
"s_waitcnt lgkmcnt(5)\n"
"v_mfma_f32_16x16x32_bf16 v[194:197], v[220:223], v[144:147], v[194:197]\n"
"ds_read_b128 v[202:205], v176 offset:5424\n"
"v_mfma_f32_16x16x32_bf16 v[194:197], v[222:225], v[24:27], v[194:197]\n"
"ds_write_addtid_b32 v9 offset:34560\n"
"s_waitcnt lgkmcnt(4)\n"
"v_mfma_f32_16x16x32_bf16 v[198:201], v[226:229], v[144:147], v[198:201]\n"
"ds_read_b128 v[206:209], v175 offset:5440\n"
"v_mfma_f32_16x16x32_bf16 v[198:201], v[228:231], v[24:27], v[198:201]\n"
"s_waitcnt lgkmcnt(1)\n"
"s_barrier\n"
"s_nop 0\n"
"v_sub_f32 v196, v196, v172\n"
"v_exp_f32 v196, v196\n"
"v_sub_f32 v197, v197, v172\n"
"v_exp_f32 v197, v197\n"
"s_nop 0\n"
"v_cvt_pk_bf16_f32 v195, v196, v197\n"
"ds_write_addtid_b32 v195 offset:58624\n"
"v_mfma_f32_16x16x32_bf16 v[202:205], v[178:181], v[144:147], v[202:205]\n"
"ds_read_b128 v[190:193], v4 offset:30720\n"
"v_sub_f32 v198, v198, v172\n"
"v_exp_f32 v198, v198\n"
"v_sub_f32 v199, v199, v172\n"
"v_exp_f32 v199, v199\n"
"v_sub_f32 v200, v200, v172\n"
"v_exp_f32 v200, v200\n"
"v_sub_f32 v201, v201, v172\n"
"v_exp_f32 v201, v201\n"
"v_mfma_f32_16x16x32_bf16 v[202:205], v[180:183], v[24:27], v[202:205]\n"
"v_cvt_pk_bf16_f32 v198, v198, v199\n"
"v_cvt_pk_bf16_f32 v199, v200, v201\n"
"ds_write_addtid_b32 v198 offset:58880\n"
"ds_write_addtid_b32 v199 offset:59136\n"
"s_waitcnt lgkmcnt(4)\n"
"v_mfma_f32_16x16x32_bf16 v[206:209], v[184:187], v[144:147], v[206:209]\n"
"v_mfma_f32_16x16x32_bf16 v[206:209], v[186:189], v[24:27], v[206:209]\n"
"s_waitcnt lgkmcnt(2)\n"
"ds_read_b128 v[194:197], v5 offset:30720\n"
"v_mfma_f32_16x16x32_bf16 v[210:213], v[84:87], v[190:193], 0\n"
"v_sub_f32 v202, v202, v172\n"
"v_exp_f32 v202, v202\n"
"v_sub_f32 v203, v203, v172\n"
"v_exp_f32 v203, v203\n"
"v_mfma_f32_16x16x32_bf16 v[184:187], v[88:91], v[190:193], 0\n"
"ds_read_b128 v[198:201], v6 offset:5120\n"
"v_cvt_pk_bf16_f32 v202, v202, v203\n"
"v_sub_f32 v204, v204, v172\n"
"v_exp_f32 v204, v204\n"
"v_mfma_f32_16x16x32_bf16 v[232:235], v[92:95], v[190:193], 0\n"
"s_waitcnt lgkmcnt(1)\n"
"ds_read_b128 v[236:239], v4 offset:32768\n"
"v_sub_f32 v205, v205, v172\n"
"v_exp_f32 v205, v205\n"
"v_mfma_f32_16x16x32_bf16 v[210:213], v[96:99], v[194:197], v[210:213]\n"
"ds_write_addtid_b32 v202 offset:57344\n"
"v_cvt_pk_bf16_f32 v203, v204, v205\n"
"v_mfma_f32_16x16x32_bf16 v[184:187], v[100:103], v[194:197], v[184:187]\n"
"ds_write_addtid_b32 v203 offset:57600\n"
"ds_read_b128 v[188:191], v6 offset:6144\n"
"s_waitcnt lgkmcnt(4)\n"
"v_mfma_f32_16x16x32_bf16 v[232:235], v[198:201], v[194:197], v[232:235]\n"
"s_waitcnt lgkmcnt(3)\n"
"ds_read_b128 v[240:243], v5 offset:32768\n"
"v_sub_f32 v206, v206, v172\n"
"v_exp_f32 v206, v206\n"
"v_mfma_f32_16x16x32_bf16 v[210:213], v[104:107], v[236:239], v[210:213]\n"
"v_sub_f32 v207, v207, v172\n"
"v_exp_f32 v207, v207\n"
"v_mfma_f32_16x16x32_bf16 v[184:187], v[108:111], v[236:239], v[184:187]\n"
"ds_read_b128 v[192:195], v6 offset:7168\n"
"v_sub_f32 v208, v208, v172\n"
"v_exp_f32 v208, v208\n"
"v_cvt_pk_bf16_f32 v206, v206, v207\n"
"s_waitcnt lgkmcnt(2)\n"
"v_mfma_f32_16x16x32_bf16 v[232:235], v[188:191], v[236:239], v[232:235]\n"
"ds_read_b128 v[196:199], v4 offset:34816\n"
"v_bfe_u32 v4, v6, 2, 6\n"
"v_sub_f32 v209, v209, v172\n"
"v_exp_f32 v209, v209\n"
"s_waitcnt lgkmcnt(2)\n"
"v_mfma_f32_16x16x32_bf16 v[210:213], v[112:115], v[240:243], v[210:213]\n"
"s_add_u32 s71, s50, 4294963248\n"
"ds_write_addtid_b32 v206 offset:57856\n"
"v_cvt_pk_bf16_f32 v207, v208, v209\n"
"v_mfma_f32_16x16x32_bf16 v[184:187], v[116:119], v[240:243], v[184:187]\n"
"ds_write_addtid_b32 v207 offset:58112\n"
"s_waitcnt lgkmcnt(3)\n"
"v_mfma_f32_16x16x32_bf16 v[232:235], v[192:195], v[240:243], v[232:235]\n"
"s_waitcnt lgkmcnt(2)\n"
"ds_read_b32 v4, v4 offset:1984\n"
"ds_read_b128 v[188:191], v6 offset:8192\n"
"ds_read_b128 v[200:203], v5 offset:34816\n"
"v_mfma_f32_16x16x32_bf16 v[210:213], v[0:3], v[196:199], v[210:213]\n"
"s_add_u32 s72, s50, 4294964288\n"
"v_mfma_f32_16x16x32_bf16 v[184:187], v[120:123], v[196:199], v[184:187]\n"
"s_waitcnt lgkmcnt(2)\n"
"ds_read_b128 v[192:195], v6 offset:9216\n"
"v_add_u32_sdwa v5, v4, v7 dst_sel:DWORD dst_unused:UNUSED_PAD src0_sel:WORD_0 src1_sel:DWORD\n"
"s_nop 0\n"
"v_max_u32 v209, s71, v5\n"
"s_waitcnt lgkmcnt(2)\n"
"v_mfma_f32_16x16x32_bf16 v[232:235], v[188:191], v[196:199], v[232:235]\n"
"s_waitcnt lgkmcnt(1)\n"
"ds_read_b128 v[204:207], v209 offset:4048\n"
"v_min_u32 v208, s72, v5\n"
"v_mfma_f32_16x16x32_bf16 v[210:213], v[124:127], v[200:203], v[210:213]\n"
"s_mov_b64 exec, s[52:53]\n"
"v_add_u32 v208, 1040, v208\n"
"s_mov_b64 exec, -1\n"
"v_mfma_f32_16x16x32_bf16 v[184:187], v[128:131], v[200:203], v[184:187]\n"
"s_waitcnt lgkmcnt(1)\n"
"s_barrier\n"
"ds_read_b128 v[188:191], v209 offset:4064\n"
"v_mfma_f32_16x16x32_bf16 v[232:235], v[192:195], v[200:203], v[232:235]\n"
"v_mul_f32 v210, s60, v210\n"
"v_mul_f32 v211, s60, v211\n"
"v_cvt_pk_bf16_f32 v210, v210, v211\n"
"v_mul_f32 v212, s60, v212\n"
"v_mul_f32 v213, s60, v213\n"
"v_cvt_pk_bf16_f32 v211, v212, v213\n"
"v_mul_f32 v184, s60, v184\n"
"v_mul_f32 v185, s60, v185\n"
"v_mul_f32 v186, s60, v186\n"
"v_mul_f32 v187, s60, v187\n"
"v_cvt_pk_bf16_f32 v212, v184, v185\n"
"v_cvt_pk_bf16_f32 v213, v186, v187\n"
"v_mul_f32 v232, s60, v232\n"
"v_mul_f32 v233, s60, v233\n"
"s_waitcnt lgkmcnt(1)\n"
"v_mfma_f32_16x16x32_bf16 v[204:207], v[214:217], v[210:213], v[204:207]\n"
"ds_read_b128 v[192:195], v208 offset:5392\n"
"v_mul_f32 v234, s60, v234\n"
"v_mul_f32 v235, s60, v235\n"
"v_cvt_pk_bf16_f32 v26, v232, v233\n"
"v_cvt_pk_bf16_f32 v27, v234, v235\n"
"s_nop 1\n"
"v_mfma_f32_16x16x32_bf16 v[204:207], v[216:219], v[24:27], v[204:207]\n"
"s_waitcnt lgkmcnt(1)\n"
"v_mfma_f32_16x16x32_bf16 v[188:191], v[220:223], v[210:213], v[188:191]\n"
"ds_read_b128 v[184:187], v209 offset:4080\n"
"v_mfma_f32_16x16x32_bf16 v[188:191], v[222:225], v[24:27], v[188:191]\n"
"s_waitcnt lgkmcnt(1)\n"
"s_nop 2\n"
"v_max3_f32 v176, v204, v205, v206\n"
"v_max_f32 v176, v176, v207\n"
"v_mfma_f32_16x16x32_bf16 v[192:195], v[244:247], v[210:213], v[192:195]\n"
"ds_read_b128 v[196:199], v208 offset:5408\n"
"v_mfma_f32_16x16x32_bf16 v[192:195], v[246:249], v[24:27], v[192:195]\n"
"s_waitcnt lgkmcnt(1)\n"
"v_max3_f32 v176, v176, v188, v189\n"
"v_max3_f32 v176, v176, v190, v191\n"
"v_mfma_f32_16x16x32_bf16 v[184:187], v[226:229], v[210:213], v[184:187]\n"
"ds_read_b128 v[200:203], v208 offset:5424\n"
"v_mfma_f32_16x16x32_bf16 v[184:187], v[228:231], v[24:27], v[184:187]\n"
"s_waitcnt lgkmcnt(1)\n"
"s_nop 0\n"
"v_max3_f32 v176, v176, v192, v193\n"
"v_max3_f32 v176, v176, v194, v195\n"
"v_mfma_f32_16x16x32_bf16 v[196:199], v[250:253], v[210:213], v[196:199]\n"
"ds_read_b128 v[188:191], v209 offset:4048\n"
"v_mfma_f32_16x16x32_bf16 v[196:199], v[252:255], v[24:27], v[196:199]\n"
"s_waitcnt lgkmcnt(1)\n"
"v_max3_f32 v176, v176, v184, v185\n"
"v_max3_f32 v176, v176, v186, v187\n"
"v_mfma_f32_16x16x32_bf16 v[200:203], v[178:181], v[210:213], v[200:203]\n"
"ds_read_b128 v[192:195], v209 offset:4064\n"
"v_mfma_f32_16x16x32_bf16 v[200:203], v[180:183], v[24:27], v[200:203]\n"
"s_waitcnt lgkmcnt(1)\n"
"s_nop 0\n"
"v_max3_f32 v176, v176, v196, v197\n"
"v_max3_f32 v176, v176, v198, v199\n"
"v_mfma_f32_16x16x32_bf16 v[188:191], v[214:217], v[210:213], v[188:191]\n"
"ds_read_b128 v[184:187], v208 offset:5392\n"
"v_mfma_f32_16x16x32_bf16 v[188:191], v[216:219], v[24:27], v[188:191]\n"
"s_nop 0\n"
"v_max3_f32 v176, v176, v200, v201\n"
"v_max3_f32 v176, v176, v202, v203\n"
"v_mov_b32 v177, v176\n"
"s_nop 1\n"
"v_permlane16_swap_b32 v176, v177\n"
"v_max_f32 v176, v176, v177\n"
"v_mov_b32 v177, v176\n"
"s_nop 1\n"
"v_permlane32_swap_b32 v176, v177\n"
"v_max_f32 v176, v176, v177\n"
"v_sub_f32 v188, v188, v176\n"
"v_exp_f32 v188, v188\n"
"v_sub_f32 v189, v189, v176\n"
"v_exp_f32 v189, v189\n"
"v_sub_f32 v190, v190, v176\n"
"v_exp_f32 v190, v190\n"
"v_sub_f32 v191, v191, v176\n"
"v_exp_f32 v191, v191\n"
"s_waitcnt lgkmcnt(1)\n"
"v_mfma_f32_16x16x32_bf16 v[192:195], v[220:223], v[210:213], v[192:195]\n"
"ds_read_b128 v[196:199], v209 offset:4080\n"
"v_cvt_pk_bf16_f32 v188, v188, v189\n"
"v_mfma_f32_16x16x32_bf16 v[192:195], v[222:225], v[24:27], v[192:195]\n"
"ds_write_addtid_b32 v188 offset:53248\n"
"v_cvt_pk_bf16_f32 v189, v190, v191\n"
"ds_write_addtid_b32 v189 offset:53504\n"
"s_waitcnt lgkmcnt(3)\n"
"v_mfma_f32_16x16x32_bf16 v[184:187], v[244:247], v[210:213], v[184:187]\n"
"ds_read_b128 v[200:203], v208 offset:5408\n"
"v_mfma_f32_16x16x32_bf16 v[184:187], v[246:249], v[24:27], v[184:187]\n"
"s_nop 0\n"
"v_sub_f32 v192, v192, v176\n"
"v_exp_f32 v192, v192\n"
"v_sub_f32 v193, v193, v176\n"
"v_exp_f32 v193, v193\n"
"v_sub_f32 v194, v194, v176\n"
"v_exp_f32 v194, v194\n"
"v_sub_f32 v195, v195, v176\n"
"v_exp_f32 v195, v195\n"
"v_sub_f32 v184, v184, v176\n"
"v_exp_f32 v184, v184\n"
"v_sub_f32 v185, v185, v176\n"
"v_exp_f32 v185, v185\n"
"v_cvt_pk_bf16_f32 v192, v192, v193\n"
"v_sub_f32 v186, v186, v176\n"
"v_exp_f32 v186, v186\n"
"v_cvt_pk_bf16_f32 v193, v194, v195\n"
"v_sub_f32 v187, v187, v176\n"
"v_exp_f32 v187, v187\n"
"v_cvt_pk_bf16_f32 v184, v184, v185\n"
"v_cvt_pk_bf16_f32 v185, v186, v187\n"
"ds_write_addtid_b32 v192 offset:53760\n"
"ds_write_addtid_b32 v193 offset:54016\n"
"s_waitcnt lgkmcnt(5)\n"
"v_mfma_f32_16x16x32_bf16 v[196:199], v[226:229], v[210:213], v[196:199]\n"
"ds_read_b128 v[188:191], v208 offset:5424\n"
"v_mfma_f32_16x16x32_bf16 v[196:199], v[228:231], v[24:27], v[196:199]\n"
"ds_write_addtid_b32 v184 offset:54272\n"
"ds_write_addtid_b32 v185 offset:54528\n"
"s_waitcnt lgkmcnt(5)\n"
"v_mfma_f32_16x16x32_bf16 v[200:203], v[250:253], v[210:213], v[200:203]\n"
"v_mfma_f32_16x16x32_bf16 v[200:203], v[252:255], v[24:27], v[200:203]\n"
"s_nop 2\n"
"v_sub_f32 v196, v196, v176\n"
"v_exp_f32 v196, v196\n"
"v_sub_f32 v197, v197, v176\n"
"v_exp_f32 v197, v197\n"
"v_sub_f32 v198, v198, v176\n"
"v_exp_f32 v198, v198\n"
"v_sub_f32 v199, v199, v176\n"
"v_exp_f32 v199, v199\n"
"v_cvt_pk_bf16_f32 v196, v196, v197\n"
"v_cvt_pk_bf16_f32 v197, v198, v199\n"
"v_sub_f32 v200, v200, v176\n"
"v_exp_f32 v200, v200\n"
"v_sub_f32 v201, v201, v176\n"
"v_exp_f32 v201, v201\n"
"v_sub_f32 v202, v202, v176\n"
"v_exp_f32 v202, v202\n"
"v_sub_f32 v203, v203, v176\n"
"v_exp_f32 v203, v203\n"
"v_cvt_pk_bf16_f32 v200, v200, v201\n"
"v_cvt_pk_bf16_f32 v201, v202, v203\n"
"ds_write_addtid_b32 v196 offset:4096\n"
"ds_write_addtid_b32 v197 offset:4352\n"
"s_waitcnt lgkmcnt(4)\n"
"v_mfma_f32_16x16x32_bf16 v[188:191], v[178:181], v[210:213], v[188:191]\n"
"ds_write_addtid_b32 v200 offset:54784\n"
"v_mfma_f32_16x16x32_bf16 v[188:191], v[180:183], v[24:27], v[188:191]\n"
"ds_write_addtid_b32 v201 offset:55040\n"
"s_nop 6\n"
"v_sub_f32 v188, v188, v176\n"
"v_exp_f32 v188, v188\n"
"v_sub_f32 v189, v189, v176\n"
"v_exp_f32 v189, v189\n"
"s_nop 0\n"
"v_cvt_pk_bf16_f32 v188, v188, v189\n"
"ds_write_addtid_b32 v188 offset:4608\n"
"v_sub_f32 v190, v190, v176\n"
"v_exp_f32 v190, v190\n"
"v_sub_f32 v191, v191, v176\n"
"v_exp_f32 v191, v191\n"
"s_nop 0\n"
"v_cvt_pk_bf16_f32 v189, v190, v191\n"
"ds_write_addtid_b32 v189 offset:4864\n"
"s_waitcnt lgkmcnt(0)\n"
"s_barrier\n"
"s_barrier\n"
"s_barrier\n"
    ,
"s_add_u32 s50, s54, 53248\n"
"s_lshr_b32 s41, s40, 1\n"
"s_sub_u32 s55, 0, s41\n"
"s_mul_i32 s51, s41, 4096\n"
"s_lshl_b32 s41, s40, 10\n"
"s_add_u32 s51, s51, s41\n"
"s_lshl_b32 s65, s40, 2\n"
"s_add_u32 s65, s65, 32192\n"
"s_andn2_b32 s41, s45, 7\n"
"s_add_u32 s56, s56, s41\n"
"s_addc_u32 s57, s57, 0\n"
"s_add_u32 s66, s66, s41\n"
"s_addc_u32 s67, s67, 0\n"
"s_mul_i32 s68, s40, 1552\n"
"s_add_u32 s68, s68, 57344\n"
"s_setprio 0\n"
"s_movk_i32 s40, 768\n"
"s_mov_b64 s[62:63], 0\n"
"s_movk_i32 s64, 0x207\n"
"s_add_u32 s58, s56, 128\n"
"s_addc_u32 s59, s57, 0\n"
"s_add_u32 s60, s56, 256\n"
"s_addc_u32 s61, s57, 0\n"
"v_subrev_u32 v179, 24576, v10\n"
"s_waitcnt vmcnt(0)\n"
"s_barrier\n"
"s_add_u32 s42, s46, 110976\n"
"s_addc_u32 s43, s47, 0\n"
"s_add_u32 m0, s50, 27648\n"
"s_nop 0\n"
"global_load_lds_dwordx4 v0, s[42:43]\n"
"s_add_u32 m0, s50, 28672\n"
"s_nop 0\n"
"global_load_lds_dwordx4 v1, s[42:43]\n"
"s_add_u32 m0, s50, 29696\n"
"s_nop 0\n"
"global_load_lds_dwordx4 v2, s[42:43]\n"
"s_add_u32 s42, s46, 147840\n"
"s_addc_u32 s43, s47, 0\n"
"s_add_u32 m0, s50, 49152\n"
"s_nop 0\n"
"global_load_lds_dwordx4 v0, s[42:43]\n"
"s_add_u32 m0, s50, 50176\n"
"s_nop 0\n"
"global_load_lds_dwordx4 v1, s[42:43]\n"
"s_add_u32 m0, s50, 51200\n"
"s_nop 0\n"
"global_load_lds_dwordx4 v2, s[42:43]\n"
"s_add_u32 s42, s46, 184704\n"
"s_addc_u32 s43, s47, 0\n"
"s_add_u32 m0, s50, 52224\n"
"s_nop 0\n"
"global_load_lds_dwordx4 v0, s[42:43]\n"
"s_add_u32 m0, s50, 53248\n"
"s_nop 0\n"
"global_load_lds_dwordx4 v1, s[42:43]\n"
"s_add_u32 m0, s50, 54272\n"
"s_nop 0\n"
"global_load_lds_dwordx4 v2, s[42:43]\n"
"s_add_u32 s42, s46, 768\n"
"s_addc_u32 s43, s47, 0\n"
"s_add_u32 m0, s50, 0\n"
"s_nop 0\n"
"global_load_lds_dwordx4 v0, s[42:43]\n"
"s_add_u32 m0, s50, 1024\n"
"s_nop 0\n"
"global_load_lds_dwordx4 v1, s[42:43]\n"
"s_add_u32 m0, s50, 2048\n"
"s_nop 0\n"
"global_load_lds_dwordx4 v2, s[42:43]\n"
"s_waitcnt vmcnt(9)\n"
"s_barrier\n"
"s_add_u32 s42, s46, 37632\n"
"s_addc_u32 s43, s47, 0\n"
"s_add_u32 m0, s50, 3072\n"
"s_nop 0\n"
"global_load_lds_dwordx4 v0, s[42:43]\n"
"s_add_u32 m0, s50, 4096\n"
"s_nop 0\n"
"global_load_lds_dwordx4 v1, s[42:43]\n"
"s_add_u32 m0, s50, 5120\n"
"s_nop 0\n"
"global_load_lds_dwordx4 v2, s[42:43]\n"
"s_add_u32 s42, s46, 74496\n"
"s_addc_u32 s43, s47, 0\n"
"s_add_u32 m0, s50, 4294918144\n"
"s_nop 0\n"
"global_load_lds_dwordx4 v0, s[42:43]\n"
"s_add_u32 m0, s50, 4294919168\n"
"s_nop 0\n"
"global_load_lds_dwordx4 v1, s[42:43]\n"
"s_add_u32 m0, s50, 4294920192\n"
"s_nop 0\n"
"global_load_lds_dwordx4 v2, s[42:43]\n"
"s_waitcnt vmcnt(9)\n"
"s_barrier\n"
"s_add_u32 s42, s46, 111360\n"
"s_addc_u32 s43, s47, 0\n"
"s_add_u32 m0, s50, 4294921216\n"
"s_nop 0\n"
"global_load_lds_dwordx4 v0, s[42:43]\n"
"s_add_u32 m0, s50, 4294922240\n"
"s_nop 0\n"
"global_load_lds_dwordx4 v1, s[42:43]\n"
"s_add_u32 m0, s50, 4294923264\n"
"s_nop 0\n"
"global_load_lds_dwordx4 v2, s[42:43]\n"
"s_barrier\n"
"s_add_u32 s42, s46, 148224\n"
"s_addc_u32 s43, s47, 0\n"
"s_add_u32 m0, s50, 24576\n"
"s_nop 0\n"
"global_load_lds_dwordx4 v0, s[42:43]\n"
"s_add_u32 m0, s50, 25600\n"
"s_nop 0\n"
"global_load_lds_dwordx4 v1, s[42:43]\n"
"s_add_u32 m0, s50, 26624\n"
"s_nop 0\n"
"global_load_lds_dwordx4 v2, s[42:43]\n"
"s_add_u32 s42, s46, 185088\n"
"s_addc_u32 s43, s47, 0\n"
"s_add_u32 m0, s50, 27648\n"
"s_nop 0\n"
"global_load_lds_dwordx4 v0, s[42:43]\n"
"s_add_u32 m0, s50, 28672\n"
"s_nop 0\n"
"global_load_lds_dwordx4 v1, s[42:43]\n"
"s_add_u32 m0, s50, 29696\n"
"s_nop 0\n"
"global_load_lds_dwordx4 v2, s[42:43]\n"
"s_mov_b32 m0, 3584\n"
"s_bfm_b64 exec, 6, 0\n"
"global_load_lds_dword v22, s[46:47]\n"
"s_mov_b64 exec, 31\n"
"global_load_lds_dword v23, s[48:49]\n"
"s_mov_b64 exec, -1\n"
"s_waitcnt vmcnt(17)\n"
"ds_read_b64_tr_b16 v[26:27], v10 offset:29504\n"
"s_andn2_b32 s41, 576, s55\n"
"v_add_u32 v178, s41, v15\n"
"ds_read_u16 v178, v178 offset:224\n"
"ds_read_b64_tr_b16 v[28:29], v10 offset:28672\n"
"ds_read_b64_tr_b16 v[30:31], v10 offset:29440\n"
"ds_read_b128 v[180:183], v12 offset:24576\n"
"ds_read_b64_tr_b16 v[32:33], v10 offset:28704\n"
"ds_read_b64_tr_b16 v[34:35], v10 offset:29472\n"
"ds_read_b64_tr_b16 v[24:25], v10 offset:28736\n"
"ds_read_b128 v[184:187], v12 offset:30720\n"
"ds_read_b128 v[188:191], v12 offset:36864\n"
"ds_read_u16 v23, v21 offset:1152\n"
"s_waitcnt lgkmcnt(9)\n"
"v_mad_u32_u24 v178, v178, s40, v16\n"
"s_add_u32 m0, s51, 102400\n"
"s_nop 0\n"
"global_load_lds_dwordx4 v178, s[56:57]\n"
"s_add_u32 m0, s51, 104448\n"
"s_nop 0\n"
"global_load_lds_dwordx4 v178, s[58:59]\n"
"s_add_u32 m0, s51, 106496\n"
"s_nop 0\n"
"global_load_lds_dwordx4 v178, s[60:61]\n"
"s_waitcnt vmcnt(17)\n"
"ds_read_b64_tr_b16 v[38:39], v10 offset:32576\n"
"s_and_b32 s41, 32, s55\n"
"v_add_u32 v178, s41, v15\n"
"ds_read_u16 v178, v178 offset:352\n"
"s_waitcnt lgkmcnt(2)\n"
"v_mad_u32_u24 v22, v23, s40, v18\n"
"v_mad_u32_u24 v23, v23, s40, v19\n"
"ds_read_b64_tr_b16 v[40:41], v10 offset:31744\n"
"ds_read_b64_tr_b16 v[42:43], v10 offset:32512\n"
"ds_read_b64_tr_b16 v[44:45], v10 offset:31776\n"
"ds_read_b64_tr_b16 v[46:47], v10 offset:32544\n"
"ds_read_b64_tr_b16 v[36:37], v10 offset:31808\n"
"ds_read_u16 v177, v21 offset:1184\n"
"s_waitcnt lgkmcnt(6)\n"
"v_mad_u32_u24 v178, v178, s40, v16\n"
"s_add_u32 m0, s51, 114688\n"
"s_nop 0\n"
"global_load_lds_dwordx4 v178, s[56:57]\n"
"s_add_u32 m0, s51, 116736\n"
"s_nop 0\n"
"global_load_lds_dwordx4 v178, s[58:59]\n"
"s_add_u32 m0, s51, 118784\n"
"s_nop 0\n"
"global_load_lds_dwordx4 v178, s[60:61]\n"
"v_mfma_f32_16x16x32_bf16 v[192:195], v[180:183], v[28:31], 0\n"
"s_waitcnt vmcnt(17)\n"
"ds_read_b64_tr_b16 v[48:49], v179 offset:4096\n"
"v_mfma_f32_16x16x32_bf16 v[196:199], v[180:183], v[32:35], 0\n"
"s_waitcnt lgkmcnt(1)\n"
"ds_read_b128 v[200:203], v13 offset:24576\n"
"v_mad_u32_u24 v176, v177, s40, v18\n"
"v_mfma_f32_16x16x32_bf16 v[204:207], v[180:183], v[24:27], 0\n"
"ds_read_b64_tr_b16 v[50:51], v179 offset:4864\n"
"v_mad_u32_u24 v177, v177, s40, v19\n"
"v_mfma_f32_16x16x32_bf16 v[180:183], v[184:187], v[28:31], 0\n"
"ds_read_b64_tr_b16 v[52:53], v179 offset:4128\n"
"v_mfma_f32_16x16x32_bf16 v[214:217], v[184:187], v[32:35], 0\n"
"ds_read_b128 v[208:211], v13 offset:30720\n"
"v_mfma_f32_16x16x32_bf16 v[218:221], v[184:187], v[24:27], 0\n"
"ds_read_b64_tr_b16 v[54:55], v179 offset:4896\n"
"v_mfma_f32_16x16x32_bf16 v[222:225], v[188:191], v[28:31], 0\n"
"ds_read_b64_tr_b16 v[56:57], v179 offset:4160\n"
"v_mfma_f32_16x16x32_bf16 v[226:229], v[188:191], v[32:35], 0\n"
"ds_read_b128 v[184:187], v13 offset:36864\n"
"v_mfma_f32_16x16x32_bf16 v[230:233], v[188:191], v[24:27], 0\n"
"s_waitcnt lgkmcnt(6)\n"
"ds_read_b64_tr_b16 v[58:59], v179 offset:4928\n"
"v_mfma_f32_16x16x32_bf16 v[192:195], v[200:203], v[40:43], v[192:195]\n"
"v_mfma_f32_16x16x32_bf16 v[196:199], v[200:203], v[44:47], v[196:199]\n"
"ds_read_b128 v[188:191], v12 offset:26624\n"
"v_mfma_f32_16x16x32_bf16 v[204:207], v[200:203], v[36:39], v[204:207]\n"
"s_waitcnt lgkmcnt(5)\n"
"v_mfma_f32_16x16x32_bf16 v[180:183], v[208:211], v[40:43], v[180:183]\n"
"v_mfma_f32_16x16x32_bf16 v[214:217], v[208:211], v[44:47], v[214:217]\n"
"ds_read_b128 v[200:203], v12 offset:32768\n"
"v_mfma_f32_16x16x32_bf16 v[218:221], v[208:211], v[36:39], v[218:221]\n"
"s_waitcnt lgkmcnt(3)\n"
"v_mfma_f32_16x16x32_bf16 v[222:225], v[184:187], v[40:43], v[222:225]\n"
"s_waitcnt vmcnt(14)\n"
"ds_read_b64_tr_b16 v[60:61], v179 offset:7168\n"
"v_mfma_f32_16x16x32_bf16 v[226:229], v[184:187], v[44:47], v[226:229]\n"
"ds_read_b128 v[208:211], v12 offset:38912\n"
"v_mfma_f32_16x16x32_bf16 v[230:233], v[184:187], v[36:39], v[230:233]\n"
"s_waitcnt lgkmcnt(3)\n"
"ds_read_b64_tr_b16 v[62:63], v179 offset:7936\n"
"v_mfma_f32_16x16x32_bf16 v[192:195], v[188:191], v[48:51], v[192:195]\n"
"ds_read_b64_tr_b16 v[64:65], v179 offset:7200\n"
"v_mfma_f32_16x16x32_bf16 v[196:199], v[188:191], v[52:55], v[196:199]\n"
"ds_read_b128 v[184:187], v13 offset:26624\n"
"v_mfma_f32_16x16x32_bf16 v[204:207], v[188:191], v[56:59], v[204:207]\n"
"s_waitcnt lgkmcnt(5)\n"
"ds_read_b64_tr_b16 v[66:67], v179 offset:7968\n"
"v_mfma_f32_16x16x32_bf16 v[180:183], v[200:203], v[48:51], v[180:183]\n"
"ds_read_b64_tr_b16 v[68:69], v179 offset:7232\n"
"v_mfma_f32_16x16x32_bf16 v[214:217], v[200:203], v[52:55], v[214:217]\n"
"ds_read_b64_tr_b16 v[70:71], v179 offset:8000\n"
"v_mfma_f32_16x16x32_bf16 v[218:221], v[200:203], v[56:59], v[218:221]\n"
"s_waitcnt lgkmcnt(6)\n"
"ds_read_b128 v[188:191], v13 offset:32768\n"
"v_mfma_f32_16x16x32_bf16 v[222:225], v[208:211], v[48:51], v[222:225]\n"
"s_waitcnt vmcnt(11)\n"
"ds_read_b64_tr_b16 v[72:73], v10 offset:53248\n"
"v_mfma_f32_16x16x32_bf16 v[226:229], v[208:211], v[52:55], v[226:229]\n"
"ds_read_b128 v[200:203], v13 offset:38912\n"
"v_mfma_f32_16x16x32_bf16 v[230:233], v[208:211], v[56:59], v[230:233]\n"
"s_waitcnt lgkmcnt(6)\n"
"ds_read_b64_tr_b16 v[74:75], v10 offset:54016\n"
"v_mfma_f32_16x16x32_bf16 v[192:195], v[184:187], v[60:63], v[192:195]\n"
"s_waitcnt lgkmcnt(4)\n"
"ds_read_b64_tr_b16 v[76:77], v10 offset:53280\n"
"s_barrier\n"
"v_mfma_f32_16x16x32_bf16 v[196:199], v[184:187], v[64:67], v[196:199]\n"
"ds_read_b128 v[208:211], v12 offset:28672\n"
"v_mfma_f32_16x16x32_bf16 v[204:207], v[184:187], v[68:71], v[204:207]\n"
"s_waitcnt lgkmcnt(5)\n"
"ds_read_b64_tr_b16 v[78:79], v10 offset:54048\n"
"v_mfma_f32_16x16x32_bf16 v[180:183], v[188:191], v[60:63], v[180:183]\n"
"ds_read_b64_tr_b16 v[80:81], v10 offset:53312\n"
"v_mfma_f32_16x16x32_bf16 v[214:217], v[188:191], v[64:67], v[214:217]\n"
"ds_read_b64_tr_b16 v[82:83], v10 offset:54080\n"
"v_mfma_f32_16x16x32_bf16 v[218:221], v[188:191], v[68:71], v[218:221]\n"
"s_waitcnt lgkmcnt(6)\n"
"ds_read_b128 v[184:187], v12 offset:34816\n"
"v_mfma_f32_16x16x32_bf16 v[222:225], v[200:203], v[60:63], v[222:225]\n"
"s_waitcnt vmcnt(8)\n"
"ds_read_b64_tr_b16 v[84:85], v10 offset:56320\n"
"v_mfma_f32_16x16x32_bf16 v[226:229], v[200:203], v[64:67], v[226:229]\n"
"ds_read_b128 v[188:191], v12 offset:40960\n"
"v_mfma_f32_16x16x32_bf16 v[230:233], v[200:203], v[68:71], v[230:233]\n"
"s_waitcnt lgkmcnt(6)\n"
"ds_read_b64_tr_b16 v[86:87], v10 offset:57088\n"
"v_mfma_f32_16x16x32_bf16 v[192:195], v[208:211], v[72:75], v[192:195]\n"
"s_waitcnt lgkmcnt(6)\n"
"ds_read_b64_tr_b16 v[88:89], v10 offset:56352\n"
"v_mfma_f32_16x16x32_bf16 v[196:199], v[208:211], v[76:79], v[196:199]\n"
"s_waitcnt lgkmcnt(5)\n"
"ds_read_b128 v[200:203], v13 offset:28672\n"
"v_mfma_f32_16x16x32_bf16 v[204:207], v[208:211], v[80:83], v[204:207]\n"
"s_waitcnt lgkmcnt(5)\n"
"ds_read_b64_tr_b16 v[90:91], v10 offset:57120\n"
"v_mfma_f32_16x16x32_bf16 v[180:183], v[184:187], v[72:75], v[180:183]\n"
"ds_read_b64_tr_b16 v[92:93], v10 offset:56384\n"
"v_mfma_f32_16x16x32_bf16 v[214:217], v[184:187], v[76:79], v[214:217]\n"
"ds_read_b64_tr_b16 v[94:95], v10 offset:57152\n"
"v_mfma_f32_16x16x32_bf16 v[218:221], v[184:187], v[80:83], v[218:221]\n"
"s_waitcnt lgkmcnt(6)\n"
"ds_read_b128 v[184:187], v13 offset:34816\n"
"v_mfma_f32_16x16x32_bf16 v[222:225], v[188:191], v[72:75], v[222:225]\n"
"v_mfma_f32_16x16x32_bf16 v[226:229], v[188:191], v[76:79], v[226:229]\n"
"ds_read_b128 v[238:241], v13 offset:40960\n"
"v_mfma_f32_16x16x32_bf16 v[230:233], v[188:191], v[80:83], v[230:233]\n"
"s_waitcnt lgkmcnt(5)\n"
"v_mfma_f32_16x16x32_bf16 v[192:195], v[200:203], v[84:87], v[192:195]\n"
"s_waitcnt lgkmcnt(2)\n"
"s_barrier\n"
"v_mfma_f32_16x16x32_bf16 v[196:199], v[200:203], v[88:91], v[196:199]\n"
"ds_read_b128 v[188:191], v12 offset:43008\n"
"v_mfma_f32_16x16x32_bf16 v[204:207], v[200:203], v[92:95], v[204:207]\n"
"s_waitcnt lgkmcnt(2)\n"
"v_mfma_f32_16x16x32_bf16 v[180:183], v[184:187], v[84:87], v[180:183]\n"
"v_mfma_f32_16x16x32_bf16 v[214:217], v[184:187], v[88:91], v[214:217]\n"
"ds_read_b128 v[200:203], v13 offset:43008\n"
"v_mfma_f32_16x16x32_bf16 v[218:221], v[184:187], v[92:95], v[218:221]\n"
"s_waitcnt lgkmcnt(2)\n"
"v_cvt_pk_bf16_f32 v184, v192, v193\n"
"v_cvt_pk_bf16_f32 v185, v194, v195\n"
"v_mfma_f32_16x16x32_bf16 v[222:225], v[238:241], v[84:87], v[222:225]\n"
"v_cvt_pk_bf16_f32 v210, v196, v197\n"
"v_cvt_pk_bf16_f32 v211, v198, v199\n"
"v_mfma_f32_16x16x32_bf16 v[226:229], v[238:241], v[88:91], v[226:229]\n"
"ds_read_b128 v[196:199], v12 offset:45056\n"
"v_cvt_pk_bf16_f32 v236, v204, v205\n"
"v_cvt_pk_bf16_f32 v237, v206, v207\n"
"v_mfma_f32_16x16x32_bf16 v[230:233], v[238:241], v[92:95], v[230:233]\n"
"s_waitcnt lgkmcnt(2)\n"
"v_cvt_pk_bf16_f32 v186, v180, v181\n"
"v_cvt_pk_bf16_f32 v187, v182, v183\n"
"v_mfma_f32_16x16x32_bf16 v[178:181], v[188:191], v[28:31], 0\n"
"v_cvt_pk_bf16_f32 v212, v214, v215\n"
"v_cvt_pk_bf16_f32 v213, v216, v217\n"
"v_mfma_f32_16x16x32_bf16 v[204:207], v[188:191], v[32:35], 0\n"
"ds_read_b128 v[214:217], v13 offset:45056\n"
"v_cvt_pk_bf16_f32 v238, v218, v219\n"
"v_cvt_pk_bf16_f32 v239, v220, v221\n"
"v_mfma_f32_16x16x32_bf16 v[240:243], v[188:191], v[24:27], 0\n"
"s_waitcnt lgkmcnt(2)\n"
"v_cvt_pk_bf16_f32 v194, v222, v223\n"
"v_cvt_pk_bf16_f32 v195, v224, v225\n"
"v_mfma_f32_16x16x32_bf16 v[178:181], v[200:203], v[40:43], v[178:181]\n"
"v_cvt_pk_bf16_f32 v220, v226, v227\n"
"v_cvt_pk_bf16_f32 v221, v228, v229\n"
"v_mfma_f32_16x16x32_bf16 v[204:207], v[200:203], v[44:47], v[204:207]\n"
"ds_read_b128 v[188:191], v12 offset:47104\n"
"v_cvt_pk_bf16_f32 v246, v230, v231\n"
"v_cvt_pk_bf16_f32 v247, v232, v233\n"
"v_mfma_f32_16x16x32_bf16 v[240:243], v[200:203], v[36:39], v[240:243]\n"
"s_waitcnt lgkmcnt(2)\n"
"v_mfma_f32_16x16x32_bf16 v[178:181], v[196:199], v[48:51], v[178:181]\n"
"v_mfma_f32_16x16x32_bf16 v[204:207], v[196:199], v[52:55], v[204:207]\n"
"ds_read_b128 v[200:203], v13 offset:47104\n"
"v_mfma_f32_16x16x32_bf16 v[240:243], v[196:199], v[56:59], v[240:243]\n"
"s_waitcnt lgkmcnt(2)\n"
"v_mfma_f32_16x16x32_bf16 v[178:181], v[214:217], v[60:63], v[178:181]\n"
"v_mfma_f32_16x16x32_bf16 v[204:207], v[214:217], v[64:67], v[204:207]\n"
"ds_read_b128 v[196:199], v12 offset:49152\n"
"v_mfma_f32_16x16x32_bf16 v[240:243], v[214:217], v[68:71], v[240:243]\n"
"s_waitcnt lgkmcnt(2)\n"
"v_mfma_f32_16x16x32_bf16 v[178:181], v[188:191], v[72:75], v[178:181]\n"
"v_mfma_f32_16x16x32_bf16 v[204:207], v[188:191], v[76:79], v[204:207]\n"
"ds_read_b128 v[214:217], v13 offset:49152\n"
"v_mfma_f32_16x16x32_bf16 v[240:243], v[188:191], v[80:83], v[240:243]\n"
"s_waitcnt lgkmcnt(2)\n"
"v_mfma_f32_16x16x32_bf16 v[178:181], v[200:203], v[84:87], v[178:181]\n"
"v_mfma_f32_16x16x32_bf16 v[204:207], v[200:203], v[88:91], v[204:207]\n"
"ds_read_b128 v[188:191], v12 offset:51200\n"
"v_mfma_f32_16x16x32_bf16 v[240:243], v[200:203], v[92:95], v[240:243]\n"
"s_waitcnt lgkmcnt(2)\n"
"v_mfma_f32_16x16x32_bf16 v[200:203], v[196:199], v[28:31], 0\n"
"v_mfma_f32_16x16x32_bf16 v[224:227], v[196:199], v[32:35], 0\n"
"ds_read_b128 v[228:231], v13 offset:51200\n"
"v_mfma_f32_16x16x32_bf16 v[232:235], v[196:199], v[24:27], 0\n"
"s_waitcnt lgkmcnt(2)\n"
"v_mfma_f32_16x16x32_bf16 v[200:203], v[214:217], v[40:43], v[200:203]\n"
"v_cvt_pk_bf16_f32 v196, v178, v179\n"
"v_cvt_pk_bf16_f32 v197, v180, v181\n"
"v_cvt_pk_bf16_f32 v222, v204, v205\n"
"v_cvt_pk_bf16_f32 v223, v206, v207\n"
"v_mfma_f32_16x16x32_bf16 v[224:227], v[214:217], v[44:47], v[224:227]\n"
"ds_read_b128 v[178:181], v12 offset:53248\n"
"v_cvt_pk_bf16_f32 v248, v240, v241\n"
"v_cvt_pk_bf16_f32 v249, v242, v243\n"
"v_mfma_f32_16x16x32_bf16 v[232:235], v[214:217], v[36:39], v[232:235]\n"
"s_waitcnt lgkmcnt(2)\n"
"v_mfma_f32_16x16x32_bf16 v[200:203], v[188:191], v[48:51], v[200:203]\n"
"v_mfma_f32_16x16x32_bf16 v[224:227], v[188:191], v[52:55], v[224:227]\n"
"ds_read_b128 v[204:207], v13 offset:53248\n"
"v_mfma_f32_16x16x32_bf16 v[232:235], v[188:191], v[56:59], v[232:235]\n"
"s_waitcnt lgkmcnt(2)\n"
"v_mfma_f32_16x16x32_bf16 v[200:203], v[228:231], v[60:63], v[200:203]\n"
"v_mfma_f32_16x16x32_bf16 v[224:227], v[228:231], v[64:67], v[224:227]\n"
"ds_read_b128 v[188:191], v12 offset:55296\n"
"v_mfma_f32_16x16x32_bf16 v[232:235], v[228:231], v[68:71], v[232:235]\n"
"s_waitcnt lgkmcnt(2)\n"
"v_mfma_f32_16x16x32_bf16 v[200:203], v[178:181], v[72:75], v[200:203]\n"
"v_mfma_f32_16x16x32_bf16 v[224:227], v[178:181], v[76:79], v[224:227]\n"
"ds_read_b128 v[216:219], v13 offset:55296\n"
"v_mfma_f32_16x16x32_bf16 v[232:235], v[178:181], v[80:83], v[232:235]\n"
"s_waitcnt lgkmcnt(2)\n"
"v_mfma_f32_16x16x32_bf16 v[200:203], v[204:207], v[84:87], v[200:203]\n"
"v_mfma_f32_16x16x32_bf16 v[224:227], v[204:207], v[88:91], v[224:227]\n"
"ds_read_b128 v[178:181], v12 offset:57344\n"
"v_mfma_f32_16x16x32_bf16 v[232:235], v[204:207], v[92:95], v[232:235]\n"
"s_waitcnt lgkmcnt(2)\n"
"v_mfma_f32_16x16x32_bf16 v[204:207], v[188:191], v[28:31], 0\n"
"v_mfma_f32_16x16x32_bf16 v[228:231], v[188:191], v[32:35], 0\n"
"ds_read_b128 v[242:245], v13 offset:57344\n"
"v_mfma_f32_16x16x32_bf16 v[250:253], v[188:191], v[24:27], 0\n"
"s_waitcnt lgkmcnt(2)\n"
"v_mfma_f32_16x16x32_bf16 v[204:207], v[216:219], v[40:43], v[204:207]\n"
"v_cvt_pk_bf16_f32 v188, v200, v201\n"
"v_cvt_pk_bf16_f32 v189, v202, v203\n"
"v_cvt_pk_bf16_f32 v214, v224, v225\n"
"v_cvt_pk_bf16_f32 v215, v226, v227\n"
"v_mfma_f32_16x16x32_bf16 v[228:231], v[216:219], v[44:47], v[228:231]\n"
"ds_read_b128 v[190:193], v12 offset:59392\n"
"v_cvt_pk_bf16_f32 v240, v232, v233\n"
"v_cvt_pk_bf16_f32 v241, v234, v235\n"
"v_mfma_f32_16x16x32_bf16 v[250:253], v[216:219], v[36:39], v[250:253]\n"
"s_waitcnt lgkmcnt(2)\n"
"v_mfma_f32_16x16x32_bf16 v[204:207], v[178:181], v[48:51], v[204:207]\n"
"v_mfma_f32_16x16x32_bf16 v[228:231], v[178:181], v[52:55], v[228:231]\n"
"ds_read_b128 v[198:201], v13 offset:59392\n"
"v_mfma_f32_16x16x32_bf16 v[250:253], v[178:181], v[56:59], v[250:253]\n"
"s_waitcnt lgkmcnt(2)\n"
"v_mfma_f32_16x16x32_bf16 v[204:207], v[242:245], v[60:63], v[204:207]\n"
"v_mfma_f32_16x16x32_bf16 v[228:231], v[242:245], v[64:67], v[228:231]\n"
"v_mfma_f32_16x16x32_bf16 v[250:253], v[242:245], v[68:71], v[250:253]\n"
"s_waitcnt lgkmcnt(1)\n"
"v_mfma_f32_16x16x32_bf16 v[204:207], v[190:193], v[72:75], v[204:207]\n"
"v_mfma_f32_16x16x32_bf16 v[228:231], v[190:193], v[76:79], v[228:231]\n"
"v_mfma_f32_16x16x32_bf16 v[250:253], v[190:193], v[80:83], v[250:253]\n"
"s_and_b32 s41, 32, s55\n"
"v_add_u32 v2, s41, v15\n"
"s_waitcnt lgkmcnt(0)\n"
"v_mfma_f32_16x16x32_bf16 v[204:207], v[198:201], v[84:87], v[204:207]\n"
"ds_read_u16 v2, v2 offset:832\n"
"v_mfma_f32_16x16x32_bf16 v[228:231], v[198:201], v[88:91], v[228:231]\n"
"s_waitcnt vmcnt(0)\n"
"s_barrier\n"
"ds_read_b128 v[180:183], v12 offset:6144\n"
"v_mfma_f32_16x16x32_bf16 v[250:253], v[198:201], v[92:95], v[250:253]\n"
"s_waitcnt lgkmcnt(1)\n"
"ds_read_b128 v[190:193], v13 offset:6144\n"
"v_mad_u32_u24 v2, v2, s40, v16\n"
"s_waitcnt lgkmcnt(1)\n"
"v_mfma_f32_16x16x32_bf16 v[200:203], v[180:183], v[28:31], 0\n"
"v_cvt_pk_bf16_f32 v178, v204, v205\n"
"v_cvt_pk_bf16_f32 v179, v206, v207\n"
"v_mfma_f32_16x16x32_bf16 v[206:209], v[180:183], v[32:35], 0\n"
"s_add_u32 m0, s51, 126976\n"
"s_nop 0\n"
"global_load_lds_dwordx4 v2, s[56:57]\n"
"ds_read_b128 v[216:219], v12 offset:8192\n"
"v_mfma_f32_16x16x32_bf16 v[232:235], v[180:183], v[24:27], 0\n"
"s_add_u32 m0, s51, 129024\n"
"s_nop 0\n"
"global_load_lds_dwordx4 v2, s[58:59]\n"
"s_waitcnt lgkmcnt(1)\n"
"v_mfma_f32_16x16x32_bf16 v[200:203], v[190:193], v[40:43], v[200:203]\n"
"s_add_u32 m0, s51, 131072\n"
"s_nop 0\n"
"global_load_lds_dwordx4 v2, s[60:61]\n"
"v_cvt_pk_bf16_f32 v204, v228, v229\n"
"v_cvt_pk_bf16_f32 v205, v230, v231\n"
"v_mfma_f32_16x16x32_bf16 v[206:209], v[190:193], v[44:47], v[206:209]\n"
"ds_read_b128 v[180:183], v13 offset:8192\n"
"v_cvt_pk_bf16_f32 v230, v250, v251\n"
"v_cvt_pk_bf16_f32 v231, v252, v253\n"
"v_mfma_f32_16x16x32_bf16 v[232:235], v[190:193], v[36:39], v[232:235]\n"
"s_and_b32 s41, 160, s55\n"
"v_add_u32 v2, s41, v15\n"
"s_waitcnt lgkmcnt(1)\n"
"v_mfma_f32_16x16x32_bf16 v[200:203], v[216:219], v[48:51], v[200:203]\n"
"ds_read_u16 v2, v2 offset:96\n"
"v_mfma_f32_16x16x32_bf16 v[206:209], v[216:219], v[52:55], v[206:209]\n"
"ds_read_b128 v[190:193], v12 offset:10240\n"
"v_mfma_f32_16x16x32_bf16 v[232:235], v[216:219], v[56:59], v[232:235]\n"
"s_waitcnt lgkmcnt(2)\n"
"v_mfma_f32_16x16x32_bf16 v[200:203], v[180:183], v[60:63], v[200:203]\n"
"v_mfma_f32_16x16x32_bf16 v[206:209], v[180:183], v[64:67], v[206:209]\n"
"ds_read_b128 v[216:219], v13 offset:10240\n"
"v_mfma_f32_16x16x32_bf16 v[232:235], v[180:183], v[68:71], v[232:235]\n"
"s_waitcnt lgkmcnt(2)\n"
"v_mad_u32_u24 v2, v2, s40, v16\n"
"s_waitcnt lgkmcnt(1)\n"
"v_mfma_f32_16x16x32_bf16 v[200:203], v[190:193], v[72:75], v[200:203]\n"
"s_add_u32 m0, s51, 139264\n"
"s_nop 0\n"
"global_load_lds_dwordx4 v2, s[56:57]\n"
"v_mfma_f32_16x16x32_bf16 v[206:209], v[190:193], v[76:79], v[206:209]\n"
"s_add_u32 m0, s51, 141312\n"
"s_nop 0\n"
"global_load_lds_dwordx4 v2, s[58:59]\n"
"ds_read_b128 v[180:183], v12 offset:12288\n"
"v_mfma_f32_16x16x32_bf16 v[232:235], v[190:193], v[80:83], v[232:235]\n"
"s_add_u32 m0, s51, 143360\n"
"s_nop 0\n"
"global_load_lds_dwordx4 v2, s[60:61]\n"
"s_waitcnt lgkmcnt(1)\n"
"v_mfma_f32_16x16x32_bf16 v[200:203], v[216:219], v[84:87], v[200:203]\n"
"s_andn2_b32 s41, 288, s55\n"
"v_add_u32 v2, s41, v15\n"
"v_mfma_f32_16x16x32_bf16 v[206:209], v[216:219], v[88:91], v[206:209]\n"
"ds_read_u16 v2, v2 offset:128\n"
"v_mfma_f32_16x16x32_bf16 v[232:235], v[216:219], v[92:95], v[232:235]\n"
"s_waitcnt lgkmcnt(1)\n"
"ds_read_b128 v[190:193], v13 offset:12288\n"
"v_mfma_f32_16x16x32_bf16 v[216:219], v[180:183], v[28:31], 0\n"
"v_mfma_f32_16x16x32_bf16 v[226:229], v[180:183], v[32:35], 0\n"
"ds_read_b128 v[242:245], v12 offset:14336\n"
"v_cvt_pk_bf16_f32 v198, v200, v201\n"
"v_mfma_f32_16x16x32_bf16 v[252:255], v[180:183], v[24:27], 0\n"
"s_waitcnt lgkmcnt(2)\n"
"v_cvt_pk_bf16_f32 v199, v202, v203\n"
"v_mad_u32_u24 v2, v2, s40, v16\n"
"v_cvt_pk_bf16_f32 v224, v206, v207\n"
"v_cvt_pk_bf16_f32 v225, v208, v209\n"
"v_cvt_pk_bf16_f32 v250, v232, v233\n"
"s_waitcnt lgkmcnt(1)\n"
"v_mfma_f32_16x16x32_bf16 v[216:219], v[190:193], v[40:43], v[216:219]\n"
"s_add_u32 m0, s51, 151552\n"
"s_nop 0\n"
"global_load_lds_dwordx4 v2, s[56:57]\n"
"v_mfma_f32_16x16x32_bf16 v[226:229], v[190:193], v[44:47], v[226:229]\n"
"s_add_u32 m0, s51, 153600\n"
"s_nop 0\n"
"global_load_lds_dwordx4 v2, s[58:59]\n"
"ds_read_b128 v[180:183], v13 offset:14336\n"
"v_cvt_pk_bf16_f32 v251, v234, v235\n"
"v_mfma_f32_16x16x32_bf16 v[252:255], v[190:193], v[36:39], v[252:255]\n"
"s_add_u32 m0, s51, 155648\n"
"s_nop 0\n"
"global_load_lds_dwordx4 v2, s[60:61]\n"
"s_waitcnt lgkmcnt(1)\n"
"v_mfma_f32_16x16x32_bf16 v[216:219], v[242:245], v[48:51], v[216:219]\n"
"s_barrier\n"
"s_add_u32 s42, s48, 0\n"
"s_addc_u32 s43, s49, 0\n"
"s_add_u32 m0, s50, 24576\n"
"s_nop 0\n"
"global_load_lds_dwordx4 v3, s[42:43]\n"
"v_mfma_f32_16x16x32_bf16 v[226:229], v[242:245], v[52:55], v[226:229]\n"
"ds_read_b128 v[190:193], v12 offset:16384\n"
"v_mfma_f32_16x16x32_bf16 v[252:255], v[242:245], v[56:59], v[252:255]\n"
"s_add_u32 m0, s50, 25600\n"
"s_nop 0\n"
"global_load_lds_dwordx4 v4, s[42:43]\n"
"s_waitcnt lgkmcnt(1)\n"
"v_mfma_f32_16x16x32_bf16 v[216:219], v[180:183], v[60:63], v[216:219]\n"
"v_mfma_f32_16x16x32_bf16 v[226:229], v[180:183], v[64:67], v[226:229]\n"
"ds_read_b128 v[200:203], v13 offset:16384\n"
"v_mfma_f32_16x16x32_bf16 v[252:255], v[180:183], v[68:71], v[252:255]\n"
"s_add_u32 m0, s50, 26624\n"
"s_nop 0\n"
"global_load_lds_dwordx4 v5, s[42:43]\n"
"s_waitcnt lgkmcnt(1)\n"
"v_mfma_f32_16x16x32_bf16 v[216:219], v[190:193], v[72:75], v[216:219]\n"
"v_mfma_f32_16x16x32_bf16 v[226:229], v[190:193], v[76:79], v[226:229]\n"
"ds_read_b128 v[180:183], v12 offset:18432\n"
"v_mfma_f32_16x16x32_bf16 v[252:255], v[190:193], v[80:83], v[252:255]\n"
"s_add_u32 s42, s48, 18432\n"
"s_addc_u32 s43, s49, 0\n"
"s_add_u32 m0, s50, 27648\n"
"s_nop 0\n"
"global_load_lds_dwordx4 v3, s[42:43]\n"
"s_waitcnt lgkmcnt(1)\n"
"v_mfma_f32_16x16x32_bf16 v[216:219], v[200:203], v[84:87], v[216:219]\n"
"v_mfma_f32_16x16x32_bf16 v[226:229], v[200:203], v[88:91], v[226:229]\n"
"ds_read_b128 v[190:193], v13 offset:18432\n"
"s_add_u32 m0, s50, 28672\n"
"s_nop 0\n"
"global_load_lds_dwordx4 v4, s[42:43]\n"
"v_mfma_f32_16x16x32_bf16 v[252:255], v[200:203], v[92:95], v[252:255]\n"
"s_waitcnt lgkmcnt(1)\n"
"v_mfma_f32_16x16x32_bf16 v[200:203], v[180:183], v[28:31], 0\n"
"v_mfma_f32_16x16x32_bf16 v[242:245], v[180:183], v[32:35], 0\n"
"s_add_u32 m0, s50, 29696\n"
"s_nop 0\n"
"global_load_lds_dwordx4 v5, s[42:43]\n"
"ds_read_b128 v[96:99], v12 offset:20480\n"
"v_mfma_f32_16x16x32_bf16 v[112:115], v[180:183], v[24:27], 0\n"
"s_waitcnt lgkmcnt(1)\n"
"v_cvt_pk_bf16_f32 v180, v216, v217\n"
"v_cvt_pk_bf16_f32 v181, v218, v219\n"
"v_mfma_f32_16x16x32_bf16 v[200:203], v[190:193], v[40:43], v[200:203]\n"
"v_cvt_pk_bf16_f32 v206, v226, v227\n"
"v_cvt_pk_bf16_f32 v207, v228, v229\n"
"v_mfma_f32_16x16x32_bf16 v[242:245], v[190:193], v[44:47], v[242:245]\n"
"ds_read_b128 v[216:219], v13 offset:20480\n"
"v_cvt_pk_bf16_f32 v232, v252, v253\n"
"v_cvt_pk_bf16_f32 v233, v254, v255\n"
"v_mfma_f32_16x16x32_bf16 v[112:115], v[190:193], v[36:39], v[112:115]\n"
"s_waitcnt lgkmcnt(1)\n"
"v_mfma_f32_16x16x32_bf16 v[200:203], v[96:99], v[48:51], v[200:203]\n"
"v_mfma_f32_16x16x32_bf16 v[242:245], v[96:99], v[52:55], v[242:245]\n"
"ds_read_b128 v[190:193], v12 offset:22528\n"
"v_mfma_f32_16x16x32_bf16 v[112:115], v[96:99], v[56:59], v[112:115]\n"
"s_waitcnt vmcnt(12)\n"
"s_barrier\n"
"s_waitcnt vmcnt(3)\n"
"ds_read_b64_tr_b16 v[96:97], v11 offset:53248\n"
"s_waitcnt lgkmcnt(2)\n"
"v_mfma_f32_16x16x32_bf16 v[200:203], v[216:219], v[60:63], v[200:203]\n"
"ds_read_b64_tr_b16 v[98:99], v11 offset:54016\n"
"ds_read_b64_tr_b16 v[100:101], v11 offset:53256\n"
"v_mfma_f32_16x16x32_bf16 v[242:245], v[216:219], v[64:67], v[242:245]\n"
"ds_read_b64_tr_b16 v[102:103], v11 offset:54024\n"
"ds_read_b64_tr_b16 v[226:227], v10 offset:53312\n"
"v_mfma_f32_16x16x32_bf16 v[112:115], v[216:219], v[68:71], v[112:115]\n"
"ds_read_b64_tr_b16 v[228:229], v10 offset:54080\n"
"s_waitcnt vmcnt(0)\n"
"ds_read_b64_tr_b16 v[104:105], v11 offset:56320\n"
"s_waitcnt lgkmcnt(7)\n"
"v_mfma_f32_16x16x32_bf16 v[200:203], v[190:193], v[72:75], v[200:203]\n"
"ds_read_b64_tr_b16 v[106:107], v11 offset:57088\n"
"ds_read_b64_tr_b16 v[108:109], v11 offset:56328\n"
"v_mfma_f32_16x16x32_bf16 v[242:245], v[190:193], v[76:79], v[242:245]\n"
"ds_read_b64_tr_b16 v[110:111], v11 offset:57096\n"
"v_mov_b32 v216, 0x3f803f80\n"
"v_mfma_f32_16x16x32_bf16 v[112:115], v[190:193], v[80:83], v[112:115]\n"
"ds_read_b64_tr_b16 v[252:253], v10 offset:56384\n"
"v_mov_b32 v217, 0x3f803f80\n"
"v_mov_b32 v218, 0x3f803f80\n"
"v_mov_b32 v219, 0x3f803f80\n"
"v_mov_b32 v193, 0\n"
"s_waitcnt lgkmcnt(5)\n"
"s_add_u32 s42, s48, 36864\n"
"s_addc_u32 s43, s49, 0\n"
"s_add_u32 m0, s50, 24576\n"
"s_nop 0\n"
"global_load_lds_dwordx4 v3, s[42:43]\n"
"ds_read_b64_tr_b16 v[254:255], v10 offset:57152\n"
"ds_read_b128 v[116:119], v13 offset:22528\n"
"s_add_u32 m0, s50, 25600\n"
"s_nop 0\n"
"global_load_lds_dwordx4 v4, s[42:43]\n"
"s_add_u32 m0, s50, 26624\n"
"s_nop 0\n"
"global_load_lds_dwordx4 v5, s[42:43]\n"
"s_waitcnt lgkmcnt(0)\n"
"v_mfma_f32_16x16x32_bf16 v[200:203], v[116:119], v[84:87], v[200:203]\n"
"v_mfma_f32_16x16x32_bf16 v[242:245], v[116:119], v[88:91], v[242:245]\n"
"v_mfma_f32_16x16x32_bf16 v[112:115], v[116:119], v[92:95], v[112:115]\n"
"s_add_u32 s42, s48, 55296\n"
"s_addc_u32 s43, s49, 0\n"
"s_add_u32 m0, s50, 27648\n"
"s_nop 0\n"
"global_load_lds_dwordx4 v3, s[42:43]\n"
"s_add_u32 m0, s50, 28672\n"
"s_nop 0\n"
"global_load_lds_dwordx4 v4, s[42:43]\n"
"v_cvt_pk_bf16_f32 v182, v200, v201\n"
"v_cvt_pk_bf16_f32 v183, v202, v203\n"
"v_cvt_pk_bf16_f32 v208, v242, v243\n"
"v_cvt_pk_bf16_f32 v209, v244, v245\n"
"v_cvt_pk_bf16_f32 v234, v112, v113\n"
"v_cvt_pk_bf16_f32 v235, v114, v115\n"
"s_add_u32 m0, s50, 29696\n"
"s_nop 0\n"
"global_load_lds_dwordx4 v5, s[42:43]\n"
"s_waitcnt vmcnt(15)\n"
"s_barrier\n"
"ds_read2st64_b32 v[190:191], v9 offset0:112 offset1:113\n"
"ds_read_b32 v192, v9 offset:29184\n"
"ds_read2st64_b32 v[200:201], v9 offset0:116 offset1:117\n"
"ds_read2st64_b32 v[202:203], v9 offset0:118 offset1:119\n"
"s_waitcnt lgkmcnt(2)\n"
"v_mfma_f32_16x16x32_bf16 v[242:245], v[216:219], v[190:193], 0\n"
"s_waitcnt vmcnt(3)\n"
"ds_read_b64_tr_b16 v[0:1], v11 offset:53248\n"
"v_mfma_f32_16x16x32_bf16 v[136:139], v[186:189], v[190:193], 0\n"
"ds_read_b64_tr_b16 v[2:3], v11 offset:54016\n"
"v_mfma_f32_16x16x32_bf16 v[124:127], v[212:215], v[190:193], 0\n"
"ds_read_b64_tr_b16 v[112:113], v11 offset:53256\n"
"v_mfma_f32_16x16x32_bf16 v[128:131], v[238:241], v[190:193], 0\n"
"s_waitcnt lgkmcnt(3)\n"
"ds_read_b64_tr_b16 v[114:115], v11 offset:54024\n"
"v_mfma_f32_16x16x32_bf16 v[242:245], v[216:219], v[200:203], v[242:245]\n"
"ds_read_b64_tr_b16 v[140:141], v10 offset:53312\n"
"ds_read2st64_b32 v[190:191], v9 offset0:128 offset1:129\n"
"v_mfma_f32_16x16x32_bf16 v[136:139], v[194:197], v[200:203], v[136:139]\n"
"ds_read_b64_tr_b16 v[142:143], v10 offset:54080\n"
"ds_read2st64_b32 v[192:193], v9 offset0:130 offset1:131\n"
"v_mfma_f32_16x16x32_bf16 v[124:127], v[220:223], v[200:203], v[124:127]\n"
"s_waitcnt vmcnt(0)\n"
"ds_read_b64_tr_b16 v[116:117], v11 offset:56320\n"
"v_mfma_f32_16x16x32_bf16 v[128:131], v[246:249], v[200:203], v[128:131]\n"
"s_waitcnt lgkmcnt(2)\n"
"ds_read_b64_tr_b16 v[118:119], v11 offset:57088\n"
"ds_read_b32 v200, v9 offset:33792\n"
"ds_read2st64_b32 v[202:203], v9 offset0:134 offset1:135\n"
"s_add_u32 s42, s48, 12288\n"
"s_addc_u32 s43, s49, 0\n"
"s_add_u32 m0, s50, 24576\n"
"s_nop 0\n"
"global_load_lds_dwordx4 v6, s[42:43]\n"
"s_waitcnt lgkmcnt(4)\n"
"v_mfma_f32_16x16x32_bf16 v[242:245], v[216:219], v[190:193], v[242:245]\n"
"ds_read_b64_tr_b16 v[120:121], v11 offset:56328\n"
"v_mov_b32 v201, 0\n"
"v_mfma_f32_16x16x32_bf16 v[136:139], v[178:181], v[190:193], v[136:139]\n"
"ds_read_b64_tr_b16 v[122:123], v11 offset:57096\n"
"v_mov_b32 v135, 0\n"
"v_mfma_f32_16x16x32_bf16 v[124:127], v[204:207], v[190:193], v[124:127]\n"
"s_add_u32 m0, s50, 25600\n"
"s_nop 0\n"
"global_load_lds_dwordx4 v7, s[42:43]\n"
"ds_read_b64_tr_b16 v[144:145], v10 offset:56384\n"
"v_mfma_f32_16x16x32_bf16 v[128:131], v[230:233], v[190:193], v[128:131]\n"
"s_waitcnt lgkmcnt(3)\n"
"ds_read_b64_tr_b16 v[146:147], v10 offset:57152\n"
"v_mov_b32 v132, 0\n"
"v_mfma_f32_16x16x32_bf16 v[242:245], v[216:219], v[200:203], v[242:245]\n"
"ds_read_b32 v134, v9 offset:9216\n"
"v_mov_b32 v133, 0\n"
"s_add_u32 m0, s50, 26624\n"
"s_nop 0\n"
"global_load_lds_dwordx4 v8, s[42:43]\n"
"s_waitcnt lgkmcnt(1)\n"
"s_add_u32 s42, s48, 49152\n"
"s_addc_u32 s43, s49, 0\n"
"s_add_u32 m0, s50, 27648\n"
"s_nop 0\n"
"global_load_lds_dwordx4 v6, s[42:43]\n"
"s_waitcnt lgkmcnt(0)\n"
"v_mfma_f32_16x16x32_bf16 v[242:245], v[216:219], v[132:135], v[242:245]\n"
"v_mfma_f32_16x16x32_bf16 v[128:131], v[234:237], v[200:203], v[128:131]\n"
"s_add_u32 m0, s50, 28672\n"
"s_nop 0\n"
"global_load_lds_dwordx4 v7, s[42:43]\n"
"v_mfma_f32_16x16x32_bf16 v[128:131], v[248:251], v[132:135], v[128:131]\n"
"ds_read_b128 v[190:193], v12 offset:36864\n"
"v_mfma_f32_16x16x32_bf16 v[136:139], v[182:185], v[200:203], v[136:139]\n"
"v_mfma_f32_16x16x32_bf16 v[136:139], v[196:199], v[132:135], v[136:139]\n"
"s_add_u32 m0, s50, 29696\n"
"s_nop 0\n"
"global_load_lds_dwordx4 v8, s[42:43]\n"
"s_andn2_b32 s41, 608, s55\n"
"v_add_u32 v8, s41, v15\n"
"v_rcp_f32 v243, v242\n"
"v_mfma_f32_16x16x32_bf16 v[124:127], v[208:211], v[200:203], v[124:127]\n"
"ds_read_u16 v8, v8 offset:288\n"
"v_mul_f32 v128, v128, v243\n"
"v_mfma_f32_16x16x32_bf16 v[124:127], v[222:225], v[132:135], v[124:127]\n"
"s_waitcnt lgkmcnt(1)\n"
"ds_read_b128 v[200:203], v13 offset:36864\n"
"v_mul_f32 v129, v129, v243\n"
"v_cvt_pk_bf16_f32 v244, v128, v129\n"
"v_mfma_f32_16x16x32_bf16 v[216:219], v[190:193], v[28:31], 0\n"
"v_mul_f32 v130, v130, v243\n"
"v_mul_f32 v131, v131, v243\n"
"v_mfma_f32_16x16x32_bf16 v[132:135], v[190:193], v[32:35], 0\n"
"ds_read_b128 v[4:7], v12 offset:38912\n"
"v_cvt_pk_bf16_f32 v245, v130, v131\n"
"v_mfma_f32_16x16x32_bf16 v[128:131], v[190:193], v[24:27], 0\n"
"s_waitcnt lgkmcnt(2)\n"
"v_mul_f32 v136, v136, v243\n"
"v_mul_f32 v137, v137, v243\n"
"v_cvt_pk_bf16_f32 v136, v136, v137\n"
"v_mad_u32_u24 v8, v8, s40, v16\n"
"v_mul_f32 v138, v138, v243\n"
"v_mul_f32 v139, v139, v243\n"
"v_cvt_pk_bf16_f32 v137, v138, v139\n"
"s_waitcnt lgkmcnt(1)\n"
"v_mfma_f32_16x16x32_bf16 v[216:219], v[200:203], v[40:43], v[216:219]\n"
"s_add_u32 m0, s51, 114688\n"
"s_nop 0\n"
"global_load_lds_dwordx4 v8, s[56:57]\n"
"v_mfma_f32_16x16x32_bf16 v[132:135], v[200:203], v[44:47], v[132:135]\n"
"ds_read_b128 v[190:193], v13 offset:38912\n"
"v_mul_f32 v124, v124, v243\n"
"v_mfma_f32_16x16x32_bf16 v[128:131], v[200:203], v[36:39], v[128:131]\n"
"s_waitcnt lgkmcnt(1)\n"
"v_mul_f32 v125, v125, v243\n"
"v_cvt_pk_bf16_f32 v138, v124, v125\n"
"v_mfma_f32_16x16x32_bf16 v[216:219], v[4:7], v[48:51], v[216:219]\n"
"v_mul_f32 v126, v126, v243\n"
"v_mul_f32 v127, v127, v243\n"
"v_mfma_f32_16x16x32_bf16 v[132:135], v[4:7], v[52:55], v[132:135]\n"
"ds_read_b128 v[200:203], v12 offset:40960\n"
"v_cvt_pk_bf16_f32 v139, v126, v127\n"
"v_mfma_f32_16x16x32_bf16 v[128:131], v[4:7], v[56:59], v[128:131]\n"
"s_add_u32 m0, s51, 116736\n"
"s_nop 0\n"
"global_load_lds_dwordx4 v8, s[58:59]\n"
"s_waitcnt lgkmcnt(1)\n"
"v_mfma_f32_16x16x32_bf16 v[216:219], v[190:193], v[60:63], v[216:219]\n"
"v_mfma_f32_16x16x32_bf16 v[132:135], v[190:193], v[64:67], v[132:135]\n"
"ds_read_b128 v[4:7], v13 offset:40960\n"
"v_mfma_f32_16x16x32_bf16 v[128:131], v[190:193], v[68:71], v[128:131]\n"
"s_waitcnt lgkmcnt(1)\n"
"v_mfma_f32_16x16x32_bf16 v[216:219], v[200:203], v[72:75], v[216:219]\n"
"v_mfma_f32_16x16x32_bf16 v[132:135], v[200:203], v[76:79], v[132:135]\n"
"ds_read_b128 v[190:193], v12 offset:43008\n"
"v_mfma_f32_16x16x32_bf16 v[128:131], v[200:203], v[80:83], v[128:131]\n"
"s_add_u32 m0, s51, 118784\n"
"s_nop 0\n"
"global_load_lds_dwordx4 v8, s[60:61]\n"
"s_waitcnt lgkmcnt(1)\n"
"v_mfma_f32_16x16x32_bf16 v[216:219], v[4:7], v[84:87], v[216:219]\n"
"v_mfma_f32_16x16x32_bf16 v[132:135], v[4:7], v[88:91], v[132:135]\n"
"ds_read_b128 v[200:203], v13 offset:43008\n"
"v_mfma_f32_16x16x32_bf16 v[128:131], v[4:7], v[92:95], v[128:131]\n"
"s_waitcnt lgkmcnt(1)\n"
"v_mfma_f32_16x16x32_bf16 v[148:151], v[190:193], v[28:31], 0\n"
"v_mfma_f32_16x16x32_bf16 v[152:155], v[190:193], v[32:35], 0\n"
"ds_read_b128 v[156:159], v12 offset:45056\n"
"v_mfma_f32_16x16x32_bf16 v[160:163], v[190:193], v[24:27], 0\n"
"s_waitcnt vmcnt(21)\n"
"s_barrier\n"
"s_waitcnt vmcnt(6)\n"
"ds_read_b64_tr_b16 v[4:5], v11 offset:53248\n"
"v_cvt_pk_bf16_f32 v190, v216, v217\n"
"v_cvt_pk_bf16_f32 v191, v218, v219\n"
"s_waitcnt lgkmcnt(2)\n"
"v_mfma_f32_16x16x32_bf16 v[148:151], v[200:203], v[40:43], v[148:151]\n"
"s_mov_b32 m0, s54\n"
"ds_read_b64_tr_b16 v[6:7], v11 offset:54016\n"
"v_cvt_pk_bf16_f32 v216, v132, v133\n"
"v_cvt_pk_bf16_f32 v217, v134, v135\n"
"v_mfma_f32_16x16x32_bf16 v[152:155], v[200:203], v[44:47], v[152:155]\n"
"ds_read_b64_tr_b16 v[124:125], v11 offset:53256\n"
"v_cvt_pk_bf16_f32 v242, v128, v129\n"
"v_cvt_pk_bf16_f32 v243, v130, v131\n"
"v_mfma_f32_16x16x32_bf16 v[160:163], v[200:203], v[36:39], v[160:163]\n"
"s_waitcnt lgkmcnt(3)\n"
"ds_read_b64_tr_b16 v[126:127], v11 offset:54024\n"
"ds_read_b64_tr_b16 v[200:201], v10 offset:53312\n"
"v_mfma_f32_16x16x32_bf16 v[148:151], v[156:159], v[48:51], v[148:151]\n"
"ds_read_b64_tr_b16 v[202:203], v10 offset:54080\n"
"s_waitcnt vmcnt(3)\n"
"ds_read_b64_tr_b16 v[128:129], v11 offset:56320\n"
"v_mfma_f32_16x16x32_bf16 v[152:155], v[156:159], v[52:55], v[152:155]\n"
"ds_read_b64_tr_b16 v[130:131], v11 offset:57088\n"
"ds_read_b64_tr_b16 v[132:133], v11 offset:56328\n"
"v_mfma_f32_16x16x32_bf16 v[160:163], v[156:159], v[56:59], v[160:163]\n"
"ds_read_b64_tr_b16 v[134:135], v11 offset:57096\n"
"ds_read_b64_tr_b16 v[156:157], v10 offset:56384\n"
"ds_read_b64_tr_b16 v[158:159], v10 offset:57152\n"
"ds_read_b128 v[164:167], v13 offset:45056\n"
"ds_write_addtid_b32 v244 offset:33280\n"
"ds_write_addtid_b32 v245 offset:33536\n"
"ds_write_addtid_b32 v136 offset:32256\n"
"ds_read_b128 v[168:171], v12 offset:47104\n"
"ds_write_addtid_b32 v137 offset:32512\n"
"ds_write_addtid_b32 v138 offset:32768\n"
"ds_write_addtid_b32 v139 offset:33024\n"
"s_waitcnt lgkmcnt(8)\n"
"ds_write_b128 v17, v[226:229] offset:53248\n"
"s_waitcnt lgkmcnt(8)\n"
"v_mfma_f32_16x16x32_bf16 v[148:151], v[164:167], v[60:63], v[148:151]\n"
"v_mfma_f32_16x16x32_bf16 v[152:155], v[164:167], v[64:67], v[152:155]\n"
"ds_write_b128 v17, v[252:255] offset:54272\n"
"v_mfma_f32_16x16x32_bf16 v[160:163], v[164:167], v[68:71], v[160:163]\n"
"s_waitcnt lgkmcnt(5)\n"
"ds_read_b128 v[226:229], v13 offset:47104\n"
"v_mfma_f32_16x16x32_bf16 v[148:151], v[168:171], v[72:75], v[148:151]\n"
"ds_write_b128 v17, v[140:143] offset:55296\n"
"v_mfma_f32_16x16x32_bf16 v[152:155], v[168:171], v[76:79], v[152:155]\n"
"v_mov_b32 v136, 0x3f803f80\n"
"v_mov_b32 v137, 0x3f803f80\n"
"v_mfma_f32_16x16x32_bf16 v[160:163], v[168:171], v[80:83], v[160:163]\n"
"s_and_b32 s41, 480, s55\n"
"v_add_u32 v8, s41, v15\n"
"v_mov_b32 v138, 0x3f803f80\n"
"v_mov_b32 v139, 0x3f803f80\n"
"ds_write_b128 v17, v[144:147] offset:56320\n"
"s_waitcnt lgkmcnt(2)\n"
"v_mfma_f32_16x16x32_bf16 v[148:151], v[226:229], v[84:87], v[148:151]\n"
"s_waitcnt vmcnt(0)\n"
"s_barrier\n"
"ds_read2st64_b32 v[252:253], v9 offset0:120 offset1:121\n"
"ds_read2st64_b32 v[254:255], v9 offset0:122 offset1:123\n"
"v_mfma_f32_16x16x32_bf16 v[152:155], v[226:229], v[88:91], v[152:155]\n"
"ds_write_b128 v17, v[200:203] offset:57344\n"
"v_mfma_f32_16x16x32_bf16 v[160:163], v[226:229], v[92:95], v[160:163]\n"
"ds_read_u16 v8, v8 offset:448\n"
"ds_read2st64_b32 v[140:141], v9 offset0:124 offset1:125\n"
"v_cvt_pk_bf16_f32 v200, v148, v149\n"
"v_cvt_pk_bf16_f32 v201, v150, v151\n"
"ds_read2st64_b32 v[142:143], v9 offset0:126 offset1:127\n"
"ds_write_b128 v17, v[156:159] offset:58368\n"
"s_waitcnt lgkmcnt(5)\n"
"v_mfma_f32_16x16x32_bf16 v[144:147], v[136:139], v[252:255], 0\n"
"ds_read2st64_b32 v[150:151], v9 offset0:128 offset1:129\n"
"v_cvt_pk_bf16_f32 v226, v152, v153\n"
"v_mfma_f32_16x16x32_bf16 v[156:159], v[186:189], v[252:255], 0\n"
"s_waitcnt lgkmcnt(4)\n"
"ds_read2st64_b32 v[152:153], v9 offset0:130 offset1:131\n"
"v_mad_u32_u24 v8, v8, s40, v16\n"
"v_mfma_f32_16x16x32_bf16 v[164:167], v[212:215], v[252:255], 0\n"
"s_add_u32 m0, s51, 139264\n"
"s_nop 0\n"
"global_load_lds_dwordx4 v8, s[56:57]\n"
"v_mfma_f32_16x16x32_bf16 v[168:171], v[238:241], v[252:255], 0\n"
"s_waitcnt lgkmcnt(3)\n"
"v_cvt_pk_bf16_f32 v227, v154, v155\n"
"v_cvt_pk_bf16_f32 v252, v160, v161\n"
"v_mfma_f32_16x16x32_bf16 v[144:147], v[136:139], v[140:143], v[144:147]\n"
"v_cvt_pk_bf16_f32 v253, v162, v163\n"
"v_mov_b32 v148, 0\n"
"v_mov_b32 v149, 0\n"
"v_mov_b32 v154, 0\n"
"v_mov_b32 v155, 0\n"
"s_waitcnt lgkmcnt(0)\n"
"v_mfma_f32_16x16x32_bf16 v[144:147], v[136:139], v[150:153], v[144:147]\n"
"v_mfma_f32_16x16x32_bf16 v[168:171], v[250:253], v[140:143], v[168:171]\n"
"v_mfma_f32_16x16x32_bf16 v[168:171], v[240:243], v[148:151], v[168:171]\n"
"s_add_u32 m0, s51, 141312\n"
"s_nop 0\n"
"global_load_lds_dwordx4 v8, s[58:59]\n"
"v_mfma_f32_16x16x32_bf16 v[168:171], v[248:251], v[152:155], v[168:171]\n"
"v_mfma_f32_16x16x32_bf16 v[156:159], v[198:201], v[140:143], v[156:159]\n"
"v_mfma_f32_16x16x32_bf16 v[156:159], v[188:191], v[148:151], v[156:159]\n"
"ds_read2st64_b32 v[136:137], v14 offset0:126 offset1:127\n"
"v_mfma_f32_16x16x32_bf16 v[156:159], v[196:199], v[152:155], v[156:159]\n"
"ds_read2st64_b32 v[138:139], v14 offset0:128 offset1:129\n"
"v_rcp_f32 v255, v144\n"
"v_mfma_f32_16x16x32_bf16 v[164:167], v[224:227], v[140:143], v[164:167]\n"
"ds_read_b128 v[140:143], v17 offset:53248\n"
"v_mul_f32 v168, v168, v255\n"
"v_mfma_f32_16x16x32_bf16 v[164:167], v[214:217], v[148:151], v[164:167]\n"
"s_add_u32 m0, s51, 143360\n"
"s_nop 0\n"
"global_load_lds_dwordx4 v8, s[60:61]\n"
"ds_read2st64_b32 v[144:145], v14 offset0:150 offset1:151\n"
"v_mfma_f32_16x16x32_bf16 v[164:167], v[222:225], v[152:155], v[164:167]\n"
"ds_read2st64_b32 v[146:147], v14 offset0:152 offset1:153\n"
"v_mul_f32 v169, v169, v255\n"
"v_cvt_pk_bf16_f32 v244, v168, v169\n"
"v_mul_f32 v170, v170, v255\n"
"v_mul_f32 v171, v171, v255\n"
"v_cvt_pk_bf16_f32 v245, v170, v171\n"
"s_waitcnt lgkmcnt(3)\n"
"v_mfma_f32_16x16x32_bf16 v[148:151], v[96:99], v[136:139], 0\n"
"ds_read_b128 v[152:155], v17 offset:54272\n"
"v_mul_f32 v156, v156, v255\n"
"v_mfma_f32_16x16x32_bf16 v[160:163], v[100:103], v[136:139], 0\n"
"s_waitcnt lgkmcnt(3)\n"
"ds_read2st64_b32 v[168:169], v14 offset0:174 offset1:175\n"
"v_mul_f32 v157, v157, v255\n"
"v_mfma_f32_16x16x32_bf16 v[172:175], v[140:143], v[136:139], 0\n"
"ds_read2st64_b32 v[170:171], v14 offset0:176 offset1:177\n"
"v_cvt_pk_bf16_f32 v156, v156, v157\n"
"v_mul_f32 v158, v158, v255\n"
"v_mul_f32 v159, v159, v255\n"
"s_waitcnt lgkmcnt(3)\n"
"v_mfma_f32_16x16x32_bf16 v[148:151], v[104:107], v[144:147], v[148:151]\n"
"ds_read_b128 v[136:139], v17 offset:55296\n"
"v_cvt_pk_bf16_f32 v157, v158, v159\n"
"v_mfma_f32_16x16x32_bf16 v[160:163], v[108:111], v[144:147], v[160:163]\n"
"s_waitcnt lgkmcnt(3)\n"
"ds_read2st64_b32 v[140:141], v14 offset0:198 offset1:199\n"
"v_mul_f32 v164, v164, v255\n"
"v_mfma_f32_16x16x32_bf16 v[172:175], v[152:155], v[144:147], v[172:175]\n"
"ds_read2st64_b32 v[142:143], v14 offset0:200 offset1:201\n"
"v_mul_f32 v165, v165, v255\n"
"v_cvt_pk_bf16_f32 v158, v164, v165\n"
"v_mul_f32 v166, v166, v255\n"
"v_mul_f32 v167, v167, v255\n"
"v_cvt_pk_bf16_f32 v159, v166, v167\n"
"s_waitcnt lgkmcnt(3)\n"
"v_mfma_f32_16x16x32_bf16 v[148:151], v[0:3], v[168:171], v[148:151]\n"
"ds_read_b128 v[144:147], v17 offset:56320\n"
"v_mfma_f32_16x16x32_bf16 v[160:163], v[112:115], v[168:171], v[160:163]\n"
"s_waitcnt lgkmcnt(3)\n"
"ds_read2st64_b32 v[152:153], v14 offset0:130 offset1:131\n"
"v_mfma_f32_16x16x32_bf16 v[172:175], v[136:139], v[168:171], v[172:175]\n"
"s_waitcnt lgkmcnt(2)\n"
"ds_read2st64_b32 v[154:155], v14 offset0:154 offset1:155\n"
"v_mfma_f32_16x16x32_bf16 v[148:151], v[116:119], v[140:143], v[148:151]\n"
"ds_read_b128 v[136:139], v17 offset:57344\n"
"v_mfma_f32_16x16x32_bf16 v[160:163], v[120:123], v[140:143], v[160:163]\n"
"s_waitcnt lgkmcnt(3)\n"
"ds_read2st64_b32 v[164:165], v14 offset0:178 offset1:179\n"
"v_mfma_f32_16x16x32_bf16 v[172:175], v[144:147], v[140:143], v[172:175]\n"
"s_waitcnt lgkmcnt(2)\n"
"ds_read2st64_b32 v[166:167], v14 offset0:202 offset1:203\n"
"v_mfma_f32_16x16x32_bf16 v[148:151], v[4:7], v[152:155], v[148:151]\n"
"ds_read_b128 v[140:143], v17 offset:58368\n"
"v_mfma_f32_16x16x32_bf16 v[160:163], v[124:127], v[152:155], v[160:163]\n"
"s_mov_b32 m0, s54\n"
"ds_read_b128 v[144:147], v12 offset:49152\n"
"s_waitcnt lgkmcnt(4)\n"
"v_mfma_f32_16x16x32_bf16 v[172:175], v[136:139], v[152:155], v[172:175]\n"
"s_waitcnt lgkmcnt(2)\n"
"v_mfma_f32_16x16x32_bf16 v[148:151], v[128:131], v[164:167], v[148:151]\n"
"v_mfma_f32_16x16x32_bf16 v[160:163], v[132:135], v[164:167], v[160:163]\n"
"s_barrier\n"
"ds_write_addtid_b32 v244 offset:33280\n"
"s_waitcnt lgkmcnt(2)\n"
"v_mfma_f32_16x16x32_bf16 v[172:175], v[140:143], v[164:167], v[172:175]\n"
"ds_write_addtid_b32 v245 offset:33536\n"
"ds_write_addtid_b32 v156 offset:32256\n"
"s_waitcnt lgkmcnt(3)\n"
"v_mfma_f32_16x16x32_bf16 v[136:139], v[144:147], v[28:31], 0\n"
"ds_write_addtid_b32 v157 offset:32512\n"
"v_cvt_pk_bf16_f32 v148, v148, v149\n"
"v_cvt_pk_bf16_f32 v149, v150, v151\n"
"v_cvt_pk_bf16_f32 v150, v160, v161\n"
"v_cvt_pk_bf16_f32 v151, v162, v163\n"
"v_cvt_pk_bf16_f32 v172, v172, v173\n"
"v_cvt_pk_bf16_f32 v173, v174, v175\n"
"v_mfma_f32_16x16x32_bf16 v[140:143], v[144:147], v[32:35], 0\n"
"ds_write_addtid_b32 v158 offset:32768\n"
"ds_write_addtid_b32 v159 offset:33024\n"
"s_mov_b32 m0, s68\n"
"ds_read_b128 v[152:155], v13 offset:49152\n"
"v_mfma_f32_16x16x32_bf16 v[156:159], v[144:147], v[24:27], 0\n"
"ds_write_addtid_b32 v148 offset:45056\n"
"ds_write_addtid_b32 v149 offset:45312\n"
"ds_write_addtid_b32 v150 offset:45568\n"
"ds_write_addtid_b32 v151 offset:45824\n"
"ds_write_addtid_b32 v172 offset:46080\n"
"ds_write_addtid_b32 v173 offset:46336\n"
"s_and_b32 s41, 32, s55\n"
"v_add_u32 v8, s41, v15\n"
"ds_read_u16 v8, v8 offset:480\n"
"ds_read_b128 v[144:147], v12 offset:51200\n"
"s_waitcnt lgkmcnt(8)\n"
"v_mfma_f32_16x16x32_bf16 v[136:139], v[152:155], v[40:43], v[136:139]\n"
"v_mfma_f32_16x16x32_bf16 v[140:143], v[152:155], v[44:47], v[140:143]\n"
"ds_read_b128 v[148:151], v13 offset:51200\n"
"v_mfma_f32_16x16x32_bf16 v[156:159], v[152:155], v[36:39], v[156:159]\n"
"s_waitcnt lgkmcnt(2)\n"
"v_mad_u32_u24 v8, v8, s40, v16\n"
"s_waitcnt lgkmcnt(1)\n"
"v_mfma_f32_16x16x32_bf16 v[136:139], v[144:147], v[48:51], v[136:139]\n"
"s_add_u32 m0, s51, 126976\n"
"s_nop 0\n"
"global_load_lds_dwordx4 v8, s[56:57]\n"
"v_mfma_f32_16x16x32_bf16 v[140:143], v[144:147], v[52:55], v[140:143]\n"
"ds_read_b128 v[152:155], v12 offset:53248\n"
"v_mfma_f32_16x16x32_bf16 v[156:159], v[144:147], v[56:59], v[156:159]\n"
"s_waitcnt lgkmcnt(1)\n"
"v_mfma_f32_16x16x32_bf16 v[136:139], v[148:151], v[60:63], v[136:139]\n"
"v_mfma_f32_16x16x32_bf16 v[140:143], v[148:151], v[64:67], v[140:143]\n"
"ds_read_b128 v[144:147], v13 offset:53248\n"
"v_mfma_f32_16x16x32_bf16 v[156:159], v[148:151], v[68:71], v[156:159]\n"
"s_waitcnt lgkmcnt(1)\n"
"v_mfma_f32_16x16x32_bf16 v[136:139], v[152:155], v[72:75], v[136:139]\n"
"s_add_u32 m0, s51, 129024\n"
"s_nop 0\n"
"global_load_lds_dwordx4 v8, s[58:59]\n"
"v_mfma_f32_16x16x32_bf16 v[140:143], v[152:155], v[76:79], v[140:143]\n"
"ds_read_b128 v[148:151], v12 offset:55296\n"
"v_mfma_f32_16x16x32_bf16 v[156:159], v[152:155], v[80:83], v[156:159]\n"
"s_waitcnt lgkmcnt(1)\n"
"v_mfma_f32_16x16x32_bf16 v[136:139], v[144:147], v[84:87], v[136:139]\n"
"v_mfma_f32_16x16x32_bf16 v[140:143], v[144:147], v[88:91], v[140:143]\n"
"ds_read_b128 v[152:155], v13 offset:55296\n"
"v_mfma_f32_16x16x32_bf16 v[156:159], v[144:147], v[92:95], v[156:159]\n"
"s_waitcnt lgkmcnt(1)\n"
"v_mfma_f32_16x16x32_bf16 v[144:147], v[148:151], v[28:31], 0\n"
"s_add_u32 m0, s51, 131072\n"
"s_nop 0\n"
"global_load_lds_dwordx4 v8, s[60:61]\n"
"v_mfma_f32_16x16x32_bf16 v[160:163], v[148:151], v[32:35], 0\n"
"ds_read_b128 v[164:167], v12 offset:57344\n"
"v_cvt_pk_bf16_f32 v184, v136, v137\n"
"v_mfma_f32_16x16x32_bf16 v[168:171], v[148:151], v[24:27], 0\n"
"s_waitcnt lgkmcnt(1)\n"
"v_cvt_pk_bf16_f32 v185, v138, v139\n"
"v_cvt_pk_bf16_f32 v210, v140, v141\n"
"v_mfma_f32_16x16x32_bf16 v[144:147], v[152:155], v[40:43], v[144:147]\n"
"v_cvt_pk_bf16_f32 v211, v142, v143\n"
"v_cvt_pk_bf16_f32 v236, v156, v157\n"
"v_mfma_f32_16x16x32_bf16 v[160:163], v[152:155], v[44:47], v[160:163]\n"
"ds_read_b128 v[136:139], v13 offset:57344\n"
"v_cvt_pk_bf16_f32 v237, v158, v159\n"
"v_mfma_f32_16x16x32_bf16 v[168:171], v[152:155], v[36:39], v[168:171]\n"
"s_waitcnt lgkmcnt(1)\n"
"v_mfma_f32_16x16x32_bf16 v[144:147], v[164:167], v[48:51], v[144:147]\n"
"v_mfma_f32_16x16x32_bf16 v[160:163], v[164:167], v[52:55], v[160:163]\n"
"ds_read_b128 v[140:143], v12 offset:59392\n"
"v_mfma_f32_16x16x32_bf16 v[168:171], v[164:167], v[56:59], v[168:171]\n"
"s_waitcnt lgkmcnt(1)\n"
"v_mfma_f32_16x16x32_bf16 v[144:147], v[136:139], v[60:63], v[144:147]\n"
"v_mfma_f32_16x16x32_bf16 v[160:163], v[136:139], v[64:67], v[160:163]\n"
"ds_read_b128 v[148:151], v13 offset:59392\n"
"v_mfma_f32_16x16x32_bf16 v[168:171], v[136:139], v[68:71], v[168:171]\n"
"s_waitcnt lgkmcnt(1)\n"
"v_mfma_f32_16x16x32_bf16 v[144:147], v[140:143], v[72:75], v[144:147]\n"
"v_mfma_f32_16x16x32_bf16 v[160:163], v[140:143], v[76:79], v[160:163]\n"
"v_mov_b32 v136, 0x3f803f80\n"
"v_mov_b32 v137, 0x3f803f80\n"
"v_mfma_f32_16x16x32_bf16 v[168:171], v[140:143], v[80:83], v[168:171]\n"
"s_waitcnt lgkmcnt(0)\n"
"v_mov_b32 v138, 0x3f803f80\n"
"v_mov_b32 v139, 0x3f803f80\n"
"v_mfma_f32_16x16x32_bf16 v[144:147], v[148:151], v[84:87], v[144:147]\n"
"s_waitcnt vmcnt(3)\n"
"s_barrier\n"
"ds_read2st64_b32 v[140:141], v9 offset0:112 offset1:113\n"
"ds_read2st64_b32 v[142:143], v9 offset0:114 offset1:115\n"
"v_mfma_f32_16x16x32_bf16 v[160:163], v[148:151], v[88:91], v[160:163]\n"
"s_and_b32 s41, 416, s55\n"
"v_add_u32 v8, s41, v15\n"
"v_mfma_f32_16x16x32_bf16 v[168:171], v[148:151], v[92:95], v[168:171]\n"
"ds_read2st64_b32 v[148:149], v9 offset0:116 offset1:117\n"
"v_cvt_pk_bf16_f32 v192, v144, v145\n"
"v_cvt_pk_bf16_f32 v193, v146, v147\n"
"ds_read2st64_b32 v[150:151], v9 offset0:118 offset1:119\n"
"ds_read_u16 v8, v8 offset:544\n"
"v_cvt_pk_bf16_f32 v218, v160, v161\n"
"v_cvt_pk_bf16_f32 v219, v162, v163\n"
"s_waitcnt lgkmcnt(3)\n"
"v_mfma_f32_16x16x32_bf16 v[144:147], v[136:139], v[140:143], 0\n"
"v_cvt_pk_bf16_f32 v244, v168, v169\n"
"v_cvt_pk_bf16_f32 v245, v170, v171\n"
"v_mfma_f32_16x16x32_bf16 v[152:155], v[180:183], v[140:143], 0\n"
"v_mfma_f32_16x16x32_bf16 v[156:159], v[206:209], v[140:143], 0\n"
"ds_read_u16 v255, v21 offset:800\n"
"v_mfma_f32_16x16x32_bf16 v[160:163], v[232:235], v[140:143], 0\n"
"s_waitcnt lgkmcnt(1)\n"
"ds_read2st64_b32 v[140:141], v20 offset0:0 offset1:1\n"
"v_mad_u32_u24 v8, v8, s40, v16\n"
"v_mfma_f32_16x16x32_bf16 v[144:147], v[136:139], v[148:151], v[144:147]\n"
"s_add_u32 m0, s51, 151552\n"
"s_nop 0\n"
"global_load_lds_dwordx4 v8, s[56:57]\n"
"ds_read2st64_b32 v[142:143], v20 offset0:2 offset1:3\n"
"v_mfma_f32_16x16x32_bf16 v[152:155], v[188:191], v[148:151], v[152:155]\n"
"ds_read2st64_b32 v[164:165], v9 offset0:128 offset1:129\n"
"ds_read2st64_b32 v[166:167], v9 offset0:130 offset1:131\n"
"v_mfma_f32_16x16x32_bf16 v[156:159], v[214:217], v[148:151], v[156:159]\n"
"s_waitcnt lgkmcnt(4)\n"
"ds_read2st64_b32 v[228:229], v20 offset0:4 offset1:5\n"
"v_mad_u32_u24 v254, v255, s40, v18\n"
"v_mfma_f32_16x16x32_bf16 v[160:163], v[240:243], v[148:151], v[160:163]\n"
"s_waitcnt lgkmcnt(3)\n"
"ds_read2st64_b32 v[148:149], v9 offset0:132 offset1:133\n"
"v_mad_u32_u24 v255, v255, s40, v19\n"
"ds_read2st64_b32 v[150:151], v9 offset0:134 offset1:135\n"
"global_store_dwordx4 v254, v[140:143], s[66:67] sc1\n"
"s_waitcnt lgkmcnt(3)\n"
"v_mfma_f32_16x16x32_bf16 v[144:147], v[136:139], v[164:167], v[144:147]\n"
"s_add_u32 m0, s51, 153600\n"
"s_nop 0\n"
"global_load_lds_dwordx4 v8, s[58:59]\n"
"ds_read2st64_b32 v[142:143], v9 offset0:36 offset1:37\n"
"v_mfma_f32_16x16x32_bf16 v[152:155], v[184:187], v[164:167], v[152:155]\n"
"s_waitcnt lgkmcnt(3)\n"
"global_store_dwordx2 v255, v[228:229], s[66:67] offset:256 sc1\n"
"v_mfma_f32_16x16x32_bf16 v[156:159], v[210:213], v[164:167], v[156:159]\n"
"v_mov_b32 v140, 0\n"
"v_mov_b32 v141, 0\n"
"v_mfma_f32_16x16x32_bf16 v[160:163], v[236:239], v[164:167], v[160:163]\n"
"s_waitcnt lgkmcnt(1)\n"
"v_mfma_f32_16x16x32_bf16 v[144:147], v[136:139], v[148:151], v[144:147]\n"
"s_waitcnt lgkmcnt(0)\n"
"v_mfma_f32_16x16x32_bf16 v[144:147], v[136:139], v[140:143], v[144:147]\n"
"s_add_u32 m0, s51, 155648\n"
"s_nop 0\n"
"global_load_lds_dwordx4 v8, s[60:61]\n"
"v_mfma_f32_16x16x32_bf16 v[160:163], v[248:251], v[148:151], v[160:163]\n"
"v_mfma_f32_16x16x32_bf16 v[160:163], v[250:253], v[140:143], v[160:163]\n"
"ds_read2st64_b32 v[136:137], v14 offset0:126 offset1:127\n"
"v_mfma_f32_16x16x32_bf16 v[152:155], v[196:199], v[148:151], v[152:155]\n"
"ds_read2st64_b32 v[138:139], v14 offset0:128 offset1:129\n"
"v_mfma_f32_16x16x32_bf16 v[152:155], v[198:201], v[140:143], v[152:155]\n"
"ds_read_b128 v[164:167], v17 offset:53248\n"
"v_rcp_f32 v8, v144\n"
"v_mfma_f32_16x16x32_bf16 v[156:159], v[222:225], v[148:151], v[156:159]\n"
"ds_read2st64_b32 v[144:145], v14 offset0:150 offset1:151\n"
"v_mul_f32 v160, v160, v8\n"
"v_mfma_f32_16x16x32_bf16 v[156:159], v[224:227], v[140:143], v[156:159]\n"
"ds_read2st64_b32 v[146:147], v14 offset0:152 offset1:153\n"
"v_mul_f32 v161, v161, v8\n"
"v_cvt_pk_bf16_f32 v254, v160, v161\n"
"v_mul_f32 v162, v162, v8\n"
"v_mul_f32 v163, v163, v8\n"
"v_cvt_pk_bf16_f32 v255, v162, v163\n"
"v_mul_f32 v152, v152, v8\n"
"s_waitcnt lgkmcnt(3)\n"
"v_mfma_f32_16x16x32_bf16 v[140:143], v[96:99], v[136:139], 0\n"
"ds_read_b128 v[148:151], v17 offset:54272\n"
"v_mul_f32 v153, v153, v8\n"
"v_mfma_f32_16x16x32_bf16 v[160:163], v[100:103], v[136:139], 0\n"
"s_waitcnt lgkmcnt(3)\n"
"ds_read2st64_b32 v[168:169], v14 offset0:174 offset1:175\n"
"v_cvt_pk_bf16_f32 v152, v152, v153\n"
"v_mfma_f32_16x16x32_bf16 v[172:175], v[164:167], v[136:139], 0\n"
"s_waitcnt lgkmcnt(2)\n"
"ds_read2st64_b32 v[170:171], v14 offset0:176 offset1:177\n"
"v_mul_f32 v154, v154, v8\n"
"v_mul_f32 v155, v155, v8\n"
"v_mfma_f32_16x16x32_bf16 v[140:143], v[104:107], v[144:147], v[140:143]\n"
"ds_read_b128 v[136:139], v17 offset:55296\n"
"v_cvt_pk_bf16_f32 v153, v154, v155\n"
"v_mfma_f32_16x16x32_bf16 v[160:163], v[108:111], v[144:147], v[160:163]\n"
"s_waitcnt lgkmcnt(3)\n"
"ds_read2st64_b32 v[164:165], v14 offset0:198 offset1:199\n"
"v_mul_f32 v156, v156, v8\n"
"v_mul_f32 v157, v157, v8\n"
"v_mfma_f32_16x16x32_bf16 v[172:175], v[148:151], v[144:147], v[172:175]\n"
"ds_read2st64_b32 v[166:167], v14 offset0:200 offset1:201\n"
"v_cvt_pk_bf16_f32 v154, v156, v157\n"
"v_mul_f32 v158, v158, v8\n"
"v_mul_f32 v159, v159, v8\n"
"v_cvt_pk_bf16_f32 v155, v158, v159\n"
"s_waitcnt lgkmcnt(3)\n"
"v_mfma_f32_16x16x32_bf16 v[140:143], v[0:3], v[168:171], v[140:143]\n"
"ds_read_b128 v[144:147], v17 offset:56320\n"
"v_mfma_f32_16x16x32_bf16 v[160:163], v[112:115], v[168:171], v[160:163]\n"
"s_waitcnt lgkmcnt(3)\n"
"ds_read2st64_b32 v[148:149], v14 offset0:130 offset1:131\n"
"v_mfma_f32_16x16x32_bf16 v[172:175], v[136:139], v[168:171], v[172:175]\n"
"s_waitcnt lgkmcnt(2)\n"
"ds_read2st64_b32 v[150:151], v14 offset0:154 offset1:155\n"
"v_mfma_f32_16x16x32_bf16 v[140:143], v[116:119], v[164:167], v[140:143]\n"
"ds_read_b128 v[136:139], v17 offset:57344\n"
"v_mfma_f32_16x16x32_bf16 v[160:163], v[120:123], v[164:167], v[160:163]\n"
"s_waitcnt lgkmcnt(3)\n"
"ds_read2st64_b32 v[156:157], v14 offset0:178 offset1:179\n"
"v_mfma_f32_16x16x32_bf16 v[172:175], v[144:147], v[164:167], v[172:175]\n"
"s_waitcnt lgkmcnt(2)\n"
"ds_read2st64_b32 v[158:159], v14 offset0:202 offset1:203\n"
"v_mfma_f32_16x16x32_bf16 v[140:143], v[4:7], v[148:151], v[140:143]\n"
"ds_read_b128 v[144:147], v17 offset:58368\n"
"v_mfma_f32_16x16x32_bf16 v[160:163], v[124:127], v[148:151], v[160:163]\n"
"s_waitcnt lgkmcnt(3)\n"
"ds_read_b128 v[164:167], v12 offset:18432\n"
"v_mfma_f32_16x16x32_bf16 v[172:175], v[136:139], v[148:151], v[172:175]\n"
"s_waitcnt lgkmcnt(2)\n"
"v_mfma_f32_16x16x32_bf16 v[140:143], v[128:131], v[156:159], v[140:143]\n"
"v_mfma_f32_16x16x32_bf16 v[160:163], v[132:135], v[156:159], v[160:163]\n"
"s_barrier\n"
"ds_read_b128 v[136:139], v13 offset:18432\n"
"s_waitcnt lgkmcnt(2)\n"
"v_mfma_f32_16x16x32_bf16 v[172:175], v[144:147], v[156:159], v[172:175]\n"
"s_waitcnt lgkmcnt(1)\n"
"v_mfma_f32_16x16x32_bf16 v[144:147], v[164:167], v[28:31], 0\n"
"v_mfma_f32_16x16x32_bf16 v[148:151], v[164:167], v[32:35], 0\n"
"ds_read_b128 v[156:159], v12 offset:20480\n"
"v_mfma_f32_16x16x32_bf16 v[168:171], v[164:167], v[24:27], 0\n"
"v_cvt_pk_bf16_f32 v140, v140, v141\n"
"v_cvt_pk_bf16_f32 v141, v142, v143\n"
"v_cvt_pk_bf16_f32 v142, v160, v161\n"
"v_cvt_pk_bf16_f32 v143, v162, v163\n"
"s_waitcnt lgkmcnt(1)\n"
"v_mfma_f32_16x16x32_bf16 v[144:147], v[136:139], v[40:43], v[144:147]\n"
"s_mov_b32 m0, s54\n"
"v_cvt_pk_bf16_f32 v172, v172, v173\n"
"v_cvt_pk_bf16_f32 v173, v174, v175\n"
"v_mfma_f32_16x16x32_bf16 v[148:151], v[136:139], v[44:47], v[148:151]\n"
"ds_read_b128 v[160:163], v13 offset:20480\n"
"v_mfma_f32_16x16x32_bf16 v[168:171], v[136:139], v[36:39], v[168:171]\n"
"ds_write_addtid_b32 v254 offset:33280\n"
"s_waitcnt lgkmcnt(2)\n"
"v_mfma_f32_16x16x32_bf16 v[144:147], v[156:159], v[48:51], v[144:147]\n"
"ds_write_addtid_b32 v255 offset:33536\n"
"v_mfma_f32_16x16x32_bf16 v[148:151], v[156:159], v[52:55], v[148:151]\n"
"ds_write_addtid_b32 v152 offset:32256\n"
"v_mfma_f32_16x16x32_bf16 v[168:171], v[156:159], v[56:59], v[168:171]\n"
"ds_read_b128 v[136:139], v12 offset:22528\n"
"ds_write_addtid_b32 v153 offset:32512\n"
"s_waitcnt lgkmcnt(5)\n"
"v_mfma_f32_16x16x32_bf16 v[144:147], v[160:163], v[60:63], v[144:147]\n"
"ds_write_addtid_b32 v154 offset:32768\n"
"v_mfma_f32_16x16x32_bf16 v[148:151], v[160:163], v[64:67], v[148:151]\n"
"ds_write_addtid_b32 v155 offset:33024\n"
"s_mov_b32 m0, s68\n"
"ds_read_b128 v[152:155], v13 offset:22528\n"
"v_mfma_f32_16x16x32_bf16 v[168:171], v[160:163], v[68:71], v[168:171]\n"
"ds_write_addtid_b32 v140 offset:45056\n"
"ds_write_addtid_b32 v141 offset:45312\n"
"ds_write_addtid_b32 v142 offset:45568\n"
"s_waitcnt lgkmcnt(7)\n"
"v_mfma_f32_16x16x32_bf16 v[144:147], v[136:139], v[72:75], v[144:147]\n"
"ds_write_addtid_b32 v143 offset:45824\n"
"v_mfma_f32_16x16x32_bf16 v[148:151], v[136:139], v[76:79], v[148:151]\n"
"ds_read_b128 v[140:143], v12 offset:36864\n"
"v_mfma_f32_16x16x32_bf16 v[168:171], v[136:139], v[80:83], v[168:171]\n"
"ds_write_addtid_b32 v172 offset:46080\n"
"s_waitcnt lgkmcnt(6)\n"
"v_mfma_f32_16x16x32_bf16 v[144:147], v[152:155], v[84:87], v[144:147]\n"
"ds_write_addtid_b32 v173 offset:46336\n"
"v_mfma_f32_16x16x32_bf16 v[148:151], v[152:155], v[88:91], v[148:151]\n"
"ds_read_b128 v[136:139], v13 offset:36864\n"
"v_mfma_f32_16x16x32_bf16 v[168:171], v[152:155], v[92:95], v[168:171]\n"
"s_waitcnt lgkmcnt(3)\n"
"v_mfma_f32_16x16x32_bf16 v[152:155], v[140:143], v[28:31], 0\n"
"v_mfma_f32_16x16x32_bf16 v[156:159], v[140:143], v[32:35], 0\n"
"ds_read_b128 v[160:163], v12 offset:38912\n"
"v_mfma_f32_16x16x32_bf16 v[164:167], v[140:143], v[24:27], 0\n"
"s_waitcnt lgkmcnt(1)\n"
"v_cvt_pk_bf16_f32 v202, v144, v145\n"
"v_cvt_pk_bf16_f32 v203, v146, v147\n"
"v_mfma_f32_16x16x32_bf16 v[152:155], v[136:139], v[40:43], v[152:155]\n"
"v_cvt_pk_bf16_f32 v228, v148, v149\n"
"v_cvt_pk_bf16_f32 v229, v150, v151\n"
"v_mfma_f32_16x16x32_bf16 v[156:159], v[136:139], v[44:47], v[156:159]\n"
"ds_read_b128 v[140:143], v13 offset:38912\n"
"v_cvt_pk_bf16_f32 v254, v168, v169\n"
"v_cvt_pk_bf16_f32 v255, v170, v171\n"
"v_mfma_f32_16x16x32_bf16 v[164:167], v[136:139], v[36:39], v[164:167]\n"
"s_waitcnt lgkmcnt(1)\n"
"v_mfma_f32_16x16x32_bf16 v[152:155], v[160:163], v[48:51], v[152:155]\n"
"v_mfma_f32_16x16x32_bf16 v[156:159], v[160:163], v[52:55], v[156:159]\n"
"ds_read_b128 v[136:139], v12 offset:40960\n"
"v_mfma_f32_16x16x32_bf16 v[164:167], v[160:163], v[56:59], v[164:167]\n"
"s_waitcnt lgkmcnt(1)\n"
"v_mfma_f32_16x16x32_bf16 v[152:155], v[140:143], v[60:63], v[152:155]\n"
"v_mfma_f32_16x16x32_bf16 v[156:159], v[140:143], v[64:67], v[156:159]\n"
"ds_read_b128 v[144:147], v13 offset:40960\n"
"v_mfma_f32_16x16x32_bf16 v[164:167], v[140:143], v[68:71], v[164:167]\n"
"s_waitcnt lgkmcnt(1)\n"
"v_mfma_f32_16x16x32_bf16 v[152:155], v[136:139], v[72:75], v[152:155]\n"
"v_mfma_f32_16x16x32_bf16 v[156:159], v[136:139], v[76:79], v[156:159]\n"
"v_mov_b32 v140, 0x3f803f80\n"
"v_mov_b32 v141, 0x3f803f80\n"
"v_mfma_f32_16x16x32_bf16 v[164:167], v[136:139], v[80:83], v[164:167]\n"
"s_waitcnt lgkmcnt(0)\n"
"v_mov_b32 v142, 0x3f803f80\n"
"v_mov_b32 v143, 0x3f803f80\n"
"v_mfma_f32_16x16x32_bf16 v[152:155], v[144:147], v[84:87], v[152:155]\n"
"s_waitcnt vmcnt(0)\n"
"s_barrier\n"
"ds_read2st64_b32 v[136:137], v9 offset0:120 offset1:121\n"
"ds_read2st64_b32 v[138:139], v9 offset0:122 offset1:123\n"
"v_mfma_f32_16x16x32_bf16 v[156:159], v[144:147], v[88:91], v[156:159]\n"
"s_and_b32 s41, 416, s55\n"
"v_add_u32 v8, s41, v15\n"
"v_mfma_f32_16x16x32_bf16 v[164:167], v[144:147], v[92:95], v[164:167]\n"
"ds_read_b32 v145, v9 offset:32000\n"
"v_cvt_pk_bf16_f32 v186, v152, v153\n"
"v_cvt_pk_bf16_f32 v187, v154, v155\n"
"ds_read2st64_b32 v[146:147], v9 offset0:126 offset1:127\n"
"ds_read_u16 v8, v8 offset:576\n"
"v_cvt_pk_bf16_f32 v212, v156, v157\n"
"v_cvt_pk_bf16_f32 v213, v158, v159\n"
"s_waitcnt lgkmcnt(3)\n"
"v_mfma_f32_16x16x32_bf16 v[148:151], v[140:143], v[136:139], 0\n"
"v_mov_b32 v144, 0\n"
"v_cvt_pk_bf16_f32 v238, v164, v165\n"
"v_mfma_f32_16x16x32_bf16 v[152:155], v[190:193], v[136:139], 0\n"
"v_cvt_pk_bf16_f32 v239, v166, v167\n"
"v_mfma_f32_16x16x32_bf16 v[190:193], v[216:219], v[136:139], 0\n"
"ds_read_u16 v11, v21 offset:832\n"
"v_mfma_f32_16x16x32_bf16 v[216:219], v[242:245], v[136:139], 0\n"
"s_waitcnt lgkmcnt(1)\n"
"ds_read2st64_b32 v[242:243], v20 offset0:0 offset1:1\n"
"v_mad_u32_u24 v8, v8, s40, v16\n"
"v_mfma_f32_16x16x32_bf16 v[148:151], v[140:143], v[144:147], v[148:151]\n"
"s_add_u32 m0, s51, 114688\n"
"s_nop 0\n"
"global_load_lds_dwordx4 v8, s[56:57]\n"
"ds_read2st64_b32 v[244:245], v20 offset0:2 offset1:3\n"
"v_mfma_f32_16x16x32_bf16 v[152:155], v[182:185], v[144:147], v[152:155]\n"
"ds_read2st64_b32 v[136:137], v9 offset0:128 offset1:129\n"
"v_mov_b32 v138, 0\n"
"v_mfma_f32_16x16x32_bf16 v[190:193], v[208:211], v[144:147], v[190:193]\n"
"ds_read_b32 v139, v9 offset:33536\n"
"v_mfma_f32_16x16x32_bf16 v[216:219], v[234:237], v[144:147], v[216:219]\n"
"s_waitcnt lgkmcnt(4)\n"
"ds_read2st64_b32 v[174:175], v20 offset0:4 offset1:5\n"
"v_mad_u32_u24 v10, v11, s40, v18\n"
"v_mad_u32_u24 v11, v11, s40, v19\n"
"ds_read_b32 v145, v9 offset:34048\n"
"ds_read2st64_b32 v[146:147], v9 offset0:134 offset1:135\n"
"s_waitcnt lgkmcnt(5)\n"
"global_store_dwordx4 v10, v[242:245], s[66:67] sc1\n"
"s_waitcnt lgkmcnt(3)\n"
"v_mfma_f32_16x16x32_bf16 v[148:151], v[140:143], v[136:139], v[148:151]\n"
"s_add_u32 m0, s51, 116736\n"
"s_nop 0\n"
"global_load_lds_dwordx4 v8, s[58:59]\n"
"ds_read2st64_b32 v[244:245], v9 offset0:38 offset1:39\n"
"v_mfma_f32_16x16x32_bf16 v[152:155], v[186:189], v[136:139], v[152:155]\n"
"s_waitcnt lgkmcnt(3)\n"
"global_store_dwordx2 v11, v[174:175], s[66:67] offset:256 sc1\n"
"v_mfma_f32_16x16x32_bf16 v[190:193], v[212:215], v[136:139], v[190:193]\n"
"v_mov_b32 v144, 0\n"
"v_mov_b32 v242, 0\n"
"v_mfma_f32_16x16x32_bf16 v[216:219], v[238:241], v[136:139], v[216:219]\n"
"s_waitcnt lgkmcnt(1)\n"
"v_mov_b32 v243, 0\n"
"v_mfma_f32_16x16x32_bf16 v[148:151], v[140:143], v[144:147], v[148:151]\n"
"s_waitcnt lgkmcnt(0)\n"
"v_mfma_f32_16x16x32_bf16 v[148:151], v[140:143], v[242:245], v[148:151]\n"
"s_add_u32 m0, s51, 118784\n"
"s_nop 0\n"
"global_load_lds_dwordx4 v8, s[60:61]\n"
"v_mfma_f32_16x16x32_bf16 v[216:219], v[250:253], v[144:147], v[216:219]\n"
"v_mfma_f32_16x16x32_bf16 v[216:219], v[252:255], v[242:245], v[216:219]\n"
"ds_read2st64_b32 v[136:137], v14 offset0:126 offset1:127\n"
"v_mfma_f32_16x16x32_bf16 v[152:155], v[198:201], v[144:147], v[152:155]\n"
"ds_read2st64_b32 v[138:139], v14 offset0:128 offset1:129\n"
"v_mfma_f32_16x16x32_bf16 v[152:155], v[200:203], v[242:245], v[152:155]\n"
"ds_read_b128 v[140:143], v17 offset:53248\n"
"v_rcp_f32 v8, v148\n"
"v_mfma_f32_16x16x32_bf16 v[190:193], v[224:227], v[144:147], v[190:193]\n"
"ds_read2st64_b32 v[240:241], v14 offset0:150 offset1:151\n"
"v_mul_f32 v216, v216, v8\n"
"v_mfma_f32_16x16x32_bf16 v[190:193], v[226:229], v[242:245], v[190:193]\n"
"ds_read2st64_b32 v[242:243], v14 offset0:152 offset1:153\n"
"v_mul_f32 v217, v217, v8\n"
"v_cvt_pk_bf16_f32 v244, v216, v217\n"
"v_mul_f32 v218, v218, v8\n"
"v_mul_f32 v219, v219, v8\n"
"v_cvt_pk_bf16_f32 v245, v218, v219\n"
"v_mul_f32 v152, v152, v8\n"
"s_waitcnt lgkmcnt(3)\n"
"v_mfma_f32_16x16x32_bf16 v[214:217], v[96:99], v[136:139], 0\n"
"ds_read_b128 v[144:147], v17 offset:54272\n"
"v_mul_f32 v153, v153, v8\n"
"v_mfma_f32_16x16x32_bf16 v[148:151], v[100:103], v[136:139], 0\n"
"s_waitcnt lgkmcnt(3)\n"
"ds_read2st64_b32 v[156:157], v14 offset0:174 offset1:175\n"
"v_cvt_pk_bf16_f32 v152, v152, v153\n"
"v_mfma_f32_16x16x32_bf16 v[160:163], v[140:143], v[136:139], 0\n"
"s_waitcnt lgkmcnt(2)\n"
"ds_read2st64_b32 v[158:159], v14 offset0:176 offset1:177\n"
"v_mul_f32 v154, v154, v8\n"
"v_mul_f32 v155, v155, v8\n"
"v_mfma_f32_16x16x32_bf16 v[214:217], v[104:107], v[240:243], v[214:217]\n"
"ds_read_b128 v[136:139], v17 offset:55296\n"
"v_cvt_pk_bf16_f32 v153, v154, v155\n"
"v_mfma_f32_16x16x32_bf16 v[148:151], v[108:111], v[240:243], v[148:151]\n"
"s_waitcnt lgkmcnt(3)\n"
"ds_read2st64_b32 v[140:141], v14 offset0:198 offset1:199\n"
"v_mul_f32 v190, v190, v8\n"
"v_mul_f32 v191, v191, v8\n"
"v_mfma_f32_16x16x32_bf16 v[160:163], v[144:147], v[240:243], v[160:163]\n"
"ds_read2st64_b32 v[142:143], v14 offset0:200 offset1:201\n"
"v_cvt_pk_bf16_f32 v154, v190, v191\n"
"v_mul_f32 v192, v192, v8\n"
"v_mul_f32 v193, v193, v8\n"
"v_cvt_pk_bf16_f32 v155, v192, v193\n"
"s_waitcnt lgkmcnt(3)\n"
"v_mfma_f32_16x16x32_bf16 v[214:217], v[0:3], v[156:159], v[214:217]\n"
"ds_read_b128 v[188:191], v17 offset:56320\n"
"v_mfma_f32_16x16x32_bf16 v[148:151], v[112:115], v[156:159], v[148:151]\n"
"s_waitcnt lgkmcnt(3)\n"
"ds_read2st64_b32 v[240:241], v14 offset0:130 offset1:131\n"
"v_mfma_f32_16x16x32_bf16 v[160:163], v[136:139], v[156:159], v[160:163]\n"
"s_waitcnt lgkmcnt(2)\n"
"ds_read2st64_b32 v[242:243], v14 offset0:154 offset1:155\n"
"v_mfma_f32_16x16x32_bf16 v[214:217], v[116:119], v[140:143], v[214:217]\n"
"ds_read_b128 v[136:139], v17 offset:57344\n"
"v_mfma_f32_16x16x32_bf16 v[148:151], v[120:123], v[140:143], v[148:151]\n"
"s_waitcnt lgkmcnt(3)\n"
"ds_read2st64_b32 v[144:145], v14 offset0:178 offset1:179\n"
"v_mfma_f32_16x16x32_bf16 v[160:163], v[188:191], v[140:143], v[160:163]\n"
"s_waitcnt lgkmcnt(2)\n"
"ds_read2st64_b32 v[146:147], v14 offset0:202 offset1:203\n"
"v_mfma_f32_16x16x32_bf16 v[214:217], v[4:7], v[240:243], v[214:217]\n"
"ds_read_b128 v[188:191], v17 offset:58368\n"
"v_mfma_f32_16x16x32_bf16 v[148:151], v[124:127], v[240:243], v[148:151]\n"
"s_waitcnt lgkmcnt(3)\n"
"ds_read_b128 v[140:143], v12 offset:24576\n"
"v_mfma_f32_16x16x32_bf16 v[160:163], v[136:139], v[240:243], v[160:163]\n"
"s_waitcnt lgkmcnt(2)\n"
"v_mfma_f32_16x16x32_bf16 v[214:217], v[128:131], v[144:147], v[214:217]\n"
"v_mfma_f32_16x16x32_bf16 v[148:151], v[132:135], v[144:147], v[148:151]\n"
"s_barrier\n"
"ds_read_b128 v[240:243], v13 offset:24576\n"
"s_mov_b32 m0, s54\n"
"s_waitcnt lgkmcnt(2)\n"
"v_mfma_f32_16x16x32_bf16 v[160:163], v[188:191], v[144:147], v[160:163]\n"
"ds_write_addtid_b32 v244 offset:33280\n"
"s_waitcnt lgkmcnt(2)\n"
"v_mfma_f32_16x16x32_bf16 v[190:193], v[140:143], v[28:31], 0\n"
"ds_write_addtid_b32 v245 offset:33536\n"
"v_cvt_pk_bf16_f32 v214, v214, v215\n"
"v_cvt_pk_bf16_f32 v215, v216, v217\n"
"v_cvt_pk_bf16_f32 v216, v148, v149\n"
"v_cvt_pk_bf16_f32 v217, v150, v151\n"
"v_mfma_f32_16x16x32_bf16 v[136:139], v[140:143], v[32:35], 0\n"
"ds_write_addtid_b32 v152 offset:32256\n"
"v_cvt_pk_bf16_f32 v160, v160, v161\n"
"v_cvt_pk_bf16_f32 v161, v162, v163\n"
"v_mfma_f32_16x16x32_bf16 v[144:147], v[140:143], v[24:27], 0\n"
"ds_write_addtid_b32 v153 offset:32512\n"
"ds_write_addtid_b32 v154 offset:32768\n"
"s_waitcnt lgkmcnt(5)\n"
"v_mfma_f32_16x16x32_bf16 v[190:193], v[240:243], v[40:43], v[190:193]\n"
"ds_write_addtid_b32 v155 offset:33024\n"
"s_mov_b32 m0, s68\n"
"ds_read_b128 v[140:143], v12 offset:26624\n"
"v_mfma_f32_16x16x32_bf16 v[136:139], v[240:243], v[44:47], v[136:139]\n"
"ds_write_addtid_b32 v214 offset:45056\n"
"ds_write_addtid_b32 v215 offset:45312\n"
"v_mfma_f32_16x16x32_bf16 v[144:147], v[240:243], v[36:39], v[144:147]\n"
"ds_write_addtid_b32 v216 offset:45568\n"
"ds_write_addtid_b32 v217 offset:45824\n"
"ds_write_addtid_b32 v160 offset:46080\n"
"ds_write_addtid_b32 v161 offset:46336\n"
"s_and_b32 s41, 416, s55\n"
"v_add_u32 v8, s41, v15\n"
"ds_read_u16 v8, v8 offset:608\n"
"s_waitcnt lgkmcnt(7)\n"
"v_mfma_f32_16x16x32_bf16 v[190:193], v[140:143], v[48:51], v[190:193]\n"
"ds_read_b128 v[214:217], v13 offset:26624\n"
"v_mfma_f32_16x16x32_bf16 v[136:139], v[140:143], v[52:55], v[136:139]\n"
"ds_read_b128 v[240:243], v12 offset:28672\n"
"v_mfma_f32_16x16x32_bf16 v[144:147], v[140:143], v[56:59], v[144:147]\n"
"s_waitcnt lgkmcnt(2)\n"
"v_mad_u32_u24 v8, v8, s40, v16\n"
"s_add_u32 m0, s51, 139264\n"
"s_nop 0\n"
"global_load_lds_dwordx4 v8, s[56:57]\n"
"s_waitcnt lgkmcnt(1)\n"
"v_mfma_f32_16x16x32_bf16 v[190:193], v[214:217], v[60:63], v[190:193]\n"
"v_mfma_f32_16x16x32_bf16 v[136:139], v[214:217], v[64:67], v[136:139]\n"
"ds_read_b128 v[140:143], v13 offset:28672\n"
"v_mfma_f32_16x16x32_bf16 v[144:147], v[214:217], v[68:71], v[144:147]\n"
"s_waitcnt lgkmcnt(1)\n"
"v_mfma_f32_16x16x32_bf16 v[190:193], v[240:243], v[72:75], v[190:193]\n"
"v_mfma_f32_16x16x32_bf16 v[136:139], v[240:243], v[76:79], v[136:139]\n"
"ds_read_b128 v[214:217], v12 offset:30720\n"
"v_mfma_f32_16x16x32_bf16 v[144:147], v[240:243], v[80:83], v[144:147]\n"
"s_add_u32 m0, s51, 141312\n"
"s_nop 0\n"
"global_load_lds_dwordx4 v8, s[58:59]\n"
"s_waitcnt lgkmcnt(1)\n"
"v_mfma_f32_16x16x32_bf16 v[190:193], v[140:143], v[84:87], v[190:193]\n"
"v_mfma_f32_16x16x32_bf16 v[136:139], v[140:143], v[88:91], v[136:139]\n"
"ds_read_b128 v[242:245], v13 offset:30720\n"
"v_mfma_f32_16x16x32_bf16 v[144:147], v[140:143], v[92:95], v[144:147]\n"
"s_waitcnt lgkmcnt(1)\n"
"v_mfma_f32_16x16x32_bf16 v[140:143], v[214:217], v[28:31], 0\n"
"v_mfma_f32_16x16x32_bf16 v[148:151], v[214:217], v[32:35], 0\n"
"ds_read_b128 v[152:155], v12 offset:32768\n"
"s_add_u32 m0, s51, 143360\n"
"s_nop 0\n"
"global_load_lds_dwordx4 v8, s[60:61]\n"
"v_mfma_f32_16x16x32_bf16 v[156:159], v[214:217], v[24:27], 0\n"
"s_waitcnt lgkmcnt(1)\n"
"v_cvt_pk_bf16_f32 v188, v190, v191\n"
"v_cvt_pk_bf16_f32 v189, v192, v193\n"
"v_mfma_f32_16x16x32_bf16 v[140:143], v[242:245], v[40:43], v[140:143]\n"
"v_cvt_pk_bf16_f32 v214, v136, v137\n"
"v_cvt_pk_bf16_f32 v215, v138, v139\n"
"v_mfma_f32_16x16x32_bf16 v[148:151], v[242:245], v[44:47], v[148:151]\n"
"ds_read_b128 v[190:193], v13 offset:32768\n"
"v_cvt_pk_bf16_f32 v240, v144, v145\n"
"v_cvt_pk_bf16_f32 v241, v146, v147\n"
"v_mfma_f32_16x16x32_bf16 v[156:159], v[242:245], v[36:39], v[156:159]\n"
"s_waitcnt lgkmcnt(1)\n"
"v_mfma_f32_16x16x32_bf16 v[140:143], v[152:155], v[48:51], v[140:143]\n"
"v_mfma_f32_16x16x32_bf16 v[148:151], v[152:155], v[52:55], v[148:151]\n"
"ds_read_b128 v[216:219], v12 offset:34816\n"
"v_mfma_f32_16x16x32_bf16 v[156:159], v[152:155], v[56:59], v[156:159]\n"
"s_waitcnt lgkmcnt(1)\n"
"v_mfma_f32_16x16x32_bf16 v[140:143], v[190:193], v[60:63], v[140:143]\n"
"v_mfma_f32_16x16x32_bf16 v[148:151], v[190:193], v[64:67], v[148:151]\n"
"ds_read_b128 v[242:245], v13 offset:34816\n"
"v_mfma_f32_16x16x32_bf16 v[156:159], v[190:193], v[68:71], v[156:159]\n"
"s_waitcnt lgkmcnt(1)\n"
"v_mfma_f32_16x16x32_bf16 v[140:143], v[216:219], v[72:75], v[140:143]\n"
"v_mfma_f32_16x16x32_bf16 v[148:151], v[216:219], v[76:79], v[148:151]\n"
"v_mov_b32 v136, 0x3f803f80\n"
"v_mov_b32 v137, 0x3f803f80\n"
"v_mfma_f32_16x16x32_bf16 v[156:159], v[216:219], v[80:83], v[156:159]\n"
"s_waitcnt lgkmcnt(0)\n"
"v_mov_b32 v138, 0x3f803f80\n"
"v_mov_b32 v139, 0x3f803f80\n"
"v_mfma_f32_16x16x32_bf16 v[140:143], v[242:245], v[84:87], v[140:143]\n"
"s_barrier\n"
"ds_read2st64_b32 v[144:145], v9 offset0:112 offset1:113\n"
"ds_read2st64_b32 v[146:147], v9 offset0:114 offset1:115\n"
"v_mfma_f32_16x16x32_bf16 v[148:151], v[242:245], v[88:91], v[148:151]\n"
"s_andn2_b32 s41, 416, s55\n"
"v_add_u32 v8, s41, v15\n"
"v_mfma_f32_16x16x32_bf16 v[156:159], v[242:245], v[92:95], v[156:159]\n"
"ds_read2st64_b32 v[152:153], v9 offset0:116 offset1:117\n"
"v_cvt_pk_bf16_f32 v190, v140, v141\n"
"v_cvt_pk_bf16_f32 v191, v142, v143\n"
"ds_read2st64_b32 v[154:155], v9 offset0:118 offset1:119\n"
"ds_read_u16 v8, v8 offset:640\n"
"v_cvt_pk_bf16_f32 v216, v148, v149\n"
"v_cvt_pk_bf16_f32 v217, v150, v151\n"
"s_waitcnt lgkmcnt(3)\n"
"v_mfma_f32_16x16x32_bf16 v[140:143], v[136:139], v[144:147], 0\n"
"ds_read2st64_b32 v[162:163], v9 offset0:128 offset1:129\n"
"v_cvt_pk_bf16_f32 v242, v156, v157\n"
"v_mfma_f32_16x16x32_bf16 v[148:151], v[178:181], v[144:147], 0\n"
"ds_read2st64_b32 v[164:165], v9 offset0:130 offset1:131\n"
"v_cvt_pk_bf16_f32 v243, v158, v159\n"
"v_mfma_f32_16x16x32_bf16 v[156:159], v[204:207], v[144:147], 0\n"
"ds_read_u16 v245, v21 offset:864\n"
"v_mfma_f32_16x16x32_bf16 v[168:171], v[230:233], v[144:147], 0\n"
"s_waitcnt lgkmcnt(3)\n"
"ds_read2st64_b32 v[144:145], v20 offset0:0 offset1:1\n"
"v_mad_u32_u24 v8, v8, s40, v16\n"
"v_mfma_f32_16x16x32_bf16 v[140:143], v[136:139], v[152:155], v[140:143]\n"
"s_add_u32 m0, s51, 126976\n"
"s_nop 0\n"
"global_load_lds_dwordx4 v8, s[56:57]\n"
"ds_read2st64_b32 v[146:147], v20 offset0:2 offset1:3\n"
"v_mov_b32 v160, 0\n"
"v_mov_b32 v161, 0\n"
"v_mov_b32 v166, 0\n"
"v_mov_b32 v167, 0\n"
"s_waitcnt lgkmcnt(3)\n"
"v_mfma_f32_16x16x32_bf16 v[140:143], v[136:139], v[162:165], v[140:143]\n"
"v_mfma_f32_16x16x32_bf16 v[168:171], v[246:249], v[152:155], v[168:171]\n"
"s_waitcnt lgkmcnt(2)\n"
"ds_read2st64_b32 v[218:219], v20 offset0:4 offset1:5\n"
"v_mad_u32_u24 v244, v245, s40, v18\n"
"v_mfma_f32_16x16x32_bf16 v[168:171], v[232:235], v[160:163], v[168:171]\n"
"v_mad_u32_u24 v245, v245, s40, v19\n"
"v_mfma_f32_16x16x32_bf16 v[168:171], v[250:253], v[164:167], v[168:171]\n"
"s_waitcnt lgkmcnt(1)\n"
"global_store_dwordx4 v244, v[144:147], s[66:67] sc1\n"
"v_mfma_f32_16x16x32_bf16 v[148:151], v[194:197], v[152:155], v[148:151]\n"
"s_add_u32 m0, s51, 129024\n"
"s_nop 0\n"
"global_load_lds_dwordx4 v8, s[58:59]\n"
"v_mfma_f32_16x16x32_bf16 v[148:151], v[180:183], v[160:163], v[148:151]\n"
"ds_read2st64_b32 v[136:137], v14 offset0:126 offset1:127\n"
"v_mfma_f32_16x16x32_bf16 v[148:151], v[198:201], v[164:167], v[148:151]\n"
"ds_read2st64_b32 v[138:139], v14 offset0:128 offset1:129\n"
"v_rcp_f32 v244, v140\n"
"v_mfma_f32_16x16x32_bf16 v[156:159], v[220:223], v[152:155], v[156:159]\n"
"s_waitcnt lgkmcnt(2)\n"
"global_store_dwordx2 v245, v[218:219], s[66:67] offset:256 sc1\n"
"ds_read_b128 v[140:143], v17 offset:53248\n"
"v_mfma_f32_16x16x32_bf16 v[156:159], v[206:209], v[160:163], v[156:159]\n"
"ds_read2st64_b32 v[144:145], v14 offset0:150 offset1:151\n"
"v_mul_f32 v168, v168, v244\n"
"v_mfma_f32_16x16x32_bf16 v[156:159], v[224:227], v[164:167], v[156:159]\n"
"ds_read2st64_b32 v[146:147], v14 offset0:152 offset1:153\n"
"v_mul_f32 v169, v169, v244\n"
"v_cvt_pk_bf16_f32 v218, v168, v169\n"
"v_mul_f32 v170, v170, v244\n"
"v_mul_f32 v171, v171, v244\n"
"s_waitcnt lgkmcnt(3)\n"
"v_mfma_f32_16x16x32_bf16 v[152:155], v[96:99], v[136:139], 0\n"
"s_add_u32 m0, s51, 131072\n"
"s_nop 0\n"
"global_load_lds_dwordx4 v8, s[60:61]\n"
"ds_read_b128 v[160:163], v17 offset:54272\n"
"v_mfma_f32_16x16x32_bf16 v[164:167], v[100:103], v[136:139], 0\n"
"s_waitcnt lgkmcnt(3)\n"
"ds_read2st64_b32 v[168:169], v14 offset0:174 offset1:175\n"
"v_cvt_pk_bf16_f32 v219, v170, v171\n"
"v_mfma_f32_16x16x32_bf16 v[172:175], v[140:143], v[136:139], 0\n"
"ds_read2st64_b32 v[170:171], v14 offset0:176 offset1:177\n"
"v_mul_f32 v148, v148, v244\n"
"v_mul_f32 v149, v149, v244\n"
"v_cvt_pk_bf16_f32 v148, v148, v149\n"
"s_waitcnt lgkmcnt(3)\n"
"v_mfma_f32_16x16x32_bf16 v[152:155], v[104:107], v[144:147], v[152:155]\n"
"ds_read_b128 v[136:139], v17 offset:55296\n"
"v_mul_f32 v150, v150, v244\n"
"v_mfma_f32_16x16x32_bf16 v[164:167], v[108:111], v[144:147], v[164:167]\n"
"s_waitcnt lgkmcnt(3)\n"
"ds_read2st64_b32 v[140:141], v14 offset0:198 offset1:199\n"
"v_mul_f32 v151, v151, v244\n"
"v_cvt_pk_bf16_f32 v149, v150, v151\n"
"v_mfma_f32_16x16x32_bf16 v[172:175], v[160:163], v[144:147], v[172:175]\n"
"ds_read2st64_b32 v[142:143], v14 offset0:200 offset1:201\n"
"v_mul_f32 v156, v156, v244\n"
"v_mul_f32 v157, v157, v244\n"
"v_cvt_pk_bf16_f32 v150, v156, v157\n"
"v_mul_f32 v158, v158, v244\n"
"s_waitcnt lgkmcnt(3)\n"
"v_mfma_f32_16x16x32_bf16 v[152:155], v[0:3], v[168:171], v[152:155]\n"
"ds_read_b128 v[144:147], v17 offset:56320\n"
"v_mul_f32 v159, v159, v244\n"
"v_mfma_f32_16x16x32_bf16 v[164:167], v[112:115], v[168:171], v[164:167]\n"
"s_waitcnt lgkmcnt(3)\n"
"ds_read2st64_b32 v[156:157], v14 offset0:130 offset1:131\n"
"v_cvt_pk_bf16_f32 v151, v158, v159\n"
"v_mfma_f32_16x16x32_bf16 v[172:175], v[136:139], v[168:171], v[172:175]\n"
"s_waitcnt lgkmcnt(2)\n"
"ds_read2st64_b32 v[158:159], v14 offset0:154 offset1:155\n"
"v_mfma_f32_16x16x32_bf16 v[152:155], v[116:119], v[140:143], v[152:155]\n"
"ds_read_b128 v[136:139], v17 offset:57344\n"
"v_mfma_f32_16x16x32_bf16 v[164:167], v[120:123], v[140:143], v[164:167]\n"
"s_waitcnt lgkmcnt(3)\n"
"ds_read2st64_b32 v[160:161], v14 offset0:178 offset1:179\n"
"v_mfma_f32_16x16x32_bf16 v[172:175], v[144:147], v[140:143], v[172:175]\n"
"s_waitcnt lgkmcnt(2)\n"
"ds_read2st64_b32 v[162:163], v14 offset0:202 offset1:203\n"
"v_mfma_f32_16x16x32_bf16 v[152:155], v[4:7], v[156:159], v[152:155]\n"
"ds_read_b128 v[140:143], v17 offset:58368\n"
"v_mfma_f32_16x16x32_bf16 v[164:167], v[124:127], v[156:159], v[164:167]\n"
"s_waitcnt lgkmcnt(3)\n"
"ds_read_b128 v[144:147], v12 offset:49152\n"
"v_mfma_f32_16x16x32_bf16 v[172:175], v[136:139], v[156:159], v[172:175]\n"
"s_waitcnt lgkmcnt(2)\n"
"v_mfma_f32_16x16x32_bf16 v[152:155], v[128:131], v[160:163], v[152:155]\n"
"v_mfma_f32_16x16x32_bf16 v[164:167], v[132:135], v[160:163], v[164:167]\n"
"s_waitcnt vmcnt(8)\n"
"s_barrier\n"
"ds_read_b128 v[136:139], v13 offset:49152\n"
"s_waitcnt lgkmcnt(2)\n"
"v_mfma_f32_16x16x32_bf16 v[172:175], v[140:143], v[160:163], v[172:175]\n"
"s_waitcnt lgkmcnt(1)\n"
"v_mfma_f32_16x16x32_bf16 v[140:143], v[144:147], v[28:31], 0\n"
"v_mfma_f32_16x16x32_bf16 v[156:159], v[144:147], v[32:35], 0\n"
"ds_read_b128 v[160:163], v12 offset:51200\n"
"v_mfma_f32_16x16x32_bf16 v[168:171], v[144:147], v[24:27], 0\n"
"v_cvt_pk_bf16_f32 v152, v152, v153\n"
"v_cvt_pk_bf16_f32 v153, v154, v155\n"
"v_cvt_pk_bf16_f32 v154, v164, v165\n"
"v_cvt_pk_bf16_f32 v155, v166, v167\n"
"s_waitcnt lgkmcnt(1)\n"
"v_mfma_f32_16x16x32_bf16 v[140:143], v[136:139], v[40:43], v[140:143]\n"
"s_mov_b32 m0, s54\n"
"v_cvt_pk_bf16_f32 v172, v172, v173\n"
"v_cvt_pk_bf16_f32 v173, v174, v175\n"
"v_mfma_f32_16x16x32_bf16 v[156:159], v[136:139], v[44:47], v[156:159]\n"
"ds_read_b128 v[144:147], v13 offset:51200\n"
"v_mfma_f32_16x16x32_bf16 v[168:171], v[136:139], v[36:39], v[168:171]\n"
"ds_write_addtid_b32 v218 offset:33280\n"
"s_waitcnt lgkmcnt(2)\n"
"v_mfma_f32_16x16x32_bf16 v[140:143], v[160:163], v[48:51], v[140:143]\n"
"ds_write_addtid_b32 v219 offset:33536\n"
"v_mfma_f32_16x16x32_bf16 v[156:159], v[160:163], v[52:55], v[156:159]\n"
"ds_write_addtid_b32 v148 offset:32256\n"
"v_mfma_f32_16x16x32_bf16 v[168:171], v[160:163], v[56:59], v[168:171]\n"
"ds_read_b128 v[136:139], v12 offset:53248\n"
"ds_write_addtid_b32 v149 offset:32512\n"
"s_waitcnt lgkmcnt(5)\n"
"v_mfma_f32_16x16x32_bf16 v[140:143], v[144:147], v[60:63], v[140:143]\n"
"ds_write_addtid_b32 v150 offset:32768\n"
"v_mfma_f32_16x16x32_bf16 v[156:159], v[144:147], v[64:67], v[156:159]\n"
"ds_write_addtid_b32 v151 offset:33024\n"
"s_mov_b32 m0, s68\n"
"ds_read_b128 v[148:151], v13 offset:53248\n"
"v_mfma_f32_16x16x32_bf16 v[168:171], v[144:147], v[68:71], v[168:171]\n"
"ds_write_addtid_b32 v152 offset:45056\n"
"ds_write_addtid_b32 v153 offset:45312\n"
"ds_write_addtid_b32 v154 offset:45568\n"
"s_waitcnt lgkmcnt(7)\n"
"v_mfma_f32_16x16x32_bf16 v[140:143], v[136:139], v[72:75], v[140:143]\n"
"ds_write_addtid_b32 v155 offset:45824\n"
"ds_write_addtid_b32 v172 offset:46080\n"
"v_mfma_f32_16x16x32_bf16 v[156:159], v[136:139], v[76:79], v[156:159]\n"
"ds_write_addtid_b32 v173 offset:46336\n"
"v_mov_b32 v144, 0x3f803f80\n"
"v_mfma_f32_16x16x32_bf16 v[168:171], v[136:139], v[80:83], v[168:171]\n"
"s_waitcnt lgkmcnt(6)\n"
"v_mov_b32 v145, 0x3f803f80\n"
"v_mov_b32 v146, 0x3f803f80\n"
"v_mfma_f32_16x16x32_bf16 v[140:143], v[148:151], v[84:87], v[140:143]\n"
"v_mov_b32 v147, 0x3f803f80\n"
"v_mfma_f32_16x16x32_bf16 v[156:159], v[148:151], v[88:91], v[156:159]\n"
"v_mfma_f32_16x16x32_bf16 v[168:171], v[148:151], v[92:95], v[168:171]\n"
"s_waitcnt lgkmcnt(0)\n"
"s_barrier\n"
"ds_read2st64_b32 v[136:137], v9 offset0:120 offset1:121\n"
"ds_read2st64_b32 v[138:139], v9 offset0:122 offset1:123\n"
"s_waitcnt lgkmcnt(0)\n"
"v_mfma_f32_16x16x32_bf16 v[148:151], v[144:147], v[136:139], 0\n"
"ds_read2st64_b32 v[152:153], v9 offset0:124 offset1:125\n"
"v_cvt_pk_bf16_f32 v192, v140, v141\n"
"v_mfma_f32_16x16x32_bf16 v[160:163], v[178:181], v[136:139], 0\n"
"ds_read2st64_b32 v[154:155], v9 offset0:126 offset1:127\n"
"v_cvt_pk_bf16_f32 v193, v142, v143\n"
"v_mfma_f32_16x16x32_bf16 v[140:143], v[204:207], v[136:139], 0\n"
"s_and_b32 s41, 416, s55\n"
"v_add_u32 v8, s41, v15\n"
"v_cvt_pk_bf16_f32 v218, v156, v157\n"
"v_mfma_f32_16x16x32_bf16 v[164:167], v[230:233], v[136:139], 0\n"
"ds_read_u16 v11, v21 offset:896\n"
"v_cvt_pk_bf16_f32 v219, v158, v159\n"
"v_cvt_pk_bf16_f32 v244, v168, v169\n"
"v_cvt_pk_bf16_f32 v245, v170, v171\n"
"ds_read2st64_b32 v[136:137], v20 offset0:0 offset1:1\n"
"ds_read2st64_b32 v[138:139], v20 offset0:2 offset1:3\n"
"ds_read_u16 v8, v8 offset:672\n"
"s_waitcnt lgkmcnt(4)\n"
"v_mfma_f32_16x16x32_bf16 v[148:151], v[144:147], v[152:155], v[148:151]\n"
"ds_read_b32 v156, v9 offset:32768\n"
"v_mfma_f32_16x16x32_bf16 v[160:163], v[188:191], v[152:155], v[160:163]\n"
"ds_read2st64_b32 v[158:159], v9 offset0:130 offset1:131\n"
"v_mov_b32 v157, 0\n"
"v_mfma_f32_16x16x32_bf16 v[140:143], v[214:217], v[152:155], v[140:143]\n"
"s_waitcnt lgkmcnt(5)\n"
"v_mad_u32_u24 v10, v11, s40, v18\n"
"v_mad_u32_u24 v11, v11, s40, v19\n"
"v_mfma_f32_16x16x32_bf16 v[164:167], v[240:243], v[152:155], v[164:167]\n"
"s_waitcnt lgkmcnt(3)\n"
"ds_read2st64_b32 v[174:175], v20 offset0:4 offset1:5\n"
"global_store_dwordx4 v10, v[136:139], s[66:67] sc1\n"
"s_waitcnt lgkmcnt(3)\n"
"v_mad_u32_u24 v8, v8, s40, v16\n"
"s_add_u32 m0, s51, 151552\n"
"s_nop 0\n"
"global_load_lds_dwordx4 v8, s[56:57]\n"
"s_waitcnt lgkmcnt(1)\n"
"v_mfma_f32_16x16x32_bf16 v[148:151], v[144:147], v[156:159], v[148:151]\n"
"ds_read2st64_b32 v[136:137], v9 offset0:132 offset1:133\n"
"v_mfma_f32_16x16x32_bf16 v[160:163], v[192:195], v[156:159], v[160:163]\n"
"ds_read_b32 v138, v9 offset:34304\n"
"v_mov_b32 v139, 0\n"
"v_mfma_f32_16x16x32_bf16 v[140:143], v[218:221], v[156:159], v[140:143]\n"
"ds_read_b32 v154, v9 offset:9728\n"
"v_mov_b32 v155, 0\n"
"v_mfma_f32_16x16x32_bf16 v[164:167], v[244:247], v[156:159], v[164:167]\n"
"s_waitcnt lgkmcnt(3)\n"
"global_store_dwordx2 v11, v[174:175], s[66:67] offset:256 sc1\n"
"v_mov_b32 v152, 0\n"
"v_mov_b32 v153, 0\n"
"s_add_u32 m0, s51, 153600\n"
"s_nop 0\n"
"global_load_lds_dwordx4 v8, s[58:59]\n"
"s_waitcnt lgkmcnt(1)\n"
"v_mfma_f32_16x16x32_bf16 v[148:151], v[144:147], v[136:139], v[148:151]\n"
"s_waitcnt lgkmcnt(0)\n"
"v_mfma_f32_16x16x32_bf16 v[148:151], v[144:147], v[152:155], v[148:151]\n"
"v_mfma_f32_16x16x32_bf16 v[164:167], v[248:251], v[136:139], v[164:167]\n"
"v_mfma_f32_16x16x32_bf16 v[164:167], v[232:235], v[152:155], v[164:167]\n"
"v_mfma_f32_16x16x32_bf16 v[160:163], v[196:199], v[136:139], v[160:163]\n"
"ds_read2st64_b32 v[144:145], v14 offset0:126 offset1:127\n"
"v_mfma_f32_16x16x32_bf16 v[160:163], v[180:183], v[152:155], v[160:163]\n"
"s_add_u32 m0, s51, 155648\n"
"s_nop 0\n"
"global_load_lds_dwordx4 v8, s[60:61]\n"
"ds_read2st64_b32 v[146:147], v14 offset0:128 offset1:129\n"
"v_rcp_f32 v8, v148\n"
"v_mfma_f32_16x16x32_bf16 v[140:143], v[222:225], v[136:139], v[140:143]\n"
"ds_read_b128 v[136:139], v17 offset:53248\n"
"v_mul_f32 v164, v164, v8\n"
"v_mfma_f32_16x16x32_bf16 v[140:143], v[206:209], v[152:155], v[140:143]\n"
"ds_read2st64_b32 v[148:149], v14 offset0:150 offset1:151\n"
"v_mul_f32 v165, v165, v8\n"
"v_cvt_pk_bf16_f32 v246, v164, v165\n"
"v_mul_f32 v166, v166, v8\n"
"v_mul_f32 v167, v167, v8\n"
"v_cvt_pk_bf16_f32 v247, v166, v167\n"
"v_mul_f32 v160, v160, v8\n"
"v_mul_f32 v161, v161, v8\n"
"v_cvt_pk_bf16_f32 v160, v160, v161\n"
"v_mul_f32 v162, v162, v8\n"
"v_mul_f32 v163, v163, v8\n"
"s_waitcnt lgkmcnt(2)\n"
"v_mfma_f32_16x16x32_bf16 v[152:155], v[96:99], v[144:147], 0\n"
"ds_read2st64_b32 v[150:151], v14 offset0:152 offset1:153\n"
"v_cvt_pk_bf16_f32 v161, v162, v163\n"
"v_mfma_f32_16x16x32_bf16 v[156:159], v[100:103], v[144:147], 0\n"
"s_waitcnt lgkmcnt(2)\n"
"ds_read_b128 v[164:167], v17 offset:54272\n"
"v_mul_f32 v140, v140, v8\n"
"v_mfma_f32_16x16x32_bf16 v[168:171], v[136:139], v[144:147], 0\n"
"ds_read2st64_b32 v[136:137], v14 offset0:174 offset1:175\n"
"v_mul_f32 v141, v141, v8\n"
"v_cvt_pk_bf16_f32 v162, v140, v141\n"
"v_mul_f32 v142, v142, v8\n"
"v_mul_f32 v143, v143, v8\n"
"v_cvt_pk_bf16_f32 v163, v142, v143\n"
"ds_read2st64_b32 v[138:139], v14 offset0:176 offset1:177\n"
"s_waitcnt lgkmcnt(3)\n"
"v_mfma_f32_16x16x32_bf16 v[152:155], v[104:107], v[148:151], v[152:155]\n"
"v_mfma_f32_16x16x32_bf16 v[156:159], v[108:111], v[148:151], v[156:159]\n"
"s_waitcnt lgkmcnt(2)\n"
"ds_read_b128 v[140:143], v17 offset:55296\n"
"v_mfma_f32_16x16x32_bf16 v[168:171], v[164:167], v[148:151], v[168:171]\n"
"s_waitcnt lgkmcnt(1)\n"
"ds_read2st64_b32 v[144:145], v14 offset0:198 offset1:199\n"
"ds_read2st64_b32 v[146:147], v14 offset0:200 offset1:201\n"
"v_mfma_f32_16x16x32_bf16 v[152:155], v[0:3], v[136:139], v[152:155]\n"
"v_mfma_f32_16x16x32_bf16 v[156:159], v[112:115], v[136:139], v[156:159]\n"
"s_waitcnt lgkmcnt(2)\n"
"ds_read_b128 v[148:151], v17 offset:56320\n"
"v_mfma_f32_16x16x32_bf16 v[168:171], v[140:143], v[136:139], v[168:171]\n"
"s_waitcnt lgkmcnt(1)\n"
"ds_read2st64_b32 v[136:137], v14 offset0:130 offset1:131\n"
"ds_read2st64_b32 v[138:139], v14 offset0:154 offset1:155\n"
"v_mfma_f32_16x16x32_bf16 v[152:155], v[116:119], v[144:147], v[152:155]\n"
"v_mfma_f32_16x16x32_bf16 v[156:159], v[120:123], v[144:147], v[156:159]\n"
"s_waitcnt lgkmcnt(2)\n"
"ds_read_b128 v[140:143], v17 offset:57344\n"
"v_mfma_f32_16x16x32_bf16 v[168:171], v[148:151], v[144:147], v[168:171]\n"
"s_waitcnt lgkmcnt(1)\n"
"ds_read2st64_b32 v[144:145], v14 offset0:178 offset1:179\n"
"ds_read2st64_b32 v[146:147], v14 offset0:202 offset1:203\n"
"v_mfma_f32_16x16x32_bf16 v[152:155], v[4:7], v[136:139], v[152:155]\n"
"v_mfma_f32_16x16x32_bf16 v[156:159], v[124:127], v[136:139], v[156:159]\n"
"s_waitcnt lgkmcnt(2)\n"
"ds_read_b128 v[148:151], v17 offset:58368\n"
"v_mfma_f32_16x16x32_bf16 v[168:171], v[140:143], v[136:139], v[168:171]\n"
"s_waitcnt lgkmcnt(1)\n"
"ds_read_b128 v[136:139], v12 offset:12288\n"
"v_mfma_f32_16x16x32_bf16 v[152:155], v[128:131], v[144:147], v[152:155]\n"
"v_mfma_f32_16x16x32_bf16 v[156:159], v[132:135], v[144:147], v[156:159]\n"
"s_waitcnt vmcnt(10)\n"
"s_barrier\n"
"s_waitcnt lgkmcnt(1)\n"
"v_mfma_f32_16x16x32_bf16 v[168:171], v[148:151], v[144:147], v[168:171]\n"
"s_waitcnt lgkmcnt(0)\n"
"ds_read_b128 v[140:143], v13 offset:12288\n"
"v_mfma_f32_16x16x32_bf16 v[144:147], v[136:139], v[28:31], 0\n"
"v_mfma_f32_16x16x32_bf16 v[148:151], v[136:139], v[32:35], 0\n"
"v_mfma_f32_16x16x32_bf16 v[164:167], v[136:139], v[24:27], 0\n"
"ds_read_b128 v[136:139], v12 offset:14336\n"
"v_cvt_pk_bf16_f32 v152, v152, v153\n"
"v_cvt_pk_bf16_f32 v153, v154, v155\n"
"v_cvt_pk_bf16_f32 v154, v156, v157\n"
"v_cvt_pk_bf16_f32 v168, v168, v169\n"
"v_cvt_pk_bf16_f32 v155, v158, v159\n"
"v_cvt_pk_bf16_f32 v169, v170, v171\n"
"s_waitcnt lgkmcnt(1)\n"
"v_mfma_f32_16x16x32_bf16 v[144:147], v[140:143], v[40:43], v[144:147]\n"
"s_mov_b32 m0, s54\n"
"v_mfma_f32_16x16x32_bf16 v[148:151], v[140:143], v[44:47], v[148:151]\n"
"ds_write_addtid_b32 v246 offset:33280\n"
"v_mfma_f32_16x16x32_bf16 v[164:167], v[140:143], v[36:39], v[164:167]\n"
"s_waitcnt lgkmcnt(1)\n"
"ds_read_b128 v[140:143], v13 offset:14336\n"
"v_mfma_f32_16x16x32_bf16 v[144:147], v[136:139], v[48:51], v[144:147]\n"
"ds_write_addtid_b32 v247 offset:33536\n"
"v_mfma_f32_16x16x32_bf16 v[148:151], v[136:139], v[52:55], v[148:151]\n"
"ds_write_addtid_b32 v160 offset:32256\n"
"v_mfma_f32_16x16x32_bf16 v[164:167], v[136:139], v[56:59], v[164:167]\n"
"ds_write_addtid_b32 v161 offset:32512\n"
"ds_read_b128 v[136:139], v12 offset:16384\n"
"ds_write_addtid_b32 v162 offset:32768\n"
"ds_write_addtid_b32 v163 offset:33024\n"
"s_waitcnt lgkmcnt(6)\n"
"v_mfma_f32_16x16x32_bf16 v[144:147], v[140:143], v[60:63], v[144:147]\n"
"s_mov_b32 m0, s68\n"
"v_mfma_f32_16x16x32_bf16 v[148:151], v[140:143], v[64:67], v[148:151]\n"
"ds_write_addtid_b32 v152 offset:45056\n"
"v_mfma_f32_16x16x32_bf16 v[164:167], v[140:143], v[68:71], v[164:167]\n"
"ds_read_b128 v[140:143], v13 offset:16384\n"
"ds_write_addtid_b32 v153 offset:45312\n"
"s_waitcnt lgkmcnt(5)\n"
"v_mfma_f32_16x16x32_bf16 v[144:147], v[136:139], v[72:75], v[144:147]\n"
"ds_write_addtid_b32 v154 offset:45568\n"
"v_mfma_f32_16x16x32_bf16 v[148:151], v[136:139], v[76:79], v[148:151]\n"
"ds_write_addtid_b32 v155 offset:45824\n"
"ds_write_addtid_b32 v168 offset:46080\n"
"v_mfma_f32_16x16x32_bf16 v[164:167], v[136:139], v[80:83], v[164:167]\n"
"ds_write_addtid_b32 v169 offset:46336\n"
"v_mov_b32 v136, 0x3f803f80\n"
"v_mov_b32 v137, 0x3f803f80\n"
"v_mov_b32 v138, 0x3f803f80\n"
"v_mov_b32 v139, 0x3f803f80\n"
"s_waitcnt lgkmcnt(5)\n"
"v_mfma_f32_16x16x32_bf16 v[144:147], v[140:143], v[84:87], v[144:147]\n"
"v_mfma_f32_16x16x32_bf16 v[148:151], v[140:143], v[88:91], v[148:151]\n"
"v_mfma_f32_16x16x32_bf16 v[164:167], v[140:143], v[92:95], v[164:167]\n"
"s_waitcnt lgkmcnt(0)\n"
"s_barrier\n"
"ds_read2st64_b32 v[140:141], v9 offset0:112 offset1:113\n"
"ds_read2st64_b32 v[142:143], v9 offset0:114 offset1:115\n"
"s_waitcnt lgkmcnt(0)\n"
"v_mfma_f32_16x16x32_bf16 v[152:155], v[136:139], v[140:143], 0\n"
"ds_read2st64_b32 v[156:157], v9 offset0:116 offset1:117\n"
"v_mfma_f32_16x16x32_bf16 v[160:163], v[180:183], v[140:143], 0\n"
"ds_read2st64_b32 v[158:159], v9 offset0:118 offset1:119\n"
"v_cvt_pk_bf16_f32 v194, v144, v145\n"
"v_mfma_f32_16x16x32_bf16 v[168:171], v[206:209], v[140:143], 0\n"
"s_and_b32 s41, 416, s55\n"
"v_add_u32 v8, s41, v15\n"
"v_cvt_pk_bf16_f32 v195, v146, v147\n"
"v_mfma_f32_16x16x32_bf16 v[144:147], v[232:235], v[140:143], 0\n"
"ds_read_u16 v11, v21 offset:928\n"
"v_cvt_pk_bf16_f32 v220, v148, v149\n"
"v_cvt_pk_bf16_f32 v221, v150, v151\n"
"v_cvt_pk_bf16_f32 v246, v164, v165\n"
"v_cvt_pk_bf16_f32 v247, v166, v167\n"
"ds_read2st64_b32 v[140:141], v20 offset0:0 offset1:1\n"
"ds_read2st64_b32 v[142:143], v20 offset0:2 offset1:3\n"
"ds_read_u16 v8, v8 offset:704\n"
"s_waitcnt lgkmcnt(4)\n"
"v_mfma_f32_16x16x32_bf16 v[152:155], v[136:139], v[156:159], v[152:155]\n"
"ds_read2st64_b32 v[148:149], v9 offset0:128 offset1:129\n"
"v_mfma_f32_16x16x32_bf16 v[160:163], v[190:193], v[156:159], v[160:163]\n"
"ds_read2st64_b32 v[150:151], v9 offset0:130 offset1:131\n"
"v_mfma_f32_16x16x32_bf16 v[168:171], v[216:219], v[156:159], v[168:171]\n"
"s_waitcnt lgkmcnt(5)\n"
"v_mad_u32_u24 v10, v11, s40, v18\n"
"v_mad_u32_u24 v11, v11, s40, v19\n"
"v_mfma_f32_16x16x32_bf16 v[144:147], v[242:245], v[156:159], v[144:147]\n"
"s_waitcnt lgkmcnt(3)\n"
"ds_read2st64_b32 v[174:175], v20 offset0:4 offset1:5\n"
"global_store_dwordx4 v10, v[140:143], s[66:67] sc1\n"
"s_waitcnt lgkmcnt(3)\n"
"v_mad_u32_u24 v8, v8, s40, v16\n"
"s_waitcnt lgkmcnt(1)\n"
"v_mfma_f32_16x16x32_bf16 v[152:155], v[136:139], v[148:151], v[152:155]\n"
"s_add_u32 m0, s51, 114688\n"
"s_nop 0\n"
"global_load_lds_dwordx4 v8, s[56:57]\n"
"ds_read2st64_b32 v[140:141], v9 offset0:132 offset1:133\n"
"v_mfma_f32_16x16x32_bf16 v[160:163], v[194:197], v[148:151], v[160:163]\n"
"ds_read2st64_b32 v[142:143], v9 offset0:134 offset1:135\n"
"v_mfma_f32_16x16x32_bf16 v[168:171], v[220:223], v[148:151], v[168:171]\n"
"ds_read2st64_b32 v[158:159], v9 offset0:36 offset1:37\n"
"v_mov_b32 v156, 0\n"
"v_mfma_f32_16x16x32_bf16 v[144:147], v[246:249], v[148:151], v[144:147]\n"
"s_waitcnt lgkmcnt(3)\n"
"global_store_dwordx2 v11, v[174:175], s[66:67] offset:256 sc1\n"
"v_mov_b32 v157, 0\n"
"s_waitcnt lgkmcnt(1)\n"
"v_mfma_f32_16x16x32_bf16 v[152:155], v[136:139], v[140:143], v[152:155]\n"
"s_add_u32 m0, s51, 116736\n"
"s_nop 0\n"
"global_load_lds_dwordx4 v8, s[58:59]\n"
"s_waitcnt lgkmcnt(0)\n"
"v_mfma_f32_16x16x32_bf16 v[152:155], v[136:139], v[156:159], v[152:155]\n"
"v_mfma_f32_16x16x32_bf16 v[144:147], v[250:253], v[140:143], v[144:147]\n"
"v_mfma_f32_16x16x32_bf16 v[144:147], v[234:237], v[156:159], v[144:147]\n"
"v_mfma_f32_16x16x32_bf16 v[160:163], v[198:201], v[140:143], v[160:163]\n"
"ds_read2st64_b32 v[136:137], v14 offset0:126 offset1:127\n"
"v_mfma_f32_16x16x32_bf16 v[160:163], v[182:185], v[156:159], v[160:163]\n"
"ds_read2st64_b32 v[138:139], v14 offset0:128 offset1:129\n"
"v_mfma_f32_16x16x32_bf16 v[168:171], v[224:227], v[140:143], v[168:171]\n"
"s_add_u32 m0, s51, 118784\n"
"s_nop 0\n"
"global_load_lds_dwordx4 v8, s[60:61]\n"
"v_rcp_f32 v8, v152\n"
"v_mfma_f32_16x16x32_bf16 v[168:171], v[208:211], v[156:159], v[168:171]\n"
"ds_read_b128 v[140:143], v17 offset:53248\n"
"v_mul_f32 v144, v144, v8\n"
"v_mul_f32 v145, v145, v8\n"
"v_cvt_pk_bf16_f32 v248, v144, v145\n"
"v_mul_f32 v146, v146, v8\n"
"v_mul_f32 v147, v147, v8\n"
"v_cvt_pk_bf16_f32 v249, v146, v147\n"
"v_mul_f32 v160, v160, v8\n"
"v_mul_f32 v161, v161, v8\n"
"v_cvt_pk_bf16_f32 v160, v160, v161\n"
"s_waitcnt lgkmcnt(1)\n"
"v_mfma_f32_16x16x32_bf16 v[144:147], v[96:99], v[136:139], 0\n"
"ds_read2st64_b32 v[148:149], v14 offset0:150 offset1:151\n"
"v_mul_f32 v162, v162, v8\n"
"v_mfma_f32_16x16x32_bf16 v[152:155], v[100:103], v[136:139], 0\n"
"ds_read2st64_b32 v[150:151], v14 offset0:152 offset1:153\n"
"v_mul_f32 v163, v163, v8\n"
"v_cvt_pk_bf16_f32 v161, v162, v163\n"
"v_mul_f32 v168, v168, v8\n"
"s_waitcnt lgkmcnt(2)\n"
"v_mfma_f32_16x16x32_bf16 v[156:159], v[140:143], v[136:139], 0\n"
"ds_read_b128 v[136:139], v17 offset:54272\n"
"v_mul_f32 v169, v169, v8\n"
"v_cvt_pk_bf16_f32 v162, v168, v169\n"
"v_mul_f32 v170, v170, v8\n"
"v_mul_f32 v171, v171, v8\n"
"v_cvt_pk_bf16_f32 v163, v170, v171\n"
"ds_read2st64_b32 v[140:141], v14 offset0:174 offset1:175\n"
"ds_read2st64_b32 v[142:143], v14 offset0:176 offset1:177\n"
"s_waitcnt lgkmcnt(3)\n"
"v_mfma_f32_16x16x32_bf16 v[144:147], v[104:107], v[148:151], v[144:147]\n"
"v_mfma_f32_16x16x32_bf16 v[152:155], v[108:111], v[148:151], v[152:155]\n"
"s_waitcnt lgkmcnt(2)\n"
"ds_read_b128 v[164:167], v17 offset:55296\n"
"v_mfma_f32_16x16x32_bf16 v[156:159], v[136:139], v[148:151], v[156:159]\n"
"s_waitcnt lgkmcnt(1)\n"
"ds_read2st64_b32 v[136:137], v14 offset0:198 offset1:199\n"
"ds_read2st64_b32 v[138:139], v14 offset0:200 offset1:201\n"
"v_mfma_f32_16x16x32_bf16 v[144:147], v[0:3], v[140:143], v[144:147]\n"
"v_mfma_f32_16x16x32_bf16 v[152:155], v[112:115], v[140:143], v[152:155]\n"
"s_waitcnt lgkmcnt(2)\n"
"ds_read_b128 v[148:151], v17 offset:56320\n"
"v_mfma_f32_16x16x32_bf16 v[156:159], v[164:167], v[140:143], v[156:159]\n"
"s_waitcnt lgkmcnt(1)\n"
"ds_read2st64_b32 v[140:141], v14 offset0:130 offset1:131\n"
"ds_read2st64_b32 v[142:143], v14 offset0:154 offset1:155\n"
"v_mfma_f32_16x16x32_bf16 v[144:147], v[116:119], v[136:139], v[144:147]\n"
"v_mfma_f32_16x16x32_bf16 v[152:155], v[120:123], v[136:139], v[152:155]\n"
"s_waitcnt lgkmcnt(2)\n"
"ds_read_b128 v[164:167], v17 offset:57344\n"
"v_mfma_f32_16x16x32_bf16 v[156:159], v[148:151], v[136:139], v[156:159]\n"
"s_waitcnt lgkmcnt(1)\n"
"ds_read2st64_b32 v[136:137], v14 offset0:178 offset1:179\n"
"ds_read2st64_b32 v[138:139], v14 offset0:202 offset1:203\n"
"v_mfma_f32_16x16x32_bf16 v[144:147], v[4:7], v[140:143], v[144:147]\n"
"v_mfma_f32_16x16x32_bf16 v[152:155], v[124:127], v[140:143], v[152:155]\n"
"s_waitcnt lgkmcnt(2)\n"
"ds_read_b128 v[148:151], v17 offset:58368\n"
"v_mfma_f32_16x16x32_bf16 v[156:159], v[164:167], v[140:143], v[156:159]\n"
"s_waitcnt lgkmcnt(1)\n"
"ds_read_b128 v[140:143], v12 offset:36864\n"
"v_mfma_f32_16x16x32_bf16 v[144:147], v[128:131], v[136:139], v[144:147]\n"
"v_mfma_f32_16x16x32_bf16 v[152:155], v[132:135], v[136:139], v[152:155]\n"
"s_waitcnt vmcnt(10)\n"
"s_barrier\n"
"s_waitcnt lgkmcnt(1)\n"
"v_mfma_f32_16x16x32_bf16 v[156:159], v[148:151], v[136:139], v[156:159]\n"
"s_waitcnt lgkmcnt(0)\n"
"ds_read_b128 v[136:139], v13 offset:36864\n"
"v_mfma_f32_16x16x32_bf16 v[148:151], v[140:143], v[28:31], 0\n"
"v_mfma_f32_16x16x32_bf16 v[164:167], v[140:143], v[32:35], 0\n"
"v_mfma_f32_16x16x32_bf16 v[168:171], v[140:143], v[24:27], 0\n"
"ds_read_b128 v[140:143], v12 offset:38912\n"
"v_cvt_pk_bf16_f32 v144, v144, v145\n"
"v_cvt_pk_bf16_f32 v145, v146, v147\n"
"v_cvt_pk_bf16_f32 v146, v152, v153\n"
"v_cvt_pk_bf16_f32 v156, v156, v157\n"
"v_cvt_pk_bf16_f32 v147, v154, v155\n"
"v_cvt_pk_bf16_f32 v157, v158, v159\n"
"s_waitcnt lgkmcnt(1)\n"
"v_mfma_f32_16x16x32_bf16 v[148:151], v[136:139], v[40:43], v[148:151]\n"
"s_mov_b32 m0, s54\n"
"v_mfma_f32_16x16x32_bf16 v[164:167], v[136:139], v[44:47], v[164:167]\n"
"ds_write_addtid_b32 v248 offset:33280\n"
"v_mfma_f32_16x16x32_bf16 v[168:171], v[136:139], v[36:39], v[168:171]\n"
"s_waitcnt lgkmcnt(1)\n"
"ds_read_b128 v[136:139], v13 offset:38912\n"
"v_mfma_f32_16x16x32_bf16 v[148:151], v[140:143], v[48:51], v[148:151]\n"
"ds_write_addtid_b32 v249 offset:33536\n"
"v_mfma_f32_16x16x32_bf16 v[164:167], v[140:143], v[52:55], v[164:167]\n"
"ds_write_addtid_b32 v160 offset:32256\n"
"v_mfma_f32_16x16x32_bf16 v[168:171], v[140:143], v[56:59], v[168:171]\n"
"ds_write_addtid_b32 v161 offset:32512\n"
"ds_read_b128 v[140:143], v12 offset:40960\n"
"ds_write_addtid_b32 v162 offset:32768\n"
"ds_write_addtid_b32 v163 offset:33024\n"
"s_waitcnt lgkmcnt(6)\n"
"v_mfma_f32_16x16x32_bf16 v[148:151], v[136:139], v[60:63], v[148:151]\n"
"s_mov_b32 m0, s68\n"
"v_mfma_f32_16x16x32_bf16 v[164:167], v[136:139], v[64:67], v[164:167]\n"
"ds_write_addtid_b32 v144 offset:45056\n"
"v_mfma_f32_16x16x32_bf16 v[168:171], v[136:139], v[68:71], v[168:171]\n"
"ds_read_b128 v[136:139], v13 offset:40960\n"
"ds_write_addtid_b32 v145 offset:45312\n"
"s_waitcnt lgkmcnt(5)\n"
"v_mfma_f32_16x16x32_bf16 v[148:151], v[140:143], v[72:75], v[148:151]\n"
"ds_write_addtid_b32 v146 offset:45568\n"
"v_mfma_f32_16x16x32_bf16 v[164:167], v[140:143], v[76:79], v[164:167]\n"
"ds_write_addtid_b32 v147 offset:45824\n"
"ds_write_addtid_b32 v156 offset:46080\n"
"v_mfma_f32_16x16x32_bf16 v[168:171], v[140:143], v[80:83], v[168:171]\n"
"ds_write_addtid_b32 v157 offset:46336\n"
"v_mov_b32 v140, 0x3f803f80\n"
"v_mov_b32 v141, 0x3f803f80\n"
"v_mov_b32 v142, 0x3f803f80\n"
"v_mov_b32 v143, 0x3f803f80\n"
"s_waitcnt lgkmcnt(5)\n"
"v_mfma_f32_16x16x32_bf16 v[148:151], v[136:139], v[84:87], v[148:151]\n"
"v_mfma_f32_16x16x32_bf16 v[164:167], v[136:139], v[88:91], v[164:167]\n"
"v_mfma_f32_16x16x32_bf16 v[168:171], v[136:139], v[92:95], v[168:171]\n"
"s_waitcnt vmcnt(5)\n"
"s_waitcnt lgkmcnt(0)\n"
"s_barrier\n"
"ds_read2st64_b32 v[136:137], v9 offset0:120 offset1:121\n"
"ds_read2st64_b32 v[138:139], v9 offset0:122 offset1:123\n"
"s_waitcnt lgkmcnt(0)\n"
"v_mfma_f32_16x16x32_bf16 v[144:147], v[140:143], v[136:139], 0\n"
"ds_read_b32 v153, v9 offset:32000\n"
"v_mfma_f32_16x16x32_bf16 v[156:159], v[200:203], v[136:139], 0\n"
"ds_read2st64_b32 v[154:155], v9 offset0:126 offset1:127\n"
"v_cvt_pk_bf16_f32 v196, v148, v149\n"
"v_mfma_f32_16x16x32_bf16 v[200:203], v[226:229], v[136:139], 0\n"
"s_and_b32 s41, 416, s55\n"
"v_add_u32 v8, s41, v15\n"
"v_cvt_pk_bf16_f32 v197, v150, v151\n"
"v_mfma_f32_16x16x32_bf16 v[226:229], v[252:255], v[136:139], 0\n"
"ds_read_u16 v11, v21 offset:960\n"
"v_mov_b32 v152, 0\n"
"v_cvt_pk_bf16_f32 v222, v164, v165\n"
"v_cvt_pk_bf16_f32 v223, v166, v167\n"
"v_cvt_pk_bf16_f32 v248, v168, v169\n"
"v_cvt_pk_bf16_f32 v249, v170, v171\n"
"ds_read2st64_b32 v[252:253], v20 offset0:0 offset1:1\n"
"ds_read2st64_b32 v[254:255], v20 offset0:2 offset1:3\n"
"s_waitcnt lgkmcnt(3)\n"
"v_mfma_f32_16x16x32_bf16 v[144:147], v[140:143], v[152:155], v[144:147]\n"
"ds_read_b32 v137, v9 offset:33024\n"
"v_mfma_f32_16x16x32_bf16 v[156:159], v[182:185], v[152:155], v[156:159]\n"
"ds_read2st64_b32 v[138:139], v9 offset0:130 offset1:131\n"
"v_mov_b32 v136, 0\n"
"v_mfma_f32_16x16x32_bf16 v[200:203], v[208:211], v[152:155], v[200:203]\n"
"s_waitcnt lgkmcnt(4)\n"
"ds_read_u16 v8, v8 offset:736\n"
"v_mad_u32_u24 v10, v11, s40, v18\n"
"v_mfma_f32_16x16x32_bf16 v[226:229], v[234:237], v[152:155], v[226:229]\n"
"s_waitcnt lgkmcnt(3)\n"
"ds_read2st64_b32 v[174:175], v20 offset0:4 offset1:5\n"
"v_mad_u32_u24 v11, v11, s40, v19\n"
"global_store_dwordx4 v10, v[252:255], s[66:67] sc1\n"
"s_waitcnt lgkmcnt(2)\n"
"v_mfma_f32_16x16x32_bf16 v[144:147], v[140:143], v[136:139], v[144:147]\n"
"s_waitcnt lgkmcnt(1)\n"
"ds_read2st64_b32 v[252:253], v9 offset0:132 offset1:133\n"
"v_mad_u32_u24 v8, v8, s40, v16\n"
"v_mfma_f32_16x16x32_bf16 v[156:159], v[192:195], v[136:139], v[156:159]\n"
"s_add_u32 m0, s51, 139264\n"
"s_nop 0\n"
"global_load_lds_dwordx4 v8, s[56:57]\n"
"ds_read_b32 v255, v9 offset:34560\n"
"v_mfma_f32_16x16x32_bf16 v[200:203], v[218:221], v[136:139], v[200:203]\n"
"ds_read2st64_b32 v[150:151], v9 offset0:38 offset1:39\n"
"v_mov_b32 v254, 0\n"
"v_mfma_f32_16x16x32_bf16 v[226:229], v[244:247], v[136:139], v[226:229]\n"
"s_waitcnt lgkmcnt(3)\n"
"global_store_dwordx2 v11, v[174:175], s[66:67] offset:256 sc1\n"
"v_mov_b32 v148, 0\n"
"v_mov_b32 v149, 0\n"
"s_waitcnt lgkmcnt(1)\n"
"v_mfma_f32_16x16x32_bf16 v[144:147], v[140:143], v[252:255], v[144:147]\n"
"s_add_u32 m0, s51, 141312\n"
"s_nop 0\n"
"global_load_lds_dwordx4 v8, s[58:59]\n"
"s_waitcnt lgkmcnt(0)\n"
"v_mfma_f32_16x16x32_bf16 v[144:147], v[140:143], v[148:151], v[144:147]\n"
"v_mfma_f32_16x16x32_bf16 v[226:229], v[248:251], v[252:255], v[226:229]\n"
"v_mfma_f32_16x16x32_bf16 v[226:229], v[236:239], v[148:151], v[226:229]\n"
"v_mfma_f32_16x16x32_bf16 v[156:159], v[196:199], v[252:255], v[156:159]\n"
"ds_read2st64_b32 v[136:137], v14 offset0:126 offset1:127\n"
"v_mfma_f32_16x16x32_bf16 v[156:159], v[184:187], v[148:151], v[156:159]\n"
"ds_read2st64_b32 v[138:139], v14 offset0:128 offset1:129\n"
"v_mfma_f32_16x16x32_bf16 v[200:203], v[222:225], v[252:255], v[200:203]\n"
"s_add_u32 m0, s51, 143360\n"
"s_nop 0\n"
"global_load_lds_dwordx4 v8, s[60:61]\n"
"v_rcp_f32 v8, v144\n"
"v_mfma_f32_16x16x32_bf16 v[200:203], v[210:213], v[148:151], v[200:203]\n"
"ds_read_b128 v[250:253], v17 offset:53248\n"
"v_mul_f32 v226, v226, v8\n"
"v_mul_f32 v227, v227, v8\n"
"v_cvt_pk_bf16_f32 v198, v226, v227\n"
"v_mul_f32 v228, v228, v8\n"
"v_mul_f32 v229, v229, v8\n"
"v_cvt_pk_bf16_f32 v199, v228, v229\n"
"v_mul_f32 v156, v156, v8\n"
"v_mul_f32 v157, v157, v8\n"
"v_cvt_pk_bf16_f32 v156, v156, v157\n"
"s_waitcnt lgkmcnt(1)\n"
"v_mfma_f32_16x16x32_bf16 v[224:227], v[96:99], v[136:139], 0\n"
"ds_read2st64_b32 v[140:141], v14 offset0:150 offset1:151\n"
"v_mul_f32 v158, v158, v8\n"
"v_mfma_f32_16x16x32_bf16 v[144:147], v[100:103], v[136:139], 0\n"
"ds_read2st64_b32 v[142:143], v14 offset0:152 offset1:153\n"
"v_mul_f32 v159, v159, v8\n"
"v_cvt_pk_bf16_f32 v157, v158, v159\n"
"v_mul_f32 v200, v200, v8\n"
"s_waitcnt lgkmcnt(2)\n"
"v_mfma_f32_16x16x32_bf16 v[148:151], v[250:253], v[136:139], 0\n"
"ds_read_b128 v[250:253], v17 offset:54272\n"
"v_mul_f32 v201, v201, v8\n"
"v_cvt_pk_bf16_f32 v158, v200, v201\n"
"v_mul_f32 v202, v202, v8\n"
"v_mul_f32 v203, v203, v8\n"
"v_cvt_pk_bf16_f32 v159, v202, v203\n"
"ds_read2st64_b32 v[200:201], v14 offset0:174 offset1:175\n"
"ds_read2st64_b32 v[202:203], v14 offset0:176 offset1:177\n"
"s_waitcnt lgkmcnt(3)\n"
"v_mfma_f32_16x16x32_bf16 v[224:227], v[104:107], v[140:143], v[224:227]\n"
"v_mfma_f32_16x16x32_bf16 v[144:147], v[108:111], v[140:143], v[144:147]\n"
"s_waitcnt lgkmcnt(2)\n"
"ds_read_b128 v[136:139], v17 offset:55296\n"
"v_mfma_f32_16x16x32_bf16 v[148:151], v[250:253], v[140:143], v[148:151]\n"
"s_waitcnt lgkmcnt(1)\n"
"ds_read2st64_b32 v[250:251], v14 offset0:198 offset1:199\n"
"ds_read2st64_b32 v[252:253], v14 offset0:200 offset1:201\n"
"v_mfma_f32_16x16x32_bf16 v[224:227], v[0:3], v[200:203], v[224:227]\n"
"v_mfma_f32_16x16x32_bf16 v[144:147], v[112:115], v[200:203], v[144:147]\n"
"s_waitcnt lgkmcnt(2)\n"
"ds_read_b128 v[140:143], v17 offset:56320\n"
"v_mfma_f32_16x16x32_bf16 v[148:151], v[136:139], v[200:203], v[148:151]\n"
"s_waitcnt lgkmcnt(1)\n"
"ds_read2st64_b32 v[200:201], v14 offset0:130 offset1:131\n"
"ds_read2st64_b32 v[202:203], v14 offset0:154 offset1:155\n"
"v_mfma_f32_16x16x32_bf16 v[224:227], v[116:119], v[250:253], v[224:227]\n"
"v_mfma_f32_16x16x32_bf16 v[144:147], v[120:123], v[250:253], v[144:147]\n"
"s_waitcnt lgkmcnt(2)\n"
"ds_read_b128 v[136:139], v17 offset:57344\n"
"v_mfma_f32_16x16x32_bf16 v[148:151], v[140:143], v[250:253], v[148:151]\n"
"s_waitcnt lgkmcnt(1)\n"
"ds_read2st64_b32 v[250:251], v14 offset0:178 offset1:179\n"
"ds_read2st64_b32 v[252:253], v14 offset0:202 offset1:203\n"
"v_mfma_f32_16x16x32_bf16 v[224:227], v[4:7], v[200:203], v[224:227]\n"
"v_mfma_f32_16x16x32_bf16 v[144:147], v[124:127], v[200:203], v[144:147]\n"
"s_waitcnt lgkmcnt(2)\n"
"ds_read_b128 v[140:143], v17 offset:58368\n"
"v_mfma_f32_16x16x32_bf16 v[148:151], v[136:139], v[200:203], v[148:151]\n"
"s_waitcnt lgkmcnt(1)\n"
"ds_read_b128 v[200:203], v12 offset:30720\n"
"v_mfma_f32_16x16x32_bf16 v[224:227], v[128:131], v[250:253], v[224:227]\n"
"v_mfma_f32_16x16x32_bf16 v[144:147], v[132:135], v[250:253], v[144:147]\n"
"s_barrier\n"
"s_waitcnt lgkmcnt(1)\n"
"v_mfma_f32_16x16x32_bf16 v[148:151], v[140:143], v[250:253], v[148:151]\n"
"s_waitcnt lgkmcnt(0)\n"
"ds_read_b128 v[250:253], v13 offset:30720\n"
"v_mfma_f32_16x16x32_bf16 v[136:139], v[200:203], v[28:31], 0\n"
"v_mfma_f32_16x16x32_bf16 v[140:143], v[200:203], v[32:35], 0\n"
"v_mfma_f32_16x16x32_bf16 v[152:155], v[200:203], v[24:27], 0\n"
"ds_read_b128 v[200:203], v12 offset:32768\n"
"v_cvt_pk_bf16_f32 v224, v224, v225\n"
"v_cvt_pk_bf16_f32 v225, v226, v227\n"
"v_cvt_pk_bf16_f32 v226, v144, v145\n"
"v_cvt_pk_bf16_f32 v227, v146, v147\n"
"v_cvt_pk_bf16_f32 v148, v148, v149\n"
"v_cvt_pk_bf16_f32 v149, v150, v151\n"
"s_waitcnt lgkmcnt(1)\n"
"v_mfma_f32_16x16x32_bf16 v[136:139], v[250:253], v[40:43], v[136:139]\n"
"s_mov_b32 m0, s54\n"
"v_mfma_f32_16x16x32_bf16 v[140:143], v[250:253], v[44:47], v[140:143]\n"
"ds_write_addtid_b32 v198 offset:33280\n"
"v_mfma_f32_16x16x32_bf16 v[152:155], v[250:253], v[36:39], v[152:155]\n"
"s_waitcnt lgkmcnt(1)\n"
"ds_read_b128 v[250:253], v13 offset:32768\n"
"v_mfma_f32_16x16x32_bf16 v[136:139], v[200:203], v[48:51], v[136:139]\n"
"ds_write_addtid_b32 v199 offset:33536\n"
"v_mfma_f32_16x16x32_bf16 v[140:143], v[200:203], v[52:55], v[140:143]\n"
"ds_write_addtid_b32 v156 offset:32256\n"
"v_mfma_f32_16x16x32_bf16 v[152:155], v[200:203], v[56:59], v[152:155]\n"
"ds_write_addtid_b32 v157 offset:32512\n"
"ds_read_b128 v[198:201], v12 offset:34816\n"
"ds_write_addtid_b32 v158 offset:32768\n"
"ds_write_addtid_b32 v159 offset:33024\n"
"s_waitcnt lgkmcnt(6)\n"
"v_mfma_f32_16x16x32_bf16 v[136:139], v[250:253], v[60:63], v[136:139]\n"
"s_mov_b32 m0, s68\n"
"v_mfma_f32_16x16x32_bf16 v[140:143], v[250:253], v[64:67], v[140:143]\n"
"ds_write_addtid_b32 v224 offset:45056\n"
"v_mfma_f32_16x16x32_bf16 v[152:155], v[250:253], v[68:71], v[152:155]\n"
"ds_read_b128 v[250:253], v13 offset:34816\n"
"ds_write_addtid_b32 v225 offset:45312\n"
"s_waitcnt lgkmcnt(5)\n"
"v_mfma_f32_16x16x32_bf16 v[136:139], v[198:201], v[72:75], v[136:139]\n"
"ds_write_addtid_b32 v226 offset:45568\n"
"v_mfma_f32_16x16x32_bf16 v[140:143], v[198:201], v[76:79], v[140:143]\n"
"ds_write_addtid_b32 v227 offset:45824\n"
"v_mfma_f32_16x16x32_bf16 v[152:155], v[198:201], v[80:83], v[152:155]\n"
"ds_read_b128 v[198:201], v12 offset:49152\n"
"ds_write_addtid_b32 v148 offset:46080\n"
"ds_write_addtid_b32 v149 offset:46336\n"
"s_waitcnt lgkmcnt(6)\n"
"v_mfma_f32_16x16x32_bf16 v[136:139], v[250:253], v[84:87], v[136:139]\n"
"v_mfma_f32_16x16x32_bf16 v[140:143], v[250:253], v[88:91], v[140:143]\n"
"v_mfma_f32_16x16x32_bf16 v[152:155], v[250:253], v[92:95], v[152:155]\n"
"s_waitcnt lgkmcnt(2)\n"
"ds_read_b128 v[226:229], v13 offset:49152\n"
"v_mfma_f32_16x16x32_bf16 v[252:255], v[198:201], v[28:31], 0\n"
"v_mfma_f32_16x16x32_bf16 v[144:147], v[198:201], v[32:35], 0\n"
"v_mfma_f32_16x16x32_bf16 v[148:151], v[198:201], v[24:27], 0\n"
"s_waitcnt lgkmcnt(0)\n"
"ds_read_b128 v[200:203], v12 offset:51200\n"
"v_mfma_f32_16x16x32_bf16 v[252:255], v[226:229], v[40:43], v[252:255]\n"
"v_cvt_pk_bf16_f32 v198, v136, v137\n"
"v_cvt_pk_bf16_f32 v199, v138, v139\n"
"v_cvt_pk_bf16_f32 v224, v140, v141\n"
"v_cvt_pk_bf16_f32 v225, v142, v143\n"
"v_mfma_f32_16x16x32_bf16 v[144:147], v[226:229], v[44:47], v[144:147]\n"
"v_cvt_pk_bf16_f32 v250, v152, v153\n"
"v_cvt_pk_bf16_f32 v251, v154, v155\n"
"v_mfma_f32_16x16x32_bf16 v[148:151], v[226:229], v[36:39], v[148:151]\n"
"s_waitcnt lgkmcnt(0)\n"
"ds_read_b128 v[226:229], v13 offset:51200\n"
"v_mfma_f32_16x16x32_bf16 v[252:255], v[200:203], v[48:51], v[252:255]\n"
"v_mfma_f32_16x16x32_bf16 v[144:147], v[200:203], v[52:55], v[144:147]\n"
"v_mfma_f32_16x16x32_bf16 v[148:151], v[200:203], v[56:59], v[148:151]\n"
"s_waitcnt lgkmcnt(0)\n"
"ds_read_b128 v[200:203], v12 offset:53248\n"
"v_mfma_f32_16x16x32_bf16 v[252:255], v[226:229], v[60:63], v[252:255]\n"
"v_mfma_f32_16x16x32_bf16 v[144:147], v[226:229], v[64:67], v[144:147]\n"
"v_mfma_f32_16x16x32_bf16 v[148:151], v[226:229], v[68:71], v[148:151]\n"
"s_waitcnt lgkmcnt(0)\n"
"ds_read_b128 v[226:229], v13 offset:53248\n"
"v_mfma_f32_16x16x32_bf16 v[252:255], v[200:203], v[72:75], v[252:255]\n"
"v_mfma_f32_16x16x32_bf16 v[144:147], v[200:203], v[76:79], v[144:147]\n"
"v_mfma_f32_16x16x32_bf16 v[148:151], v[200:203], v[80:83], v[148:151]\n"
"v_mov_b32 v136, 0x3f803f80\n"
"v_mov_b32 v137, 0x3f803f80\n"
"v_mov_b32 v138, 0x3f803f80\n"
"v_mov_b32 v139, 0x3f803f80\n"
"s_waitcnt lgkmcnt(0)\n"
"v_mfma_f32_16x16x32_bf16 v[252:255], v[226:229], v[84:87], v[252:255]\n"
"s_waitcnt vmcnt(5)\n"
"s_barrier\n"
"ds_read_b32 v141, v9 offset:28928\n"
"v_mov_b32 v140, 0\n"
"v_mfma_f32_16x16x32_bf16 v[144:147], v[226:229], v[88:91], v[144:147]\n"
"s_and_b32 s41, 416, s55\n"
"v_add_u32 v8, s41, v15\n"
"v_mfma_f32_16x16x32_bf16 v[148:151], v[226:229], v[92:95], v[148:151]\n"
"ds_read2st64_b32 v[142:143], v9 offset0:114 offset1:115\n"
"v_cvt_pk_bf16_f32 v200, v252, v253\n"
"v_cvt_pk_bf16_f32 v201, v254, v255\n"
"ds_read_u16 v8, v8 offset:768\n"
"s_waitcnt lgkmcnt(1)\n"
"v_mfma_f32_16x16x32_bf16 v[152:155], v[136:139], v[140:143], 0\n"
"ds_read2st64_b32 v[156:157], v9 offset0:116 offset1:117\n"
"v_cvt_pk_bf16_f32 v226, v144, v145\n"
"v_mfma_f32_16x16x32_bf16 v[160:163], v[180:183], v[140:143], 0\n"
"ds_read2st64_b32 v[158:159], v9 offset0:118 offset1:119\n"
"v_cvt_pk_bf16_f32 v227, v146, v147\n"
"v_mfma_f32_16x16x32_bf16 v[144:147], v[206:209], v[140:143], 0\n"
"ds_read_u16 v15, v21 offset:992\n"
"v_cvt_pk_bf16_f32 v252, v148, v149\n"
"v_mfma_f32_16x16x32_bf16 v[164:167], v[232:235], v[140:143], 0\n"
"s_waitcnt lgkmcnt(3)\n"
"ds_read2st64_b32 v[140:141], v20 offset0:0 offset1:1\n"
"v_cvt_pk_bf16_f32 v253, v150, v151\n"
"v_mad_u32_u24 v8, v8, s40, v16\n"
"s_add_u32 m0, s51, 126976\n"
"s_nop 0\n"
"global_load_lds_dwordx4 v8, s[56:57]\n"
"ds_read2st64_b32 v[142:143], v20 offset0:2 offset1:3\n"
"ds_read_b32 v149, v9 offset:33024\n"
"ds_read2st64_b32 v[150:151], v9 offset0:130 offset1:131\n"
"s_waitcnt lgkmcnt(5)\n"
"v_mfma_f32_16x16x32_bf16 v[152:155], v[136:139], v[156:159], v[152:155]\n"
"ds_read2st64_b32 v[168:169], v9 offset0:132 offset1:133\n"
"v_mov_b32 v148, 0\n"
"v_mfma_f32_16x16x32_bf16 v[160:163], v[184:187], v[156:159], v[160:163]\n"
"s_waitcnt lgkmcnt(5)\n"
"ds_read2st64_b32 v[170:171], v9 offset0:134 offset1:135\n"
"v_mad_u32_u24 v16, v15, s40, v18\n"
"v_mfma_f32_16x16x32_bf16 v[144:147], v[210:213], v[156:159], v[144:147]\n"
"ds_read2st64_b32 v[254:255], v20 offset0:4 offset1:5\n"
"v_mad_u32_u24 v15, v15, s40, v19\n"
"v_mfma_f32_16x16x32_bf16 v[164:167], v[236:239], v[156:159], v[164:167]\n"
"s_add_u32 m0, s51, 129024\n"
"s_nop 0\n"
"global_load_lds_dwordx4 v8, s[58:59]\n"
"s_waitcnt lgkmcnt(5)\n"
"global_store_dwordx4 v16, v[140:143], s[66:67] sc1\n"
"s_waitcnt lgkmcnt(3)\n"
"v_mfma_f32_16x16x32_bf16 v[152:155], v[136:139], v[148:151], v[152:155]\n"
"s_waitcnt lgkmcnt(1)\n"
"v_mfma_f32_16x16x32_bf16 v[152:155], v[136:139], v[168:171], v[152:155]\n"
"v_mfma_f32_16x16x32_bf16 v[164:167], v[242:245], v[148:151], v[164:167]\n"
"s_waitcnt lgkmcnt(0)\n"
"global_store_dwordx2 v15, v[254:255], s[66:67] offset:256 sc1\n"
"v_mfma_f32_16x16x32_bf16 v[164:167], v[246:249], v[168:171], v[164:167]\n"
"ds_read2st64_b32 v[136:137], v14 offset0:126 offset1:127\n"
"v_mfma_f32_16x16x32_bf16 v[160:163], v[190:193], v[148:151], v[160:163]\n"
"s_add_u32 m0, s51, 131072\n"
"s_nop 0\n"
"global_load_lds_dwordx4 v8, s[60:61]\n"
"ds_read2st64_b32 v[138:139], v14 offset0:128 offset1:129\n"
"v_rcp_f32 v16, v152\n"
"v_mfma_f32_16x16x32_bf16 v[160:163], v[194:197], v[168:171], v[160:163]\n"
"ds_read_b128 v[140:143], v17 offset:53248\n"
"v_mul_f32 v164, v164, v16\n"
"v_mfma_f32_16x16x32_bf16 v[144:147], v[216:219], v[148:151], v[144:147]\n"
"ds_read2st64_b32 v[148:149], v14 offset0:150 offset1:151\n"
"v_mul_f32 v165, v165, v16\n"
"v_mfma_f32_16x16x32_bf16 v[144:147], v[220:223], v[168:171], v[144:147]\n"
"ds_read2st64_b32 v[150:151], v14 offset0:152 offset1:153\n"
"v_cvt_pk_bf16_f32 v254, v164, v165\n"
"v_mul_f32 v166, v166, v16\n"
"v_mul_f32 v167, v167, v16\n"
"v_cvt_pk_bf16_f32 v255, v166, v167\n"
"v_mul_f32 v160, v160, v16\n"
"v_mul_f32 v161, v161, v16\n"
"s_waitcnt lgkmcnt(3)\n"
"v_mfma_f32_16x16x32_bf16 v[152:155], v[96:99], v[136:139], 0\n"
"ds_read_b128 v[156:159], v17 offset:54272\n"
"v_cvt_pk_bf16_f32 v160, v160, v161\n"
"v_mfma_f32_16x16x32_bf16 v[164:167], v[100:103], v[136:139], 0\n"
"s_waitcnt lgkmcnt(3)\n"
"ds_read2st64_b32 v[168:169], v14 offset0:174 offset1:175\n"
"v_mul_f32 v162, v162, v16\n"
"v_mfma_f32_16x16x32_bf16 v[172:175], v[140:143], v[136:139], 0\n"
"s_waitcnt lgkmcnt(2)\n"
"ds_read2st64_b32 v[170:171], v14 offset0:176 offset1:177\n"
"v_mul_f32 v163, v163, v16\n"
"v_cvt_pk_bf16_f32 v161, v162, v163\n"
"v_mfma_f32_16x16x32_bf16 v[152:155], v[104:107], v[148:151], v[152:155]\n"
"ds_read_b128 v[136:139], v17 offset:55296\n"
"v_mul_f32 v144, v144, v16\n"
"v_mfma_f32_16x16x32_bf16 v[164:167], v[108:111], v[148:151], v[164:167]\n"
"s_waitcnt lgkmcnt(3)\n"
"ds_read2st64_b32 v[140:141], v14 offset0:198 offset1:199\n"
"v_mul_f32 v145, v145, v16\n"
"v_cvt_pk_bf16_f32 v162, v144, v145\n"
"v_mfma_f32_16x16x32_bf16 v[172:175], v[156:159], v[148:151], v[172:175]\n"
"ds_read2st64_b32 v[142:143], v14 offset0:200 offset1:201\n"
"v_mul_f32 v146, v146, v16\n"
"v_mul_f32 v147, v147, v16\n"
"v_cvt_pk_bf16_f32 v163, v146, v147\n"
"s_waitcnt lgkmcnt(3)\n"
"v_mfma_f32_16x16x32_bf16 v[152:155], v[0:3], v[168:171], v[152:155]\n"
"ds_read_b128 v[144:147], v17 offset:56320\n"
"v_mfma_f32_16x16x32_bf16 v[164:167], v[112:115], v[168:171], v[164:167]\n"
"s_waitcnt lgkmcnt(3)\n"
"ds_read2st64_b32 v[148:149], v14 offset0:130 offset1:131\n"
"v_mfma_f32_16x16x32_bf16 v[172:175], v[136:139], v[168:171], v[172:175]\n"
"s_waitcnt lgkmcnt(2)\n"
"ds_read2st64_b32 v[150:151], v14 offset0:154 offset1:155\n"
"v_mfma_f32_16x16x32_bf16 v[152:155], v[116:119], v[140:143], v[152:155]\n"
"ds_read_b128 v[136:139], v17 offset:57344\n"
"v_mfma_f32_16x16x32_bf16 v[164:167], v[120:123], v[140:143], v[164:167]\n"
"s_waitcnt lgkmcnt(3)\n"
"ds_read2st64_b32 v[156:157], v14 offset0:178 offset1:179\n"
"v_mfma_f32_16x16x32_bf16 v[172:175], v[144:147], v[140:143], v[172:175]\n"
"s_waitcnt lgkmcnt(2)\n"
"ds_read2st64_b32 v[158:159], v14 offset0:202 offset1:203\n"
"v_mfma_f32_16x16x32_bf16 v[152:155], v[4:7], v[148:151], v[152:155]\n"
"ds_read_b128 v[140:143], v17 offset:58368\n"
"v_mfma_f32_16x16x32_bf16 v[164:167], v[124:127], v[148:151], v[164:167]\n"
"s_waitcnt lgkmcnt(3)\n"
"ds_read_b128 v[144:147], v12 offset:12288\n"
"v_mfma_f32_16x16x32_bf16 v[172:175], v[136:139], v[148:151], v[172:175]\n"
"s_waitcnt lgkmcnt(2)\n"
"v_mfma_f32_16x16x32_bf16 v[152:155], v[128:131], v[156:159], v[152:155]\n"
"v_mfma_f32_16x16x32_bf16 v[164:167], v[132:135], v[156:159], v[164:167]\n"
"s_barrier\n"
"ds_read_b128 v[136:139], v13 offset:12288\n"
"s_waitcnt lgkmcnt(2)\n"
"v_mfma_f32_16x16x32_bf16 v[172:175], v[140:143], v[156:159], v[172:175]\n"
"s_waitcnt lgkmcnt(1)\n"
"v_mfma_f32_16x16x32_bf16 v[140:143], v[144:147], v[28:31], 0\n"
"v_mfma_f32_16x16x32_bf16 v[148:151], v[144:147], v[32:35], 0\n"
"ds_read_b128 v[156:159], v12 offset:14336\n"
"v_mfma_f32_16x16x32_bf16 v[168:171], v[144:147], v[24:27], 0\n"
"v_cvt_pk_bf16_f32 v152, v152, v153\n"
"v_cvt_pk_bf16_f32 v153, v154, v155\n"
"v_cvt_pk_bf16_f32 v154, v164, v165\n"
"v_cvt_pk_bf16_f32 v155, v166, v167\n"
"s_waitcnt lgkmcnt(1)\n"
"v_mfma_f32_16x16x32_bf16 v[140:143], v[136:139], v[40:43], v[140:143]\n"
"s_mov_b32 m0, s54\n"
"v_cvt_pk_bf16_f32 v172, v172, v173\n"
"v_cvt_pk_bf16_f32 v173, v174, v175\n"
"v_mfma_f32_16x16x32_bf16 v[148:151], v[136:139], v[44:47], v[148:151]\n"
"ds_read_b128 v[144:147], v13 offset:14336\n"
"v_mfma_f32_16x16x32_bf16 v[168:171], v[136:139], v[36:39], v[168:171]\n"
"ds_write_addtid_b32 v254 offset:33280\n"
"s_waitcnt lgkmcnt(2)\n"
"v_mfma_f32_16x16x32_bf16 v[140:143], v[156:159], v[48:51], v[140:143]\n"
"ds_write_addtid_b32 v255 offset:33536\n"
"v_mfma_f32_16x16x32_bf16 v[148:151], v[156:159], v[52:55], v[148:151]\n"
"ds_write_addtid_b32 v160 offset:32256\n"
"v_mfma_f32_16x16x32_bf16 v[168:171], v[156:159], v[56:59], v[168:171]\n"
"ds_read_b128 v[136:139], v12 offset:16384\n"
"ds_write_addtid_b32 v161 offset:32512\n"
"s_waitcnt lgkmcnt(5)\n"
"v_mfma_f32_16x16x32_bf16 v[140:143], v[144:147], v[60:63], v[140:143]\n"
"ds_write_addtid_b32 v162 offset:32768\n"
"v_mfma_f32_16x16x32_bf16 v[148:151], v[144:147], v[64:67], v[148:151]\n"
"ds_write_addtid_b32 v163 offset:33024\n"
"s_mov_b32 m0, s68\n"
"ds_read_b128 v[156:159], v13 offset:16384\n"
"v_mfma_f32_16x16x32_bf16 v[168:171], v[144:147], v[68:71], v[168:171]\n"
"ds_write_addtid_b32 v152 offset:45056\n"
"ds_write_addtid_b32 v153 offset:45312\n"
"ds_write_addtid_b32 v154 offset:45568\n"
"s_waitcnt lgkmcnt(7)\n"
"v_mfma_f32_16x16x32_bf16 v[140:143], v[136:139], v[72:75], v[140:143]\n"
"ds_write_addtid_b32 v155 offset:45824\n"
"ds_write_addtid_b32 v172 offset:46080\n"
"v_mfma_f32_16x16x32_bf16 v[148:151], v[136:139], v[76:79], v[148:151]\n"
"ds_write_addtid_b32 v173 offset:46336\n"
"v_mov_b32 v144, 0x3f803f80\n"
"v_mfma_f32_16x16x32_bf16 v[168:171], v[136:139], v[80:83], v[168:171]\n"
"s_waitcnt lgkmcnt(6)\n"
"v_mov_b32 v145, 0x3f803f80\n"
"v_mov_b32 v146, 0x3f803f80\n"
"v_mfma_f32_16x16x32_bf16 v[140:143], v[156:159], v[84:87], v[140:143]\n"
"v_mov_b32 v147, 0x3f803f80\n"
"v_mfma_f32_16x16x32_bf16 v[148:151], v[156:159], v[88:91], v[148:151]\n"
"v_mfma_f32_16x16x32_bf16 v[168:171], v[156:159], v[92:95], v[168:171]\n"
"s_waitcnt vmcnt(5)\n"
"s_waitcnt lgkmcnt(0)\n"
"s_barrier\n"
"ds_read2st64_b32 v[136:137], v9 offset0:120 offset1:121\n"
"ds_read2st64_b32 v[138:139], v9 offset0:122 offset1:123\n"
"s_waitcnt lgkmcnt(0)\n"
"v_mfma_f32_16x16x32_bf16 v[152:155], v[144:147], v[136:139], 0\n"
"ds_read2st64_b32 v[156:157], v9 offset0:124 offset1:125\n"
"v_cvt_pk_bf16_f32 v202, v140, v141\n"
"v_mfma_f32_16x16x32_bf16 v[160:163], v[178:181], v[136:139], 0\n"
"ds_read2st64_b32 v[158:159], v9 offset0:126 offset1:127\n"
"v_cvt_pk_bf16_f32 v203, v142, v143\n"
"v_mfma_f32_16x16x32_bf16 v[140:143], v[204:207], v[136:139], 0\n"
"v_cvt_pk_bf16_f32 v228, v148, v149\n"
"v_cvt_pk_bf16_f32 v229, v150, v151\n"
"v_mfma_f32_16x16x32_bf16 v[148:151], v[230:233], v[136:139], 0\n"
"ds_read_u16 v16, v21 offset:1024\n"
"v_cvt_pk_bf16_f32 v254, v168, v169\n"
"v_cvt_pk_bf16_f32 v255, v170, v171\n"
"ds_read2st64_b32 v[136:137], v20 offset0:0 offset1:1\n"
"ds_read2st64_b32 v[138:139], v20 offset0:2 offset1:3\n"
"s_waitcnt lgkmcnt(3)\n"
"v_mfma_f32_16x16x32_bf16 v[152:155], v[144:147], v[156:159], v[152:155]\n"
"ds_read2st64_b32 v[164:165], v9 offset0:128 offset1:129\n"
"v_mfma_f32_16x16x32_bf16 v[160:163], v[188:191], v[156:159], v[160:163]\n"
"ds_read2st64_b32 v[166:167], v9 offset0:130 offset1:131\n"
"v_mfma_f32_16x16x32_bf16 v[140:143], v[214:217], v[156:159], v[140:143]\n"
"s_waitcnt lgkmcnt(4)\n"
"v_mad_u32_u24 v15, v16, s40, v18\n"
"v_mad_u32_u24 v16, v16, s40, v19\n"
"v_mfma_f32_16x16x32_bf16 v[148:151], v[240:243], v[156:159], v[148:151]\n"
"s_waitcnt lgkmcnt(2)\n"
"ds_read2st64_b32 v[240:241], v20 offset0:4 offset1:5\n"
"global_store_dwordx4 v15, v[136:139], s[66:67] sc1\n"
"s_waitcnt lgkmcnt(1)\n"
"v_mfma_f32_16x16x32_bf16 v[152:155], v[144:147], v[164:167], v[152:155]\n"
"ds_read_b32 v170, v9 offset:33792\n"
"v_mfma_f32_16x16x32_bf16 v[160:163], v[198:201], v[164:167], v[160:163]\n"
"ds_read_b32 v172, v9 offset:34304\n"
"v_mov_b32 v171, 0\n"
"v_mfma_f32_16x16x32_bf16 v[140:143], v[224:227], v[164:167], v[140:143]\n"
"ds_read_b32 v138, v9 offset:9728\n"
"v_mov_b32 v173, 0\n"
"v_mfma_f32_16x16x32_bf16 v[148:151], v[250:253], v[164:167], v[148:151]\n"
"s_waitcnt lgkmcnt(3)\n"
"global_store_dwordx2 v16, v[240:241], s[66:67] offset:256 sc1\n"
"v_mov_b32 v139, 0\n"
"v_mov_b32 v136, 0\n"
"v_mov_b32 v137, 0\n"
"v_mov_b32 v168, 0\n"
"v_mov_b32 v169, 0\n"
"s_waitcnt lgkmcnt(1)\n"
"v_mfma_f32_16x16x32_bf16 v[152:155], v[144:147], v[170:173], v[152:155]\n"
"s_waitcnt lgkmcnt(0)\n"
"v_mov_b32 v174, 0\n"
"v_mov_b32 v175, 0\n"
"v_mfma_f32_16x16x32_bf16 v[152:155], v[144:147], v[136:139], v[152:155]\n"
"v_mfma_f32_16x16x32_bf16 v[148:151], v[232:235], v[168:171], v[148:151]\n"
"v_mfma_f32_16x16x32_bf16 v[148:151], v[244:247], v[172:175], v[148:151]\n"
"v_mfma_f32_16x16x32_bf16 v[148:151], v[252:255], v[136:139], v[148:151]\n"
"v_mfma_f32_16x16x32_bf16 v[160:163], v[180:183], v[168:171], v[160:163]\n"
"v_mfma_f32_16x16x32_bf16 v[160:163], v[192:195], v[172:175], v[160:163]\n"
"v_mfma_f32_16x16x32_bf16 v[160:163], v[200:203], v[136:139], v[160:163]\n"
"ds_read2st64_b32 v[144:145], v14 offset0:126 offset1:127\n"
"v_mfma_f32_16x16x32_bf16 v[140:143], v[206:209], v[168:171], v[140:143]\n"
"ds_read2st64_b32 v[146:147], v14 offset0:128 offset1:129\n"
"v_rcp_f32 v16, v152\n"
"v_mfma_f32_16x16x32_bf16 v[140:143], v[218:221], v[172:175], v[140:143]\n"
"ds_read_b128 v[152:155], v17 offset:53248\n"
"v_mul_f32 v148, v148, v16\n"
"v_mfma_f32_16x16x32_bf16 v[140:143], v[226:229], v[136:139], v[140:143]\n"
"ds_read2st64_b32 v[136:137], v14 offset0:150 offset1:151\n"
"v_mul_f32 v149, v149, v16\n"
"v_cvt_pk_bf16_f32 v240, v148, v149\n"
"v_mul_f32 v150, v150, v16\n"
"v_mul_f32 v151, v151, v16\n"
"v_cvt_pk_bf16_f32 v241, v150, v151\n"
"v_mul_f32 v160, v160, v16\n"
"v_mul_f32 v161, v161, v16\n"
"v_cvt_pk_bf16_f32 v160, v160, v161\n"
"v_mul_f32 v162, v162, v16\n"
"v_mul_f32 v163, v163, v16\n"
"s_waitcnt lgkmcnt(2)\n"
"v_mfma_f32_16x16x32_bf16 v[148:151], v[96:99], v[144:147], 0\n"
"ds_read2st64_b32 v[138:139], v14 offset0:152 offset1:153\n"
"v_cvt_pk_bf16_f32 v161, v162, v163\n"
"v_mfma_f32_16x16x32_bf16 v[156:159], v[100:103], v[144:147], 0\n"
"s_waitcnt lgkmcnt(2)\n"
"ds_read_b128 v[164:167], v17 offset:54272\n"
"v_mul_f32 v140, v140, v16\n"
"v_mfma_f32_16x16x32_bf16 v[168:171], v[152:155], v[144:147], 0\n"
"ds_read2st64_b32 v[144:145], v14 offset0:174 offset1:175\n"
"v_mul_f32 v141, v141, v16\n"
"v_cvt_pk_bf16_f32 v162, v140, v141\n"
"v_mul_f32 v142, v142, v16\n"
"v_mul_f32 v143, v143, v16\n"
"v_cvt_pk_bf16_f32 v163, v142, v143\n"
"ds_read2st64_b32 v[146:147], v14 offset0:176 offset1:177\n"
"s_waitcnt lgkmcnt(3)\n"
"v_mfma_f32_16x16x32_bf16 v[148:151], v[104:107], v[136:139], v[148:151]\n"
"v_mfma_f32_16x16x32_bf16 v[156:159], v[108:111], v[136:139], v[156:159]\n"
"s_waitcnt lgkmcnt(2)\n"
"ds_read_b128 v[140:143], v17 offset:55296\n"
"v_mfma_f32_16x16x32_bf16 v[168:171], v[164:167], v[136:139], v[168:171]\n"
"s_waitcnt lgkmcnt(1)\n"
"ds_read2st64_b32 v[136:137], v14 offset0:198 offset1:199\n"
"ds_read2st64_b32 v[138:139], v14 offset0:200 offset1:201\n"
"v_mfma_f32_16x16x32_bf16 v[148:151], v[0:3], v[144:147], v[148:151]\n"
"v_mfma_f32_16x16x32_bf16 v[156:159], v[112:115], v[144:147], v[156:159]\n"
"s_waitcnt lgkmcnt(2)\n"
"ds_read_b128 v[152:155], v17 offset:56320\n"
"v_mfma_f32_16x16x32_bf16 v[168:171], v[140:143], v[144:147], v[168:171]\n"
"s_waitcnt lgkmcnt(1)\n"
"ds_read2st64_b32 v[140:141], v14 offset0:130 offset1:131\n"
"ds_read2st64_b32 v[142:143], v14 offset0:154 offset1:155\n"
"v_mfma_f32_16x16x32_bf16 v[148:151], v[116:119], v[136:139], v[148:151]\n"
"v_mfma_f32_16x16x32_bf16 v[156:159], v[120:123], v[136:139], v[156:159]\n"
"s_waitcnt lgkmcnt(2)\n"
"ds_read_b128 v[144:147], v17 offset:57344\n"
"v_mfma_f32_16x16x32_bf16 v[168:171], v[152:155], v[136:139], v[168:171]\n"
"s_waitcnt lgkmcnt(1)\n"
"ds_read2st64_b32 v[136:137], v14 offset0:178 offset1:179\n"
"ds_read2st64_b32 v[138:139], v14 offset0:202 offset1:203\n"
"v_mfma_f32_16x16x32_bf16 v[148:151], v[4:7], v[140:143], v[148:151]\n"
"v_mfma_f32_16x16x32_bf16 v[156:159], v[124:127], v[140:143], v[156:159]\n"
"s_waitcnt lgkmcnt(2)\n"
"ds_read_b128 v[152:155], v17 offset:58368\n"
"v_mfma_f32_16x16x32_bf16 v[168:171], v[144:147], v[140:143], v[168:171]\n"
"s_waitcnt lgkmcnt(1)\n"
"ds_read_b128 v[140:143], v12 offset:36864\n"
"v_mfma_f32_16x16x32_bf16 v[148:151], v[128:131], v[136:139], v[148:151]\n"
"v_mfma_f32_16x16x32_bf16 v[156:159], v[132:135], v[136:139], v[156:159]\n"
"s_barrier\n"
"s_waitcnt lgkmcnt(1)\n"
"v_mfma_f32_16x16x32_bf16 v[168:171], v[152:155], v[136:139], v[168:171]\n"
"s_waitcnt lgkmcnt(0)\n"
"ds_read_b128 v[136:139], v13 offset:36864\n"
"v_mfma_f32_16x16x32_bf16 v[144:147], v[140:143], v[28:31], 0\n"
"v_mfma_f32_16x16x32_bf16 v[152:155], v[140:143], v[32:35], 0\n"
"v_mfma_f32_16x16x32_bf16 v[164:167], v[140:143], v[24:27], 0\n"
"ds_read_b128 v[140:143], v12 offset:38912\n"
"v_cvt_pk_bf16_f32 v148, v148, v149\n"
"v_cvt_pk_bf16_f32 v149, v150, v151\n"
"v_cvt_pk_bf16_f32 v150, v156, v157\n"
"v_cvt_pk_bf16_f32 v168, v168, v169\n"
"v_cvt_pk_bf16_f32 v151, v158, v159\n"
"v_cvt_pk_bf16_f32 v169, v170, v171\n"
"s_waitcnt lgkmcnt(1)\n"
"v_mfma_f32_16x16x32_bf16 v[144:147], v[136:139], v[40:43], v[144:147]\n"
"s_mov_b32 m0, s54\n"
"v_mfma_f32_16x16x32_bf16 v[152:155], v[136:139], v[44:47], v[152:155]\n"
"ds_write_addtid_b32 v240 offset:33280\n"
"v_mfma_f32_16x16x32_bf16 v[164:167], v[136:139], v[36:39], v[164:167]\n"
"s_waitcnt lgkmcnt(1)\n"
"ds_read_b128 v[136:139], v13 offset:38912\n"
"v_mfma_f32_16x16x32_bf16 v[144:147], v[140:143], v[48:51], v[144:147]\n"
"ds_write_addtid_b32 v241 offset:33536\n"
"v_mfma_f32_16x16x32_bf16 v[152:155], v[140:143], v[52:55], v[152:155]\n"
"ds_write_addtid_b32 v160 offset:32256\n"
"v_mfma_f32_16x16x32_bf16 v[164:167], v[140:143], v[56:59], v[164:167]\n"
"ds_write_addtid_b32 v161 offset:32512\n"
"ds_read_b128 v[140:143], v12 offset:40960\n"
"ds_write_addtid_b32 v162 offset:32768\n"
"ds_write_addtid_b32 v163 offset:33024\n"
"s_waitcnt lgkmcnt(6)\n"
"v_mfma_f32_16x16x32_bf16 v[144:147], v[136:139], v[60:63], v[144:147]\n"
"s_mov_b32 m0, s68\n"
"v_mfma_f32_16x16x32_bf16 v[152:155], v[136:139], v[64:67], v[152:155]\n"
"ds_write_addtid_b32 v148 offset:45056\n"
"v_mfma_f32_16x16x32_bf16 v[164:167], v[136:139], v[68:71], v[164:167]\n"
"ds_read_b128 v[136:139], v13 offset:40960\n"
"ds_write_addtid_b32 v149 offset:45312\n"
"s_waitcnt lgkmcnt(5)\n"
"v_mfma_f32_16x16x32_bf16 v[144:147], v[140:143], v[72:75], v[144:147]\n"
"ds_write_addtid_b32 v150 offset:45568\n"
"v_mfma_f32_16x16x32_bf16 v[152:155], v[140:143], v[76:79], v[152:155]\n"
"ds_write_addtid_b32 v151 offset:45824\n"
"ds_write_addtid_b32 v168 offset:46080\n"
"v_mfma_f32_16x16x32_bf16 v[164:167], v[140:143], v[80:83], v[164:167]\n"
"ds_write_addtid_b32 v169 offset:46336\n"
"v_mov_b32 v140, 0x3f803f80\n"
"v_mov_b32 v141, 0x3f803f80\n"
"v_mov_b32 v142, 0x3f803f80\n"
"v_mov_b32 v143, 0x3f803f80\n"
"s_waitcnt lgkmcnt(5)\n"
"v_mfma_f32_16x16x32_bf16 v[144:147], v[136:139], v[84:87], v[144:147]\n"
"v_mfma_f32_16x16x32_bf16 v[152:155], v[136:139], v[88:91], v[152:155]\n"
"v_mfma_f32_16x16x32_bf16 v[164:167], v[136:139], v[92:95], v[164:167]\n"
"s_waitcnt vmcnt(2)\n"
"s_waitcnt lgkmcnt(0)\n"
"s_barrier\n"
"ds_read2st64_b32 v[136:137], v9 offset0:112 offset1:113\n"
"ds_read2st64_b32 v[138:139], v9 offset0:114 offset1:115\n"
"s_waitcnt lgkmcnt(0)\n"
"v_mfma_f32_16x16x32_bf16 v[148:151], v[140:143], v[136:139], 0\n"
"ds_read2st64_b32 v[156:157], v9 offset0:116 offset1:117\n"
"v_mfma_f32_16x16x32_bf16 v[160:163], v[182:185], v[136:139], 0\n"
"ds_read2st64_b32 v[158:159], v9 offset0:118 offset1:119\n"
"v_cvt_pk_bf16_f32 v178, v144, v145\n"
"v_mfma_f32_16x16x32_bf16 v[168:171], v[208:211], v[136:139], 0\n"
"ds_read_u16 v16, v21 offset:1056\n"
"v_cvt_pk_bf16_f32 v179, v146, v147\n"
"v_mfma_f32_16x16x32_bf16 v[144:147], v[234:237], v[136:139], 0\n"
"ds_read2st64_b32 v[136:137], v20 offset0:0 offset1:1\n"
"v_cvt_pk_bf16_f32 v204, v152, v153\n"
"v_cvt_pk_bf16_f32 v205, v154, v155\n"
"v_cvt_pk_bf16_f32 v230, v164, v165\n"
"v_cvt_pk_bf16_f32 v231, v166, v167\n"
"ds_read2st64_b32 v[138:139], v20 offset0:2 offset1:3\n"
"ds_read2st64_b32 v[152:153], v9 offset0:128 offset1:129\n"
"ds_read2st64_b32 v[154:155], v9 offset0:130 offset1:131\n"
"s_waitcnt lgkmcnt(5)\n"
"v_mfma_f32_16x16x32_bf16 v[148:151], v[140:143], v[156:159], v[148:151]\n"
"v_mfma_f32_16x16x32_bf16 v[160:163], v[190:193], v[156:159], v[160:163]\n"
"s_waitcnt lgkmcnt(4)\n"
"v_mad_u32_u24 v15, v16, s40, v18\n"
"v_mad_u32_u24 v16, v16, s40, v19\n"
"v_mfma_f32_16x16x32_bf16 v[168:171], v[216:219], v[156:159], v[168:171]\n"
"ds_read2st64_b32 v[240:241], v20 offset0:4 offset1:5\n"
"v_mfma_f32_16x16x32_bf16 v[144:147], v[242:245], v[156:159], v[144:147]\n"
"s_waitcnt lgkmcnt(1)\n"
"ds_read2st64_b32 v[156:157], v9 offset0:132 offset1:133\n"
"ds_read2st64_b32 v[158:159], v9 offset0:134 offset1:135\n"
"v_mfma_f32_16x16x32_bf16 v[148:151], v[140:143], v[152:155], v[148:151]\n"
"global_store_dwordx4 v15, v[136:139], s[66:67] sc1\n"
"s_nop 1\n"
"ds_read2st64_b32 v[138:139], v9 offset0:36 offset1:37\n"
"v_mfma_f32_16x16x32_bf16 v[160:163], v[178:181], v[152:155], v[160:163]\n"
"v_mov_b32 v136, 0\n"
"v_mov_b32 v137, 0\n"
"v_mfma_f32_16x16x32_bf16 v[168:171], v[204:207], v[152:155], v[168:171]\n"
"s_waitcnt lgkmcnt(3)\n"
"global_store_dwordx2 v16, v[240:241], s[66:67] offset:256 sc1\n"
"v_mfma_f32_16x16x32_bf16 v[144:147], v[230:233], v[152:155], v[144:147]\n"
"s_waitcnt lgkmcnt(1)\n"
"v_mfma_f32_16x16x32_bf16 v[148:151], v[140:143], v[156:159], v[148:151]\n"
"s_waitcnt lgkmcnt(0)\n"
"v_mfma_f32_16x16x32_bf16 v[148:151], v[140:143], v[136:139], v[148:151]\n"
"v_mfma_f32_16x16x32_bf16 v[144:147], v[252:255], v[156:159], v[144:147]\n"
"v_mfma_f32_16x16x32_bf16 v[144:147], v[244:247], v[136:139], v[144:147]\n"
"ds_read2st64_b32 v[140:141], v14 offset0:126 offset1:127\n"
"v_mfma_f32_16x16x32_bf16 v[160:163], v[200:203], v[156:159], v[160:163]\n"
"ds_read2st64_b32 v[142:143], v14 offset0:128 offset1:129\n"
"v_mfma_f32_16x16x32_bf16 v[160:163], v[192:195], v[136:139], v[160:163]\n"
"ds_read_b128 v[152:155], v17 offset:53248\n"
"v_mfma_f32_16x16x32_bf16 v[168:171], v[226:229], v[156:159], v[168:171]\n"
"ds_read2st64_b32 v[156:157], v14 offset0:150 offset1:151\n"
"v_rcp_f32 v16, v148\n"
"v_mfma_f32_16x16x32_bf16 v[168:171], v[218:221], v[136:139], v[168:171]\n"
"ds_read2st64_b32 v[158:159], v14 offset0:152 offset1:153\n"
"v_mul_f32 v144, v144, v16\n"
"v_mul_f32 v145, v145, v16\n"
"v_cvt_pk_bf16_f32 v240, v144, v145\n"
"v_mul_f32 v146, v146, v16\n"
"v_mul_f32 v147, v147, v16\n"
"v_cvt_pk_bf16_f32 v241, v146, v147\n"
"s_waitcnt lgkmcnt(3)\n"
"v_mfma_f32_16x16x32_bf16 v[136:139], v[96:99], v[140:143], 0\n"
"ds_read_b128 v[144:147], v17 offset:54272\n"
"v_mul_f32 v160, v160, v16\n"
"v_mfma_f32_16x16x32_bf16 v[148:151], v[100:103], v[140:143], 0\n"
"s_waitcnt lgkmcnt(3)\n"
"ds_read2st64_b32 v[164:165], v14 offset0:174 offset1:175\n"
"v_mul_f32 v161, v161, v16\n"
"v_mfma_f32_16x16x32_bf16 v[172:175], v[152:155], v[140:143], 0\n"
"s_waitcnt lgkmcnt(2)\n"
"ds_read2st64_b32 v[166:167], v14 offset0:176 offset1:177\n"
"v_cvt_pk_bf16_f32 v160, v160, v161\n"
"v_mul_f32 v162, v162, v16\n"
"v_mfma_f32_16x16x32_bf16 v[136:139], v[104:107], v[156:159], v[136:139]\n"
"ds_read_b128 v[140:143], v17 offset:55296\n"
"v_mul_f32 v163, v163, v16\n"
"v_mfma_f32_16x16x32_bf16 v[148:151], v[108:111], v[156:159], v[148:151]\n"
"s_waitcnt lgkmcnt(3)\n"
"ds_read2st64_b32 v[152:153], v14 offset0:198 offset1:199\n"
"v_cvt_pk_bf16_f32 v161, v162, v163\n"
"v_mul_f32 v168, v168, v16\n"
"v_mfma_f32_16x16x32_bf16 v[172:175], v[144:147], v[156:159], v[172:175]\n"
"ds_read2st64_b32 v[154:155], v14 offset0:200 offset1:201\n"
"v_mul_f32 v169, v169, v16\n"
"v_cvt_pk_bf16_f32 v162, v168, v169\n"
"v_mul_f32 v170, v170, v16\n"
"v_mul_f32 v171, v171, v16\n"
"v_cvt_pk_bf16_f32 v163, v170, v171\n"
"s_waitcnt lgkmcnt(3)\n"
"v_mfma_f32_16x16x32_bf16 v[136:139], v[0:3], v[164:167], v[136:139]\n"
"ds_read_b128 v[144:147], v17 offset:56320\n"
"v_mfma_f32_16x16x32_bf16 v[148:151], v[112:115], v[164:167], v[148:151]\n"
"s_waitcnt lgkmcnt(3)\n"
"ds_read2st64_b32 v[156:157], v14 offset0:130 offset1:131\n"
"v_mfma_f32_16x16x32_bf16 v[172:175], v[140:143], v[164:167], v[172:175]\n"
"s_waitcnt lgkmcnt(2)\n"
"ds_read2st64_b32 v[158:159], v14 offset0:154 offset1:155\n"
"v_mfma_f32_16x16x32_bf16 v[136:139], v[116:119], v[152:155], v[136:139]\n"
"ds_read_b128 v[140:143], v17 offset:57344\n"
"v_mfma_f32_16x16x32_bf16 v[148:151], v[120:123], v[152:155], v[148:151]\n"
"s_waitcnt lgkmcnt(3)\n"
"ds_read2st64_b32 v[164:165], v14 offset0:178 offset1:179\n"
"v_mfma_f32_16x16x32_bf16 v[172:175], v[144:147], v[152:155], v[172:175]\n"
"s_waitcnt lgkmcnt(2)\n"
"ds_read2st64_b32 v[166:167], v14 offset0:202 offset1:203\n"
"v_mfma_f32_16x16x32_bf16 v[136:139], v[4:7], v[156:159], v[136:139]\n"
"ds_read_b128 v[144:147], v17 offset:58368\n"
"v_mfma_f32_16x16x32_bf16 v[148:151], v[124:127], v[156:159], v[148:151]\n"
"s_waitcnt lgkmcnt(3)\n"
"ds_read_b128 v[152:155], v12 offset:24576\n"
"v_mfma_f32_16x16x32_bf16 v[172:175], v[140:143], v[156:159], v[172:175]\n"
"s_waitcnt lgkmcnt(2)\n"
"v_mfma_f32_16x16x32_bf16 v[136:139], v[128:131], v[164:167], v[136:139]\n"
"v_mfma_f32_16x16x32_bf16 v[148:151], v[132:135], v[164:167], v[148:151]\n"
"s_barrier\n"
"ds_read_b128 v[140:143], v13 offset:24576\n"
"s_waitcnt lgkmcnt(2)\n"
"v_mfma_f32_16x16x32_bf16 v[172:175], v[144:147], v[164:167], v[172:175]\n"
"s_waitcnt lgkmcnt(1)\n"
"v_mfma_f32_16x16x32_bf16 v[144:147], v[152:155], v[28:31], 0\n"
"v_mfma_f32_16x16x32_bf16 v[28:31], v[152:155], v[32:35], 0\n"
"ds_read_b128 v[32:35], v12 offset:26624\n"
"v_mfma_f32_16x16x32_bf16 v[156:159], v[152:155], v[24:27], 0\n"
"v_cvt_pk_bf16_f32 v136, v136, v137\n"
"v_cvt_pk_bf16_f32 v137, v138, v139\n"
"v_cvt_pk_bf16_f32 v138, v148, v149\n"
"v_cvt_pk_bf16_f32 v139, v150, v151\n"
"s_waitcnt lgkmcnt(1)\n"
"v_mfma_f32_16x16x32_bf16 v[144:147], v[140:143], v[40:43], v[144:147]\n"
"s_mov_b32 m0, s54\n"
"v_cvt_pk_bf16_f32 v172, v172, v173\n"
"v_cvt_pk_bf16_f32 v173, v174, v175\n"
"v_mfma_f32_16x16x32_bf16 v[28:31], v[140:143], v[44:47], v[28:31]\n"
"ds_read_b128 v[24:27], v13 offset:26624\n"
"v_mfma_f32_16x16x32_bf16 v[156:159], v[140:143], v[36:39], v[156:159]\n"
"ds_write_addtid_b32 v240 offset:33280\n"
"s_waitcnt lgkmcnt(2)\n"
"v_mfma_f32_16x16x32_bf16 v[144:147], v[32:35], v[48:51], v[144:147]\n"
"ds_write_addtid_b32 v241 offset:33536\n"
"v_mfma_f32_16x16x32_bf16 v[28:31], v[32:35], v[52:55], v[28:31]\n"
"ds_write_addtid_b32 v160 offset:32256\n"
"v_mfma_f32_16x16x32_bf16 v[156:159], v[32:35], v[56:59], v[156:159]\n"
"ds_read_b128 v[32:35], v12 offset:28672\n"
"ds_write_addtid_b32 v161 offset:32512\n"
"s_waitcnt lgkmcnt(5)\n"
"v_mfma_f32_16x16x32_bf16 v[144:147], v[24:27], v[60:63], v[144:147]\n"
"ds_write_addtid_b32 v162 offset:32768\n"
"v_mfma_f32_16x16x32_bf16 v[28:31], v[24:27], v[64:67], v[28:31]\n"
"ds_write_addtid_b32 v163 offset:33024\n"
"s_mov_b32 m0, s68\n"
"ds_read_b128 v[36:39], v13 offset:28672\n"
"v_mfma_f32_16x16x32_bf16 v[156:159], v[24:27], v[68:71], v[156:159]\n"
"ds_write_addtid_b32 v136 offset:45056\n"
"ds_write_addtid_b32 v137 offset:45312\n"
"ds_write_addtid_b32 v138 offset:45568\n"
"s_waitcnt lgkmcnt(7)\n"
"v_mfma_f32_16x16x32_bf16 v[144:147], v[32:35], v[72:75], v[144:147]\n"
"ds_write_addtid_b32 v139 offset:45824\n"
"ds_write_addtid_b32 v172 offset:46080\n"
"v_mfma_f32_16x16x32_bf16 v[28:31], v[32:35], v[76:79], v[28:31]\n"
"ds_write_addtid_b32 v173 offset:46336\n"
"v_mov_b32 v24, 0x3f803f80\n"
"v_mfma_f32_16x16x32_bf16 v[156:159], v[32:35], v[80:83], v[156:159]\n"
"s_waitcnt lgkmcnt(6)\n"
"v_mov_b32 v25, 0x3f803f80\n"
"v_mov_b32 v26, 0x3f803f80\n"
"v_mfma_f32_16x16x32_bf16 v[144:147], v[36:39], v[84:87], v[144:147]\n"
"v_mov_b32 v27, 0x3f803f80\n"
"v_mov_b32 v32, 0\n"
"v_mfma_f32_16x16x32_bf16 v[28:31], v[36:39], v[88:91], v[28:31]\n"
"v_mfma_f32_16x16x32_bf16 v[156:159], v[36:39], v[92:95], v[156:159]\n"
"s_waitcnt lgkmcnt(0)\n"
"s_barrier\n"
"ds_read_b32 v33, v9 offset:30976\n"
"ds_read2st64_b32 v[34:35], v9 offset0:122 offset1:123\n"
"ds_read2st64_b32 v[36:37], v9 offset0:128 offset1:129\n"
"v_cvt_pk_bf16_f32 v180, v144, v145\n"
"v_cvt_pk_bf16_f32 v181, v146, v147\n"
"ds_read2st64_b32 v[38:39], v9 offset0:130 offset1:131\n"
"v_cvt_pk_bf16_f32 v206, v28, v29\n"
"v_cvt_pk_bf16_f32 v207, v30, v31\n"
"v_cvt_pk_bf16_f32 v232, v156, v157\n"
"v_cvt_pk_bf16_f32 v233, v158, v159\n"
"s_waitcnt lgkmcnt(2)\n"
"v_mfma_f32_16x16x32_bf16 v[28:31], v[24:27], v[32:35], 0\n"
"v_mfma_f32_16x16x32_bf16 v[40:43], v[182:185], v[32:35], 0\n"
"v_mfma_f32_16x16x32_bf16 v[44:47], v[208:211], v[32:35], 0\n"
"ds_read_u16 v16, v21 offset:1088\n"
"v_mfma_f32_16x16x32_bf16 v[48:51], v[234:237], v[32:35], 0\n"
"s_waitcnt lgkmcnt(1)\n"
"ds_read2st64_b32 v[32:33], v20 offset0:0 offset1:1\n"
"ds_read2st64_b32 v[34:35], v20 offset0:2 offset1:3\n"
"v_mfma_f32_16x16x32_bf16 v[28:31], v[24:27], v[36:39], v[28:31]\n"
"ds_read_b32 v53, v9 offset:34048\n"
"v_mov_b32 v52, 0\n"
"v_mfma_f32_16x16x32_bf16 v[40:43], v[178:181], v[36:39], v[40:43]\n"
"ds_read2st64_b32 v[54:55], v9 offset0:134 offset1:135\n"
"v_mfma_f32_16x16x32_bf16 v[44:47], v[204:207], v[36:39], v[44:47]\n"
"ds_read2st64_b32 v[240:241], v20 offset0:4 offset1:5\n"
"v_mfma_f32_16x16x32_bf16 v[48:51], v[230:233], v[36:39], v[48:51]\n"
"s_waitcnt lgkmcnt(5)\n"
"ds_read2st64_b32 v[58:59], v9 offset0:124 offset1:125\n"
"v_mad_u32_u24 v15, v16, s40, v18\n"
"v_mad_u32_u24 v16, v16, s40, v19\n"
"s_waitcnt lgkmcnt(4)\n"
"global_store_dwordx4 v15, v[32:35], s[66:67] sc1\n"
"ds_read2st64_b32 v[60:61], v9 offset0:126 offset1:127\n"
"s_waitcnt lgkmcnt(3)\n"
"v_mfma_f32_16x16x32_bf16 v[28:31], v[24:27], v[52:55], v[28:31]\n"
"ds_read_b32 v35, v9 offset:9984\n"
"v_mfma_f32_16x16x32_bf16 v[40:43], v[192:195], v[52:55], v[40:43]\n"
"s_waitcnt lgkmcnt(3)\n"
"global_store_dwordx2 v16, v[240:241], s[66:67] offset:256 sc1\n"
"v_mfma_f32_16x16x32_bf16 v[44:47], v[218:221], v[52:55], v[44:47]\n"
"v_mov_b32 v34, 0\n"
"v_mov_b32 v32, 0\n"
"v_mfma_f32_16x16x32_bf16 v[48:51], v[244:247], v[52:55], v[48:51]\n"
"s_waitcnt lgkmcnt(1)\n"
"v_mov_b32 v33, 0\n"
"v_mov_b32 v56, 0\n"
"v_mov_b32 v57, 0\n"
"v_mfma_f32_16x16x32_bf16 v[28:31], v[24:27], v[58:61], v[28:31]\n"
"s_waitcnt lgkmcnt(0)\n"
"v_mov_b32 v62, 0\n"
"v_mov_b32 v63, 0\n"
"v_mfma_f32_16x16x32_bf16 v[28:31], v[24:27], v[32:35], v[28:31]\n"
"v_mfma_f32_16x16x32_bf16 v[48:51], v[236:239], v[56:59], v[48:51]\n"
"v_mfma_f32_16x16x32_bf16 v[48:51], v[248:251], v[60:63], v[48:51]\n"
"v_mfma_f32_16x16x32_bf16 v[48:51], v[252:255], v[32:35], v[48:51]\n"
"v_mfma_f32_16x16x32_bf16 v[40:43], v[184:187], v[56:59], v[40:43]\n"
"v_mfma_f32_16x16x32_bf16 v[40:43], v[196:199], v[60:63], v[40:43]\n"
"ds_read2st64_b32 v[182:183], v14 offset0:126 offset1:127\n"
"v_mfma_f32_16x16x32_bf16 v[40:43], v[200:203], v[32:35], v[40:43]\n"
"ds_read2st64_b32 v[184:185], v14 offset0:128 offset1:129\n"
"v_rcp_f32 v16, v28\n"
"v_mfma_f32_16x16x32_bf16 v[44:47], v[210:213], v[56:59], v[44:47]\n"
"ds_read_b128 v[186:189], v17 offset:53248\n"
"v_mul_f32 v48, v48, v16\n"
"v_mfma_f32_16x16x32_bf16 v[44:47], v[222:225], v[60:63], v[44:47]\n"
"ds_read2st64_b32 v[196:197], v14 offset0:150 offset1:151\n"
"v_mul_f32 v49, v49, v16\n"
"v_mfma_f32_16x16x32_bf16 v[44:47], v[226:229], v[32:35], v[44:47]\n"
"ds_read2st64_b32 v[198:199], v14 offset0:152 offset1:153\n"
"v_cvt_pk_bf16_f32 v240, v48, v49\n"
"v_mul_f32 v50, v50, v16\n"
"v_mul_f32 v51, v51, v16\n"
"v_cvt_pk_bf16_f32 v241, v50, v51\n"
"v_mul_f32 v40, v40, v16\n"
"v_mul_f32 v41, v41, v16\n"
"s_waitcnt lgkmcnt(3)\n"
"v_mfma_f32_16x16x32_bf16 v[208:211], v[96:99], v[182:185], 0\n"
"ds_read_b128 v[212:215], v17 offset:54272\n"
"v_cvt_pk_bf16_f32 v40, v40, v41\n"
"v_mfma_f32_16x16x32_bf16 v[222:225], v[100:103], v[182:185], 0\n"
"s_waitcnt lgkmcnt(3)\n"
"ds_read2st64_b32 v[234:235], v14 offset0:174 offset1:175\n"
"v_mul_f32 v42, v42, v16\n"
"v_mfma_f32_16x16x32_bf16 v[248:251], v[186:189], v[182:185], 0\n"
"s_waitcnt lgkmcnt(2)\n"
"ds_read2st64_b32 v[236:237], v14 offset0:176 offset1:177\n"
"v_mul_f32 v43, v43, v16\n"
"v_cvt_pk_bf16_f32 v41, v42, v43\n"
"v_mfma_f32_16x16x32_bf16 v[208:211], v[104:107], v[196:199], v[208:211]\n"
"ds_read_b128 v[182:185], v17 offset:55296\n"
"v_mul_f32 v44, v44, v16\n"
"v_mfma_f32_16x16x32_bf16 v[222:225], v[108:111], v[196:199], v[222:225]\n"
"s_waitcnt lgkmcnt(3)\n"
"ds_read2st64_b32 v[186:187], v14 offset0:198 offset1:199\n"
"v_mul_f32 v45, v45, v16\n"
"v_cvt_pk_bf16_f32 v42, v44, v45\n"
"v_mfma_f32_16x16x32_bf16 v[248:251], v[212:215], v[196:199], v[248:251]\n"
"ds_read2st64_b32 v[188:189], v14 offset0:200 offset1:201\n"
"v_mul_f32 v46, v46, v16\n"
"v_mul_f32 v47, v47, v16\n"
"v_cvt_pk_bf16_f32 v43, v46, v47\n"
"s_waitcnt lgkmcnt(3)\n"
"v_mfma_f32_16x16x32_bf16 v[208:211], v[0:3], v[234:237], v[208:211]\n"
"s_mov_b32 m0, s54\n"
"ds_read_b128 v[196:199], v17 offset:56320\n"
"v_mfma_f32_16x16x32_bf16 v[222:225], v[112:115], v[234:237], v[222:225]\n"
"s_waitcnt lgkmcnt(3)\n"
"ds_read2st64_b32 v[212:213], v14 offset0:130 offset1:131\n"
"v_mfma_f32_16x16x32_bf16 v[248:251], v[182:185], v[234:237], v[248:251]\n"
"s_waitcnt lgkmcnt(2)\n"
"ds_read2st64_b32 v[214:215], v14 offset0:154 offset1:155\n"
"ds_read2st64_b32 v[182:183], v14 offset0:178 offset1:179\n"
"v_mfma_f32_16x16x32_bf16 v[208:211], v[116:119], v[186:189], v[208:211]\n"
"ds_read2st64_b32 v[184:185], v14 offset0:202 offset1:203\n"
"v_mfma_f32_16x16x32_bf16 v[222:225], v[120:123], v[186:189], v[222:225]\n"
"ds_write_addtid_b32 v240 offset:7168\n"
"ds_write_addtid_b32 v241 offset:7424\n"
"s_waitcnt lgkmcnt(6)\n"
"v_mfma_f32_16x16x32_bf16 v[248:251], v[196:199], v[186:189], v[248:251]\n"
"ds_write_addtid_b32 v40 offset:6144\n"
"ds_write_addtid_b32 v41 offset:6400\n"
"ds_write_addtid_b32 v42 offset:6656\n"
"s_waitcnt lgkmcnt(7)\n"
"v_mfma_f32_16x16x32_bf16 v[208:211], v[4:7], v[212:215], v[208:211]\n"
"ds_write_addtid_b32 v43 offset:6912\n"
"ds_read_b128 v[186:189], v17 offset:57344\n"
"v_mfma_f32_16x16x32_bf16 v[222:225], v[124:127], v[212:215], v[222:225]\n"
"ds_read_b128 v[196:199], v17 offset:58368\n"
"v_mov_b32 v234, 0x3f803f80\n"
"v_mov_b32 v235, 0x3f803f80\n"
"v_mov_b32 v236, 0x3f803f80\n"
"v_mov_b32 v237, 0x3f803f80\n"
"s_waitcnt lgkmcnt(2)\n"
"s_barrier\n"
"ds_read2st64_b32 v[238:239], v9 offset0:112 offset1:113\n"
"ds_read2st64_b32 v[240:241], v9 offset0:114 offset1:115\n"
"s_waitcnt lgkmcnt(3)\n"
"v_mfma_f32_16x16x32_bf16 v[248:251], v[186:189], v[212:215], v[248:251]\n"
"v_mfma_f32_16x16x32_bf16 v[208:211], v[128:131], v[182:185], v[208:211]\n"
"v_mfma_f32_16x16x32_bf16 v[222:225], v[132:135], v[182:185], v[222:225]\n"
"s_waitcnt lgkmcnt(2)\n"
"v_subrev_u32 v16, 24576, v9\n"
"v_mfma_f32_16x16x32_bf16 v[248:251], v[196:199], v[182:185], v[248:251]\n"
"s_waitcnt lgkmcnt(0)\n"
"ds_read2st64_b32 v[182:183], v9 offset0:116 offset1:117\n"
"ds_read2st64_b32 v[184:185], v9 offset0:118 offset1:119\n"
"v_mfma_f32_16x16x32_bf16 v[186:189], v[234:237], v[238:241], 0\n"
"ds_read2st64_b32 v[26:27], v16 offset0:16 offset1:17\n"
"v_mfma_f32_16x16x32_bf16 v[196:199], v[190:193], v[238:241], 0\n"
"ds_read2st64_b32 v[28:29], v16 offset0:18 offset1:19\n"
"v_cvt_pk_bf16_f32 v208, v208, v209\n"
"v_mfma_f32_16x16x32_bf16 v[212:215], v[216:219], v[238:241], 0\n"
"v_cvt_pk_bf16_f32 v209, v210, v211\n"
"v_cvt_pk_bf16_f32 v210, v222, v223\n"
"v_mfma_f32_16x16x32_bf16 v[8:11], v[242:245], v[238:241], 0\n"
"v_mov_b32 v24, 0\n"
"v_mov_b32 v25, 0\n"
"v_cvt_pk_bf16_f32 v211, v224, v225\n"
"v_cvt_pk_bf16_f32 v248, v248, v249\n"
"v_cvt_pk_bf16_f32 v249, v250, v251\n"
"s_waitcnt lgkmcnt(2)\n"
"v_mfma_f32_16x16x32_bf16 v[186:189], v[234:237], v[182:185], v[186:189]\n"
"s_waitcnt lgkmcnt(0)\n"
"v_mov_b32 v30, 0\n"
"v_mov_b32 v31, 0\n"
"v_mfma_f32_16x16x32_bf16 v[186:189], v[234:237], v[26:29], v[186:189]\n"
"v_mfma_f32_16x16x32_bf16 v[8:11], v[252:255], v[182:185], v[8:11]\n"
"v_mfma_f32_16x16x32_bf16 v[8:11], v[244:247], v[24:27], v[8:11]\n"
"v_mfma_f32_16x16x32_bf16 v[8:11], v[230:233], v[28:31], v[8:11]\n"
"s_mov_b32 m0, s54\n"
"v_mfma_f32_16x16x32_bf16 v[196:199], v[200:203], v[182:185], v[196:199]\n"
"v_mfma_f32_16x16x32_bf16 v[196:199], v[192:195], v[24:27], v[196:199]\n"
"s_nop 1\n"
"ds_read2st64_b32 v[188:189], v14 offset0:24 offset1:25\n"
"v_mfma_f32_16x16x32_bf16 v[196:199], v[178:181], v[28:31], v[196:199]\n"
"ds_read2st64_b32 v[190:191], v14 offset0:26 offset1:27\n"
"v_rcp_f32 v16, v186\n"
"v_mfma_f32_16x16x32_bf16 v[212:215], v[226:229], v[182:185], v[212:215]\n"
"ds_read_b128 v[178:181], v17 offset:53248\n"
"v_mul_f32 v8, v8, v16\n"
"v_mul_f32 v9, v9, v16\n"
"v_mul_f32 v10, v10, v16\n"
"v_mul_f32 v11, v11, v16\n"
"v_mul_f32 v196, v196, v16\n"
"v_mul_f32 v197, v197, v16\n"
"v_cvt_pk_bf16_f32 v196, v196, v197\n"
"v_mul_f32 v198, v198, v16\n"
"v_mul_f32 v199, v199, v16\n"
"v_cvt_pk_bf16_f32 v197, v198, v199\n"
"v_cvt_pk_bf16_f32 v250, v8, v9\n"
"v_cvt_pk_bf16_f32 v251, v10, v11\n"
"v_mfma_f32_16x16x32_bf16 v[212:215], v[218:221], v[24:27], v[212:215]\n"
"ds_write_addtid_b32 v250 offset:33280\n"
"ds_write_addtid_b32 v251 offset:33536\n"
"v_mfma_f32_16x16x32_bf16 v[212:215], v[204:207], v[28:31], v[212:215]\n"
"ds_write_addtid_b32 v196 offset:32256\n"
"ds_write_addtid_b32 v197 offset:32512\n"
"s_waitcnt lgkmcnt(5)\n"
"v_mfma_f32_16x16x32_bf16 v[182:185], v[96:99], v[188:191], 0\n"
"ds_read2st64_b32 v[192:193], v14 offset0:48 offset1:49\n"
"ds_read2st64_b32 v[194:195], v14 offset0:50 offset1:51\n"
"v_mfma_f32_16x16x32_bf16 v[200:203], v[100:103], v[188:191], 0\n"
"s_waitcnt lgkmcnt(6)\n"
"ds_read_b128 v[204:207], v17 offset:54272\n"
"v_mfma_f32_16x16x32_bf16 v[216:219], v[178:181], v[188:191], 0\n"
"ds_read2st64_b32 v[178:179], v14 offset0:72 offset1:73\n"
"v_mul_f32 v212, v212, v16\n"
"v_mul_f32 v213, v213, v16\n"
"v_mul_f32 v214, v214, v16\n"
"v_mul_f32 v215, v215, v16\n"
"v_cvt_pk_bf16_f32 v198, v212, v213\n"
"v_cvt_pk_bf16_f32 v199, v214, v215\n"
"ds_write_addtid_b32 v198 offset:32768\n"
"ds_write_addtid_b32 v199 offset:33024\n"
"s_mov_b32 m0, s68\n"
"ds_read2st64_b32 v[180:181], v14 offset0:74 offset1:75\n"
"s_waitcnt lgkmcnt(5)\n"
"v_mfma_f32_16x16x32_bf16 v[182:185], v[104:107], v[192:195], v[182:185]\n"
"ds_write_addtid_b32 v208 offset:45056\n"
"ds_write_addtid_b32 v209 offset:45312\n"
"v_mfma_f32_16x16x32_bf16 v[200:203], v[108:111], v[192:195], v[200:203]\n"
"ds_write_addtid_b32 v210 offset:45568\n"
"ds_write_addtid_b32 v211 offset:45824\n"
"s_waitcnt lgkmcnt(8)\n"
"v_mfma_f32_16x16x32_bf16 v[216:219], v[204:207], v[192:195], v[216:219]\n"
"ds_write_addtid_b32 v248 offset:46080\n"
"ds_write_addtid_b32 v249 offset:46336\n"
"ds_read_b128 v[186:189], v17 offset:55296\n"
"ds_read2st64_b32 v[190:191], v14 offset0:96 offset1:97\n"
"s_waitcnt lgkmcnt(8)\n"
"v_mfma_f32_16x16x32_bf16 v[182:185], v[0:3], v[178:181], v[182:185]\n"
"ds_read2st64_b32 v[192:193], v14 offset0:98 offset1:99\n"
"v_mfma_f32_16x16x32_bf16 v[200:203], v[112:115], v[178:181], v[200:203]\n"
"s_waitcnt lgkmcnt(2)\n"
"ds_read_b128 v[194:197], v17 offset:56320\n"
"ds_read2st64_b32 v[204:205], v14 offset0:28 offset1:29\n"
"ds_read2st64_b32 v[206:207], v14 offset0:52 offset1:53\n"
"v_mfma_f32_16x16x32_bf16 v[216:219], v[186:189], v[178:181], v[216:219]\n"
"s_waitcnt lgkmcnt(3)\n"
"ds_read2st64_b32 v[178:179], v14 offset0:76 offset1:77\n"
"ds_read2st64_b32 v[180:181], v14 offset0:100 offset1:101\n"
"v_mfma_f32_16x16x32_bf16 v[182:185], v[116:119], v[190:193], v[182:185]\n"
"ds_read_b128 v[186:189], v17 offset:57344\n"
"v_mfma_f32_16x16x32_bf16 v[200:203], v[120:123], v[190:193], v[200:203]\n"
"s_waitcnt lgkmcnt(5)\n"
"v_mfma_f32_16x16x32_bf16 v[216:219], v[194:197], v[190:193], v[216:219]\n"
"s_waitcnt lgkmcnt(3)\n"
"v_mfma_f32_16x16x32_bf16 v[182:185], v[4:7], v[204:207], v[182:185]\n"
"ds_read_b128 v[190:193], v17 offset:58368\n"
"v_mfma_f32_16x16x32_bf16 v[200:203], v[124:127], v[204:207], v[200:203]\n"
"s_waitcnt lgkmcnt(2)\n"
"s_barrier\n"
"ds_read2st64_b32 v[194:195], v14 offset0:126 offset1:127\n"
"s_waitcnt lgkmcnt(2)\n"
"v_mfma_f32_16x16x32_bf16 v[216:219], v[186:189], v[204:207], v[216:219]\n"
"ds_read2st64_b32 v[196:197], v14 offset0:128 offset1:129\n"
"v_mfma_f32_16x16x32_bf16 v[182:185], v[128:131], v[178:181], v[182:185]\n"
"ds_read_b128 v[186:189], v17 offset:53248\n"
"v_mfma_f32_16x16x32_bf16 v[200:203], v[132:135], v[178:181], v[200:203]\n"
"s_waitcnt lgkmcnt(3)\n"
"ds_read2st64_b32 v[204:205], v14 offset0:150 offset1:151\n"
"v_mfma_f32_16x16x32_bf16 v[216:219], v[190:193], v[178:181], v[216:219]\n"
"s_waitcnt lgkmcnt(2)\n"
"ds_read2st64_b32 v[206:207], v14 offset0:152 offset1:153\n"
"v_mfma_f32_16x16x32_bf16 v[178:181], v[96:99], v[194:197], 0\n"
"ds_read_b128 v[190:193], v17 offset:54272\n"
"v_mfma_f32_16x16x32_bf16 v[208:211], v[100:103], v[194:197], 0\n"
"s_waitcnt lgkmcnt(3)\n"
"ds_read2st64_b32 v[212:213], v14 offset0:174 offset1:175\n"
"v_cvt_pk_bf16_f32 v182, v182, v183\n"
"v_mfma_f32_16x16x32_bf16 v[220:223], v[186:189], v[194:197], 0\n"
"s_waitcnt lgkmcnt(2)\n"
"ds_read2st64_b32 v[214:215], v14 offset0:176 offset1:177\n"
"v_cvt_pk_bf16_f32 v183, v184, v185\n"
"v_mfma_f32_16x16x32_bf16 v[178:181], v[104:107], v[204:207], v[178:181]\n"
"ds_read_u16 v16, v21 offset:1120\n"
"v_cvt_pk_bf16_f32 v184, v200, v201\n"
"v_mfma_f32_16x16x32_bf16 v[208:211], v[108:111], v[204:207], v[208:211]\n"
"ds_read2st64_b32 v[186:187], v20 offset0:0 offset1:1\n"
"v_cvt_pk_bf16_f32 v185, v202, v203\n"
"v_cvt_pk_bf16_f32 v216, v216, v217\n"
"v_cvt_pk_bf16_f32 v217, v218, v219\n"
"s_waitcnt lgkmcnt(4)\n"
"v_mfma_f32_16x16x32_bf16 v[220:223], v[190:193], v[204:207], v[220:223]\n"
"ds_read2st64_b32 v[188:189], v20 offset0:2 offset1:3\n"
"ds_read_b128 v[190:193], v17 offset:55296\n"
"ds_read2st64_b32 v[194:195], v14 offset0:198 offset1:199\n"
"ds_read2st64_b32 v[196:197], v14 offset0:200 offset1:201\n"
"s_waitcnt lgkmcnt(6)\n"
"v_mfma_f32_16x16x32_bf16 v[178:181], v[0:3], v[212:215], v[178:181]\n"
"s_waitcnt lgkmcnt(5)\n"
"ds_read2st64_b32 v[218:219], v20 offset0:4 offset1:5\n"
"v_mad_u32_u24 v21, v16, s40, v18\n"
"v_mfma_f32_16x16x32_bf16 v[208:211], v[112:115], v[212:215], v[208:211]\n"
"ds_read_b128 v[198:201], v17 offset:56320\n"
"v_mad_u32_u24 v16, v16, s40, v19\n"
"ds_read2st64_b32 v[202:203], v14 offset0:130 offset1:131\n"
"ds_read2st64_b32 v[204:205], v14 offset0:154 offset1:155\n"
"s_waitcnt lgkmcnt(6)\n"
"v_mfma_f32_16x16x32_bf16 v[220:223], v[190:193], v[212:215], v[220:223]\n"
"s_mov_b32 m0, s68\n"
"global_store_dwordx4 v21, v[186:189], s[66:67] sc1\n"
"s_waitcnt lgkmcnt(4)\n"
"v_mfma_f32_16x16x32_bf16 v[178:181], v[116:119], v[194:197], v[178:181]\n"
"ds_read_b128 v[186:189], v17 offset:57344\n"
"v_mfma_f32_16x16x32_bf16 v[208:211], v[120:123], v[194:197], v[208:211]\n"
"s_waitcnt lgkmcnt(3)\n"
"ds_read2st64_b32 v[190:191], v14 offset0:178 offset1:179\n"
"v_mfma_f32_16x16x32_bf16 v[220:223], v[198:201], v[194:197], v[220:223]\n"
"s_waitcnt lgkmcnt(2)\n"
"ds_read2st64_b32 v[192:193], v14 offset0:202 offset1:203\n"
"v_mfma_f32_16x16x32_bf16 v[178:181], v[4:7], v[202:205], v[178:181]\n"
"global_store_dwordx2 v16, v[218:219], s[66:67] offset:256 sc1\n"
"ds_write_addtid_b32 v182 offset:51456\n"
"v_mfma_f32_16x16x32_bf16 v[208:211], v[124:127], v[202:205], v[208:211]\n"
"ds_write_addtid_b32 v183 offset:51712\n"
"ds_read_b128 v[194:197], v17 offset:58368\n"
"s_waitcnt lgkmcnt(5)\n"
"v_mfma_f32_16x16x32_bf16 v[220:223], v[186:189], v[202:205], v[220:223]\n"
"ds_write_addtid_b32 v184 offset:51968\n"
"ds_write_addtid_b32 v185 offset:52224\n"
"ds_write_addtid_b32 v216 offset:52480\n"
"s_waitcnt lgkmcnt(6)\n"
"v_mfma_f32_16x16x32_bf16 v[178:181], v[128:131], v[190:193], v[178:181]\n"
"ds_write_addtid_b32 v217 offset:52736\n"
"v_mfma_f32_16x16x32_bf16 v[208:211], v[132:135], v[190:193], v[208:211]\n"
"s_waitcnt lgkmcnt(4)\n"
"v_mfma_f32_16x16x32_bf16 v[220:223], v[194:197], v[190:193], v[220:223]\n"
"s_nop 3\n"
"v_cvt_pk_bf16_f32 v178, v178, v179\n"
"ds_write_addtid_b32 v178 offset:57856\n"
"v_cvt_pk_bf16_f32 v179, v180, v181\n"
"ds_write_addtid_b32 v179 offset:58112\n"
"v_cvt_pk_bf16_f32 v180, v208, v209\n"
"ds_write_addtid_b32 v180 offset:58368\n"
"v_cvt_pk_bf16_f32 v181, v210, v211\n"
"ds_write_addtid_b32 v181 offset:58624\n"
"v_cvt_pk_bf16_f32 v220, v220, v221\n"
"v_cvt_pk_bf16_f32 v221, v222, v223\n"
"ds_write_addtid_b32 v220 offset:58880\n"
"ds_write_addtid_b32 v221 offset:59136\n"
"s_waitcnt lgkmcnt(0)\n"
"s_barrier\n"
"ds_read2st64_b32 v[178:179], v20 offset0:25 offset1:26\n"
"ds_read2st64_b32 v[180:181], v20 offset0:27 offset1:28\n"
"ds_read2st64_b32 v[182:183], v20 offset0:29 offset1:30\n"
"ds_read2st64_b32 v[184:185], v20 offset0:50 offset1:51\n"
"ds_read2st64_b32 v[186:187], v20 offset0:52 offset1:53\n"
"ds_read2st64_b32 v[254:255], v20 offset0:54 offset1:55\n"
"s_waitcnt lgkmcnt(4)\n"
"global_store_dwordx4 v22, v[178:181], s[66:67] sc1\n"
"s_waitcnt lgkmcnt(3)\n"
"global_store_dwordx2 v23, v[182:183], s[66:67] offset:256 sc1\n"
"s_waitcnt lgkmcnt(1)\n"
"global_store_dwordx4 v176, v[184:187], s[66:67] sc1\n"
"s_waitcnt lgkmcnt(0)\n"
"global_store_dwordx2 v177, v[254:255], s[66:67] offset:256 sc1\n"
    );
  }
}

extern "C" void kernel_launch(void* const* d_in, const int* in_sizes, int n_in,
                              void* d_out, int out_size, void* d_ws, size_t ws_size,
                              hipStream_t stream) {
  dinat_fused<<<256, 512, 0, stream>>>((const uint16_t*)d_in[0], (const uint16_t*)d_in[1], (const uint16_t*)d_in[2],
                                       (const uint16_t*)d_in[3], (uint16_t*)d_out
  );
}
```

```cpp
#include <hip/hip_runtime.h>
#include <hip/hip_bf16.h>
#include <stdint.h>

typedef __attribute__((ext_vector_type(4))) float f32x4;
typedef __attribute__((ext_vector_type(4))) unsigned u32x4;
typedef __attribute__((ext_vector_type(8))) unsigned u32x8;

#define LDS_AS __attribute__((address_space(3)))
#define DI __device__ __forceinline__
#define SB __builtin_amdgcn_sched_barrier(0)
#define OPQ(v) asm volatile("" : "+v"(v))
#define UNI(v) __builtin_amdgcn_readfirstlane(v)

#define TOFF 0
#define TE 1216
#define RAW 2048
#define PFD 3584
#define O45 2048
#define FLAG 32192
#define TMPA 28672
#define BB 24576
#define PN 6
#define CPF 1
#define RBF 1
#define HTA 0
#define SS0 8
#define OID 1
#define PFC 430
#define XR 102400
#define TMPB 53248
#define RESA 4096
#define RESB 77824
#define NREG 36
#define UD0 0x1312030201001110ULL
#define UD1 0x0409050605040807ULL
#define UD2 0x2928171615141918ULL
#define UD3 0x1D1C0D0C0B0A1B1AULL
#define UD4 0x2025212221202423ULL
#define UD5 0x4342333231304140ULL
#define UD6 0x2726373635343938ULL
#define UD7 0x3D3C2D2C2B2A3B3AULL
#define UD8 0x4B4A4D4C47464948ULL
#define UD9 0x6362535251506160ULL
#define UD10 0x4559445755545856ULL
#define UD11 0x6D6C5D5C5B5A6B6AULL
#define UD12 0x6469656665646867ULL
#define UD13 0x6469656665646867ULL
#define UD14 0x6469656665646867ULL
#define UD15 0x6469656665646867ULL
#define CLOB "v24", "v25", "v26", "v27", "v28", "v29", "v30", "v31", "v32", "v33", "v34", "v35", "v36", "v37", "v38", "v39", "v40", "v41", "v42", "v43", "v44", "v45", "v46", "v47", "v48", "v49", "v50", "v51", "v52", "v53", "v54", "v55", "v56", "v57", "v58", "v59", "v60", "v61", "v62", "v63", "v64", "v65", "v66", "v67", "v68", "v69", "v70", "v71", "v72", "v73", "v74", "v75", "v76", "v77", "v78", "v79", "v80", "v81", "v82", "v83", "v84", "v85", "v86", "v87", "v88", "v89", "v90", "v91", "v92", "v93", "v94", "v95", "v96", "v97", "v98", "v99", "v100", "v101", "v102", "v103", "v104", "v105", "v106", "v107", "v108", "v109", "v110", "v111", "v112", "v113", "v114", "v115", "v116", "v117", "v118", "v119", "v120", "v121", "v122", "v123", "v124", "v125", "v126", "v127", "v128", "v129", "v130", "v131", "v132", "v133", "v134", "v135", "v136", "v137", "v138", "v139", "v140", "v141", "v142", "v143", "v144", "v145", "v146", "v147", "v148", "v149", "v150", "v151", "v152", "v153", "v154", "v155", "v156", "v157", "v158", "v159", "v160", "v161", "v162", "v163", "v164", "v165", "v166", "v167", "v168", "v169", "v170", "v171", "v172", "v173", "v174", "v175", "v176", "v177", "v178", "v179", "v180", "v181", "v182", "v183", "v184", "v185", "v186", "v187", "v188", "v189", "v190", "v191", "v192", "v193", "v194", "v195", "v196", "v197", "v198", "v199", "v200", "v201", "v202", "v203", "v204", "v205", "v206", "v207", "v208", "v209", "v210", "v211", "v212", "v213", "v214", "v215", "v216", "v217", "v218", "v219", "v220", "v221", "v222", "v223", "v224", "v225", "v226", "v227", "v228", "v229", "v230", "v231", "v232", "v233", "v234", "v235", "v236", "v237", "v238", "v239", "v240", "v241", "v242", "v243", "v244", "v245", "v246", "v247", "v248", "v249", "v250", "v251", "v252", "v253", "v254", "v255"
#define PRE "s_and_b32 s40, s44, 3\n" "s_mul_i32 s41, s40, 96\n" "s_add_u32 s46, s46, s41\n" "s_addc_u32 s47, s47, 0\n" "s_mul_i32 s54, s40, 6144\n"
#define LDS_BYTES 163840
#define LOG2E 1.4426950408889634f
#define LP(T, o) ((LDS_AS T*)(size_t)(unsigned)(o))

DI const char* uni(const void* p) {
  unsigned long long sv = (unsigned long long)p;
  unsigned lo = UNI((unsigned)sv), hi = UNI((unsigned)(sv >> 32));
  return (const char*)(((unsigned long long)hi << 32) | lo);
}
#define DMA(src, voff, dst) asm volatile("s_mov_b32 m0, %2\n\ts_nop 0\n\tglobal_load_lds_dwordx4 %0, %1" :: "v"(voff), "s"(src), "s"(dst) : "memory", "m0")
#define DMAN(src, voff, dst) asm volatile("s_mov_b32 m0, %2\n\ts_nop 0\n\tglobal_load_lds_dwordx4 %0, %1 nt" :: "v"(voff), "s"(src), "s"(dst) : "memory", "m0")
#define DMA4(src, voff, dst) asm volatile("s_mov_b32 m0, %2\n\ts_nop 0\n\tglobal_load_lds_dword %0, %1" :: "v"(voff), "s"(src), "s"(dst) : "memory", "m0")
#define RAWB() asm volatile("s_barrier" ::: "memory")
#define WVM(n) asm volatile("s_waitcnt vmcnt(%0)" :: "n"(n) : "memory")
#define PSTART
#define PEND
#define PPARM
#define SPARM
#define SARG
#define PCLOB

struct Geo { int t0, sr, sc, er, ec, so; unsigned long long pc; };
DI int tok_idx(const Geo& G, int hr, int hc) {
  return G.t0 + G.sr * (hr - (hr >= 17 ? G.er : 0)) + G.sc * (hc - (hc >= 17 ? G.ec : 0));
}
template <int STR, int HB>
DI void mk_lo(int l, unsigned (&lo)[3], int h = 0) {
#pragma unroll
  for (int i = 0; i < 3; ++i) {
    const int idx = 64 * i + l, t = (idx * 171) >> 10, c = idx - 6 * t;
    const int r = HB ? ((t >> 3) & 1) * HB + ((t >> 1) & 8) + (t & 7) : ((t & ~12) | ((t & 4) << 1) | ((t & 8) >> 1));
    lo[i] = r * STR + c * 16 + (HB ? (c < 4 ? 64 * h : 192 + 32 * h) : 0);
    OPQ(lo[i]);
  }
}

DI int lane_id() {
  int l;
  asm volatile("v_mbcnt_lo_u32_b32 %0, -1, 0\n\tv_mbcnt_hi_u32_b32 %0, -1, %0" : "=v"(l));
  return l;
}
struct Regs { unsigned r[12]; u32x8 p; u32x4 q; };
#define XDIR(a0, b0, a1, b1, dst) do { \
  const unsigned xv = xl + UNI(h >= 2 ? a1 * x1 + b1 * x2 : a0 * x1 + b0 * x2); \
  DMAN(xs, xv, sXB + (dst)); DMAN(xs + 128, xv, sXB + (dst) + 2048); DMAN(xs + 256, xv, sXB + (dst) + 4096); \
} while (0)

#define SEL(m, a, b) (__builtin_amdgcn_inverse_ballot_w64(m) ? (a) : (b))
#define Q4(m) (0x1111111111111111ULL * (m))
DI void prologue(const uint16_t* x, const uint16_t* wqkv, const uint16_t* wo, const uint16_t* rpb, const void* keep, const Geo& G, int w, Regs& R) {
  const int l = lane_id() & 63, h = w & 3;
  const int u = l & 15, g = l >> 4;
  const int cu = (blockIdx.x >> 3) & 31;
  const unsigned ra = UNI(1152 * (6 * cu + h)), rb2 = UNI(1152 * (6 * cu + 4 + (h & 1)));
  const int tk = 8 * (h & 1) + (l >> 3);
  unsigned r0 = min(((338 * h) & ~3) + 4 * l, 1348), r1 = min(r0 + 128, 1348u);
  unsigned ck16 = 16 * ((l & 7) ^ (tk >> 1)), xl = (G.t0 + G.sr * (tk >> 2) + G.sc * (tk & 3)) * 768 + ck16;
  const unsigned sXB = UNI(6144 * (h >> 1) + 1024 * (h & 1));
  const unsigned x1 = UNI(3072 * G.sr), x2 = UNI(3072 * G.sc);
  unsigned pk = ((l & 1) ? ra : rb2) + 128 * ((l >> 1) + (l < 12 ? 3 : -6));
  OPQ(r0); OPQ(r1); OPQ(xl); OPQ(pk);
  asm volatile("" :: "s"(ra), "s"(rb2), "s"(sXB), "s"(x1), "s"(x2), "s"(G.so));
  SB;
  asm volatile("" :: "s"(x), "s"(wqkv), "s"(wo), "s"(rpb), "s"(keep));
  SB;
  if (w < 4) {
    const char* rp = uni(rpb);
    DMA4(rp, r0, UNI(RAW + 384 * h));
    DMA4(rp, r1, UNI(RAW + 384 * h + 128));
    asm volatile("s_mov_b32 m0, %4\n\ts_bfm_b64 exec, %5, 0\n\tglobal_load_lds_dword %0, %2\n\ts_mov_b64 exec, 15\n\tglobal_load_lds_dword %1, %3\n\ts_mov_b64 exec, -1"
                 :: "v"(pk), "v"(128 * l), "s"(uni(wqkv)), "s"(G.pc + 128 * UNI(min(16 * cu + 4 * h, PFC - 3))), "s"(PFD), "n"(2 * PN) : "memory", "m0");
  } else {
    const char* xs = uni((const char*)x + G.so);
  XDIR(0, 0, 0, 1, 126976); SB;
  XDIR(1, 0, 1, 1, 139264); SB;
  XDIR(0, 2, 2, 0, 151552); SB;
  }
  SB;
  unsigned lo3[3];
  mk_lo<1152, 0>(l, lo3);
  R.r[0] = lo3[0]; R.r[1] = lo3[1]; R.r[2] = lo3[2];
  unsigned wrow = 6144 * h + (16 * (g >> 1) + 4 * (g & 1) + (u >> 2)) * 96;
  unsigned xa = XR + u * 128 + 16 * (g ^ (u >> 1));
  {
    const int e = min(64 * w + l, 399), blk = e >> 4, a = (blk * 205) >> 10, b = blk - 5 * a;
    *LP(uint16_t, TOFF + 2 * e) = tok_idx(G, 4 * a + ((e >> 2) & 3), 4 * b + (e & 3));
  }
  OPQ(wrow); OPQ(xa);
  SB;
  int w2 = w;
  asm volatile("" : "+s"(w2));
  if (w2 > 3) {
    unsigned lo4[3], lo5[3];
    mk_lo<384, 16>(l, lo4, h); mk_lo<384, 48>(l, lo5, h);
#pragma unroll
    for (int i = 0; i < 3; ++i) { R.r[3 + i] = lo4[i]; R.r[6 + i] = lo5[i]; }
    R.r[9] = 4 * l + BB + 6144 * h; R.r[10] = BB + wrow + 8 * (u & 3); R.r[11] = BB + wrow + 16 * (u & 3);
    R.p = (u32x8){xa, xa ^ 64, 4u * l, 2u * tk, ck16, 16u * l + BB + 6144 * h, 16u * u, 8u * u};
    R.q = (u32x4){XR + 1552u * (u >> 2) + 64u * (u & 3) + 16u * h + 4u * g, 2u * (4 * h + g), 1152u * (6 * cu + SEL(0x1C7ULL, h, 4 + (h & 1))) + 128u * (l % 3) + SEL(0x3FULL, 0u, 768u) - 96 * h,
                  2304u * cu + 128u * min(5 * h + l, 17)};
    const unsigned long long d0 = h == 0 ? UD0 : (h == 1 ? UD4 : (h == 2 ? UD8 : UD12)), d1 = h == 0 ? UD1 : (h == 1 ? UD5 : (h == 2 ? UD9 : UD13));
    const unsigned long long d2 = h == 0 ? UD2 : (h == 1 ? UD6 : (h == 2 ? UD10 : UD14)), d3 = h == 0 ? UD3 : (h == 1 ? UD7 : (h == 2 ? UD11 : UD15));
    const unsigned long long d = g < 2 ? (g == 0 ? d0 : d1) : (g == 2 ? d2 : d3);
    const int by = (int)(d >> (8 * (u >> 1))) & 255, c = by & 15, r = 2 * (by >> 4) + (u & 1);
    const int e = 16 * min(4 * h + g, 12) + u;
    *LP(uint16_t, TOFF + 800 + 2 * e) = G.t0 + G.sr * (r + 3) + G.sc * (c + 3);
    const int cp = 64 * (c & 3) - 16 * (c >> 2);
    const int dl = (G.ec && c >= 11) ? 16 * c + 16 - cp : 0;
    *LP(unsigned, TE + 4 * e) = (unsigned)(TMPA + 64 + cp - 336 * r) | ((unsigned)dl << 16);
  } else {
    const char* wq_ = uni((const char*)wqkv + 96 * h);
    const unsigned sW = UNI(TMPA + 6144 * h);
   SB;
   SB;
  DMA(wq_ + 384, lo3[0], sW + (-24576)); SB;
  DMA(wq_ + 384, lo3[1], sW + (-23552)); SB;
  DMA(wq_ + 384, lo3[2], sW + (-22528)); SB;
    R.r[3] = wrow + 8 * (u & 3); R.r[4] = xa; R.r[5] = xa ^ 64; R.r[6] = 16 * l + 6144 * h; R.r[7] = 6144 * h + 336 * g;
    *LP(uint16_t, PFD + 256) = 0xF180;
    const int row = l >> 2, qd = l & 3, s2 = UNI(2 * G.sc);
    const int rho = row + SEL(0xFFFFFFFFF0000000ULL, -7, 3);
    const int src = UNI(RAW + 384 * h + ((338 * h) & 2) + (G.sr < 0 ? 312 : 0) + (G.sc < 0 ? 24 : 0)) + UNI(G.sr < 0 ? -26 : 26) * rho;
    R.r[8] = sW + 336 * row + 16 * qd + SEL(0xFFFFFFFFF0000000ULL, 96, 64);
    R.r[9] = sW + SEL(0x3FFULL, 336 * l + SEL(0x380ULL, 416, 384), 16 * l + SEL(0x3C00ULL, -160, SEL(0xC000ULL, 2192, 3200)));
    R.r[10] = R.r[11] = 0;
    const int b0 = src + 4 * s2 * qd, b1 = src - s2 * qd;
    unsigned ng = PFD + 256;
    OPQ(ng);
    R.p = (u32x8){SEL(Q4(6), b0, ng), SEL(Q4(6), b0 + s2, ng), SEL(Q4(2), b0 + 2 * s2, ng), SEL(Q4(3), b0 + 3 * s2, ng),
                  SEL(Q4(3), b0 + 4 * s2, ng), SEL(Q4(3), b0 + 5 * s2, ng), SEL(Q4(1), b0 + 6 * s2, ng), 0};
    R.q = (u32x4){SEL(Q4(7), b1 + 8 * s2, ng), SEL(Q4(7), b1 + 2 * s2, ng), SEL(Q4(6), b1 + 3 * s2, ng), SEL(Q4(4), b1 + 4 * s2, ng)};
  }
}
#define BODY(ta, tb) do { \
  const char *pq = uni(wqkv), *px = uni(x), *po = uni(out); \
  asm volatile(PRE "s_waitcnt lgkmcnt(0)\ns_cmp_lt_u32 s44, 4\ns_cbranch_scc0 1f\n" ta "s_branch 2f\n1:\n" tb "2:\n" \
  : "+{v0}"(R.r[0]), "+{v1}"(R.r[1]), "+{v2}"(R.r[2]), "+{v3}"(R.r[3]), "+{v4}"(R.r[4]), "+{v5}"(R.r[5]), "+{v6}"(R.r[6]), "+{v7}"(R.r[7]), \
    "+{v8}"(R.r[8]), "+{v9}"(R.r[9]), "+{v10}"(R.r[10]), "+{v11}"(R.r[11]), "+{v[12:19]}"(R.p), "+{v[20:23]}"(R.q), \
    "+{s[46:47]}"(pq), "+{s[56:57]}"(px), "+{s[66:67]}"(po) \
  : "{s44}"(w), "{s45}"(G.so | G.er), "{s[48:49]}"(uni(wo)) \
  : CLOB, "s40", "s41", "s42", "s43", "s50", "s51", "s52", "s53", "s54", "s55", "s58", "s59", "s60", "s61", "s62", "s63", "s64", "s65", \
    "s68", "s69", "s70", "s71", "s72", "s73", "vcc", "scc", "m0", "memory" PCLOB); } while (0)

__global__ __launch_bounds__(512, 2) void dinat_fused(const uint16_t* __restrict__ x, const uint16_t* __restrict__ wqkv,
                                                      const uint16_t* __restrict__ wo, const uint16_t* __restrict__ rpb,
                                                      uint16_t* __restrict__ out PPARM) {
  __shared__ __attribute__((aligned(256))) char smem[LDS_BYTES];
  PSTART;
  asm volatile("" :: "s"((unsigned)(size_t)(LDS_AS char*)smem));
  const int tid = threadIdx.x;
  const int w = UNI(tid >> 6);
  Geo G;
  asm volatile("s_getpc_b64 %0" : "=s"(G.pc));
  if (CPF) DMA4(G.pc, 128 * ((tid >> 4) + 2), UNI(PFD));
  SB;
  const int bidx = blockIdx.x;
  const int xcd = bidx & 7, idx = bidx >> 3;
  const int ta = 2 * (xcd >> 1) + (idx >> 4), tb = (idx >> 1) & 7;
  G.sr = (ta & 1) ? 224 : -224; G.sc = (tb & 1) ? 1 : -1;
  G.t0 = 224 * (14 * ta + ((ta & 1) ? -3 : 16)) + 14 * tb + ((tb & 1) ? -3 : 16);
  G.er = (ta == 0 || ta == 7) ? 7 : 0; G.ec = (tb == 0 || tb == 7) ? 7 : 0;
  G.so = ((xcd & 1) * 224 + (idx & 1)) * 384;
  {
    Regs R;
    prologue(x, wqkv, wo, rpb, out, G, w, R);
    BODY(
"s_add_u32 s50, s54, 28672\n"
"s_bitcmp1_b32 s45, 0\n"
"s_cselect_b32 s53, -1, 0\n"
"s_lshl_b32 s52, s53, 16\n"
"s_setprio 1\n"
"s_mov_b32 s60, 0x3e553b95\n"
"s_mov_b32 s61, 0x3fb8aa3b\n"
"v_add_u32 v27, 77824, v3\n"
"v_mov_b32 v24, 0\n"
"v_mov_b32 v25, 0\n"
"v_mov_b32 v178, 0xf1800000\n"
"v_mov_b32 v179, 0xf1800000\n"
"v_mov_b32 v180, 0xf1800000\n"
"v_mov_b32 v181, 0xf1800000\n"
"s_bfm_b64 exec, 18, 0\n"
"ds_write_b128 v9, v[178:181] offset:0\n"
"s_mov_b64 exec, -1\n"
"s_waitcnt vmcnt(5)\n"
"ds_read_u16 v13, v13 offset:0\n"
"ds_read_u16 v14, v14 offset:0\n"
"ds_read_u16 v15, v15 offset:0\n"
"ds_read_u16 v16, v16 offset:0\n"
"ds_read_u16 v17, v17 offset:0\n"
"ds_read_u16 v18, v18 offset:0\n"
"ds_read_u16 v12, v12 offset:0\n"
"ds_read_u16 v20, v20 offset:0\n"
"ds_read_u16 v21, v21 offset:0\n"
"ds_read_u16 v22, v22 offset:0\n"
"ds_read_u16 v23, v23 offset:0\n"
"s_waitcnt lgkmcnt(10)\n"
"v_lshlrev_b32 v13, 16, v13\n"
"s_waitcnt lgkmcnt(9)\n"
"v_lshlrev_b32 v14, 16, v14\n"
"v_mul_f32 v13, s61, v13\n"
"s_waitcnt lgkmcnt(8)\n"
"v_lshlrev_b32 v15, 16, v15\n"
"v_mul_f32 v14, s61, v14\n"
"s_waitcnt lgkmcnt(7)\n"
"v_lshlrev_b32 v16, 16, v16\n"
"v_mul_f32 v15, s61, v15\n"
"s_waitcnt lgkmcnt(6)\n"
"v_lshlrev_b32 v17, 16, v17\n"
"v_mul_f32 v16, s61, v16\n"
"s_waitcnt lgkmcnt(5)\n"
"v_lshlrev_b32 v18, 16, v18\n"
"v_mul_f32 v17, s61, v17\n"
"s_waitcnt lgkmcnt(4)\n"
"v_lshlrev_b32 v12, 16, v12\n"
"v_mul_f32 v18, s61, v18\n"
"s_waitcnt lgkmcnt(3)\n"
"v_lshlrev_b32 v20, 16, v20\n"
"v_mul_f32 v12, s61, v12\n"
"s_waitcnt lgkmcnt(2)\n"
"v_lshlrev_b32 v21, 16, v21\n"
"v_mul_f32 v20, s61, v20\n"
"s_waitcnt lgkmcnt(1)\n"
"v_lshlrev_b32 v22, 16, v22\n"
"v_mul_f32 v21, s61, v21\n"
"s_waitcnt lgkmcnt(0)\n"
"v_lshlrev_b32 v23, 16, v23\n"
"v_mul_f32 v22, s61, v22\n"
"v_mul_f32 v23, s61, v23\n"
"v_mov_b32 v178, v13\n"
"v_mov_b32 v179, v14\n"
"v_mov_b32 v180, v15\n"
"v_mov_b32 v181, v16\n"
"v_mov_b32 v182, v17\n"
"v_mov_b32 v183, v18\n"
"s_bfm_b64 exec, 40, 0\n"
"ds_write_b128 v8, v[180:183] offset:0\n"
"ds_write_b128 v8, v[14:17] offset:64\n"
"ds_write_b128 v8, v[178:181] offset:128\n"
"ds_write_b128 v8, v[12:15] offset:192\n"
"ds_write_b128 v8, v[20:23] offset:256\n"
"s_mov_b64 exec, -1\n"
"s_waitcnt lgkmcnt(0)\n"
"s_add_u32 s42, s46, 37248\n"
"s_addc_u32 s43, s47, 0\n"
"s_add_u32 m0, s50, 4294945792\n"
"s_nop 0\n"
"global_load_lds_dwordx4 v0, s[42:43]\n"
"s_add_u32 m0, s50, 4294946816\n"
"s_nop 0\n"
"global_load_lds_dwordx4 v1, s[42:43]\n"
"s_add_u32 m0, s50, 4294947840\n"
"s_nop 0\n"
"global_load_lds_dwordx4 v2, s[42:43]\n"
"s_add_u32 s42, s46, 74112\n"
"s_addc_u32 s43, s47, 0\n"
"s_add_u32 m0, s50, 49152\n"
"s_nop 0\n"
"global_load_lds_dwordx4 v0, s[42:43]\n"
"s_add_u32 m0, s50, 50176\n"
"s_nop 0\n"
"global_load_lds_dwordx4 v1, s[42:43]\n"
"s_add_u32 m0, s50, 51200\n"
"s_nop 0\n"
"global_load_lds_dwordx4 v2, s[42:43]\n"
"s_barrier\n"
"ds_read_b128 v[178:181], v4 offset:24576\n"
"s_waitcnt vmcnt(6)\n"
"ds_read_b64_tr_b16 v[8:9], v3 offset:4096\n"
"ds_read_b64_tr_b16 v[10:11], v3 offset:4864\n"
"ds_read_b64_tr_b16 v[12:13], v3 offset:4128\n"
"ds_read_b64_tr_b16 v[14:15], v3 offset:4896\n"
"ds_read_b64_tr_b16 v[16:17], v3 offset:4160\n"
"ds_read_b64_tr_b16 v[18:19], v3 offset:4928\n"
"ds_read_b128 v[182:185], v4 offset:30720\n"
"ds_read_b128 v[186:189], v4 offset:36864\n"
"s_waitcnt lgkmcnt(6)\n"
"v_mfma_f32_16x16x32_bf16 v[196:199], v[8:11], v[178:181], 0\n"
"s_waitcnt lgkmcnt(4)\n"
"v_mfma_f32_16x16x32_bf16 v[212:215], v[12:15], v[178:181], 0\n"
"s_waitcnt lgkmcnt(2)\n"
"ds_read_b128 v[190:193], v4 offset:43008\n"
"v_mfma_f32_16x16x32_bf16 v[216:219], v[16:19], v[178:181], 0\n"
"s_waitcnt lgkmcnt(2)\n"
"ds_read_b128 v[178:181], v4 offset:49152\n"
"v_mfma_f32_16x16x32_bf16 v[202:205], v[8:11], v[182:185], 0\n"
"v_mfma_f32_16x16x32_bf16 v[220:223], v[12:15], v[182:185], 0\n"
"v_mfma_f32_16x16x32_bf16 v[236:239], v[16:19], v[182:185], 0\n"
"s_waitcnt lgkmcnt(2)\n"
"v_mfma_f32_16x16x32_bf16 v[226:229], v[8:11], v[186:189], 0\n"
"s_waitcnt vmcnt(3)\n"
"ds_read_b64_tr_b16 v[22:23], v3 offset:7936\n"
"v_mfma_f32_16x16x32_bf16 v[240:243], v[12:15], v[186:189], 0\n"
"ds_read_b64_tr_b16 v[20:21], v3 offset:7168\n"
"v_mfma_f32_16x16x32_bf16 v[244:247], v[16:19], v[186:189], 0\n"
"s_waitcnt lgkmcnt(3)\n"
"ds_read_b128 v[182:185], v4 offset:55296\n"
"v_mfma_f32_16x16x32_bf16 v[232:235], v[8:11], v[190:193], 0\n"
"ds_read_b128 v[36:39], v5 offset:36864\n"
"v_mfma_f32_16x16x32_bf16 v[248:251], v[12:15], v[190:193], 0\n"
"ds_read_b64_tr_b16 v[28:29], v3 offset:7200\n"
"v_mfma_f32_16x16x32_bf16 v[252:255], v[16:19], v[190:193], 0\n"
"s_waitcnt lgkmcnt(5)\n"
"ds_read_b128 v[186:189], v5 offset:24576\n"
"v_mfma_f32_16x16x32_bf16 v[208:211], v[8:11], v[178:181], 0\n"
"ds_read_b64_tr_b16 v[30:31], v3 offset:7968\n"
"v_mfma_f32_16x16x32_bf16 v[190:193], v[12:15], v[178:181], 0\n"
"ds_read_b64_tr_b16 v[34:35], v3 offset:8000\n"
"v_mfma_f32_16x16x32_bf16 v[84:87], v[16:19], v[178:181], 0\n"
"s_waitcnt lgkmcnt(5)\n"
"v_mfma_f32_16x16x32_bf16 v[178:181], v[8:11], v[182:185], 0\n"
"ds_read_b64_tr_b16 v[32:33], v3 offset:7232\n"
"v_mfma_f32_16x16x32_bf16 v[88:91], v[12:15], v[182:185], 0\n"
"ds_read_b128 v[40:43], v5 offset:43008\n"
"v_mfma_f32_16x16x32_bf16 v[92:95], v[16:19], v[182:185], 0\n"
"s_waitcnt lgkmcnt(4)\n"
"ds_read_b128 v[182:185], v5 offset:30720\n"
"v_mfma_f32_16x16x32_bf16 v[196:199], v[20:23], v[186:189], v[196:199]\n"
"s_waitcnt lgkmcnt(4)\n"
"v_mfma_f32_16x16x32_bf16 v[212:215], v[28:31], v[186:189], v[212:215]\n"
"s_waitcnt lgkmcnt(2)\n"
"v_mfma_f32_16x16x32_bf16 v[216:219], v[32:35], v[186:189], v[216:219]\n"
"s_waitcnt lgkmcnt(0)\n"
"v_mfma_f32_16x16x32_bf16 v[202:205], v[20:23], v[182:185], v[202:205]\n"
"ds_read_b128 v[186:189], v5 offset:49152\n"
"v_mfma_f32_16x16x32_bf16 v[220:223], v[28:31], v[182:185], v[220:223]\n"
"v_mfma_f32_16x16x32_bf16 v[236:239], v[32:35], v[182:185], v[236:239]\n"
"ds_read_b128 v[182:185], v5 offset:55296\n"
"v_mfma_f32_16x16x32_bf16 v[226:229], v[20:23], v[36:39], v[226:229]\n"
"v_mfma_f32_16x16x32_bf16 v[240:243], v[28:31], v[36:39], v[240:243]\n"
"s_waitcnt vmcnt(0)\n"
"ds_read_b128 v[48:51], v4 offset:26624\n"
"v_mfma_f32_16x16x32_bf16 v[244:247], v[32:35], v[36:39], v[244:247]\n"
"ds_read_b64_tr_b16 v[36:37], v27 offset:0\n"
"v_mfma_f32_16x16x32_bf16 v[232:235], v[20:23], v[40:43], v[232:235]\n"
"v_mfma_f32_16x16x32_bf16 v[248:251], v[28:31], v[40:43], v[248:251]\n"
"ds_read_b64_tr_b16 v[44:45], v27 offset:64\n"
"v_mfma_f32_16x16x32_bf16 v[252:255], v[32:35], v[40:43], v[252:255]\n"
"s_waitcnt lgkmcnt(4)\n"
"ds_read_b64_tr_b16 v[40:41], v27 offset:32\n"
"v_mfma_f32_16x16x32_bf16 v[208:211], v[20:23], v[186:189], v[208:211]\n"
"ds_read_b64_tr_b16 v[38:39], v27 offset:768\n"
"v_mfma_f32_16x16x32_bf16 v[190:193], v[28:31], v[186:189], v[190:193]\n"
"ds_read_b64_tr_b16 v[46:47], v27 offset:832\n"
"v_mfma_f32_16x16x32_bf16 v[84:87], v[32:35], v[186:189], v[84:87]\n"
"s_waitcnt lgkmcnt(6)\n"
"ds_read_b64_tr_b16 v[42:43], v27 offset:800\n"
"v_mfma_f32_16x16x32_bf16 v[178:181], v[20:23], v[182:185], v[178:181]\n"
"ds_read_b128 v[52:55], v4 offset:45056\n"
"v_mfma_f32_16x16x32_bf16 v[88:91], v[28:31], v[182:185], v[88:91]\n"
"v_mfma_f32_16x16x32_bf16 v[92:95], v[32:35], v[182:185], v[92:95]\n"
"s_waitcnt lgkmcnt(3)\n"
"ds_read_b128 v[182:185], v4 offset:32768\n"
"v_mfma_f32_16x16x32_bf16 v[196:199], v[36:39], v[48:51], v[196:199]\n"
"s_waitcnt lgkmcnt(2)\n"
"ds_read_b128 v[186:189], v4 offset:38912\n"
"v_mfma_f32_16x16x32_bf16 v[212:215], v[40:43], v[48:51], v[212:215]\n"
"v_mfma_f32_16x16x32_bf16 v[216:219], v[44:47], v[48:51], v[216:219]\n"
"s_waitcnt lgkmcnt(1)\n"
"v_mfma_f32_16x16x32_bf16 v[202:205], v[36:39], v[182:185], v[202:205]\n"
"ds_read_b128 v[56:59], v4 offset:51200\n"
"v_mfma_f32_16x16x32_bf16 v[220:223], v[40:43], v[182:185], v[220:223]\n"
"s_barrier\n"
"v_mfma_f32_16x16x32_bf16 v[236:239], v[44:47], v[182:185], v[236:239]\n"
"s_waitcnt lgkmcnt(1)\n"
"ds_read_b64_tr_b16 v[48:49], v27 offset:3072\n"
"v_mfma_f32_16x16x32_bf16 v[226:229], v[36:39], v[186:189], v[226:229]\n"
"v_mfma_f32_16x16x32_bf16 v[240:243], v[40:43], v[186:189], v[240:243]\n"
"ds_read_b128 v[182:185], v4 offset:57344\n"
"v_mfma_f32_16x16x32_bf16 v[244:247], v[44:47], v[186:189], v[244:247]\n"
"v_mfma_f32_16x16x32_bf16 v[232:235], v[36:39], v[52:55], v[232:235]\n"
"ds_read_b128 v[64:67], v5 offset:45056\n"
"v_mfma_f32_16x16x32_bf16 v[248:251], v[40:43], v[52:55], v[248:251]\n"
"ds_read_b128 v[186:189], v5 offset:26624\n"
"v_mfma_f32_16x16x32_bf16 v[252:255], v[44:47], v[52:55], v[252:255]\n"
"s_waitcnt lgkmcnt(4)\n"
"ds_read_b64_tr_b16 v[50:51], v27 offset:3840\n"
"v_mfma_f32_16x16x32_bf16 v[208:211], v[36:39], v[56:59], v[208:211]\n"
"ds_read_b64_tr_b16 v[52:53], v27 offset:3104\n"
"v_mfma_f32_16x16x32_bf16 v[190:193], v[40:43], v[56:59], v[190:193]\n"
"v_mfma_f32_16x16x32_bf16 v[84:87], v[44:47], v[56:59], v[84:87]\n"
"s_waitcnt lgkmcnt(4)\n"
"ds_read_b64_tr_b16 v[58:59], v27 offset:3904\n"
"v_mfma_f32_16x16x32_bf16 v[178:181], v[36:39], v[182:185], v[178:181]\n"
"ds_read_b64_tr_b16 v[54:55], v27 offset:3872\n"
"v_mfma_f32_16x16x32_bf16 v[88:91], v[40:43], v[182:185], v[88:91]\n"
"ds_read_b64_tr_b16 v[56:57], v27 offset:3136\n"
"v_mfma_f32_16x16x32_bf16 v[92:95], v[44:47], v[182:185], v[92:95]\n"
"s_waitcnt lgkmcnt(4)\n"
"ds_read_b128 v[182:185], v5 offset:32768\n"
"v_mfma_f32_16x16x32_bf16 v[196:199], v[48:51], v[186:189], v[196:199]\n"
"s_waitcnt lgkmcnt(2)\n"
"ds_read_b128 v[80:83], v4 offset:28672\n"
"v_mfma_f32_16x16x32_bf16 v[212:215], v[52:55], v[186:189], v[212:215]\n"
"s_waitcnt lgkmcnt(2)\n"
"ds_read_b128 v[60:63], v5 offset:38912\n"
"v_mfma_f32_16x16x32_bf16 v[216:219], v[56:59], v[186:189], v[216:219]\n"
"s_waitcnt lgkmcnt(2)\n"
"v_mfma_f32_16x16x32_bf16 v[202:205], v[48:51], v[182:185], v[202:205]\n"
"ds_read_b128 v[186:189], v5 offset:51200\n"
"v_mfma_f32_16x16x32_bf16 v[220:223], v[52:55], v[182:185], v[220:223]\n"
"v_mfma_f32_16x16x32_bf16 v[236:239], v[56:59], v[182:185], v[236:239]\n"
"s_waitcnt lgkmcnt(1)\n"
"v_mfma_f32_16x16x32_bf16 v[226:229], v[48:51], v[60:63], v[226:229]\n"
"ds_read_b128 v[182:185], v5 offset:57344\n"
"v_mfma_f32_16x16x32_bf16 v[240:243], v[52:55], v[60:63], v[240:243]\n"
"v_mfma_f32_16x16x32_bf16 v[244:247], v[56:59], v[60:63], v[244:247]\n"
"s_barrier\n"
"v_mfma_f32_16x16x32_bf16 v[232:235], v[48:51], v[64:67], v[232:235]\n"
"ds_read_b64_tr_b16 v[70:71], v27 offset:25408\n"
"v_mfma_f32_16x16x32_bf16 v[248:251], v[52:55], v[64:67], v[248:251]\n"
"ds_read_b64_tr_b16 v[60:61], v27 offset:24576\n"
"v_mfma_f32_16x16x32_bf16 v[252:255], v[56:59], v[64:67], v[252:255]\n"
"s_waitcnt lgkmcnt(3)\n"
"ds_read_b64_tr_b16 v[64:65], v27 offset:24608\n"
"v_mfma_f32_16x16x32_bf16 v[208:211], v[48:51], v[186:189], v[208:211]\n"
"ds_read_b64_tr_b16 v[66:67], v27 offset:25376\n"
"v_mfma_f32_16x16x32_bf16 v[190:193], v[52:55], v[186:189], v[190:193]\n"
"ds_read_b64_tr_b16 v[62:63], v27 offset:25344\n"
"v_mfma_f32_16x16x32_bf16 v[84:87], v[56:59], v[186:189], v[84:87]\n"
"s_waitcnt lgkmcnt(5)\n"
"ds_read_b64_tr_b16 v[68:69], v27 offset:24640\n"
"v_mfma_f32_16x16x32_bf16 v[178:181], v[48:51], v[182:185], v[178:181]\n"
"ds_read_b64_tr_b16 v[72:73], v27 offset:27648\n"
"v_mfma_f32_16x16x32_bf16 v[88:91], v[52:55], v[182:185], v[88:91]\n"
"ds_read_b64_tr_b16 v[74:75], v27 offset:28416\n"
"v_mfma_f32_16x16x32_bf16 v[92:95], v[56:59], v[182:185], v[92:95]\n"
"s_waitcnt lgkmcnt(3)\n"
"ds_read_b128 v[182:185], v4 offset:34816\n"
"ds_read_b64_tr_b16 v[78:79], v27 offset:28448\n"
"v_mfma_f32_16x16x32_bf16 v[196:199], v[60:63], v[80:83], v[196:199]\n"
"ds_read_b64_tr_b16 v[76:77], v27 offset:27680\n"
"v_mfma_f32_16x16x32_bf16 v[212:215], v[64:67], v[80:83], v[212:215]\n"
"s_waitcnt lgkmcnt(5)\n"
"v_mfma_f32_16x16x32_bf16 v[216:219], v[68:71], v[80:83], v[216:219]\n"
"s_waitcnt lgkmcnt(2)\n"
"ds_read_b64_tr_b16 v[82:83], v27 offset:28480\n"
"ds_read_b64_tr_b16 v[80:81], v27 offset:27712\n"
"v_mfma_f32_16x16x32_bf16 v[202:205], v[60:63], v[182:185], v[202:205]\n"
"v_mfma_f32_16x16x32_bf16 v[220:223], v[64:67], v[182:185], v[220:223]\n"
"ds_read_b128 v[186:189], v4 offset:40960\n"
"ds_read_b128 v[108:111], v4 offset:53248\n"
"v_mfma_f32_16x16x32_bf16 v[236:239], v[68:71], v[182:185], v[236:239]\n"
"s_waitcnt lgkmcnt(2)\n"
"s_barrier\n"
"ds_read_b128 v[96:99], v4 offset:47104\n"
"s_waitcnt lgkmcnt(2)\n"
"v_mfma_f32_16x16x32_bf16 v[226:229], v[60:63], v[186:189], v[226:229]\n"
"ds_read_b128 v[100:103], v5 offset:34816\n"
"v_mfma_f32_16x16x32_bf16 v[240:243], v[64:67], v[186:189], v[240:243]\n"
"v_mfma_f32_16x16x32_bf16 v[244:247], v[68:71], v[186:189], v[244:247]\n"
"s_waitcnt lgkmcnt(1)\n"
"v_mfma_f32_16x16x32_bf16 v[232:235], v[60:63], v[96:99], v[232:235]\n"
"ds_read_b128 v[182:185], v5 offset:28672\n"
"ds_read_b128 v[116:119], v4 offset:59392\n"
"v_mfma_f32_16x16x32_bf16 v[248:251], v[64:67], v[96:99], v[248:251]\n"
"v_mfma_f32_16x16x32_bf16 v[252:255], v[68:71], v[96:99], v[252:255]\n"
"ds_read_b128 v[186:189], v5 offset:47104\n"
"ds_read_b128 v[104:107], v5 offset:40960\n"
"ds_read_b128 v[112:115], v5 offset:53248\n"
"v_mfma_f32_16x16x32_bf16 v[208:211], v[60:63], v[108:111], v[208:211]\n"
"v_mfma_f32_16x16x32_bf16 v[190:193], v[64:67], v[108:111], v[190:193]\n"
"ds_read_b128 v[120:123], v5 offset:59392\n"
"v_mfma_f32_16x16x32_bf16 v[84:87], v[68:71], v[108:111], v[84:87]\n"
"s_waitcnt lgkmcnt(4)\n"
"v_mfma_f32_16x16x32_bf16 v[178:181], v[60:63], v[116:119], v[178:181]\n"
"v_mfma_f32_16x16x32_bf16 v[88:91], v[64:67], v[116:119], v[88:91]\n"
"v_mfma_f32_16x16x32_bf16 v[92:95], v[68:71], v[116:119], v[92:95]\n"
"v_mfma_f32_16x16x32_bf16 v[196:199], v[72:75], v[182:185], v[196:199]\n"
"v_mfma_f32_16x16x32_bf16 v[212:215], v[76:79], v[182:185], v[212:215]\n"
"v_mfma_f32_16x16x32_bf16 v[216:219], v[80:83], v[182:185], v[216:219]\n"
"v_mfma_f32_16x16x32_bf16 v[202:205], v[72:75], v[100:103], v[202:205]\n"
"v_mfma_f32_16x16x32_bf16 v[220:223], v[76:79], v[100:103], v[220:223]\n"
"v_mfma_f32_16x16x32_bf16 v[236:239], v[80:83], v[100:103], v[236:239]\n"
"s_waitcnt lgkmcnt(2)\n"
"v_mfma_f32_16x16x32_bf16 v[226:229], v[72:75], v[104:107], v[226:229]\n"
"v_mfma_f32_16x16x32_bf16 v[240:243], v[76:79], v[104:107], v[240:243]\n"
"v_mfma_f32_16x16x32_bf16 v[244:247], v[80:83], v[104:107], v[244:247]\n"
"v_mfma_f32_16x16x32_bf16 v[232:235], v[72:75], v[186:189], v[232:235]\n"
"v_mfma_f32_16x16x32_bf16 v[248:251], v[76:79], v[186:189], v[248:251]\n"
"v_mfma_f32_16x16x32_bf16 v[252:255], v[80:83], v[186:189], v[252:255]\n"
"s_waitcnt lgkmcnt(1)\n"
"v_mfma_f32_16x16x32_bf16 v[208:211], v[72:75], v[112:115], v[208:211]\n"
"v_mfma_f32_16x16x32_bf16 v[190:193], v[76:79], v[112:115], v[190:193]\n"
"s_nop 1\n"
"v_cvt_pk_bf16_f32 v231, v246, v247\n"
"v_mfma_f32_16x16x32_bf16 v[84:87], v[80:83], v[112:115], v[84:87]\n"
"s_waitcnt lgkmcnt(0)\n"
"v_cvt_pk_bf16_f32 v206, v236, v237\n"
"v_cvt_pk_bf16_f32 v196, v196, v197\n"
"v_mfma_f32_16x16x32_bf16 v[178:181], v[72:75], v[120:123], v[178:181]\n"
"v_mfma_f32_16x16x32_bf16 v[88:91], v[76:79], v[120:123], v[88:91]\n"
"v_mfma_f32_16x16x32_bf16 v[92:95], v[80:83], v[120:123], v[92:95]\n"
"s_barrier\n"
"v_cvt_pk_bf16_f32 v197, v198, v199\n"
"v_cvt_pk_bf16_f32 v237, v254, v255\n"
"v_cvt_pk_bf16_f32 v208, v208, v209\n"
"v_cvt_pk_bf16_f32 v226, v226, v227\n"
"v_cvt_pk_bf16_f32 v230, v244, v245\n"
"v_cvt_pk_bf16_f32 v201, v218, v219\n"
"s_add_u32 s42, s46, 0\n"
"s_addc_u32 s43, s47, 0\n"
"s_add_u32 m0, s50, 24576\n"
"s_nop 0\n"
"global_load_lds_dwordx4 v0, s[42:43]\n"
"v_cvt_pk_bf16_f32 v227, v228, v229\n"
"v_cvt_pk_bf16_f32 v232, v232, v233\n"
"v_cvt_pk_bf16_f32 v233, v234, v235\n"
"v_cvt_pk_bf16_f32 v198, v212, v213\n"
"v_cvt_pk_bf16_f32 v200, v216, v217\n"
"s_add_u32 m0, s50, 25600\n"
"s_nop 0\n"
"global_load_lds_dwordx4 v1, s[42:43]\n"
"s_add_u32 m0, s50, 26624\n"
"s_nop 0\n"
"global_load_lds_dwordx4 v2, s[42:43]\n"
"v_cvt_pk_bf16_f32 v202, v202, v203\n"
"v_cvt_pk_bf16_f32 v203, v204, v205\n"
"v_cvt_pk_bf16_f32 v205, v222, v223\n"
"v_cvt_pk_bf16_f32 v209, v210, v211\n"
"v_cvt_pk_bf16_f32 v183, v94, v95\n"
"s_barrier\n"
"v_cvt_pk_bf16_f32 v178, v178, v179\n"
"s_add_u32 s42, s46, 36864\n"
"s_addc_u32 s43, s47, 0\n"
"s_add_u32 m0, s50, 27648\n"
"s_nop 0\n"
"global_load_lds_dwordx4 v0, s[42:43]\n"
"s_add_u32 m0, s50, 28672\n"
"s_nop 0\n"
"global_load_lds_dwordx4 v1, s[42:43]\n"
"v_cvt_pk_bf16_f32 v179, v180, v181\n"
"s_add_u32 m0, s50, 29696\n"
"s_nop 0\n"
"global_load_lds_dwordx4 v2, s[42:43]\n"
"v_cvt_pk_bf16_f32 v235, v250, v251\n"
"s_add_u32 s42, s46, 73728\n"
"s_addc_u32 s43, s47, 0\n"
"s_add_u32 m0, s50, 4294942720\n"
"s_nop 0\n"
"global_load_lds_dwordx4 v0, s[42:43]\n"
"v_cvt_pk_bf16_f32 v234, v248, v249\n"
"s_add_u32 m0, s50, 4294943744\n"
"s_nop 0\n"
"global_load_lds_dwordx4 v1, s[42:43]\n"
"v_cvt_pk_bf16_f32 v212, v84, v85\n"
"v_cvt_pk_bf16_f32 v180, v88, v89\n"
"v_cvt_pk_bf16_f32 v213, v86, v87\n"
"v_cvt_pk_bf16_f32 v181, v90, v91\n"
"s_add_u32 m0, s50, 4294944768\n"
"s_nop 0\n"
"global_load_lds_dwordx4 v2, s[42:43]\n"
"s_add_u32 s42, s46, 110592\n"
"s_addc_u32 s43, s47, 0\n"
"s_add_u32 m0, s50, 4294945792\n"
"s_nop 0\n"
"global_load_lds_dwordx4 v0, s[42:43]\n"
"v_cvt_pk_bf16_f32 v211, v192, v193\n"
"v_cvt_pk_bf16_f32 v210, v190, v191\n"
"s_add_u32 m0, s50, 4294946816\n"
"s_nop 0\n"
"global_load_lds_dwordx4 v1, s[42:43]\n"
"s_add_u32 m0, s50, 4294947840\n"
"s_nop 0\n"
"global_load_lds_dwordx4 v2, s[42:43]\n"
"v_cvt_pk_bf16_f32 v199, v214, v215\n"
"s_add_u32 s42, s46, 147456\n"
"s_addc_u32 s43, s47, 0\n"
"s_add_u32 m0, s50, 49152\n"
"s_nop 0\n"
"global_load_lds_dwordx4 v0, s[42:43]\n"
"s_add_u32 m0, s50, 50176\n"
"s_nop 0\n"
"global_load_lds_dwordx4 v1, s[42:43]\n"
"v_cvt_pk_bf16_f32 v228, v240, v241\n"
"s_add_u32 m0, s50, 51200\n"
"s_nop 0\n"
"global_load_lds_dwordx4 v2, s[42:43]\n"
"s_add_u32 s42, s46, 184320\n"
"s_addc_u32 s43, s47, 0\n"
"s_add_u32 m0, s50, 52224\n"
"s_nop 0\n"
"global_load_lds_dwordx4 v0, s[42:43]\n"
"v_cvt_pk_bf16_f32 v207, v238, v239\n"
"s_add_u32 m0, s50, 53248\n"
"s_nop 0\n"
"global_load_lds_dwordx4 v1, s[42:43]\n"
"v_cvt_pk_bf16_f32 v204, v220, v221\n"
"s_add_u32 m0, s50, 54272\n"
"s_nop 0\n"
"global_load_lds_dwordx4 v2, s[42:43]\n"
"v_cvt_pk_bf16_f32 v236, v252, v253\n"
"s_barrier\n"
"ds_read_b128 v[184:187], v4 offset:6144\n"
"s_waitcnt vmcnt(15)\n"
"ds_read_b64_tr_b16 v[86:87], v3 offset:54016\n"
"s_waitcnt vmcnt(12)\n"
"ds_read_b64_tr_b16 v[96:97], v3 offset:56320\n"
"ds_read_b128 v[188:191], v5 offset:6144\n"
"v_cvt_pk_bf16_f32 v229, v242, v243\n"
"s_waitcnt lgkmcnt(3)\n"
"v_mfma_f32_16x16x32_bf16 v[238:241], v[8:11], v[184:187], 0\n"
"v_cvt_pk_bf16_f32 v182, v92, v93\n"
"s_waitcnt vmcnt(9)\n"
"s_sub_u32 m0, s50, 28672\n"
"v_mfma_f32_16x16x32_bf16 v[214:217], v[12:15], v[184:187], 0\n"
"ds_read_b64_tr_b16 v[104:105], v3 offset:4096\n"
"s_waitcnt vmcnt(6)\n"
"v_mfma_f32_16x16x32_bf16 v[218:221], v[16:19], v[184:187], 0\n"
"s_waitcnt lgkmcnt(1)\n"
"v_mfma_f32_16x16x32_bf16 v[238:241], v[20:23], v[188:191], v[238:241]\n"
"ds_read_b64_tr_b16 v[88:89], v3 offset:53280\n"
"v_mfma_f32_16x16x32_bf16 v[214:217], v[28:31], v[188:191], v[214:217]\n"
"ds_read_b64_tr_b16 v[106:107], v3 offset:4864\n"
"ds_read_b128 v[192:195], v4 offset:8192\n"
"ds_read_b64_tr_b16 v[222:223], v3 offset:56384\n"
"ds_read_b64_tr_b16 v[94:95], v3 offset:54080\n"
"ds_read_b64_tr_b16 v[92:93], v3 offset:53312\n"
"ds_read_b64_tr_b16 v[102:103], v3 offset:57120\n"
"v_mfma_f32_16x16x32_bf16 v[218:221], v[32:35], v[188:191], v[218:221]\n"
"s_waitcnt lgkmcnt(4)\n"
"ds_read_b64_tr_b16 v[224:225], v3 offset:57152\n"
"v_mfma_f32_16x16x32_bf16 v[238:241], v[36:39], v[192:195], v[238:241]\n"
"ds_read_b64_tr_b16 v[244:245], v3 offset:4160\n"
"ds_read_b128 v[184:187], v5 offset:8192\n"
"ds_read_b64_tr_b16 v[114:115], v3 offset:7936\n"
"v_mfma_f32_16x16x32_bf16 v[214:217], v[40:43], v[192:195], v[214:217]\n"
"ds_read_b64_tr_b16 v[100:101], v3 offset:56352\n"
"ds_read_b64_tr_b16 v[98:99], v3 offset:57088\n"
"ds_read_b64_tr_b16 v[84:85], v3 offset:53248\n"
"v_mfma_f32_16x16x32_bf16 v[218:221], v[44:47], v[192:195], v[218:221]\n"
"ds_read_b64_tr_b16 v[90:91], v3 offset:54048\n"
"s_waitcnt vmcnt(3)\n"
"ds_read_b128 v[188:191], v4 offset:10240\n"
"ds_read_b64_tr_b16 v[116:117], v3 offset:7200\n"
"s_waitcnt lgkmcnt(7)\n"
"v_mfma_f32_16x16x32_bf16 v[238:241], v[48:51], v[184:187], v[238:241]\n"
"ds_read_b64_tr_b16 v[108:109], v3 offset:4128\n"
"ds_read_b64_tr_b16 v[110:111], v3 offset:4896\n"
"v_mfma_f32_16x16x32_bf16 v[214:217], v[52:55], v[184:187], v[214:217]\n"
"ds_read_b64_tr_b16 v[248:249], v3 offset:7232\n"
"ds_read_b128 v[192:195], v5 offset:10240\n"
"v_mfma_f32_16x16x32_bf16 v[218:221], v[56:59], v[184:187], v[218:221]\n"
"s_waitcnt lgkmcnt(5)\n"
"ds_read_b64_tr_b16 v[112:113], v3 offset:7168\n"
"ds_read_b64_tr_b16 v[246:247], v3 offset:4928\n"
"v_mfma_f32_16x16x32_bf16 v[238:241], v[60:63], v[188:191], v[238:241]\n"
"ds_read_b64_tr_b16 v[250:251], v3 offset:8000\n"
"ds_read_b64_tr_b16 v[120:121], v27 offset:32\n"
"v_mfma_f32_16x16x32_bf16 v[214:217], v[64:67], v[188:191], v[214:217]\n"
"ds_read_b128 v[184:187], v4 offset:0\n"
"v_mfma_f32_16x16x32_bf16 v[218:221], v[68:71], v[188:191], v[218:221]\n"
"ds_read_b128 v[188:191], v5 offset:0\n"
"ds_read_b64_tr_b16 v[118:119], v3 offset:7968\n"
"s_waitcnt vmcnt(0)\n"
"ds_read_b64_tr_b16 v[130:131], v27 offset:3872\n"
"s_waitcnt lgkmcnt(8)\n"
"v_mfma_f32_16x16x32_bf16 v[238:241], v[72:75], v[192:195], v[238:241]\n"
"ds_read_b64_tr_b16 v[0:1], v27 offset:0\n"
"ds_read_b64_tr_b16 v[122:123], v27 offset:800\n"
"ds_read_b64_tr_b16 v[2:3], v27 offset:768\n"
"v_mfma_f32_16x16x32_bf16 v[214:217], v[76:79], v[192:195], v[214:217]\n"
"ds_read_b64_tr_b16 v[254:255], v27 offset:832\n"
"ds_read_b64_tr_b16 v[128:129], v27 offset:3104\n"
"v_mfma_f32_16x16x32_bf16 v[218:221], v[80:83], v[192:195], v[218:221]\n"
"s_waitcnt lgkmcnt(8)\n"
"ds_read_b64_tr_b16 v[252:253], v27 offset:64\n"
"v_cvt_pk_bf16_f32 v238, v238, v239\n"
"v_mfma_f32_16x16x32_bf16 v[132:135], v[84:87], v[184:187], 0\n"
"ds_read_b64_tr_b16 v[126:127], v27 offset:3840\n"
"v_cvt_pk_bf16_f32 v239, v240, v241\n"
"v_cvt_pk_bf16_f32 v240, v214, v215\n"
"v_mfma_f32_16x16x32_bf16 v[192:195], v[88:91], v[184:187], 0\n"
"ds_read_b64_tr_b16 v[214:215], v27 offset:3136\n"
"v_cvt_pk_bf16_f32 v243, v220, v221\n"
"v_cvt_pk_bf16_f32 v242, v218, v219\n"
"v_mfma_f32_16x16x32_bf16 v[218:221], v[92:95], v[184:187], 0\n"
"s_waitcnt lgkmcnt(10)\n"
"ds_read_b128 v[184:187], v4 offset:2048\n"
"v_cvt_pk_bf16_f32 v241, v216, v217\n"
"v_mfma_f32_16x16x32_bf16 v[132:135], v[96:99], v[188:191], v[132:135]\n"
"ds_read_b64_tr_b16 v[216:217], v27 offset:3904\n"
"v_mfma_f32_16x16x32_bf16 v[192:195], v[100:103], v[188:191], v[192:195]\n"
"ds_read_b64_tr_b16 v[124:125], v27 offset:3072\n"
"ds_read_b128 v[136:139], v5 offset:2048\n"
"v_mfma_f32_16x16x32_bf16 v[218:221], v[222:225], v[188:191], v[218:221]\n"
"s_waitcnt lgkmcnt(3)\n"
"v_mfma_f32_16x16x32_bf16 v[132:135], v[104:107], v[184:187], v[132:135]\n"
"v_mfma_f32_16x16x32_bf16 v[192:195], v[108:111], v[184:187], v[192:195]\n"
"v_mfma_f32_16x16x32_bf16 v[218:221], v[244:247], v[184:187], v[218:221]\n"
"s_waitcnt lgkmcnt(0)\n"
"v_mfma_f32_16x16x32_bf16 v[132:135], v[112:115], v[136:139], v[132:135]\n"
"ds_read_b128 v[188:191], v4 offset:4096\n"
"v_mfma_f32_16x16x32_bf16 v[192:195], v[116:119], v[136:139], v[192:195]\n"
"s_barrier\n"
"v_bfe_u32 v177, v6, 2, 6\n"
"ds_read_b128 v[184:187], v5 offset:4096\n"
"v_mfma_f32_16x16x32_bf16 v[218:221], v[248:251], v[136:139], v[218:221]\n"
"s_waitcnt lgkmcnt(1)\n"
"ds_read_b32 v177, v177 offset:1216\n"
"v_mfma_f32_16x16x32_bf16 v[132:135], v[0:3], v[188:191], v[132:135]\n"
"v_mfma_f32_16x16x32_bf16 v[192:195], v[120:123], v[188:191], v[192:195]\n"
"v_mfma_f32_16x16x32_bf16 v[218:221], v[252:255], v[188:191], v[218:221]\n"
"s_waitcnt lgkmcnt(1)\n"
"v_mfma_f32_16x16x32_bf16 v[132:135], v[124:127], v[184:187], v[132:135]\n"
"ds_read_b128 v[136:139], v4 offset:12288\n"
"v_mfma_f32_16x16x32_bf16 v[192:195], v[128:131], v[184:187], v[192:195]\n"
"s_waitcnt lgkmcnt(1)\n"
"ds_read_b128 v[140:143], v4 offset:14336\n"
"ds_read_b128 v[188:191], v5 offset:12288\n"
"v_add_u32_sdwa v176, v177, v7 dst_sel:DWORD dst_unused:UNUSED_PAD src0_sel:WORD_0 src1_sel:DWORD\n"
"v_mfma_f32_16x16x32_bf16 v[218:221], v[214:217], v[184:187], v[218:221]\n"
"s_waitcnt lgkmcnt(2)\n"
"v_mfma_f32_16x16x32_bf16 v[184:187], v[8:11], v[136:139], 0\n"
"s_add_u32 s68, s50, 0\n"
"v_mul_f32 v133, s60, v133\n"
"v_mfma_f32_16x16x32_bf16 v[144:147], v[12:15], v[136:139], 0\n"
"v_mul_f32 v134, s60, v134\n"
"v_max_u32 v177, s68, v176\n"
"v_mfma_f32_16x16x32_bf16 v[152:155], v[16:19], v[136:139], 0\n"
"s_waitcnt lgkmcnt(0)\n"
"ds_read_b128 v[136:139], v5 offset:14336\n"
"v_mul_f32 v135, s60, v135\n"
"v_mfma_f32_16x16x32_bf16 v[184:187], v[20:23], v[188:191], v[184:187]\n"
"v_mul_f32 v132, s60, v132\n"
"v_mul_f32 v193, s60, v193\n"
"v_mfma_f32_16x16x32_bf16 v[144:147], v[28:31], v[188:191], v[144:147]\n"
"ds_read_b128 v[148:151], v177 offset:16\n"
"v_mul_f32 v194, s60, v194\n"
"v_mul_f32 v192, s60, v192\n"
"v_cvt_pk_bf16_f32 v132, v132, v133\n"
"v_mfma_f32_16x16x32_bf16 v[152:155], v[32:35], v[188:191], v[152:155]\n"
"ds_read_b128 v[188:191], v4 offset:16384\n"
"v_mul_f32 v219, s60, v219\n"
"v_mfma_f32_16x16x32_bf16 v[184:187], v[36:39], v[140:143], v[184:187]\n"
"v_cvt_pk_bf16_f32 v133, v134, v135\n"
"v_cvt_pk_bf16_f32 v134, v192, v193\n"
"v_mfma_f32_16x16x32_bf16 v[144:147], v[40:43], v[140:143], v[144:147]\n"
"v_mul_f32 v195, s60, v195\n"
"v_cvt_pk_bf16_f32 v135, v194, v195\n"
"v_mul_f32 v220, s60, v220\n"
"v_mul_f32 v221, s60, v221\n"
"v_mul_f32 v218, s60, v218\n"
"v_cvt_pk_bf16_f32 v27, v220, v221\n"
"v_cvt_pk_bf16_f32 v26, v218, v219\n"
"ds_read_b128 v[218:221], v177 offset:32\n"
"s_waitcnt lgkmcnt(2)\n"
"v_mfma_f32_16x16x32_bf16 v[148:151], v[202:205], v[132:135], v[148:151]\n"
"s_add_u32 s69, s50, 1040\n"
"v_min_u32 v175, s69, v176\n"
"v_mfma_f32_16x16x32_bf16 v[148:151], v[204:207], v[24:27], v[148:151]\n"
"ds_write_b128 v6, v[222:225] offset:5120\n"
"v_mfma_f32_16x16x32_bf16 v[152:155], v[44:47], v[140:143], v[152:155]\n"
"v_mfma_f32_16x16x32_bf16 v[184:187], v[48:51], v[136:139], v[184:187]\n"
"v_mfma_f32_16x16x32_bf16 v[144:147], v[52:55], v[136:139], v[144:147]\n"
"v_mfma_f32_16x16x32_bf16 v[152:155], v[56:59], v[136:139], v[152:155]\n"
"s_waitcnt lgkmcnt(2)\n"
"ds_read_b128 v[192:195], v5 offset:16384\n"
"v_mfma_f32_16x16x32_bf16 v[184:187], v[60:63], v[188:191], v[184:187]\n"
"ds_read_b128 v[140:143], v175 offset:1344\n"
"ds_read_b128 v[136:139], v4 offset:18432\n"
"v_mfma_f32_16x16x32_bf16 v[144:147], v[64:67], v[188:191], v[144:147]\n"
"s_waitcnt lgkmcnt(4)\n"
"v_mfma_f32_16x16x32_bf16 v[218:221], v[208:211], v[132:135], v[218:221]\n"
"ds_read_b128 v[156:159], v5 offset:18432\n"
"v_mfma_f32_16x16x32_bf16 v[218:221], v[210:213], v[24:27], v[218:221]\n"
"v_exp_f32 v148, v148\n"
"v_exp_f32 v149, v149\n"
"s_nop 0\n"
"v_cvt_pk_bf16_f32 v148, v148, v149\n"
"v_exp_f32 v150, v150\n"
"v_mfma_f32_16x16x32_bf16 v[152:155], v[68:71], v[188:191], v[152:155]\n"
"ds_write_addtid_b32 v148 offset:53248\n"
"v_exp_f32 v151, v151\n"
"s_waitcnt lgkmcnt(4)\n"
"v_mfma_f32_16x16x32_bf16 v[184:187], v[72:75], v[192:195], v[184:187]\n"
"v_cvt_pk_bf16_f32 v149, v150, v151\n"
"v_mfma_f32_16x16x32_bf16 v[144:147], v[76:79], v[192:195], v[144:147]\n"
"ds_write_addtid_b32 v149 offset:53504\n"
"v_mfma_f32_16x16x32_bf16 v[152:155], v[80:83], v[192:195], v[152:155]\n"
"ds_write_b128 v6, v[244:247] offset:6144\n"
"s_waitcnt lgkmcnt(4)\n"
"v_mfma_f32_16x16x32_bf16 v[190:193], v[8:11], v[136:139], 0\n"
"s_nop 0\n"
"v_cvt_pk_bf16_f32 v184, v184, v185\n"
"v_cvt_pk_bf16_f32 v185, v186, v187\n"
"v_exp_f32 v218, v218\n"
"v_mfma_f32_16x16x32_bf16 v[140:143], v[226:229], v[132:135], v[140:143]\n"
"v_cvt_pk_bf16_f32 v188, v152, v153\n"
"v_cvt_pk_bf16_f32 v187, v146, v147\n"
"v_mfma_f32_16x16x32_bf16 v[140:143], v[228:231], v[24:27], v[140:143]\n"
"v_cvt_pk_bf16_f32 v186, v144, v145\n"
"v_cvt_pk_bf16_f32 v189, v154, v155\n"
"ds_read_b128 v[144:147], v175 offset:1360\n"
"v_mfma_f32_16x16x32_bf16 v[148:151], v[12:15], v[136:139], 0\n"
"ds_read_b128 v[152:155], v4 offset:20480\n"
"v_mfma_f32_16x16x32_bf16 v[160:163], v[16:19], v[136:139], 0\n"
"s_waitcnt lgkmcnt(5)\n"
"v_exp_f32 v219, v219\n"
"v_mfma_f32_16x16x32_bf16 v[190:193], v[20:23], v[156:159], v[190:193]\n"
"v_exp_f32 v140, v140\n"
"v_mfma_f32_16x16x32_bf16 v[148:151], v[28:31], v[156:159], v[148:151]\n"
"v_cvt_pk_bf16_f32 v218, v218, v219\n"
"ds_write_addtid_b32 v218 offset:53760\n"
"ds_read_b128 v[218:221], v5 offset:20480\n"
"v_mfma_f32_16x16x32_bf16 v[160:163], v[32:35], v[156:159], v[160:163]\n"
"s_waitcnt lgkmcnt(3)\n"
"v_exp_f32 v141, v141\n"
"v_mfma_f32_16x16x32_bf16 v[144:147], v[232:235], v[132:135], v[144:147]\n"
"ds_read_b128 v[156:159], v4 offset:22528\n"
"v_exp_f32 v142, v142\n"
"v_mfma_f32_16x16x32_bf16 v[144:147], v[234:237], v[24:27], v[144:147]\n"
"s_waitcnt lgkmcnt(3)\n"
"ds_read_b128 v[136:139], v177 offset:0\n"
"v_mfma_f32_16x16x32_bf16 v[190:193], v[36:39], v[152:155], v[190:193]\n"
"v_exp_f32 v143, v143\n"
"v_mfma_f32_16x16x32_bf16 v[148:151], v[40:43], v[152:155], v[148:151]\n"
"ds_write_b128 v6, v[248:251] offset:7168\n"
"v_cvt_pk_bf16_f32 v140, v140, v141\n"
"v_cvt_pk_bf16_f32 v141, v142, v143\n"
"v_mfma_f32_16x16x32_bf16 v[160:163], v[44:47], v[152:155], v[160:163]\n"
"s_add_u32 s70, s50, 4294966992\n"
"ds_write_addtid_b32 v140 offset:54272\n"
"s_waitcnt lgkmcnt(4)\n"
"v_mfma_f32_16x16x32_bf16 v[190:193], v[48:51], v[218:221], v[190:193]\n"
"ds_write_addtid_b32 v141 offset:54528\n"
"v_exp_f32 v144, v144\n"
"v_exp_f32 v145, v145\n"
"v_mfma_f32_16x16x32_bf16 v[148:151], v[52:55], v[218:221], v[148:151]\n"
"s_waitcnt lgkmcnt(3)\n"
"ds_read_b128 v[140:143], v5 offset:22528\n"
"v_exp_f32 v146, v146\n"
"v_min_u32 v177, s70, v176\n"
"v_mfma_f32_16x16x32_bf16 v[136:139], v[196:199], v[132:135], v[136:139]\n"
"v_exp_f32 v147, v147\n"
"v_mfma_f32_16x16x32_bf16 v[136:139], v[198:201], v[24:27], v[136:139]\n"
"ds_read_b128 v[152:155], v177 offset:2688\n"
"v_cvt_pk_bf16_f32 v144, v144, v145\n"
"v_cvt_pk_bf16_f32 v145, v146, v147\n"
"v_mfma_f32_16x16x32_bf16 v[160:163], v[56:59], v[218:221], v[160:163]\n"
"ds_write_addtid_b32 v144 offset:54784\n"
"ds_write_addtid_b32 v145 offset:55040\n"
"v_mfma_f32_16x16x32_bf16 v[190:193], v[60:63], v[156:159], v[190:193]\n"
"s_nop 0\n"
"v_exp_f32 v136, v136\n"
"v_mfma_f32_16x16x32_bf16 v[148:151], v[64:67], v[156:159], v[148:151]\n"
"v_exp_f32 v137, v137\n"
"v_mfma_f32_16x16x32_bf16 v[160:163], v[68:71], v[156:159], v[160:163]\n"
"s_waitcnt lgkmcnt(3)\n"
"ds_read_b128 v[218:221], v175 offset:1376\n"
"v_mfma_f32_16x16x32_bf16 v[190:193], v[72:75], v[140:143], v[190:193]\n"
"s_waitcnt lgkmcnt(3)\n"
"v_cvt_pk_bf16_f32 v136, v136, v137\n"
"v_mfma_f32_16x16x32_bf16 v[152:155], v[178:181], v[132:135], v[152:155]\n"
"v_exp_f32 v138, v138\n"
"v_mfma_f32_16x16x32_bf16 v[152:155], v[180:183], v[24:27], v[152:155]\n"
"s_waitcnt lgkmcnt(1)\n"
"s_barrier\n"
"v_mfma_f32_16x16x32_bf16 v[148:151], v[76:79], v[140:143], v[148:151]\n"
"ds_read_b128 v[164:167], v4 offset:38912\n"
"ds_read_b128 v[196:199], v4 offset:36864\n"
"v_mfma_f32_16x16x32_bf16 v[160:163], v[80:83], v[140:143], v[160:163]\n"
"ds_write_addtid_b32 v136 offset:58880\n"
"v_cvt_pk_bf16_f32 v190, v190, v191\n"
"v_cvt_pk_bf16_f32 v191, v192, v193\n"
"v_exp_f32 v139, v139\n"
"s_nop 0\n"
"v_cvt_pk_bf16_f32 v137, v138, v139\n"
"v_cvt_pk_bf16_f32 v192, v148, v149\n"
"ds_read_b128 v[144:147], v5 offset:36864\n"
"s_waitcnt lgkmcnt(2)\n"
"v_mfma_f32_16x16x32_bf16 v[140:143], v[84:87], v[196:199], 0\n"
"ds_write_b128 v6, v[252:255] offset:8192\n"
"v_mfma_f32_16x16x32_bf16 v[156:159], v[88:91], v[196:199], 0\n"
"ds_write_addtid_b32 v137 offset:59136\n"
"v_cvt_pk_bf16_f32 v194, v160, v161\n"
"v_mfma_f32_16x16x32_bf16 v[136:139], v[92:95], v[196:199], 0\n"
"v_exp_f32 v152, v152\n"
"v_mfma_f32_16x16x32_bf16 v[218:221], v[238:241], v[132:135], v[218:221]\n"
"ds_read_b128 v[196:199], v177 offset:2704\n"
"v_cvt_pk_bf16_f32 v195, v162, v163\n"
"v_mfma_f32_16x16x32_bf16 v[218:221], v[240:243], v[24:27], v[218:221]\n"
"s_waitcnt lgkmcnt(3)\n"
"v_exp_f32 v153, v153\n"
"v_exp_f32 v154, v154\n"
"v_exp_f32 v155, v155\n"
"v_mfma_f32_16x16x32_bf16 v[140:143], v[96:99], v[144:147], v[140:143]\n"
"v_cvt_pk_bf16_f32 v152, v152, v153\n"
"v_cvt_pk_bf16_f32 v193, v150, v151\n"
"v_mfma_f32_16x16x32_bf16 v[156:159], v[100:103], v[144:147], v[156:159]\n"
"ds_read_b128 v[148:151], v5 offset:38912\n"
"v_mfma_f32_16x16x32_bf16 v[136:139], v[222:225], v[144:147], v[136:139]\n"
"ds_read_b128 v[144:147], v177 offset:2720\n"
"v_mfma_f32_16x16x32_bf16 v[140:143], v[104:107], v[164:167], v[140:143]\n"
"v_exp_f32 v218, v218\n"
"v_mfma_f32_16x16x32_bf16 v[156:159], v[108:111], v[164:167], v[156:159]\n"
"ds_write_addtid_b32 v152 offset:57344\n"
"s_waitcnt lgkmcnt(3)\n"
"v_mfma_f32_16x16x32_bf16 v[196:199], v[184:187], v[132:135], v[196:199]\n"
"ds_read_b128 v[220:223], v4 offset:40960\n"
"v_cvt_pk_bf16_f32 v153, v154, v155\n"
"ds_write_addtid_b32 v153 offset:57600\n"
"v_mfma_f32_16x16x32_bf16 v[196:199], v[186:189], v[24:27], v[196:199]\n"
"v_exp_f32 v219, v219\n"
"v_mfma_f32_16x16x32_bf16 v[136:139], v[244:247], v[164:167], v[136:139]\n"
"s_waitcnt lgkmcnt(4)\n"
"ds_read_b128 v[244:247], v5 offset:40960\n"
"v_mfma_f32_16x16x32_bf16 v[140:143], v[112:115], v[148:151], v[140:143]\n"
"ds_write_b128 v6, v[214:217] offset:9216\n"
"v_mfma_f32_16x16x32_bf16 v[156:159], v[116:119], v[148:151], v[156:159]\n"
"v_bfe_u32 v225, v6, 2, 6\n"
"v_cvt_pk_bf16_f32 v218, v218, v219\n"
"v_mfma_f32_16x16x32_bf16 v[136:139], v[248:251], v[148:151], v[136:139]\n"
"s_waitcnt lgkmcnt(3)\n"
"ds_read_b128 v[248:251], v4 offset:24576\n"
"v_mfma_f32_16x16x32_bf16 v[140:143], v[0:3], v[220:223], v[140:143]\n"
"ds_read_b32 v225, v225 offset:1280\n"
"v_mfma_f32_16x16x32_bf16 v[144:147], v[190:193], v[132:135], v[144:147]\n"
"s_add_u32 s71, s50, 4294967280\n"
"v_mfma_f32_16x16x32_bf16 v[144:147], v[192:195], v[24:27], v[144:147]\n"
"v_exp_f32 v196, v196\n"
"v_mfma_f32_16x16x32_bf16 v[156:159], v[120:123], v[220:223], v[156:159]\n"
"ds_write_addtid_b32 v218 offset:33792\n"
"v_exp_f32 v197, v197\n"
"v_exp_f32 v198, v198\n"
"v_mfma_f32_16x16x32_bf16 v[136:139], v[252:255], v[220:223], v[136:139]\n"
"s_waitcnt lgkmcnt(4)\n"
"ds_read_b128 v[218:221], v5 offset:24576\n"
"v_cvt_pk_bf16_f32 v196, v196, v197\n"
"v_mfma_f32_16x16x32_bf16 v[140:143], v[124:127], v[244:247], v[140:143]\n"
"ds_write_addtid_b32 v196 offset:57856\n"
"v_exp_f32 v199, v199\n"
"v_mfma_f32_16x16x32_bf16 v[156:159], v[128:131], v[244:247], v[156:159]\n"
"s_waitcnt lgkmcnt(3)\n"
"v_cvt_pk_bf16_f32 v197, v198, v199\n"
"v_exp_f32 v144, v144\n"
"v_add_u32_sdwa v224, v225, v7 dst_sel:DWORD dst_unused:UNUSED_PAD src0_sel:WORD_0 src1_sel:DWORD\n"
"v_mfma_f32_16x16x32_bf16 v[136:139], v[214:217], v[244:247], v[136:139]\n"
"ds_write_addtid_b32 v197 offset:58112\n"
"v_mul_f32 v140, s60, v140\n"
"v_mfma_f32_16x16x32_bf16 v[214:217], v[8:11], v[248:251], 0\n"
"ds_read_b128 v[196:199], v4 offset:26624\n"
"v_exp_f32 v145, v145\n"
"v_mul_f32 v142, s60, v142\n"
"v_mul_f32 v157, s60, v157\n"
"v_mfma_f32_16x16x32_bf16 v[252:255], v[12:15], v[248:251], 0\n"
"v_mul_f32 v158, s60, v158\n"
"v_max_u32 v225, s71, v224\n"
"v_mfma_f32_16x16x32_bf16 v[132:135], v[16:19], v[248:251], 0\n"
"s_waitcnt lgkmcnt(3)\n"
"v_cvt_pk_bf16_f32 v144, v144, v145\n"
"v_mul_f32 v139, s60, v139\n"
"v_mfma_f32_16x16x32_bf16 v[214:217], v[20:23], v[218:221], v[214:217]\n"
"ds_write_addtid_b32 v144 offset:58368\n"
"ds_read_b128 v[248:251], v225 offset:16\n"
"v_mul_f32 v141, s60, v141\n"
"v_mul_f32 v138, s60, v138\n"
"v_cvt_pk_bf16_f32 v140, v140, v141\n"
"v_mfma_f32_16x16x32_bf16 v[252:255], v[28:31], v[218:221], v[252:255]\n"
"v_mul_f32 v137, s60, v137\n"
"v_mul_f32 v143, s60, v143\n"
"v_cvt_pk_bf16_f32 v141, v142, v143\n"
"v_mfma_f32_16x16x32_bf16 v[132:135], v[32:35], v[218:221], v[132:135]\n"
"s_waitcnt lgkmcnt(2)\n"
"ds_read_b128 v[244:247], v4 offset:28672\n"
"v_mul_f32 v156, s60, v156\n"
"v_mfma_f32_16x16x32_bf16 v[214:217], v[36:39], v[196:199], v[214:217]\n"
"ds_read_b128 v[218:221], v5 offset:26624\n"
"v_mul_f32 v159, s60, v159\n"
"v_cvt_pk_bf16_f32 v27, v138, v139\n"
"v_cvt_pk_bf16_f32 v142, v156, v157\n"
"v_cvt_pk_bf16_f32 v143, v158, v159\n"
"v_mfma_f32_16x16x32_bf16 v[252:255], v[40:43], v[196:199], v[252:255]\n"
"s_waitcnt lgkmcnt(2)\n"
"v_mul_f32 v136, s60, v136\n"
"v_cvt_pk_bf16_f32 v26, v136, v137\n"
"v_mfma_f32_16x16x32_bf16 v[248:251], v[202:205], v[140:143], v[248:251]\n"
"s_barrier\n"
"v_mfma_f32_16x16x32_bf16 v[248:251], v[204:207], v[24:27], v[248:251]\n"
"ds_read_b128 v[136:139], v225 offset:32\n"
"v_mfma_f32_16x16x32_bf16 v[132:135], v[44:47], v[196:199], v[132:135]\n"
"s_waitcnt lgkmcnt(1)\n"
"ds_read_b128 v[196:199], v5 offset:28672\n"
"v_mfma_f32_16x16x32_bf16 v[214:217], v[48:51], v[218:221], v[214:217]\n"
"v_mfma_f32_16x16x32_bf16 v[252:255], v[52:55], v[218:221], v[252:255]\n"
"v_mfma_f32_16x16x32_bf16 v[132:135], v[56:59], v[218:221], v[132:135]\n"
"ds_read_b128 v[220:223], v4 offset:30720\n"
"v_mfma_f32_16x16x32_bf16 v[214:217], v[60:63], v[244:247], v[214:217]\n"
"v_mfma_f32_16x16x32_bf16 v[252:255], v[64:67], v[244:247], v[252:255]\n"
"v_exp_f32 v248, v248\n"
"v_mfma_f32_16x16x32_bf16 v[132:135], v[68:71], v[244:247], v[132:135]\n"
"s_waitcnt lgkmcnt(1)\n"
"v_mfma_f32_16x16x32_bf16 v[214:217], v[72:75], v[196:199], v[214:217]\n"
"v_mfma_f32_16x16x32_bf16 v[136:139], v[208:211], v[140:143], v[136:139]\n"
"v_exp_f32 v249, v249\n"
"s_add_u32 s72, s50, 1024\n"
"v_cvt_pk_bf16_f32 v248, v248, v249\n"
"v_mfma_f32_16x16x32_bf16 v[136:139], v[210:213], v[24:27], v[136:139]\n"
"ds_read_b128 v[144:147], v5 offset:30720\n"
"ds_write_addtid_b32 v248 offset:55296\n"
"v_min_u32 v201, s72, v224\n"
"v_mfma_f32_16x16x32_bf16 v[252:255], v[76:79], v[196:199], v[252:255]\n"
"v_exp_f32 v250, v250\n"
"v_cvt_pk_bf16_f32 v214, v214, v215\n"
"v_cvt_pk_bf16_f32 v215, v216, v217\n"
"v_mfma_f32_16x16x32_bf16 v[132:135], v[80:83], v[196:199], v[132:135]\n"
"s_waitcnt lgkmcnt(2)\n"
"v_mfma_f32_16x16x32_bf16 v[244:247], v[8:11], v[220:223], 0\n"
"ds_read_b128 v[196:199], v201 offset:1376\n"
"v_mfma_f32_16x16x32_bf16 v[148:151], v[12:15], v[220:223], 0\n"
"ds_read_b128 v[152:155], v4 offset:32768\n"
"v_cvt_pk_bf16_f32 v217, v254, v255\n"
"v_exp_f32 v251, v251\n"
"s_nop 0\n"
"v_cvt_pk_bf16_f32 v249, v250, v251\n"
"v_cvt_pk_bf16_f32 v216, v252, v253\n"
"ds_write_addtid_b32 v249 offset:55552\n"
"v_cvt_pk_bf16_f32 v219, v134, v135\n"
"v_cvt_pk_bf16_f32 v218, v132, v133\n"
"v_mfma_f32_16x16x32_bf16 v[250:253], v[16:19], v[220:223], 0\n"
"s_waitcnt lgkmcnt(4)\n"
"v_mfma_f32_16x16x32_bf16 v[244:247], v[20:23], v[144:147], v[244:247]\n"
"v_exp_f32 v136, v136\n"
"ds_read_b128 v[220:223], v5 offset:32768\n"
"v_exp_f32 v137, v137\n"
"v_mfma_f32_16x16x32_bf16 v[148:151], v[28:31], v[144:147], v[148:151]\n"
"v_cvt_pk_bf16_f32 v136, v136, v137\n"
"ds_write_addtid_b32 v136 offset:55808\n"
"v_mfma_f32_16x16x32_bf16 v[250:253], v[32:35], v[144:147], v[250:253]\n"
"s_waitcnt lgkmcnt(4)\n"
"v_mfma_f32_16x16x32_bf16 v[196:199], v[238:241], v[140:143], v[196:199]\n"
"v_exp_f32 v138, v138\n"
"v_mfma_f32_16x16x32_bf16 v[196:199], v[240:243], v[24:27], v[196:199]\n"
"s_waitcnt lgkmcnt(3)\n"
"ds_read_b128 v[132:135], v4 offset:34816\n"
"v_mfma_f32_16x16x32_bf16 v[244:247], v[36:39], v[152:155], v[244:247]\n"
"v_exp_f32 v139, v139\n"
"ds_read_b128 v[144:147], v201 offset:1360\n"
"v_mfma_f32_16x16x32_bf16 v[148:151], v[40:43], v[152:155], v[148:151]\n"
"v_cvt_pk_bf16_f32 v137, v138, v139\n"
"v_mfma_f32_16x16x32_bf16 v[250:253], v[44:47], v[152:155], v[250:253]\n"
"v_exp_f32 v196, v196\n"
"ds_write_addtid_b32 v137 offset:56064\n"
"s_waitcnt lgkmcnt(4)\n"
"v_mfma_f32_16x16x32_bf16 v[244:247], v[48:51], v[220:223], v[244:247]\n"
"v_mfma_f32_16x16x32_bf16 v[148:151], v[52:55], v[220:223], v[148:151]\n"
"v_exp_f32 v197, v197\n"
"v_mfma_f32_16x16x32_bf16 v[250:253], v[56:59], v[220:223], v[250:253]\n"
"s_waitcnt lgkmcnt(2)\n"
"v_mfma_f32_16x16x32_bf16 v[244:247], v[60:63], v[132:135], v[244:247]\n"
"s_waitcnt lgkmcnt(1)\n"
"v_mfma_f32_16x16x32_bf16 v[144:147], v[232:235], v[140:143], v[144:147]\n"
"ds_read_b128 v[220:223], v4 offset:43008\n"
"ds_read_b128 v[136:139], v5 offset:34816\n"
"v_cvt_pk_bf16_f32 v196, v196, v197\n"
"v_mfma_f32_16x16x32_bf16 v[144:147], v[234:237], v[24:27], v[144:147]\n"
"v_mfma_f32_16x16x32_bf16 v[148:151], v[64:67], v[132:135], v[148:151]\n"
"ds_write_addtid_b32 v196 offset:56320\n"
"v_mfma_f32_16x16x32_bf16 v[250:253], v[68:71], v[132:135], v[250:253]\n"
"s_waitcnt lgkmcnt(1)\n"
"ds_read_b128 v[132:135], v225 offset:48\n"
"v_mfma_f32_16x16x32_bf16 v[244:247], v[72:75], v[136:139], v[244:247]\n"
"v_exp_f32 v198, v198\n"
"v_mfma_f32_16x16x32_bf16 v[148:151], v[76:79], v[136:139], v[148:151]\n"
"ds_read_b128 v[152:155], v5 offset:43008\n"
"v_mfma_f32_16x16x32_bf16 v[250:253], v[80:83], v[136:139], v[250:253]\n"
"v_mfma_f32_16x16x32_bf16 v[136:139], v[84:87], v[220:223], 0\n"
"v_exp_f32 v199, v199\n"
"s_nop 1\n"
"v_cvt_pk_bf16_f32 v244, v244, v245\n"
"v_cvt_pk_bf16_f32 v245, v246, v247\n"
"v_cvt_pk_bf16_f32 v246, v148, v149\n"
"v_cvt_pk_bf16_f32 v197, v198, v199\n"
"ds_write_addtid_b32 v197 offset:56576\n"
"v_cvt_pk_bf16_f32 v247, v150, v151\n"
"v_mfma_f32_16x16x32_bf16 v[148:151], v[88:91], v[220:223], 0\n"
"ds_read_b128 v[156:159], v4 offset:45056\n"
"v_cvt_pk_bf16_f32 v249, v252, v253\n"
"v_cvt_pk_bf16_f32 v248, v250, v251\n"
"ds_read_b128 v[196:199], v6 offset:5120\n"
"s_waitcnt lgkmcnt(2)\n"
"s_barrier\n"
"v_mfma_f32_16x16x32_bf16 v[250:253], v[92:95], v[220:223], 0\n"
"v_exp_f32 v144, v144\n"
"v_mfma_f32_16x16x32_bf16 v[132:135], v[214:217], v[140:143], v[132:135]\n"
"ds_read_b128 v[220:223], v6 offset:6144\n"
"v_mfma_f32_16x16x32_bf16 v[132:135], v[216:219], v[24:27], v[132:135]\n"
"v_exp_f32 v145, v145\n"
"ds_read_b128 v[164:167], v201 offset:1392\n"
"v_mfma_f32_16x16x32_bf16 v[136:139], v[96:99], v[152:155], v[136:139]\n"
"v_exp_f32 v146, v146\n"
"v_mfma_f32_16x16x32_bf16 v[148:151], v[100:103], v[152:155], v[148:151]\n"
"ds_read_b128 v[160:163], v5 offset:45056\n"
"v_cvt_pk_bf16_f32 v144, v144, v145\n"
"ds_write_addtid_b32 v144 offset:57856\n"
"s_waitcnt lgkmcnt(4)\n"
"v_mfma_f32_16x16x32_bf16 v[250:253], v[196:199], v[152:155], v[250:253]\n"
"v_exp_f32 v147, v147\n"
"ds_read_b128 v[196:199], v6 offset:7168\n"
"v_mfma_f32_16x16x32_bf16 v[136:139], v[104:107], v[156:159], v[136:139]\n"
"v_cvt_pk_bf16_f32 v145, v146, v147\n"
"v_mfma_f32_16x16x32_bf16 v[148:151], v[108:111], v[156:159], v[148:151]\n"
"ds_write_addtid_b32 v145 offset:58112\n"
"s_waitcnt lgkmcnt(5)\n"
"v_mfma_f32_16x16x32_bf16 v[250:253], v[220:223], v[156:159], v[250:253]\n"
"s_waitcnt lgkmcnt(3)\n"
"ds_read_b128 v[220:223], v4 offset:47104\n"
"v_exp_f32 v132, v132\n"
"ds_read_b128 v[152:155], v5 offset:47104\n"
"v_mfma_f32_16x16x32_bf16 v[136:139], v[112:115], v[160:163], v[136:139]\n"
"ds_read_b128 v[144:147], v6 offset:8192\n"
"v_mfma_f32_16x16x32_bf16 v[148:151], v[116:119], v[160:163], v[148:151]\n"
"v_mfma_f32_16x16x32_bf16 v[164:167], v[244:247], v[140:143], v[164:167]\n"
"v_exp_f32 v133, v133\n"
"v_mfma_f32_16x16x32_bf16 v[164:167], v[246:249], v[24:27], v[164:167]\n"
"s_waitcnt lgkmcnt(4)\n"
"ds_read_b128 v[156:159], v4 offset:49152\n"
"v_bfe_u32 v255, v6, 2, 6\n"
"v_mfma_f32_16x16x32_bf16 v[250:253], v[196:199], v[160:163], v[250:253]\n"
"ds_read_b128 v[140:143], v6 offset:9216\n"
"s_add_u32 s69, s50, 4294966976\n"
"s_waitcnt lgkmcnt(4)\n"
"v_mfma_f32_16x16x32_bf16 v[136:139], v[0:3], v[220:223], v[136:139]\n"
"v_exp_f32 v134, v134\n"
"v_cvt_pk_bf16_f32 v132, v132, v133\n"
"v_exp_f32 v135, v135\n"
"ds_write_addtid_b32 v132 offset:57344\n"
"v_mfma_f32_16x16x32_bf16 v[148:151], v[120:123], v[220:223], v[148:151]\n"
"s_waitcnt lgkmcnt(3)\n"
"ds_read_b32 v255, v255 offset:1344\n"
"v_mfma_f32_16x16x32_bf16 v[250:253], v[144:147], v[220:223], v[250:253]\n"
"ds_read_b128 v[220:223], v5 offset:49152\n"
"v_mfma_f32_16x16x32_bf16 v[136:139], v[124:127], v[152:155], v[136:139]\n"
"v_cvt_pk_bf16_f32 v133, v134, v135\n"
"ds_write_addtid_b32 v133 offset:57600\n"
"ds_read_b128 v[132:135], v4 offset:51200\n"
"v_mfma_f32_16x16x32_bf16 v[148:151], v[128:131], v[152:155], v[148:151]\n"
"s_waitcnt lgkmcnt(5)\n"
"v_exp_f32 v164, v164\n"
"v_mfma_f32_16x16x32_bf16 v[250:253], v[140:143], v[152:155], v[250:253]\n"
"s_waitcnt lgkmcnt(3)\n"
"v_add_u32_sdwa v225, v255, v7 dst_sel:DWORD dst_unused:UNUSED_PAD src0_sel:WORD_0 src1_sel:DWORD\n"
"s_nop 0\n"
"v_min_u32 v177, s69, v225\n"
"ds_read_b128 v[152:155], v177 offset:2704\n"
"v_mfma_f32_16x16x32_bf16 v[196:199], v[8:11], v[156:159], 0\n"
"v_exp_f32 v165, v165\n"
"v_mfma_f32_16x16x32_bf16 v[140:143], v[12:15], v[156:159], 0\n"
"v_mul_f32 v150, s60, v150\n"
"v_cvt_pk_bf16_f32 v164, v164, v165\n"
"v_mul_f32 v136, s60, v136\n"
"v_mfma_f32_16x16x32_bf16 v[144:147], v[16:19], v[156:159], 0\n"
"s_waitcnt lgkmcnt(3)\n"
"v_mul_f32 v137, s60, v137\n"
"v_exp_f32 v166, v166\n"
"v_mfma_f32_16x16x32_bf16 v[196:199], v[20:23], v[220:223], v[196:199]\n"
"v_mul_f32 v138, s60, v138\n"
"v_exp_f32 v167, v167\n"
"v_mul_f32 v139, s60, v139\n"
"v_cvt_pk_bf16_f32 v165, v166, v167\n"
"v_mfma_f32_16x16x32_bf16 v[140:143], v[28:31], v[220:223], v[140:143]\n"
"ds_write_addtid_b32 v164 offset:56832\n"
"ds_write_addtid_b32 v165 offset:57088\n"
"v_cvt_pk_bf16_f32 v136, v136, v137\n"
"v_mul_f32 v149, s60, v149\n"
"v_mfma_f32_16x16x32_bf16 v[144:147], v[32:35], v[220:223], v[144:147]\n"
"s_waitcnt lgkmcnt(3)\n"
"v_mul_f32 v253, s60, v253\n"
"v_mul_f32 v250, s60, v250\n"
"v_cvt_pk_bf16_f32 v137, v138, v139\n"
"v_mfma_f32_16x16x32_bf16 v[196:199], v[36:39], v[132:135], v[196:199]\n"
"v_mul_f32 v151, s60, v151\n"
"v_cvt_pk_bf16_f32 v139, v150, v151\n"
"v_mul_f32 v148, s60, v148\n"
"v_mfma_f32_16x16x32_bf16 v[140:143], v[40:43], v[132:135], v[140:143]\n"
"ds_read_b128 v[220:223], v5 offset:51200\n"
"v_mul_f32 v252, s60, v252\n"
"v_cvt_pk_bf16_f32 v138, v148, v149\n"
"v_cvt_pk_bf16_f32 v27, v252, v253\n"
"s_waitcnt lgkmcnt(3)\n"
"v_mfma_f32_16x16x32_bf16 v[152:155], v[184:187], v[136:139], v[152:155]\n"
"s_waitcnt lgkmcnt(1)\n"
"s_barrier\n"
"ds_read_b128 v[148:151], v4 offset:53248\n"
"v_mul_f32 v251, s60, v251\n"
"v_cvt_pk_bf16_f32 v26, v250, v251\n"
"s_nop 1\n"
"v_mfma_f32_16x16x32_bf16 v[152:155], v[186:189], v[24:27], v[152:155]\n"
"v_mfma_f32_16x16x32_bf16 v[144:147], v[44:47], v[132:135], v[144:147]\n"
"s_add_u32 s73, s50, 4294967280\n"
"ds_read_b128 v[250:253], v177 offset:2720\n"
"v_max_u32 v176, s73, v225\n"
"s_waitcnt lgkmcnt(2)\n"
"v_mfma_f32_16x16x32_bf16 v[196:199], v[48:51], v[220:223], v[196:199]\n"
"v_mfma_f32_16x16x32_bf16 v[140:143], v[52:55], v[220:223], v[140:143]\n"
"v_mfma_f32_16x16x32_bf16 v[144:147], v[56:59], v[220:223], v[144:147]\n"
"s_waitcnt lgkmcnt(1)\n"
"ds_read_b128 v[220:223], v5 offset:53248\n"
"v_exp_f32 v152, v152\n"
"v_mfma_f32_16x16x32_bf16 v[196:199], v[60:63], v[148:151], v[196:199]\n"
"ds_read_b128 v[132:135], v4 offset:55296\n"
"ds_read_b128 v[156:159], v176 offset:32\n"
"v_mfma_f32_16x16x32_bf16 v[140:143], v[64:67], v[148:151], v[140:143]\n"
"s_waitcnt lgkmcnt(3)\n"
"v_exp_f32 v153, v153\n"
"v_mfma_f32_16x16x32_bf16 v[250:253], v[190:193], v[136:139], v[250:253]\n"
"v_exp_f32 v154, v154\n"
"v_mfma_f32_16x16x32_bf16 v[250:253], v[192:195], v[24:27], v[250:253]\n"
"v_mfma_f32_16x16x32_bf16 v[144:147], v[68:71], v[148:151], v[144:147]\n"
"s_waitcnt lgkmcnt(2)\n"
"ds_read_b128 v[148:151], v5 offset:55296\n"
"v_cvt_pk_bf16_f32 v152, v152, v153\n"
"v_mfma_f32_16x16x32_bf16 v[196:199], v[72:75], v[220:223], v[196:199]\n"
"v_exp_f32 v155, v155\n"
"v_mfma_f32_16x16x32_bf16 v[140:143], v[76:79], v[220:223], v[140:143]\n"
"v_cvt_pk_bf16_f32 v153, v154, v155\n"
"v_mfma_f32_16x16x32_bf16 v[144:147], v[80:83], v[220:223], v[144:147]\n"
"ds_write_addtid_b32 v152 offset:53248\n"
"ds_write_addtid_b32 v153 offset:53504\n"
"v_exp_f32 v250, v250\n"
"s_waitcnt lgkmcnt(4)\n"
"v_mfma_f32_16x16x32_bf16 v[220:223], v[8:11], v[132:135], 0\n"
"s_waitcnt lgkmcnt(3)\n"
"v_cvt_pk_bf16_f32 v196, v196, v197\n"
"v_cvt_pk_bf16_f32 v197, v198, v199\n"
"v_cvt_pk_bf16_f32 v199, v142, v143\n"
"v_mfma_f32_16x16x32_bf16 v[156:159], v[208:211], v[136:139], v[156:159]\n"
"v_cvt_pk_bf16_f32 v198, v140, v141\n"
"v_mfma_f32_16x16x32_bf16 v[156:159], v[210:213], v[24:27], v[156:159]\n"
"v_cvt_pk_bf16_f32 v201, v146, v147\n"
"v_cvt_pk_bf16_f32 v200, v144, v145\n"
"v_mfma_f32_16x16x32_bf16 v[144:147], v[12:15], v[132:135], 0\n"
"ds_read_b128 v[140:143], v176 offset:48\n"
"v_mfma_f32_16x16x32_bf16 v[152:155], v[16:19], v[132:135], 0\n"
"s_waitcnt lgkmcnt(3)\n"
"v_exp_f32 v251, v251\n"
"v_mfma_f32_16x16x32_bf16 v[220:223], v[20:23], v[148:151], v[220:223]\n"
"ds_read_b128 v[132:135], v4 offset:57344\n"
"v_exp_f32 v252, v252\n"
"v_mfma_f32_16x16x32_bf16 v[144:147], v[28:31], v[148:151], v[144:147]\n"
"s_add_u32 s68, s50, 4294965936\n"
"v_cvt_pk_bf16_f32 v250, v250, v251\n"
"v_mfma_f32_16x16x32_bf16 v[152:155], v[32:35], v[148:151], v[152:155]\n"
"v_exp_f32 v253, v253\n"
"ds_write_addtid_b32 v250 offset:53760\n"
"v_cvt_pk_bf16_f32 v251, v252, v253\n"
"ds_write_addtid_b32 v251 offset:54016\n"
"s_waitcnt lgkmcnt(3)\n"
"v_mfma_f32_16x16x32_bf16 v[140:143], v[214:217], v[136:139], v[140:143]\n"
"v_exp_f32 v156, v156\n"
"v_mfma_f32_16x16x32_bf16 v[140:143], v[216:219], v[24:27], v[140:143]\n"
"s_waitcnt lgkmcnt(2)\n"
"ds_read_b128 v[252:255], v5 offset:57344\n"
"v_max_u32 v250, s68, v225\n"
"v_mfma_f32_16x16x32_bf16 v[220:223], v[36:39], v[132:135], v[220:223]\n"
"v_exp_f32 v157, v157\n"
"v_exp_f32 v158, v158\n"
"ds_read_b128 v[148:151], v250 offset:1392\n"
"v_mfma_f32_16x16x32_bf16 v[144:147], v[40:43], v[132:135], v[144:147]\n"
"ds_read_b128 v[160:163], v176 offset:16\n"
"v_mfma_f32_16x16x32_bf16 v[152:155], v[44:47], v[132:135], v[152:155]\n"
"s_waitcnt lgkmcnt(2)\n"
"v_mfma_f32_16x16x32_bf16 v[220:223], v[48:51], v[252:255], v[220:223]\n"
"v_exp_f32 v159, v159\n"
"v_exp_f32 v140, v140\n"
"v_cvt_pk_bf16_f32 v156, v156, v157\n"
"v_cvt_pk_bf16_f32 v157, v158, v159\n"
"v_mfma_f32_16x16x32_bf16 v[144:147], v[52:55], v[252:255], v[144:147]\n"
"s_waitcnt lgkmcnt(1)\n"
"ds_read_b128 v[132:135], v4 offset:59392\n"
"v_exp_f32 v141, v141\n"
"v_mfma_f32_16x16x32_bf16 v[148:151], v[244:247], v[136:139], v[148:151]\n"
"ds_write_addtid_b32 v156 offset:54272\n"
"ds_write_addtid_b32 v157 offset:54528\n"
"ds_read_b128 v[156:159], v5 offset:59392\n"
"v_mfma_f32_16x16x32_bf16 v[148:151], v[246:249], v[24:27], v[148:151]\n"
"v_cvt_pk_bf16_f32 v140, v140, v141\n"
"v_mfma_f32_16x16x32_bf16 v[152:155], v[56:59], v[252:255], v[152:155]\n"
"s_waitcnt lgkmcnt(3)\n"
"v_exp_f32 v142, v142\n"
"v_mfma_f32_16x16x32_bf16 v[220:223], v[60:63], v[132:135], v[220:223]\n"
"ds_read_b128 v[252:255], v4 offset:12288\n"
"v_mfma_f32_16x16x32_bf16 v[144:147], v[64:67], v[132:135], v[144:147]\n"
"v_exp_f32 v143, v143\n"
"v_mfma_f32_16x16x32_bf16 v[152:155], v[68:71], v[132:135], v[152:155]\n"
"s_waitcnt lgkmcnt(1)\n"
"v_cvt_pk_bf16_f32 v141, v142, v143\n"
"v_mfma_f32_16x16x32_bf16 v[220:223], v[72:75], v[156:159], v[220:223]\n"
"ds_write_addtid_b32 v140 offset:54784\n"
"ds_write_addtid_b32 v141 offset:55040\n"
"v_mfma_f32_16x16x32_bf16 v[160:163], v[202:205], v[136:139], v[160:163]\n"
"v_mfma_f32_16x16x32_bf16 v[160:163], v[204:207], v[24:27], v[160:163]\n"
"ds_read_b128 v[202:205], v250 offset:1360\n"
"v_mfma_f32_16x16x32_bf16 v[144:147], v[76:79], v[156:159], v[144:147]\n"
"v_mfma_f32_16x16x32_bf16 v[152:155], v[80:83], v[156:159], v[152:155]\n"
"v_exp_f32 v148, v148\n"
"v_exp_f32 v149, v149\n"
"v_exp_f32 v150, v150\n"
"v_exp_f32 v151, v151\n"
"v_cvt_pk_bf16_f32 v220, v220, v221\n"
"s_waitcnt lgkmcnt(3)\n"
"v_mfma_f32_16x16x32_bf16 v[140:143], v[84:87], v[252:255], 0\n"
"ds_read_b128 v[132:135], v5 offset:12288\n"
"v_cvt_pk_bf16_f32 v148, v148, v149\n"
"ds_write_addtid_b32 v148 offset:33792\n"
"v_cvt_pk_bf16_f32 v149, v150, v151\n"
"ds_write_addtid_b32 v149 offset:34048\n"
"v_cvt_pk_bf16_f32 v221, v222, v223\n"
"v_cvt_pk_bf16_f32 v223, v146, v147\n"
"v_cvt_pk_bf16_f32 v222, v144, v145\n"
"v_mfma_f32_16x16x32_bf16 v[144:147], v[88:91], v[252:255], 0\n"
"v_cvt_pk_bf16_f32 v224, v152, v153\n"
"v_cvt_pk_bf16_f32 v225, v154, v155\n"
"v_mfma_f32_16x16x32_bf16 v[152:155], v[92:95], v[252:255], 0\n"
"s_waitcnt lgkmcnt(3)\n"
"v_exp_f32 v160, v160\n"
"ds_read_b128 v[252:255], v4 offset:14336\n"
"v_mfma_f32_16x16x32_bf16 v[202:205], v[232:235], v[136:139], v[202:205]\n"
"ds_read_b128 v[148:151], v6 offset:5120\n"
"v_mfma_f32_16x16x32_bf16 v[202:205], v[234:237], v[24:27], v[202:205]\n"
"s_waitcnt lgkmcnt(2)\n"
"ds_read_b128 v[156:159], v250 offset:1376\n"
"s_barrier\n"
"v_mfma_f32_16x16x32_bf16 v[140:143], v[96:99], v[132:135], v[140:143]\n"
"v_exp_f32 v161, v161\n"
"v_exp_f32 v162, v162\n"
"v_mfma_f32_16x16x32_bf16 v[144:147], v[100:103], v[132:135], v[144:147]\n"
"s_waitcnt lgkmcnt(1)\n"
"ds_read_b128 v[164:167], v6 offset:6144\n"
"v_cvt_pk_bf16_f32 v160, v160, v161\n"
"v_mfma_f32_16x16x32_bf16 v[152:155], v[148:151], v[132:135], v[152:155]\n"
"v_exp_f32 v163, v163\n"
"v_mfma_f32_16x16x32_bf16 v[140:143], v[104:107], v[252:255], v[140:143]\n"
"ds_read_b128 v[132:135], v5 offset:14336\n"
"v_mfma_f32_16x16x32_bf16 v[144:147], v[108:111], v[252:255], v[144:147]\n"
"ds_write_addtid_b32 v160 offset:57856\n"
"v_cvt_pk_bf16_f32 v161, v162, v163\n"
"ds_write_addtid_b32 v161 offset:58112\n"
"s_waitcnt lgkmcnt(4)\n"
"v_mfma_f32_16x16x32_bf16 v[156:159], v[238:241], v[136:139], v[156:159]\n"
"ds_read_b128 v[160:163], v4 offset:16384\n"
"v_mfma_f32_16x16x32_bf16 v[156:159], v[240:243], v[24:27], v[156:159]\n"
"s_waitcnt lgkmcnt(4)\n"
"v_exp_f32 v202, v202\n"
"ds_read_b128 v[148:151], v6 offset:7168\n"
"v_mfma_f32_16x16x32_bf16 v[152:155], v[164:167], v[252:255], v[152:155]\n"
"s_waitcnt lgkmcnt(4)\n"
"ds_read_b128 v[164:167], v177 offset:2736\n"
"v_mfma_f32_16x16x32_bf16 v[140:143], v[112:115], v[132:135], v[140:143]\n"
"v_exp_f32 v203, v203\n"
"s_nop 0\n"
"v_cvt_pk_bf16_f32 v202, v202, v203\n"
"v_mfma_f32_16x16x32_bf16 v[144:147], v[116:119], v[132:135], v[144:147]\n"
"v_exp_f32 v204, v204\n"
"v_exp_f32 v205, v205\n"
"ds_write_addtid_b32 v202 offset:58368\n"
"v_cvt_pk_bf16_f32 v203, v204, v205\n"
"s_waitcnt lgkmcnt(2)\n"
"v_mfma_f32_16x16x32_bf16 v[152:155], v[148:151], v[132:135], v[152:155]\n"
"ds_read_b128 v[132:135], v5 offset:16384\n"
"v_mfma_f32_16x16x32_bf16 v[140:143], v[0:3], v[160:163], v[140:143]\n"
"s_waitcnt lgkmcnt(2)\n"
"ds_read_b128 v[250:253], v6 offset:8192\n"
"v_mfma_f32_16x16x32_bf16 v[164:167], v[196:199], v[136:139], v[164:167]\n"
"ds_write_addtid_b32 v203 offset:58624\n"
"ds_read_b128 v[202:205], v6 offset:9216\n"
"v_mfma_f32_16x16x32_bf16 v[164:167], v[198:201], v[24:27], v[164:167]\n"
"v_exp_f32 v156, v156\n"
"ds_read_b128 v[136:139], v4 offset:18432\n"
"v_mfma_f32_16x16x32_bf16 v[144:147], v[120:123], v[160:163], v[144:147]\n"
"s_waitcnt lgkmcnt(3)\n"
"v_exp_f32 v157, v157\n"
"v_bfe_u32 v177, v6, 2, 6\n"
"v_mfma_f32_16x16x32_bf16 v[152:155], v[250:253], v[160:163], v[152:155]\n"
"v_exp_f32 v158, v158\n"
"v_mfma_f32_16x16x32_bf16 v[140:143], v[124:127], v[132:135], v[140:143]\n"
"ds_read_b128 v[148:151], v5 offset:18432\n"
"v_cvt_pk_bf16_f32 v156, v156, v157\n"
"ds_read_b32 v177, v177 offset:1408\n"
"v_mfma_f32_16x16x32_bf16 v[144:147], v[128:131], v[132:135], v[144:147]\n"
"s_nop 3\n"
"v_mul_f32 v141, s60, v141\n"
"v_mul_f32 v140, s60, v140\n"
"s_waitcnt lgkmcnt(3)\n"
"v_mfma_f32_16x16x32_bf16 v[152:155], v[202:205], v[132:135], v[152:155]\n"
"s_waitcnt lgkmcnt(2)\n"
"v_exp_f32 v159, v159\n"
"v_mfma_f32_16x16x32_bf16 v[250:253], v[8:11], v[136:139], 0\n"
"ds_read_b128 v[202:205], v4 offset:20480\n"
"v_mfma_f32_16x16x32_bf16 v[132:135], v[12:15], v[136:139], 0\n"
"ds_write_addtid_b32 v156 offset:58880\n"
"v_cvt_pk_bf16_f32 v157, v158, v159\n"
"s_waitcnt lgkmcnt(2)\n"
"v_add_u32_sdwa v176, v177, v7 dst_sel:DWORD dst_unused:UNUSED_PAD src0_sel:WORD_0 src1_sel:DWORD\n"
"ds_write_addtid_b32 v157 offset:59136\n"
"v_mfma_f32_16x16x32_bf16 v[156:159], v[16:19], v[136:139], 0\n"
"s_add_u32 s70, s50, 4294967264\n"
"v_mul_f32 v152, s60, v152\n"
"v_mfma_f32_16x16x32_bf16 v[250:253], v[20:23], v[148:151], v[250:253]\n"
"v_exp_f32 v164, v164\n"
"v_exp_f32 v165, v165\n"
"s_nop 0\n"
"v_cvt_pk_bf16_f32 v164, v164, v165\n"
"v_mul_f32 v155, s60, v155\n"
"v_exp_f32 v166, v166\n"
"v_mfma_f32_16x16x32_bf16 v[132:135], v[28:31], v[148:151], v[132:135]\n"
"v_exp_f32 v167, v167\n"
"v_mfma_f32_16x16x32_bf16 v[156:159], v[32:35], v[148:151], v[156:159]\n"
"s_waitcnt lgkmcnt(2)\n"
"ds_read_b128 v[148:151], v5 offset:20480\n"
"v_mfma_f32_16x16x32_bf16 v[250:253], v[36:39], v[202:205], v[250:253]\n"
"ds_write_addtid_b32 v164 offset:57344\n"
"v_mul_f32 v145, s60, v145\n"
"v_mul_f32 v144, s60, v144\n"
"v_mul_f32 v142, s60, v142\n"
"v_cvt_pk_bf16_f32 v165, v166, v167\n"
"v_mul_f32 v147, s60, v147\n"
"v_mul_f32 v143, s60, v143\n"
"ds_write_addtid_b32 v165 offset:57600\n"
"v_max_u32 v207, s70, v176\n"
"v_cvt_pk_bf16_f32 v140, v140, v141\n"
"v_mul_f32 v153, s60, v153\n"
"v_cvt_pk_bf16_f32 v26, v152, v153\n"
"v_mul_f32 v146, s60, v146\n"
"v_cvt_pk_bf16_f32 v141, v142, v143\n"
"v_cvt_pk_bf16_f32 v142, v144, v145\n"
"ds_read_b128 v[136:139], v207 offset:48\n"
"v_mfma_f32_16x16x32_bf16 v[132:135], v[40:43], v[202:205], v[132:135]\n"
"s_waitcnt lgkmcnt(1)\n"
"v_mul_f32 v154, s60, v154\n"
"v_cvt_pk_bf16_f32 v143, v146, v147\n"
"ds_read_b128 v[144:147], v4 offset:22528\n"
"s_barrier\n"
"v_add_u32_sdwa v177, sext(v177), v176 dst_sel:DWORD dst_unused:UNUSED_PAD src0_sel:WORD_1 src1_sel:DWORD\n"
"v_cvt_pk_bf16_f32 v27, v154, v155\n"
"s_waitcnt lgkmcnt(1)\n"
"v_mfma_f32_16x16x32_bf16 v[136:139], v[214:217], v[140:143], v[136:139]\n"
"s_add_u32 s72, s50, 4294966960\n"
"v_max_u32 v206, s70, v177\n"
"v_mfma_f32_16x16x32_bf16 v[136:139], v[216:219], v[24:27], v[136:139]\n"
"ds_read_b128 v[214:217], v206 offset:64\n"
"v_min_u32 v206, s72, v176\n"
"v_mfma_f32_16x16x32_bf16 v[156:159], v[44:47], v[202:205], v[156:159]\n"
"ds_read_b128 v[202:205], v5 offset:22528\n"
"v_mfma_f32_16x16x32_bf16 v[250:253], v[48:51], v[148:151], v[250:253]\n"
"v_mfma_f32_16x16x32_bf16 v[132:135], v[52:55], v[148:151], v[132:135]\n"
"v_mfma_f32_16x16x32_bf16 v[156:159], v[56:59], v[148:151], v[156:159]\n"
"s_waitcnt lgkmcnt(2)\n"
"v_mfma_f32_16x16x32_bf16 v[250:253], v[60:63], v[144:147], v[250:253]\n"
"v_mfma_f32_16x16x32_bf16 v[132:135], v[64:67], v[144:147], v[132:135]\n"
"s_waitcnt lgkmcnt(1)\n"
"v_exp_f32 v136, v136\n"
"v_mfma_f32_16x16x32_bf16 v[214:217], v[220:223], v[140:143], v[214:217]\n"
"ds_read_b128 v[218:221], v4 offset:24576\n"
"v_mfma_f32_16x16x32_bf16 v[214:217], v[222:225], v[24:27], v[214:217]\n"
"v_exp_f32 v137, v137\n"
"v_exp_f32 v138, v138\n"
"v_mfma_f32_16x16x32_bf16 v[156:159], v[68:71], v[144:147], v[156:159]\n"
"s_waitcnt lgkmcnt(1)\n"
"v_mfma_f32_16x16x32_bf16 v[250:253], v[72:75], v[202:205], v[250:253]\n"
"ds_read_b128 v[222:225], v206 offset:2720\n"
"v_mfma_f32_16x16x32_bf16 v[132:135], v[76:79], v[202:205], v[132:135]\n"
"v_exp_f32 v139, v139\n"
"v_cvt_pk_bf16_f32 v136, v136, v137\n"
"v_cvt_pk_bf16_f32 v137, v138, v139\n"
"v_mfma_f32_16x16x32_bf16 v[156:159], v[80:83], v[202:205], v[156:159]\n"
"ds_write_addtid_b32 v136 offset:55296\n"
"ds_write_addtid_b32 v137 offset:55552\n"
"s_waitcnt lgkmcnt(3)\n"
"v_mfma_f32_16x16x32_bf16 v[202:205], v[8:11], v[218:221], 0\n"
"s_waitcnt lgkmcnt(2)\n"
"ds_read_b128 v[136:139], v5 offset:24576\n"
"v_cvt_pk_bf16_f32 v250, v250, v251\n"
"v_cvt_pk_bf16_f32 v251, v252, v253\n"
"v_mfma_f32_16x16x32_bf16 v[222:225], v[190:193], v[140:143], v[222:225]\n"
"v_cvt_pk_bf16_f32 v254, v156, v157\n"
"v_cvt_pk_bf16_f32 v252, v132, v133\n"
"v_mfma_f32_16x16x32_bf16 v[222:225], v[192:195], v[24:27], v[222:225]\n"
"v_cvt_pk_bf16_f32 v255, v158, v159\n"
"v_cvt_pk_bf16_f32 v253, v134, v135\n"
"v_exp_f32 v214, v214\n"
"v_exp_f32 v215, v215\n"
"v_mfma_f32_16x16x32_bf16 v[144:147], v[12:15], v[218:221], 0\n"
"ds_read_b128 v[132:135], v206 offset:2736\n"
"v_cvt_pk_bf16_f32 v214, v214, v215\n"
"v_mfma_f32_16x16x32_bf16 v[148:151], v[16:19], v[218:221], 0\n"
"s_waitcnt lgkmcnt(1)\n"
"v_exp_f32 v216, v216\n"
"v_exp_f32 v217, v217\n"
"v_mfma_f32_16x16x32_bf16 v[202:205], v[20:23], v[136:139], v[202:205]\n"
"ds_read_b128 v[218:221], v4 offset:26624\n"
"s_add_u32 s71, s50, 1008\n"
"v_mfma_f32_16x16x32_bf16 v[144:147], v[28:31], v[136:139], v[144:147]\n"
"ds_write_addtid_b32 v214 offset:55808\n"
"v_mfma_f32_16x16x32_bf16 v[148:151], v[32:35], v[136:139], v[148:151]\n"
"s_waitcnt lgkmcnt(2)\n"
"ds_read_b128 v[136:139], v207 offset:32\n"
"v_mfma_f32_16x16x32_bf16 v[132:135], v[196:199], v[140:143], v[132:135]\n"
"v_cvt_pk_bf16_f32 v215, v216, v217\n"
"ds_write_addtid_b32 v215 offset:56064\n"
"v_mfma_f32_16x16x32_bf16 v[132:135], v[198:201], v[24:27], v[132:135]\n"
"s_waitcnt lgkmcnt(3)\n"
"v_exp_f32 v224, v224\n"
"ds_read_b128 v[214:217], v5 offset:26624\n"
"v_mfma_f32_16x16x32_bf16 v[202:205], v[36:39], v[218:221], v[202:205]\n"
"v_exp_f32 v225, v225\n"
"v_mfma_f32_16x16x32_bf16 v[144:147], v[40:43], v[218:221], v[144:147]\n"
"v_cvt_pk_bf16_f32 v223, v224, v225\n"
"v_min_u32 v225, s71, v176\n"
"v_mfma_f32_16x16x32_bf16 v[148:151], v[44:47], v[218:221], v[148:151]\n"
"s_waitcnt lgkmcnt(0)\n"
"v_exp_f32 v132, v132\n"
"v_mfma_f32_16x16x32_bf16 v[202:205], v[48:51], v[214:217], v[202:205]\n"
"ds_read_b128 v[218:221], v4 offset:28672\n"
"v_mfma_f32_16x16x32_bf16 v[144:147], v[52:55], v[214:217], v[144:147]\n"
"v_exp_f32 v133, v133\n"
"v_mfma_f32_16x16x32_bf16 v[136:139], v[208:211], v[140:143], v[136:139]\n"
"ds_read_b128 v[206:209], v5 offset:28672\n"
"v_mfma_f32_16x16x32_bf16 v[136:139], v[210:213], v[24:27], v[136:139]\n"
"v_cvt_pk_bf16_f32 v132, v132, v133\n"
"v_mfma_f32_16x16x32_bf16 v[148:151], v[56:59], v[214:217], v[148:151]\n"
"s_waitcnt lgkmcnt(1)\n"
"ds_read_b128 v[210:213], v225 offset:1376\n"
"v_mfma_f32_16x16x32_bf16 v[202:205], v[60:63], v[218:221], v[202:205]\n"
"v_mfma_f32_16x16x32_bf16 v[144:147], v[64:67], v[218:221], v[144:147]\n"
"v_exp_f32 v134, v134\n"
"v_mfma_f32_16x16x32_bf16 v[148:151], v[68:71], v[218:221], v[148:151]\n"
"s_waitcnt lgkmcnt(1)\n"
"v_mfma_f32_16x16x32_bf16 v[202:205], v[72:75], v[206:209], v[202:205]\n"
"ds_write_addtid_b32 v223 offset:56576\n"
"s_waitcnt lgkmcnt(1)\n"
"v_mfma_f32_16x16x32_bf16 v[210:213], v[238:241], v[140:143], v[210:213]\n"
"v_exp_f32 v135, v135\n"
"ds_write_addtid_b32 v132 offset:56832\n"
"v_cvt_pk_bf16_f32 v133, v134, v135\n"
"v_exp_f32 v138, v138\n"
"v_mfma_f32_16x16x32_bf16 v[210:213], v[240:243], v[24:27], v[210:213]\n"
"ds_write_addtid_b32 v133 offset:57088\n"
"ds_read_b128 v[218:221], v225 offset:1392\n"
"v_mfma_f32_16x16x32_bf16 v[144:147], v[76:79], v[206:209], v[144:147]\n"
"v_mfma_f32_16x16x32_bf16 v[148:151], v[80:83], v[206:209], v[148:151]\n"
"ds_read_b128 v[214:217], v4 offset:30720\n"
"v_cvt_pk_bf16_f32 v202, v202, v203\n"
"v_cvt_pk_bf16_f32 v203, v204, v205\n"
"s_nop 3\n"
"v_cvt_pk_bf16_f32 v205, v146, v147\n"
"s_waitcnt lgkmcnt(2)\n"
"s_barrier\n"
"v_exp_f32 v139, v139\n"
"s_waitcnt lgkmcnt(0)\n"
"v_mfma_f32_16x16x32_bf16 v[132:135], v[84:87], v[214:217], 0\n"
"ds_read_b128 v[222:225], v5 offset:30720\n"
"v_cvt_pk_bf16_f32 v207, v150, v151\n"
"v_cvt_pk_bf16_f32 v204, v144, v145\n"
"v_min_u32 v176, s71, v177\n"
"v_cvt_pk_bf16_f32 v137, v138, v139\n"
"ds_write_addtid_b32 v137 offset:58112\n"
"v_mfma_f32_16x16x32_bf16 v[136:139], v[88:91], v[214:217], 0\n"
"ds_read_b128 v[144:147], v6 offset:5120\n"
"v_cvt_pk_bf16_f32 v206, v148, v149\n"
"v_mfma_f32_16x16x32_bf16 v[148:151], v[92:95], v[214:217], 0\n"
"ds_read_b128 v[214:217], v4 offset:32768\n"
"ds_read_b128 v[152:155], v176 offset:1408\n"
"v_mfma_f32_16x16x32_bf16 v[218:221], v[244:247], v[140:143], v[218:221]\n"
"v_exp_f32 v212, v212\n"
"v_mfma_f32_16x16x32_bf16 v[218:221], v[246:249], v[24:27], v[218:221]\n"
"s_waitcnt lgkmcnt(4)\n"
"v_mfma_f32_16x16x32_bf16 v[132:135], v[96:99], v[222:225], v[132:135]\n"
"v_exp_f32 v213, v213\n"
"s_nop 0\n"
"v_cvt_pk_bf16_f32 v211, v212, v213\n"
"v_mfma_f32_16x16x32_bf16 v[136:139], v[100:103], v[222:225], v[136:139]\n"
"s_waitcnt lgkmcnt(2)\n"
"ds_read_b128 v[156:159], v6 offset:6144\n"
"v_mfma_f32_16x16x32_bf16 v[148:151], v[144:147], v[222:225], v[148:151]\n"
"ds_read_b128 v[222:225], v6 offset:7168\n"
"ds_write_addtid_b32 v211 offset:58624\n"
"ds_read_b128 v[208:211], v5 offset:32768\n"
"s_waitcnt lgkmcnt(5)\n"
"v_mfma_f32_16x16x32_bf16 v[132:135], v[104:107], v[214:217], v[132:135]\n"
"v_min_u32 v176, s72, v177\n"
"v_mfma_f32_16x16x32_bf16 v[136:139], v[108:111], v[214:217], v[136:139]\n"
"s_waitcnt lgkmcnt(4)\n"
"v_exp_f32 v218, v218\n"
"v_mfma_f32_16x16x32_bf16 v[152:155], v[250:253], v[140:143], v[152:155]\n"
"ds_read_b128 v[144:147], v4 offset:34816\n"
"v_mfma_f32_16x16x32_bf16 v[152:155], v[252:255], v[24:27], v[152:155]\n"
"s_waitcnt lgkmcnt(4)\n"
"v_exp_f32 v219, v219\n"
"ds_read_b128 v[160:163], v176 offset:2752\n"
"v_mfma_f32_16x16x32_bf16 v[148:151], v[156:159], v[214:217], v[148:151]\n"
"s_waitcnt lgkmcnt(2)\n"
"ds_read_b128 v[212:215], v6 offset:8192\n"
"v_mfma_f32_16x16x32_bf16 v[132:135], v[112:115], v[208:211], v[132:135]\n"
"v_mfma_f32_16x16x32_bf16 v[136:139], v[116:119], v[208:211], v[136:139]\n"
"v_exp_f32 v220, v220\n"
"v_exp_f32 v221, v221\n"
"v_mfma_f32_16x16x32_bf16 v[148:151], v[222:225], v[208:211], v[148:151]\n"
"s_waitcnt lgkmcnt(2)\n"
"ds_read_b128 v[208:211], v5 offset:34816\n"
"v_cvt_pk_bf16_f32 v218, v218, v219\n"
"v_mfma_f32_16x16x32_bf16 v[132:135], v[0:3], v[144:147], v[132:135]\n"
"s_waitcnt lgkmcnt(2)\n"
"v_exp_f32 v152, v152\n"
"v_exp_f32 v153, v153\n"
"v_exp_f32 v154, v154\n"
"v_mfma_f32_16x16x32_bf16 v[160:163], v[202:205], v[140:143], v[160:163]\n"
"ds_write_addtid_b32 v218 offset:58880\n"
"v_cvt_pk_bf16_f32 v219, v220, v221\n"
"ds_write_addtid_b32 v219 offset:59136\n"
"v_cvt_pk_bf16_f32 v152, v152, v153\n"
"v_mfma_f32_16x16x32_bf16 v[160:163], v[204:207], v[24:27], v[160:163]\n"
"ds_read_b128 v[216:219], v6 offset:9216\n"
"v_mfma_f32_16x16x32_bf16 v[136:139], v[120:123], v[144:147], v[136:139]\n"
"s_waitcnt lgkmcnt(4)\n"
"v_exp_f32 v155, v155\n"
"v_mfma_f32_16x16x32_bf16 v[148:151], v[212:215], v[144:147], v[148:151]\n"
"s_waitcnt lgkmcnt(3)\n"
"v_bfe_u32 v225, v6, 2, 6\n"
"v_mfma_f32_16x16x32_bf16 v[132:135], v[124:127], v[208:211], v[132:135]\n"
"ds_read_b128 v[220:223], v4 offset:36864\n"
"v_mfma_f32_16x16x32_bf16 v[136:139], v[128:131], v[208:211], v[136:139]\n"
"s_waitcnt lgkmcnt(1)\n"
"v_mfma_f32_16x16x32_bf16 v[148:151], v[216:219], v[208:211], v[148:151]\n"
"ds_read_b128 v[212:215], v5 offset:36864\n"
"v_cvt_pk_bf16_f32 v153, v154, v155\n"
"v_exp_f32 v160, v160\n"
"v_exp_f32 v161, v161\n"
"v_exp_f32 v162, v162\n"
"s_waitcnt lgkmcnt(1)\n"
"v_mfma_f32_16x16x32_bf16 v[208:211], v[8:11], v[220:223], 0\n"
"ds_write_addtid_b32 v152 offset:34304\n"
"v_mul_f32 v132, s60, v132\n"
"v_mul_f32 v133, s60, v133\n"
"v_mul_f32 v136, s60, v136\n"
"v_mul_f32 v148, s60, v148\n"
"v_mfma_f32_16x16x32_bf16 v[140:143], v[12:15], v[220:223], 0\n"
"ds_read_b32 v225, v225 offset:1472\n"
"v_mul_f32 v135, s60, v135\n"
"v_mul_f32 v137, s60, v137\n"
"v_mfma_f32_16x16x32_bf16 v[144:147], v[16:19], v[220:223], 0\n"
"s_waitcnt lgkmcnt(2)\n"
"v_exp_f32 v163, v163\n"
"ds_read_b128 v[216:219], v4 offset:38912\n"
"v_mul_f32 v139, s60, v139\n"
"v_mfma_f32_16x16x32_bf16 v[208:211], v[20:23], v[212:215], v[208:211]\n"
"v_mul_f32 v134, s60, v134\n"
"v_cvt_pk_bf16_f32 v160, v160, v161\n"
"v_cvt_pk_bf16_f32 v161, v162, v163\n"
"ds_write_addtid_b32 v160 offset:57344\n"
"v_mul_f32 v149, s60, v149\n"
"v_mfma_f32_16x16x32_bf16 v[140:143], v[28:31], v[212:215], v[140:143]\n"
"v_cvt_pk_bf16_f32 v132, v132, v133\n"
"v_mul_f32 v138, s60, v138\n"
"ds_write_addtid_b32 v153 offset:34560\n"
"v_mul_f32 v150, s60, v150\n"
"v_mfma_f32_16x16x32_bf16 v[144:147], v[32:35], v[212:215], v[144:147]\n"
"s_waitcnt lgkmcnt(3)\n"
"v_cvt_pk_bf16_f32 v133, v134, v135\n"
"v_add_u32_sdwa v224, v225, v7 dst_sel:DWORD dst_unused:UNUSED_PAD src0_sel:WORD_0 src1_sel:DWORD\n"
"v_cvt_pk_bf16_f32 v134, v136, v137\n"
"s_add_u32 s68, s50, 4294966992\n"
"s_waitcnt lgkmcnt(2)\n"
"v_mfma_f32_16x16x32_bf16 v[208:211], v[36:39], v[216:219], v[208:211]\n"
"ds_read_b128 v[212:215], v5 offset:38912\n"
"ds_write_addtid_b32 v161 offset:57600\n"
"v_min_u32 v225, s68, v224\n"
"ds_read_b128 v[220:223], v225 offset:2688\n"
"v_cvt_pk_bf16_f32 v26, v148, v149\n"
"s_waitcnt lgkmcnt(1)\n"
"s_barrier\n"
"v_mfma_f32_16x16x32_bf16 v[140:143], v[40:43], v[216:219], v[140:143]\n"
"v_cvt_pk_bf16_f32 v135, v138, v139\n"
"v_mul_f32 v151, s60, v151\n"
"v_cvt_pk_bf16_f32 v27, v150, v151\n"
"ds_read_b128 v[136:139], v4 offset:40960\n"
"s_waitcnt lgkmcnt(1)\n"
"v_mfma_f32_16x16x32_bf16 v[220:223], v[178:181], v[132:135], v[220:223]\n"
"v_mfma_f32_16x16x32_bf16 v[220:223], v[180:183], v[24:27], v[220:223]\n"
"v_mfma_f32_16x16x32_bf16 v[144:147], v[44:47], v[216:219], v[144:147]\n"
"ds_read_b128 v[216:219], v5 offset:40960\n"
"v_mfma_f32_16x16x32_bf16 v[208:211], v[48:51], v[212:215], v[208:211]\n"
"ds_read_b128 v[148:151], v225 offset:2704\n"
"v_mfma_f32_16x16x32_bf16 v[140:143], v[52:55], v[212:215], v[140:143]\n"
"v_mfma_f32_16x16x32_bf16 v[144:147], v[56:59], v[212:215], v[144:147]\n"
"s_waitcnt lgkmcnt(2)\n"
"v_mfma_f32_16x16x32_bf16 v[208:211], v[60:63], v[136:139], v[208:211]\n"
"v_mfma_f32_16x16x32_bf16 v[140:143], v[64:67], v[136:139], v[140:143]\n"
"v_exp_f32 v220, v220\n"
"v_exp_f32 v221, v221\n"
"s_nop 0\n"
"v_cvt_pk_bf16_f32 v220, v220, v221\n"
"v_mfma_f32_16x16x32_bf16 v[144:147], v[68:71], v[136:139], v[144:147]\n"
"s_waitcnt lgkmcnt(1)\n"
"v_exp_f32 v222, v222\n"
"v_mfma_f32_16x16x32_bf16 v[208:211], v[72:75], v[216:219], v[208:211]\n"
"s_waitcnt lgkmcnt(0)\n"
"v_mfma_f32_16x16x32_bf16 v[148:151], v[184:187], v[132:135], v[148:151]\n"
"ds_read_b128 v[136:139], v4 offset:43008\n"
"ds_read_b128 v[152:155], v5 offset:43008\n"
"s_add_u32 s73, s50, 4294965952\n"
"v_mfma_f32_16x16x32_bf16 v[148:151], v[186:189], v[24:27], v[148:151]\n"
"v_mfma_f32_16x16x32_bf16 v[140:143], v[76:79], v[216:219], v[140:143]\n"
"ds_write_addtid_b32 v220 offset:53248\n"
"v_max_u32 v177, s73, v224\n"
"ds_read_b128 v[156:159], v177 offset:1344\n"
"v_mfma_f32_16x16x32_bf16 v[144:147], v[80:83], v[216:219], v[144:147]\n"
"s_waitcnt lgkmcnt(3)\n"
"v_exp_f32 v223, v223\n"
"v_mfma_f32_16x16x32_bf16 v[214:217], v[8:11], v[136:139], 0\n"
"v_cvt_pk_bf16_f32 v208, v208, v209\n"
"v_cvt_pk_bf16_f32 v209, v210, v211\n"
"v_cvt_pk_bf16_f32 v221, v222, v223\n"
"ds_write_addtid_b32 v221 offset:53504\n"
"v_mfma_f32_16x16x32_bf16 v[218:221], v[12:15], v[136:139], 0\n"
"v_exp_f32 v148, v148\n"
"v_cvt_pk_bf16_f32 v211, v142, v143\n"
"v_cvt_pk_bf16_f32 v210, v140, v141\n"
"v_mfma_f32_16x16x32_bf16 v[140:143], v[16:19], v[136:139], 0\n"
"s_waitcnt lgkmcnt(3)\n"
"ds_read_b128 v[136:139], v4 offset:45056\n"
"v_cvt_pk_bf16_f32 v213, v146, v147\n"
"v_cvt_pk_bf16_f32 v212, v144, v145\n"
"v_mfma_f32_16x16x32_bf16 v[214:217], v[20:23], v[152:155], v[214:217]\n"
"v_mfma_f32_16x16x32_bf16 v[218:221], v[28:31], v[152:155], v[218:221]\n"
"v_exp_f32 v149, v149\n"
"v_mfma_f32_16x16x32_bf16 v[140:143], v[32:35], v[152:155], v[140:143]\n"
"s_waitcnt lgkmcnt(2)\n"
"v_mfma_f32_16x16x32_bf16 v[156:159], v[226:229], v[132:135], v[156:159]\n"
"v_exp_f32 v150, v150\n"
"ds_read_b128 v[152:155], v4 offset:47104\n"
"v_exp_f32 v151, v151\n"
"v_cvt_pk_bf16_f32 v148, v148, v149\n"
"v_mfma_f32_16x16x32_bf16 v[156:159], v[228:231], v[24:27], v[156:159]\n"
"ds_read_b128 v[160:163], v177 offset:1360\n"
"ds_write_addtid_b32 v148 offset:53760\n"
"ds_read_b128 v[144:147], v5 offset:45056\n"
"s_waitcnt lgkmcnt(4)\n"
"v_mfma_f32_16x16x32_bf16 v[214:217], v[36:39], v[136:139], v[214:217]\n"
"s_nop 2\n"
"v_exp_f32 v156, v156\n"
"v_mfma_f32_16x16x32_bf16 v[218:221], v[40:43], v[136:139], v[218:221]\n"
"v_mfma_f32_16x16x32_bf16 v[140:143], v[44:47], v[136:139], v[140:143]\n"
"s_waitcnt lgkmcnt(0)\n"
"ds_read_b128 v[136:139], v5 offset:47104\n"
"v_mfma_f32_16x16x32_bf16 v[214:217], v[48:51], v[144:147], v[214:217]\n"
"v_cvt_pk_bf16_f32 v149, v150, v151\n"
"v_mfma_f32_16x16x32_bf16 v[218:221], v[52:55], v[144:147], v[218:221]\n"
"ds_write_addtid_b32 v149 offset:54016\n"
"v_mfma_f32_16x16x32_bf16 v[140:143], v[56:59], v[144:147], v[140:143]\n"
"v_exp_f32 v157, v157\n"
"v_mfma_f32_16x16x32_bf16 v[214:217], v[60:63], v[152:155], v[214:217]\n"
"v_cvt_pk_bf16_f32 v156, v156, v157\n"
"v_mfma_f32_16x16x32_bf16 v[160:163], v[232:235], v[132:135], v[160:163]\n"
"ds_read_b128 v[148:151], v177 offset:1376\n"
"v_mfma_f32_16x16x32_bf16 v[160:163], v[234:237], v[24:27], v[160:163]\n"
"v_mfma_f32_16x16x32_bf16 v[218:221], v[64:67], v[152:155], v[218:221]\n"
"v_mfma_f32_16x16x32_bf16 v[140:143], v[68:71], v[152:155], v[140:143]\n"
"ds_write_addtid_b32 v156 offset:54272\n"
"s_waitcnt lgkmcnt(3)\n"
"v_mfma_f32_16x16x32_bf16 v[214:217], v[72:75], v[136:139], v[214:217]\n"
"v_exp_f32 v158, v158\n"
"v_mfma_f32_16x16x32_bf16 v[218:221], v[76:79], v[136:139], v[218:221]\n"
"v_exp_f32 v159, v159\n"
"ds_read_b128 v[144:147], v4 offset:55296\n"
"v_cvt_pk_bf16_f32 v157, v158, v159\n"
"ds_write_addtid_b32 v157 offset:54528\n"
"s_nop 1\n"
"v_cvt_pk_bf16_f32 v214, v214, v215\n"
"v_cvt_pk_bf16_f32 v215, v216, v217\n"
"v_mfma_f32_16x16x32_bf16 v[140:143], v[80:83], v[136:139], v[140:143]\n"
"s_waitcnt lgkmcnt(1)\n"
"v_mfma_f32_16x16x32_bf16 v[136:139], v[84:87], v[144:147], 0\n"
"v_cvt_pk_bf16_f32 v216, v218, v219\n"
"s_nop 4\n"
"v_cvt_pk_bf16_f32 v219, v142, v143\n"
"v_cvt_pk_bf16_f32 v217, v220, v221\n"
"v_mfma_f32_16x16x32_bf16 v[156:159], v[88:91], v[144:147], 0\n"
"ds_read_b128 v[152:155], v5 offset:55296\n"
"v_cvt_pk_bf16_f32 v218, v140, v141\n"
"v_mfma_f32_16x16x32_bf16 v[140:143], v[92:95], v[144:147], 0\n"
"s_waitcnt lgkmcnt(1)\n"
"s_barrier\n"
"ds_read_b128 v[164:167], v6 offset:5120\n"
"v_mfma_f32_16x16x32_bf16 v[148:151], v[238:241], v[132:135], v[148:151]\n"
"ds_read_b128 v[220:223], v4 offset:57344\n"
"v_mfma_f32_16x16x32_bf16 v[148:151], v[240:243], v[24:27], v[148:151]\n"
"s_waitcnt lgkmcnt(2)\n"
"ds_read_b128 v[144:147], v6 offset:6144\n"
"v_mfma_f32_16x16x32_bf16 v[136:139], v[96:99], v[152:155], v[136:139]\n"
"v_exp_f32 v160, v160\n"
"v_exp_f32 v161, v161\n"
"ds_read_b128 v[168:171], v5 offset:57344\n"
"v_exp_f32 v162, v162\n"
"v_mfma_f32_16x16x32_bf16 v[156:159], v[100:103], v[152:155], v[156:159]\n"
"s_waitcnt lgkmcnt(3)\n"
"v_mfma_f32_16x16x32_bf16 v[140:143], v[164:167], v[152:155], v[140:143]\n"
"s_waitcnt lgkmcnt(2)\n"
"v_cvt_pk_bf16_f32 v160, v160, v161\n"
"v_mfma_f32_16x16x32_bf16 v[136:139], v[104:107], v[220:223], v[136:139]\n"
"ds_read_b128 v[152:155], v225 offset:2720\n"
"v_mfma_f32_16x16x32_bf16 v[156:159], v[108:111], v[220:223], v[156:159]\n"
"s_waitcnt lgkmcnt(2)\n"
"v_exp_f32 v163, v163\n"
"v_mfma_f32_16x16x32_bf16 v[140:143], v[144:147], v[220:223], v[140:143]\n"
"s_waitcnt lgkmcnt(1)\n"
"v_cvt_pk_bf16_f32 v161, v162, v163\n"
"v_mfma_f32_16x16x32_bf16 v[136:139], v[112:115], v[168:171], v[136:139]\n"
"ds_read_b128 v[220:223], v6 offset:7168\n"
"v_mfma_f32_16x16x32_bf16 v[156:159], v[116:119], v[168:171], v[156:159]\n"
"ds_read_b128 v[144:147], v4 offset:59392\n"
"ds_write_addtid_b32 v160 offset:54784\n"
"ds_write_addtid_b32 v161 offset:55040\n"
"s_waitcnt lgkmcnt(4)\n"
"v_mfma_f32_16x16x32_bf16 v[152:155], v[190:193], v[132:135], v[152:155]\n"
"ds_read_b128 v[160:163], v6 offset:8192\n"
"v_mfma_f32_16x16x32_bf16 v[152:155], v[192:195], v[24:27], v[152:155]\n"
"s_waitcnt lgkmcnt(4)\n"
"v_exp_f32 v148, v148\n"
"v_mfma_f32_16x16x32_bf16 v[140:143], v[220:223], v[168:171], v[140:143]\n"
"v_exp_f32 v149, v149\n"
"v_exp_f32 v150, v150\n"
"ds_read_b128 v[220:223], v6 offset:9216\n"
"v_cvt_pk_bf16_f32 v148, v148, v149\n"
"ds_write_addtid_b32 v148 offset:57856\n"
"s_waitcnt lgkmcnt(5)\n"
"v_mfma_f32_16x16x32_bf16 v[136:139], v[0:3], v[144:147], v[136:139]\n"
"v_bfe_u32 v225, v6, 2, 6\n"
"v_mfma_f32_16x16x32_bf16 v[156:159], v[120:123], v[144:147], v[156:159]\n"
"s_waitcnt lgkmcnt(2)\n"
"ds_read_b128 v[132:135], v5 offset:59392\n"
"ds_read_b128 v[164:167], v4 offset:49152\n"
"v_mfma_f32_16x16x32_bf16 v[140:143], v[160:163], v[144:147], v[140:143]\n"
"s_waitcnt lgkmcnt(1)\n"
"v_mfma_f32_16x16x32_bf16 v[136:139], v[124:127], v[132:135], v[136:139]\n"
"v_exp_f32 v151, v151\n"
"v_exp_f32 v152, v152\n"
"v_mfma_f32_16x16x32_bf16 v[156:159], v[128:131], v[132:135], v[156:159]\n"
"ds_read_b32 v225, v225 offset:1536\n"
"s_add_u32 s70, s50, 4294964608\n"
"v_mfma_f32_16x16x32_bf16 v[140:143], v[220:223], v[132:135], v[140:143]\n"
"s_nop 1\n"
"v_mul_f32 v136, s60, v136\n"
"s_waitcnt lgkmcnt(1)\n"
"v_mfma_f32_16x16x32_bf16 v[220:223], v[8:11], v[164:167], 0\n"
"v_mul_f32 v138, s60, v138\n"
"v_cvt_pk_bf16_f32 v149, v150, v151\n"
"ds_read_b128 v[144:147], v5 offset:49152\n"
"v_mul_f32 v156, s60, v156\n"
"v_mfma_f32_16x16x32_bf16 v[132:135], v[12:15], v[164:167], 0\n"
"v_exp_f32 v153, v153\n"
"v_exp_f32 v154, v154\n"
"v_mul_f32 v142, s60, v142\n"
"v_cvt_pk_bf16_f32 v152, v152, v153\n"
"ds_write_addtid_b32 v152 offset:57344\n"
"v_mfma_f32_16x16x32_bf16 v[160:163], v[16:19], v[164:167], 0\n"
"s_waitcnt lgkmcnt(1)\n"
"v_mfma_f32_16x16x32_bf16 v[220:223], v[20:23], v[144:147], v[220:223]\n"
"ds_write_addtid_b32 v149 offset:58112\n"
"ds_read_b128 v[148:151], v4 offset:51200\n"
"v_mfma_f32_16x16x32_bf16 v[132:135], v[28:31], v[144:147], v[132:135]\n"
"v_exp_f32 v155, v155\n"
"v_mul_f32 v139, s60, v139\n"
"v_mul_f32 v159, s60, v159\n"
"v_mul_f32 v141, s60, v141\n"
"v_mul_f32 v137, s60, v137\n"
"v_cvt_pk_bf16_f32 v136, v136, v137\n"
"v_mul_f32 v158, s60, v158\n"
"v_mul_f32 v157, s60, v157\n"
"v_mfma_f32_16x16x32_bf16 v[160:163], v[32:35], v[144:147], v[160:163]\n"
"s_waitcnt lgkmcnt(0)\n"
"ds_read_b128 v[144:147], v5 offset:51200\n"
"v_mfma_f32_16x16x32_bf16 v[220:223], v[36:39], v[148:151], v[220:223]\n"
"v_mfma_f32_16x16x32_bf16 v[132:135], v[40:43], v[148:151], v[132:135]\n"
"v_cvt_pk_bf16_f32 v137, v138, v139\n"
"v_cvt_pk_bf16_f32 v138, v156, v157\n"
"v_add_u32_sdwa v177, v225, v7 dst_sel:DWORD dst_unused:UNUSED_PAD src0_sel:WORD_0 src1_sel:DWORD\n"
"v_mul_f32 v143, s60, v143\n"
"v_cvt_pk_bf16_f32 v153, v154, v155\n"
"v_mul_f32 v140, s60, v140\n"
"s_add_u32 s71, s50, 4294965648\n"
"v_max_u32 v176, s70, v177\n"
"ds_write_addtid_b32 v153 offset:57600\n"
"v_cvt_pk_bf16_f32 v139, v158, v159\n"
"ds_read_b128 v[152:155], v176 offset:2688\n"
"s_waitcnt lgkmcnt(1)\n"
"s_barrier\n"
"s_waitcnt lgkmcnt(0)\n"
"v_mfma_f32_16x16x32_bf16 v[152:155], v[178:181], v[136:139], v[152:155]\n"
"v_cvt_pk_bf16_f32 v27, v142, v143\n"
"v_cvt_pk_bf16_f32 v26, v140, v141\n"
"s_nop 1\n"
"v_mfma_f32_16x16x32_bf16 v[152:155], v[180:183], v[24:27], v[152:155]\n"
"ds_read_b128 v[140:143], v176 offset:2704\n"
"v_mfma_f32_16x16x32_bf16 v[160:163], v[44:47], v[148:151], v[160:163]\n"
"ds_read_b128 v[148:151], v4 offset:53248\n"
"v_mfma_f32_16x16x32_bf16 v[220:223], v[48:51], v[144:147], v[220:223]\n"
"v_mfma_f32_16x16x32_bf16 v[132:135], v[52:55], v[144:147], v[132:135]\n"
"v_mfma_f32_16x16x32_bf16 v[160:163], v[56:59], v[144:147], v[160:163]\n"
"s_waitcnt lgkmcnt(1)\n"
"v_min_u32 v175, s71, v177\n"
"ds_read_b128 v[144:147], v175 offset:4032\n"
"v_mfma_f32_16x16x32_bf16 v[140:143], v[184:187], v[136:139], v[140:143]\n"
"v_mfma_f32_16x16x32_bf16 v[140:143], v[186:189], v[24:27], v[140:143]\n"
"s_waitcnt lgkmcnt(1)\n"
"v_exp_f32 v152, v152\n"
"v_exp_f32 v153, v153\n"
"v_mfma_f32_16x16x32_bf16 v[220:223], v[60:63], v[148:151], v[220:223]\n"
"ds_read_b128 v[156:159], v5 offset:53248\n"
"v_mfma_f32_16x16x32_bf16 v[132:135], v[64:67], v[148:151], v[132:135]\n"
"v_exp_f32 v154, v154\n"
"v_exp_f32 v155, v155\n"
"v_mfma_f32_16x16x32_bf16 v[160:163], v[68:71], v[148:151], v[160:163]\n"
"s_waitcnt lgkmcnt(1)\n"
"ds_read_b128 v[148:151], v175 offset:4048\n"
"v_mfma_f32_16x16x32_bf16 v[144:147], v[208:211], v[136:139], v[144:147]\n"
"v_cvt_pk_bf16_f32 v152, v152, v153\n"
"v_cvt_pk_bf16_f32 v153, v154, v155\n"
"ds_write_addtid_b32 v152 offset:55296\n"
"ds_write_addtid_b32 v153 offset:55552\n"
"v_mfma_f32_16x16x32_bf16 v[144:147], v[210:213], v[24:27], v[144:147]\n"
"s_waitcnt lgkmcnt(3)\n"
"v_mfma_f32_16x16x32_bf16 v[220:223], v[72:75], v[156:159], v[220:223]\n"
"v_exp_f32 v140, v140\n"
"v_mfma_f32_16x16x32_bf16 v[132:135], v[76:79], v[156:159], v[132:135]\n"
"v_exp_f32 v141, v141\n"
"v_exp_f32 v142, v142\n"
"ds_read_b128 v[152:155], v4 offset:30720\n"
"v_mfma_f32_16x16x32_bf16 v[160:163], v[80:83], v[156:159], v[160:163]\n"
"v_cvt_pk_bf16_f32 v140, v140, v141\n"
"v_exp_f32 v143, v143\n"
"ds_write_addtid_b32 v140 offset:55808\n"
"v_cvt_pk_bf16_f32 v141, v142, v143\n"
"ds_write_addtid_b32 v141 offset:56064\n"
"s_add_u32 s69, s50, 4294965952\n"
"v_cvt_pk_bf16_f32 v220, v220, v221\n"
"v_cvt_pk_bf16_f32 v221, v222, v223\n"
"s_waitcnt lgkmcnt(5)\n"
"v_mfma_f32_16x16x32_bf16 v[148:151], v[214:217], v[136:139], v[148:151]\n"
"v_cvt_pk_bf16_f32 v225, v162, v163\n"
"v_mfma_f32_16x16x32_bf16 v[148:151], v[216:219], v[24:27], v[148:151]\n"
"s_waitcnt lgkmcnt(2)\n"
"ds_read_b128 v[140:143], v176 offset:2720\n"
"v_cvt_pk_bf16_f32 v223, v134, v135\n"
"v_cvt_pk_bf16_f32 v222, v132, v133\n"
"v_mfma_f32_16x16x32_bf16 v[132:135], v[84:87], v[152:155], 0\n"
"ds_read_b128 v[156:159], v5 offset:30720\n"
"v_cvt_pk_bf16_f32 v224, v160, v161\n"
"v_mfma_f32_16x16x32_bf16 v[160:163], v[88:91], v[152:155], 0\n"
"v_exp_f32 v144, v144\n"
"v_exp_f32 v145, v145\n"
"v_exp_f32 v146, v146\n"
"v_exp_f32 v147, v147\n"
"v_mfma_f32_16x16x32_bf16 v[164:167], v[92:95], v[152:155], 0\n"
"s_waitcnt lgkmcnt(1)\n"
"v_cvt_pk_bf16_f32 v144, v144, v145\n"
"v_mfma_f32_16x16x32_bf16 v[140:143], v[190:193], v[136:139], v[140:143]\n"
"v_exp_f32 v148, v148\n"
"v_max_u32 v174, s69, v177\n"
"ds_read_b128 v[168:171], v174 offset:1344\n"
"ds_write_addtid_b32 v144 offset:56320\n"
"ds_read_b128 v[152:155], v6 offset:5120\n"
"v_mfma_f32_16x16x32_bf16 v[140:143], v[192:195], v[24:27], v[140:143]\n"
"s_waitcnt lgkmcnt(3)\n"
"v_exp_f32 v149, v149\n"
"v_mfma_f32_16x16x32_bf16 v[132:135], v[96:99], v[156:159], v[132:135]\n"
"v_exp_f32 v150, v150\n"
"v_cvt_pk_bf16_f32 v145, v146, v147\n"
"v_mfma_f32_16x16x32_bf16 v[160:163], v[100:103], v[156:159], v[160:163]\n"
"ds_write_addtid_b32 v145 offset:56576\n"
"ds_read_b128 v[144:147], v4 offset:32768\n"
"v_exp_f32 v151, v151\n"
"s_waitcnt lgkmcnt(2)\n"
"v_mfma_f32_16x16x32_bf16 v[164:167], v[152:155], v[156:159], v[164:167]\n"
"ds_read_b128 v[156:159], v174 offset:1360\n"
"v_mfma_f32_16x16x32_bf16 v[168:171], v[226:229], v[136:139], v[168:171]\n"
"ds_read_b128 v[152:155], v6 offset:6144\n"
"v_mfma_f32_16x16x32_bf16 v[168:171], v[228:231], v[24:27], v[168:171]\n"
"s_waitcnt lgkmcnt(2)\n"
"v_cvt_pk_bf16_f32 v148, v148, v149\n"
"v_cvt_pk_bf16_f32 v149, v150, v151\n"
"v_mfma_f32_16x16x32_bf16 v[132:135], v[104:107], v[144:147], v[132:135]\n"
"v_mfma_f32_16x16x32_bf16 v[160:163], v[108:111], v[144:147], v[160:163]\n"
"v_exp_f32 v140, v140\n"
"ds_write_addtid_b32 v148 offset:56832\n"
"s_waitcnt lgkmcnt(1)\n"
"v_mfma_f32_16x16x32_bf16 v[164:167], v[152:155], v[144:147], v[164:167]\n"
"ds_read_b128 v[226:229], v5 offset:32768\n"
"v_mfma_f32_16x16x32_bf16 v[156:159], v[232:235], v[136:139], v[156:159]\n"
"v_exp_f32 v141, v141\n"
"s_nop 0\n"
"v_cvt_pk_bf16_f32 v140, v140, v141\n"
"ds_write_addtid_b32 v140 offset:34304\n"
"ds_write_addtid_b32 v149 offset:57088\n"
"v_mfma_f32_16x16x32_bf16 v[156:159], v[234:237], v[24:27], v[156:159]\n"
"s_waitcnt lgkmcnt(2)\n"
"v_mfma_f32_16x16x32_bf16 v[132:135], v[112:115], v[226:229], v[132:135]\n"
"v_exp_f32 v168, v168\n"
"ds_read_b128 v[144:147], v6 offset:7168\n"
"v_mfma_f32_16x16x32_bf16 v[160:163], v[116:119], v[226:229], v[160:163]\n"
"s_waitcnt lgkmcnt(0)\n"
"ds_read_b128 v[148:151], v174 offset:1376\n"
"v_mfma_f32_16x16x32_bf16 v[164:167], v[144:147], v[226:229], v[164:167]\n"
"v_exp_f32 v169, v169\n"
"v_exp_f32 v170, v170\n"
"v_exp_f32 v171, v171\n"
"s_barrier\n"
"ds_read_b128 v[140:143], v4 offset:34816\n"
"ds_read_b128 v[226:229], v6 offset:8192\n"
"v_cvt_pk_bf16_f32 v168, v168, v169\n"
"v_exp_f32 v156, v156\n"
"ds_write_addtid_b32 v168 offset:57856\n"
"s_waitcnt lgkmcnt(3)\n"
"v_mfma_f32_16x16x32_bf16 v[148:151], v[238:241], v[136:139], v[148:151]\n"
"ds_read_b128 v[144:147], v175 offset:4064\n"
"v_mfma_f32_16x16x32_bf16 v[148:151], v[240:243], v[24:27], v[148:151]\n"
"s_waitcnt lgkmcnt(3)\n"
"v_exp_f32 v157, v157\n"
"v_mfma_f32_16x16x32_bf16 v[132:135], v[0:3], v[140:143], v[132:135]\n"
"ds_read_b128 v[152:155], v5 offset:34816\n"
"v_cvt_pk_bf16_f32 v169, v170, v171\n"
"v_exp_f32 v158, v158\n"
"v_exp_f32 v159, v159\n"
"ds_write_addtid_b32 v169 offset:58112\n"
"v_mfma_f32_16x16x32_bf16 v[160:163], v[120:123], v[140:143], v[160:163]\n"
"s_add_u32 s73, s50, 4294966976\n"
"v_cvt_pk_bf16_f32 v156, v156, v157\n"
"s_waitcnt lgkmcnt(4)\n"
"v_mfma_f32_16x16x32_bf16 v[164:167], v[226:229], v[140:143], v[164:167]\n"
"s_waitcnt lgkmcnt(2)\n"
"ds_read_b128 v[226:229], v6 offset:9216\n"
"v_mfma_f32_16x16x32_bf16 v[144:147], v[220:223], v[136:139], v[144:147]\n"
"ds_read_b128 v[136:139], v4 offset:24576\n"
"v_mfma_f32_16x16x32_bf16 v[144:147], v[222:225], v[24:27], v[144:147]\n"
"ds_write_addtid_b32 v156 offset:58368\n"
"v_cvt_pk_bf16_f32 v157, v158, v159\n"
"ds_read_b128 v[140:143], v5 offset:24576\n"
"s_waitcnt lgkmcnt(5)\n"
"v_mfma_f32_16x16x32_bf16 v[132:135], v[124:127], v[152:155], v[132:135]\n"
"v_mfma_f32_16x16x32_bf16 v[160:163], v[128:131], v[152:155], v[160:163]\n"
"v_exp_f32 v148, v148\n"
"v_exp_f32 v149, v149\n"
"s_nop 0\n"
"v_cvt_pk_bf16_f32 v148, v148, v149\n"
"v_bfe_u32 v177, v6, 2, 6\n"
"ds_write_addtid_b32 v157 offset:58624\n"
"s_waitcnt lgkmcnt(4)\n"
"v_mfma_f32_16x16x32_bf16 v[164:167], v[226:229], v[152:155], v[164:167]\n"
"s_waitcnt lgkmcnt(3)\n"
"v_mul_f32 v132, s60, v132\n"
"v_mfma_f32_16x16x32_bf16 v[226:229], v[8:11], v[136:139], 0\n"
"ds_read_b32 v177, v177 offset:1600\n"
"v_mul_f32 v163, s60, v163\n"
"v_mfma_f32_16x16x32_bf16 v[152:155], v[12:15], v[136:139], 0\n"
"ds_write_addtid_b32 v148 offset:58880\n"
"v_mul_f32 v162, s60, v162\n"
"v_mul_f32 v133, s60, v133\n"
"v_mfma_f32_16x16x32_bf16 v[148:151], v[16:19], v[136:139], 0\n"
"s_waitcnt lgkmcnt(3)\n"
"v_exp_f32 v144, v144\n"
"v_exp_f32 v145, v145\n"
"v_mfma_f32_16x16x32_bf16 v[226:229], v[20:23], v[140:143], v[226:229]\n"
"ds_read_b128 v[136:139], v4 offset:26624\n"
"v_mul_f32 v160, s60, v160\n"
"v_mfma_f32_16x16x32_bf16 v[152:155], v[28:31], v[140:143], v[152:155]\n"
"v_cvt_pk_bf16_f32 v132, v132, v133\n"
"v_mul_f32 v161, s60, v161\n"
"v_cvt_pk_bf16_f32 v144, v144, v145\n"
"v_mul_f32 v164, s60, v164\n"
"s_waitcnt lgkmcnt(2)\n"
"v_add_u32_sdwa v176, v177, v7 dst_sel:DWORD dst_unused:UNUSED_PAD src0_sel:WORD_0 src1_sel:DWORD\n"
"v_mul_f32 v135, s60, v135\n"
"v_mfma_f32_16x16x32_bf16 v[148:151], v[32:35], v[140:143], v[148:151]\n"
"ds_write_addtid_b32 v144 offset:57344\n"
"v_mul_f32 v134, s60, v134\n"
"v_cvt_pk_bf16_f32 v133, v134, v135\n"
"v_cvt_pk_bf16_f32 v134, v160, v161\n"
"v_cvt_pk_bf16_f32 v135, v162, v163\n"
"s_waitcnt lgkmcnt(1)\n"
"v_mfma_f32_16x16x32_bf16 v[226:229], v[36:39], v[136:139], v[226:229]\n"
"v_min_u32 v177, s73, v176\n"
"ds_read_b128 v[156:159], v177 offset:2704\n"
"ds_read_b128 v[140:143], v5 offset:26624\n"
"v_mul_f32 v167, s60, v167\n"
"v_mul_f32 v166, s60, v166\n"
"v_mfma_f32_16x16x32_bf16 v[152:155], v[40:43], v[136:139], v[152:155]\n"
"s_waitcnt lgkmcnt(2)\n"
"s_barrier\n"
"v_mul_f32 v165, s60, v165\n"
"v_cvt_pk_bf16_f32 v26, v164, v165\n"
"v_cvt_pk_bf16_f32 v27, v166, v167\n"
"s_waitcnt lgkmcnt(1)\n"
"v_mfma_f32_16x16x32_bf16 v[156:159], v[184:187], v[132:135], v[156:159]\n"
"v_mfma_f32_16x16x32_bf16 v[156:159], v[186:189], v[24:27], v[156:159]\n"
"ds_read_b128 v[144:147], v177 offset:2720\n"
"v_mfma_f32_16x16x32_bf16 v[148:151], v[44:47], v[136:139], v[148:151]\n"
"s_waitcnt lgkmcnt(1)\n"
"ds_read_b128 v[136:139], v4 offset:28672\n"
"v_mfma_f32_16x16x32_bf16 v[226:229], v[48:51], v[140:143], v[226:229]\n"
"v_mfma_f32_16x16x32_bf16 v[152:155], v[52:55], v[140:143], v[152:155]\n"
"v_mfma_f32_16x16x32_bf16 v[148:151], v[56:59], v[140:143], v[148:151]\n"
"s_waitcnt lgkmcnt(1)\n"
"v_mfma_f32_16x16x32_bf16 v[144:147], v[190:193], v[132:135], v[144:147]\n"
"s_add_u32 s68, s50, 4294965632\n"
"v_exp_f32 v156, v156\n"
"v_min_u32 v175, s68, v176\n"
"ds_read_b128 v[140:143], v175 offset:4048\n"
"v_mfma_f32_16x16x32_bf16 v[144:147], v[192:195], v[24:27], v[144:147]\n"
"s_waitcnt lgkmcnt(1)\n"
"v_exp_f32 v157, v157\n"
"v_exp_f32 v158, v158\n"
"v_mfma_f32_16x16x32_bf16 v[226:229], v[60:63], v[136:139], v[226:229]\n"
"ds_read_b128 v[160:163], v5 offset:28672\n"
"v_mfma_f32_16x16x32_bf16 v[152:155], v[64:67], v[136:139], v[152:155]\n"
"v_cvt_pk_bf16_f32 v156, v156, v157\n"
"ds_write_addtid_b32 v156 offset:53248\n"
"v_mfma_f32_16x16x32_bf16 v[148:151], v[68:71], v[136:139], v[148:151]\n"
"s_waitcnt lgkmcnt(2)\n"
"v_exp_f32 v159, v159\n"
"v_mfma_f32_16x16x32_bf16 v[140:143], v[214:217], v[132:135], v[140:143]\n"
"ds_read_b128 v[136:139], v175 offset:4064\n"
"v_cvt_pk_bf16_f32 v157, v158, v159\n"
"v_mfma_f32_16x16x32_bf16 v[140:143], v[216:219], v[24:27], v[140:143]\n"
"s_waitcnt lgkmcnt(2)\n"
"v_exp_f32 v144, v144\n"
"v_mfma_f32_16x16x32_bf16 v[226:229], v[72:75], v[160:163], v[226:229]\n"
"ds_write_addtid_b32 v157 offset:53504\n"
"ds_read_b128 v[156:159], v4 offset:18432\n"
"v_mfma_f32_16x16x32_bf16 v[152:155], v[76:79], v[160:163], v[152:155]\n"
"s_add_u32 s72, s50, 4294965936\n"
"v_exp_f32 v145, v145\n"
"v_exp_f32 v146, v146\n"
"v_exp_f32 v147, v147\n"
"v_mfma_f32_16x16x32_bf16 v[148:151], v[80:83], v[160:163], v[148:151]\n"
"v_cvt_pk_bf16_f32 v226, v226, v227\n"
"v_cvt_pk_bf16_f32 v144, v144, v145\n"
"ds_write_addtid_b32 v144 offset:53760\n"
"v_cvt_pk_bf16_f32 v145, v146, v147\n"
"ds_write_addtid_b32 v145 offset:54016\n"
"v_cvt_pk_bf16_f32 v227, v228, v229\n"
"s_waitcnt lgkmcnt(4)\n"
"v_mfma_f32_16x16x32_bf16 v[136:139], v[220:223], v[132:135], v[136:139]\n"
"v_cvt_pk_bf16_f32 v229, v154, v155\n"
"v_cvt_pk_bf16_f32 v228, v152, v153\n"
"v_cvt_pk_bf16_f32 v230, v148, v149\n"
"v_mfma_f32_16x16x32_bf16 v[136:139], v[222:225], v[24:27], v[136:139]\n"
"s_waitcnt lgkmcnt(2)\n"
"ds_read_b128 v[144:147], v177 offset:2736\n"
"v_cvt_pk_bf16_f32 v231, v150, v151\n"
"v_mfma_f32_16x16x32_bf16 v[148:151], v[84:87], v[156:159], 0\n"
"v_exp_f32 v140, v140\n"
"v_mfma_f32_16x16x32_bf16 v[160:163], v[88:91], v[156:159], 0\n"
"v_exp_f32 v141, v141\n"
"ds_read_b128 v[152:155], v5 offset:18432\n"
"v_mfma_f32_16x16x32_bf16 v[164:167], v[92:95], v[156:159], 0\n"
"v_cvt_pk_bf16_f32 v140, v140, v141\n"
"v_exp_f32 v142, v142\n"
"ds_write_addtid_b32 v140 offset:54272\n"
"v_max_u32 v174, s72, v176\n"
"s_waitcnt lgkmcnt(2)\n"
"v_mfma_f32_16x16x32_bf16 v[144:147], v[196:199], v[132:135], v[144:147]\n"
"ds_read_b128 v[156:159], v6 offset:5120\n"
"v_mfma_f32_16x16x32_bf16 v[144:147], v[198:201], v[24:27], v[144:147]\n"
"v_exp_f32 v143, v143\n"
"s_nop 0\n"
"v_cvt_pk_bf16_f32 v141, v142, v143\n"
"ds_write_addtid_b32 v141 offset:54528\n"
"ds_read_b128 v[168:171], v174 offset:1360\n"
"ds_read_b128 v[140:143], v4 offset:20480\n"
"s_waitcnt lgkmcnt(5)\n"
"v_mfma_f32_16x16x32_bf16 v[148:151], v[96:99], v[152:155], v[148:151]\n"
"v_exp_f32 v136, v136\n"
"v_mfma_f32_16x16x32_bf16 v[160:163], v[100:103], v[152:155], v[160:163]\n"
"s_waitcnt lgkmcnt(3)\n"
"v_exp_f32 v137, v137\n"
"v_exp_f32 v138, v138\n"
"v_mfma_f32_16x16x32_bf16 v[164:167], v[156:159], v[152:155], v[164:167]\n"
"v_exp_f32 v139, v139\n"
"v_cvt_pk_bf16_f32 v136, v136, v137\n"
"ds_write_addtid_b32 v136 offset:54784\n"
"ds_read_b128 v[152:155], v6 offset:6144\n"
"s_waitcnt lgkmcnt(3)\n"
"v_mfma_f32_16x16x32_bf16 v[168:171], v[232:235], v[132:135], v[168:171]\n"
"v_cvt_pk_bf16_f32 v137, v138, v139\n"
"ds_read_b128 v[156:159], v174 offset:1376\n"
"v_mfma_f32_16x16x32_bf16 v[168:171], v[234:237], v[24:27], v[168:171]\n"
"s_waitcnt lgkmcnt(3)\n"
"v_exp_f32 v144, v144\n"
"v_exp_f32 v145, v145\n"
"v_mfma_f32_16x16x32_bf16 v[148:151], v[104:107], v[140:143], v[148:151]\n"
"ds_write_addtid_b32 v137 offset:55040\n"
"v_exp_f32 v146, v146\n"
"v_mfma_f32_16x16x32_bf16 v[160:163], v[108:111], v[140:143], v[160:163]\n"
"s_waitcnt lgkmcnt(2)\n"
"ds_read_b128 v[232:235], v5 offset:20480\n"
"v_mfma_f32_16x16x32_bf16 v[164:167], v[152:155], v[140:143], v[164:167]\n"
"s_waitcnt lgkmcnt(2)\n"
"ds_read_b128 v[136:139], v6 offset:7168\n"
"v_cvt_pk_bf16_f32 v144, v144, v145\n"
"v_mfma_f32_16x16x32_bf16 v[156:159], v[238:241], v[132:135], v[156:159]\n"
"ds_write_addtid_b32 v144 offset:33792\n"
"v_exp_f32 v147, v147\n"
"s_nop 0\n"
"v_cvt_pk_bf16_f32 v145, v146, v147\n"
"v_mfma_f32_16x16x32_bf16 v[156:159], v[240:243], v[24:27], v[156:159]\n"
"ds_write_addtid_b32 v145 offset:34048\n"
"s_waitcnt lgkmcnt(3)\n"
"v_mfma_f32_16x16x32_bf16 v[148:151], v[112:115], v[232:235], v[148:151]\n"
"v_exp_f32 v168, v168\n"
"ds_read_b128 v[144:147], v4 offset:22528\n"
"ds_read_b128 v[152:155], v5 offset:22528\n"
"v_mfma_f32_16x16x32_bf16 v[160:163], v[116:119], v[232:235], v[160:163]\n"
"s_waitcnt lgkmcnt(4)\n"
"v_exp_f32 v169, v169\n"
"v_exp_f32 v170, v170\n"
"v_mfma_f32_16x16x32_bf16 v[164:167], v[136:139], v[232:235], v[164:167]\n"
"s_waitcnt lgkmcnt(2)\n"
"ds_read_b128 v[140:143], v174 offset:1392\n"
"ds_read_b128 v[232:235], v6 offset:8192\n"
"s_barrier\n"
"ds_read_b128 v[136:139], v175 offset:4080\n"
"v_exp_f32 v171, v171\n"
"v_cvt_pk_bf16_f32 v168, v168, v169\n"
"v_cvt_pk_bf16_f32 v169, v170, v171\n"
"s_waitcnt lgkmcnt(2)\n"
"v_mfma_f32_16x16x32_bf16 v[140:143], v[244:247], v[132:135], v[140:143]\n"
"v_mfma_f32_16x16x32_bf16 v[140:143], v[246:249], v[24:27], v[140:143]\n"
"ds_write_addtid_b32 v168 offset:57856\n"
"ds_write_addtid_b32 v169 offset:58112\n"
"v_mfma_f32_16x16x32_bf16 v[148:151], v[0:3], v[144:147], v[148:151]\n"
"v_exp_f32 v156, v156\n"
"v_exp_f32 v157, v157\n"
"v_mfma_f32_16x16x32_bf16 v[160:163], v[120:123], v[144:147], v[160:163]\n"
"s_waitcnt lgkmcnt(3)\n"
"v_exp_f32 v158, v158\n"
"v_mfma_f32_16x16x32_bf16 v[164:167], v[232:235], v[144:147], v[164:167]\n"
"s_waitcnt lgkmcnt(2)\n"
"v_exp_f32 v159, v159\n"
"ds_read_b128 v[232:235], v6 offset:9216\n"
"v_cvt_pk_bf16_f32 v156, v156, v157\n"
"v_mfma_f32_16x16x32_bf16 v[136:139], v[226:229], v[132:135], v[136:139]\n"
"ds_read_b128 v[132:135], v4 offset:12288\n"
"ds_write_addtid_b32 v156 offset:58368\n"
"v_mfma_f32_16x16x32_bf16 v[136:139], v[228:231], v[24:27], v[136:139]\n"
"v_cvt_pk_bf16_f32 v157, v158, v159\n"
"ds_write_addtid_b32 v157 offset:58624\n"
"v_bfe_u32 v177, v6, 2, 6\n"
"ds_read_b32 v177, v177 offset:1664\n"
"v_mfma_f32_16x16x32_bf16 v[148:151], v[124:127], v[152:155], v[148:151]\n"
"v_exp_f32 v140, v140\n"
"v_exp_f32 v141, v141\n"
"v_exp_f32 v142, v142\n"
"v_cvt_pk_bf16_f32 v140, v140, v141\n"
"v_mfma_f32_16x16x32_bf16 v[160:163], v[128:131], v[152:155], v[160:163]\n"
"s_waitcnt lgkmcnt(4)\n"
"ds_read_b128 v[144:147], v5 offset:12288\n"
"v_exp_f32 v143, v143\n"
"v_mfma_f32_16x16x32_bf16 v[164:167], v[232:235], v[152:155], v[164:167]\n"
"s_waitcnt lgkmcnt(1)\n"
"v_cvt_pk_bf16_f32 v141, v142, v143\n"
"v_add_u32_sdwa v176, v177, v7 dst_sel:DWORD dst_unused:UNUSED_PAD src0_sel:WORD_0 src1_sel:DWORD\n"
"v_mfma_f32_16x16x32_bf16 v[232:235], v[8:11], v[132:135], 0\n"
"ds_write_addtid_b32 v140 offset:58880\n"
"v_mfma_f32_16x16x32_bf16 v[152:155], v[12:15], v[132:135], 0\n"
"s_add_u32 s69, s50, 4294965920\n"
"v_mul_f32 v148, s60, v148\n"
"v_max_u32 v175, s69, v176\n"
"ds_write_addtid_b32 v141 offset:59136\n"
"v_mfma_f32_16x16x32_bf16 v[140:143], v[16:19], v[132:135], 0\n"
"s_waitcnt lgkmcnt(2)\n"
"ds_read_b128 v[156:159], v175 offset:1392\n"
"ds_read_b128 v[132:135], v4 offset:14336\n"
"v_mfma_f32_16x16x32_bf16 v[232:235], v[20:23], v[144:147], v[232:235]\n"
"v_mul_f32 v149, s60, v149\n"
"v_mul_f32 v151, s60, v151\n"
"v_exp_f32 v136, v136\n"
"v_exp_f32 v137, v137\n"
"s_nop 0\n"
"v_cvt_pk_bf16_f32 v136, v136, v137\n"
"v_cvt_pk_bf16_f32 v148, v148, v149\n"
"v_exp_f32 v138, v138\n"
"v_mfma_f32_16x16x32_bf16 v[152:155], v[28:31], v[144:147], v[152:155]\n"
"ds_write_addtid_b32 v136 offset:57344\n"
"v_mul_f32 v162, s60, v162\n"
"v_exp_f32 v139, v139\n"
"v_mfma_f32_16x16x32_bf16 v[140:143], v[32:35], v[144:147], v[140:143]\n"
"s_waitcnt lgkmcnt(1)\n"
"v_mul_f32 v166, s60, v166\n"
"v_cvt_pk_bf16_f32 v137, v138, v139\n"
"v_mfma_f32_16x16x32_bf16 v[232:235], v[36:39], v[132:135], v[232:235]\n"
"ds_write_addtid_b32 v137 offset:57600\n"
"v_mul_f32 v150, s60, v150\n"
"v_mul_f32 v161, s60, v161\n"
"v_mul_f32 v163, s60, v163\n"
"v_mfma_f32_16x16x32_bf16 v[152:155], v[40:43], v[132:135], v[152:155]\n"
"ds_read_b128 v[136:139], v5 offset:14336\n"
"v_mul_f32 v167, s60, v167\n"
"v_mul_f32 v160, s60, v160\n"
"v_cvt_pk_bf16_f32 v149, v150, v151\n"
"v_cvt_pk_bf16_f32 v151, v162, v163\n"
"v_add_u32_sdwa v177, sext(v177), v176 dst_sel:DWORD dst_unused:UNUSED_PAD src0_sel:WORD_1 src1_sel:DWORD\n"
"v_cvt_pk_bf16_f32 v150, v160, v161\n"
"v_mul_f32 v165, s60, v165\n"
"v_mul_f32 v164, s60, v164\n"
"v_cvt_pk_bf16_f32 v27, v166, v167\n"
"v_mfma_f32_16x16x32_bf16 v[156:159], v[244:247], v[148:151], v[156:159]\n"
"s_waitcnt lgkmcnt(1)\n"
"s_barrier\n"
"v_max_u32 v174, s69, v177\n"
"v_cvt_pk_bf16_f32 v26, v164, v165\n"
"s_add_u32 s70, s50, 4294966960\n"
"s_nop 0\n"
"v_mfma_f32_16x16x32_bf16 v[156:159], v[246:249], v[24:27], v[156:159]\n"
"ds_read_b128 v[244:247], v174 offset:1408\n"
"v_mfma_f32_16x16x32_bf16 v[140:143], v[44:47], v[132:135], v[140:143]\n"
"s_waitcnt lgkmcnt(1)\n"
"ds_read_b128 v[132:135], v4 offset:16384\n"
"v_mfma_f32_16x16x32_bf16 v[232:235], v[48:51], v[136:139], v[232:235]\n"
"v_min_u32 v174, s70, v176\n"
"v_mfma_f32_16x16x32_bf16 v[152:155], v[52:55], v[136:139], v[152:155]\n"
"v_mfma_f32_16x16x32_bf16 v[140:143], v[56:59], v[136:139], v[140:143]\n"
"s_waitcnt lgkmcnt(1)\n"
"v_mfma_f32_16x16x32_bf16 v[244:247], v[250:253], v[148:151], v[244:247]\n"
"ds_read_b128 v[248:251], v174 offset:2720\n"
"v_mfma_f32_16x16x32_bf16 v[244:247], v[252:255], v[24:27], v[244:247]\n"
"s_waitcnt lgkmcnt(1)\n"
"v_exp_f32 v156, v156\n"
"v_exp_f32 v157, v157\n"
"v_mfma_f32_16x16x32_bf16 v[232:235], v[60:63], v[132:135], v[232:235]\n"
"ds_read_b128 v[252:255], v5 offset:16384\n"
"ds_read_b128 v[136:139], v4 offset:36864\n"
"v_mfma_f32_16x16x32_bf16 v[152:155], v[64:67], v[132:135], v[152:155]\n"
"v_cvt_pk_bf16_f32 v156, v156, v157\n"
"ds_write_addtid_b32 v156 offset:55296\n"
"v_mfma_f32_16x16x32_bf16 v[140:143], v[68:71], v[132:135], v[140:143]\n"
"s_waitcnt lgkmcnt(3)\n"
"v_exp_f32 v158, v158\n"
"v_mfma_f32_16x16x32_bf16 v[248:251], v[190:193], v[148:151], v[248:251]\n"
"ds_read_b128 v[132:135], v174 offset:2736\n"
"v_mfma_f32_16x16x32_bf16 v[248:251], v[192:195], v[24:27], v[248:251]\n"
"s_waitcnt lgkmcnt(3)\n"
"v_exp_f32 v159, v159\n"
"v_exp_f32 v244, v244\n"
"v_mfma_f32_16x16x32_bf16 v[232:235], v[72:75], v[252:255], v[232:235]\n"
"v_cvt_pk_bf16_f32 v157, v158, v159\n"
"v_mfma_f32_16x16x32_bf16 v[152:155], v[76:79], v[252:255], v[152:155]\n"
"v_exp_f32 v245, v245\n"
"v_exp_f32 v246, v246\n"
"ds_write_addtid_b32 v157 offset:55552\n"
"v_mfma_f32_16x16x32_bf16 v[140:143], v[80:83], v[252:255], v[140:143]\n"
"v_exp_f32 v247, v247\n"
"v_cvt_pk_bf16_f32 v244, v244, v245\n"
"v_cvt_pk_bf16_f32 v232, v232, v233\n"
"v_cvt_pk_bf16_f32 v245, v246, v247\n"
"v_min_u32 v174, s70, v177\n"
"ds_write_addtid_b32 v244 offset:55808\n"
"s_waitcnt lgkmcnt(2)\n"
"v_mfma_f32_16x16x32_bf16 v[132:135], v[196:199], v[148:151], v[132:135]\n"
"ds_write_addtid_b32 v245 offset:56064\n"
"v_cvt_pk_bf16_f32 v233, v234, v235\n"
"v_mfma_f32_16x16x32_bf16 v[132:135], v[198:201], v[24:27], v[132:135]\n"
"ds_read_b128 v[244:247], v174 offset:2752\n"
"v_cvt_pk_bf16_f32 v234, v152, v153\n"
"v_cvt_pk_bf16_f32 v237, v142, v143\n"
"v_cvt_pk_bf16_f32 v236, v140, v141\n"
"v_mfma_f32_16x16x32_bf16 v[252:255], v[84:87], v[136:139], 0\n"
"v_exp_f32 v250, v250\n"
"v_cvt_pk_bf16_f32 v235, v154, v155\n"
"v_mfma_f32_16x16x32_bf16 v[144:147], v[88:91], v[136:139], 0\n"
"v_exp_f32 v251, v251\n"
"v_exp_f32 v132, v132\n"
"ds_read_b128 v[140:143], v5 offset:36864\n"
"v_mfma_f32_16x16x32_bf16 v[152:155], v[92:95], v[136:139], 0\n"
"s_waitcnt lgkmcnt(1)\n"
"ds_read_b128 v[136:139], v6 offset:5120\n"
"v_mfma_f32_16x16x32_bf16 v[244:247], v[202:205], v[148:151], v[244:247]\n"
"v_cvt_pk_bf16_f32 v249, v250, v251\n"
"ds_write_addtid_b32 v249 offset:56576\n"
"s_add_u32 s71, s50, 4294965616\n"
"ds_read_b128 v[248:251], v175 offset:1376\n"
"v_mfma_f32_16x16x32_bf16 v[244:247], v[204:207], v[24:27], v[244:247]\n"
"s_waitcnt lgkmcnt(3)\n"
"v_mfma_f32_16x16x32_bf16 v[252:255], v[96:99], v[140:143], v[252:255]\n"
"v_exp_f32 v133, v133\n"
"ds_read_b128 v[156:159], v4 offset:38912\n"
"v_mfma_f32_16x16x32_bf16 v[144:147], v[100:103], v[140:143], v[144:147]\n"
"v_cvt_pk_bf16_f32 v132, v132, v133\n"
"ds_write_addtid_b32 v132 offset:56832\n"
"s_waitcnt lgkmcnt(4)\n"
"v_mfma_f32_16x16x32_bf16 v[152:155], v[136:139], v[140:143], v[152:155]\n"
"s_waitcnt lgkmcnt(2)\n"
"ds_read_b128 v[136:139], v6 offset:6144\n"
"v_min_u32 v132, s71, v176\n"
"ds_read_b128 v[140:143], v132 offset:4064\n"
"v_mfma_f32_16x16x32_bf16 v[248:251], v[238:241], v[148:151], v[248:251]\n"
"v_exp_f32 v134, v134\n"
"v_exp_f32 v135, v135\n"
"v_exp_f32 v244, v244\n"
"v_exp_f32 v245, v245\n"
"v_cvt_pk_bf16_f32 v133, v134, v135\n"
"ds_write_addtid_b32 v133 offset:57088\n"
"v_mfma_f32_16x16x32_bf16 v[248:251], v[240:243], v[24:27], v[248:251]\n"
"s_waitcnt lgkmcnt(4)\n"
"v_mfma_f32_16x16x32_bf16 v[252:255], v[104:107], v[156:159], v[252:255]\n"
"ds_read_b128 v[238:241], v5 offset:38912\n"
"v_mfma_f32_16x16x32_bf16 v[144:147], v[108:111], v[156:159], v[144:147]\n"
"s_waitcnt lgkmcnt(3)\n"
"v_cvt_pk_bf16_f32 v244, v244, v245\n"
"v_mfma_f32_16x16x32_bf16 v[152:155], v[136:139], v[156:159], v[152:155]\n"
"s_waitcnt lgkmcnt(2)\n"
"ds_read_b128 v[156:159], v132 offset:4080\n"
"v_mfma_f32_16x16x32_bf16 v[140:143], v[220:223], v[148:151], v[140:143]\n"
"v_exp_f32 v246, v246\n"
"v_exp_f32 v247, v247\n"
"v_mfma_f32_16x16x32_bf16 v[140:143], v[222:225], v[24:27], v[140:143]\n"
"s_waitcnt lgkmcnt(1)\n"
"ds_read_b128 v[136:139], v6 offset:7168\n"
"v_cvt_pk_bf16_f32 v245, v246, v247\n"
"v_mfma_f32_16x16x32_bf16 v[252:255], v[112:115], v[238:241], v[252:255]\n"
"ds_write_addtid_b32 v244 offset:34304\n"
"ds_write_addtid_b32 v245 offset:34560\n"
"v_min_u32 v176, s71, v177\n"
"v_mfma_f32_16x16x32_bf16 v[144:147], v[116:119], v[238:241], v[144:147]\n"
"s_waitcnt lgkmcnt(2)\n"
"v_mfma_f32_16x16x32_bf16 v[152:155], v[136:139], v[238:241], v[152:155]\n"
"v_exp_f32 v250, v250\n"
"v_exp_f32 v251, v251\n"
"ds_read_b128 v[242:245], v4 offset:40960\n"
"s_add_u32 s72, s50, 4294964592\n"
"v_cvt_pk_bf16_f32 v249, v250, v251\n"
"ds_read_b128 v[238:241], v6 offset:8192\n"
"v_bfe_u32 v177, v6, 2, 6\n"
"s_waitcnt lgkmcnt(2)\n"
"s_barrier\n"
"ds_write_addtid_b32 v249 offset:59136\n"
"v_mfma_f32_16x16x32_bf16 v[156:159], v[226:229], v[148:151], v[156:159]\n"
"ds_read_b128 v[246:249], v176 offset:4096\n"
"v_mfma_f32_16x16x32_bf16 v[156:159], v[228:231], v[24:27], v[156:159]\n"
"s_waitcnt lgkmcnt(3)\n"
"ds_read_b128 v[132:135], v5 offset:40960\n"
"v_mfma_f32_16x16x32_bf16 v[252:255], v[0:3], v[242:245], v[252:255]\n"
"v_mfma_f32_16x16x32_bf16 v[144:147], v[120:123], v[242:245], v[144:147]\n"
"s_waitcnt lgkmcnt(3)\n"
"v_exp_f32 v142, v142\n"
"v_exp_f32 v143, v143\n"
"v_mfma_f32_16x16x32_bf16 v[152:155], v[238:241], v[242:245], v[152:155]\n"
"s_waitcnt lgkmcnt(1)\n"
"ds_read_b128 v[238:241], v6 offset:9216\n"
"v_mfma_f32_16x16x32_bf16 v[246:249], v[232:235], v[148:151], v[246:249]\n"
"ds_read_b32 v177, v177 offset:1728\n"
"v_cvt_pk_bf16_f32 v141, v142, v143\n"
"v_exp_f32 v156, v156\n"
"ds_read_b128 v[242:245], v4 offset:43008\n"
"v_mfma_f32_16x16x32_bf16 v[246:249], v[234:237], v[24:27], v[246:249]\n"
"s_waitcnt lgkmcnt(3)\n"
"v_exp_f32 v157, v157\n"
"v_exp_f32 v158, v158\n"
"v_mfma_f32_16x16x32_bf16 v[252:255], v[124:127], v[132:135], v[252:255]\n"
"v_cvt_pk_bf16_f32 v156, v156, v157\n"
"v_mfma_f32_16x16x32_bf16 v[144:147], v[128:131], v[132:135], v[144:147]\n"
"v_exp_f32 v159, v159\n"
"ds_write_addtid_b32 v141 offset:57600\n"
"s_waitcnt lgkmcnt(3)\n"
"v_mfma_f32_16x16x32_bf16 v[152:155], v[238:241], v[132:135], v[152:155]\n"
"s_waitcnt lgkmcnt(2)\n"
"ds_read_b128 v[136:139], v5 offset:43008\n"
"v_add_u32_sdwa v176, v177, v7 dst_sel:DWORD dst_unused:UNUSED_PAD src0_sel:WORD_0 src1_sel:DWORD\n"
"v_mul_f32 v254, s60, v254\n"
"s_waitcnt lgkmcnt(2)\n"
"v_mfma_f32_16x16x32_bf16 v[238:241], v[8:11], v[242:245], 0\n"
"ds_write_addtid_b32 v156 offset:57856\n"
"v_cvt_pk_bf16_f32 v157, v158, v159\n"
"v_mul_f32 v253, s60, v253\n"
"v_mfma_f32_16x16x32_bf16 v[132:135], v[12:15], v[242:245], 0\n"
"v_mfma_f32_16x16x32_bf16 v[140:143], v[16:19], v[242:245], 0\n"
"ds_write_addtid_b32 v157 offset:58112\n"
"v_max_u32 v251, s72, v176\n"
"ds_read_b128 v[148:151], v251 offset:2704\n"
"v_mul_f32 v147, s60, v147\n"
"s_waitcnt lgkmcnt(3)\n"
"v_mfma_f32_16x16x32_bf16 v[238:241], v[20:23], v[136:139], v[238:241]\n"
"v_exp_f32 v246, v246\n"
"v_exp_f32 v247, v247\n"
"ds_read_b128 v[242:245], v4 offset:45056\n"
"v_mul_f32 v146, s60, v146\n"
"v_mfma_f32_16x16x32_bf16 v[132:135], v[28:31], v[136:139], v[132:135]\n"
"v_exp_f32 v248, v248\n"
"v_mul_f32 v252, s60, v252\n"
"v_mul_f32 v153, s60, v153\n"
"v_exp_f32 v249, v249\n"
"v_mfma_f32_16x16x32_bf16 v[140:143], v[32:35], v[136:139], v[140:143]\n"
"ds_read_b128 v[136:139], v5 offset:45056\n"
"v_mul_f32 v144, s60, v144\n"
"v_cvt_pk_bf16_f32 v252, v252, v253\n"
"v_cvt_pk_bf16_f32 v246, v246, v247\n"
"v_cvt_pk_bf16_f32 v247, v248, v249\n"
"v_mul_f32 v255, s60, v255\n"
"v_mul_f32 v154, s60, v154\n"
"v_mul_f32 v155, s60, v155\n"
"s_waitcnt lgkmcnt(1)\n"
"v_mfma_f32_16x16x32_bf16 v[238:241], v[36:39], v[242:245], v[238:241]\n"
"ds_write_addtid_b32 v246 offset:58368\n"
"v_mul_f32 v145, s60, v145\n"
"ds_write_addtid_b32 v247 offset:58624\n"
"v_cvt_pk_bf16_f32 v253, v254, v255\n"
"v_mfma_f32_16x16x32_bf16 v[132:135], v[40:43], v[242:245], v[132:135]\n"
"v_cvt_pk_bf16_f32 v254, v144, v145\n"
"v_cvt_pk_bf16_f32 v27, v154, v155\n"
"v_cvt_pk_bf16_f32 v255, v146, v147\n"
"v_mul_f32 v152, s60, v152\n"
"v_cvt_pk_bf16_f32 v26, v152, v153\n"
"v_mfma_f32_16x16x32_bf16 v[148:151], v[184:187], v[252:255], v[148:151]\n"
"ds_read_b128 v[246:249], v4 offset:47104\n"
"v_mfma_f32_16x16x32_bf16 v[148:151], v[186:189], v[24:27], v[148:151]\n"
"s_waitcnt lgkmcnt(1)\n"
"s_barrier\n"
"v_mfma_f32_16x16x32_bf16 v[140:143], v[44:47], v[242:245], v[140:143]\n"
"ds_read_b128 v[144:147], v251 offset:2720\n"
"v_add_u32_sdwa v177, sext(v177), v176 dst_sel:DWORD dst_unused:UNUSED_PAD src0_sel:WORD_1 src1_sel:DWORD\n"
"v_mfma_f32_16x16x32_bf16 v[238:241], v[48:51], v[136:139], v[238:241]\n"
"v_mfma_f32_16x16x32_bf16 v[132:135], v[52:55], v[136:139], v[132:135]\n"
"v_mfma_f32_16x16x32_bf16 v[140:143], v[56:59], v[136:139], v[140:143]\n"
"s_waitcnt lgkmcnt(1)\n"
"v_mfma_f32_16x16x32_bf16 v[238:241], v[60:63], v[246:249], v[238:241]\n"
"ds_read_b128 v[242:245], v5 offset:47104\n"
"v_mfma_f32_16x16x32_bf16 v[132:135], v[64:67], v[246:249], v[132:135]\n"
"s_waitcnt lgkmcnt(1)\n"
"ds_read_b128 v[136:139], v4 offset:49152\n"
"v_mfma_f32_16x16x32_bf16 v[144:147], v[190:193], v[252:255], v[144:147]\n"
"v_mfma_f32_16x16x32_bf16 v[144:147], v[192:195], v[24:27], v[144:147]\n"
"v_exp_f32 v150, v150\n"
"v_exp_f32 v151, v151\n"
"v_mfma_f32_16x16x32_bf16 v[140:143], v[68:71], v[246:249], v[140:143]\n"
"s_waitcnt lgkmcnt(1)\n"
"v_cvt_pk_bf16_f32 v149, v150, v151\n"
"ds_read_b128 v[152:155], v251 offset:2736\n"
"v_mfma_f32_16x16x32_bf16 v[238:241], v[72:75], v[242:245], v[238:241]\n"
"s_nop 0\n"
"v_exp_f32 v144, v144\n"
"ds_write_addtid_b32 v149 offset:53504\n"
"v_mfma_f32_16x16x32_bf16 v[132:135], v[76:79], v[242:245], v[132:135]\n"
"v_exp_f32 v145, v145\n"
"v_mfma_f32_16x16x32_bf16 v[140:143], v[80:83], v[242:245], v[140:143]\n"
"s_waitcnt lgkmcnt(2)\n"
"ds_read_b128 v[248:251], v5 offset:49152\n"
"v_mfma_f32_16x16x32_bf16 v[244:247], v[8:11], v[136:139], 0\n"
"v_cvt_pk_bf16_f32 v238, v238, v239\n"
"v_cvt_pk_bf16_f32 v239, v240, v241\n"
"s_nop 0\n"
"v_cvt_pk_bf16_f32 v240, v132, v133\n"
"v_cvt_pk_bf16_f32 v241, v134, v135\n"
"v_mfma_f32_16x16x32_bf16 v[148:151], v[12:15], v[136:139], 0\n"
"s_waitcnt lgkmcnt(2)\n"
"ds_read_b128 v[132:135], v4 offset:51200\n"
"v_mfma_f32_16x16x32_bf16 v[152:155], v[196:199], v[252:255], v[152:155]\n"
"v_cvt_pk_bf16_f32 v243, v142, v143\n"
"v_cvt_pk_bf16_f32 v242, v140, v141\n"
"v_mfma_f32_16x16x32_bf16 v[152:155], v[198:201], v[24:27], v[152:155]\n"
"v_exp_f32 v146, v146\n"
"v_mfma_f32_16x16x32_bf16 v[140:143], v[16:19], v[136:139], 0\n"
"s_waitcnt lgkmcnt(1)\n"
"v_max_u32 v175, s72, v177\n"
"v_mfma_f32_16x16x32_bf16 v[244:247], v[20:23], v[248:251], v[244:247]\n"
"v_cvt_pk_bf16_f32 v144, v144, v145\n"
"v_mfma_f32_16x16x32_bf16 v[148:151], v[28:31], v[248:251], v[148:151]\n"
"v_exp_f32 v147, v147\n"
"v_exp_f32 v152, v152\n"
"v_cvt_pk_bf16_f32 v145, v146, v147\n"
"v_mfma_f32_16x16x32_bf16 v[140:143], v[32:35], v[248:251], v[140:143]\n"
"s_waitcnt lgkmcnt(0)\n"
"ds_read_b128 v[136:139], v175 offset:2752\n"
"v_mfma_f32_16x16x32_bf16 v[244:247], v[36:39], v[132:135], v[244:247]\n"
"s_add_u32 s73, s50, 4294965632\n"
"ds_write_addtid_b32 v144 offset:53760\n"
"ds_write_addtid_b32 v145 offset:54016\n"
"v_mfma_f32_16x16x32_bf16 v[148:151], v[40:43], v[132:135], v[148:151]\n"
"s_waitcnt lgkmcnt(2)\n"
"ds_read_b128 v[144:147], v4 offset:53248\n"
"ds_read_b128 v[248:251], v5 offset:51200\n"
"v_mfma_f32_16x16x32_bf16 v[136:139], v[202:205], v[252:255], v[136:139]\n"
"v_exp_f32 v153, v153\n"
"v_exp_f32 v154, v154\n"
"v_mfma_f32_16x16x32_bf16 v[136:139], v[204:207], v[24:27], v[136:139]\n"
"v_cvt_pk_bf16_f32 v152, v152, v153\n"
"v_mfma_f32_16x16x32_bf16 v[140:143], v[44:47], v[132:135], v[140:143]\n"
"s_waitcnt lgkmcnt(0)\n"
"v_min_u32 v175, s73, v176\n"
"v_mfma_f32_16x16x32_bf16 v[244:247], v[48:51], v[248:251], v[244:247]\n"
"ds_read_b128 v[156:159], v175 offset:4048\n"
"v_mfma_f32_16x16x32_bf16 v[148:151], v[52:55], v[248:251], v[148:151]\n"
"ds_write_addtid_b32 v152 offset:54272\n"
"v_mfma_f32_16x16x32_bf16 v[140:143], v[56:59], v[248:251], v[140:143]\n"
"v_exp_f32 v155, v155\n"
"v_mfma_f32_16x16x32_bf16 v[244:247], v[60:63], v[144:147], v[244:247]\n"
"s_waitcnt lgkmcnt(1)\n"
"ds_read_b128 v[248:251], v5 offset:53248\n"
"v_cvt_pk_bf16_f32 v153, v154, v155\n"
"v_mfma_f32_16x16x32_bf16 v[156:159], v[214:217], v[252:255], v[156:159]\n"
"v_exp_f32 v136, v136\n"
"v_mfma_f32_16x16x32_bf16 v[156:159], v[216:219], v[24:27], v[156:159]\n"
"ds_write_addtid_b32 v153 offset:54528\n"
"ds_read_b128 v[132:135], v4 offset:55296\n"
"v_mfma_f32_16x16x32_bf16 v[148:151], v[64:67], v[144:147], v[148:151]\n"
"v_exp_f32 v137, v137\n"
"v_mfma_f32_16x16x32_bf16 v[140:143], v[68:71], v[144:147], v[140:143]\n"
"s_waitcnt lgkmcnt(2)\n"
"ds_read_b128 v[152:155], v175 offset:4064\n"
"ds_read_b128 v[144:147], v5 offset:55296\n"
"v_cvt_pk_bf16_f32 v136, v136, v137\n"
"v_mfma_f32_16x16x32_bf16 v[244:247], v[72:75], v[248:251], v[244:247]\n"
"v_exp_f32 v138, v138\n"
"v_exp_f32 v139, v139\n"
"v_mfma_f32_16x16x32_bf16 v[148:151], v[76:79], v[248:251], v[148:151]\n"
"ds_write_addtid_b32 v136 offset:54784\n"
"v_cvt_pk_bf16_f32 v137, v138, v139\n"
"ds_write_addtid_b32 v137 offset:55040\n"
"v_mfma_f32_16x16x32_bf16 v[140:143], v[80:83], v[248:251], v[140:143]\n"
"v_exp_f32 v158, v158\n"
"v_cvt_pk_bf16_f32 v244, v244, v245\n"
"v_cvt_pk_bf16_f32 v245, v246, v247\n"
"ds_read_b128 v[160:163], v6 offset:5120\n"
"s_waitcnt lgkmcnt(5)\n"
"v_mfma_f32_16x16x32_bf16 v[136:139], v[84:87], v[132:135], 0\n"
"s_waitcnt lgkmcnt(1)\n"
"s_barrier\n"
"v_mfma_f32_16x16x32_bf16 v[152:155], v[220:223], v[252:255], v[152:155]\n"
"v_cvt_pk_bf16_f32 v247, v150, v151\n"
"v_mfma_f32_16x16x32_bf16 v[152:155], v[222:225], v[24:27], v[152:155]\n"
"v_cvt_pk_bf16_f32 v246, v148, v149\n"
"v_cvt_pk_bf16_f32 v248, v140, v141\n"
"ds_read_b128 v[164:167], v175 offset:4080\n"
"v_mfma_f32_16x16x32_bf16 v[148:151], v[88:91], v[132:135], 0\n"
"v_mfma_f32_16x16x32_bf16 v[168:171], v[92:95], v[132:135], 0\n"
"ds_read_b128 v[132:135], v6 offset:6144\n"
"v_mfma_f32_16x16x32_bf16 v[136:139], v[96:99], v[144:147], v[136:139]\n"
"v_mfma_f32_16x16x32_bf16 v[148:151], v[100:103], v[144:147], v[148:151]\n"
"s_waitcnt lgkmcnt(2)\n"
"v_cvt_pk_bf16_f32 v249, v142, v143\n"
"ds_read_b128 v[140:143], v4 offset:57344\n"
"v_mfma_f32_16x16x32_bf16 v[168:171], v[160:163], v[144:147], v[168:171]\n"
"ds_read_b128 v[144:147], v5 offset:57344\n"
"v_min_u32 v176, s73, v177\n"
"v_exp_f32 v159, v159\n"
"v_exp_f32 v152, v152\n"
"v_exp_f32 v153, v153\n"
"v_cvt_pk_bf16_f32 v157, v158, v159\n"
"ds_write_addtid_b32 v157 offset:57600\n"
"s_waitcnt lgkmcnt(2)\n"
"v_mfma_f32_16x16x32_bf16 v[136:139], v[104:107], v[140:143], v[136:139]\n"
"ds_read_b128 v[160:163], v4 offset:59392\n"
"v_mfma_f32_16x16x32_bf16 v[164:167], v[226:229], v[252:255], v[164:167]\n"
"v_mfma_f32_16x16x32_bf16 v[164:167], v[228:231], v[24:27], v[164:167]\n"
"ds_read_b128 v[156:159], v6 offset:7168\n"
"v_mfma_f32_16x16x32_bf16 v[148:151], v[108:111], v[140:143], v[148:151]\n"
"ds_read_b128 v[172:175], v176 offset:4096\n"
"v_mfma_f32_16x16x32_bf16 v[168:171], v[132:135], v[140:143], v[168:171]\n"
"s_waitcnt lgkmcnt(4)\n"
"v_exp_f32 v154, v154\n"
"v_exp_f32 v155, v155\n"
"v_mfma_f32_16x16x32_bf16 v[136:139], v[112:115], v[144:147], v[136:139]\n"
"ds_read_b128 v[132:135], v6 offset:8192\n"
"v_mfma_f32_16x16x32_bf16 v[148:151], v[116:119], v[144:147], v[148:151]\n"
"s_waitcnt lgkmcnt(2)\n"
"v_mfma_f32_16x16x32_bf16 v[168:171], v[156:159], v[144:147], v[168:171]\n"
"s_add_u32 s68, s50, 4294964608\n"
"v_exp_f32 v164, v164\n"
"ds_read_b128 v[140:143], v6 offset:9216\n"
"v_mfma_f32_16x16x32_bf16 v[136:139], v[0:3], v[160:163], v[136:139]\n"
"s_waitcnt lgkmcnt(2)\n"
"v_mfma_f32_16x16x32_bf16 v[172:175], v[232:235], v[252:255], v[172:175]\n"
"ds_read_b128 v[250:253], v5 offset:59392\n"
"v_cvt_pk_bf16_f32 v152, v152, v153\n"
"ds_read_b128 v[144:147], v4 offset:24576\n"
"v_mfma_f32_16x16x32_bf16 v[172:175], v[234:237], v[24:27], v[172:175]\n"
"v_exp_f32 v165, v165\n"
"v_cvt_pk_bf16_f32 v153, v154, v155\n"
"ds_write_addtid_b32 v152 offset:57856\n"
"v_mfma_f32_16x16x32_bf16 v[148:151], v[120:123], v[160:163], v[148:151]\n"
"ds_write_addtid_b32 v153 offset:58112\n"
"v_cvt_pk_bf16_f32 v164, v164, v165\n"
"s_waitcnt lgkmcnt(5)\n"
"v_mfma_f32_16x16x32_bf16 v[168:171], v[132:135], v[160:163], v[168:171]\n"
"s_waitcnt lgkmcnt(3)\n"
"v_bfe_u32 v177, v6, 2, 6\n"
"v_mfma_f32_16x16x32_bf16 v[136:139], v[124:127], v[250:253], v[136:139]\n"
"ds_write_addtid_b32 v164 offset:58368\n"
"v_mfma_f32_16x16x32_bf16 v[148:151], v[128:131], v[250:253], v[148:151]\n"
"v_exp_f32 v166, v166\n"
"v_exp_f32 v167, v167\n"
"v_mfma_f32_16x16x32_bf16 v[168:171], v[140:143], v[250:253], v[168:171]\n"
"s_waitcnt lgkmcnt(3)\n"
"v_mfma_f32_16x16x32_bf16 v[250:253], v[8:11], v[144:147], 0\n"
"ds_read_b128 v[132:135], v5 offset:24576\n"
"v_mul_f32 v139, s60, v139\n"
"v_mul_f32 v136, s60, v136\n"
"ds_read_b32 v177, v177 offset:1792\n"
"v_mfma_f32_16x16x32_bf16 v[140:143], v[12:15], v[144:147], 0\n"
"v_exp_f32 v172, v172\n"
"v_cvt_pk_bf16_f32 v165, v166, v167\n"
"v_mfma_f32_16x16x32_bf16 v[152:155], v[16:19], v[144:147], 0\n"
"v_exp_f32 v173, v173\n"
"v_exp_f32 v174, v174\n"
"v_exp_f32 v175, v175\n"
"v_mul_f32 v137, s60, v137\n"
"v_cvt_pk_bf16_f32 v136, v136, v137\n"
"ds_write_addtid_b32 v165 offset:58624\n"
"v_mul_f32 v170, s60, v170\n"
"v_mul_f32 v151, s60, v151\n"
"ds_read_b128 v[156:159], v5 offset:26624\n"
"v_mul_f32 v168, s60, v168\n"
"s_waitcnt lgkmcnt(2)\n"
"v_add_u32_sdwa v255, v177, v7 dst_sel:DWORD dst_unused:UNUSED_PAD src0_sel:WORD_0 src1_sel:DWORD\n"
"ds_read_b128 v[144:147], v4 offset:26624\n"
"v_cvt_pk_bf16_f32 v172, v172, v173\n"
"v_cvt_pk_bf16_f32 v173, v174, v175\n"
"v_mfma_f32_16x16x32_bf16 v[250:253], v[20:23], v[132:135], v[250:253]\n"
"ds_write_addtid_b32 v172 offset:58880\n"
"v_max_u32 v177, s68, v255\n"
"ds_write_addtid_b32 v173 offset:59136\n"
"v_mul_f32 v138, s60, v138\n"
"v_mfma_f32_16x16x32_bf16 v[140:143], v[28:31], v[132:135], v[140:143]\n"
"v_mfma_f32_16x16x32_bf16 v[152:155], v[32:35], v[132:135], v[152:155]\n"
"s_waitcnt lgkmcnt(2)\n"
"v_mfma_f32_16x16x32_bf16 v[250:253], v[36:39], v[144:147], v[250:253]\n"
"v_mul_f32 v150, s60, v150\n"
"ds_read_b128 v[132:135], v177 offset:2688\n"
"v_mul_f32 v148, s60, v148\n"
"v_mfma_f32_16x16x32_bf16 v[140:143], v[40:43], v[144:147], v[140:143]\n"
"s_waitcnt lgkmcnt(1)\n"
"v_cvt_pk_bf16_f32 v137, v138, v139\n"
"s_barrier\n"
"v_cvt_pk_bf16_f32 v139, v150, v151\n"
"v_mul_f32 v171, s60, v171\n"
"v_mul_f32 v149, s60, v149\n"
"v_cvt_pk_bf16_f32 v138, v148, v149\n"
"v_cvt_pk_bf16_f32 v27, v170, v171\n"
"s_waitcnt lgkmcnt(0)\n"
"v_mfma_f32_16x16x32_bf16 v[132:135], v[178:181], v[136:139], v[132:135]\n"
"v_mul_f32 v169, s60, v169\n"
"v_cvt_pk_bf16_f32 v26, v168, v169\n"
"s_nop 1\n"
"v_mfma_f32_16x16x32_bf16 v[132:135], v[180:183], v[24:27], v[132:135]\n"
"s_add_u32 s69, s50, 4294963264\n"
"v_max_u32 v176, s69, v255\n"
"ds_read_b128 v[178:181], v176 offset:4032\n"
"v_mfma_f32_16x16x32_bf16 v[152:155], v[44:47], v[144:147], v[152:155]\n"
"ds_read_b128 v[144:147], v4 offset:28672\n"
"v_mfma_f32_16x16x32_bf16 v[250:253], v[48:51], v[156:159], v[250:253]\n"
"ds_read_b128 v[148:151], v177 offset:2704\n"
"v_mfma_f32_16x16x32_bf16 v[140:143], v[52:55], v[156:159], v[140:143]\n"
"v_mfma_f32_16x16x32_bf16 v[152:155], v[56:59], v[156:159], v[152:155]\n"
"s_waitcnt lgkmcnt(2)\n"
"v_mfma_f32_16x16x32_bf16 v[178:181], v[208:211], v[136:139], v[178:181]\n"
"v_exp_f32 v132, v132\n"
"v_mfma_f32_16x16x32_bf16 v[178:181], v[210:213], v[24:27], v[178:181]\n"
"s_waitcnt lgkmcnt(1)\n"
"ds_read_b128 v[208:211], v5 offset:28672\n"
"v_mfma_f32_16x16x32_bf16 v[250:253], v[60:63], v[144:147], v[250:253]\n"
"v_mfma_f32_16x16x32_bf16 v[140:143], v[64:67], v[144:147], v[140:143]\n"
"v_exp_f32 v133, v133\n"
"v_exp_f32 v134, v134\n"
"v_exp_f32 v135, v135\n"
"v_mfma_f32_16x16x32_bf16 v[152:155], v[68:71], v[144:147], v[152:155]\n"
"s_waitcnt lgkmcnt(1)\n"
"v_cvt_pk_bf16_f32 v132, v132, v133\n"
"v_cvt_pk_bf16_f32 v133, v134, v135\n"
"v_mfma_f32_16x16x32_bf16 v[148:151], v[184:187], v[136:139], v[148:151]\n"
"ds_read_b128 v[144:147], v4 offset:30720\n"
"ds_write_addtid_b32 v132 offset:55296\n"
"ds_write_addtid_b32 v133 offset:55552\n"
"v_mfma_f32_16x16x32_bf16 v[148:151], v[186:189], v[24:27], v[148:151]\n"
"s_waitcnt lgkmcnt(3)\n"
"ds_read_b128 v[132:135], v176 offset:4048\n"
"v_mfma_f32_16x16x32_bf16 v[250:253], v[72:75], v[208:211], v[250:253]\n"
"s_add_u32 s70, s50, 4294964304\n"
"v_min_u32 v213, s70, v255\n"
"v_mfma_f32_16x16x32_bf16 v[140:143], v[76:79], v[208:211], v[140:143]\n"
"v_exp_f32 v178, v178\n"
"v_exp_f32 v179, v179\n"
"v_mfma_f32_16x16x32_bf16 v[152:155], v[80:83], v[208:211], v[152:155]\n"
"v_exp_f32 v180, v180\n"
"v_cvt_pk_bf16_f32 v178, v178, v179\n"
"ds_write_addtid_b32 v178 offset:56320\n"
"v_cvt_pk_bf16_f32 v250, v250, v251\n"
"s_mov_b64 exec, s[52:53]\n"
"v_add_u32 v213, 1040, v213\n"
"s_mov_b64 exec, -1\n"
"v_exp_f32 v181, v181\n"
"v_cvt_pk_bf16_f32 v254, v152, v153\n"
"v_cvt_pk_bf16_f32 v255, v154, v155\n"
"v_cvt_pk_bf16_f32 v179, v180, v181\n"
"s_waitcnt lgkmcnt(1)\n"
"v_mfma_f32_16x16x32_bf16 v[132:135], v[214:217], v[136:139], v[132:135]\n"
"ds_write_addtid_b32 v179 offset:56576\n"
"v_cvt_pk_bf16_f32 v251, v252, v253\n"
"v_cvt_pk_bf16_f32 v252, v140, v141\n"
"v_cvt_pk_bf16_f32 v253, v142, v143\n"
"v_mfma_f32_16x16x32_bf16 v[132:135], v[216:219], v[24:27], v[132:135]\n"
"ds_read_b128 v[178:181], v213 offset:5376\n"
"ds_read_b128 v[140:143], v5 offset:30720\n"
"v_mfma_f32_16x16x32_bf16 v[208:211], v[84:87], v[144:147], 0\n"
"v_exp_f32 v148, v148\n"
"v_mfma_f32_16x16x32_bf16 v[152:155], v[88:91], v[144:147], 0\n"
"v_exp_f32 v149, v149\n"
"s_nop 0\n"
"v_cvt_pk_bf16_f32 v148, v148, v149\n"
"v_mfma_f32_16x16x32_bf16 v[156:159], v[92:95], v[144:147], 0\n"
"v_exp_f32 v150, v150\n"
"ds_read_b128 v[144:147], v6 offset:5120\n"
"ds_write_addtid_b32 v148 offset:55808\n"
"s_waitcnt lgkmcnt(3)\n"
"v_mfma_f32_16x16x32_bf16 v[178:181], v[238:241], v[136:139], v[178:181]\n"
"v_mfma_f32_16x16x32_bf16 v[178:181], v[240:243], v[24:27], v[178:181]\n"
"v_exp_f32 v151, v151\n"
"s_nop 0\n"
"v_cvt_pk_bf16_f32 v149, v150, v151\n"
"v_exp_f32 v132, v132\n"
"s_waitcnt lgkmcnt(2)\n"
"v_mfma_f32_16x16x32_bf16 v[208:211], v[96:99], v[140:143], v[208:211]\n"
"ds_read_b128 v[238:241], v4 offset:32768\n"
"ds_read_b128 v[160:163], v177 offset:2720\n"
"ds_write_addtid_b32 v149 offset:56064\n"
"v_mfma_f32_16x16x32_bf16 v[152:155], v[100:103], v[140:143], v[152:155]\n"
"s_waitcnt lgkmcnt(4)\n"
"v_exp_f32 v133, v133\n"
"v_mfma_f32_16x16x32_bf16 v[156:159], v[144:147], v[140:143], v[156:159]\n"
"s_waitcnt lgkmcnt(1)\n"
"ds_read_b128 v[144:147], v176 offset:4064\n"
"v_mfma_f32_16x16x32_bf16 v[160:163], v[190:193], v[136:139], v[160:163]\n"
"ds_read_b128 v[140:143], v6 offset:6144\n"
"v_mfma_f32_16x16x32_bf16 v[160:163], v[192:195], v[24:27], v[160:163]\n"
"v_exp_f32 v134, v134\n"
"v_exp_f32 v135, v135\n"
"v_cvt_pk_bf16_f32 v132, v132, v133\n"
"v_cvt_pk_bf16_f32 v133, v134, v135\n"
"v_mfma_f32_16x16x32_bf16 v[208:211], v[104:107], v[238:241], v[208:211]\n"
"ds_write_addtid_b32 v132 offset:56832\n"
"ds_write_addtid_b32 v133 offset:57088\n"
"v_mfma_f32_16x16x32_bf16 v[152:155], v[108:111], v[238:241], v[152:155]\n"
"s_waitcnt lgkmcnt(2)\n"
"v_mfma_f32_16x16x32_bf16 v[156:159], v[140:143], v[238:241], v[156:159]\n"
"v_exp_f32 v178, v178\n"
"v_exp_f32 v179, v179\n"
"ds_read_b128 v[238:241], v6 offset:7168\n"
"v_mfma_f32_16x16x32_bf16 v[144:147], v[220:223], v[136:139], v[144:147]\n"
"v_exp_f32 v180, v180\n"
"v_exp_f32 v181, v181\n"
"v_cvt_pk_bf16_f32 v178, v178, v179\n"
"v_cvt_pk_bf16_f32 v179, v180, v181\n"
"ds_read_b128 v[132:135], v5 offset:32768\n"
"s_waitcnt lgkmcnt(2)\n"
"s_barrier\n"
"v_mfma_f32_16x16x32_bf16 v[144:147], v[222:225], v[24:27], v[144:147]\n"
"s_waitcnt lgkmcnt(0)\n"
"ds_read_b128 v[140:143], v213 offset:5392\n"
"v_mfma_f32_16x16x32_bf16 v[208:211], v[112:115], v[132:135], v[208:211]\n"
"ds_write_addtid_b32 v178 offset:57344\n"
"ds_write_addtid_b32 v179 offset:57600\n"
"v_mfma_f32_16x16x32_bf16 v[152:155], v[116:119], v[132:135], v[152:155]\n"
"v_exp_f32 v160, v160\n"
"ds_read_b128 v[178:181], v4 offset:34816\n"
"v_mfma_f32_16x16x32_bf16 v[156:159], v[238:241], v[132:135], v[156:159]\n"
"s_waitcnt lgkmcnt(3)\n"
"v_exp_f32 v161, v161\n"
"v_mfma_f32_16x16x32_bf16 v[140:143], v[244:247], v[136:139], v[140:143]\n"
"ds_read_b128 v[238:241], v6 offset:8192\n"
"v_cvt_pk_bf16_f32 v160, v160, v161\n"
"ds_write_addtid_b32 v160 offset:58368\n"
"ds_read_b128 v[132:135], v213 offset:5408\n"
"v_mfma_f32_16x16x32_bf16 v[140:143], v[246:249], v[24:27], v[140:143]\n"
"s_waitcnt lgkmcnt(3)\n"
"v_mfma_f32_16x16x32_bf16 v[208:211], v[0:3], v[178:181], v[208:211]\n"
"ds_read_b128 v[148:151], v5 offset:34816\n"
"v_bfe_u32 v243, v6, 2, 6\n"
"v_mfma_f32_16x16x32_bf16 v[152:155], v[120:123], v[178:181], v[152:155]\n"
"s_waitcnt lgkmcnt(3)\n"
"v_exp_f32 v144, v144\n"
"v_exp_f32 v145, v145\n"
"v_mfma_f32_16x16x32_bf16 v[156:159], v[238:241], v[178:181], v[156:159]\n"
"s_waitcnt lgkmcnt(1)\n"
"ds_read_b128 v[178:181], v6 offset:9216\n"
"v_cvt_pk_bf16_f32 v144, v144, v145\n"
"v_mfma_f32_16x16x32_bf16 v[132:135], v[250:253], v[136:139], v[132:135]\n"
"v_exp_f32 v140, v140\n"
"ds_read_b128 v[238:241], v4 offset:12288\n"
"v_mfma_f32_16x16x32_bf16 v[132:135], v[252:255], v[24:27], v[132:135]\n"
"s_waitcnt lgkmcnt(2)\n"
"ds_read_b32 v243, v243 offset:1856\n"
"v_mfma_f32_16x16x32_bf16 v[208:211], v[124:127], v[148:151], v[208:211]\n"
"v_exp_f32 v141, v141\n"
"v_mfma_f32_16x16x32_bf16 v[152:155], v[128:131], v[148:151], v[152:155]\n"
"s_waitcnt lgkmcnt(2)\n"
"ds_read_b128 v[136:139], v5 offset:12288\n"
"v_cvt_pk_bf16_f32 v140, v140, v141\n"
"v_exp_f32 v142, v142\n"
"v_mfma_f32_16x16x32_bf16 v[156:159], v[178:181], v[148:151], v[156:159]\n"
"ds_write_addtid_b32 v144 offset:58880\n"
"s_waitcnt lgkmcnt(3)\n"
"v_mfma_f32_16x16x32_bf16 v[178:181], v[8:11], v[238:241], 0\n"
"v_exp_f32 v143, v143\n"
"ds_write_addtid_b32 v140 offset:57856\n"
"v_cvt_pk_bf16_f32 v141, v142, v143\n"
"v_mfma_f32_16x16x32_bf16 v[144:147], v[12:15], v[238:241], 0\n"
"ds_write_addtid_b32 v141 offset:58112\n"
"v_exp_f32 v132, v132\n"
"v_mfma_f32_16x16x32_bf16 v[140:143], v[16:19], v[238:241], 0\n"
"s_waitcnt lgkmcnt(3)\n"
"ds_read_b128 v[238:241], v4 offset:14336\n"
"v_mfma_f32_16x16x32_bf16 v[178:181], v[20:23], v[136:139], v[178:181]\n"
"v_add_u32_sdwa v213, v243, v7 dst_sel:DWORD dst_unused:UNUSED_PAD src0_sel:WORD_0 src1_sel:DWORD\n"
"v_mul_f32 v208, s60, v208\n"
"v_mul_f32 v209, s60, v209\n"
"v_cvt_pk_bf16_f32 v208, v208, v209\n"
"v_mfma_f32_16x16x32_bf16 v[144:147], v[28:31], v[136:139], v[144:147]\n"
"v_exp_f32 v133, v133\n"
"s_add_u32 s71, s50, 4294964592\n"
"v_max_u32 v212, s71, v213\n"
"v_mul_f32 v159, s60, v159\n"
"v_mfma_f32_16x16x32_bf16 v[140:143], v[32:35], v[136:139], v[140:143]\n"
"ds_read_b128 v[148:151], v212 offset:2720\n"
"v_mul_f32 v211, s60, v211\n"
"v_cvt_pk_bf16_f32 v132, v132, v133\n"
"v_mul_f32 v210, s60, v210\n"
"v_mul_f32 v154, s60, v154\n"
"v_mul_f32 v155, s60, v155\n"
"v_mul_f32 v158, s60, v158\n"
"v_cvt_pk_bf16_f32 v27, v158, v159\n"
"v_cvt_pk_bf16_f32 v209, v210, v211\n"
"ds_write_addtid_b32 v132 offset:34304\n"
"s_waitcnt lgkmcnt(2)\n"
"v_mfma_f32_16x16x32_bf16 v[178:181], v[36:39], v[238:241], v[178:181]\n"
"v_mul_f32 v157, s60, v157\n"
"v_cvt_pk_bf16_f32 v211, v154, v155\n"
"s_add_u32 s72, s50, 4294965632\n"
"ds_read_b128 v[132:135], v5 offset:14336\n"
"v_mul_f32 v152, s60, v152\n"
"v_mul_f32 v156, s60, v156\n"
"v_mul_f32 v153, s60, v153\n"
"v_mfma_f32_16x16x32_bf16 v[144:147], v[40:43], v[238:241], v[144:147]\n"
"s_waitcnt lgkmcnt(1)\n"
"v_cvt_pk_bf16_f32 v210, v152, v153\n"
"s_barrier\n"
"v_min_u32 v177, s72, v213\n"
"v_cvt_pk_bf16_f32 v26, v156, v157\n"
"v_mfma_f32_16x16x32_bf16 v[148:151], v[190:193], v[208:211], v[148:151]\n"
"s_nop 0\n"
"v_mfma_f32_16x16x32_bf16 v[148:151], v[192:195], v[24:27], v[148:151]\n"
"ds_read_b128 v[136:139], v212 offset:2736\n"
"v_mfma_f32_16x16x32_bf16 v[140:143], v[44:47], v[238:241], v[140:143]\n"
"s_waitcnt lgkmcnt(1)\n"
"ds_read_b128 v[238:241], v4 offset:16384\n"
"v_mfma_f32_16x16x32_bf16 v[178:181], v[48:51], v[132:135], v[178:181]\n"
"v_mfma_f32_16x16x32_bf16 v[144:147], v[52:55], v[132:135], v[144:147]\n"
"v_mfma_f32_16x16x32_bf16 v[140:143], v[56:59], v[132:135], v[140:143]\n"
"s_nop 0\n"
"v_exp_f32 v148, v148\n"
"s_waitcnt lgkmcnt(1)\n"
"v_mfma_f32_16x16x32_bf16 v[136:139], v[196:199], v[208:211], v[136:139]\n"
"v_exp_f32 v149, v149\n"
"ds_read_b128 v[132:135], v177 offset:4048\n"
"v_cvt_pk_bf16_f32 v148, v148, v149\n"
"v_mfma_f32_16x16x32_bf16 v[136:139], v[198:201], v[24:27], v[136:139]\n"
"s_waitcnt lgkmcnt(1)\n"
"v_mfma_f32_16x16x32_bf16 v[178:181], v[60:63], v[238:241], v[178:181]\n"
"v_exp_f32 v150, v150\n"
"v_mfma_f32_16x16x32_bf16 v[144:147], v[64:67], v[238:241], v[144:147]\n"
"ds_write_addtid_b32 v148 offset:53248\n"
"ds_read_b128 v[152:155], v5 offset:16384\n"
"v_mfma_f32_16x16x32_bf16 v[140:143], v[68:71], v[238:241], v[140:143]\n"
"s_waitcnt lgkmcnt(2)\n"
"v_exp_f32 v151, v151\n"
"v_mfma_f32_16x16x32_bf16 v[132:135], v[214:217], v[208:211], v[132:135]\n"
"ds_read_b128 v[238:241], v177 offset:4064\n"
"v_cvt_pk_bf16_f32 v149, v150, v151\n"
"v_mfma_f32_16x16x32_bf16 v[132:135], v[216:219], v[24:27], v[132:135]\n"
"s_waitcnt lgkmcnt(1)\n"
"v_mfma_f32_16x16x32_bf16 v[178:181], v[72:75], v[152:155], v[178:181]\n"
"v_exp_f32 v136, v136\n"
"v_exp_f32 v137, v137\n"
"v_mfma_f32_16x16x32_bf16 v[144:147], v[76:79], v[152:155], v[144:147]\n"
"ds_write_addtid_b32 v149 offset:53504\n"
"v_cvt_pk_bf16_f32 v136, v136, v137\n"
"v_exp_f32 v138, v138\n"
"v_exp_f32 v139, v139\n"
"v_mfma_f32_16x16x32_bf16 v[140:143], v[80:83], v[152:155], v[140:143]\n"
"ds_write_addtid_b32 v136 offset:53760\n"
"ds_read_b128 v[148:151], v4 offset:18432\n"
"v_cvt_pk_bf16_f32 v137, v138, v139\n"
"ds_write_addtid_b32 v137 offset:54016\n"
"v_cvt_pk_bf16_f32 v178, v178, v179\n"
"s_nop 2\n"
"v_cvt_pk_bf16_f32 v183, v142, v143\n"
"ds_read_b128 v[136:139], v177 offset:4080\n"
"v_cvt_pk_bf16_f32 v182, v140, v141\n"
"s_waitcnt lgkmcnt(5)\n"
"v_mfma_f32_16x16x32_bf16 v[238:241], v[220:223], v[208:211], v[238:241]\n"
"v_cvt_pk_bf16_f32 v179, v180, v181\n"
"v_cvt_pk_bf16_f32 v180, v144, v145\n"
"v_cvt_pk_bf16_f32 v181, v146, v147\n"
"v_mfma_f32_16x16x32_bf16 v[238:241], v[222:225], v[24:27], v[238:241]\n"
"s_waitcnt lgkmcnt(2)\n"
"v_exp_f32 v132, v132\n"
"v_mfma_f32_16x16x32_bf16 v[140:143], v[84:87], v[148:151], 0\n"
"v_exp_f32 v133, v133\n"
"v_mfma_f32_16x16x32_bf16 v[152:155], v[88:91], v[148:151], 0\n"
"s_add_u32 s73, s50, 4294964288\n"
"ds_read_b128 v[144:147], v5 offset:18432\n"
"v_cvt_pk_bf16_f32 v132, v132, v133\n"
"v_mfma_f32_16x16x32_bf16 v[156:159], v[92:95], v[148:151], 0\n"
"s_waitcnt lgkmcnt(1)\n"
"v_exp_f32 v134, v134\n"
"v_exp_f32 v135, v135\n"
"v_mfma_f32_16x16x32_bf16 v[136:139], v[226:229], v[208:211], v[136:139]\n"
"ds_write_addtid_b32 v132 offset:54272\n"
"ds_read_b128 v[148:151], v6 offset:5120\n"
"ds_read_b128 v[160:163], v212 offset:2704\n"
"v_cvt_pk_bf16_f32 v133, v134, v135\n"
"v_mfma_f32_16x16x32_bf16 v[136:139], v[228:231], v[24:27], v[136:139]\n"
"s_waitcnt lgkmcnt(3)\n"
"v_exp_f32 v238, v238\n"
"v_mfma_f32_16x16x32_bf16 v[140:143], v[96:99], v[144:147], v[140:143]\n"
"ds_write_addtid_b32 v133 offset:54528\n"
"v_min_u32 v212, s73, v213\n"
"ds_read_b128 v[132:135], v4 offset:20480\n"
"v_mfma_f32_16x16x32_bf16 v[152:155], v[100:103], v[144:147], v[152:155]\n"
"s_waitcnt lgkmcnt(3)\n"
"v_exp_f32 v239, v239\n"
"v_exp_f32 v240, v240\n"
"v_mfma_f32_16x16x32_bf16 v[156:159], v[148:151], v[144:147], v[156:159]\n"
"s_mov_b64 exec, s[52:53]\n"
"v_add_u32 v212, 1040, v212\n"
"s_mov_b64 exec, -1\n"
"ds_read_b128 v[144:147], v6 offset:6144\n"
"s_waitcnt lgkmcnt(3)\n"
"v_mfma_f32_16x16x32_bf16 v[160:163], v[184:187], v[208:211], v[160:163]\n"
"v_exp_f32 v241, v241\n"
"ds_read_b128 v[148:151], v212 offset:5392\n"
"v_mfma_f32_16x16x32_bf16 v[160:163], v[186:189], v[24:27], v[160:163]\n"
"s_waitcnt lgkmcnt(2)\n"
"ds_read_b128 v[184:187], v5 offset:20480\n"
"v_cvt_pk_bf16_f32 v238, v238, v239\n"
"v_exp_f32 v136, v136\n"
"v_mfma_f32_16x16x32_bf16 v[140:143], v[104:107], v[132:135], v[140:143]\n"
"ds_write_addtid_b32 v238 offset:54784\n"
"v_mfma_f32_16x16x32_bf16 v[152:155], v[108:111], v[132:135], v[152:155]\n"
"s_waitcnt lgkmcnt(3)\n"
"v_exp_f32 v137, v137\n"
"v_mfma_f32_16x16x32_bf16 v[156:159], v[144:147], v[132:135], v[156:159]\n"
"s_waitcnt lgkmcnt(2)\n"
"v_exp_f32 v138, v138\n"
"v_cvt_pk_bf16_f32 v239, v240, v241\n"
"v_cvt_pk_bf16_f32 v136, v136, v137\n"
"v_mfma_f32_16x16x32_bf16 v[148:151], v[244:247], v[208:211], v[148:151]\n"
"ds_write_addtid_b32 v136 offset:33792\n"
"ds_write_addtid_b32 v239 offset:55040\n"
"v_mfma_f32_16x16x32_bf16 v[148:151], v[246:249], v[24:27], v[148:151]\n"
"v_exp_f32 v139, v139\n"
"v_exp_f32 v160, v160\n"
"ds_read_b128 v[238:241], v6 offset:7168\n"
"v_cvt_pk_bf16_f32 v137, v138, v139\n"
"ds_write_addtid_b32 v137 offset:34048\n"
"s_waitcnt lgkmcnt(5)\n"
"v_mfma_f32_16x16x32_bf16 v[140:143], v[112:115], v[184:187], v[140:143]\n"
"v_exp_f32 v161, v161\n"
"s_nop 0\n"
"v_cvt_pk_bf16_f32 v160, v160, v161\n"
"ds_read_b128 v[132:135], v212 offset:5408\n"
"v_mfma_f32_16x16x32_bf16 v[152:155], v[116:119], v[184:187], v[152:155]\n"
"s_waitcnt lgkmcnt(2)\n"
"v_exp_f32 v162, v162\n"
"v_mfma_f32_16x16x32_bf16 v[156:159], v[238:241], v[184:187], v[156:159]\n"
"s_waitcnt lgkmcnt(1)\n"
"s_barrier\n"
"ds_read_b128 v[184:187], v6 offset:8192\n"
"ds_read_b128 v[136:139], v4 offset:22528\n"
"s_waitcnt lgkmcnt(2)\n"
"v_mfma_f32_16x16x32_bf16 v[132:135], v[250:253], v[208:211], v[132:135]\n"
"v_exp_f32 v163, v163\n"
"ds_read_b128 v[238:241], v212 offset:5424\n"
"v_cvt_pk_bf16_f32 v161, v162, v163\n"
"v_exp_f32 v148, v148\n"
"v_exp_f32 v149, v149\n"
"v_mfma_f32_16x16x32_bf16 v[132:135], v[252:255], v[24:27], v[132:135]\n"
"ds_write_addtid_b32 v160 offset:57856\n"
"s_waitcnt lgkmcnt(2)\n"
"v_mfma_f32_16x16x32_bf16 v[140:143], v[0:3], v[136:139], v[140:143]\n"
"ds_read_b128 v[144:147], v5 offset:22528\n"
"v_cvt_pk_bf16_f32 v148, v148, v149\n"
"v_mfma_f32_16x16x32_bf16 v[152:155], v[120:123], v[136:139], v[152:155]\n"
"v_exp_f32 v150, v150\n"
"ds_write_addtid_b32 v161 offset:58112\n"
"v_exp_f32 v151, v151\n"
"v_mfma_f32_16x16x32_bf16 v[156:159], v[184:187], v[136:139], v[156:159]\n"
"s_waitcnt lgkmcnt(3)\n"
"ds_read_b128 v[184:187], v6 offset:9216\n"
"v_cvt_pk_bf16_f32 v149, v150, v151\n"
"v_mfma_f32_16x16x32_bf16 v[238:241], v[178:181], v[208:211], v[238:241]\n"
"v_exp_f32 v132, v132\n"
"ds_read_b128 v[136:139], v5 offset:36864\n"
"ds_read_b128 v[208:211], v4 offset:36864\n"
"v_exp_f32 v133, v133\n"
"v_mfma_f32_16x16x32_bf16 v[238:241], v[180:183], v[24:27], v[238:241]\n"
"ds_write_addtid_b32 v148 offset:58368\n"
"s_waitcnt lgkmcnt(5)\n"
"v_mfma_f32_16x16x32_bf16 v[140:143], v[124:127], v[144:147], v[140:143]\n"
"v_exp_f32 v134, v134\n"
"v_mfma_f32_16x16x32_bf16 v[152:155], v[128:131], v[144:147], v[152:155]\n"
"ds_write_addtid_b32 v149 offset:58624\n"
"v_bfe_u32 v213, v6, 2, 6\n"
"ds_read_b32 v213, v213 offset:1920\n"
"s_waitcnt lgkmcnt(5)\n"
"v_mfma_f32_16x16x32_bf16 v[156:159], v[184:187], v[144:147], v[156:159]\n"
"s_waitcnt lgkmcnt(3)\n"
"v_exp_f32 v135, v135\n"
"v_mfma_f32_16x16x32_bf16 v[184:187], v[8:11], v[208:211], 0\n"
"v_cvt_pk_bf16_f32 v132, v132, v133\n"
"v_cvt_pk_bf16_f32 v133, v134, v135\n"
"ds_write_addtid_b32 v132 offset:58880\n"
"s_add_u32 s68, s50, 4294964576\n"
"v_mfma_f32_16x16x32_bf16 v[8:11], v[12:15], v[208:211], 0\n"
"ds_write_addtid_b32 v133 offset:59136\n"
"v_mul_f32 v140, s60, v140\n"
"s_waitcnt lgkmcnt(2)\n"
"v_add_u32_sdwa v212, v213, v7 dst_sel:DWORD dst_unused:UNUSED_PAD src0_sel:WORD_0 src1_sel:DWORD\n"
"v_exp_f32 v238, v238\n"
"v_mfma_f32_16x16x32_bf16 v[12:15], v[16:19], v[208:211], 0\n"
"ds_read_b128 v[208:211], v4 offset:38912\n"
"v_max_u32 v177, s68, v212\n"
"v_mfma_f32_16x16x32_bf16 v[184:187], v[20:23], v[136:139], v[184:187]\n"
"v_exp_f32 v239, v239\n"
"ds_read_b128 v[16:19], v177 offset:2720\n"
"v_mul_f32 v141, s60, v141\n"
"v_mfma_f32_16x16x32_bf16 v[8:11], v[28:31], v[136:139], v[8:11]\n"
"v_exp_f32 v240, v240\n"
"v_exp_f32 v241, v241\n"
"v_mfma_f32_16x16x32_bf16 v[12:15], v[32:35], v[136:139], v[12:15]\n"
"s_waitcnt lgkmcnt(1)\n"
"v_cvt_pk_bf16_f32 v238, v238, v239\n"
"v_cvt_pk_bf16_f32 v239, v240, v241\n"
"v_mfma_f32_16x16x32_bf16 v[184:187], v[36:39], v[208:211], v[184:187]\n"
"ds_write_addtid_b32 v238 offset:57344\n"
"v_mul_f32 v143, s60, v143\n"
"v_mfma_f32_16x16x32_bf16 v[8:11], v[40:43], v[208:211], v[8:11]\n"
"ds_write_addtid_b32 v239 offset:57600\n"
"v_mul_f32 v157, s60, v157\n"
"ds_read_b128 v[238:241], v5 offset:38912\n"
"v_mul_f32 v155, s60, v155\n"
"v_mul_f32 v156, s60, v156\n"
"v_cvt_pk_bf16_f32 v26, v156, v157\n"
"v_mul_f32 v152, s60, v152\n"
"v_cvt_pk_bf16_f32 v140, v140, v141\n"
"v_mul_f32 v142, s60, v142\n"
"v_mul_f32 v153, s60, v153\n"
"v_cvt_pk_bf16_f32 v141, v142, v143\n"
"v_mul_f32 v154, s60, v154\n"
"v_cvt_pk_bf16_f32 v143, v154, v155\n"
"v_cvt_pk_bf16_f32 v142, v152, v153\n"
"s_waitcnt lgkmcnt(1)\n"
"s_barrier\n"
"v_mfma_f32_16x16x32_bf16 v[16:19], v[190:193], v[140:143], v[16:19]\n"
"s_add_u32 s69, s50, 4294963232\n"
"v_mul_f32 v159, s60, v159\n"
"v_mul_f32 v158, s60, v158\n"
"v_cvt_pk_bf16_f32 v27, v158, v159\n"
"v_add_u32_sdwa v213, sext(v213), v212 dst_sel:DWORD dst_unused:UNUSED_PAD src0_sel:WORD_1 src1_sel:DWORD\n"
"s_nop 0\n"
"v_mfma_f32_16x16x32_bf16 v[16:19], v[192:195], v[24:27], v[16:19]\n"
"ds_read_b128 v[188:191], v177 offset:2736\n"
"v_max_u32 v177, s68, v213\n"
"v_mfma_f32_16x16x32_bf16 v[12:15], v[44:47], v[208:211], v[12:15]\n"
"s_waitcnt lgkmcnt(1)\n"
"v_mfma_f32_16x16x32_bf16 v[184:187], v[48:51], v[238:241], v[184:187]\n"
"ds_read_b128 v[192:195], v4 offset:40960\n"
"v_mfma_f32_16x16x32_bf16 v[8:11], v[52:55], v[238:241], v[8:11]\n"
"v_mfma_f32_16x16x32_bf16 v[12:15], v[56:59], v[238:241], v[12:15]\n"
"s_waitcnt lgkmcnt(1)\n"
"v_mfma_f32_16x16x32_bf16 v[188:191], v[196:199], v[140:143], v[188:191]\n"
"s_add_u32 s70, s50, 4294964272\n"
"ds_read_b128 v[208:211], v177 offset:2752\n"
"v_exp_f32 v18, v18\n"
"v_mfma_f32_16x16x32_bf16 v[188:191], v[198:201], v[24:27], v[188:191]\n"
"s_waitcnt lgkmcnt(1)\n"
"ds_read_b128 v[196:199], v5 offset:40960\n"
"v_mfma_f32_16x16x32_bf16 v[184:187], v[60:63], v[192:195], v[184:187]\n"
"v_exp_f32 v19, v19\n"
"v_mfma_f32_16x16x32_bf16 v[8:11], v[64:67], v[192:195], v[8:11]\n"
"v_cvt_pk_bf16_f32 v17, v18, v19\n"
"ds_write_addtid_b32 v17 offset:55552\n"
"v_mfma_f32_16x16x32_bf16 v[12:15], v[68:71], v[192:195], v[12:15]\n"
"s_waitcnt lgkmcnt(2)\n"
"v_max_u32 v239, s69, v213\n"
"v_mfma_f32_16x16x32_bf16 v[208:211], v[202:205], v[140:143], v[208:211]\n"
"v_exp_f32 v188, v188\n"
"v_mfma_f32_16x16x32_bf16 v[208:211], v[204:207], v[24:27], v[208:211]\n"
"s_waitcnt lgkmcnt(1)\n"
"v_min_u32 v177, s70, v212\n"
"ds_read_b128 v[192:195], v239 offset:4096\n"
"v_mfma_f32_16x16x32_bf16 v[184:187], v[72:75], v[196:199], v[184:187]\n"
"v_exp_f32 v189, v189\n"
"v_exp_f32 v190, v190\n"
"v_mfma_f32_16x16x32_bf16 v[8:11], v[76:79], v[196:199], v[8:11]\n"
"ds_read_b128 v[200:203], v4 offset:43008\n"
"v_cvt_pk_bf16_f32 v188, v188, v189\n"
"ds_write_addtid_b32 v188 offset:55808\n"
"v_exp_f32 v191, v191\n"
"v_mfma_f32_16x16x32_bf16 v[12:15], v[80:83], v[196:199], v[12:15]\n"
"v_cvt_pk_bf16_f32 v184, v184, v185\n"
"v_cvt_pk_bf16_f32 v185, v186, v187\n"
"v_cvt_pk_bf16_f32 v189, v190, v191\n"
"ds_write_addtid_b32 v189 offset:56064\n"
"v_cvt_pk_bf16_f32 v187, v10, v11\n"
"v_min_u32 v176, s70, v213\n"
"s_waitcnt lgkmcnt(3)\n"
"v_mfma_f32_16x16x32_bf16 v[192:195], v[232:235], v[140:143], v[192:195]\n"
"s_mov_b64 exec, s[52:53]\n"
"v_add_u32 v177, 1040, v177\n"
"v_add_u32 v176, 1040, v176\n"
"s_mov_b64 exec, -1\n"
"ds_read_b128 v[196:199], v177 offset:5408\n"
"v_mfma_f32_16x16x32_bf16 v[192:195], v[234:237], v[24:27], v[192:195]\n"
"s_waitcnt lgkmcnt(3)\n"
"ds_read_b128 v[232:235], v5 offset:43008\n"
"v_cvt_pk_bf16_f32 v189, v14, v15\n"
"v_cvt_pk_bf16_f32 v188, v12, v13\n"
"v_mfma_f32_16x16x32_bf16 v[204:207], v[84:87], v[200:203], 0\n"
"v_exp_f32 v208, v208\n"
"v_mfma_f32_16x16x32_bf16 v[236:239], v[88:91], v[200:203], 0\n"
"v_exp_f32 v209, v209\n"
"v_mfma_f32_16x16x32_bf16 v[240:243], v[92:95], v[200:203], 0\n"
"s_waitcnt lgkmcnt(1)\n"
"v_exp_f32 v210, v210\n"
"v_exp_f32 v211, v211\n"
"v_cvt_pk_bf16_f32 v208, v208, v209\n"
"v_mfma_f32_16x16x32_bf16 v[196:199], v[250:253], v[140:143], v[196:199]\n"
"v_exp_f32 v192, v192\n"
"ds_read_b128 v[200:203], v6 offset:5120\n"
"v_cvt_pk_bf16_f32 v209, v210, v211\n"
"v_mfma_f32_16x16x32_bf16 v[196:199], v[252:255], v[24:27], v[196:199]\n"
"s_waitcnt lgkmcnt(1)\n"
"v_exp_f32 v193, v193\n"
"v_cvt_pk_bf16_f32 v186, v8, v9\n"
"v_mfma_f32_16x16x32_bf16 v[204:207], v[96:99], v[232:235], v[204:207]\n"
"v_cvt_pk_bf16_f32 v192, v192, v193\n"
"ds_write_addtid_b32 v208 offset:56320\n"
"ds_write_addtid_b32 v209 offset:56576\n"
"ds_write_addtid_b32 v192 offset:56832\n"
"v_mfma_f32_16x16x32_bf16 v[236:239], v[100:103], v[232:235], v[236:239]\n"
"v_exp_f32 v194, v194\n"
"v_exp_f32 v195, v195\n"
"s_nop 0\n"
"v_cvt_pk_bf16_f32 v193, v194, v195\n"
"v_max_u32 v213, s69, v212\n"
"ds_read_b128 v[8:11], v213 offset:4064\n"
"v_exp_f32 v198, v198\n"
"ds_read_b128 v[208:211], v4 offset:45056\n"
"s_waitcnt lgkmcnt(5)\n"
"v_mfma_f32_16x16x32_bf16 v[240:243], v[200:203], v[232:235], v[240:243]\n"
"ds_write_addtid_b32 v193 offset:57088\n"
"s_waitcnt lgkmcnt(2)\n"
"v_mfma_f32_16x16x32_bf16 v[8:11], v[220:223], v[140:143], v[8:11]\n"
"ds_read_b128 v[200:203], v6 offset:6144\n"
"v_mfma_f32_16x16x32_bf16 v[8:11], v[222:225], v[24:27], v[8:11]\n"
"s_waitcnt lgkmcnt(2)\n"
"ds_read_b128 v[232:235], v213 offset:4080\n"
"v_mfma_f32_16x16x32_bf16 v[204:207], v[104:107], v[208:211], v[204:207]\n"
"s_add_u32 s71, s50, 4294963248\n"
"ds_read_b128 v[190:193], v5 offset:45056\n"
"v_mfma_f32_16x16x32_bf16 v[236:239], v[108:111], v[208:211], v[236:239]\n"
"s_waitcnt lgkmcnt(2)\n"
"v_exp_f32 v199, v199\n"
"v_mfma_f32_16x16x32_bf16 v[240:243], v[200:203], v[208:211], v[240:243]\n"
"s_waitcnt lgkmcnt(1)\n"
"v_exp_f32 v10, v10\n"
"v_mfma_f32_16x16x32_bf16 v[232:235], v[226:229], v[140:143], v[232:235]\n"
"ds_read_b128 v[208:211], v177 offset:5424\n"
"v_mfma_f32_16x16x32_bf16 v[232:235], v[228:231], v[24:27], v[232:235]\n"
"v_cvt_pk_bf16_f32 v197, v198, v199\n"
"s_add_u32 s72, s50, 4294964288\n"
"ds_write_addtid_b32 v197 offset:34560\n"
"s_waitcnt lgkmcnt(2)\n"
"v_mfma_f32_16x16x32_bf16 v[204:207], v[112:115], v[190:193], v[204:207]\n"
"ds_read_b128 v[200:203], v6 offset:7168\n"
"ds_read_b128 v[194:197], v4 offset:47104\n"
"v_bfe_u32 v4, v6, 2, 6\n"
"v_mfma_f32_16x16x32_bf16 v[236:239], v[116:119], v[190:193], v[236:239]\n"
"s_waitcnt lgkmcnt(1)\n"
"v_mfma_f32_16x16x32_bf16 v[240:243], v[200:203], v[190:193], v[240:243]\n"
"v_exp_f32 v11, v11\n"
"v_exp_f32 v232, v232\n"
"v_mfma_f32_16x16x32_bf16 v[208:211], v[178:181], v[140:143], v[208:211]\n"
"ds_read_b128 v[190:193], v6 offset:8192\n"
"ds_read_b32 v4, v4 offset:1984\n"
"v_exp_f32 v233, v233\n"
"s_barrier\n"
"v_mfma_f32_16x16x32_bf16 v[208:211], v[180:183], v[24:27], v[208:211]\n"
"s_waitcnt lgkmcnt(0)\n"
"v_add_u32_sdwa v213, v4, v7 dst_sel:DWORD dst_unused:UNUSED_PAD src0_sel:WORD_0 src1_sel:DWORD\n"
"ds_read_b128 v[198:201], v176 offset:5440\n"
"v_cvt_pk_bf16_f32 v232, v232, v233\n"
"v_mfma_f32_16x16x32_bf16 v[204:207], v[0:3], v[194:197], v[204:207]\n"
"ds_read_b128 v[0:3], v5 offset:47104\n"
"v_mfma_f32_16x16x32_bf16 v[236:239], v[120:123], v[194:197], v[236:239]\n"
"v_exp_f32 v234, v234\n"
"v_mfma_f32_16x16x32_bf16 v[240:243], v[190:193], v[194:197], v[240:243]\n"
"ds_read_b128 v[190:193], v6 offset:9216\n"
"v_max_u32 v203, s71, v213\n"
"ds_write_addtid_b32 v232 offset:58880\n"
"v_cvt_pk_bf16_f32 v9, v10, v11\n"
"ds_write_addtid_b32 v9 offset:58624\n"
"v_exp_f32 v235, v235\n"
"v_exp_f32 v208, v208\n"
"v_cvt_pk_bf16_f32 v233, v234, v235\n"
"s_waitcnt lgkmcnt(4)\n"
"v_mfma_f32_16x16x32_bf16 v[198:201], v[184:187], v[140:143], v[198:201]\n"
"ds_write_addtid_b32 v233 offset:59136\n"
"v_exp_f32 v209, v209\n"
"v_mfma_f32_16x16x32_bf16 v[198:201], v[186:189], v[24:27], v[198:201]\n"
"s_waitcnt lgkmcnt(4)\n"
"ds_read_b128 v[184:187], v203 offset:4048\n"
"v_mfma_f32_16x16x32_bf16 v[204:207], v[124:127], v[0:3], v[204:207]\n"
"v_cvt_pk_bf16_f32 v208, v208, v209\n"
"v_min_u32 v177, s72, v213\n"
"s_mov_b64 exec, s[52:53]\n"
"v_add_u32 v177, 1040, v177\n"
"s_mov_b64 exec, -1\n"
"v_mfma_f32_16x16x32_bf16 v[236:239], v[128:131], v[0:3], v[236:239]\n"
"s_waitcnt lgkmcnt(4)\n"
"v_exp_f32 v210, v210\n"
"v_exp_f32 v211, v211\n"
"v_mul_f32 v205, s60, v205\n"
"v_mfma_f32_16x16x32_bf16 v[240:243], v[190:193], v[0:3], v[240:243]\n"
"ds_read_b128 v[194:197], v203 offset:4064\n"
"v_mul_f32 v207, s60, v207\n"
"v_mul_f32 v204, s60, v204\n"
"v_cvt_pk_bf16_f32 v204, v204, v205\n"
"ds_read_b128 v[188:191], v177 offset:5392\n"
"v_mul_f32 v236, s60, v236\n"
"v_mul_f32 v206, s60, v206\n"
"v_cvt_pk_bf16_f32 v205, v206, v207\n"
"v_mul_f32 v241, s60, v241\n"
"v_mul_f32 v242, s60, v242\n"
"v_mul_f32 v240, s60, v240\n"
"v_cvt_pk_bf16_f32 v26, v240, v241\n"
"v_mul_f32 v243, s60, v243\n"
"v_mul_f32 v237, s60, v237\n"
"v_cvt_pk_bf16_f32 v206, v236, v237\n"
"v_mul_f32 v239, s60, v239\n"
"v_cvt_pk_bf16_f32 v27, v242, v243\n"
"v_mul_f32 v238, s60, v238\n"
"v_cvt_pk_bf16_f32 v207, v238, v239\n"
"v_exp_f32 v198, v198\n"
"s_waitcnt lgkmcnt(2)\n"
"v_mfma_f32_16x16x32_bf16 v[184:187], v[214:217], v[204:207], v[184:187]\n"
"ds_write_addtid_b32 v208 offset:57344\n"
"v_cvt_pk_bf16_f32 v209, v210, v211\n"
"ds_write_addtid_b32 v209 offset:57600\n"
"v_exp_f32 v199, v199\n"
"ds_read_b128 v[212:215], v177 offset:5408\n"
"v_exp_f32 v200, v200\n"
"v_mfma_f32_16x16x32_bf16 v[184:187], v[216:219], v[24:27], v[184:187]\n"
"s_waitcnt lgkmcnt(4)\n"
"ds_read_b128 v[208:211], v203 offset:4080\n"
"v_cvt_pk_bf16_f32 v198, v198, v199\n"
"v_mfma_f32_16x16x32_bf16 v[194:197], v[220:223], v[204:207], v[194:197]\n"
"v_exp_f32 v201, v201\n"
"v_mfma_f32_16x16x32_bf16 v[194:197], v[222:225], v[24:27], v[194:197]\n"
"ds_write_addtid_b32 v198 offset:57856\n"
"v_cvt_pk_bf16_f32 v199, v200, v201\n"
"ds_write_addtid_b32 v199 offset:58112\n"
"s_waitcnt lgkmcnt(6)\n"
"v_mfma_f32_16x16x32_bf16 v[188:191], v[244:247], v[204:207], v[188:191]\n"
"v_exp_f32 v184, v184\n"
"v_exp_f32 v185, v185\n"
"ds_read_b128 v[198:201], v177 offset:5424\n"
"v_mfma_f32_16x16x32_bf16 v[188:191], v[246:249], v[24:27], v[188:191]\n"
"s_waitcnt lgkmcnt(1)\n"
"s_barrier\n"
"v_mfma_f32_16x16x32_bf16 v[208:211], v[226:229], v[204:207], v[208:211]\n"
"v_exp_f32 v186, v186\n"
"v_exp_f32 v187, v187\n"
"v_cvt_pk_bf16_f32 v184, v184, v185\n"
"v_mfma_f32_16x16x32_bf16 v[208:211], v[228:231], v[24:27], v[208:211]\n"
"ds_write_addtid_b32 v184 offset:53248\n"
"v_exp_f32 v194, v194\n"
"v_exp_f32 v195, v195\n"
"v_cvt_pk_bf16_f32 v185, v186, v187\n"
"v_cvt_pk_bf16_f32 v194, v194, v195\n"
"ds_write_addtid_b32 v185 offset:53504\n"
"v_exp_f32 v196, v196\n"
"v_exp_f32 v197, v197\n"
"s_nop 0\n"
"v_cvt_pk_bf16_f32 v195, v196, v197\n"
"v_mfma_f32_16x16x32_bf16 v[212:215], v[250:253], v[204:207], v[212:215]\n"
"v_exp_f32 v188, v188\n"
"ds_write_addtid_b32 v194 offset:53760\n"
"ds_write_addtid_b32 v195 offset:54016\n"
"v_mfma_f32_16x16x32_bf16 v[212:215], v[252:255], v[24:27], v[212:215]\n"
"s_waitcnt lgkmcnt(4)\n"
"v_exp_f32 v189, v189\n"
"v_mfma_f32_16x16x32_bf16 v[198:201], v[178:181], v[204:207], v[198:201]\n"
"v_exp_f32 v190, v190\n"
"v_exp_f32 v191, v191\n"
"v_exp_f32 v208, v208\n"
"v_exp_f32 v209, v209\n"
"v_exp_f32 v210, v210\n"
"v_cvt_pk_bf16_f32 v208, v208, v209\n"
"v_mfma_f32_16x16x32_bf16 v[198:201], v[180:183], v[24:27], v[198:201]\n"
"ds_write_addtid_b32 v208 offset:4096\n"
"v_cvt_pk_bf16_f32 v188, v188, v189\n"
"v_exp_f32 v211, v211\n"
"v_exp_f32 v212, v212\n"
"ds_write_addtid_b32 v188 offset:54272\n"
"v_exp_f32 v213, v213\n"
"v_exp_f32 v214, v214\n"
"v_exp_f32 v215, v215\n"
"v_exp_f32 v198, v198\n"
"v_cvt_pk_bf16_f32 v189, v190, v191\n"
"v_cvt_pk_bf16_f32 v212, v212, v213\n"
"v_cvt_pk_bf16_f32 v213, v214, v215\n"
"ds_write_addtid_b32 v212 offset:54784\n"
"ds_write_addtid_b32 v213 offset:55040\n"
"v_exp_f32 v199, v199\n"
"ds_write_addtid_b32 v189 offset:54528\n"
"v_exp_f32 v200, v200\n"
"v_exp_f32 v201, v201\n"
"v_cvt_pk_bf16_f32 v198, v198, v199\n"
"v_cvt_pk_bf16_f32 v199, v200, v201\n"
"ds_write_addtid_b32 v198 offset:4608\n"
"ds_write_addtid_b32 v199 offset:4864\n"
"v_cvt_pk_bf16_f32 v209, v210, v211\n"
"ds_write_addtid_b32 v209 offset:4352\n"
"s_waitcnt lgkmcnt(0)\n"
"s_barrier\n"
"s_barrier\n"
"s_barrier\n"
    ,
"s_add_u32 s50, s54, 53248\n"
"s_lshr_b32 s41, s40, 1\n"
"s_sub_u32 s55, 0, s41\n"
"s_mul_i32 s51, s41, 4096\n"
"s_lshl_b32 s41, s40, 10\n"
"s_add_u32 s51, s51, s41\n"
"s_lshl_b32 s65, s40, 2\n"
"s_add_u32 s65, s65, 32192\n"
"s_andn2_b32 s41, s45, 7\n"
"s_add_u32 s56, s56, s41\n"
"s_addc_u32 s57, s57, 0\n"
"s_add_u32 s66, s66, s41\n"
"s_addc_u32 s67, s67, 0\n"
"s_mul_i32 s68, s40, 1552\n"
"s_add_u32 s68, s68, 57344\n"
"s_setprio 0\n"
"s_movk_i32 s40, 768\n"
"s_mov_b64 s[62:63], 0\n"
"s_movk_i32 s64, 0x207\n"
"s_add_u32 s58, s56, 128\n"
"s_addc_u32 s59, s57, 0\n"
"s_add_u32 s60, s56, 256\n"
"s_addc_u32 s61, s57, 0\n"
"v_subrev_u32 v179, 24576, v10\n"
"s_waitcnt vmcnt(0)\n"
"s_barrier\n"
"s_add_u32 s42, s46, 110976\n"
"s_addc_u32 s43, s47, 0\n"
"s_add_u32 m0, s50, 27648\n"
"s_nop 0\n"
"global_load_lds_dwordx4 v0, s[42:43]\n"
"s_add_u32 m0, s50, 28672\n"
"s_nop 0\n"
"global_load_lds_dwordx4 v1, s[42:43]\n"
"s_add_u32 m0, s50, 29696\n"
"s_nop 0\n"
"global_load_lds_dwordx4 v2, s[42:43]\n"
"s_add_u32 s42, s46, 147840\n"
"s_addc_u32 s43, s47, 0\n"
"s_add_u32 m0, s50, 49152\n"
"s_nop 0\n"
"global_load_lds_dwordx4 v0, s[42:43]\n"
"s_add_u32 m0, s50, 50176\n"
"s_nop 0\n"
"global_load_lds_dwordx4 v1, s[42:43]\n"
"s_add_u32 m0, s50, 51200\n"
"s_nop 0\n"
"global_load_lds_dwordx4 v2, s[42:43]\n"
"s_add_u32 s42, s46, 184704\n"
"s_addc_u32 s43, s47, 0\n"
"s_add_u32 m0, s50, 52224\n"
"s_nop 0\n"
"global_load_lds_dwordx4 v0, s[42:43]\n"
"s_add_u32 m0, s50, 53248\n"
"s_nop 0\n"
"global_load_lds_dwordx4 v1, s[42:43]\n"
"s_add_u32 m0, s50, 54272\n"
"s_nop 0\n"
"global_load_lds_dwordx4 v2, s[42:43]\n"
"s_add_u32 s42, s46, 768\n"
"s_addc_u32 s43, s47, 0\n"
"s_add_u32 m0, s50, 0\n"
"s_nop 0\n"
"global_load_lds_dwordx4 v0, s[42:43]\n"
"s_add_u32 m0, s50, 1024\n"
"s_nop 0\n"
"global_load_lds_dwordx4 v1, s[42:43]\n"
"s_add_u32 m0, s50, 2048\n"
"s_nop 0\n"
"global_load_lds_dwordx4 v2, s[42:43]\n"
"s_waitcnt vmcnt(9)\n"
"s_barrier\n"
"s_add_u32 s42, s46, 37632\n"
"s_addc_u32 s43, s47, 0\n"
"s_add_u32 m0, s50, 3072\n"
"s_nop 0\n"
"global_load_lds_dwordx4 v0, s[42:43]\n"
"s_add_u32 m0, s50, 4096\n"
"s_nop 0\n"
"global_load_lds_dwordx4 v1, s[42:43]\n"
"s_add_u32 m0, s50, 5120\n"
"s_nop 0\n"
"global_load_lds_dwordx4 v2, s[42:43]\n"
"s_add_u32 s42, s46, 74496\n"
"s_addc_u32 s43, s47, 0\n"
"s_add_u32 m0, s50, 4294918144\n"
"s_nop 0\n"
"global_load_lds_dwordx4 v0, s[42:43]\n"
"s_add_u32 m0, s50, 4294919168\n"
"s_nop 0\n"
"global_load_lds_dwordx4 v1, s[42:43]\n"
"s_add_u32 m0, s50, 4294920192\n"
"s_nop 0\n"
"global_load_lds_dwordx4 v2, s[42:43]\n"
"s_waitcnt vmcnt(9)\n"
"s_barrier\n"
"s_add_u32 s42, s46, 111360\n"
"s_addc_u32 s43, s47, 0\n"
"s_add_u32 m0, s50, 4294921216\n"
"s_nop 0\n"
"global_load_lds_dwordx4 v0, s[42:43]\n"
"s_add_u32 m0, s50, 4294922240\n"
"s_nop 0\n"
"global_load_lds_dwordx4 v1, s[42:43]\n"
"s_add_u32 m0, s50, 4294923264\n"
"s_nop 0\n"
"global_load_lds_dwordx4 v2, s[42:43]\n"
"s_barrier\n"
"s_add_u32 s42, s46, 148224\n"
"s_addc_u32 s43, s47, 0\n"
"s_add_u32 m0, s50, 24576\n"
"s_nop 0\n"
"global_load_lds_dwordx4 v0, s[42:43]\n"
"s_add_u32 m0, s50, 25600\n"
"s_nop 0\n"
"global_load_lds_dwordx4 v1, s[42:43]\n"
"s_add_u32 m0, s50, 26624\n"
"s_nop 0\n"
"global_load_lds_dwordx4 v2, s[42:43]\n"
"s_add_u32 s42, s46, 185088\n"
"s_addc_u32 s43, s47, 0\n"
"s_add_u32 m0, s50, 27648\n"
"s_nop 0\n"
"global_load_lds_dwordx4 v0, s[42:43]\n"
"ds_read_b128 v[180:183], v12 offset:24576\n"
"s_add_u32 m0, s50, 28672\n"
"s_nop 0\n"
"global_load_lds_dwordx4 v1, s[42:43]\n"
"s_add_u32 m0, s50, 29696\n"
"s_nop 0\n"
"global_load_lds_dwordx4 v2, s[42:43]\n"
"s_mov_b32 m0, 3584\n"
"s_bfm_b64 exec, 6, 0\n"
"global_load_lds_dword v22, s[46:47]\n"
"s_mov_b64 exec, 31\n"
"global_load_lds_dword v23, s[48:49]\n"
"s_mov_b64 exec, -1\n"
"s_waitcnt vmcnt(17)\n"
"ds_read_b64_tr_b16 v[26:27], v10 offset:29504\n"
"ds_read_b64_tr_b16 v[24:25], v10 offset:28736\n"
"ds_read_u16 v23, v21 offset:1152\n"
"ds_read_b64_tr_b16 v[34:35], v10 offset:29472\n"
"s_andn2_b32 s41, 576, s55\n"
"v_add_u32 v178, s41, v15\n"
"ds_read_u16 v178, v178 offset:224\n"
"ds_read_b128 v[184:187], v12 offset:30720\n"
"ds_read_b64_tr_b16 v[28:29], v10 offset:28672\n"
"s_waitcnt lgkmcnt(2)\n"
"v_mad_u32_u24 v178, v178, s40, v16\n"
"s_add_u32 m0, s51, 102400\n"
"s_nop 0\n"
"global_load_lds_dwordx4 v178, s[56:57] nt\n"
"ds_read_b64_tr_b16 v[32:33], v10 offset:28704\n"
"ds_read_b128 v[188:191], v12 offset:36864\n"
"s_add_u32 m0, s51, 104448\n"
"s_nop 0\n"
"global_load_lds_dwordx4 v178, s[58:59] nt\n"
"s_add_u32 m0, s51, 106496\n"
"s_nop 0\n"
"global_load_lds_dwordx4 v178, s[60:61] nt\n"
"s_waitcnt vmcnt(17)\n"
"ds_read_b64_tr_b16 v[38:39], v10 offset:32576\n"
"s_and_b32 s41, 32, s55\n"
"v_add_u32 v178, s41, v15\n"
"ds_read_u16 v178, v178 offset:352\n"
"ds_read_b64_tr_b16 v[30:31], v10 offset:29440\n"
"ds_read_u16 v22, v21 offset:1184\n"
"ds_read_b64_tr_b16 v[44:45], v10 offset:31776\n"
"ds_read_b128 v[192:195], v13 offset:24576\n"
"ds_read_b64_tr_b16 v[46:47], v10 offset:32544\n"
"s_waitcnt lgkmcnt(5)\n"
"v_mad_u32_u24 v178, v178, s40, v16\n"
"s_waitcnt lgkmcnt(3)\n"
"v_mad_u32_u24 v176, v22, s40, v18\n"
"s_add_u32 m0, s51, 114688\n"
"s_nop 0\n"
"global_load_lds_dwordx4 v178, s[56:57] nt\n"
"ds_read_b64_tr_b16 v[42:43], v10 offset:32512\n"
"v_mad_u32_u24 v22, v22, s40, v19\n"
"s_add_u32 m0, s51, 116736\n"
"s_nop 0\n"
"global_load_lds_dwordx4 v178, s[58:59] nt\n"
"s_add_u32 m0, s51, 118784\n"
"s_nop 0\n"
"global_load_lds_dwordx4 v178, s[60:61] nt\n"
"v_mfma_f32_16x16x32_bf16 v[196:199], v[180:183], v[28:31], 0\n"
"v_mfma_f32_16x16x32_bf16 v[200:203], v[180:183], v[32:35], 0\n"
"v_mad_u32_u24 v177, v23, s40, v18\n"
"v_mfma_f32_16x16x32_bf16 v[204:207], v[180:183], v[24:27], 0\n"
"ds_read_b128 v[180:183], v13 offset:30720\n"
"v_mfma_f32_16x16x32_bf16 v[212:215], v[184:187], v[28:31], 0\n"
"s_waitcnt vmcnt(17)\n"
"ds_read_b64_tr_b16 v[40:41], v10 offset:31744\n"
"v_mfma_f32_16x16x32_bf16 v[216:219], v[184:187], v[32:35], 0\n"
"ds_read_b64_tr_b16 v[58:59], v179 offset:4928\n"
"ds_read_b64_tr_b16 v[36:37], v10 offset:31808\n"
"v_mfma_f32_16x16x32_bf16 v[220:223], v[184:187], v[24:27], 0\n"
"v_mfma_f32_16x16x32_bf16 v[224:227], v[188:191], v[28:31], 0\n"
"ds_read_b128 v[184:187], v13 offset:36864\n"
"v_mad_u32_u24 v23, v23, s40, v19\n"
"v_mfma_f32_16x16x32_bf16 v[228:231], v[188:191], v[32:35], 0\n"
"ds_read_b64_tr_b16 v[50:51], v179 offset:4864\n"
"v_mfma_f32_16x16x32_bf16 v[232:235], v[188:191], v[24:27], 0\n"
"s_waitcnt lgkmcnt(4)\n"
"ds_read_b128 v[188:191], v12 offset:26624\n"
"v_mfma_f32_16x16x32_bf16 v[196:199], v[192:195], v[40:43], v[196:199]\n"
"ds_read_b64_tr_b16 v[48:49], v179 offset:4096\n"
"v_mfma_f32_16x16x32_bf16 v[200:203], v[192:195], v[44:47], v[200:203]\n"
"s_waitcnt vmcnt(14)\n"
"ds_read_b64_tr_b16 v[60:61], v179 offset:7168\n"
"s_waitcnt lgkmcnt(5)\n"
"v_mfma_f32_16x16x32_bf16 v[204:207], v[192:195], v[36:39], v[204:207]\n"
"ds_read_b64_tr_b16 v[68:69], v179 offset:7232\n"
"ds_read_b64_tr_b16 v[52:53], v179 offset:4128\n"
"v_mfma_f32_16x16x32_bf16 v[212:215], v[180:183], v[40:43], v[212:215]\n"
"ds_read_b64_tr_b16 v[54:55], v179 offset:4896\n"
"v_mfma_f32_16x16x32_bf16 v[216:219], v[180:183], v[44:47], v[216:219]\n"
"ds_read_b64_tr_b16 v[56:57], v179 offset:4160\n"
"v_mfma_f32_16x16x32_bf16 v[220:223], v[180:183], v[36:39], v[220:223]\n"
"s_waitcnt lgkmcnt(8)\n"
"v_mfma_f32_16x16x32_bf16 v[224:227], v[184:187], v[40:43], v[224:227]\n"
"ds_read_b64_tr_b16 v[64:65], v179 offset:7200\n"
"v_mfma_f32_16x16x32_bf16 v[228:231], v[184:187], v[44:47], v[228:231]\n"
"ds_read_b128 v[192:195], v12 offset:32768\n"
"v_mfma_f32_16x16x32_bf16 v[232:235], v[184:187], v[36:39], v[232:235]\n"
"s_waitcnt lgkmcnt(7)\n"
"ds_read_b128 v[180:183], v12 offset:38912\n"
"v_mfma_f32_16x16x32_bf16 v[196:199], v[188:191], v[48:51], v[196:199]\n"
"s_waitcnt lgkmcnt(4)\n"
"ds_read_b64_tr_b16 v[70:71], v179 offset:8000\n"
"v_mfma_f32_16x16x32_bf16 v[200:203], v[188:191], v[52:55], v[200:203]\n"
"s_waitcnt lgkmcnt(4)\n"
"ds_read_b64_tr_b16 v[62:63], v179 offset:7936\n"
"v_mfma_f32_16x16x32_bf16 v[204:207], v[188:191], v[56:59], v[204:207]\n"
"s_waitcnt lgkmcnt(3)\n"
"ds_read_b128 v[188:191], v13 offset:32768\n"
"v_mfma_f32_16x16x32_bf16 v[212:215], v[192:195], v[48:51], v[212:215]\n"
"ds_read_b64_tr_b16 v[66:67], v179 offset:7968\n"
"s_waitcnt vmcnt(11)\n"
"v_mfma_f32_16x16x32_bf16 v[216:219], v[192:195], v[52:55], v[216:219]\n"
"ds_read_b128 v[184:187], v13 offset:26624\n"
"v_mfma_f32_16x16x32_bf16 v[220:223], v[192:195], v[56:59], v[220:223]\n"
"s_waitcnt lgkmcnt(5)\n"
"ds_read_b64_tr_b16 v[82:83], v10 offset:54080\n"
"v_mfma_f32_16x16x32_bf16 v[224:227], v[180:183], v[48:51], v[224:227]\n"
"ds_read_b64_tr_b16 v[80:81], v10 offset:53312\n"
"v_mfma_f32_16x16x32_bf16 v[228:231], v[180:183], v[52:55], v[228:231]\n"
"ds_read_b128 v[192:195], v13 offset:38912\n"
"v_mfma_f32_16x16x32_bf16 v[232:235], v[180:183], v[56:59], v[232:235]\n"
"s_waitcnt lgkmcnt(3)\n"
"ds_read_b64_tr_b16 v[78:79], v10 offset:54048\n"
"v_mfma_f32_16x16x32_bf16 v[196:199], v[184:187], v[60:63], v[196:199]\n"
"ds_read_b64_tr_b16 v[74:75], v10 offset:54016\n"
"v_mfma_f32_16x16x32_bf16 v[200:203], v[184:187], v[64:67], v[200:203]\n"
"ds_read_b128 v[178:181], v12 offset:28672\n"
"v_mfma_f32_16x16x32_bf16 v[204:207], v[184:187], v[68:71], v[204:207]\n"
"ds_read_b128 v[182:185], v12 offset:34816\n"
"ds_read_b64_tr_b16 v[76:77], v10 offset:53280\n"
"v_mfma_f32_16x16x32_bf16 v[212:215], v[188:191], v[60:63], v[212:215]\n"
"s_barrier\n"
"v_mfma_f32_16x16x32_bf16 v[216:219], v[188:191], v[64:67], v[216:219]\n"
"ds_read_b64_tr_b16 v[72:73], v10 offset:53248\n"
"v_mfma_f32_16x16x32_bf16 v[220:223], v[188:191], v[68:71], v[220:223]\n"
"s_waitcnt lgkmcnt(6)\n"
"ds_read_b128 v[186:189], v12 offset:40960\n"
"v_mfma_f32_16x16x32_bf16 v[224:227], v[192:195], v[60:63], v[224:227]\n"
"s_waitcnt vmcnt(8)\n"
"ds_read_b64_tr_b16 v[92:93], v10 offset:56384\n"
"v_mfma_f32_16x16x32_bf16 v[228:231], v[192:195], v[64:67], v[228:231]\n"
"ds_read_b64_tr_b16 v[84:85], v10 offset:56320\n"
"v_mfma_f32_16x16x32_bf16 v[232:235], v[192:195], v[68:71], v[232:235]\n"
"s_waitcnt lgkmcnt(3)\n"
"ds_read_b64_tr_b16 v[94:95], v10 offset:57152\n"
"v_mfma_f32_16x16x32_bf16 v[196:199], v[178:181], v[72:75], v[196:199]\n"
"ds_read_b64_tr_b16 v[86:87], v10 offset:57088\n"
"v_mfma_f32_16x16x32_bf16 v[200:203], v[178:181], v[76:79], v[200:203]\n"
"ds_read_b64_tr_b16 v[88:89], v10 offset:56352\n"
"v_mfma_f32_16x16x32_bf16 v[204:207], v[178:181], v[80:83], v[204:207]\n"
"ds_read_b64_tr_b16 v[90:91], v10 offset:57120\n"
"v_mfma_f32_16x16x32_bf16 v[212:215], v[182:185], v[72:75], v[212:215]\n"
"v_mfma_f32_16x16x32_bf16 v[216:219], v[182:185], v[76:79], v[216:219]\n"
"ds_read_b128 v[190:193], v13 offset:28672\n"
"v_mfma_f32_16x16x32_bf16 v[220:223], v[182:185], v[80:83], v[220:223]\n"
"s_waitcnt lgkmcnt(7)\n"
"ds_read_b128 v[178:181], v13 offset:34816\n"
"v_mfma_f32_16x16x32_bf16 v[224:227], v[186:189], v[72:75], v[224:227]\n"
"v_mfma_f32_16x16x32_bf16 v[228:231], v[186:189], v[76:79], v[228:231]\n"
"v_mfma_f32_16x16x32_bf16 v[232:235], v[186:189], v[80:83], v[232:235]\n"
"s_waitcnt lgkmcnt(1)\n"
"v_mfma_f32_16x16x32_bf16 v[196:199], v[190:193], v[84:87], v[196:199]\n"
"ds_read_b128 v[238:241], v13 offset:40960\n"
"v_mfma_f32_16x16x32_bf16 v[200:203], v[190:193], v[88:91], v[200:203]\n"
"s_barrier\n"
"v_mfma_f32_16x16x32_bf16 v[204:207], v[190:193], v[92:95], v[204:207]\n"
"s_waitcnt lgkmcnt(1)\n"
"s_setprio 2\n"
"v_mfma_f32_16x16x32_bf16 v[212:215], v[178:181], v[84:87], v[212:215]\n"
"ds_read_b128 v[242:245], v12 offset:43008\n"
"v_mfma_f32_16x16x32_bf16 v[216:219], v[178:181], v[88:91], v[216:219]\n"
"s_nop 0\n"
"v_cvt_pk_bf16_f32 v210, v200, v201\n"
"v_mfma_f32_16x16x32_bf16 v[220:223], v[178:181], v[92:95], v[220:223]\n"
"s_waitcnt lgkmcnt(1)\n"
"v_cvt_pk_bf16_f32 v237, v206, v207\n"
"v_mfma_f32_16x16x32_bf16 v[224:227], v[238:241], v[84:87], v[224:227]\n"
"v_cvt_pk_bf16_f32 v211, v202, v203\n"
"v_cvt_pk_bf16_f32 v187, v214, v215\n"
"v_mfma_f32_16x16x32_bf16 v[228:231], v[238:241], v[88:91], v[228:231]\n"
"ds_read_b128 v[188:191], v13 offset:43008\n"
"v_cvt_pk_bf16_f32 v185, v198, v199\n"
"v_mfma_f32_16x16x32_bf16 v[232:235], v[238:241], v[92:95], v[232:235]\n"
"s_waitcnt lgkmcnt(1)\n"
"v_cvt_pk_bf16_f32 v184, v196, v197\n"
"v_cvt_pk_bf16_f32 v236, v204, v205\n"
"v_mfma_f32_16x16x32_bf16 v[200:203], v[242:245], v[28:31], 0\n"
"v_cvt_pk_bf16_f32 v186, v212, v213\n"
"v_cvt_pk_bf16_f32 v212, v216, v217\n"
"v_mfma_f32_16x16x32_bf16 v[204:207], v[242:245], v[32:35], 0\n"
"ds_read_b128 v[178:181], v12 offset:45056\n"
"v_cvt_pk_bf16_f32 v195, v226, v227\n"
"v_mfma_f32_16x16x32_bf16 v[214:217], v[242:245], v[24:27], 0\n"
"s_waitcnt lgkmcnt(1)\n"
"ds_read_b128 v[196:199], v13 offset:45056\n"
"v_mfma_f32_16x16x32_bf16 v[200:203], v[188:191], v[40:43], v[200:203]\n"
"v_cvt_pk_bf16_f32 v238, v220, v221\n"
"v_cvt_pk_bf16_f32 v194, v224, v225\n"
"v_mfma_f32_16x16x32_bf16 v[204:207], v[188:191], v[44:47], v[204:207]\n"
"v_cvt_pk_bf16_f32 v220, v228, v229\n"
"v_mfma_f32_16x16x32_bf16 v[214:217], v[188:191], v[36:39], v[214:217]\n"
"s_waitcnt lgkmcnt(1)\n"
"ds_read_b128 v[188:191], v13 offset:47104\n"
"v_mfma_f32_16x16x32_bf16 v[200:203], v[178:181], v[48:51], v[200:203]\n"
"v_cvt_pk_bf16_f32 v239, v222, v223\n"
"v_cvt_pk_bf16_f32 v247, v234, v235\n"
"v_mfma_f32_16x16x32_bf16 v[204:207], v[178:181], v[52:55], v[204:207]\n"
"ds_read_b128 v[240:243], v12 offset:47104\n"
"v_mfma_f32_16x16x32_bf16 v[214:217], v[178:181], v[56:59], v[214:217]\n"
"s_waitcnt lgkmcnt(2)\n"
"v_mfma_f32_16x16x32_bf16 v[200:203], v[196:199], v[60:63], v[200:203]\n"
"v_cvt_pk_bf16_f32 v213, v218, v219\n"
"s_setprio 0\n"
"v_mfma_f32_16x16x32_bf16 v[204:207], v[196:199], v[64:67], v[204:207]\n"
"ds_read_b128 v[178:181], v12 offset:49152\n"
"v_mfma_f32_16x16x32_bf16 v[214:217], v[196:199], v[68:71], v[214:217]\n"
"s_waitcnt lgkmcnt(1)\n"
"v_cvt_pk_bf16_f32 v246, v232, v233\n"
"v_mfma_f32_16x16x32_bf16 v[200:203], v[240:243], v[72:75], v[200:203]\n"
"v_cvt_pk_bf16_f32 v221, v230, v231\n"
"v_mfma_f32_16x16x32_bf16 v[204:207], v[240:243], v[76:79], v[204:207]\n"
"v_mfma_f32_16x16x32_bf16 v[214:217], v[240:243], v[80:83], v[214:217]\n"
"v_mfma_f32_16x16x32_bf16 v[200:203], v[188:191], v[84:87], v[200:203]\n"
"ds_read_b128 v[224:227], v13 offset:49152\n"
"v_mfma_f32_16x16x32_bf16 v[204:207], v[188:191], v[88:91], v[204:207]\n"
"ds_read_b128 v[228:231], v12 offset:51200\n"
"v_mfma_f32_16x16x32_bf16 v[214:217], v[188:191], v[92:95], v[214:217]\n"
"s_waitcnt lgkmcnt(2)\n"
"v_mfma_f32_16x16x32_bf16 v[232:235], v[178:181], v[28:31], 0\n"
"s_nop 1\n"
"v_cvt_pk_bf16_f32 v196, v200, v201\n"
"v_mfma_f32_16x16x32_bf16 v[240:243], v[178:181], v[32:35], 0\n"
"ds_read_b128 v[188:191], v13 offset:51200\n"
"v_cvt_pk_bf16_f32 v197, v202, v203\n"
"v_mfma_f32_16x16x32_bf16 v[250:253], v[178:181], v[24:27], 0\n"
"s_waitcnt lgkmcnt(2)\n"
"v_mfma_f32_16x16x32_bf16 v[232:235], v[224:227], v[40:43], v[232:235]\n"
"v_cvt_pk_bf16_f32 v249, v216, v217\n"
"v_mfma_f32_16x16x32_bf16 v[240:243], v[224:227], v[44:47], v[240:243]\n"
"v_cvt_pk_bf16_f32 v222, v204, v205\n"
"v_mfma_f32_16x16x32_bf16 v[250:253], v[224:227], v[36:39], v[250:253]\n"
"s_waitcnt lgkmcnt(1)\n"
"v_mfma_f32_16x16x32_bf16 v[232:235], v[228:231], v[48:51], v[232:235]\n"
"v_cvt_pk_bf16_f32 v223, v206, v207\n"
"v_mfma_f32_16x16x32_bf16 v[240:243], v[228:231], v[52:55], v[240:243]\n"
"ds_read_b128 v[178:181], v12 offset:53248\n"
"v_mfma_f32_16x16x32_bf16 v[250:253], v[228:231], v[56:59], v[250:253]\n"
"s_waitcnt lgkmcnt(1)\n"
"v_mfma_f32_16x16x32_bf16 v[232:235], v[188:191], v[60:63], v[232:235]\n"
"ds_read_b128 v[198:201], v13 offset:53248\n"
"v_mfma_f32_16x16x32_bf16 v[240:243], v[188:191], v[64:67], v[240:243]\n"
"v_cvt_pk_bf16_f32 v248, v214, v215\n"
"v_mfma_f32_16x16x32_bf16 v[250:253], v[188:191], v[68:71], v[250:253]\n"
"s_waitcnt lgkmcnt(1)\n"
"v_mfma_f32_16x16x32_bf16 v[232:235], v[178:181], v[72:75], v[232:235]\n"
"v_mfma_f32_16x16x32_bf16 v[240:243], v[178:181], v[76:79], v[240:243]\n"
"ds_read_b128 v[202:205], v12 offset:55296\n"
"v_mfma_f32_16x16x32_bf16 v[250:253], v[178:181], v[80:83], v[250:253]\n"
"s_waitcnt lgkmcnt(1)\n"
"v_mfma_f32_16x16x32_bf16 v[232:235], v[198:201], v[84:87], v[232:235]\n"
"v_mfma_f32_16x16x32_bf16 v[240:243], v[198:201], v[88:91], v[240:243]\n"
"ds_read_b128 v[190:193], v13 offset:55296\n"
"v_mfma_f32_16x16x32_bf16 v[250:253], v[198:201], v[92:95], v[250:253]\n"
"s_waitcnt lgkmcnt(1)\n"
"v_mfma_f32_16x16x32_bf16 v[206:209], v[202:205], v[28:31], 0\n"
"ds_read_b128 v[178:181], v12 offset:57344\n"
"v_mfma_f32_16x16x32_bf16 v[216:219], v[202:205], v[32:35], 0\n"
"ds_read_b128 v[198:201], v13 offset:57344\n"
"v_mfma_f32_16x16x32_bf16 v[224:227], v[202:205], v[24:27], 0\n"
"s_waitcnt lgkmcnt(2)\n"
"ds_read_b128 v[202:205], v12 offset:59392\n"
"v_mfma_f32_16x16x32_bf16 v[206:209], v[190:193], v[40:43], v[206:209]\n"
"v_mfma_f32_16x16x32_bf16 v[216:219], v[190:193], v[44:47], v[216:219]\n"
"v_cvt_pk_bf16_f32 v188, v232, v233\n"
"v_mfma_f32_16x16x32_bf16 v[224:227], v[190:193], v[36:39], v[224:227]\n"
"s_waitcnt lgkmcnt(2)\n"
"ds_read_b128 v[190:193], v13 offset:59392\n"
"v_mfma_f32_16x16x32_bf16 v[206:209], v[178:181], v[48:51], v[206:209]\n"
"v_cvt_pk_bf16_f32 v215, v242, v243\n"
"v_mfma_f32_16x16x32_bf16 v[216:219], v[178:181], v[52:55], v[216:219]\n"
"v_cvt_pk_bf16_f32 v214, v240, v241\n"
"v_mfma_f32_16x16x32_bf16 v[224:227], v[178:181], v[56:59], v[224:227]\n"
"s_waitcnt lgkmcnt(2)\n"
"v_mfma_f32_16x16x32_bf16 v[206:209], v[198:201], v[60:63], v[206:209]\n"
"v_mfma_f32_16x16x32_bf16 v[216:219], v[198:201], v[64:67], v[216:219]\n"
"v_cvt_pk_bf16_f32 v241, v252, v253\n"
"v_mfma_f32_16x16x32_bf16 v[224:227], v[198:201], v[68:71], v[224:227]\n"
"s_waitcnt lgkmcnt(0)\n"
"s_waitcnt vmcnt(0)\n"
"s_barrier\n"
"v_mfma_f32_16x16x32_bf16 v[206:209], v[202:205], v[72:75], v[206:209]\n"
"v_cvt_pk_bf16_f32 v189, v234, v235\n"
"v_mfma_f32_16x16x32_bf16 v[216:219], v[202:205], v[76:79], v[216:219]\n"
"v_cvt_pk_bf16_f32 v240, v250, v251\n"
"v_mfma_f32_16x16x32_bf16 v[224:227], v[202:205], v[80:83], v[224:227]\n"
"s_setprio 2\n"
"v_mfma_f32_16x16x32_bf16 v[206:209], v[190:193], v[84:87], v[206:209]\n"
"ds_read_b128 v[180:183], v12 offset:6144\n"
"s_and_b32 s41, 160, s55\n"
"v_add_u32 v2, s41, v15\n"
"v_mfma_f32_16x16x32_bf16 v[216:219], v[190:193], v[88:91], v[216:219]\n"
"ds_read_b128 v[198:201], v12 offset:8192\n"
"ds_read_u16 v2, v2 offset:96\n"
"v_mfma_f32_16x16x32_bf16 v[224:227], v[190:193], v[92:95], v[224:227]\n"
"ds_read_b128 v[190:193], v13 offset:6144\n"
"v_cvt_pk_bf16_f32 v178, v206, v207\n"
"v_cvt_pk_bf16_f32 v179, v208, v209\n"
"s_nop 1\n"
"v_cvt_pk_bf16_f32 v205, v218, v219\n"
"v_cvt_pk_bf16_f32 v204, v216, v217\n"
"s_waitcnt lgkmcnt(1)\n"
"v_mad_u32_u24 v2, v2, s40, v16\n"
"v_cvt_pk_bf16_f32 v230, v224, v225\n"
"v_cvt_pk_bf16_f32 v231, v226, v227\n"
"v_mfma_f32_16x16x32_bf16 v[206:209], v[180:183], v[28:31], 0\n"
"v_mfma_f32_16x16x32_bf16 v[216:219], v[180:183], v[32:35], 0\n"
"s_add_u32 m0, s51, 126976\n"
"s_nop 0\n"
"global_load_lds_dwordx4 v2, s[56:57] nt\n"
"v_mfma_f32_16x16x32_bf16 v[232:235], v[180:183], v[24:27], 0\n"
"s_waitcnt lgkmcnt(0)\n"
"v_mfma_f32_16x16x32_bf16 v[206:209], v[190:193], v[40:43], v[206:209]\n"
"s_add_u32 m0, s51, 129024\n"
"s_nop 0\n"
"global_load_lds_dwordx4 v2, s[58:59] nt\n"
"s_add_u32 m0, s51, 131072\n"
"s_nop 0\n"
"global_load_lds_dwordx4 v2, s[60:61] nt\n"
"v_mfma_f32_16x16x32_bf16 v[216:219], v[190:193], v[44:47], v[216:219]\n"
"s_and_b32 s41, 32, s55\n"
"v_add_u32 v2, s41, v15\n"
"v_mfma_f32_16x16x32_bf16 v[232:235], v[190:193], v[36:39], v[232:235]\n"
"ds_read_b128 v[180:183], v13 offset:8192\n"
"v_mfma_f32_16x16x32_bf16 v[206:209], v[198:201], v[48:51], v[206:209]\n"
"ds_read_b128 v[190:193], v12 offset:10240\n"
"ds_read_u16 v2, v2 offset:832\n"
"v_mfma_f32_16x16x32_bf16 v[216:219], v[198:201], v[52:55], v[216:219]\n"
"v_mfma_f32_16x16x32_bf16 v[232:235], v[198:201], v[56:59], v[232:235]\n"
"s_waitcnt lgkmcnt(2)\n"
"v_mfma_f32_16x16x32_bf16 v[206:209], v[180:183], v[60:63], v[206:209]\n"
"s_waitcnt lgkmcnt(0)\n"
"v_mad_u32_u24 v2, v2, s40, v16\n"
"v_mfma_f32_16x16x32_bf16 v[216:219], v[180:183], v[64:67], v[216:219]\n"
"v_mfma_f32_16x16x32_bf16 v[232:235], v[180:183], v[68:71], v[232:235]\n"
"v_mfma_f32_16x16x32_bf16 v[206:209], v[190:193], v[72:75], v[206:209]\n"
"ds_read_b128 v[198:201], v13 offset:10240\n"
"s_add_u32 m0, s51, 139264\n"
"s_nop 0\n"
"global_load_lds_dwordx4 v2, s[56:57] nt\n"
"v_mfma_f32_16x16x32_bf16 v[216:219], v[190:193], v[76:79], v[216:219]\n"
"v_mfma_f32_16x16x32_bf16 v[232:235], v[190:193], v[80:83], v[232:235]\n"
"s_add_u32 m0, s51, 141312\n"
"s_nop 0\n"
"global_load_lds_dwordx4 v2, s[58:59] nt\n"
"s_waitcnt lgkmcnt(0)\n"
"v_mfma_f32_16x16x32_bf16 v[206:209], v[198:201], v[84:87], v[206:209]\n"
"s_add_u32 m0, s51, 143360\n"
"s_nop 0\n"
"global_load_lds_dwordx4 v2, s[60:61] nt\n"
"ds_read_b128 v[180:183], v12 offset:12288\n"
"v_mfma_f32_16x16x32_bf16 v[216:219], v[198:201], v[88:91], v[216:219]\n"
"s_andn2_b32 s41, 288, s55\n"
"v_add_u32 v2, s41, v15\n"
"ds_read_b128 v[190:193], v13 offset:12288\n"
"v_mfma_f32_16x16x32_bf16 v[232:235], v[198:201], v[92:95], v[232:235]\n"
"s_waitcnt lgkmcnt(1)\n"
"ds_read_u16 v2, v2 offset:128\n"
"v_mfma_f32_16x16x32_bf16 v[200:203], v[180:183], v[28:31], 0\n"
"ds_read_b128 v[226:229], v12 offset:14336\n"
"s_setprio 0\n"
"v_mfma_f32_16x16x32_bf16 v[242:245], v[180:183], v[32:35], 0\n"
"v_cvt_pk_bf16_f32 v225, v218, v219\n"
"v_mfma_f32_16x16x32_bf16 v[252:255], v[180:183], v[24:27], 0\n"
"s_waitcnt lgkmcnt(2)\n"
"v_mfma_f32_16x16x32_bf16 v[200:203], v[190:193], v[40:43], v[200:203]\n"
"v_cvt_pk_bf16_f32 v198, v206, v207\n"
"v_mfma_f32_16x16x32_bf16 v[242:245], v[190:193], v[44:47], v[242:245]\n"
"v_mfma_f32_16x16x32_bf16 v[252:255], v[190:193], v[36:39], v[252:255]\n"
"s_waitcnt lgkmcnt(1)\n"
"v_mad_u32_u24 v2, v2, s40, v16\n"
"s_add_u32 m0, s51, 151552\n"
"s_nop 0\n"
"global_load_lds_dwordx4 v2, s[56:57] nt\n"
"s_add_u32 m0, s51, 153600\n"
"s_nop 0\n"
"global_load_lds_dwordx4 v2, s[58:59] nt\n"
"s_add_u32 m0, s51, 155648\n"
"s_nop 0\n"
"global_load_lds_dwordx4 v2, s[60:61] nt\n"
"s_waitcnt lgkmcnt(0)\n"
"v_mfma_f32_16x16x32_bf16 v[200:203], v[226:229], v[48:51], v[200:203]\n"
"ds_read_b128 v[180:183], v13 offset:14336\n"
"v_cvt_pk_bf16_f32 v251, v234, v235\n"
"s_barrier\n"
"v_mfma_f32_16x16x32_bf16 v[242:245], v[226:229], v[52:55], v[242:245]\n"
"v_cvt_pk_bf16_f32 v250, v232, v233\n"
"v_mfma_f32_16x16x32_bf16 v[252:255], v[226:229], v[56:59], v[252:255]\n"
"v_cvt_pk_bf16_f32 v224, v216, v217\n"
"s_add_u32 s42, s48, 0\n"
"s_addc_u32 s43, s49, 0\n"
"s_add_u32 m0, s50, 24576\n"
"s_nop 0\n"
"global_load_lds_dwordx4 v3, s[42:43]\n"
"s_waitcnt lgkmcnt(0)\n"
"v_mfma_f32_16x16x32_bf16 v[200:203], v[180:183], v[60:63], v[200:203]\n"
"ds_read_b128 v[190:193], v12 offset:16384\n"
"v_mfma_f32_16x16x32_bf16 v[242:245], v[180:183], v[64:67], v[242:245]\n"
"v_cvt_pk_bf16_f32 v199, v208, v209\n"
"ds_read_b128 v[206:209], v13 offset:16384\n"
"v_mfma_f32_16x16x32_bf16 v[252:255], v[180:183], v[68:71], v[252:255]\n"
"s_add_u32 m0, s50, 25600\n"
"s_nop 0\n"
"global_load_lds_dwordx4 v4, s[42:43]\n"
"s_add_u32 m0, s50, 26624\n"
"s_nop 0\n"
"global_load_lds_dwordx4 v5, s[42:43]\n"
"s_waitcnt lgkmcnt(1)\n"
"v_mfma_f32_16x16x32_bf16 v[200:203], v[190:193], v[72:75], v[200:203]\n"
"ds_read_b128 v[180:183], v12 offset:18432\n"
"ds_read_b128 v[216:219], v12 offset:20480\n"
"v_mfma_f32_16x16x32_bf16 v[242:245], v[190:193], v[76:79], v[242:245]\n"
"v_mfma_f32_16x16x32_bf16 v[252:255], v[190:193], v[80:83], v[252:255]\n"
"s_waitcnt lgkmcnt(2)\n"
"ds_read_b128 v[190:193], v13 offset:18432\n"
"v_mfma_f32_16x16x32_bf16 v[200:203], v[206:209], v[84:87], v[200:203]\n"
"v_mfma_f32_16x16x32_bf16 v[242:245], v[206:209], v[88:91], v[242:245]\n"
"v_mfma_f32_16x16x32_bf16 v[252:255], v[206:209], v[92:95], v[252:255]\n"
"s_waitcnt lgkmcnt(2)\n"
"v_mfma_f32_16x16x32_bf16 v[226:229], v[180:183], v[28:31], 0\n"
"s_nop 4\n"
"v_cvt_pk_bf16_f32 v207, v244, v245\n"
"v_mfma_f32_16x16x32_bf16 v[112:115], v[180:183], v[32:35], 0\n"
"v_cvt_pk_bf16_f32 v206, v242, v243\n"
"v_mfma_f32_16x16x32_bf16 v[116:119], v[180:183], v[24:27], 0\n"
"s_waitcnt lgkmcnt(0)\n"
"v_cvt_pk_bf16_f32 v180, v200, v201\n"
"v_mfma_f32_16x16x32_bf16 v[226:229], v[190:193], v[40:43], v[226:229]\n"
"v_cvt_pk_bf16_f32 v181, v202, v203\n"
"v_mfma_f32_16x16x32_bf16 v[112:115], v[190:193], v[44:47], v[112:115]\n"
"s_add_u32 s42, s48, 18432\n"
"s_addc_u32 s43, s49, 0\n"
"s_add_u32 m0, s50, 27648\n"
"s_nop 0\n"
"global_load_lds_dwordx4 v3, s[42:43]\n"
"v_cvt_pk_bf16_f32 v232, v252, v253\n"
"v_mfma_f32_16x16x32_bf16 v[116:119], v[190:193], v[36:39], v[116:119]\n"
"v_mfma_f32_16x16x32_bf16 v[226:229], v[216:219], v[48:51], v[226:229]\n"
"ds_read_b128 v[104:107], v13 offset:20480\n"
"s_add_u32 m0, s50, 28672\n"
"s_nop 0\n"
"global_load_lds_dwordx4 v4, s[42:43]\n"
"v_mfma_f32_16x16x32_bf16 v[112:115], v[216:219], v[52:55], v[112:115]\n"
"v_cvt_pk_bf16_f32 v233, v254, v255\n"
"s_add_u32 m0, s50, 29696\n"
"s_nop 0\n"
"global_load_lds_dwordx4 v5, s[42:43]\n"
"ds_read_b128 v[190:193], v12 offset:22528\n"
"v_mfma_f32_16x16x32_bf16 v[116:119], v[216:219], v[56:59], v[116:119]\n"
"s_waitcnt lgkmcnt(1)\n"
"v_mfma_f32_16x16x32_bf16 v[226:229], v[104:107], v[60:63], v[226:229]\n"
"s_waitcnt vmcnt(9)\n"
"s_barrier\n"
"s_waitcnt vmcnt(3)\n"
"ds_read_b64_tr_b16 v[254:255], v10 offset:54080\n"
"v_mfma_f32_16x16x32_bf16 v[112:115], v[104:107], v[64:67], v[112:115]\n"
"ds_read_b64_tr_b16 v[252:253], v10 offset:53312\n"
"ds_read_b64_tr_b16 v[98:99], v11 offset:54016\n"
"ds_read_b64_tr_b16 v[100:101], v11 offset:53256\n"
"v_mfma_f32_16x16x32_bf16 v[116:119], v[104:107], v[68:71], v[116:119]\n"
"s_waitcnt lgkmcnt(4)\n"
"ds_read_b64_tr_b16 v[102:103], v11 offset:54024\n"
"v_mov_b32 v219, 0x3f803f80\n"
"v_mfma_f32_16x16x32_bf16 v[226:229], v[190:193], v[72:75], v[226:229]\n"
"ds_read_b64_tr_b16 v[96:97], v11 offset:53248\n"
"v_mfma_f32_16x16x32_bf16 v[112:115], v[190:193], v[76:79], v[112:115]\n"
"ds_read_b128 v[200:203], v13 offset:22528\n"
"s_waitcnt vmcnt(0)\n"
"ds_read_b64_tr_b16 v[108:109], v11 offset:56328\n"
"ds_read_b64_tr_b16 v[104:105], v11 offset:56320\n"
"v_mov_b32 v217, 0x3f803f80\n"
"v_mov_b32 v218, 0x3f803f80\n"
"v_mfma_f32_16x16x32_bf16 v[116:119], v[190:193], v[80:83], v[116:119]\n"
"s_waitcnt lgkmcnt(3)\n"
"s_add_u32 s42, s48, 36864\n"
"s_addc_u32 s43, s49, 0\n"
"s_add_u32 m0, s50, 24576\n"
"s_nop 0\n"
"global_load_lds_dwordx4 v3, s[42:43]\n"
"s_waitcnt lgkmcnt(2)\n"
"v_mfma_f32_16x16x32_bf16 v[226:229], v[200:203], v[84:87], v[226:229]\n"
"s_nop 7\n"
"v_cvt_pk_bf16_f32 v183, v228, v229\n"
"v_mov_b32 v216, 0x3f803f80\n"
"v_mov_b32 v193, 0\n"
"v_cvt_pk_bf16_f32 v182, v226, v227\n"
"ds_read_b64_tr_b16 v[110:111], v11 offset:57096\n"
"s_add_u32 m0, s50, 25600\n"
"s_nop 0\n"
"global_load_lds_dwordx4 v4, s[42:43]\n"
"ds_read_b64_tr_b16 v[138:139], v10 offset:57152\n"
"v_mfma_f32_16x16x32_bf16 v[112:115], v[200:203], v[88:91], v[112:115]\n"
"ds_read_b64_tr_b16 v[106:107], v11 offset:57088\n"
"ds_read_b64_tr_b16 v[136:137], v10 offset:56384\n"
"v_mfma_f32_16x16x32_bf16 v[116:119], v[200:203], v[92:95], v[116:119]\n"
"s_nop 4\n"
"v_cvt_pk_bf16_f32 v208, v112, v113\n"
"s_add_u32 m0, s50, 26624\n"
"s_nop 0\n"
"global_load_lds_dwordx4 v5, s[42:43]\n"
"v_cvt_pk_bf16_f32 v209, v114, v115\n"
"s_waitcnt lgkmcnt(0)\n"
"s_add_u32 s42, s48, 55296\n"
"s_addc_u32 s43, s49, 0\n"
"s_add_u32 m0, s50, 27648\n"
"s_nop 0\n"
"global_load_lds_dwordx4 v3, s[42:43]\n"
"s_add_u32 m0, s50, 28672\n"
"s_nop 0\n"
"global_load_lds_dwordx4 v4, s[42:43]\n"
"s_add_u32 m0, s50, 29696\n"
"s_nop 0\n"
"global_load_lds_dwordx4 v5, s[42:43]\n"
"s_barrier\n"
"ds_read2st64_b32 v[190:191], v9 offset0:112 offset1:113\n"
"s_waitcnt vmcnt(3)\n"
"ds_read_b64_tr_b16 v[114:115], v11 offset:54024\n"
"ds_read_b64_tr_b16 v[2:3], v11 offset:54016\n"
"ds_read_b32 v192, v9 offset:29184\n"
"ds_read_b64_tr_b16 v[112:113], v11 offset:53256\n"
"v_cvt_pk_bf16_f32 v235, v118, v119\n"
"ds_read_b64_tr_b16 v[146:147], v10 offset:54080\n"
"ds_read2st64_b32 v[202:203], v9 offset0:118 offset1:119\n"
"s_waitcnt lgkmcnt(3)\n"
"v_mfma_f32_16x16x32_bf16 v[226:229], v[216:219], v[190:193], 0\n"
"ds_read2st64_b32 v[200:201], v9 offset0:116 offset1:117\n"
"v_mfma_f32_16x16x32_bf16 v[140:143], v[186:189], v[190:193], 0\n"
"ds_read_b64_tr_b16 v[144:145], v10 offset:53312\n"
"v_cvt_pk_bf16_f32 v234, v116, v117\n"
"v_mfma_f32_16x16x32_bf16 v[242:245], v[212:215], v[190:193], 0\n"
"ds_read_b64_tr_b16 v[0:1], v11 offset:53248\n"
"v_mfma_f32_16x16x32_bf16 v[124:127], v[238:241], v[190:193], 0\n"
"s_waitcnt lgkmcnt(2)\n"
"ds_read2st64_b32 v[192:193], v9 offset0:130 offset1:131\n"
"v_mfma_f32_16x16x32_bf16 v[226:229], v[216:219], v[200:203], v[226:229]\n"
"v_mfma_f32_16x16x32_bf16 v[140:143], v[194:197], v[200:203], v[140:143]\n"
"ds_read2st64_b32 v[190:191], v9 offset0:128 offset1:129\n"
"s_waitcnt vmcnt(0)\n"
"ds_read_b64_tr_b16 v[118:119], v11 offset:57088\n"
"ds_read_b32 v130, v9 offset:9216\n"
"v_mfma_f32_16x16x32_bf16 v[242:245], v[220:223], v[200:203], v[242:245]\n"
"ds_read_b64_tr_b16 v[116:117], v11 offset:56320\n"
"ds_read_b64_tr_b16 v[120:121], v11 offset:56328\n"
"v_mfma_f32_16x16x32_bf16 v[124:127], v[246:249], v[200:203], v[124:127]\n"
"s_waitcnt lgkmcnt(6)\n"
"s_add_u32 s42, s48, 12288\n"
"s_addc_u32 s43, s49, 0\n"
"s_add_u32 m0, s50, 24576\n"
"s_nop 0\n"
"global_load_lds_dwordx4 v6, s[42:43]\n"
"v_mov_b32 v131, 0\n"
"ds_read_b32 v200, v9 offset:33792\n"
"s_waitcnt lgkmcnt(5)\n"
"v_mfma_f32_16x16x32_bf16 v[226:229], v[216:219], v[190:193], v[226:229]\n"
"s_setprio 2\n"
"v_mov_b32 v201, 0\n"
"ds_read2st64_b32 v[202:203], v9 offset0:134 offset1:135\n"
"s_add_u32 m0, s50, 25600\n"
"s_nop 0\n"
"global_load_lds_dwordx4 v7, s[42:43]\n"
"ds_read_b64_tr_b16 v[150:151], v10 offset:57152\n"
"v_mfma_f32_16x16x32_bf16 v[140:143], v[178:181], v[190:193], v[140:143]\n"
"v_mfma_f32_16x16x32_bf16 v[242:245], v[204:207], v[190:193], v[242:245]\n"
"v_mfma_f32_16x16x32_bf16 v[124:127], v[230:233], v[190:193], v[124:127]\n"
"ds_read_b64_tr_b16 v[148:149], v10 offset:56384\n"
"v_mov_b32 v128, 0\n"
"s_add_u32 m0, s50, 26624\n"
"s_nop 0\n"
"global_load_lds_dwordx4 v8, s[42:43]\n"
"v_mov_b32 v129, 0\n"
"s_waitcnt lgkmcnt(2)\n"
"v_mfma_f32_16x16x32_bf16 v[226:229], v[216:219], v[200:203], v[226:229]\n"
"v_mfma_f32_16x16x32_bf16 v[226:229], v[216:219], v[128:131], v[226:229]\n"
"ds_read_b64_tr_b16 v[122:123], v11 offset:57096\n"
"s_nop 6\n"
"v_rcp_f32 v227, v226\n"
"v_mfma_f32_16x16x32_bf16 v[124:127], v[234:237], v[200:203], v[124:127]\n"
"ds_read_b128 v[190:193], v12 offset:24576\n"
"v_add_u32 v226, 0xd4800000, v226\n"
"v_mfma_f32_16x16x32_bf16 v[124:127], v[248:251], v[128:131], v[124:127]\n"
"s_waitcnt lgkmcnt(1)\n"
"s_add_u32 s42, s48, 49152\n"
"s_addc_u32 s43, s49, 0\n"
"s_add_u32 m0, s50, 27648\n"
"s_nop 0\n"
"global_load_lds_dwordx4 v6, s[42:43]\n"
"v_mfma_f32_16x16x32_bf16 v[140:143], v[182:185], v[200:203], v[140:143]\n"
"s_nop 0\n"
"v_mul_f32 v127, v127, v227\n"
"v_mul_f32 v126, v126, v227\n"
"v_mfma_f32_16x16x32_bf16 v[140:143], v[196:199], v[128:131], v[140:143]\n"
"v_cmp_lt_u32 vcc, 0x28000000, v226\n"
"v_mul_f32 v125, v125, v227\n"
"v_mfma_f32_16x16x32_bf16 v[242:245], v[208:211], v[200:203], v[242:245]\n"
"ds_read_b128 v[200:203], v12 offset:26624\n"
"v_mfma_f32_16x16x32_bf16 v[242:245], v[222:225], v[128:131], v[242:245]\n"
"s_waitcnt lgkmcnt(1)\n"
"ds_read_b128 v[216:219], v13 offset:24576\n"
"v_mfma_f32_16x16x32_bf16 v[128:131], v[190:193], v[28:31], 0\n"
"v_mul_f32 v141, v141, v227\n"
"v_mul_f32 v124, v124, v227\n"
"s_or_b64 s[62:63], s[62:63], vcc\n"
"s_add_u32 m0, s50, 28672\n"
"s_nop 0\n"
"global_load_lds_dwordx4 v7, s[42:43]\n"
"v_cvt_pk_bf16_f32 v236, v124, v125\n"
"s_add_u32 m0, s50, 29696\n"
"s_nop 0\n"
"global_load_lds_dwordx4 v8, s[42:43]\n"
"v_cvt_pk_bf16_f32 v237, v126, v127\n"
"v_mfma_f32_16x16x32_bf16 v[132:135], v[190:193], v[32:35], 0\n"
"s_andn2_b32 s41, 608, s55\n"
"v_add_u32 v8, s41, v15\n"
"v_mul_f32 v143, v143, v227\n"
"v_mfma_f32_16x16x32_bf16 v[152:155], v[190:193], v[24:27], 0\n"
"s_waitcnt lgkmcnt(0)\n"
"ds_read_u16 v8, v8 offset:288\n"
"v_mul_f32 v140, v140, v227\n"
"v_mfma_f32_16x16x32_bf16 v[128:131], v[216:219], v[40:43], v[128:131]\n"
"ds_read_b128 v[190:193], v13 offset:26624\n"
"v_mul_f32 v142, v142, v227\n"
"v_mfma_f32_16x16x32_bf16 v[132:135], v[216:219], v[44:47], v[132:135]\n"
"v_cvt_pk_bf16_f32 v140, v140, v141\n"
"v_mfma_f32_16x16x32_bf16 v[152:155], v[216:219], v[36:39], v[152:155]\n"
"ds_read_b128 v[216:219], v12 offset:28672\n"
"v_mul_f32 v243, v243, v227\n"
"v_mfma_f32_16x16x32_bf16 v[128:131], v[200:203], v[48:51], v[128:131]\n"
"s_waitcnt lgkmcnt(2)\n"
"v_mad_u32_u24 v8, v8, s40, v16\n"
"v_mul_f32 v242, v242, v227\n"
"v_mfma_f32_16x16x32_bf16 v[132:135], v[200:203], v[52:55], v[132:135]\n"
"v_mul_f32 v244, v244, v227\n"
"v_mul_f32 v245, v245, v227\n"
"v_mfma_f32_16x16x32_bf16 v[152:155], v[200:203], v[56:59], v[152:155]\n"
"s_waitcnt lgkmcnt(1)\n"
"ds_read_b128 v[200:203], v13 offset:28672\n"
"v_cvt_pk_bf16_f32 v141, v142, v143\n"
"v_mfma_f32_16x16x32_bf16 v[128:131], v[190:193], v[60:63], v[128:131]\n"
"s_add_u32 m0, s51, 114688\n"
"s_nop 0\n"
"global_load_lds_dwordx4 v8, s[56:57] nt\n"
"v_mfma_f32_16x16x32_bf16 v[132:135], v[190:193], v[64:67], v[132:135]\n"
"v_cvt_pk_bf16_f32 v143, v244, v245\n"
"v_mfma_f32_16x16x32_bf16 v[152:155], v[190:193], v[68:71], v[152:155]\n"
"s_waitcnt lgkmcnt(1)\n"
"s_setprio 0\n"
"ds_read_b128 v[190:193], v12 offset:30720\n"
"v_mfma_f32_16x16x32_bf16 v[128:131], v[216:219], v[72:75], v[128:131]\n"
"v_mfma_f32_16x16x32_bf16 v[132:135], v[216:219], v[76:79], v[132:135]\n"
"v_mfma_f32_16x16x32_bf16 v[152:155], v[216:219], v[80:83], v[152:155]\n"
"s_waitcnt lgkmcnt(1)\n"
"v_mfma_f32_16x16x32_bf16 v[128:131], v[200:203], v[84:87], v[128:131]\n"
"v_cvt_pk_bf16_f32 v142, v242, v243\n"
"v_mfma_f32_16x16x32_bf16 v[132:135], v[200:203], v[88:91], v[132:135]\n"
"s_add_u32 m0, s51, 116736\n"
"s_nop 0\n"
"global_load_lds_dwordx4 v8, s[58:59] nt\n"
"v_mfma_f32_16x16x32_bf16 v[152:155], v[200:203], v[92:95], v[152:155]\n"
"s_waitcnt lgkmcnt(0)\n"
"ds_read_b128 v[200:203], v12 offset:32768\n"
"v_mfma_f32_16x16x32_bf16 v[156:159], v[190:193], v[28:31], 0\n"
"ds_read_b128 v[226:229], v13 offset:30720\n"
"v_mfma_f32_16x16x32_bf16 v[160:163], v[190:193], v[32:35], 0\n"
"s_add_u32 m0, s51, 118784\n"
"s_nop 0\n"
"global_load_lds_dwordx4 v8, s[60:61] nt\n"
"v_mfma_f32_16x16x32_bf16 v[164:167], v[190:193], v[24:27], 0\n"
"s_waitcnt vmcnt(21)\n"
"s_barrier\n"
"s_waitcnt vmcnt(6)\n"
"ds_read_b64_tr_b16 v[6:7], v11 offset:54016\n"
"v_cvt_pk_bf16_f32 v242, v152, v153\n"
"ds_read_b128 v[168:171], v13 offset:32768\n"
"s_waitcnt lgkmcnt(2)\n"
"v_mfma_f32_16x16x32_bf16 v[156:159], v[226:229], v[40:43], v[156:159]\n"
"v_cvt_pk_bf16_f32 v243, v154, v155\n"
"v_cvt_pk_bf16_f32 v190, v128, v129\n"
"ds_read_b64_tr_b16 v[4:5], v11 offset:53248\n"
"v_cvt_pk_bf16_f32 v216, v132, v133\n"
"v_mfma_f32_16x16x32_bf16 v[160:163], v[226:229], v[44:47], v[160:163]\n"
"s_mov_b32 m0, s54\n"
"v_mfma_f32_16x16x32_bf16 v[164:167], v[226:229], v[36:39], v[164:167]\n"
"v_cvt_pk_bf16_f32 v191, v130, v131\n"
"ds_read_b64_tr_b16 v[226:227], v10 offset:53312\n"
"ds_read_b64_tr_b16 v[126:127], v11 offset:54024\n"
"v_mfma_f32_16x16x32_bf16 v[156:159], v[200:203], v[48:51], v[156:159]\n"
"v_cvt_pk_bf16_f32 v217, v134, v135\n"
"ds_read_b64_tr_b16 v[124:125], v11 offset:53256\n"
"ds_read_b64_tr_b16 v[228:229], v10 offset:54080\n"
"v_mfma_f32_16x16x32_bf16 v[160:163], v[200:203], v[52:55], v[160:163]\n"
"s_waitcnt vmcnt(3)\n"
"ds_read_b64_tr_b16 v[132:133], v11 offset:56328\n"
"ds_read_b64_tr_b16 v[128:129], v11 offset:56320\n"
"ds_read_b64_tr_b16 v[152:153], v10 offset:56384\n"
"v_mfma_f32_16x16x32_bf16 v[164:167], v[200:203], v[56:59], v[164:167]\n"
"s_waitcnt lgkmcnt(8)\n"
"ds_read_b64_tr_b16 v[154:155], v10 offset:57152\n"
"ds_read_b64_tr_b16 v[134:135], v11 offset:57096\n"
"v_mfma_f32_16x16x32_bf16 v[156:159], v[168:171], v[60:63], v[156:159]\n"
"s_and_b32 s41, 480, s55\n"
"v_add_u32 v8, s41, v15\n"
"ds_read_b64_tr_b16 v[130:131], v11 offset:57088\n"
"ds_write_addtid_b32 v236 offset:33280\n"
"v_mfma_f32_16x16x32_bf16 v[160:163], v[168:171], v[64:67], v[160:163]\n"
"ds_read_b128 v[200:203], v12 offset:34816\n"
"ds_write_addtid_b32 v141 offset:32512\n"
"v_mfma_f32_16x16x32_bf16 v[164:167], v[168:171], v[68:71], v[164:167]\n"
"ds_write_addtid_b32 v140 offset:32256\n"
"ds_read_u16 v8, v8 offset:448\n"
"ds_write_addtid_b32 v237 offset:33536\n"
"ds_write_addtid_b32 v143 offset:33024\n"
"s_waitcnt lgkmcnt(7)\n"
"ds_write_b128 v17, v[252:255] offset:53248\n"
"ds_read_b128 v[252:255], v13 offset:34816\n"
"s_waitcnt lgkmcnt(7)\n"
"v_mfma_f32_16x16x32_bf16 v[156:159], v[200:203], v[72:75], v[156:159]\n"
"ds_write_addtid_b32 v142 offset:32768\n"
"ds_write_b128 v17, v[136:139] offset:54272\n"
"v_mfma_f32_16x16x32_bf16 v[160:163], v[200:203], v[76:79], v[160:163]\n"
"ds_write_b128 v17, v[144:147] offset:55296\n"
"v_mfma_f32_16x16x32_bf16 v[164:167], v[200:203], v[80:83], v[164:167]\n"
"s_waitcnt lgkmcnt(3)\n"
"v_mov_b32 v137, 0x3f803f80\n"
"v_mov_b32 v136, 0x3f803f80\n"
"v_mfma_f32_16x16x32_bf16 v[156:159], v[252:255], v[84:87], v[156:159]\n"
"s_waitcnt lgkmcnt(2)\n"
"s_waitcnt vmcnt(0)\n"
"s_barrier\n"
"v_mfma_f32_16x16x32_bf16 v[160:163], v[252:255], v[88:91], v[160:163]\n"
"ds_read2st64_b32 v[140:141], v9 offset0:120 offset1:121\n"
"v_mfma_f32_16x16x32_bf16 v[164:167], v[252:255], v[92:95], v[164:167]\n"
"ds_write_b128 v17, v[148:151] offset:56320\n"
"v_mov_b32 v138, 0x3f803f80\n"
"v_mad_u32_u24 v8, v8, s40, v16\n"
"s_nop 4\n"
"v_cvt_pk_bf16_f32 v252, v164, v165\n"
"v_mov_b32 v139, 0x3f803f80\n"
"ds_write_b128 v17, v[226:229] offset:57344\n"
"v_cvt_pk_bf16_f32 v226, v160, v161\n"
"v_cvt_pk_bf16_f32 v200, v156, v157\n"
"ds_read2st64_b32 v[150:151], v9 offset0:128 offset1:129\n"
"s_add_u32 m0, s51, 126976\n"
"s_nop 0\n"
"global_load_lds_dwordx4 v8, s[56:57] nt\n"
"ds_read2st64_b32 v[144:145], v9 offset0:124 offset1:125\n"
"ds_read2st64_b32 v[142:143], v9 offset0:122 offset1:123\n"
"v_cvt_pk_bf16_f32 v201, v158, v159\n"
"ds_write_b128 v17, v[152:155] offset:58368\n"
"s_waitcnt lgkmcnt(1)\n"
"v_mfma_f32_16x16x32_bf16 v[156:159], v[136:139], v[140:143], 0\n"
"ds_read2st64_b32 v[146:147], v9 offset0:126 offset1:127\n"
"v_cvt_pk_bf16_f32 v227, v162, v163\n"
"v_mfma_f32_16x16x32_bf16 v[160:163], v[186:189], v[140:143], 0\n"
"s_add_u32 m0, s51, 129024\n"
"s_nop 0\n"
"global_load_lds_dwordx4 v8, s[58:59] nt\n"
"ds_read2st64_b32 v[152:153], v9 offset0:130 offset1:131\n"
"v_cvt_pk_bf16_f32 v253, v166, v167\n"
"v_mfma_f32_16x16x32_bf16 v[164:167], v[212:215], v[140:143], 0\n"
"ds_read2st64_b32 v[172:173], v14 offset0:150 offset1:151\n"
"v_mfma_f32_16x16x32_bf16 v[168:171], v[238:241], v[140:143], 0\n"
"s_waitcnt lgkmcnt(2)\n"
"s_setprio 2\n"
"v_mfma_f32_16x16x32_bf16 v[156:159], v[136:139], v[144:147], v[156:159]\n"
"s_waitcnt lgkmcnt(1)\n"
"v_mov_b32 v155, 0\n"
"v_mov_b32 v154, 0\n"
"v_mfma_f32_16x16x32_bf16 v[156:159], v[136:139], v[150:153], v[156:159]\n"
"v_mov_b32 v148, 0\n"
"v_mfma_f32_16x16x32_bf16 v[168:171], v[250:253], v[144:147], v[168:171]\n"
"v_mov_b32 v149, 0\n"
"s_add_u32 m0, s51, 131072\n"
"s_nop 0\n"
"global_load_lds_dwordx4 v8, s[60:61] nt\n"
"s_and_b32 s41, 32, s55\n"
"v_add_u32 v255, s41, v15\n"
"v_mfma_f32_16x16x32_bf16 v[168:171], v[240:243], v[148:151], v[168:171]\n"
"ds_read_b128 v[140:143], v17 offset:53248\n"
"ds_read2st64_b32 v[138:139], v14 offset0:128 offset1:129\n"
"v_mfma_f32_16x16x32_bf16 v[168:171], v[248:251], v[152:155], v[168:171]\n"
"v_mfma_f32_16x16x32_bf16 v[160:163], v[198:201], v[144:147], v[160:163]\n"
"v_rcp_f32 v8, v156\n"
"v_mfma_f32_16x16x32_bf16 v[160:163], v[188:191], v[148:151], v[160:163]\n"
"ds_read2st64_b32 v[136:137], v14 offset0:126 offset1:127\n"
"v_mfma_f32_16x16x32_bf16 v[160:163], v[196:199], v[152:155], v[160:163]\n"
"v_add_u32 v156, 0xd4800000, v156\n"
"v_mfma_f32_16x16x32_bf16 v[164:167], v[224:227], v[144:147], v[164:167]\n"
"v_cmp_lt_u32 vcc, 0x28000000, v156\n"
"v_mul_f32 v169, v169, v8\n"
"v_mfma_f32_16x16x32_bf16 v[164:167], v[214:217], v[148:151], v[164:167]\n"
"ds_read2st64_b32 v[174:175], v14 offset0:152 offset1:153\n"
"v_mul_f32 v168, v168, v8\n"
"ds_read_b128 v[148:151], v17 offset:54272\n"
"ds_read_u16 v255, v255 offset:480\n"
"v_mfma_f32_16x16x32_bf16 v[164:167], v[222:225], v[152:155], v[164:167]\n"
"s_waitcnt lgkmcnt(3)\n"
"v_mul_f32 v161, v161, v8\n"
"v_cvt_pk_bf16_f32 v244, v168, v169\n"
"v_mfma_f32_16x16x32_bf16 v[144:147], v[96:99], v[136:139], 0\n"
"ds_read2st64_b32 v[158:159], v14 offset0:176 offset1:177\n"
"v_mfma_f32_16x16x32_bf16 v[152:155], v[100:103], v[136:139], 0\n"
"s_or_b64 s[62:63], s[62:63], vcc\n"
"ds_read2st64_b32 v[156:157], v14 offset0:174 offset1:175\n"
"v_mul_f32 v171, v171, v8\n"
"v_mul_f32 v170, v170, v8\n"
"v_cvt_pk_bf16_f32 v245, v170, v171\n"
"v_mfma_f32_16x16x32_bf16 v[168:171], v[140:143], v[136:139], 0\n"
"s_waitcnt lgkmcnt(4)\n"
"ds_read2st64_b32 v[140:141], v14 offset0:198 offset1:199\n"
"v_mul_f32 v160, v160, v8\n"
"v_mfma_f32_16x16x32_bf16 v[144:147], v[104:107], v[172:175], v[144:147]\n"
"s_waitcnt lgkmcnt(3)\n"
"v_mul_f32 v165, v165, v8\n"
"v_mad_u32_u24 v255, v255, s40, v16\n"
"v_mfma_f32_16x16x32_bf16 v[152:155], v[108:111], v[172:175], v[152:155]\n"
"v_mul_f32 v162, v162, v8\n"
"s_add_u32 m0, s51, 139264\n"
"s_nop 0\n"
"global_load_lds_dwordx4 v255, s[56:57] nt\n"
"v_mfma_f32_16x16x32_bf16 v[168:171], v[148:151], v[172:175], v[168:171]\n"
"s_waitcnt lgkmcnt(1)\n"
"ds_read2st64_b32 v[142:143], v14 offset0:200 offset1:201\n"
"v_mul_f32 v164, v164, v8\n"
"v_mfma_f32_16x16x32_bf16 v[144:147], v[0:3], v[156:159], v[144:147]\n"
"ds_read_b128 v[148:151], v17 offset:56320\n"
"ds_read_b128 v[136:139], v17 offset:55296\n"
"v_cvt_pk_bf16_f32 v160, v160, v161\n"
"ds_read2st64_b32 v[172:173], v14 offset0:130 offset1:131\n"
"ds_read2st64_b32 v[174:175], v14 offset0:154 offset1:155\n"
"v_mfma_f32_16x16x32_bf16 v[152:155], v[112:115], v[156:159], v[152:155]\n"
"s_setprio 0\n"
"v_mul_f32 v166, v166, v8\n"
"s_add_u32 m0, s51, 141312\n"
"s_nop 0\n"
"global_load_lds_dwordx4 v255, s[58:59] nt\n"
"v_mul_f32 v163, v163, v8\n"
"v_mul_f32 v167, v167, v8\n"
"v_cvt_pk_bf16_f32 v161, v162, v163\n"
"v_cvt_pk_bf16_f32 v162, v164, v165\n"
"s_waitcnt lgkmcnt(2)\n"
"v_mfma_f32_16x16x32_bf16 v[168:171], v[136:139], v[156:159], v[168:171]\n"
"ds_read2st64_b32 v[158:159], v14 offset0:202 offset1:203\n"
"v_mfma_f32_16x16x32_bf16 v[144:147], v[116:119], v[140:143], v[144:147]\n"
"ds_read_b128 v[136:139], v17 offset:57344\n"
"v_mfma_f32_16x16x32_bf16 v[152:155], v[120:123], v[140:143], v[152:155]\n"
"ds_read2st64_b32 v[156:157], v14 offset0:178 offset1:179\n"
"v_mfma_f32_16x16x32_bf16 v[168:171], v[148:151], v[140:143], v[168:171]\n"
"s_waitcnt lgkmcnt(3)\n"
"ds_read_b128 v[140:143], v17 offset:58368\n"
"v_mfma_f32_16x16x32_bf16 v[144:147], v[4:7], v[172:175], v[144:147]\n"
"v_cvt_pk_bf16_f32 v163, v166, v167\n"
"v_mfma_f32_16x16x32_bf16 v[152:155], v[124:127], v[172:175], v[152:155]\n"
"s_waitcnt lgkmcnt(2)\n"
"v_mfma_f32_16x16x32_bf16 v[168:171], v[136:139], v[172:175], v[168:171]\n"
"s_waitcnt lgkmcnt(1)\n"
"ds_read_b128 v[148:151], v12 offset:49152\n"
"v_mfma_f32_16x16x32_bf16 v[144:147], v[128:131], v[156:159], v[144:147]\n"
"s_add_u32 m0, s51, 143360\n"
"s_nop 0\n"
"global_load_lds_dwordx4 v255, s[60:61] nt\n"
"v_mfma_f32_16x16x32_bf16 v[152:155], v[132:135], v[156:159], v[152:155]\n"
"s_barrier\n"
"s_waitcnt lgkmcnt(1)\n"
"v_mfma_f32_16x16x32_bf16 v[168:171], v[140:143], v[156:159], v[168:171]\n"
"ds_read_b128 v[140:143], v12 offset:51200\n"
"s_mov_b32 m0, s54\n"
"v_cmp_class_f32_e64 vcc, v144, s64\n"
"ds_read_b128 v[136:139], v13 offset:49152\n"
"s_waitcnt lgkmcnt(2)\n"
"v_mfma_f32_16x16x32_bf16 v[156:159], v[148:151], v[28:31], 0\n"
"v_cvt_pk_bf16_f32 v144, v144, v145\n"
"v_cvt_pk_bf16_f32 v145, v146, v147\n"
"ds_write_addtid_b32 v244 offset:33280\n"
"v_mfma_f32_16x16x32_bf16 v[164:167], v[148:151], v[32:35], 0\n"
"v_cvt_pk_bf16_f32 v146, v152, v153\n"
"v_mfma_f32_16x16x32_bf16 v[172:175], v[148:151], v[24:27], 0\n"
"ds_write_addtid_b32 v245 offset:33536\n"
"s_waitcnt lgkmcnt(2)\n"
"v_mfma_f32_16x16x32_bf16 v[156:159], v[136:139], v[40:43], v[156:159]\n"
"ds_read_b128 v[148:151], v13 offset:51200\n"
"v_mfma_f32_16x16x32_bf16 v[164:167], v[136:139], v[44:47], v[164:167]\n"
"v_mfma_f32_16x16x32_bf16 v[172:175], v[136:139], v[36:39], v[172:175]\n"
"ds_write_addtid_b32 v161 offset:32512\n"
"v_mfma_f32_16x16x32_bf16 v[156:159], v[140:143], v[48:51], v[156:159]\n"
"ds_write_addtid_b32 v163 offset:33024\n"
"s_or_b64 s[62:63], s[62:63], vcc\n"
"v_mfma_f32_16x16x32_bf16 v[164:167], v[140:143], v[52:55], v[164:167]\n"
"v_cvt_pk_bf16_f32 v168, v168, v169\n"
"ds_read_b128 v[136:139], v12 offset:53248\n"
"v_mfma_f32_16x16x32_bf16 v[172:175], v[140:143], v[56:59], v[172:175]\n"
"ds_read_b128 v[140:143], v13 offset:53248\n"
"ds_write_addtid_b32 v162 offset:32768\n"
"s_waitcnt lgkmcnt(5)\n"
"v_mfma_f32_16x16x32_bf16 v[156:159], v[148:151], v[60:63], v[156:159]\n"
"ds_write_addtid_b32 v160 offset:32256\n"
"v_mfma_f32_16x16x32_bf16 v[164:167], v[148:151], v[64:67], v[164:167]\n"
"s_mov_b32 m0, s68\n"
"s_nop 0\n"
"ds_write_addtid_b32 v146 offset:45568\n"
"v_mfma_f32_16x16x32_bf16 v[172:175], v[148:151], v[68:71], v[172:175]\n"
"s_waitcnt lgkmcnt(4)\n"
"v_cvt_pk_bf16_f32 v147, v154, v155\n"
"v_cvt_pk_bf16_f32 v169, v170, v171\n"
"v_mfma_f32_16x16x32_bf16 v[156:159], v[136:139], v[72:75], v[156:159]\n"
"ds_read_b128 v[148:151], v12 offset:55296\n"
"v_mfma_f32_16x16x32_bf16 v[164:167], v[136:139], v[76:79], v[164:167]\n"
"ds_write_addtid_b32 v145 offset:45312\n"
"v_mfma_f32_16x16x32_bf16 v[172:175], v[136:139], v[80:83], v[172:175]\n"
"s_waitcnt lgkmcnt(5)\n"
"ds_read_b128 v[136:139], v13 offset:55296\n"
"v_mfma_f32_16x16x32_bf16 v[156:159], v[140:143], v[84:87], v[156:159]\n"
"s_setprio 2\n"
"ds_write_addtid_b32 v169 offset:46336\n"
"v_mfma_f32_16x16x32_bf16 v[164:167], v[140:143], v[88:91], v[164:167]\n"
"ds_write_addtid_b32 v168 offset:46080\n"
"v_mfma_f32_16x16x32_bf16 v[172:175], v[140:143], v[92:95], v[172:175]\n"
"ds_write_addtid_b32 v147 offset:45824\n"
"ds_write_addtid_b32 v144 offset:45056\n"
"s_waitcnt lgkmcnt(6)\n"
"v_mfma_f32_16x16x32_bf16 v[144:147], v[148:151], v[28:31], 0\n"
"ds_read_b128 v[140:143], v12 offset:57344\n"
"v_cvt_pk_bf16_f32 v185, v158, v159\n"
"v_mfma_f32_16x16x32_bf16 v[152:155], v[148:151], v[32:35], 0\n"
"v_cvt_pk_bf16_f32 v184, v156, v157\n"
"v_mfma_f32_16x16x32_bf16 v[156:159], v[148:151], v[24:27], 0\n"
"s_waitcnt lgkmcnt(5)\n"
"ds_read_b128 v[148:151], v13 offset:57344\n"
"v_cvt_pk_bf16_f32 v210, v164, v165\n"
"v_cvt_pk_bf16_f32 v211, v166, v167\n"
"v_mfma_f32_16x16x32_bf16 v[144:147], v[136:139], v[40:43], v[144:147]\n"
"v_cvt_pk_bf16_f32 v236, v172, v173\n"
"s_setprio 0\n"
"v_mfma_f32_16x16x32_bf16 v[152:155], v[136:139], v[44:47], v[152:155]\n"
"v_mfma_f32_16x16x32_bf16 v[156:159], v[136:139], v[36:39], v[156:159]\n"
"s_waitcnt lgkmcnt(1)\n"
"ds_read_b128 v[136:139], v12 offset:59392\n"
"v_mfma_f32_16x16x32_bf16 v[144:147], v[140:143], v[48:51], v[144:147]\n"
"v_cvt_pk_bf16_f32 v237, v174, v175\n"
"v_mfma_f32_16x16x32_bf16 v[152:155], v[140:143], v[52:55], v[152:155]\n"
"v_mfma_f32_16x16x32_bf16 v[156:159], v[140:143], v[56:59], v[156:159]\n"
"s_waitcnt lgkmcnt(1)\n"
"ds_read_b128 v[140:143], v13 offset:59392\n"
"v_mfma_f32_16x16x32_bf16 v[144:147], v[148:151], v[60:63], v[144:147]\n"
"v_mfma_f32_16x16x32_bf16 v[152:155], v[148:151], v[64:67], v[152:155]\n"
"v_mfma_f32_16x16x32_bf16 v[156:159], v[148:151], v[68:71], v[156:159]\n"
"s_waitcnt lgkmcnt(1)\n"
"v_mov_b32 v151, 0x3f803f80\n"
"v_mfma_f32_16x16x32_bf16 v[144:147], v[136:139], v[72:75], v[144:147]\n"
"v_mov_b32 v150, 0x3f803f80\n"
"v_mfma_f32_16x16x32_bf16 v[152:155], v[136:139], v[76:79], v[152:155]\n"
"v_mov_b32 v149, 0x3f803f80\n"
"v_mfma_f32_16x16x32_bf16 v[156:159], v[136:139], v[80:83], v[156:159]\n"
"s_waitcnt lgkmcnt(0)\n"
"v_mfma_f32_16x16x32_bf16 v[144:147], v[140:143], v[84:87], v[144:147]\n"
"s_waitcnt vmcnt(3)\n"
"s_barrier\n"
"v_mfma_f32_16x16x32_bf16 v[152:155], v[140:143], v[88:91], v[152:155]\n"
"ds_read2st64_b32 v[136:137], v9 offset0:112 offset1:113\n"
"v_mfma_f32_16x16x32_bf16 v[156:159], v[140:143], v[92:95], v[156:159]\n"
"ds_read2st64_b32 v[138:139], v9 offset0:114 offset1:115\n"
"ds_read2st64_b32 v[162:163], v20 offset0:2 offset1:3\n"
"s_nop 0\n"
"v_cvt_pk_bf16_f32 v193, v146, v147\n"
"s_nop 3\n"
"v_cvt_pk_bf16_f32 v245, v158, v159\n"
"ds_read_u16 v255, v21 offset:800\n"
"v_mov_b32 v148, 0x3f803f80\n"
"s_and_b32 s41, 416, s55\n"
"v_add_u32 v8, s41, v15\n"
"v_cvt_pk_bf16_f32 v244, v156, v157\n"
"ds_read2st64_b32 v[160:161], v20 offset0:0 offset1:1\n"
"ds_read_u16 v8, v8 offset:544\n"
"v_cvt_pk_bf16_f32 v192, v144, v145\n"
"s_waitcnt lgkmcnt(4)\n"
"v_mfma_f32_16x16x32_bf16 v[144:147], v[148:151], v[136:139], 0\n"
"ds_read2st64_b32 v[140:141], v9 offset0:116 offset1:117\n"
"v_cvt_pk_bf16_f32 v218, v152, v153\n"
"v_cvt_pk_bf16_f32 v219, v154, v155\n"
"v_mfma_f32_16x16x32_bf16 v[152:155], v[180:183], v[136:139], 0\n"
"ds_read2st64_b32 v[142:143], v9 offset0:118 offset1:119\n"
"v_mfma_f32_16x16x32_bf16 v[156:159], v[206:209], v[136:139], 0\n"
"s_waitcnt lgkmcnt(2)\n"
"v_mad_u32_u24 v8, v8, s40, v16\n"
"v_mfma_f32_16x16x32_bf16 v[164:167], v[232:235], v[136:139], 0\n"
"ds_read2st64_b32 v[136:137], v9 offset0:128 offset1:129\n"
"s_add_u32 m0, s51, 151552\n"
"s_nop 0\n"
"global_load_lds_dwordx4 v8, s[56:57] nt\n"
"s_waitcnt lgkmcnt(1)\n"
"v_mfma_f32_16x16x32_bf16 v[144:147], v[148:151], v[140:143], v[144:147]\n"
"ds_read2st64_b32 v[228:229], v20 offset0:4 offset1:5\n"
"v_mfma_f32_16x16x32_bf16 v[152:155], v[188:191], v[140:143], v[152:155]\n"
"ds_read2st64_b32 v[138:139], v9 offset0:130 offset1:131\n"
"v_mfma_f32_16x16x32_bf16 v[156:159], v[214:217], v[140:143], v[156:159]\n"
"v_mad_u32_u24 v254, v255, s40, v18\n"
"s_setprio 2\n"
"v_mfma_f32_16x16x32_bf16 v[164:167], v[240:243], v[140:143], v[164:167]\n"
"global_store_dwordx4 v254, v[160:163], s[66:67] sc1\n"
"v_mad_u32_u24 v255, v255, s40, v19\n"
"ds_read2st64_b32 v[140:141], v9 offset0:132 offset1:133\n"
"s_waitcnt lgkmcnt(1)\n"
"v_mfma_f32_16x16x32_bf16 v[144:147], v[148:151], v[136:139], v[144:147]\n"
"v_mov_b32 v160, 0\n"
"s_add_u32 m0, s51, 153600\n"
"s_nop 0\n"
"global_load_lds_dwordx4 v8, s[58:59] nt\n"
"ds_read2st64_b32 v[142:143], v9 offset0:134 offset1:135\n"
"v_mfma_f32_16x16x32_bf16 v[152:155], v[184:187], v[136:139], v[152:155]\n"
"ds_read2st64_b32 v[162:163], v9 offset0:36 offset1:37\n"
"v_mov_b32 v161, 0\n"
"v_mfma_f32_16x16x32_bf16 v[156:159], v[210:213], v[136:139], v[156:159]\n"
"global_store_dwordx2 v255, v[228:229], s[66:67] offset:256 sc1\n"
"v_mfma_f32_16x16x32_bf16 v[164:167], v[236:239], v[136:139], v[164:167]\n"
"s_waitcnt lgkmcnt(1)\n"
"ds_read2st64_b32 v[138:139], v14 offset0:128 offset1:129\n"
"v_mfma_f32_16x16x32_bf16 v[144:147], v[148:151], v[140:143], v[144:147]\n"
"s_waitcnt lgkmcnt(1)\n"
"v_mfma_f32_16x16x32_bf16 v[144:147], v[148:151], v[160:163], v[144:147]\n"
"v_mfma_f32_16x16x32_bf16 v[164:167], v[248:251], v[140:143], v[164:167]\n"
"ds_read2st64_b32 v[136:137], v14 offset0:126 offset1:127\n"
"s_add_u32 m0, s51, 155648\n"
"s_nop 0\n"
"global_load_lds_dwordx4 v8, s[60:61] nt\n"
"v_mfma_f32_16x16x32_bf16 v[164:167], v[250:253], v[160:163], v[164:167]\n"
"ds_read_b128 v[148:151], v17 offset:53248\n"
"v_mfma_f32_16x16x32_bf16 v[152:155], v[196:199], v[140:143], v[152:155]\n"
"v_rcp_f32 v8, v144\n"
"v_mfma_f32_16x16x32_bf16 v[152:155], v[198:201], v[160:163], v[152:155]\n"
"ds_read2st64_b32 v[170:171], v14 offset0:176 offset1:177\n"
"v_add_u32 v144, 0xd4800000, v144\n"
"v_mfma_f32_16x16x32_bf16 v[156:159], v[222:225], v[140:143], v[156:159]\n"
"s_nop 0\n"
"v_mul_f32 v166, v166, v8\n"
"v_mul_f32 v167, v167, v8\n"
"ds_read2st64_b32 v[140:141], v14 offset0:150 offset1:151\n"
"ds_read2st64_b32 v[142:143], v14 offset0:152 offset1:153\n"
"v_mfma_f32_16x16x32_bf16 v[156:159], v[224:227], v[160:163], v[156:159]\n"
"s_waitcnt lgkmcnt(4)\n"
"v_mul_f32 v165, v165, v8\n"
"v_cmp_lt_u32 vcc, 0x28000000, v144\n"
"v_mfma_f32_16x16x32_bf16 v[144:147], v[96:99], v[136:139], 0\n"
"v_mul_f32 v164, v164, v8\n"
"v_cvt_pk_bf16_f32 v254, v164, v165\n"
"ds_read_b128 v[160:163], v17 offset:54272\n"
"v_cvt_pk_bf16_f32 v255, v166, v167\n"
"v_mfma_f32_16x16x32_bf16 v[164:167], v[100:103], v[136:139], 0\n"
"s_waitcnt lgkmcnt(4)\n"
"ds_read2st64_b32 v[168:169], v14 offset0:174 offset1:175\n"
"v_mul_f32 v152, v152, v8\n"
"v_mfma_f32_16x16x32_bf16 v[172:175], v[148:151], v[136:139], 0\n"
"s_waitcnt lgkmcnt(2)\n"
"ds_read_b128 v[136:139], v17 offset:55296\n"
"v_mul_f32 v154, v154, v8\n"
"v_mfma_f32_16x16x32_bf16 v[144:147], v[104:107], v[140:143], v[144:147]\n"
"v_mul_f32 v153, v153, v8\n"
"v_mul_f32 v155, v155, v8\n"
"ds_read2st64_b32 v[148:149], v14 offset0:198 offset1:199\n"
"v_mul_f32 v159, v159, v8\n"
"v_mfma_f32_16x16x32_bf16 v[164:167], v[108:111], v[140:143], v[164:167]\n"
"s_waitcnt lgkmcnt(3)\n"
"ds_read2st64_b32 v[150:151], v14 offset0:200 offset1:201\n"
"v_mul_f32 v156, v156, v8\n"
"v_mfma_f32_16x16x32_bf16 v[172:175], v[160:163], v[140:143], v[172:175]\n"
"s_waitcnt lgkmcnt(3)\n"
"ds_read_b128 v[140:143], v17 offset:56320\n"
"v_cvt_pk_bf16_f32 v152, v152, v153\n"
"v_cvt_pk_bf16_f32 v153, v154, v155\n"
"v_mfma_f32_16x16x32_bf16 v[144:147], v[0:3], v[168:171], v[144:147]\n"
"v_mul_f32 v157, v157, v8\n"
"ds_read2st64_b32 v[162:163], v14 offset0:202 offset1:203\n"
"v_cvt_pk_bf16_f32 v154, v156, v157\n"
"v_mfma_f32_16x16x32_bf16 v[164:167], v[112:115], v[168:171], v[164:167]\n"
"s_or_b64 s[62:63], s[62:63], vcc\n"
"v_mul_f32 v158, v158, v8\n"
"v_cvt_pk_bf16_f32 v155, v158, v159\n"
"ds_read2st64_b32 v[158:159], v14 offset0:154 offset1:155\n"
"s_waitcnt lgkmcnt(5)\n"
"v_mfma_f32_16x16x32_bf16 v[172:175], v[136:139], v[168:171], v[172:175]\n"
"s_setprio 0\n"
"ds_read_b128 v[136:139], v17 offset:57344\n"
"ds_read2st64_b32 v[156:157], v14 offset0:130 offset1:131\n"
"ds_read2st64_b32 v[160:161], v14 offset0:178 offset1:179\n"
"s_waitcnt lgkmcnt(6)\n"
"v_mfma_f32_16x16x32_bf16 v[144:147], v[116:119], v[148:151], v[144:147]\n"
"v_mfma_f32_16x16x32_bf16 v[164:167], v[120:123], v[148:151], v[164:167]\n"
"s_waitcnt lgkmcnt(5)\n"
"v_mfma_f32_16x16x32_bf16 v[172:175], v[140:143], v[148:151], v[172:175]\n"
"s_waitcnt lgkmcnt(1)\n"
"ds_read_b128 v[140:143], v17 offset:58368\n"
"v_mfma_f32_16x16x32_bf16 v[144:147], v[4:7], v[156:159], v[144:147]\n"
"ds_read_b128 v[148:151], v12 offset:18432\n"
"v_mfma_f32_16x16x32_bf16 v[164:167], v[124:127], v[156:159], v[164:167]\n"
"s_mov_b32 m0, s54\n"
"v_mfma_f32_16x16x32_bf16 v[172:175], v[136:139], v[156:159], v[172:175]\n"
"s_waitcnt lgkmcnt(2)\n"
"v_mfma_f32_16x16x32_bf16 v[144:147], v[128:131], v[160:163], v[144:147]\n"
"s_waitcnt vmcnt(5)\n"
"s_barrier\n"
"v_mfma_f32_16x16x32_bf16 v[164:167], v[132:135], v[160:163], v[164:167]\n"
"s_waitcnt lgkmcnt(1)\n"
"ds_read_b128 v[156:159], v12 offset:20480\n"
"v_mfma_f32_16x16x32_bf16 v[172:175], v[140:143], v[160:163], v[172:175]\n"
"ds_write_addtid_b32 v255 offset:33536\n"
"s_nop 0\n"
"v_cmp_class_f32_e64 vcc, v144, s64\n"
"s_waitcnt lgkmcnt(2)\n"
"v_mfma_f32_16x16x32_bf16 v[140:143], v[148:151], v[28:31], 0\n"
"ds_write_addtid_b32 v153 offset:32512\n"
"ds_read_b128 v[136:139], v13 offset:18432\n"
"v_mfma_f32_16x16x32_bf16 v[160:163], v[148:151], v[32:35], 0\n"
"v_cvt_pk_bf16_f32 v144, v144, v145\n"
"v_cvt_pk_bf16_f32 v145, v146, v147\n"
"v_mfma_f32_16x16x32_bf16 v[168:171], v[148:151], v[24:27], 0\n"
"ds_write_addtid_b32 v254 offset:33280\n"
"v_cvt_pk_bf16_f32 v172, v172, v173\n"
"s_waitcnt lgkmcnt(1)\n"
"v_mfma_f32_16x16x32_bf16 v[140:143], v[136:139], v[40:43], v[140:143]\n"
"v_cvt_pk_bf16_f32 v173, v174, v175\n"
"v_cvt_pk_bf16_f32 v146, v164, v165\n"
"v_mfma_f32_16x16x32_bf16 v[160:163], v[136:139], v[44:47], v[160:163]\n"
"v_cvt_pk_bf16_f32 v147, v166, v167\n"
"v_mfma_f32_16x16x32_bf16 v[168:171], v[136:139], v[36:39], v[168:171]\n"
"ds_read_b128 v[136:139], v12 offset:22528\n"
"ds_read_b128 v[148:151], v13 offset:20480\n"
"v_mfma_f32_16x16x32_bf16 v[140:143], v[156:159], v[48:51], v[140:143]\n"
"ds_write_addtid_b32 v154 offset:32768\n"
"v_mfma_f32_16x16x32_bf16 v[160:163], v[156:159], v[52:55], v[160:163]\n"
"ds_write_addtid_b32 v152 offset:32256\n"
"v_mfma_f32_16x16x32_bf16 v[168:171], v[156:159], v[56:59], v[168:171]\n"
"ds_write_addtid_b32 v155 offset:33024\n"
"s_waitcnt lgkmcnt(3)\n"
"v_mfma_f32_16x16x32_bf16 v[140:143], v[148:151], v[60:63], v[140:143]\n"
"s_mov_b32 m0, s68\n"
"ds_read_b128 v[152:155], v13 offset:22528\n"
"ds_write_addtid_b32 v144 offset:45056\n"
"v_mfma_f32_16x16x32_bf16 v[160:163], v[148:151], v[64:67], v[160:163]\n"
"v_mfma_f32_16x16x32_bf16 v[168:171], v[148:151], v[68:71], v[168:171]\n"
"ds_write_addtid_b32 v172 offset:46080\n"
"v_mfma_f32_16x16x32_bf16 v[140:143], v[136:139], v[72:75], v[140:143]\n"
"ds_read_b128 v[148:151], v12 offset:24576\n"
"s_or_b64 s[62:63], s[62:63], vcc\n"
"v_mfma_f32_16x16x32_bf16 v[160:163], v[136:139], v[76:79], v[160:163]\n"
"ds_write_addtid_b32 v145 offset:45312\n"
"v_mfma_f32_16x16x32_bf16 v[168:171], v[136:139], v[80:83], v[168:171]\n"
"ds_write_addtid_b32 v147 offset:45824\n"
"s_waitcnt lgkmcnt(5)\n"
"v_mfma_f32_16x16x32_bf16 v[140:143], v[152:155], v[84:87], v[140:143]\n"
"ds_write_addtid_b32 v146 offset:45568\n"
"v_mfma_f32_16x16x32_bf16 v[160:163], v[152:155], v[88:91], v[160:163]\n"
"ds_write_addtid_b32 v173 offset:46336\n"
"v_mfma_f32_16x16x32_bf16 v[168:171], v[152:155], v[92:95], v[168:171]\n"
"s_waitcnt lgkmcnt(4)\n"
"ds_read_b128 v[152:155], v12 offset:26624\n"
"ds_read_b128 v[136:139], v13 offset:24576\n"
"v_mfma_f32_16x16x32_bf16 v[144:147], v[148:151], v[28:31], 0\n"
"v_cvt_pk_bf16_f32 v203, v142, v143\n"
"v_mfma_f32_16x16x32_bf16 v[156:159], v[148:151], v[32:35], 0\n"
"v_mfma_f32_16x16x32_bf16 v[164:167], v[148:151], v[24:27], 0\n"
"s_waitcnt lgkmcnt(0)\n"
"v_cvt_pk_bf16_f32 v202, v140, v141\n"
"v_mfma_f32_16x16x32_bf16 v[144:147], v[136:139], v[40:43], v[144:147]\n"
"ds_read_b128 v[140:143], v13 offset:26624\n"
"v_mfma_f32_16x16x32_bf16 v[156:159], v[136:139], v[44:47], v[156:159]\n"
"v_mfma_f32_16x16x32_bf16 v[164:167], v[136:139], v[36:39], v[164:167]\n"
"ds_read_b128 v[136:139], v12 offset:28672\n"
"v_mfma_f32_16x16x32_bf16 v[144:147], v[152:155], v[48:51], v[144:147]\n"
"v_cvt_pk_bf16_f32 v255, v170, v171\n"
"v_mfma_f32_16x16x32_bf16 v[156:159], v[152:155], v[52:55], v[156:159]\n"
"v_mfma_f32_16x16x32_bf16 v[164:167], v[152:155], v[56:59], v[164:167]\n"
"s_waitcnt lgkmcnt(1)\n"
"v_mfma_f32_16x16x32_bf16 v[144:147], v[140:143], v[60:63], v[144:147]\n"
"v_cvt_pk_bf16_f32 v229, v162, v163\n"
"v_mfma_f32_16x16x32_bf16 v[156:159], v[140:143], v[64:67], v[156:159]\n"
"ds_read_b128 v[148:151], v13 offset:28672\n"
"v_cvt_pk_bf16_f32 v228, v160, v161\n"
"v_mfma_f32_16x16x32_bf16 v[164:167], v[140:143], v[68:71], v[164:167]\n"
"s_waitcnt lgkmcnt(1)\n"
"v_mfma_f32_16x16x32_bf16 v[144:147], v[136:139], v[72:75], v[144:147]\n"
"v_mov_b32 v143, 0x3f803f80\n"
"v_cvt_pk_bf16_f32 v254, v168, v169\n"
"v_mfma_f32_16x16x32_bf16 v[156:159], v[136:139], v[76:79], v[156:159]\n"
"v_mov_b32 v140, 0x3f803f80\n"
"v_mov_b32 v142, 0x3f803f80\n"
"v_mfma_f32_16x16x32_bf16 v[164:167], v[136:139], v[80:83], v[164:167]\n"
"s_waitcnt lgkmcnt(0)\n"
"v_mov_b32 v141, 0x3f803f80\n"
"v_mfma_f32_16x16x32_bf16 v[144:147], v[148:151], v[84:87], v[144:147]\n"
"s_waitcnt vmcnt(0)\n"
"s_barrier\n"
"ds_read2st64_b32 v[136:137], v9 offset0:120 offset1:121\n"
"v_mfma_f32_16x16x32_bf16 v[156:159], v[148:151], v[88:91], v[156:159]\n"
"ds_read2st64_b32 v[138:139], v9 offset0:122 offset1:123\n"
"v_mfma_f32_16x16x32_bf16 v[164:167], v[148:151], v[92:95], v[164:167]\n"
"s_and_b32 s41, 416, s55\n"
"v_add_u32 v8, s41, v15\n"
"v_cvt_pk_bf16_f32 v186, v144, v145\n"
"ds_read2st64_b32 v[150:151], v9 offset0:126 offset1:127\n"
"s_nop 1\n"
"v_cvt_pk_bf16_f32 v212, v156, v157\n"
"v_cvt_pk_bf16_f32 v187, v146, v147\n"
"ds_read_b32 v149, v9 offset:32000\n"
"ds_read_u16 v8, v8 offset:576\n"
"v_cvt_pk_bf16_f32 v239, v166, v167\n"
"v_cvt_pk_bf16_f32 v213, v158, v159\n"
"v_cvt_pk_bf16_f32 v238, v164, v165\n"
"s_waitcnt lgkmcnt(3)\n"
"v_mfma_f32_16x16x32_bf16 v[144:147], v[140:143], v[136:139], 0\n"
"v_mfma_f32_16x16x32_bf16 v[152:155], v[190:193], v[136:139], 0\n"
"s_waitcnt lgkmcnt(0)\n"
"v_mad_u32_u24 v8, v8, s40, v16\n"
"ds_read_u16 v193, v21 offset:832\n"
"v_mov_b32 v148, 0\n"
"v_mfma_f32_16x16x32_bf16 v[156:159], v[216:219], v[136:139], 0\n"
"ds_read2st64_b32 v[216:217], v20 offset0:0 offset1:1\n"
"ds_read2st64_b32 v[218:219], v20 offset0:2 offset1:3\n"
"v_mfma_f32_16x16x32_bf16 v[160:163], v[242:245], v[136:139], 0\n"
"s_setprio 2\n"
"v_mov_b32 v244, 0\n"
"ds_read_b32 v245, v9 offset:33536\n"
"s_add_u32 m0, s51, 126976\n"
"s_nop 0\n"
"global_load_lds_dwordx4 v8, s[56:57] nt\n"
"v_mfma_f32_16x16x32_bf16 v[144:147], v[140:143], v[148:151], v[144:147]\n"
"v_mfma_f32_16x16x32_bf16 v[152:155], v[182:185], v[148:151], v[152:155]\n"
"ds_read2st64_b32 v[242:243], v9 offset0:128 offset1:129\n"
"v_mfma_f32_16x16x32_bf16 v[156:159], v[208:211], v[148:151], v[156:159]\n"
"s_waitcnt lgkmcnt(4)\n"
"v_mad_u32_u24 v192, v193, s40, v18\n"
"v_mad_u32_u24 v193, v193, s40, v19\n"
"ds_read2st64_b32 v[190:191], v9 offset0:38 offset1:39\n"
"s_waitcnt lgkmcnt(3)\n"
"global_store_dwordx4 v192, v[216:219], s[66:67] sc1\n"
"v_mfma_f32_16x16x32_bf16 v[160:163], v[234:237], v[148:151], v[160:163]\n"
"s_waitcnt lgkmcnt(1)\n"
"v_mfma_f32_16x16x32_bf16 v[144:147], v[140:143], v[242:245], v[144:147]\n"
"ds_read_b32 v137, v9 offset:34048\n"
"ds_read2st64_b32 v[138:139], v9 offset0:134 offset1:135\n"
"v_mfma_f32_16x16x32_bf16 v[152:155], v[186:189], v[242:245], v[152:155]\n"
"ds_read2st64_b32 v[10:11], v20 offset0:4 offset1:5\n"
"v_mov_b32 v188, 0\n"
"v_mov_b32 v189, 0\n"
"v_mfma_f32_16x16x32_bf16 v[156:159], v[212:215], v[242:245], v[156:159]\n"
"ds_read2st64_b32 v[214:215], v14 offset0:126 offset1:127\n"
"v_mov_b32 v136, 0\n"
"v_mfma_f32_16x16x32_bf16 v[160:163], v[238:241], v[242:245], v[160:163]\n"
"ds_read_b128 v[240:243], v17 offset:53248\n"
"s_add_u32 m0, s51, 129024\n"
"s_nop 0\n"
"global_load_lds_dwordx4 v8, s[58:59] nt\n"
"s_waitcnt lgkmcnt(3)\n"
"v_mfma_f32_16x16x32_bf16 v[144:147], v[140:143], v[136:139], v[144:147]\n"
"s_waitcnt lgkmcnt(2)\n"
"global_store_dwordx2 v193, v[10:11], s[66:67] offset:256 sc1\n"
"v_mfma_f32_16x16x32_bf16 v[144:147], v[140:143], v[188:191], v[144:147]\n"
"v_mfma_f32_16x16x32_bf16 v[160:163], v[250:253], v[136:139], v[160:163]\n"
"s_add_u32 m0, s51, 131072\n"
"s_nop 0\n"
"global_load_lds_dwordx4 v8, s[60:61] nt\n"
"s_and_b32 s41, 416, s55\n"
"v_add_u32 v245, s41, v15\n"
"v_mfma_f32_16x16x32_bf16 v[160:163], v[252:255], v[188:191], v[160:163]\n"
"v_mfma_f32_16x16x32_bf16 v[152:155], v[198:201], v[136:139], v[152:155]\n"
"v_mfma_f32_16x16x32_bf16 v[152:155], v[200:203], v[188:191], v[152:155]\n"
"v_rcp_f32 v8, v144\n"
"ds_read2st64_b32 v[216:217], v14 offset0:128 offset1:129\n"
"v_add_u32 v144, 0xd4800000, v144\n"
"v_mfma_f32_16x16x32_bf16 v[156:159], v[224:227], v[136:139], v[156:159]\n"
"ds_read2st64_b32 v[136:137], v14 offset0:150 offset1:151\n"
"v_cmp_lt_u32 vcc, 0x28000000, v144\n"
"v_mfma_f32_16x16x32_bf16 v[156:159], v[226:229], v[188:191], v[156:159]\n"
"s_nop 0\n"
"v_mul_f32 v153, v153, v8\n"
"v_mul_f32 v162, v162, v8\n"
"v_mul_f32 v163, v163, v8\n"
"v_cvt_pk_bf16_f32 v193, v162, v163\n"
"ds_read_b128 v[144:147], v17 offset:54272\n"
"s_waitcnt lgkmcnt(2)\n"
"v_mfma_f32_16x16x32_bf16 v[188:191], v[96:99], v[214:217], 0\n"
"v_mul_f32 v160, v160, v8\n"
"v_mul_f32 v159, v159, v8\n"
"v_mfma_f32_16x16x32_bf16 v[140:143], v[100:103], v[214:217], 0\n"
"v_mul_f32 v161, v161, v8\n"
"v_cvt_pk_bf16_f32 v192, v160, v161\n"
"ds_read2st64_b32 v[138:139], v14 offset0:152 offset1:153\n"
"v_mul_f32 v157, v157, v8\n"
"v_mfma_f32_16x16x32_bf16 v[148:151], v[240:243], v[214:217], 0\n"
"v_mul_f32 v155, v155, v8\n"
"v_mul_f32 v152, v152, v8\n"
"ds_read2st64_b32 v[214:215], v14 offset0:174 offset1:175\n"
"v_cvt_pk_bf16_f32 v152, v152, v153\n"
"s_setprio 0\n"
"v_mul_f32 v156, v156, v8\n"
"s_or_b64 s[62:63], s[62:63], vcc\n"
"v_mul_f32 v158, v158, v8\n"
"v_mul_f32 v154, v154, v8\n"
"ds_read2st64_b32 v[216:217], v14 offset0:176 offset1:177\n"
"ds_read_b128 v[240:243], v17 offset:55296\n"
"ds_read_u16 v245, v245 offset:608\n"
"s_waitcnt lgkmcnt(4)\n"
"v_mfma_f32_16x16x32_bf16 v[188:191], v[104:107], v[136:139], v[188:191]\n"
"v_cvt_pk_bf16_f32 v153, v154, v155\n"
"v_mfma_f32_16x16x32_bf16 v[140:143], v[108:111], v[136:139], v[140:143]\n"
"v_cvt_pk_bf16_f32 v155, v158, v159\n"
"v_cvt_pk_bf16_f32 v154, v156, v157\n"
"v_mfma_f32_16x16x32_bf16 v[148:151], v[144:147], v[136:139], v[148:151]\n"
"s_waitcnt lgkmcnt(2)\n"
"ds_read2st64_b32 v[138:139], v14 offset0:200 offset1:201\n"
"ds_read2st64_b32 v[136:137], v14 offset0:198 offset1:199\n"
"ds_read2st64_b32 v[158:159], v14 offset0:154 offset1:155\n"
"v_mfma_f32_16x16x32_bf16 v[188:191], v[0:3], v[214:217], v[188:191]\n"
"ds_read2st64_b32 v[156:157], v14 offset0:130 offset1:131\n"
"v_mfma_f32_16x16x32_bf16 v[140:143], v[112:115], v[214:217], v[140:143]\n"
"s_waitcnt lgkmcnt(4)\n"
"ds_read_b128 v[144:147], v17 offset:56320\n"
"v_mad_u32_u24 v245, v245, s40, v16\n"
"v_mfma_f32_16x16x32_bf16 v[148:151], v[240:243], v[214:217], v[148:151]\n"
"s_waitcnt lgkmcnt(3)\n"
"ds_read_b128 v[214:217], v17 offset:57344\n"
"ds_read2st64_b32 v[242:243], v14 offset0:202 offset1:203\n"
"v_mfma_f32_16x16x32_bf16 v[188:191], v[116:119], v[136:139], v[188:191]\n"
"s_add_u32 m0, s51, 114688\n"
"s_nop 0\n"
"global_load_lds_dwordx4 v245, s[56:57] nt\n"
"v_mfma_f32_16x16x32_bf16 v[140:143], v[120:123], v[136:139], v[140:143]\n"
"s_waitcnt lgkmcnt(2)\n"
"ds_read2st64_b32 v[240:241], v14 offset0:178 offset1:179\n"
"v_mfma_f32_16x16x32_bf16 v[148:151], v[144:147], v[136:139], v[148:151]\n"
"ds_read_b128 v[136:139], v17 offset:58368\n"
"v_mfma_f32_16x16x32_bf16 v[188:191], v[4:7], v[156:159], v[188:191]\n"
"v_mfma_f32_16x16x32_bf16 v[140:143], v[124:127], v[156:159], v[140:143]\n"
"s_waitcnt lgkmcnt(3)\n"
"ds_read_b128 v[144:147], v12 offset:36864\n"
"v_mfma_f32_16x16x32_bf16 v[148:151], v[214:217], v[156:159], v[148:151]\n"
"s_waitcnt lgkmcnt(2)\n"
"ds_read_b128 v[156:159], v12 offset:38912\n"
"v_mfma_f32_16x16x32_bf16 v[188:191], v[128:131], v[240:243], v[188:191]\n"
"s_add_u32 m0, s51, 116736\n"
"s_nop 0\n"
"global_load_lds_dwordx4 v245, s[58:59] nt\n"
"v_mfma_f32_16x16x32_bf16 v[140:143], v[132:135], v[240:243], v[140:143]\n"
"s_barrier\n"
"s_nop 2\n"
"v_cmp_class_f32_e64 vcc, v188, s64\n"
"v_cvt_pk_bf16_f32 v188, v188, v189\n"
"v_cvt_pk_bf16_f32 v189, v190, v191\n"
"s_waitcnt lgkmcnt(2)\n"
"v_mfma_f32_16x16x32_bf16 v[148:151], v[136:139], v[240:243], v[148:151]\n"
"s_waitcnt lgkmcnt(1)\n"
"v_mfma_f32_16x16x32_bf16 v[240:243], v[144:147], v[28:31], 0\n"
"v_cvt_pk_bf16_f32 v191, v142, v143\n"
"s_add_u32 m0, s51, 118784\n"
"s_nop 0\n"
"global_load_lds_dwordx4 v245, s[60:61] nt\n"
"ds_read_b128 v[214:217], v13 offset:36864\n"
"v_cvt_pk_bf16_f32 v190, v140, v141\n"
"v_cvt_pk_bf16_f32 v148, v148, v149\n"
"v_mfma_f32_16x16x32_bf16 v[136:139], v[144:147], v[32:35], 0\n"
"s_mov_b32 m0, s54\n"
"v_mfma_f32_16x16x32_bf16 v[160:163], v[144:147], v[24:27], 0\n"
"ds_write_addtid_b32 v155 offset:33024\n"
"ds_read_b128 v[144:147], v13 offset:38912\n"
"s_waitcnt lgkmcnt(2)\n"
"v_mfma_f32_16x16x32_bf16 v[240:243], v[214:217], v[40:43], v[240:243]\n"
"ds_write_addtid_b32 v193 offset:33536\n"
"v_mfma_f32_16x16x32_bf16 v[136:139], v[214:217], v[44:47], v[136:139]\n"
"v_mfma_f32_16x16x32_bf16 v[160:163], v[214:217], v[36:39], v[160:163]\n"
"v_cvt_pk_bf16_f32 v149, v150, v151\n"
"ds_read_b128 v[214:217], v12 offset:40960\n"
"v_mfma_f32_16x16x32_bf16 v[240:243], v[156:159], v[48:51], v[240:243]\n"
"ds_write_addtid_b32 v154 offset:32768\n"
"v_mfma_f32_16x16x32_bf16 v[136:139], v[156:159], v[52:55], v[136:139]\n"
"ds_write_addtid_b32 v153 offset:32512\n"
"v_mfma_f32_16x16x32_bf16 v[160:163], v[156:159], v[56:59], v[160:163]\n"
"ds_write_addtid_b32 v192 offset:33280\n"
"ds_read_b128 v[140:143], v13 offset:40960\n"
"s_waitcnt lgkmcnt(6)\n"
"v_mfma_f32_16x16x32_bf16 v[240:243], v[144:147], v[60:63], v[240:243]\n"
"ds_write_addtid_b32 v152 offset:32256\n"
"s_mov_b32 m0, s68\n"
"s_nop 0\n"
"ds_write_addtid_b32 v190 offset:45568\n"
"v_mfma_f32_16x16x32_bf16 v[136:139], v[144:147], v[64:67], v[136:139]\n"
"s_or_b64 s[62:63], s[62:63], vcc\n"
"ds_write_addtid_b32 v191 offset:45824\n"
"v_mfma_f32_16x16x32_bf16 v[160:163], v[144:147], v[68:71], v[160:163]\n"
"s_waitcnt lgkmcnt(7)\n"
"ds_read_b128 v[190:193], v13 offset:43008\n"
"v_mfma_f32_16x16x32_bf16 v[240:243], v[214:217], v[72:75], v[240:243]\n"
"ds_read_b128 v[144:147], v12 offset:43008\n"
"v_mfma_f32_16x16x32_bf16 v[136:139], v[214:217], v[76:79], v[136:139]\n"
"ds_write_addtid_b32 v189 offset:45312\n"
"v_mfma_f32_16x16x32_bf16 v[160:163], v[214:217], v[80:83], v[160:163]\n"
"s_waitcnt lgkmcnt(6)\n"
"v_mfma_f32_16x16x32_bf16 v[240:243], v[140:143], v[84:87], v[240:243]\n"
"ds_write_addtid_b32 v188 offset:45056\n"
"v_mfma_f32_16x16x32_bf16 v[136:139], v[140:143], v[88:91], v[136:139]\n"
"ds_write_addtid_b32 v149 offset:46336\n"
"v_mfma_f32_16x16x32_bf16 v[160:163], v[140:143], v[92:95], v[160:163]\n"
"s_waitcnt lgkmcnt(3)\n"
"ds_read_b128 v[140:143], v12 offset:45056\n"
"s_setprio 2\n"
"v_mfma_f32_16x16x32_bf16 v[216:219], v[144:147], v[28:31], 0\n"
"ds_write_addtid_b32 v148 offset:46080\n"
"v_mfma_f32_16x16x32_bf16 v[148:151], v[144:147], v[32:35], 0\n"
"v_cvt_pk_bf16_f32 v214, v136, v137\n"
"v_mfma_f32_16x16x32_bf16 v[152:155], v[144:147], v[24:27], 0\n"
"v_mfma_f32_16x16x32_bf16 v[216:219], v[190:193], v[40:43], v[216:219]\n"
"v_cvt_pk_bf16_f32 v189, v242, v243\n"
"v_cvt_pk_bf16_f32 v215, v138, v139\n"
"v_mfma_f32_16x16x32_bf16 v[148:151], v[190:193], v[44:47], v[148:151]\n"
"v_mfma_f32_16x16x32_bf16 v[152:155], v[190:193], v[36:39], v[152:155]\n"
"v_cvt_pk_bf16_f32 v188, v240, v241\n"
"v_cvt_pk_bf16_f32 v241, v162, v163\n"
"s_setprio 0\n"
"ds_read_b128 v[242:245], v13 offset:45056\n"
"s_waitcnt lgkmcnt(2)\n"
"v_mfma_f32_16x16x32_bf16 v[216:219], v[140:143], v[48:51], v[216:219]\n"
"ds_read_b128 v[136:139], v13 offset:47104\n"
"v_mfma_f32_16x16x32_bf16 v[148:151], v[140:143], v[52:55], v[148:151]\n"
"ds_read_b128 v[190:193], v12 offset:47104\n"
"v_mfma_f32_16x16x32_bf16 v[152:155], v[140:143], v[56:59], v[152:155]\n"
"s_waitcnt lgkmcnt(2)\n"
"v_mfma_f32_16x16x32_bf16 v[216:219], v[242:245], v[60:63], v[216:219]\n"
"v_cvt_pk_bf16_f32 v240, v160, v161\n"
"v_mfma_f32_16x16x32_bf16 v[148:151], v[242:245], v[64:67], v[148:151]\n"
"v_mov_b32 v142, 0x3f803f80\n"
"v_mfma_f32_16x16x32_bf16 v[152:155], v[242:245], v[68:71], v[152:155]\n"
"s_waitcnt lgkmcnt(0)\n"
"v_mfma_f32_16x16x32_bf16 v[216:219], v[190:193], v[72:75], v[216:219]\n"
"v_mfma_f32_16x16x32_bf16 v[148:151], v[190:193], v[76:79], v[148:151]\n"
"v_mov_b32 v141, 0x3f803f80\n"
"v_mfma_f32_16x16x32_bf16 v[152:155], v[190:193], v[80:83], v[152:155]\n"
"v_mov_b32 v140, 0x3f803f80\n"
"v_mfma_f32_16x16x32_bf16 v[216:219], v[136:139], v[84:87], v[216:219]\n"
"s_waitcnt vmcnt(3)\n"
"s_barrier\n"
"v_mfma_f32_16x16x32_bf16 v[148:151], v[136:139], v[88:91], v[148:151]\n"
"ds_read2st64_b32 v[144:145], v9 offset0:112 offset1:113\n"
"ds_read2st64_b32 v[146:147], v9 offset0:114 offset1:115\n"
"v_mfma_f32_16x16x32_bf16 v[152:155], v[136:139], v[92:95], v[152:155]\n"
"ds_read2st64_b32 v[160:161], v9 offset0:130 offset1:131\n"
"s_andn2_b32 s41, 416, s55\n"
"v_add_u32 v8, s41, v15\n"
"v_cvt_pk_bf16_f32 v191, v218, v219\n"
"v_cvt_pk_bf16_f32 v190, v216, v217\n"
"v_cvt_pk_bf16_f32 v217, v150, v151\n"
"ds_read2st64_b32 v[136:137], v9 offset0:116 offset1:117\n"
"v_mov_b32 v143, 0x3f803f80\n"
"ds_read2st64_b32 v[168:169], v20 offset0:0 offset1:1\n"
"v_cvt_pk_bf16_f32 v216, v148, v149\n"
"s_waitcnt lgkmcnt(3)\n"
"v_mfma_f32_16x16x32_bf16 v[148:151], v[140:143], v[144:147], 0\n"
"ds_read2st64_b32 v[138:139], v9 offset0:118 offset1:119\n"
"v_cvt_pk_bf16_f32 v243, v154, v155\n"
"v_mfma_f32_16x16x32_bf16 v[164:167], v[178:181], v[144:147], 0\n"
"ds_read_u16 v8, v8 offset:640\n"
"v_cvt_pk_bf16_f32 v242, v152, v153\n"
"v_mfma_f32_16x16x32_bf16 v[152:155], v[204:207], v[144:147], 0\n"
"ds_read_u16 v245, v21 offset:864\n"
"v_mov_b32 v156, 0\n"
"ds_read2st64_b32 v[158:159], v9 offset0:128 offset1:129\n"
"v_mfma_f32_16x16x32_bf16 v[172:175], v[230:233], v[144:147], 0\n"
"s_waitcnt lgkmcnt(3)\n"
"v_mov_b32 v157, 0\n"
"v_mfma_f32_16x16x32_bf16 v[148:151], v[140:143], v[136:139], v[148:151]\n"
"s_waitcnt lgkmcnt(0)\n"
"ds_read2st64_b32 v[218:219], v20 offset0:4 offset1:5\n"
"ds_read2st64_b32 v[170:171], v20 offset0:2 offset1:3\n"
"v_mfma_f32_16x16x32_bf16 v[148:151], v[140:143], v[158:161], v[148:151]\n"
"v_mad_u32_u24 v8, v8, s40, v16\n"
"s_add_u32 m0, s51, 139264\n"
"s_nop 0\n"
"global_load_lds_dwordx4 v8, s[56:57] nt\n"
"v_mfma_f32_16x16x32_bf16 v[172:175], v[246:249], v[136:139], v[172:175]\n"
"v_mov_b32 v162, 0\n"
"v_mad_u32_u24 v244, v245, s40, v18\n"
"v_mov_b32 v163, 0\n"
"v_mfma_f32_16x16x32_bf16 v[172:175], v[232:235], v[156:159], v[172:175]\n"
"v_mad_u32_u24 v245, v245, s40, v19\n"
"v_mfma_f32_16x16x32_bf16 v[172:175], v[250:253], v[160:163], v[172:175]\n"
"s_waitcnt lgkmcnt(0)\n"
"ds_read2st64_b32 v[142:143], v14 offset0:128 offset1:129\n"
"global_store_dwordx4 v244, v[168:171], s[66:67] sc1\n"
"v_mfma_f32_16x16x32_bf16 v[164:167], v[194:197], v[136:139], v[164:167]\n"
"ds_read2st64_b32 v[144:145], v14 offset0:150 offset1:151\n"
"ds_read2st64_b32 v[140:141], v14 offset0:126 offset1:127\n"
"v_mfma_f32_16x16x32_bf16 v[164:167], v[180:183], v[156:159], v[164:167]\n"
"v_mfma_f32_16x16x32_bf16 v[164:167], v[198:201], v[160:163], v[164:167]\n"
"s_add_u32 m0, s51, 141312\n"
"s_nop 0\n"
"global_load_lds_dwordx4 v8, s[58:59] nt\n"
"global_store_dwordx2 v245, v[218:219], s[66:67] offset:256 sc1\n"
"v_mfma_f32_16x16x32_bf16 v[152:155], v[220:223], v[136:139], v[152:155]\n"
"s_setprio 2\n"
"ds_read_b128 v[136:139], v17 offset:53248\n"
"v_rcp_f32 v245, v148\n"
"v_add_u32 v148, 0xd4800000, v148\n"
"v_mul_f32 v164, v164, v245\n"
"v_mfma_f32_16x16x32_bf16 v[152:155], v[206:209], v[156:159], v[152:155]\n"
"v_mul_f32 v174, v174, v245\n"
"v_mul_f32 v172, v172, v245\n"
"v_mul_f32 v165, v165, v245\n"
"v_mul_f32 v173, v173, v245\n"
"v_mfma_f32_16x16x32_bf16 v[152:155], v[224:227], v[160:163], v[152:155]\n"
"s_waitcnt lgkmcnt(1)\n"
"ds_read_b128 v[156:159], v17 offset:54272\n"
"ds_read2st64_b32 v[146:147], v14 offset0:152 offset1:153\n"
"v_cmp_lt_u32 vcc, 0x28000000, v148\n"
"v_mfma_f32_16x16x32_bf16 v[148:151], v[96:99], v[140:143], 0\n"
"v_cvt_pk_bf16_f32 v218, v172, v173\n"
"s_add_u32 m0, s51, 143360\n"
"s_nop 0\n"
"global_load_lds_dwordx4 v8, s[60:61] nt\n"
"s_or_b64 s[62:63], s[62:63], vcc\n"
"v_mfma_f32_16x16x32_bf16 v[160:163], v[100:103], v[140:143], 0\n"
"s_waitcnt lgkmcnt(2)\n"
"ds_read2st64_b32 v[170:171], v14 offset0:176 offset1:177\n"
"v_mul_f32 v175, v175, v245\n"
"v_cvt_pk_bf16_f32 v219, v174, v175\n"
"v_mfma_f32_16x16x32_bf16 v[172:175], v[136:139], v[140:143], 0\n"
"s_waitcnt lgkmcnt(1)\n"
"ds_read2st64_b32 v[168:169], v14 offset0:174 offset1:175\n"
"v_mul_f32 v167, v167, v245\n"
"ds_read_b128 v[136:139], v17 offset:55296\n"
"v_mfma_f32_16x16x32_bf16 v[148:151], v[104:107], v[144:147], v[148:151]\n"
"v_cvt_pk_bf16_f32 v164, v164, v165\n"
"v_mul_f32 v166, v166, v245\n"
"ds_read2st64_b32 v[142:143], v14 offset0:200 offset1:201\n"
"ds_read2st64_b32 v[140:141], v14 offset0:198 offset1:199\n"
"v_mfma_f32_16x16x32_bf16 v[160:163], v[108:111], v[144:147], v[160:163]\n"
"v_mul_f32 v155, v155, v245\n"
"v_cvt_pk_bf16_f32 v165, v166, v167\n"
"s_setprio 0\n"
"v_mul_f32 v152, v152, v245\n"
"v_mfma_f32_16x16x32_bf16 v[172:175], v[156:159], v[144:147], v[172:175]\n"
"s_waitcnt lgkmcnt(3)\n"
"ds_read_b128 v[144:147], v17 offset:56320\n"
"v_mul_f32 v153, v153, v245\n"
"v_mfma_f32_16x16x32_bf16 v[148:151], v[0:3], v[168:171], v[148:151]\n"
"v_mul_f32 v154, v154, v245\n"
"v_cvt_pk_bf16_f32 v166, v152, v153\n"
"ds_read2st64_b32 v[152:153], v14 offset0:130 offset1:131\n"
"v_cvt_pk_bf16_f32 v167, v154, v155\n"
"ds_read2st64_b32 v[156:157], v14 offset0:178 offset1:179\n"
"v_mfma_f32_16x16x32_bf16 v[160:163], v[112:115], v[168:171], v[160:163]\n"
"s_waitcnt lgkmcnt(5)\n"
"ds_read2st64_b32 v[154:155], v14 offset0:154 offset1:155\n"
"v_mfma_f32_16x16x32_bf16 v[172:175], v[136:139], v[168:171], v[172:175]\n"
"s_waitcnt lgkmcnt(4)\n"
"ds_read_b128 v[136:139], v17 offset:57344\n"
"v_mfma_f32_16x16x32_bf16 v[148:151], v[116:119], v[140:143], v[148:151]\n"
"v_mfma_f32_16x16x32_bf16 v[160:163], v[120:123], v[140:143], v[160:163]\n"
"s_waitcnt lgkmcnt(4)\n"
"ds_read2st64_b32 v[158:159], v14 offset0:202 offset1:203\n"
"v_mfma_f32_16x16x32_bf16 v[172:175], v[144:147], v[140:143], v[172:175]\n"
"s_waitcnt lgkmcnt(2)\n"
"v_mfma_f32_16x16x32_bf16 v[148:151], v[4:7], v[152:155], v[148:151]\n"
"ds_read_b128 v[140:143], v17 offset:58368\n"
"s_mov_b32 m0, s54\n"
"v_mfma_f32_16x16x32_bf16 v[160:163], v[124:127], v[152:155], v[160:163]\n"
"s_waitcnt lgkmcnt(2)\n"
"ds_read_b128 v[144:147], v12 offset:49152\n"
"v_mfma_f32_16x16x32_bf16 v[172:175], v[136:139], v[152:155], v[172:175]\n"
"s_waitcnt lgkmcnt(2)\n"
"ds_read_b128 v[136:139], v13 offset:49152\n"
"v_mfma_f32_16x16x32_bf16 v[148:151], v[128:131], v[156:159], v[148:151]\n"
"v_mfma_f32_16x16x32_bf16 v[160:163], v[132:135], v[156:159], v[160:163]\n"
"s_barrier\n"
"s_waitcnt lgkmcnt(2)\n"
"v_mfma_f32_16x16x32_bf16 v[172:175], v[140:143], v[156:159], v[172:175]\n"
"s_waitcnt lgkmcnt(1)\n"
"v_mfma_f32_16x16x32_bf16 v[140:143], v[144:147], v[28:31], 0\n"
"ds_read_b128 v[152:155], v12 offset:51200\n"
"v_mfma_f32_16x16x32_bf16 v[156:159], v[144:147], v[32:35], 0\n"
"ds_write_addtid_b32 v219 offset:33536\n"
"v_mfma_f32_16x16x32_bf16 v[168:171], v[144:147], v[24:27], 0\n"
"ds_write_addtid_b32 v218 offset:33280\n"
"s_waitcnt lgkmcnt(3)\n"
"v_mfma_f32_16x16x32_bf16 v[140:143], v[136:139], v[40:43], v[140:143]\n"
"v_mfma_f32_16x16x32_bf16 v[156:159], v[136:139], v[44:47], v[156:159]\n"
"v_cmp_class_f32_e64 vcc, v148, s64\n"
"v_cvt_pk_bf16_f32 v148, v148, v149\n"
"v_cvt_pk_bf16_f32 v172, v172, v173\n"
"v_cvt_pk_bf16_f32 v173, v174, v175\n"
"v_cvt_pk_bf16_f32 v149, v150, v151\n"
"v_cvt_pk_bf16_f32 v151, v162, v163\n"
"v_cvt_pk_bf16_f32 v150, v160, v161\n"
"v_mfma_f32_16x16x32_bf16 v[168:171], v[136:139], v[36:39], v[168:171]\n"
"s_waitcnt lgkmcnt(2)\n"
"ds_read_b128 v[144:147], v13 offset:51200\n"
"ds_read_b128 v[136:139], v12 offset:53248\n"
"v_mfma_f32_16x16x32_bf16 v[140:143], v[152:155], v[48:51], v[140:143]\n"
"ds_write_addtid_b32 v167 offset:33024\n"
"v_mfma_f32_16x16x32_bf16 v[156:159], v[152:155], v[52:55], v[156:159]\n"
"ds_write_addtid_b32 v165 offset:32512\n"
"v_mfma_f32_16x16x32_bf16 v[168:171], v[152:155], v[56:59], v[168:171]\n"
"ds_write_addtid_b32 v166 offset:32768\n"
"s_waitcnt lgkmcnt(4)\n"
"v_mfma_f32_16x16x32_bf16 v[140:143], v[144:147], v[60:63], v[140:143]\n"
"ds_write_addtid_b32 v164 offset:32256\n"
"s_mov_b32 m0, s68\n"
"s_or_b64 s[62:63], s[62:63], vcc\n"
"ds_write_addtid_b32 v173 offset:46336\n"
"v_mfma_f32_16x16x32_bf16 v[156:159], v[144:147], v[64:67], v[156:159]\n"
"ds_write_addtid_b32 v149 offset:45312\n"
"ds_read_b128 v[152:155], v13 offset:53248\n"
"v_mfma_f32_16x16x32_bf16 v[168:171], v[144:147], v[68:71], v[168:171]\n"
"ds_write_addtid_b32 v172 offset:46080\n"
"s_waitcnt lgkmcnt(8)\n"
"v_mfma_f32_16x16x32_bf16 v[140:143], v[136:139], v[72:75], v[140:143]\n"
"ds_write_addtid_b32 v150 offset:45568\n"
"v_mfma_f32_16x16x32_bf16 v[156:159], v[136:139], v[76:79], v[156:159]\n"
"ds_write_addtid_b32 v148 offset:45056\n"
"v_mfma_f32_16x16x32_bf16 v[168:171], v[136:139], v[80:83], v[168:171]\n"
"ds_write_addtid_b32 v151 offset:45824\n"
"s_waitcnt lgkmcnt(4)\n"
"v_mfma_f32_16x16x32_bf16 v[140:143], v[152:155], v[84:87], v[140:143]\n"
"v_mov_b32 v145, 0x3f803f80\n"
"v_mov_b32 v144, 0x3f803f80\n"
"v_mfma_f32_16x16x32_bf16 v[156:159], v[152:155], v[88:91], v[156:159]\n"
"v_mfma_f32_16x16x32_bf16 v[168:171], v[152:155], v[92:95], v[168:171]\n"
"s_waitcnt lgkmcnt(0)\n"
"s_waitcnt vmcnt(5)\n"
"s_barrier\n"
"ds_read2st64_b32 v[148:149], v9 offset0:124 offset1:125\n"
"ds_read2st64_b32 v[136:137], v9 offset0:120 offset1:121\n"
"ds_read2st64_b32 v[138:139], v9 offset0:122 offset1:123\n"
"v_mov_b32 v146, 0x3f803f80\n"
"v_cvt_pk_bf16_f32 v218, v156, v157\n"
"v_cvt_pk_bf16_f32 v193, v142, v143\n"
"s_and_b32 s41, 416, s55\n"
"v_add_u32 v11, s41, v15\n"
"v_mov_b32 v147, 0x3f803f80\n"
"v_cvt_pk_bf16_f32 v244, v168, v169\n"
"ds_read2st64_b32 v[150:151], v9 offset0:126 offset1:127\n"
"ds_read_u16 v11, v11 offset:672\n"
"v_cvt_pk_bf16_f32 v192, v140, v141\n"
"s_waitcnt lgkmcnt(2)\n"
"v_mfma_f32_16x16x32_bf16 v[140:143], v[144:147], v[136:139], 0\n"
"v_cvt_pk_bf16_f32 v219, v158, v159\n"
"v_mfma_f32_16x16x32_bf16 v[152:155], v[178:181], v[136:139], 0\n"
"ds_read_b32 v164, v9 offset:32768\n"
"v_mfma_f32_16x16x32_bf16 v[156:159], v[204:207], v[136:139], 0\n"
"v_cvt_pk_bf16_f32 v245, v170, v171\n"
"v_mfma_f32_16x16x32_bf16 v[160:163], v[230:233], v[136:139], 0\n"
"s_waitcnt lgkmcnt(2)\n"
"ds_read_u16 v8, v21 offset:896\n"
"ds_read2st64_b32 v[136:137], v20 offset0:0 offset1:1\n"
"ds_read2st64_b32 v[138:139], v20 offset0:2 offset1:3\n"
"v_mfma_f32_16x16x32_bf16 v[140:143], v[144:147], v[148:151], v[140:143]\n"
"v_mfma_f32_16x16x32_bf16 v[152:155], v[188:191], v[148:151], v[152:155]\n"
"ds_read2st64_b32 v[166:167], v9 offset0:130 offset1:131\n"
"v_mfma_f32_16x16x32_bf16 v[156:159], v[214:217], v[148:151], v[156:159]\n"
"v_mov_b32 v165, 0\n"
"v_mfma_f32_16x16x32_bf16 v[160:163], v[240:243], v[148:151], v[160:163]\n"
"s_waitcnt lgkmcnt(3)\n"
"ds_read2st64_b32 v[148:149], v9 offset0:132 offset1:133\n"
"v_mad_u32_u24 v10, v8, s40, v18\n"
"v_mad_u32_u24 v8, v8, s40, v19\n"
"s_waitcnt lgkmcnt(2)\n"
"global_store_dwordx4 v10, v[136:139], s[66:67] sc1\n"
"ds_read2st64_b32 v[174:175], v20 offset0:4 offset1:5\n"
"ds_read_b32 v150, v9 offset:34304\n"
"ds_read_b32 v138, v9 offset:9728\n"
"s_waitcnt lgkmcnt(4)\n"
"v_mfma_f32_16x16x32_bf16 v[140:143], v[144:147], v[164:167], v[140:143]\n"
"v_mad_u32_u24 v11, v11, s40, v16\n"
"v_mov_b32 v136, 0\n"
"v_mfma_f32_16x16x32_bf16 v[152:155], v[192:195], v[164:167], v[152:155]\n"
"v_mov_b32 v151, 0\n"
"v_mfma_f32_16x16x32_bf16 v[156:159], v[218:221], v[164:167], v[156:159]\n"
"s_add_u32 m0, s51, 151552\n"
"s_nop 0\n"
"global_load_lds_dwordx4 v11, s[56:57] nt\n"
"s_waitcnt lgkmcnt(2)\n"
"global_store_dwordx2 v8, v[174:175], s[66:67] offset:256 sc1\n"
"v_mfma_f32_16x16x32_bf16 v[160:163], v[244:247], v[164:167], v[160:163]\n"
"s_waitcnt lgkmcnt(1)\n"
"s_setprio 2\n"
"ds_read2st64_b32 v[168:169], v14 offset0:150 offset1:151\n"
"v_mfma_f32_16x16x32_bf16 v[140:143], v[144:147], v[148:151], v[140:143]\n"
"s_waitcnt lgkmcnt(1)\n"
"v_mov_b32 v137, 0\n"
"v_mov_b32 v139, 0\n"
"s_nop 1\n"
"v_mfma_f32_16x16x32_bf16 v[140:143], v[144:147], v[136:139], v[140:143]\n"
"ds_read2st64_b32 v[146:147], v14 offset0:128 offset1:129\n"
"ds_read_b128 v[164:167], v17 offset:53248\n"
"v_mfma_f32_16x16x32_bf16 v[160:163], v[248:251], v[148:151], v[160:163]\n"
"ds_read2st64_b32 v[144:145], v14 offset0:126 offset1:127\n"
"v_mfma_f32_16x16x32_bf16 v[160:163], v[232:235], v[136:139], v[160:163]\n"
"s_add_u32 m0, s51, 153600\n"
"s_nop 0\n"
"global_load_lds_dwordx4 v11, s[58:59] nt\n"
"v_mfma_f32_16x16x32_bf16 v[152:155], v[196:199], v[148:151], v[152:155]\n"
"v_rcp_f32 v8, v140\n"
"v_mfma_f32_16x16x32_bf16 v[152:155], v[180:183], v[136:139], v[152:155]\n"
"v_add_u32 v140, 0xd4800000, v140\n"
"v_cmp_lt_u32 vcc, 0x28000000, v140\n"
"v_mfma_f32_16x16x32_bf16 v[156:159], v[222:225], v[148:151], v[156:159]\n"
"v_mul_f32 v161, v161, v8\n"
"ds_read2st64_b32 v[170:171], v14 offset0:152 offset1:153\n"
"v_mfma_f32_16x16x32_bf16 v[156:159], v[206:209], v[136:139], v[156:159]\n"
"s_waitcnt lgkmcnt(1)\n"
"ds_read_b128 v[136:139], v17 offset:54272\n"
"v_mul_f32 v160, v160, v8\n"
"v_cvt_pk_bf16_f32 v246, v160, v161\n"
"v_mfma_f32_16x16x32_bf16 v[140:143], v[96:99], v[144:147], 0\n"
"s_add_u32 m0, s51, 155648\n"
"s_nop 0\n"
"global_load_lds_dwordx4 v11, s[60:61] nt\n"
"v_mfma_f32_16x16x32_bf16 v[148:151], v[100:103], v[144:147], 0\n"
"ds_read2st64_b32 v[160:161], v14 offset0:174 offset1:175\n"
"v_mul_f32 v163, v163, v8\n"
"s_or_b64 s[62:63], s[62:63], vcc\n"
"v_mul_f32 v162, v162, v8\n"
"v_cvt_pk_bf16_f32 v247, v162, v163\n"
"v_mfma_f32_16x16x32_bf16 v[172:175], v[164:167], v[144:147], 0\n"
"s_waitcnt lgkmcnt(2)\n"
"ds_read_b128 v[144:147], v17 offset:55296\n"
"ds_read2st64_b32 v[162:163], v14 offset0:176 offset1:177\n"
"v_mfma_f32_16x16x32_bf16 v[140:143], v[104:107], v[168:171], v[140:143]\n"
"ds_read2st64_b32 v[166:167], v14 offset0:200 offset1:201\n"
"v_mul_f32 v152, v152, v8\n"
"v_mul_f32 v153, v153, v8\n"
"v_mul_f32 v157, v157, v8\n"
"v_mfma_f32_16x16x32_bf16 v[148:151], v[108:111], v[168:171], v[148:151]\n"
"s_waitcnt lgkmcnt(4)\n"
"ds_read2st64_b32 v[164:165], v14 offset0:198 offset1:199\n"
"v_mul_f32 v154, v154, v8\n"
"v_mfma_f32_16x16x32_bf16 v[172:175], v[136:139], v[168:171], v[172:175]\n"
"s_waitcnt lgkmcnt(2)\n"
"v_mul_f32 v155, v155, v8\n"
"v_cvt_pk_bf16_f32 v152, v152, v153\n"
"v_cvt_pk_bf16_f32 v153, v154, v155\n"
"v_mfma_f32_16x16x32_bf16 v[140:143], v[0:3], v[160:163], v[140:143]\n"
"ds_read_b128 v[136:139], v17 offset:56320\n"
"v_mul_f32 v156, v156, v8\n"
"v_mfma_f32_16x16x32_bf16 v[148:151], v[112:115], v[160:163], v[148:151]\n"
"v_cvt_pk_bf16_f32 v154, v156, v157\n"
"v_mul_f32 v159, v159, v8\n"
"ds_read2st64_b32 v[156:157], v14 offset0:130 offset1:131\n"
"v_mfma_f32_16x16x32_bf16 v[172:175], v[144:147], v[160:163], v[172:175]\n"
"s_waitcnt lgkmcnt(2)\n"
"v_mul_f32 v158, v158, v8\n"
"v_cvt_pk_bf16_f32 v155, v158, v159\n"
"v_mfma_f32_16x16x32_bf16 v[140:143], v[116:119], v[164:167], v[140:143]\n"
"ds_read2st64_b32 v[158:159], v14 offset0:154 offset1:155\n"
"v_mfma_f32_16x16x32_bf16 v[148:151], v[120:123], v[164:167], v[148:151]\n"
"s_waitcnt lgkmcnt(2)\n"
"ds_read2st64_b32 v[162:163], v14 offset0:202 offset1:203\n"
"ds_read_b128 v[144:147], v17 offset:57344\n"
"ds_read2st64_b32 v[160:161], v14 offset0:178 offset1:179\n"
"v_mfma_f32_16x16x32_bf16 v[172:175], v[136:139], v[164:167], v[172:175]\n"
"s_waitcnt lgkmcnt(3)\n"
"s_setprio 0\n"
"v_mfma_f32_16x16x32_bf16 v[140:143], v[4:7], v[156:159], v[140:143]\n"
"s_mov_b32 m0, s54\n"
"ds_read_b128 v[164:167], v12 offset:24576\n"
"v_mfma_f32_16x16x32_bf16 v[148:151], v[124:127], v[156:159], v[148:151]\n"
"s_waitcnt lgkmcnt(2)\n"
"ds_read_b128 v[136:139], v17 offset:58368\n"
"v_mfma_f32_16x16x32_bf16 v[172:175], v[144:147], v[156:159], v[172:175]\n"
"s_waitcnt lgkmcnt(2)\n"
"v_mfma_f32_16x16x32_bf16 v[140:143], v[128:131], v[160:163], v[140:143]\n"
"s_barrier\n"
"v_mfma_f32_16x16x32_bf16 v[148:151], v[132:135], v[160:163], v[148:151]\n"
"s_waitcnt lgkmcnt(0)\n"
"v_mfma_f32_16x16x32_bf16 v[172:175], v[136:139], v[160:163], v[172:175]\n"
"ds_read_b128 v[156:159], v12 offset:26624\n"
"v_mfma_f32_16x16x32_bf16 v[136:139], v[164:167], v[28:31], 0\n"
"ds_read_b128 v[144:147], v13 offset:24576\n"
"s_nop 0\n"
"v_cmp_class_f32_e64 vcc, v140, s64\n"
"v_cvt_pk_bf16_f32 v140, v140, v141\n"
"v_cvt_pk_bf16_f32 v141, v142, v143\n"
"v_cvt_pk_bf16_f32 v142, v148, v149\n"
"ds_write_addtid_b32 v246 offset:33280\n"
"v_mfma_f32_16x16x32_bf16 v[160:163], v[164:167], v[32:35], 0\n"
"v_mfma_f32_16x16x32_bf16 v[168:171], v[164:167], v[24:27], 0\n"
"v_cvt_pk_bf16_f32 v143, v150, v151\n"
"ds_write_addtid_b32 v154 offset:32768\n"
"s_waitcnt lgkmcnt(2)\n"
"v_mfma_f32_16x16x32_bf16 v[136:139], v[144:147], v[40:43], v[136:139]\n"
"v_cvt_pk_bf16_f32 v172, v172, v173\n"
"v_mfma_f32_16x16x32_bf16 v[160:163], v[144:147], v[44:47], v[160:163]\n"
"ds_write_addtid_b32 v155 offset:33024\n"
"v_mfma_f32_16x16x32_bf16 v[168:171], v[144:147], v[36:39], v[168:171]\n"
"ds_read_b128 v[144:147], v12 offset:28672\n"
"v_mfma_f32_16x16x32_bf16 v[136:139], v[156:159], v[48:51], v[136:139]\n"
"ds_read_b128 v[164:167], v13 offset:26624\n"
"v_mfma_f32_16x16x32_bf16 v[160:163], v[156:159], v[52:55], v[160:163]\n"
"ds_write_addtid_b32 v153 offset:32512\n"
"ds_write_addtid_b32 v152 offset:32256\n"
"ds_write_addtid_b32 v247 offset:33536\n"
"v_cvt_pk_bf16_f32 v173, v174, v175\n"
"v_mfma_f32_16x16x32_bf16 v[168:171], v[156:159], v[56:59], v[168:171]\n"
"s_mov_b32 m0, s68\n"
"s_nop 0\n"
"ds_write_addtid_b32 v172 offset:46080\n"
"ds_write_addtid_b32 v173 offset:46336\n"
"s_waitcnt lgkmcnt(5)\n"
"v_mfma_f32_16x16x32_bf16 v[136:139], v[164:167], v[60:63], v[136:139]\n"
"ds_write_addtid_b32 v143 offset:45824\n"
"v_mfma_f32_16x16x32_bf16 v[160:163], v[164:167], v[64:67], v[160:163]\n"
"v_mfma_f32_16x16x32_bf16 v[168:171], v[164:167], v[68:71], v[168:171]\n"
"ds_read_b128 v[148:151], v13 offset:28672\n"
"v_mfma_f32_16x16x32_bf16 v[136:139], v[144:147], v[72:75], v[136:139]\n"
"ds_write_addtid_b32 v142 offset:45568\n"
"ds_write_addtid_b32 v140 offset:45056\n"
"v_mfma_f32_16x16x32_bf16 v[160:163], v[144:147], v[76:79], v[160:163]\n"
"ds_write_addtid_b32 v141 offset:45312\n"
"v_mov_b32 v140, 0x3f803f80\n"
"s_or_b64 s[62:63], s[62:63], vcc\n"
"v_mfma_f32_16x16x32_bf16 v[168:171], v[144:147], v[80:83], v[168:171]\n"
"s_waitcnt lgkmcnt(3)\n"
"v_mfma_f32_16x16x32_bf16 v[136:139], v[148:151], v[84:87], v[136:139]\n"
"v_mfma_f32_16x16x32_bf16 v[160:163], v[148:151], v[88:91], v[160:163]\n"
"v_mfma_f32_16x16x32_bf16 v[168:171], v[148:151], v[92:95], v[168:171]\n"
"s_waitcnt lgkmcnt(0)\n"
"s_waitcnt vmcnt(5)\n"
"s_barrier\n"
"ds_read2st64_b32 v[146:147], v9 offset0:114 offset1:115\n"
"ds_read2st64_b32 v[144:145], v9 offset0:112 offset1:113\n"
"v_mov_b32 v142, 0x3f803f80\n"
"s_nop 0\n"
"v_cvt_pk_bf16_f32 v221, v162, v163\n"
"v_cvt_pk_bf16_f32 v195, v138, v139\n"
"v_mov_b32 v143, 0x3f803f80\n"
"v_cvt_pk_bf16_f32 v194, v136, v137\n"
"ds_read2st64_b32 v[150:151], v9 offset0:118 offset1:119\n"
"v_cvt_pk_bf16_f32 v247, v170, v171\n"
"ds_read_u16 v8, v21 offset:928\n"
"v_mov_b32 v141, 0x3f803f80\n"
"v_cvt_pk_bf16_f32 v220, v160, v161\n"
"s_waitcnt lgkmcnt(2)\n"
"v_mfma_f32_16x16x32_bf16 v[136:139], v[140:143], v[144:147], 0\n"
"ds_read2st64_b32 v[148:149], v9 offset0:116 offset1:117\n"
"v_mfma_f32_16x16x32_bf16 v[152:155], v[180:183], v[144:147], 0\n"
"ds_read2st64_b32 v[166:167], v9 offset0:130 offset1:131\n"
"v_mfma_f32_16x16x32_bf16 v[156:159], v[206:209], v[144:147], 0\n"
"s_and_b32 s41, 416, s55\n"
"v_add_u32 v11, s41, v15\n"
"v_cvt_pk_bf16_f32 v246, v168, v169\n"
"v_mfma_f32_16x16x32_bf16 v[160:163], v[232:235], v[144:147], 0\n"
"s_waitcnt lgkmcnt(1)\n"
"ds_read2st64_b32 v[146:147], v20 offset0:2 offset1:3\n"
"v_mfma_f32_16x16x32_bf16 v[136:139], v[140:143], v[148:151], v[136:139]\n"
"ds_read2st64_b32 v[164:165], v9 offset0:128 offset1:129\n"
"v_mad_u32_u24 v10, v8, s40, v18\n"
"v_mfma_f32_16x16x32_bf16 v[152:155], v[190:193], v[148:151], v[152:155]\n"
"ds_read2st64_b32 v[144:145], v20 offset0:0 offset1:1\n"
"ds_read_u16 v11, v11 offset:704\n"
"v_mfma_f32_16x16x32_bf16 v[156:159], v[216:219], v[148:151], v[156:159]\n"
"ds_read2st64_b32 v[174:175], v20 offset0:4 offset1:5\n"
"v_mad_u32_u24 v8, v8, s40, v19\n"
"v_mfma_f32_16x16x32_bf16 v[160:163], v[242:245], v[148:151], v[160:163]\n"
"s_waitcnt lgkmcnt(2)\n"
"global_store_dwordx4 v10, v[144:147], s[66:67] sc1\n"
"v_mfma_f32_16x16x32_bf16 v[136:139], v[140:143], v[164:167], v[136:139]\n"
"ds_read2st64_b32 v[148:149], v9 offset0:132 offset1:133\n"
"ds_read2st64_b32 v[146:147], v9 offset0:36 offset1:37\n"
"v_mfma_f32_16x16x32_bf16 v[152:155], v[194:197], v[164:167], v[152:155]\n"
"s_waitcnt lgkmcnt(3)\n"
"v_mad_u32_u24 v11, v11, s40, v16\n"
"s_add_u32 m0, s51, 126976\n"
"s_nop 0\n"
"global_load_lds_dwordx4 v11, s[56:57] nt\n"
"ds_read2st64_b32 v[150:151], v9 offset0:134 offset1:135\n"
"v_mfma_f32_16x16x32_bf16 v[156:159], v[220:223], v[164:167], v[156:159]\n"
"s_waitcnt lgkmcnt(3)\n"
"v_mov_b32 v144, 0\n"
"v_mov_b32 v145, 0\n"
"global_store_dwordx2 v8, v[174:175], s[66:67] offset:256 sc1\n"
"v_mfma_f32_16x16x32_bf16 v[160:163], v[246:249], v[164:167], v[160:163]\n"
"s_waitcnt lgkmcnt(0)\n"
"v_mfma_f32_16x16x32_bf16 v[136:139], v[140:143], v[148:151], v[136:139]\n"
"v_mfma_f32_16x16x32_bf16 v[136:139], v[140:143], v[144:147], v[136:139]\n"
"s_add_u32 m0, s51, 129024\n"
"s_nop 0\n"
"global_load_lds_dwordx4 v11, s[58:59] nt\n"
"ds_read_b128 v[164:167], v17 offset:53248\n"
"v_mfma_f32_16x16x32_bf16 v[160:163], v[250:253], v[148:151], v[160:163]\n"
"s_setprio 2\n"
"ds_read2st64_b32 v[142:143], v14 offset0:128 offset1:129\n"
"v_mfma_f32_16x16x32_bf16 v[160:163], v[234:237], v[144:147], v[160:163]\n"
"v_rcp_f32 v8, v136\n"
"v_mfma_f32_16x16x32_bf16 v[152:155], v[198:201], v[148:151], v[152:155]\n"
"ds_read2st64_b32 v[140:141], v14 offset0:126 offset1:127\n"
"v_mfma_f32_16x16x32_bf16 v[152:155], v[182:185], v[144:147], v[152:155]\n"
"ds_read2st64_b32 v[168:169], v14 offset0:150 offset1:151\n"
"v_add_u32 v136, 0xd4800000, v136\n"
"v_mfma_f32_16x16x32_bf16 v[156:159], v[224:227], v[148:151], v[156:159]\n"
"ds_read2st64_b32 v[170:171], v14 offset0:152 offset1:153\n"
"v_mul_f32 v161, v161, v8\n"
"v_mul_f32 v160, v160, v8\n"
"v_mul_f32 v163, v163, v8\n"
"v_mul_f32 v162, v162, v8\n"
"v_cmp_lt_u32 vcc, 0x28000000, v136\n"
"v_mfma_f32_16x16x32_bf16 v[156:159], v[208:211], v[144:147], v[156:159]\n"
"s_waitcnt lgkmcnt(2)\n"
"ds_read_b128 v[136:139], v17 offset:54272\n"
"v_cvt_pk_bf16_f32 v248, v160, v161\n"
"v_mfma_f32_16x16x32_bf16 v[144:147], v[96:99], v[140:143], 0\n"
"s_add_u32 m0, s51, 131072\n"
"s_nop 0\n"
"global_load_lds_dwordx4 v11, s[60:61] nt\n"
"v_mfma_f32_16x16x32_bf16 v[148:151], v[100:103], v[140:143], 0\n"
"v_mul_f32 v156, v156, v8\n"
"v_mul_f32 v153, v153, v8\n"
"ds_read2st64_b32 v[160:161], v14 offset0:174 offset1:175\n"
"v_cvt_pk_bf16_f32 v249, v162, v163\n"
"v_mfma_f32_16x16x32_bf16 v[172:175], v[164:167], v[140:143], 0\n"
"s_waitcnt lgkmcnt(2)\n"
"ds_read2st64_b32 v[162:163], v14 offset0:176 offset1:177\n"
"v_mfma_f32_16x16x32_bf16 v[144:147], v[104:107], v[168:171], v[144:147]\n"
"ds_read_b128 v[140:143], v17 offset:55296\n"
"v_mul_f32 v152, v152, v8\n"
"v_mul_f32 v154, v154, v8\n"
"v_mul_f32 v158, v158, v8\n"
"v_mfma_f32_16x16x32_bf16 v[148:151], v[108:111], v[168:171], v[148:151]\n"
"s_waitcnt lgkmcnt(3)\n"
"ds_read2st64_b32 v[166:167], v14 offset0:200 offset1:201\n"
"ds_read2st64_b32 v[164:165], v14 offset0:198 offset1:199\n"
"v_mfma_f32_16x16x32_bf16 v[172:175], v[136:139], v[168:171], v[172:175]\n"
"s_waitcnt lgkmcnt(3)\n"
"ds_read_b128 v[136:139], v17 offset:56320\n"
"v_cvt_pk_bf16_f32 v152, v152, v153\n"
"v_mfma_f32_16x16x32_bf16 v[144:147], v[0:3], v[160:163], v[144:147]\n"
"v_mul_f32 v155, v155, v8\n"
"v_cvt_pk_bf16_f32 v153, v154, v155\n"
"s_or_b64 s[62:63], s[62:63], vcc\n"
"v_mul_f32 v157, v157, v8\n"
"v_cvt_pk_bf16_f32 v154, v156, v157\n"
"v_mfma_f32_16x16x32_bf16 v[148:151], v[112:115], v[160:163], v[148:151]\n"
"s_waitcnt lgkmcnt(3)\n"
"ds_read2st64_b32 v[156:157], v14 offset0:130 offset1:131\n"
"v_mul_f32 v159, v159, v8\n"
"v_cvt_pk_bf16_f32 v155, v158, v159\n"
"v_mfma_f32_16x16x32_bf16 v[172:175], v[140:143], v[160:163], v[172:175]\n"
"s_waitcnt lgkmcnt(2)\n"
"s_setprio 0\n"
"ds_read2st64_b32 v[158:159], v14 offset0:154 offset1:155\n"
"ds_read_b128 v[140:143], v17 offset:57344\n"
"v_mfma_f32_16x16x32_bf16 v[144:147], v[116:119], v[164:167], v[144:147]\n"
"ds_read2st64_b32 v[162:163], v14 offset0:202 offset1:203\n"
"ds_read2st64_b32 v[160:161], v14 offset0:178 offset1:179\n"
"v_mfma_f32_16x16x32_bf16 v[148:151], v[120:123], v[164:167], v[148:151]\n"
"s_waitcnt lgkmcnt(5)\n"
"v_mfma_f32_16x16x32_bf16 v[172:175], v[136:139], v[164:167], v[172:175]\n"
"s_waitcnt lgkmcnt(3)\n"
"ds_read_b128 v[136:139], v17 offset:58368\n"
"v_mfma_f32_16x16x32_bf16 v[144:147], v[4:7], v[156:159], v[144:147]\n"
"v_mfma_f32_16x16x32_bf16 v[148:151], v[124:127], v[156:159], v[148:151]\n"
"s_waitcnt lgkmcnt(3)\n"
"v_mfma_f32_16x16x32_bf16 v[172:175], v[140:143], v[156:159], v[172:175]\n"
"s_waitcnt lgkmcnt(1)\n"
"ds_read_b128 v[164:167], v12 offset:12288\n"
"v_mfma_f32_16x16x32_bf16 v[144:147], v[128:131], v[160:163], v[144:147]\n"
"ds_read_b128 v[156:159], v12 offset:14336\n"
"s_mov_b32 m0, s54\n"
"v_mfma_f32_16x16x32_bf16 v[148:151], v[132:135], v[160:163], v[148:151]\n"
"s_barrier\n"
"s_nop 3\n"
"v_cmp_class_f32_e64 vcc, v144, s64\n"
"v_cvt_pk_bf16_f32 v144, v144, v145\n"
"ds_read_b128 v[140:143], v13 offset:12288\n"
"s_waitcnt lgkmcnt(3)\n"
"v_mfma_f32_16x16x32_bf16 v[172:175], v[136:139], v[160:163], v[172:175]\n"
"ds_write_addtid_b32 v248 offset:33280\n"
"v_cvt_pk_bf16_f32 v145, v146, v147\n"
"v_cvt_pk_bf16_f32 v147, v150, v151\n"
"s_waitcnt lgkmcnt(3)\n"
"v_mfma_f32_16x16x32_bf16 v[136:139], v[164:167], v[28:31], 0\n"
"v_mfma_f32_16x16x32_bf16 v[160:163], v[164:167], v[32:35], 0\n"
"v_mfma_f32_16x16x32_bf16 v[168:171], v[164:167], v[24:27], 0\n"
"ds_read_b128 v[164:167], v13 offset:14336\n"
"ds_write_addtid_b32 v249 offset:33536\n"
"ds_write_addtid_b32 v152 offset:32256\n"
"s_waitcnt lgkmcnt(4)\n"
"v_mfma_f32_16x16x32_bf16 v[136:139], v[140:143], v[40:43], v[136:139]\n"
"v_cvt_pk_bf16_f32 v146, v148, v149\n"
"v_mfma_f32_16x16x32_bf16 v[160:163], v[140:143], v[44:47], v[160:163]\n"
"v_cvt_pk_bf16_f32 v172, v172, v173\n"
"v_mfma_f32_16x16x32_bf16 v[168:171], v[140:143], v[36:39], v[168:171]\n"
"ds_write_addtid_b32 v153 offset:32512\n"
"s_or_b64 s[62:63], s[62:63], vcc\n"
"ds_read_b128 v[140:143], v12 offset:16384\n"
"v_mfma_f32_16x16x32_bf16 v[136:139], v[156:159], v[48:51], v[136:139]\n"
"v_cvt_pk_bf16_f32 v173, v174, v175\n"
"v_mfma_f32_16x16x32_bf16 v[160:163], v[156:159], v[52:55], v[160:163]\n"
"ds_read_b128 v[148:151], v13 offset:16384\n"
"ds_write_addtid_b32 v154 offset:32768\n"
"v_mfma_f32_16x16x32_bf16 v[168:171], v[156:159], v[56:59], v[168:171]\n"
"ds_write_addtid_b32 v155 offset:33024\n"
"s_waitcnt lgkmcnt(7)\n"
"v_mfma_f32_16x16x32_bf16 v[136:139], v[164:167], v[60:63], v[136:139]\n"
"s_mov_b32 m0, s68\n"
"s_nop 0\n"
"ds_write_addtid_b32 v172 offset:46080\n"
"v_mfma_f32_16x16x32_bf16 v[160:163], v[164:167], v[64:67], v[160:163]\n"
"ds_write_addtid_b32 v146 offset:45568\n"
"v_mfma_f32_16x16x32_bf16 v[168:171], v[164:167], v[68:71], v[168:171]\n"
"ds_write_addtid_b32 v145 offset:45312\n"
"s_waitcnt lgkmcnt(6)\n"
"v_mfma_f32_16x16x32_bf16 v[136:139], v[140:143], v[72:75], v[136:139]\n"
"ds_write_addtid_b32 v173 offset:46336\n"
"v_mfma_f32_16x16x32_bf16 v[160:163], v[140:143], v[76:79], v[160:163]\n"
"v_mfma_f32_16x16x32_bf16 v[168:171], v[140:143], v[80:83], v[168:171]\n"
"ds_write_addtid_b32 v147 offset:45824\n"
"s_waitcnt lgkmcnt(7)\n"
"v_mfma_f32_16x16x32_bf16 v[136:139], v[148:151], v[84:87], v[136:139]\n"
"ds_write_addtid_b32 v144 offset:45056\n"
"v_mfma_f32_16x16x32_bf16 v[160:163], v[148:151], v[88:91], v[160:163]\n"
"v_mfma_f32_16x16x32_bf16 v[168:171], v[148:151], v[92:95], v[168:171]\n"
"s_nop 4\n"
"v_cvt_pk_bf16_f32 v196, v136, v137\n"
"s_waitcnt lgkmcnt(0)\n"
"s_waitcnt vmcnt(5)\n"
"s_barrier\n"
"v_cvt_pk_bf16_f32 v222, v160, v161\n"
"ds_read2st64_b32 v[146:147], v9 offset0:122 offset1:123\n"
"v_mov_b32 v142, 0x3f803f80\n"
"ds_read2st64_b32 v[144:145], v9 offset0:120 offset1:121\n"
"v_mov_b32 v143, 0x3f803f80\n"
"v_mov_b32 v141, 0x3f803f80\n"
"v_mov_b32 v140, 0x3f803f80\n"
"v_cvt_pk_bf16_f32 v223, v162, v163\n"
"v_cvt_pk_bf16_f32 v197, v138, v139\n"
"v_mov_b32 v148, 0\n"
"ds_read_b32 v149, v9 offset:32000\n"
"ds_read2st64_b32 v[150:151], v9 offset0:126 offset1:127\n"
"s_waitcnt lgkmcnt(2)\n"
"v_mfma_f32_16x16x32_bf16 v[136:139], v[140:143], v[144:147], 0\n"
"v_mfma_f32_16x16x32_bf16 v[152:155], v[200:203], v[144:147], 0\n"
"ds_read_u16 v8, v21 offset:960\n"
"s_setprio 2\n"
"v_mfma_f32_16x16x32_bf16 v[200:203], v[226:229], v[144:147], 0\n"
"s_and_b32 s41, 416, s55\n"
"v_add_u32 v229, s41, v15\n"
"v_cvt_pk_bf16_f32 v249, v170, v171\n"
"v_mfma_f32_16x16x32_bf16 v[156:159], v[252:255], v[144:147], 0\n"
"s_waitcnt lgkmcnt(1)\n"
"ds_read2st64_b32 v[252:253], v20 offset0:0 offset1:1\n"
"ds_read2st64_b32 v[254:255], v20 offset0:2 offset1:3\n"
"v_mfma_f32_16x16x32_bf16 v[136:139], v[140:143], v[148:151], v[136:139]\n"
"v_mov_b32 v144, 0\n"
"ds_read_b32 v145, v9 offset:33024\n"
"v_mfma_f32_16x16x32_bf16 v[152:155], v[182:185], v[148:151], v[152:155]\n"
"ds_read_u16 v229, v229 offset:736\n"
"v_cvt_pk_bf16_f32 v248, v168, v169\n"
"v_mfma_f32_16x16x32_bf16 v[200:203], v[208:211], v[148:151], v[200:203]\n"
"s_waitcnt lgkmcnt(4)\n"
"ds_read2st64_b32 v[146:147], v9 offset0:130 offset1:131\n"
"v_mad_u32_u24 v228, v8, s40, v18\n"
"s_waitcnt lgkmcnt(3)\n"
"global_store_dwordx4 v228, v[252:255], s[66:67] sc1\n"
"v_mad_u32_u24 v8, v8, s40, v19\n"
"ds_read2st64_b32 v[226:227], v20 offset0:4 offset1:5\n"
"v_mfma_f32_16x16x32_bf16 v[156:159], v[234:237], v[148:151], v[156:159]\n"
"s_waitcnt lgkmcnt(2)\n"
"v_mad_u32_u24 v229, v229, s40, v16\n"
"s_waitcnt lgkmcnt(1)\n"
"v_mfma_f32_16x16x32_bf16 v[136:139], v[140:143], v[144:147], v[136:139]\n"
"ds_read2st64_b32 v[254:255], v9 offset0:38 offset1:39\n"
"s_add_u32 m0, s51, 114688\n"
"s_nop 0\n"
"global_load_lds_dwordx4 v229, s[56:57] nt\n"
"ds_read2st64_b32 v[148:149], v9 offset0:132 offset1:133\n"
"ds_read_b32 v151, v9 offset:34560\n"
"v_mfma_f32_16x16x32_bf16 v[152:155], v[192:195], v[144:147], v[152:155]\n"
"v_mov_b32 v253, 0\n"
"v_mov_b32 v150, 0\n"
"v_mfma_f32_16x16x32_bf16 v[200:203], v[218:221], v[144:147], v[200:203]\n"
"s_waitcnt lgkmcnt(3)\n"
"global_store_dwordx2 v8, v[226:227], s[66:67] offset:256 sc1\n"
"v_mfma_f32_16x16x32_bf16 v[156:159], v[244:247], v[144:147], v[156:159]\n"
"s_waitcnt lgkmcnt(0)\n"
"ds_read_b128 v[144:147], v17 offset:53248\n"
"v_mov_b32 v252, 0\n"
"v_mfma_f32_16x16x32_bf16 v[136:139], v[140:143], v[148:151], v[136:139]\n"
"s_nop 0\n"
"v_mfma_f32_16x16x32_bf16 v[136:139], v[140:143], v[252:255], v[136:139]\n"
"ds_read2st64_b32 v[140:141], v14 offset0:126 offset1:127\n"
"s_add_u32 m0, s51, 116736\n"
"s_nop 0\n"
"global_load_lds_dwordx4 v229, s[58:59] nt\n"
"ds_read2st64_b32 v[142:143], v14 offset0:128 offset1:129\n"
"v_mfma_f32_16x16x32_bf16 v[156:159], v[248:251], v[148:151], v[156:159]\n"
"ds_read2st64_b32 v[160:161], v14 offset0:150 offset1:151\n"
"v_mfma_f32_16x16x32_bf16 v[156:159], v[236:239], v[252:255], v[156:159]\n"
"ds_read2st64_b32 v[162:163], v14 offset0:152 offset1:153\n"
"v_mfma_f32_16x16x32_bf16 v[152:155], v[196:199], v[148:151], v[152:155]\n"
"v_rcp_f32 v8, v136\n"
"v_add_u32 v136, 0xd4800000, v136\n"
"v_cmp_lt_u32 vcc, 0x28000000, v136\n"
"v_mfma_f32_16x16x32_bf16 v[152:155], v[184:187], v[252:255], v[152:155]\n"
"v_mfma_f32_16x16x32_bf16 v[200:203], v[222:225], v[148:151], v[200:203]\n"
"ds_read2st64_b32 v[148:149], v14 offset0:174 offset1:175\n"
"ds_read_b128 v[224:227], v17 offset:54272\n"
"v_mul_f32 v156, v156, v8\n"
"v_mfma_f32_16x16x32_bf16 v[200:203], v[210:213], v[252:255], v[200:203]\n"
"s_or_b64 s[62:63], s[62:63], vcc\n"
"v_mul_f32 v157, v157, v8\n"
"ds_read2st64_b32 v[150:151], v14 offset0:176 offset1:177\n"
"s_waitcnt lgkmcnt(5)\n"
"v_mfma_f32_16x16x32_bf16 v[252:255], v[96:99], v[140:143], 0\n"
"v_cvt_pk_bf16_f32 v250, v156, v157\n"
"s_add_u32 m0, s51, 118784\n"
"s_nop 0\n"
"global_load_lds_dwordx4 v229, s[60:61] nt\n"
"v_mul_f32 v158, v158, v8\n"
"v_mfma_f32_16x16x32_bf16 v[136:139], v[100:103], v[140:143], 0\n"
"v_mul_f32 v159, v159, v8\n"
"v_cvt_pk_bf16_f32 v251, v158, v159\n"
"v_mfma_f32_16x16x32_bf16 v[156:159], v[144:147], v[140:143], 0\n"
"s_waitcnt lgkmcnt(3)\n"
"ds_read2st64_b32 v[144:145], v14 offset0:198 offset1:199\n"
"v_mul_f32 v152, v152, v8\n"
"v_mfma_f32_16x16x32_bf16 v[252:255], v[104:107], v[160:163], v[252:255]\n"
"v_mul_f32 v201, v201, v8\n"
"v_mul_f32 v153, v153, v8\n"
"ds_read_b128 v[140:143], v17 offset:55296\n"
"v_mul_f32 v154, v154, v8\n"
"v_mfma_f32_16x16x32_bf16 v[136:139], v[108:111], v[160:163], v[136:139]\n"
"s_waitcnt lgkmcnt(3)\n"
"ds_read2st64_b32 v[146:147], v14 offset0:200 offset1:201\n"
"v_cvt_pk_bf16_f32 v152, v152, v153\n"
"v_mfma_f32_16x16x32_bf16 v[156:159], v[224:227], v[160:163], v[156:159]\n"
"s_waitcnt lgkmcnt(3)\n"
"v_mul_f32 v155, v155, v8\n"
"v_cvt_pk_bf16_f32 v153, v154, v155\n"
"v_mfma_f32_16x16x32_bf16 v[252:255], v[0:3], v[148:151], v[252:255]\n"
"ds_read2st64_b32 v[224:225], v14 offset0:130 offset1:131\n"
"v_mul_f32 v200, v200, v8\n"
"v_cvt_pk_bf16_f32 v154, v200, v201\n"
"v_mul_f32 v203, v203, v8\n"
"v_mfma_f32_16x16x32_bf16 v[136:139], v[112:115], v[148:151], v[136:139]\n"
"s_waitcnt lgkmcnt(2)\n"
"ds_read_b128 v[198:201], v17 offset:56320\n"
"ds_read2st64_b32 v[226:227], v14 offset0:154 offset1:155\n"
"v_mfma_f32_16x16x32_bf16 v[156:159], v[140:143], v[148:151], v[156:159]\n"
"s_waitcnt lgkmcnt(3)\n"
"ds_read_b128 v[140:143], v17 offset:57344\n"
"v_mul_f32 v202, v202, v8\n"
"v_cvt_pk_bf16_f32 v155, v202, v203\n"
"v_mfma_f32_16x16x32_bf16 v[252:255], v[116:119], v[144:147], v[252:255]\n"
"s_setprio 0\n"
"ds_read2st64_b32 v[148:149], v14 offset0:178 offset1:179\n"
"v_mfma_f32_16x16x32_bf16 v[136:139], v[120:123], v[144:147], v[136:139]\n"
"s_waitcnt lgkmcnt(3)\n"
"ds_read2st64_b32 v[150:151], v14 offset0:202 offset1:203\n"
"v_mfma_f32_16x16x32_bf16 v[156:159], v[198:201], v[144:147], v[156:159]\n"
"s_waitcnt lgkmcnt(3)\n"
"ds_read_b128 v[144:147], v12 offset:43008\n"
"v_mfma_f32_16x16x32_bf16 v[252:255], v[4:7], v[224:227], v[252:255]\n"
"v_mfma_f32_16x16x32_bf16 v[136:139], v[124:127], v[224:227], v[136:139]\n"
"s_mov_b32 m0, s54\n"
"s_waitcnt lgkmcnt(3)\n"
"v_mfma_f32_16x16x32_bf16 v[156:159], v[140:143], v[224:227], v[156:159]\n"
"s_waitcnt lgkmcnt(1)\n"
"ds_read_b128 v[198:201], v17 offset:58368\n"
"v_mfma_f32_16x16x32_bf16 v[252:255], v[128:131], v[148:151], v[252:255]\n"
"v_mfma_f32_16x16x32_bf16 v[136:139], v[132:135], v[148:151], v[136:139]\n"
"s_waitcnt vmcnt(5)\n"
"s_barrier\n"
"ds_write_addtid_b32 v251 offset:33536\n"
"ds_read_b128 v[224:227], v13 offset:43008\n"
"s_waitcnt lgkmcnt(2)\n"
"v_mfma_f32_16x16x32_bf16 v[156:159], v[198:201], v[148:151], v[156:159]\n"
"s_nop 0\n"
"v_cmp_class_f32_e64 vcc, v252, s64\n"
"v_cvt_pk_bf16_f32 v252, v252, v253\n"
"v_cvt_pk_bf16_f32 v253, v254, v255\n"
"v_cvt_pk_bf16_f32 v255, v138, v139\n"
"ds_read_b128 v[140:143], v12 offset:45056\n"
"v_cvt_pk_bf16_f32 v254, v136, v137\n"
"v_mfma_f32_16x16x32_bf16 v[200:203], v[144:147], v[28:31], 0\n"
"v_mfma_f32_16x16x32_bf16 v[148:151], v[144:147], v[32:35], 0\n"
"ds_write_addtid_b32 v250 offset:33280\n"
"v_mfma_f32_16x16x32_bf16 v[160:163], v[144:147], v[24:27], 0\n"
"s_waitcnt lgkmcnt(2)\n"
"ds_read_b128 v[144:147], v13 offset:45056\n"
"v_mfma_f32_16x16x32_bf16 v[200:203], v[224:227], v[40:43], v[200:203]\n"
"v_mfma_f32_16x16x32_bf16 v[148:151], v[224:227], v[44:47], v[148:151]\n"
"ds_write_addtid_b32 v152 offset:32256\n"
"v_mfma_f32_16x16x32_bf16 v[160:163], v[224:227], v[36:39], v[160:163]\n"
"s_waitcnt lgkmcnt(3)\n"
"v_cvt_pk_bf16_f32 v156, v156, v157\n"
"ds_read_b128 v[136:139], v13 offset:47104\n"
"ds_read_b128 v[224:227], v12 offset:47104\n"
"v_mfma_f32_16x16x32_bf16 v[200:203], v[140:143], v[48:51], v[200:203]\n"
"ds_write_addtid_b32 v154 offset:32768\n"
"v_mfma_f32_16x16x32_bf16 v[148:151], v[140:143], v[52:55], v[148:151]\n"
"ds_write_addtid_b32 v155 offset:33024\n"
"v_mfma_f32_16x16x32_bf16 v[160:163], v[140:143], v[56:59], v[160:163]\n"
"s_waitcnt lgkmcnt(5)\n"
"v_mfma_f32_16x16x32_bf16 v[200:203], v[144:147], v[60:63], v[200:203]\n"
"ds_write_addtid_b32 v153 offset:32512\n"
"s_mov_b32 m0, s68\n"
"v_mfma_f32_16x16x32_bf16 v[148:151], v[144:147], v[64:67], v[148:151]\n"
"v_cvt_pk_bf16_f32 v157, v158, v159\n"
"v_mfma_f32_16x16x32_bf16 v[160:163], v[144:147], v[68:71], v[160:163]\n"
"s_or_b64 s[62:63], s[62:63], vcc\n"
"ds_write_addtid_b32 v252 offset:45056\n"
"s_waitcnt lgkmcnt(4)\n"
"v_mfma_f32_16x16x32_bf16 v[200:203], v[224:227], v[72:75], v[200:203]\n"
"ds_write_addtid_b32 v254 offset:45568\n"
"ds_write_addtid_b32 v253 offset:45312\n"
"v_mfma_f32_16x16x32_bf16 v[148:151], v[224:227], v[76:79], v[148:151]\n"
"ds_write_addtid_b32 v156 offset:46080\n"
"v_mfma_f32_16x16x32_bf16 v[160:163], v[224:227], v[80:83], v[160:163]\n"
"ds_read_b128 v[250:253], v12 offset:49152\n"
"v_mfma_f32_16x16x32_bf16 v[200:203], v[136:139], v[84:87], v[200:203]\n"
"ds_write_addtid_b32 v157 offset:46336\n"
"ds_read_b128 v[226:229], v13 offset:49152\n"
"v_mfma_f32_16x16x32_bf16 v[148:151], v[136:139], v[88:91], v[148:151]\n"
"ds_read_b128 v[140:143], v12 offset:51200\n"
"v_mfma_f32_16x16x32_bf16 v[160:163], v[136:139], v[92:95], v[160:163]\n"
"s_waitcnt lgkmcnt(3)\n"
"v_mfma_f32_16x16x32_bf16 v[136:139], v[250:253], v[28:31], 0\n"
"ds_write_addtid_b32 v255 offset:45824\n"
"v_mfma_f32_16x16x32_bf16 v[144:147], v[250:253], v[32:35], 0\n"
"v_mfma_f32_16x16x32_bf16 v[152:155], v[250:253], v[24:27], 0\n"
"s_waitcnt lgkmcnt(2)\n"
"ds_read_b128 v[252:255], v13 offset:51200\n"
"v_mfma_f32_16x16x32_bf16 v[136:139], v[226:229], v[40:43], v[136:139]\n"
"v_mfma_f32_16x16x32_bf16 v[144:147], v[226:229], v[44:47], v[144:147]\n"
"v_cvt_pk_bf16_f32 v250, v160, v161\n"
"v_mfma_f32_16x16x32_bf16 v[152:155], v[226:229], v[36:39], v[152:155]\n"
"s_waitcnt lgkmcnt(2)\n"
"v_cvt_pk_bf16_f32 v198, v200, v201\n"
"v_mfma_f32_16x16x32_bf16 v[136:139], v[140:143], v[48:51], v[136:139]\n"
"ds_read_b128 v[226:229], v13 offset:53248\n"
"v_cvt_pk_bf16_f32 v199, v202, v203\n"
"v_mfma_f32_16x16x32_bf16 v[144:147], v[140:143], v[52:55], v[144:147]\n"
"ds_read_b128 v[200:203], v12 offset:53248\n"
"v_mfma_f32_16x16x32_bf16 v[152:155], v[140:143], v[56:59], v[152:155]\n"
"s_waitcnt lgkmcnt(2)\n"
"v_mov_b32 v141, 0x3f803f80\n"
"v_mfma_f32_16x16x32_bf16 v[136:139], v[252:255], v[60:63], v[136:139]\n"
"v_cvt_pk_bf16_f32 v225, v150, v151\n"
"v_mfma_f32_16x16x32_bf16 v[144:147], v[252:255], v[64:67], v[144:147]\n"
"v_cvt_pk_bf16_f32 v224, v148, v149\n"
"v_mfma_f32_16x16x32_bf16 v[152:155], v[252:255], v[68:71], v[152:155]\n"
"s_waitcnt lgkmcnt(0)\n"
"v_cvt_pk_bf16_f32 v251, v162, v163\n"
"v_mov_b32 v140, 0x3f803f80\n"
"v_mfma_f32_16x16x32_bf16 v[136:139], v[200:203], v[72:75], v[136:139]\n"
"s_barrier\n"
"v_mfma_f32_16x16x32_bf16 v[144:147], v[200:203], v[76:79], v[144:147]\n"
"ds_read2st64_b32 v[150:151], v9 offset0:114 offset1:115\n"
"v_mfma_f32_16x16x32_bf16 v[152:155], v[200:203], v[80:83], v[152:155]\n"
"v_mfma_f32_16x16x32_bf16 v[136:139], v[226:229], v[84:87], v[136:139]\n"
"ds_read_b32 v149, v9 offset:28928\n"
"v_mov_b32 v142, 0x3f803f80\n"
"v_mfma_f32_16x16x32_bf16 v[144:147], v[226:229], v[88:91], v[144:147]\n"
"v_mov_b32 v143, 0x3f803f80\n"
"v_mfma_f32_16x16x32_bf16 v[152:155], v[226:229], v[92:95], v[152:155]\n"
"s_and_b32 s41, 416, s55\n"
"v_add_u32 v8, s41, v15\n"
"s_nop 0\n"
"v_cvt_pk_bf16_f32 v200, v136, v137\n"
"v_mov_b32 v148, 0\n"
"s_nop 0\n"
"v_cvt_pk_bf16_f32 v226, v144, v145\n"
"ds_read2st64_b32 v[136:137], v9 offset0:116 offset1:117\n"
"v_cvt_pk_bf16_f32 v201, v138, v139\n"
"ds_read_u16 v15, v21 offset:992\n"
"ds_read2st64_b32 v[138:139], v9 offset0:118 offset1:119\n"
"v_cvt_pk_bf16_f32 v253, v154, v155\n"
"v_cvt_pk_bf16_f32 v252, v152, v153\n"
"v_cvt_pk_bf16_f32 v227, v146, v147\n"
"ds_read_u16 v8, v8 offset:768\n"
"s_waitcnt lgkmcnt(4)\n"
"v_mfma_f32_16x16x32_bf16 v[144:147], v[140:143], v[148:151], 0\n"
"ds_read2st64_b32 v[168:169], v9 offset0:132 offset1:133\n"
"v_mfma_f32_16x16x32_bf16 v[152:155], v[180:183], v[148:151], 0\n"
"ds_read2st64_b32 v[160:161], v20 offset0:0 offset1:1\n"
"ds_read2st64_b32 v[162:163], v20 offset0:2 offset1:3\n"
"v_mfma_f32_16x16x32_bf16 v[156:159], v[206:209], v[148:151], 0\n"
"s_waitcnt lgkmcnt(3)\n"
"v_mad_u32_u24 v8, v8, s40, v16\n"
"v_mfma_f32_16x16x32_bf16 v[164:167], v[232:235], v[148:151], 0\n"
"s_add_u32 m0, s51, 139264\n"
"s_nop 0\n"
"global_load_lds_dwordx4 v8, s[56:57] nt\n"
"v_mfma_f32_16x16x32_bf16 v[144:147], v[140:143], v[136:139], v[144:147]\n"
"ds_read_b32 v149, v9 offset:33024\n"
"ds_read2st64_b32 v[150:151], v9 offset0:130 offset1:131\n"
"v_mfma_f32_16x16x32_bf16 v[152:155], v[184:187], v[136:139], v[152:155]\n"
"v_mov_b32 v148, 0\n"
"v_mfma_f32_16x16x32_bf16 v[156:159], v[210:213], v[136:139], v[156:159]\n"
"v_mad_u32_u24 v16, v15, s40, v18\n"
"v_mfma_f32_16x16x32_bf16 v[164:167], v[236:239], v[136:139], v[164:167]\n"
"s_waitcnt lgkmcnt(2)\n"
"ds_read2st64_b32 v[254:255], v20 offset0:4 offset1:5\n"
"ds_read2st64_b32 v[170:171], v9 offset0:134 offset1:135\n"
"global_store_dwordx4 v16, v[160:163], s[66:67] sc1\n"
"s_waitcnt lgkmcnt(2)\n"
"s_setprio 2\n"
"v_mfma_f32_16x16x32_bf16 v[144:147], v[140:143], v[148:151], v[144:147]\n"
"s_add_u32 m0, s51, 141312\n"
"s_nop 0\n"
"global_load_lds_dwordx4 v8, s[58:59] nt\n"
"s_waitcnt lgkmcnt(0)\n"
"v_mfma_f32_16x16x32_bf16 v[144:147], v[140:143], v[168:171], v[144:147]\n"
"ds_read_b128 v[140:143], v17 offset:53248\n"
"v_mfma_f32_16x16x32_bf16 v[164:167], v[242:245], v[148:151], v[164:167]\n"
"v_mad_u32_u24 v15, v15, s40, v19\n"
"v_mfma_f32_16x16x32_bf16 v[164:167], v[246:249], v[168:171], v[164:167]\n"
"s_setprio 0\n"
"ds_read2st64_b32 v[136:137], v14 offset0:126 offset1:127\n"
"ds_read2st64_b32 v[138:139], v14 offset0:128 offset1:129\n"
"global_store_dwordx2 v15, v[254:255], s[66:67] offset:256 sc1\n"
"v_mfma_f32_16x16x32_bf16 v[152:155], v[190:193], v[148:151], v[152:155]\n"
"s_setprio 2\n"
"v_mfma_f32_16x16x32_bf16 v[152:155], v[194:197], v[168:171], v[152:155]\n"
"v_rcp_f32 v16, v144\n"
"s_add_u32 m0, s51, 143360\n"
"s_nop 0\n"
"global_load_lds_dwordx4 v8, s[60:61] nt\n"
"v_mul_f32 v166, v166, v16\n"
"v_mfma_f32_16x16x32_bf16 v[156:159], v[216:219], v[148:151], v[156:159]\n"
"v_mul_f32 v165, v165, v16\n"
"v_mul_f32 v164, v164, v16\n"
"v_add_u32 v144, 0xd4800000, v144\n"
"v_cmp_lt_u32 vcc, 0x28000000, v144\n"
"v_mfma_f32_16x16x32_bf16 v[156:159], v[220:223], v[168:171], v[156:159]\n"
"s_waitcnt lgkmcnt(0)\n"
"ds_read2st64_b32 v[146:147], v14 offset0:152 offset1:153\n"
"v_cvt_pk_bf16_f32 v254, v164, v165\n"
"v_mfma_f32_16x16x32_bf16 v[148:151], v[96:99], v[136:139], 0\n"
"ds_read2st64_b32 v[144:145], v14 offset0:150 offset1:151\n"
"v_mul_f32 v167, v167, v16\n"
"ds_read_b128 v[168:171], v17 offset:54272\n"
"v_mul_f32 v153, v153, v16\n"
"v_mul_f32 v152, v152, v16\n"
"v_cvt_pk_bf16_f32 v152, v152, v153\n"
"v_mul_f32 v158, v158, v16\n"
"v_mfma_f32_16x16x32_bf16 v[160:163], v[100:103], v[136:139], 0\n"
"s_or_b64 s[62:63], s[62:63], vcc\n"
"v_mul_f32 v157, v157, v16\n"
"v_mul_f32 v156, v156, v16\n"
"v_mul_f32 v154, v154, v16\n"
"v_cvt_pk_bf16_f32 v255, v166, v167\n"
"v_mfma_f32_16x16x32_bf16 v[164:167], v[140:143], v[136:139], 0\n"
"s_waitcnt lgkmcnt(1)\n"
"ds_read_b128 v[140:143], v17 offset:55296\n"
"ds_read2st64_b32 v[136:137], v14 offset0:174 offset1:175\n"
"v_mfma_f32_16x16x32_bf16 v[148:151], v[104:107], v[144:147], v[148:151]\n"
"ds_read2st64_b32 v[138:139], v14 offset0:176 offset1:177\n"
"v_mul_f32 v155, v155, v16\n"
"v_cvt_pk_bf16_f32 v153, v154, v155\n"
"v_mfma_f32_16x16x32_bf16 v[160:163], v[108:111], v[144:147], v[160:163]\n"
"s_waitcnt lgkmcnt(3)\n"
"v_mul_f32 v159, v159, v16\n"
"v_cvt_pk_bf16_f32 v155, v158, v159\n"
"v_mfma_f32_16x16x32_bf16 v[164:167], v[168:171], v[144:147], v[164:167]\n"
"s_setprio 0\n"
"ds_read2st64_b32 v[146:147], v14 offset0:200 offset1:201\n"
"v_cvt_pk_bf16_f32 v154, v156, v157\n"
"ds_read2st64_b32 v[144:145], v14 offset0:198 offset1:199\n"
"s_waitcnt lgkmcnt(2)\n"
"v_mfma_f32_16x16x32_bf16 v[148:151], v[0:3], v[136:139], v[148:151]\n"
"ds_read2st64_b32 v[168:169], v14 offset0:130 offset1:131\n"
"ds_read_b128 v[156:159], v17 offset:56320\n"
"v_mfma_f32_16x16x32_bf16 v[160:163], v[112:115], v[136:139], v[160:163]\n"
"ds_read2st64_b32 v[170:171], v14 offset0:154 offset1:155\n"
"v_mfma_f32_16x16x32_bf16 v[164:167], v[140:143], v[136:139], v[164:167]\n"
"s_waitcnt lgkmcnt(3)\n"
"ds_read2st64_b32 v[140:141], v14 offset0:178 offset1:179\n"
"v_mfma_f32_16x16x32_bf16 v[148:151], v[116:119], v[144:147], v[148:151]\n"
"ds_read_b128 v[136:139], v17 offset:57344\n"
"ds_read2st64_b32 v[142:143], v14 offset0:202 offset1:203\n"
"v_mfma_f32_16x16x32_bf16 v[160:163], v[120:123], v[144:147], v[160:163]\n"
"s_waitcnt lgkmcnt(4)\n"
"v_mfma_f32_16x16x32_bf16 v[164:167], v[156:159], v[144:147], v[164:167]\n"
"s_waitcnt lgkmcnt(3)\n"
"ds_read_b128 v[144:147], v17 offset:58368\n"
"v_mfma_f32_16x16x32_bf16 v[148:151], v[4:7], v[168:171], v[148:151]\n"
"ds_read_b128 v[156:159], v12 offset:24576\n"
"v_mfma_f32_16x16x32_bf16 v[160:163], v[124:127], v[168:171], v[160:163]\n"
"s_waitcnt lgkmcnt(3)\n"
"v_mfma_f32_16x16x32_bf16 v[164:167], v[136:139], v[168:171], v[164:167]\n"
"s_waitcnt lgkmcnt(2)\n"
"ds_read_b128 v[136:139], v13 offset:24576\n"
"v_mfma_f32_16x16x32_bf16 v[148:151], v[128:131], v[140:143], v[148:151]\n"
"v_mfma_f32_16x16x32_bf16 v[160:163], v[132:135], v[140:143], v[160:163]\n"
"s_waitcnt vmcnt(5)\n"
"s_barrier\n"
"s_waitcnt lgkmcnt(2)\n"
"v_mfma_f32_16x16x32_bf16 v[164:167], v[144:147], v[140:143], v[164:167]\n"
"s_waitcnt lgkmcnt(1)\n"
"v_mfma_f32_16x16x32_bf16 v[140:143], v[156:159], v[28:31], 0\n"
"s_nop 0\n"
"v_cmp_class_f32_e64 vcc, v148, s64\n"
"v_cvt_pk_bf16_f32 v148, v148, v149\n"
"ds_read_b128 v[144:147], v12 offset:26624\n"
"v_mfma_f32_16x16x32_bf16 v[168:171], v[156:159], v[32:35], 0\n"
"v_cvt_pk_bf16_f32 v149, v150, v151\n"
"s_mov_b32 m0, s54\n"
"v_mfma_f32_16x16x32_bf16 v[172:175], v[156:159], v[24:27], 0\n"
"s_waitcnt lgkmcnt(1)\n"
"v_cvt_pk_bf16_f32 v164, v164, v165\n"
"v_cvt_pk_bf16_f32 v165, v166, v167\n"
"ds_read_b128 v[156:159], v13 offset:26624\n"
"v_mfma_f32_16x16x32_bf16 v[140:143], v[136:139], v[40:43], v[140:143]\n"
"ds_write_addtid_b32 v154 offset:32768\n"
"v_cvt_pk_bf16_f32 v150, v160, v161\n"
"ds_write_addtid_b32 v155 offset:33024\n"
"v_mfma_f32_16x16x32_bf16 v[168:171], v[136:139], v[44:47], v[168:171]\n"
"v_cvt_pk_bf16_f32 v151, v162, v163\n"
"ds_write_addtid_b32 v254 offset:33280\n"
"v_mfma_f32_16x16x32_bf16 v[172:175], v[136:139], v[36:39], v[172:175]\n"
"ds_write_addtid_b32 v152 offset:32256\n"
"s_waitcnt lgkmcnt(5)\n"
"v_mfma_f32_16x16x32_bf16 v[140:143], v[144:147], v[48:51], v[140:143]\n"
"ds_read_b128 v[136:139], v12 offset:28672\n"
"ds_write_addtid_b32 v153 offset:32512\n"
"v_mfma_f32_16x16x32_bf16 v[168:171], v[144:147], v[52:55], v[168:171]\n"
"ds_write_addtid_b32 v255 offset:33536\n"
"s_mov_b32 m0, s68\n"
"s_or_b64 s[62:63], s[62:63], vcc\n"
"ds_write_addtid_b32 v149 offset:45312\n"
"v_mfma_f32_16x16x32_bf16 v[172:175], v[144:147], v[56:59], v[172:175]\n"
"s_waitcnt lgkmcnt(8)\n"
"ds_read_b128 v[144:147], v13 offset:28672\n"
"v_mfma_f32_16x16x32_bf16 v[140:143], v[156:159], v[60:63], v[140:143]\n"
"ds_write_addtid_b32 v148 offset:45056\n"
"v_mfma_f32_16x16x32_bf16 v[168:171], v[156:159], v[64:67], v[168:171]\n"
"ds_write_addtid_b32 v150 offset:45568\n"
"v_mfma_f32_16x16x32_bf16 v[172:175], v[156:159], v[68:71], v[172:175]\n"
"ds_write_addtid_b32 v151 offset:45824\n"
"s_waitcnt lgkmcnt(7)\n"
"v_mfma_f32_16x16x32_bf16 v[140:143], v[136:139], v[72:75], v[140:143]\n"
"ds_write_addtid_b32 v164 offset:46080\n"
"v_mfma_f32_16x16x32_bf16 v[168:171], v[136:139], v[76:79], v[168:171]\n"
"ds_write_addtid_b32 v165 offset:46336\n"
"v_mfma_f32_16x16x32_bf16 v[172:175], v[136:139], v[80:83], v[172:175]\n"
"s_waitcnt lgkmcnt(5)\n"
"v_mov_b32 v151, 0x3f803f80\n"
"v_mfma_f32_16x16x32_bf16 v[140:143], v[144:147], v[84:87], v[140:143]\n"
"v_mov_b32 v149, 0x3f803f80\n"
"v_mfma_f32_16x16x32_bf16 v[168:171], v[144:147], v[88:91], v[168:171]\n"
"s_waitcnt lgkmcnt(0)\n"
"s_barrier\n"
"v_mfma_f32_16x16x32_bf16 v[172:175], v[144:147], v[92:95], v[172:175]\n"
"ds_read2st64_b32 v[138:139], v9 offset0:122 offset1:123\n"
"ds_read2st64_b32 v[136:137], v9 offset0:120 offset1:121\n"
"s_nop 2\n"
"v_cvt_pk_bf16_f32 v229, v170, v171\n"
"v_cvt_pk_bf16_f32 v228, v168, v169\n"
"v_mov_b32 v150, 0x3f803f80\n"
"v_cvt_pk_bf16_f32 v203, v142, v143\n"
"v_mov_b32 v148, 0x3f803f80\n"
"v_cvt_pk_bf16_f32 v202, v140, v141\n"
"ds_read2st64_b32 v[144:145], v9 offset0:124 offset1:125\n"
"ds_read2st64_b32 v[162:163], v20 offset0:2 offset1:3\n"
"ds_read2st64_b32 v[146:147], v9 offset0:126 offset1:127\n"
"s_waitcnt lgkmcnt(3)\n"
"v_mfma_f32_16x16x32_bf16 v[140:143], v[148:151], v[136:139], 0\n"
"ds_read_u16 v16, v21 offset:1024\n"
"v_mfma_f32_16x16x32_bf16 v[152:155], v[178:181], v[136:139], 0\n"
"v_cvt_pk_bf16_f32 v254, v172, v173\n"
"v_cvt_pk_bf16_f32 v255, v174, v175\n"
"ds_read2st64_b32 v[160:161], v20 offset0:0 offset1:1\n"
"v_mfma_f32_16x16x32_bf16 v[156:159], v[204:207], v[136:139], 0\n"
"v_mfma_f32_16x16x32_bf16 v[164:167], v[230:233], v[136:139], 0\n"
"s_waitcnt lgkmcnt(1)\n"
"ds_read2st64_b32 v[138:139], v9 offset0:130 offset1:131\n"
"ds_read2st64_b32 v[136:137], v9 offset0:128 offset1:129\n"
"ds_read2st64_b32 v[230:231], v20 offset0:4 offset1:5\n"
"v_mad_u32_u24 v15, v16, s40, v18\n"
"v_mfma_f32_16x16x32_bf16 v[140:143], v[148:151], v[144:147], v[140:143]\n"
"v_mov_b32 v168, 0\n"
"v_mfma_f32_16x16x32_bf16 v[152:155], v[188:191], v[144:147], v[152:155]\n"
"v_mfma_f32_16x16x32_bf16 v[156:159], v[214:217], v[144:147], v[156:159]\n"
"ds_read_b32 v170, v9 offset:33792\n"
"v_mfma_f32_16x16x32_bf16 v[164:167], v[240:243], v[144:147], v[164:167]\n"
"s_waitcnt lgkmcnt(2)\n"
"s_setprio 2\n"
"v_mfma_f32_16x16x32_bf16 v[140:143], v[148:151], v[136:139], v[140:143]\n"
"ds_read_b32 v146, v9 offset:9728\n"
"v_mov_b32 v144, 0\n"
"ds_read_b32 v172, v9 offset:34304\n"
"v_mov_b32 v145, 0\n"
"v_mfma_f32_16x16x32_bf16 v[152:155], v[198:201], v[136:139], v[152:155]\n"
"global_store_dwordx4 v15, v[160:163], s[66:67] sc1\n"
"v_mfma_f32_16x16x32_bf16 v[156:159], v[224:227], v[136:139], v[156:159]\n"
"v_mov_b32 v147, 0\n"
"v_mov_b32 v171, 0\n"
"v_mov_b32 v174, 0\n"
"v_mfma_f32_16x16x32_bf16 v[164:167], v[250:253], v[136:139], v[164:167]\n"
"s_waitcnt lgkmcnt(0)\n"
"v_mad_u32_u24 v16, v16, s40, v19\n"
"v_mov_b32 v173, 0\n"
"s_nop 1\n"
"v_mfma_f32_16x16x32_bf16 v[140:143], v[148:151], v[170:173], v[140:143]\n"
"global_store_dwordx2 v16, v[230:231], s[66:67] offset:256 sc1\n"
"v_mov_b32 v169, 0\n"
"v_mfma_f32_16x16x32_bf16 v[140:143], v[148:151], v[144:147], v[140:143]\n"
"s_nop 0\n"
"v_mfma_f32_16x16x32_bf16 v[164:167], v[232:235], v[168:171], v[164:167]\n"
"v_mov_b32 v175, 0\n"
"s_nop 1\n"
"v_mfma_f32_16x16x32_bf16 v[164:167], v[244:247], v[172:175], v[164:167]\n"
"v_mfma_f32_16x16x32_bf16 v[164:167], v[252:255], v[144:147], v[164:167]\n"
"s_nop 0\n"
"v_rcp_f32 v16, v140\n"
"v_mfma_f32_16x16x32_bf16 v[152:155], v[180:183], v[168:171], v[152:155]\n"
"ds_read2st64_b32 v[138:139], v14 offset0:128 offset1:129\n"
"v_mfma_f32_16x16x32_bf16 v[152:155], v[192:195], v[172:175], v[152:155]\n"
"ds_read2st64_b32 v[136:137], v14 offset0:126 offset1:127\n"
"v_add_u32 v140, 0xd4800000, v140\n"
"ds_read2st64_b32 v[160:161], v14 offset0:150 offset1:151\n"
"v_mul_f32 v166, v166, v16\n"
"v_mfma_f32_16x16x32_bf16 v[152:155], v[200:203], v[144:147], v[152:155]\n"
"v_cmp_lt_u32 vcc, 0x28000000, v140\n"
"v_mfma_f32_16x16x32_bf16 v[156:159], v[206:209], v[168:171], v[156:159]\n"
"v_mul_f32 v164, v164, v16\n"
"v_mul_f32 v165, v165, v16\n"
"ds_read_b128 v[140:143], v17 offset:54272\n"
"v_mfma_f32_16x16x32_bf16 v[156:159], v[218:221], v[172:175], v[156:159]\n"
"ds_read_b128 v[148:151], v17 offset:53248\n"
"v_cvt_pk_bf16_f32 v240, v164, v165\n"
"v_mfma_f32_16x16x32_bf16 v[156:159], v[226:229], v[144:147], v[156:159]\n"
"s_waitcnt lgkmcnt(3)\n"
"ds_read2st64_b32 v[162:163], v14 offset0:152 offset1:153\n"
"v_mfma_f32_16x16x32_bf16 v[144:147], v[96:99], v[136:139], 0\n"
"v_mul_f32 v167, v167, v16\n"
"v_mul_f32 v153, v153, v16\n"
"ds_read2st64_b32 v[170:171], v14 offset0:176 offset1:177\n"
"v_mfma_f32_16x16x32_bf16 v[172:175], v[100:103], v[136:139], 0\n"
"s_or_b64 s[62:63], s[62:63], vcc\n"
"v_mul_f32 v152, v152, v16\n"
"v_cvt_pk_bf16_f32 v241, v166, v167\n"
"s_waitcnt lgkmcnt(2)\n"
"v_mfma_f32_16x16x32_bf16 v[164:167], v[148:151], v[136:139], 0\n"
"s_waitcnt lgkmcnt(1)\n"
"ds_read2st64_b32 v[168:169], v14 offset0:174 offset1:175\n"
"v_mfma_f32_16x16x32_bf16 v[144:147], v[104:107], v[160:163], v[144:147]\n"
"v_mul_f32 v155, v155, v16\n"
"v_cvt_pk_bf16_f32 v152, v152, v153\n"
"ds_read2st64_b32 v[148:149], v14 offset0:198 offset1:199\n"
"ds_read_b128 v[136:139], v17 offset:55296\n"
"v_mfma_f32_16x16x32_bf16 v[172:175], v[108:111], v[160:163], v[172:175]\n"
"v_mul_f32 v154, v154, v16\n"
"v_mul_f32 v156, v156, v16\n"
"v_mfma_f32_16x16x32_bf16 v[164:167], v[140:143], v[160:163], v[164:167]\n"
"s_waitcnt lgkmcnt(2)\n"
"ds_read2st64_b32 v[150:151], v14 offset0:200 offset1:201\n"
"ds_read2st64_b32 v[160:161], v14 offset0:130 offset1:131\n"
"ds_read_b128 v[140:143], v17 offset:56320\n"
"v_mfma_f32_16x16x32_bf16 v[144:147], v[0:3], v[168:171], v[144:147]\n"
"v_mul_f32 v158, v158, v16\n"
"v_mul_f32 v157, v157, v16\n"
"v_mfma_f32_16x16x32_bf16 v[172:175], v[112:115], v[168:171], v[172:175]\n"
"s_waitcnt lgkmcnt(3)\n"
"v_cvt_pk_bf16_f32 v153, v154, v155\n"
"v_cvt_pk_bf16_f32 v154, v156, v157\n"
"v_mfma_f32_16x16x32_bf16 v[164:167], v[136:139], v[168:171], v[164:167]\n"
"s_waitcnt lgkmcnt(2)\n"
"ds_read_b128 v[136:139], v17 offset:57344\n"
"ds_read2st64_b32 v[162:163], v14 offset0:154 offset1:155\n"
"v_mfma_f32_16x16x32_bf16 v[144:147], v[116:119], v[148:151], v[144:147]\n"
"s_setprio 0\n"
"v_mul_f32 v159, v159, v16\n"
"v_cvt_pk_bf16_f32 v155, v158, v159\n"
"ds_read2st64_b32 v[158:159], v14 offset0:202 offset1:203\n"
"v_mfma_f32_16x16x32_bf16 v[172:175], v[120:123], v[148:151], v[172:175]\n"
"s_waitcnt lgkmcnt(3)\n"
"ds_read2st64_b32 v[156:157], v14 offset0:178 offset1:179\n"
"v_mfma_f32_16x16x32_bf16 v[164:167], v[140:143], v[148:151], v[164:167]\n"
"s_waitcnt lgkmcnt(2)\n"
"v_mfma_f32_16x16x32_bf16 v[144:147], v[4:7], v[160:163], v[144:147]\n"
"ds_read_b128 v[148:151], v12 offset:12288\n"
"v_mfma_f32_16x16x32_bf16 v[172:175], v[124:127], v[160:163], v[172:175]\n"
"ds_read_b128 v[140:143], v17 offset:58368\n"
"v_mfma_f32_16x16x32_bf16 v[164:167], v[136:139], v[160:163], v[164:167]\n"
"s_waitcnt lgkmcnt(2)\n"
"ds_read_b128 v[136:139], v13 offset:12288\n"
"v_mfma_f32_16x16x32_bf16 v[144:147], v[128:131], v[156:159], v[144:147]\n"
"s_waitcnt vmcnt(2)\n"
"s_barrier\n"
"v_mfma_f32_16x16x32_bf16 v[172:175], v[132:135], v[156:159], v[172:175]\n"
"s_waitcnt lgkmcnt(1)\n"
"v_mfma_f32_16x16x32_bf16 v[164:167], v[140:143], v[156:159], v[164:167]\n"
"ds_read_b128 v[156:159], v12 offset:14336\n"
"v_mfma_f32_16x16x32_bf16 v[140:143], v[148:151], v[28:31], 0\n"
"v_mfma_f32_16x16x32_bf16 v[160:163], v[148:151], v[32:35], 0\n"
"s_mov_b32 m0, s54\n"
"s_nop 0\n"
"ds_write_addtid_b32 v241 offset:33536\n"
"v_mfma_f32_16x16x32_bf16 v[168:171], v[148:151], v[24:27], 0\n"
"s_waitcnt lgkmcnt(2)\n"
"v_cmp_class_f32_e64 vcc, v144, s64\n"
"ds_read_b128 v[148:151], v13 offset:14336\n"
"v_mfma_f32_16x16x32_bf16 v[140:143], v[136:139], v[40:43], v[140:143]\n"
"ds_write_addtid_b32 v154 offset:32768\n"
"ds_write_addtid_b32 v240 offset:33280\n"
"v_mfma_f32_16x16x32_bf16 v[160:163], v[136:139], v[44:47], v[160:163]\n"
"v_cvt_pk_bf16_f32 v144, v144, v145\n"
"v_mfma_f32_16x16x32_bf16 v[168:171], v[136:139], v[36:39], v[168:171]\n"
"s_waitcnt lgkmcnt(4)\n"
"ds_read_b128 v[136:139], v12 offset:16384\n"
"v_cvt_pk_bf16_f32 v145, v146, v147\n"
"v_cvt_pk_bf16_f32 v146, v172, v173\n"
"v_mfma_f32_16x16x32_bf16 v[140:143], v[156:159], v[48:51], v[140:143]\n"
"ds_write_addtid_b32 v155 offset:33024\n"
"ds_write_addtid_b32 v153 offset:32512\n"
"v_mfma_f32_16x16x32_bf16 v[160:163], v[156:159], v[52:55], v[160:163]\n"
"v_cvt_pk_bf16_f32 v164, v164, v165\n"
"v_cvt_pk_bf16_f32 v165, v166, v167\n"
"v_mfma_f32_16x16x32_bf16 v[168:171], v[156:159], v[56:59], v[168:171]\n"
"ds_write_addtid_b32 v152 offset:32256\n"
"ds_read_b128 v[152:155], v13 offset:16384\n"
"s_waitcnt lgkmcnt(7)\n"
"v_mfma_f32_16x16x32_bf16 v[140:143], v[148:151], v[60:63], v[140:143]\n"
"s_mov_b32 m0, s68\n"
"s_nop 0\n"
"ds_write_addtid_b32 v146 offset:45568\n"
"v_mfma_f32_16x16x32_bf16 v[160:163], v[148:151], v[64:67], v[160:163]\n"
"ds_write_addtid_b32 v164 offset:46080\n"
"v_cvt_pk_bf16_f32 v147, v174, v175\n"
"ds_write_addtid_b32 v165 offset:46336\n"
"v_mfma_f32_16x16x32_bf16 v[168:171], v[148:151], v[68:71], v[168:171]\n"
"s_waitcnt lgkmcnt(7)\n"
"v_mfma_f32_16x16x32_bf16 v[140:143], v[136:139], v[72:75], v[140:143]\n"
"ds_write_addtid_b32 v144 offset:45056\n"
"v_mfma_f32_16x16x32_bf16 v[160:163], v[136:139], v[76:79], v[160:163]\n"
"ds_write_addtid_b32 v145 offset:45312\n"
"s_or_b64 s[62:63], s[62:63], vcc\n"
"v_mfma_f32_16x16x32_bf16 v[168:171], v[136:139], v[80:83], v[168:171]\n"
"ds_write_addtid_b32 v147 offset:45824\n"
"s_waitcnt lgkmcnt(6)\n"
"v_mfma_f32_16x16x32_bf16 v[140:143], v[152:155], v[84:87], v[140:143]\n"
"v_mfma_f32_16x16x32_bf16 v[160:163], v[152:155], v[88:91], v[160:163]\n"
"v_mfma_f32_16x16x32_bf16 v[168:171], v[152:155], v[92:95], v[168:171]\n"
"s_waitcnt lgkmcnt(0)\n"
"s_barrier\n"
"ds_read2st64_b32 v[144:145], v9 offset0:112 offset1:113\n"
"s_nop 3\n"
"v_cvt_pk_bf16_f32 v204, v160, v161\n"
"ds_read2st64_b32 v[146:147], v9 offset0:114 offset1:115\n"
"v_cvt_pk_bf16_f32 v178, v140, v141\n"
"v_mov_b32 v137, 0x3f803f80\n"
"v_mov_b32 v136, 0x3f803f80\n"
"ds_read2st64_b32 v[148:149], v9 offset0:116 offset1:117\n"
"v_cvt_pk_bf16_f32 v179, v142, v143\n"
"ds_read_u16 v16, v21 offset:1056\n"
"v_mov_b32 v138, 0x3f803f80\n"
"v_mov_b32 v139, 0x3f803f80\n"
"v_cvt_pk_bf16_f32 v231, v170, v171\n"
"ds_read2st64_b32 v[150:151], v9 offset0:118 offset1:119\n"
"ds_read2st64_b32 v[240:241], v20 offset0:4 offset1:5\n"
"s_waitcnt lgkmcnt(4)\n"
"v_mfma_f32_16x16x32_bf16 v[140:143], v[136:139], v[144:147], 0\n"
"v_cvt_pk_bf16_f32 v230, v168, v169\n"
"v_cvt_pk_bf16_f32 v205, v162, v163\n"
"ds_read2st64_b32 v[160:161], v20 offset0:0 offset1:1\n"
"v_mfma_f32_16x16x32_bf16 v[152:155], v[182:185], v[144:147], 0\n"
"v_mfma_f32_16x16x32_bf16 v[156:159], v[208:211], v[144:147], 0\n"
"ds_read2st64_b32 v[162:163], v20 offset0:2 offset1:3\n"
"v_mfma_f32_16x16x32_bf16 v[164:167], v[234:237], v[144:147], 0\n"
"s_waitcnt lgkmcnt(3)\n"
"ds_read2st64_b32 v[146:147], v9 offset0:130 offset1:131\n"
"ds_read2st64_b32 v[144:145], v9 offset0:128 offset1:129\n"
"v_mfma_f32_16x16x32_bf16 v[140:143], v[136:139], v[148:151], v[140:143]\n"
"v_mfma_f32_16x16x32_bf16 v[152:155], v[190:193], v[148:151], v[152:155]\n"
"v_mad_u32_u24 v15, v16, s40, v18\n"
"v_mfma_f32_16x16x32_bf16 v[156:159], v[216:219], v[148:151], v[156:159]\n"
"v_mad_u32_u24 v16, v16, s40, v19\n"
"v_mfma_f32_16x16x32_bf16 v[164:167], v[242:245], v[148:151], v[164:167]\n"
"s_waitcnt lgkmcnt(2)\n"
"global_store_dwordx4 v15, v[160:163], s[66:67] sc1\n"
"s_waitcnt lgkmcnt(0)\n"
"v_mfma_f32_16x16x32_bf16 v[140:143], v[136:139], v[144:147], v[140:143]\n"
"ds_read2st64_b32 v[148:149], v9 offset0:132 offset1:133\n"
"v_mfma_f32_16x16x32_bf16 v[152:155], v[178:181], v[144:147], v[152:155]\n"
"ds_read2st64_b32 v[150:151], v9 offset0:134 offset1:135\n"
"ds_read2st64_b32 v[162:163], v9 offset0:36 offset1:37\n"
"global_store_dwordx2 v16, v[240:241], s[66:67] offset:256 sc1\n"
"v_mfma_f32_16x16x32_bf16 v[156:159], v[204:207], v[144:147], v[156:159]\n"
"v_mov_b32 v160, 0\n"
"v_mov_b32 v161, 0\n"
"v_mfma_f32_16x16x32_bf16 v[164:167], v[230:233], v[144:147], v[164:167]\n"
"s_waitcnt lgkmcnt(1)\n"
"v_mfma_f32_16x16x32_bf16 v[140:143], v[136:139], v[148:151], v[140:143]\n"
"s_waitcnt lgkmcnt(0)\n"
"ds_read2st64_b32 v[170:171], v14 offset0:152 offset1:153\n"
"v_mfma_f32_16x16x32_bf16 v[140:143], v[136:139], v[160:163], v[140:143]\n"
"ds_read2st64_b32 v[138:139], v14 offset0:128 offset1:129\n"
"s_setprio 2\n"
"v_mfma_f32_16x16x32_bf16 v[164:167], v[252:255], v[148:151], v[164:167]\n"
"ds_read2st64_b32 v[136:137], v14 offset0:126 offset1:127\n"
"v_mfma_f32_16x16x32_bf16 v[164:167], v[244:247], v[160:163], v[164:167]\n"
"ds_read_b128 v[144:147], v17 offset:53248\n"
"v_mfma_f32_16x16x32_bf16 v[152:155], v[200:203], v[148:151], v[152:155]\n"
"s_nop 0\n"
"v_rcp_f32 v16, v140\n"
"v_mfma_f32_16x16x32_bf16 v[152:155], v[192:195], v[160:163], v[152:155]\n"
"ds_read2st64_b32 v[168:169], v14 offset0:150 offset1:151\n"
"v_mfma_f32_16x16x32_bf16 v[156:159], v[226:229], v[148:151], v[156:159]\n"
"s_nop 0\n"
"v_mul_f32 v167, v167, v16\n"
"v_add_u32 v140, 0xd4800000, v140\n"
"v_mfma_f32_16x16x32_bf16 v[156:159], v[218:221], v[160:163], v[156:159]\n"
"s_waitcnt lgkmcnt(2)\n"
"ds_read2st64_b32 v[162:163], v14 offset0:176 offset1:177\n"
"v_cmp_lt_u32 vcc, 0x28000000, v140\n"
"ds_read_b128 v[140:143], v17 offset:54272\n"
"v_mfma_f32_16x16x32_bf16 v[148:151], v[96:99], v[136:139], 0\n"
"v_mul_f32 v155, v155, v16\n"
"v_mul_f32 v165, v165, v16\n"
"v_mul_f32 v164, v164, v16\n"
"v_mul_f32 v166, v166, v16\n"
"ds_read2st64_b32 v[160:161], v14 offset0:174 offset1:175\n"
"v_mul_f32 v152, v152, v16\n"
"v_mfma_f32_16x16x32_bf16 v[172:175], v[100:103], v[136:139], 0\n"
"s_waitcnt lgkmcnt(4)\n"
"v_cvt_pk_bf16_f32 v241, v166, v167\n"
"v_cvt_pk_bf16_f32 v240, v164, v165\n"
"v_mfma_f32_16x16x32_bf16 v[164:167], v[144:147], v[136:139], 0\n"
"s_waitcnt lgkmcnt(3)\n"
"ds_read_b128 v[136:139], v17 offset:55296\n"
"v_mfma_f32_16x16x32_bf16 v[148:151], v[104:107], v[168:171], v[148:151]\n"
"v_mul_f32 v153, v153, v16\n"
"s_or_b64 s[62:63], s[62:63], vcc\n"
"v_mfma_f32_16x16x32_bf16 v[172:175], v[108:111], v[168:171], v[172:175]\n"
"s_waitcnt lgkmcnt(2)\n"
"ds_read2st64_b32 v[146:147], v14 offset0:200 offset1:201\n"
"v_mfma_f32_16x16x32_bf16 v[164:167], v[140:143], v[168:171], v[164:167]\n"
"s_waitcnt lgkmcnt(2)\n"
"ds_read2st64_b32 v[144:145], v14 offset0:198 offset1:199\n"
"v_mfma_f32_16x16x32_bf16 v[148:151], v[0:3], v[160:163], v[148:151]\n"
"ds_read2st64_b32 v[170:171], v14 offset0:154 offset1:155\n"
"v_cvt_pk_bf16_f32 v152, v152, v153\n"
"ds_read2st64_b32 v[168:169], v14 offset0:130 offset1:131\n"
"v_mfma_f32_16x16x32_bf16 v[172:175], v[112:115], v[160:163], v[172:175]\n"
"s_waitcnt lgkmcnt(4)\n"
"v_mul_f32 v159, v159, v16\n"
"v_mul_f32 v158, v158, v16\n"
"ds_read_b128 v[140:143], v17 offset:56320\n"
"v_mfma_f32_16x16x32_bf16 v[164:167], v[136:139], v[160:163], v[164:167]\n"
"s_waitcnt lgkmcnt(3)\n"
"v_mul_f32 v156, v156, v16\n"
"s_setprio 0\n"
"ds_read_b128 v[136:139], v17 offset:57344\n"
"v_mfma_f32_16x16x32_bf16 v[148:151], v[116:119], v[144:147], v[148:151]\n"
"v_mul_f32 v154, v154, v16\n"
"v_cvt_pk_bf16_f32 v153, v154, v155\n"
"v_mfma_f32_16x16x32_bf16 v[172:175], v[120:123], v[144:147], v[172:175]\n"
"s_waitcnt lgkmcnt(1)\n"
"v_mul_f32 v157, v157, v16\n"
"v_cvt_pk_bf16_f32 v154, v156, v157\n"
"v_cvt_pk_bf16_f32 v155, v158, v159\n"
"v_mfma_f32_16x16x32_bf16 v[164:167], v[140:143], v[144:147], v[164:167]\n"
"ds_read2st64_b32 v[158:159], v14 offset0:202 offset1:203\n"
"ds_read2st64_b32 v[156:157], v14 offset0:178 offset1:179\n"
"v_mfma_f32_16x16x32_bf16 v[148:151], v[4:7], v[168:171], v[148:151]\n"
"ds_read_b128 v[140:143], v17 offset:58368\n"
"v_mfma_f32_16x16x32_bf16 v[172:175], v[124:127], v[168:171], v[172:175]\n"
"s_waitcnt lgkmcnt(3)\n"
"v_mfma_f32_16x16x32_bf16 v[164:167], v[136:139], v[168:171], v[164:167]\n"
"s_waitcnt lgkmcnt(1)\n"
"v_mfma_f32_16x16x32_bf16 v[148:151], v[128:131], v[156:159], v[148:151]\n"
"ds_read_b128 v[144:147], v12 offset:36864\n"
"ds_read_b128 v[136:139], v13 offset:36864\n"
"v_mfma_f32_16x16x32_bf16 v[172:175], v[132:135], v[156:159], v[172:175]\n"
"s_barrier\n"
"s_mov_b32 m0, s54\n"
"s_waitcnt lgkmcnt(2)\n"
"v_mfma_f32_16x16x32_bf16 v[164:167], v[140:143], v[156:159], v[164:167]\n"
"s_nop 0\n"
"v_cmp_class_f32_e64 vcc, v148, s64\n"
"s_nop 5\n"
"v_cvt_pk_bf16_f32 v164, v164, v165\n"
"s_waitcnt lgkmcnt(1)\n"
"v_mfma_f32_16x16x32_bf16 v[140:143], v[144:147], v[28:31], 0\n"
"v_mfma_f32_16x16x32_bf16 v[156:159], v[144:147], v[32:35], 0\n"
"ds_write_addtid_b32 v153 offset:32512\n"
"v_mfma_f32_16x16x32_bf16 v[32:35], v[144:147], v[24:27], 0\n"
"s_waitcnt lgkmcnt(1)\n"
"ds_read_b128 v[24:27], v13 offset:38912\n"
"v_mfma_f32_16x16x32_bf16 v[140:143], v[136:139], v[40:43], v[140:143]\n"
"ds_read_b128 v[28:31], v12 offset:38912\n"
"v_cvt_pk_bf16_f32 v148, v148, v149\n"
"v_cvt_pk_bf16_f32 v149, v150, v151\n"
"v_cvt_pk_bf16_f32 v165, v166, v167\n"
"ds_write_addtid_b32 v240 offset:33280\n"
"v_mfma_f32_16x16x32_bf16 v[156:159], v[136:139], v[44:47], v[156:159]\n"
"s_or_b64 s[62:63], s[62:63], vcc\n"
"ds_write_addtid_b32 v155 offset:33024\n"
"v_cvt_pk_bf16_f32 v151, v174, v175\n"
"ds_write_addtid_b32 v154 offset:32768\n"
"v_mfma_f32_16x16x32_bf16 v[32:35], v[136:139], v[36:39], v[32:35]\n"
"ds_write_addtid_b32 v241 offset:33536\n"
"ds_write_addtid_b32 v152 offset:32256\n"
"s_mov_b32 m0, s68\n"
"s_waitcnt lgkmcnt(5)\n"
"v_mfma_f32_16x16x32_bf16 v[140:143], v[28:31], v[48:51], v[140:143]\n"
"ds_read_b128 v[36:39], v12 offset:40960\n"
"v_mfma_f32_16x16x32_bf16 v[156:159], v[28:31], v[52:55], v[156:159]\n"
"v_cvt_pk_bf16_f32 v150, v172, v173\n"
"v_mfma_f32_16x16x32_bf16 v[32:35], v[28:31], v[56:59], v[32:35]\n"
"ds_write_addtid_b32 v149 offset:45312\n"
"v_mfma_f32_16x16x32_bf16 v[140:143], v[24:27], v[60:63], v[140:143]\n"
"ds_write_addtid_b32 v151 offset:45824\n"
"v_mfma_f32_16x16x32_bf16 v[156:159], v[24:27], v[64:67], v[156:159]\n"
"ds_write_addtid_b32 v164 offset:46080\n"
"v_mfma_f32_16x16x32_bf16 v[32:35], v[24:27], v[68:71], v[32:35]\n"
"s_waitcnt lgkmcnt(3)\n"
"ds_read_b128 v[28:31], v13 offset:40960\n"
"v_mfma_f32_16x16x32_bf16 v[140:143], v[36:39], v[72:75], v[140:143]\n"
"v_mfma_f32_16x16x32_bf16 v[156:159], v[36:39], v[76:79], v[156:159]\n"
"ds_write_addtid_b32 v165 offset:46336\n"
"ds_write_addtid_b32 v148 offset:45056\n"
"ds_write_addtid_b32 v150 offset:45568\n"
"v_mov_b32 v25, 0x3f803f80\n"
"v_mfma_f32_16x16x32_bf16 v[32:35], v[36:39], v[80:83], v[32:35]\n"
"s_waitcnt lgkmcnt(3)\n"
"v_mov_b32 v24, 0x3f803f80\n"
"v_mfma_f32_16x16x32_bf16 v[140:143], v[28:31], v[84:87], v[140:143]\n"
"v_mfma_f32_16x16x32_bf16 v[156:159], v[28:31], v[88:91], v[156:159]\n"
"v_mov_b32 v26, 0x3f803f80\n"
"v_mfma_f32_16x16x32_bf16 v[32:35], v[28:31], v[92:95], v[32:35]\n"
"s_waitcnt lgkmcnt(0)\n"
"v_mov_b32 v36, 0\n"
"v_mov_b32 v27, 0x3f803f80\n"
"s_barrier\n"
"ds_read_b32 v37, v9 offset:30976\n"
"ds_read2st64_b32 v[38:39], v9 offset0:122 offset1:123\n"
"ds_read2st64_b32 v[30:31], v9 offset0:130 offset1:131\n"
"v_cvt_pk_bf16_f32 v206, v156, v157\n"
"s_setprio 2\n"
"ds_read2st64_b32 v[28:29], v9 offset0:128 offset1:129\n"
"ds_read_u16 v16, v21 offset:1088\n"
"v_cvt_pk_bf16_f32 v180, v140, v141\n"
"v_cvt_pk_bf16_f32 v232, v32, v33\n"
"v_cvt_pk_bf16_f32 v233, v34, v35\n"
"s_waitcnt lgkmcnt(3)\n"
"v_mfma_f32_16x16x32_bf16 v[32:35], v[24:27], v[36:39], 0\n"
"v_cvt_pk_bf16_f32 v207, v158, v159\n"
"v_cvt_pk_bf16_f32 v181, v142, v143\n"
"v_mfma_f32_16x16x32_bf16 v[40:43], v[182:185], v[36:39], 0\n"
"ds_read2st64_b32 v[44:45], v20 offset0:0 offset1:1\n"
"v_mfma_f32_16x16x32_bf16 v[48:51], v[208:211], v[36:39], 0\n"
"ds_read2st64_b32 v[46:47], v20 offset0:2 offset1:3\n"
"v_mfma_f32_16x16x32_bf16 v[52:55], v[234:237], v[36:39], 0\n"
"s_waitcnt lgkmcnt(3)\n"
"ds_read2st64_b32 v[38:39], v9 offset0:134 offset1:135\n"
"ds_read_b32 v37, v9 offset:34048\n"
"v_mfma_f32_16x16x32_bf16 v[32:35], v[24:27], v[28:31], v[32:35]\n"
"ds_read2st64_b32 v[240:241], v20 offset0:4 offset1:5\n"
"v_mfma_f32_16x16x32_bf16 v[40:43], v[178:181], v[28:31], v[40:43]\n"
"s_waitcnt lgkmcnt(5)\n"
"v_mov_b32 v36, 0\n"
"v_mad_u32_u24 v15, v16, s40, v18\n"
"ds_read2st64_b32 v[60:61], v9 offset0:126 offset1:127\n"
"v_mfma_f32_16x16x32_bf16 v[48:51], v[204:207], v[28:31], v[48:51]\n"
"s_waitcnt lgkmcnt(4)\n"
"global_store_dwordx4 v15, v[44:47], s[66:67] sc1\n"
"v_mfma_f32_16x16x32_bf16 v[52:55], v[230:233], v[28:31], v[52:55]\n"
"s_waitcnt lgkmcnt(2)\n"
"ds_read_b32 v31, v9 offset:9984\n"
"ds_read2st64_b32 v[58:59], v9 offset0:124 offset1:125\n"
"v_mfma_f32_16x16x32_bf16 v[32:35], v[24:27], v[36:39], v[32:35]\n"
"v_mad_u32_u24 v16, v16, s40, v19\n"
"v_mfma_f32_16x16x32_bf16 v[40:43], v[192:195], v[36:39], v[40:43]\n"
"v_mov_b32 v30, 0\n"
"v_mov_b32 v56, 0\n"
"v_mfma_f32_16x16x32_bf16 v[48:51], v[218:221], v[36:39], v[48:51]\n"
"v_mov_b32 v29, 0\n"
"v_mov_b32 v28, 0\n"
"v_mfma_f32_16x16x32_bf16 v[52:55], v[244:247], v[36:39], v[52:55]\n"
"s_waitcnt lgkmcnt(3)\n"
"global_store_dwordx2 v16, v[240:241], s[66:67] offset:256 sc1\n"
"s_waitcnt lgkmcnt(0)\n"
"v_mfma_f32_16x16x32_bf16 v[32:35], v[24:27], v[58:61], v[32:35]\n"
"ds_read2st64_b32 v[182:183], v14 offset0:126 offset1:127\n"
"v_mov_b32 v62, 0\n"
"v_mfma_f32_16x16x32_bf16 v[32:35], v[24:27], v[28:31], v[32:35]\n"
"v_mov_b32 v63, 0\n"
"v_mov_b32 v57, 0\n"
"s_nop 1\n"
"v_mfma_f32_16x16x32_bf16 v[52:55], v[236:239], v[56:59], v[52:55]\n"
"v_mfma_f32_16x16x32_bf16 v[52:55], v[248:251], v[60:63], v[52:55]\n"
"v_mfma_f32_16x16x32_bf16 v[52:55], v[252:255], v[28:31], v[52:55]\n"
"v_mfma_f32_16x16x32_bf16 v[40:43], v[184:187], v[56:59], v[40:43]\n"
"ds_read2st64_b32 v[184:185], v14 offset0:128 offset1:129\n"
"v_mfma_f32_16x16x32_bf16 v[40:43], v[196:199], v[60:63], v[40:43]\n"
"ds_read2st64_b32 v[196:197], v14 offset0:150 offset1:151\n"
"ds_read_b128 v[186:189], v17 offset:53248\n"
"v_mfma_f32_16x16x32_bf16 v[40:43], v[200:203], v[28:31], v[40:43]\n"
"v_rcp_f32 v16, v32\n"
"v_add_u32 v32, 0xd4800000, v32\n"
"v_mfma_f32_16x16x32_bf16 v[48:51], v[210:213], v[56:59], v[48:51]\n"
"ds_read2st64_b32 v[212:213], v14 offset0:174 offset1:175\n"
"ds_read_b128 v[208:211], v17 offset:54272\n"
"v_cmp_lt_u32 vcc, 0x28000000, v32\n"
"v_mfma_f32_16x16x32_bf16 v[48:51], v[222:225], v[60:63], v[48:51]\n"
"ds_read2st64_b32 v[198:199], v14 offset0:152 offset1:153\n"
"v_mul_f32 v40, v40, v16\n"
"v_mul_f32 v53, v53, v16\n"
"v_mfma_f32_16x16x32_bf16 v[48:51], v[226:229], v[28:31], v[48:51]\n"
"s_waitcnt lgkmcnt(5)\n"
"v_mul_f32 v52, v52, v16\n"
"v_mul_f32 v55, v55, v16\n"
"v_mfma_f32_16x16x32_bf16 v[222:225], v[96:99], v[182:185], 0\n"
"ds_read_b128 v[248:251], v17 offset:55296\n"
"ds_read2st64_b32 v[214:215], v14 offset0:176 offset1:177\n"
"v_mul_f32 v54, v54, v16\n"
"v_mfma_f32_16x16x32_bf16 v[234:237], v[100:103], v[182:185], 0\n"
"s_waitcnt lgkmcnt(5)\n"
"v_cvt_pk_bf16_f32 v241, v54, v55\n"
"v_mul_f32 v42, v42, v16\n"
"v_mfma_f32_16x16x32_bf16 v[24:27], v[186:189], v[182:185], 0\n"
"ds_read_b128 v[186:189], v17 offset:56320\n"
"ds_read2st64_b32 v[182:183], v14 offset0:198 offset1:199\n"
"v_cvt_pk_bf16_f32 v240, v52, v53\n"
"v_mul_f32 v41, v41, v16\n"
"s_waitcnt lgkmcnt(4)\n"
"v_mfma_f32_16x16x32_bf16 v[222:225], v[104:107], v[196:199], v[222:225]\n"
"ds_read2st64_b32 v[184:185], v14 offset0:200 offset1:201\n"
"v_mfma_f32_16x16x32_bf16 v[234:237], v[108:111], v[196:199], v[234:237]\n"
"s_or_b64 s[62:63], s[62:63], vcc\n"
"v_cvt_pk_bf16_f32 v40, v40, v41\n"
"v_mul_f32 v43, v43, v16\n"
"v_cvt_pk_bf16_f32 v41, v42, v43\n"
"v_mfma_f32_16x16x32_bf16 v[24:27], v[208:211], v[196:199], v[24:27]\n"
"s_waitcnt lgkmcnt(3)\n"
"ds_read2st64_b32 v[210:211], v14 offset0:202 offset1:203\n"
"ds_read2st64_b32 v[196:197], v14 offset0:130 offset1:131\n"
"v_mfma_f32_16x16x32_bf16 v[222:225], v[0:3], v[212:215], v[222:225]\n"
"ds_read2st64_b32 v[208:209], v14 offset0:178 offset1:179\n"
"v_mul_f32 v48, v48, v16\n"
"v_mfma_f32_16x16x32_bf16 v[234:237], v[112:115], v[212:215], v[234:237]\n"
"v_mul_f32 v51, v51, v16\n"
"v_mul_f32 v49, v49, v16\n"
"v_mfma_f32_16x16x32_bf16 v[24:27], v[248:251], v[212:215], v[24:27]\n"
"s_mov_b32 m0, s54\n"
"ds_read2st64_b32 v[198:199], v14 offset0:154 offset1:155\n"
"ds_write_addtid_b32 v40 offset:6144\n"
"v_mul_f32 v50, v50, v16\n"
"v_cvt_pk_bf16_f32 v42, v48, v49\n"
"s_waitcnt lgkmcnt(5)\n"
"v_mfma_f32_16x16x32_bf16 v[222:225], v[116:119], v[182:185], v[222:225]\n"
"s_setprio 0\n"
"ds_write_addtid_b32 v41 offset:6400\n"
"ds_write_addtid_b32 v42 offset:6656\n"
"ds_write_addtid_b32 v240 offset:7168\n"
"v_cvt_pk_bf16_f32 v43, v50, v51\n"
"v_mfma_f32_16x16x32_bf16 v[234:237], v[120:123], v[182:185], v[234:237]\n"
"ds_write_addtid_b32 v241 offset:7424\n"
"ds_write_addtid_b32 v43 offset:6912\n"
"v_mfma_f32_16x16x32_bf16 v[24:27], v[186:189], v[182:185], v[24:27]\n"
"s_waitcnt lgkmcnt(6)\n"
"v_mov_b32 v215, 0x3f803f80\n"
"v_mfma_f32_16x16x32_bf16 v[222:225], v[4:7], v[196:199], v[222:225]\n"
"v_mov_b32 v213, 0x3f803f80\n"
"v_mov_b32 v214, 0x3f803f80\n"
"ds_read_b128 v[182:185], v17 offset:57344\n"
"v_mfma_f32_16x16x32_bf16 v[234:237], v[124:127], v[196:199], v[234:237]\n"
"s_waitcnt lgkmcnt(1)\n"
"ds_read_b128 v[186:189], v17 offset:58368\n"
"s_barrier\n"
"ds_read2st64_b32 v[238:239], v9 offset0:112 offset1:113\n"
"s_waitcnt lgkmcnt(2)\n"
"v_mfma_f32_16x16x32_bf16 v[24:27], v[182:185], v[196:199], v[24:27]\n"
"ds_read2st64_b32 v[240:241], v9 offset0:114 offset1:115\n"
"v_subrev_u32 v16, 24576, v9\n"
"v_mfma_f32_16x16x32_bf16 v[222:225], v[128:131], v[208:211], v[222:225]\n"
"s_setprio 2\n"
"ds_read2st64_b32 v[30:31], v16 offset0:16 offset1:17\n"
"v_mov_b32 v212, 0x3f803f80\n"
"v_mfma_f32_16x16x32_bf16 v[234:237], v[132:135], v[208:211], v[234:237]\n"
"s_waitcnt lgkmcnt(3)\n"
"ds_read2st64_b32 v[184:185], v9 offset0:118 offset1:119\n"
"ds_read2st64_b32 v[182:183], v9 offset0:116 offset1:117\n"
"v_mfma_f32_16x16x32_bf16 v[24:27], v[186:189], v[208:211], v[24:27]\n"
"s_waitcnt lgkmcnt(3)\n"
"v_mfma_f32_16x16x32_bf16 v[186:189], v[212:215], v[238:241], 0\n"
"ds_read2st64_b32 v[32:33], v16 offset0:18 offset1:19\n"
"v_cmp_class_f32_e64 vcc, v222, s64\n"
"v_mfma_f32_16x16x32_bf16 v[196:199], v[190:193], v[238:241], 0\n"
"v_cvt_pk_bf16_f32 v222, v222, v223\n"
"v_cvt_pk_bf16_f32 v223, v224, v225\n"
"v_mfma_f32_16x16x32_bf16 v[208:211], v[216:219], v[238:241], 0\n"
"v_cvt_pk_bf16_f32 v224, v234, v235\n"
"v_cvt_pk_bf16_f32 v225, v236, v237\n"
"v_mfma_f32_16x16x32_bf16 v[234:237], v[242:245], v[238:241], 0\n"
"s_waitcnt lgkmcnt(1)\n"
"v_mov_b32 v29, 0\n"
"v_mov_b32 v28, 0\n"
"v_mfma_f32_16x16x32_bf16 v[186:189], v[212:215], v[182:185], v[186:189]\n"
"s_waitcnt lgkmcnt(0)\n"
"s_setprio 0\n"
"ds_read2st64_b32 v[190:191], v14 offset0:26 offset1:27\n"
"v_mfma_f32_16x16x32_bf16 v[186:189], v[212:215], v[30:33], v[186:189]\n"
"s_setprio 2\n"
"v_cvt_pk_bf16_f32 v24, v24, v25\n"
"v_mov_b32 v35, 0\n"
"v_mfma_f32_16x16x32_bf16 v[234:237], v[252:255], v[182:185], v[234:237]\n"
"v_cvt_pk_bf16_f32 v25, v26, v27\n"
"v_mov_b32 v34, 0\n"
"v_mfma_f32_16x16x32_bf16 v[234:237], v[244:247], v[28:31], v[234:237]\n"
"s_setprio 0\n"
"v_rcp_f32 v16, v186\n"
"v_mfma_f32_16x16x32_bf16 v[234:237], v[230:233], v[32:35], v[234:237]\n"
"v_add_u32 v186, 0xd4800000, v186\n"
"v_mfma_f32_16x16x32_bf16 v[196:199], v[200:203], v[182:185], v[196:199]\n"
"s_mov_b32 m0, s54\n"
"s_or_b64 s[62:63], s[62:63], vcc\n"
"ds_read2st64_b32 v[188:189], v14 offset0:24 offset1:25\n"
"v_cmp_lt_u32 vcc, 0x28000000, v186\n"
"v_mfma_f32_16x16x32_bf16 v[196:199], v[192:195], v[28:31], v[196:199]\n"
"ds_read_b128 v[192:195], v17 offset:53248\n"
"v_mul_f32 v235, v235, v16\n"
"v_mfma_f32_16x16x32_bf16 v[196:199], v[178:181], v[32:35], v[196:199]\n"
"v_mul_f32 v237, v237, v16\n"
"v_mul_f32 v234, v234, v16\n"
"v_cvt_pk_bf16_f32 v216, v234, v235\n"
"v_mfma_f32_16x16x32_bf16 v[208:211], v[226:229], v[182:185], v[208:211]\n"
"s_or_b64 s[62:63], s[62:63], vcc\n"
"ds_write_addtid_b32 v216 offset:33280\n"
"v_mul_f32 v236, v236, v16\n"
"v_cvt_pk_bf16_f32 v217, v236, v237\n"
"v_mfma_f32_16x16x32_bf16 v[208:211], v[218:221], v[28:31], v[208:211]\n"
"ds_write_addtid_b32 v217 offset:33536\n"
"v_mul_f32 v197, v197, v16\n"
"v_mfma_f32_16x16x32_bf16 v[208:211], v[204:207], v[32:35], v[208:211]\n"
"s_waitcnt lgkmcnt(3)\n"
"ds_read2st64_b32 v[180:181], v14 offset0:50 offset1:51\n"
"v_mul_f32 v196, v196, v16\n"
"v_cvt_pk_bf16_f32 v196, v196, v197\n"
"v_mfma_f32_16x16x32_bf16 v[182:185], v[96:99], v[188:191], 0\n"
"ds_read2st64_b32 v[178:179], v14 offset0:48 offset1:49\n"
"ds_write_addtid_b32 v196 offset:32256\n"
"v_mul_f32 v198, v198, v16\n"
"v_mul_f32 v199, v199, v16\n"
"v_mul_f32 v208, v208, v16\n"
"v_mul_f32 v210, v210, v16\n"
"v_cvt_pk_bf16_f32 v197, v198, v199\n"
"v_mul_f32 v211, v211, v16\n"
"v_cvt_pk_bf16_f32 v199, v210, v211\n"
"v_mfma_f32_16x16x32_bf16 v[200:203], v[100:103], v[188:191], 0\n"
"ds_write_addtid_b32 v199 offset:33024\n"
"ds_write_addtid_b32 v197 offset:32512\n"
"s_waitcnt lgkmcnt(7)\n"
"v_mfma_f32_16x16x32_bf16 v[204:207], v[192:195], v[188:191], 0\n"
"ds_read2st64_b32 v[192:193], v14 offset0:74 offset1:75\n"
"ds_read_b128 v[194:197], v17 offset:55296\n"
"ds_read2st64_b32 v[190:191], v14 offset0:72 offset1:73\n"
"ds_read_b128 v[186:189], v17 offset:54272\n"
"v_mul_f32 v209, v209, v16\n"
"v_cvt_pk_bf16_f32 v198, v208, v209\n"
"ds_write_addtid_b32 v198 offset:32768\n"
"s_waitcnt lgkmcnt(8)\n"
"v_mfma_f32_16x16x32_bf16 v[182:185], v[104:107], v[178:181], v[182:185]\n"
"s_mov_b32 m0, s68\n"
"s_nop 0\n"
"ds_write_addtid_b32 v24 offset:46080\n"
"ds_write_addtid_b32 v222 offset:45056\n"
"v_mfma_f32_16x16x32_bf16 v[200:203], v[108:111], v[178:181], v[200:203]\n"
"ds_read2st64_b32 v[208:209], v14 offset0:28 offset1:29\n"
"ds_write_addtid_b32 v225 offset:45824\n"
"s_waitcnt lgkmcnt(5)\n"
"v_mfma_f32_16x16x32_bf16 v[204:207], v[186:189], v[178:181], v[204:207]\n"
"ds_read2st64_b32 v[178:179], v14 offset0:96 offset1:97\n"
"ds_read2st64_b32 v[180:181], v14 offset0:98 offset1:99\n"
"ds_write_addtid_b32 v224 offset:45568\n"
"ds_read2st64_b32 v[210:211], v14 offset0:52 offset1:53\n"
"v_mfma_f32_16x16x32_bf16 v[182:185], v[0:3], v[190:193], v[182:185]\n"
"ds_read_b128 v[186:189], v17 offset:56320\n"
"ds_write_addtid_b32 v223 offset:45312\n"
"v_mfma_f32_16x16x32_bf16 v[200:203], v[112:115], v[190:193], v[200:203]\n"
"ds_read2st64_b32 v[214:215], v14 offset0:100 offset1:101\n"
"ds_write_addtid_b32 v25 offset:46336\n"
"v_mfma_f32_16x16x32_bf16 v[204:207], v[194:197], v[190:193], v[204:207]\n"
"s_waitcnt lgkmcnt(6)\n"
"v_mfma_f32_16x16x32_bf16 v[182:185], v[116:119], v[178:181], v[182:185]\n"
"ds_read2st64_b32 v[212:213], v14 offset0:76 offset1:77\n"
"v_mfma_f32_16x16x32_bf16 v[200:203], v[120:123], v[178:181], v[200:203]\n"
"s_waitcnt lgkmcnt(4)\n"
"v_mfma_f32_16x16x32_bf16 v[204:207], v[186:189], v[178:181], v[204:207]\n"
"ds_read_b128 v[194:197], v17 offset:58368\n"
"ds_read_b128 v[190:193], v17 offset:57344\n"
"v_mfma_f32_16x16x32_bf16 v[182:185], v[4:7], v[208:211], v[182:185]\n"
"s_waitcnt lgkmcnt(2)\n"
"ds_read_b128 v[186:189], v17 offset:53248\n"
"s_barrier\n"
"ds_read2st64_b32 v[178:179], v14 offset0:126 offset1:127\n"
"v_mfma_f32_16x16x32_bf16 v[200:203], v[124:127], v[208:211], v[200:203]\n"
"s_waitcnt lgkmcnt(2)\n"
"ds_read2st64_b32 v[180:181], v14 offset0:128 offset1:129\n"
"s_setprio 2\n"
"v_mfma_f32_16x16x32_bf16 v[204:207], v[190:193], v[208:211], v[204:207]\n"
"ds_read_u16 v16, v21 offset:1120\n"
"v_mfma_f32_16x16x32_bf16 v[182:185], v[128:131], v[212:215], v[182:185]\n"
"ds_read2st64_b32 v[192:193], v14 offset0:152 offset1:153\n"
"v_mfma_f32_16x16x32_bf16 v[200:203], v[132:135], v[212:215], v[200:203]\n"
"ds_read2st64_b32 v[190:191], v14 offset0:150 offset1:151\n"
"v_mfma_f32_16x16x32_bf16 v[204:207], v[194:197], v[212:215], v[204:207]\n"
"s_waitcnt lgkmcnt(3)\n"
"ds_read_b128 v[194:197], v17 offset:54272\n"
"ds_read2st64_b32 v[210:211], v14 offset0:176 offset1:177\n"
"v_mfma_f32_16x16x32_bf16 v[212:215], v[96:99], v[178:181], 0\n"
"ds_read2st64_b32 v[208:209], v14 offset0:174 offset1:175\n"
"v_cmp_class_f32_e64 vcc, v182, s64\n"
"v_mfma_f32_16x16x32_bf16 v[216:219], v[100:103], v[178:181], 0\n"
"s_waitcnt lgkmcnt(5)\n"
"v_mad_u32_u24 v21, v16, s40, v18\n"
"v_cvt_pk_bf16_f32 v182, v182, v183\n"
"v_mfma_f32_16x16x32_bf16 v[220:223], v[186:189], v[178:181], 0\n"
"s_waitcnt lgkmcnt(3)\n"
"ds_read2st64_b32 v[178:179], v20 offset0:0 offset1:1\n"
"v_cvt_pk_bf16_f32 v183, v184, v185\n"
"v_mfma_f32_16x16x32_bf16 v[212:215], v[104:107], v[190:193], v[212:215]\n"
"ds_read_b128 v[186:189], v17 offset:55296\n"
"v_cvt_pk_bf16_f32 v184, v200, v201\n"
"v_mfma_f32_16x16x32_bf16 v[216:219], v[108:111], v[190:193], v[216:219]\n"
"s_waitcnt lgkmcnt(4)\n"
"ds_read2st64_b32 v[180:181], v20 offset0:2 offset1:3\n"
"v_cvt_pk_bf16_f32 v185, v202, v203\n"
"v_mfma_f32_16x16x32_bf16 v[220:223], v[194:197], v[190:193], v[220:223]\n"
"s_waitcnt lgkmcnt(3)\n"
"ds_read2st64_b32 v[192:193], v14 offset0:200 offset1:201\n"
"v_mad_u32_u24 v16, v16, s40, v19\n"
"v_mfma_f32_16x16x32_bf16 v[212:215], v[0:3], v[208:211], v[212:215]\n"
"ds_read2st64_b32 v[190:191], v14 offset0:198 offset1:199\n"
"v_cvt_pk_bf16_f32 v204, v204, v205\n"
"v_mfma_f32_16x16x32_bf16 v[216:219], v[112:115], v[208:211], v[216:219]\n"
"s_mov_b32 m0, s68\n"
"ds_read_b128 v[196:199], v17 offset:56320\n"
"s_or_b64 s[62:63], s[62:63], vcc\n"
"ds_read2st64_b32 v[194:195], v20 offset0:4 offset1:5\n"
"s_waitcnt lgkmcnt(5)\n"
"v_mfma_f32_16x16x32_bf16 v[220:223], v[186:189], v[208:211], v[220:223]\n"
"s_waitcnt lgkmcnt(4)\n"
"ds_read2st64_b32 v[200:201], v14 offset0:130 offset1:131\n"
"ds_read2st64_b32 v[202:203], v14 offset0:154 offset1:155\n"
"global_store_dwordx4 v21, v[178:181], s[66:67] sc1\n"
"ds_read2st64_b32 v[186:187], v14 offset0:178 offset1:179\n"
"s_waitcnt lgkmcnt(5)\n"
"v_mfma_f32_16x16x32_bf16 v[212:215], v[116:119], v[190:193], v[212:215]\n"
"ds_read_b128 v[178:181], v17 offset:57344\n"
"v_cvt_pk_bf16_f32 v205, v206, v207\n"
"v_mfma_f32_16x16x32_bf16 v[216:219], v[120:123], v[190:193], v[216:219]\n"
"s_waitcnt lgkmcnt(5)\n"
"ds_read2st64_b32 v[188:189], v14 offset0:202 offset1:203\n"
"v_mfma_f32_16x16x32_bf16 v[220:223], v[196:199], v[190:193], v[220:223]\n"
"s_waitcnt lgkmcnt(5)\n"
"ds_read_b128 v[190:193], v17 offset:58368\n"
"global_store_dwordx2 v16, v[194:195], s[66:67] offset:256 sc1\n"
"s_waitcnt lgkmcnt(4)\n"
"v_mfma_f32_16x16x32_bf16 v[212:215], v[4:7], v[200:203], v[212:215]\n"
"ds_write_addtid_b32 v182 offset:51456\n"
"v_mfma_f32_16x16x32_bf16 v[216:219], v[124:127], v[200:203], v[216:219]\n"
"ds_write_addtid_b32 v184 offset:51968\n"
"s_waitcnt lgkmcnt(4)\n"
"v_mfma_f32_16x16x32_bf16 v[220:223], v[178:181], v[200:203], v[220:223]\n"
"ds_write_addtid_b32 v183 offset:51712\n"
"s_waitcnt lgkmcnt(4)\n"
"v_mfma_f32_16x16x32_bf16 v[212:215], v[128:131], v[186:189], v[212:215]\n"
"ds_write_addtid_b32 v185 offset:52224\n"
"ds_write_addtid_b32 v204 offset:52480\n"
"v_mfma_f32_16x16x32_bf16 v[216:219], v[132:135], v[186:189], v[216:219]\n"
"s_setprio 0\n"
"ds_write_addtid_b32 v205 offset:52736\n"
"s_waitcnt lgkmcnt(6)\n"
"v_mfma_f32_16x16x32_bf16 v[220:223], v[190:193], v[186:189], v[220:223]\n"
"s_nop 0\n"
"v_cmp_class_f32_e64 vcc, v212, s64\n"
"v_cvt_pk_bf16_f32 v212, v212, v213\n"
"v_cvt_pk_bf16_f32 v213, v214, v215\n"
"v_cvt_pk_bf16_f32 v214, v216, v217\n"
"v_cvt_pk_bf16_f32 v215, v218, v219\n"
"s_nop 0\n"
"s_or_b64 s[62:63], s[62:63], vcc\n"
"ds_write_addtid_b32 v214 offset:58368\n"
"ds_write_addtid_b32 v213 offset:58112\n"
"v_cvt_pk_bf16_f32 v220, v220, v221\n"
"v_cvt_pk_bf16_f32 v221, v222, v223\n"
"s_cmp_lg_u64 s[62:63], 0\n"
"s_cselect_b32 s41, 1, 0\n"
"v_mov_b32 v178, s65\n"
"v_mov_b32 v179, s41\n"
"ds_write_b32 v178, v179 offset:0\n"
"ds_write_addtid_b32 v212 offset:57856\n"
"ds_write_addtid_b32 v221 offset:59136\n"
"ds_write_addtid_b32 v215 offset:58624\n"
"ds_write_addtid_b32 v220 offset:58880\n"
"s_waitcnt lgkmcnt(0)\n"
"s_barrier\n"
"ds_read2st64_b32 v[180:181], v20 offset0:27 offset1:28\n"
"ds_read2st64_b32 v[178:179], v20 offset0:25 offset1:26\n"
"ds_read2st64_b32 v[182:183], v20 offset0:29 offset1:30\n"
"ds_read2st64_b32 v[184:185], v20 offset0:50 offset1:51\n"
"ds_read2st64_b32 v[254:255], v20 offset0:54 offset1:55\n"
"s_waitcnt lgkmcnt(3)\n"
"global_store_dwordx4 v177, v[178:181], s[66:67] sc1\n"
"s_waitcnt lgkmcnt(2)\n"
"global_store_dwordx2 v23, v[182:183], s[66:67] offset:256 sc1\n"
"ds_read2st64_b32 v[186:187], v20 offset0:52 offset1:53\n"
"s_waitcnt lgkmcnt(0)\n"
"global_store_dwordx4 v176, v[184:187], s[66:67] sc1\n"
"global_store_dwordx2 v22, v[254:255], s[66:67] offset:256 sc1\n"
    );
    PEND;
  }
  const u32x4 fl = *LP(const u32x4, FLAG);
  if (__builtin_expect(UNI(fl[0] | fl[1] | fl[2] | fl[3]) != 0, 0)) {
    asm volatile("s_waitcnt vmcnt(0) lgkmcnt(0)\n\ts_barrier" ::: "memory");
    __builtin_amdgcn_s_setprio(0);
    Regs R;
    int w2 = w;
    asm volatile("" : "+s"(w2), "+s"(G.t0), "+s"(G.sr), "+s"(G.sc));
    prologue(x, wqkv, wo, rpb, out, G, w2, R);
    BODY(
"s_add_u32 s50, s54, 28672\n"
"s_bitcmp1_b32 s45, 0\n"
"s_cselect_b32 s53, -1, 0\n"
"s_lshl_b32 s52, s53, 16\n"
"s_setprio 1\n"
"s_mov_b32 s60, 0x3e553b95\n"
"s_mov_b32 s61, 0x3fb8aa3b\n"
"v_add_u32 v27, 77824, v3\n"
"v_mov_b32 v24, 0\n"
"v_mov_b32 v25, 0\n"
"v_mov_b32 v178, 0xf1800000\n"
"v_mov_b32 v179, 0xf1800000\n"
"v_mov_b32 v180, 0xf1800000\n"
"v_mov_b32 v181, 0xf1800000\n"
"s_bfm_b64 exec, 18, 0\n"
"ds_write_b128 v9, v[178:181] offset:0\n"
"s_mov_b64 exec, -1\n"
"s_waitcnt vmcnt(5)\n"
"ds_read_u16 v13, v13 offset:0\n"
"ds_read_u16 v14, v14 offset:0\n"
"ds_read_u16 v15, v15 offset:0\n"
"ds_read_u16 v16, v16 offset:0\n"
"ds_read_u16 v17, v17 offset:0\n"
"ds_read_u16 v18, v18 offset:0\n"
"ds_read_u16 v12, v12 offset:0\n"
"ds_read_u16 v20, v20 offset:0\n"
"ds_read_u16 v21, v21 offset:0\n"
"ds_read_u16 v22, v22 offset:0\n"
"ds_read_u16 v23, v23 offset:0\n"
"s_waitcnt lgkmcnt(10)\n"
"v_lshlrev_b32 v13, 16, v13\n"
"s_waitcnt lgkmcnt(9)\n"
"v_lshlrev_b32 v14, 16, v14\n"
"v_mul_f32 v13, s61, v13\n"
"s_waitcnt lgkmcnt(8)\n"
"v_lshlrev_b32 v15, 16, v15\n"
"v_mul_f32 v14, s61, v14\n"
"s_waitcnt lgkmcnt(7)\n"
"v_lshlrev_b32 v16, 16, v16\n"
"v_mul_f32 v15, s61, v15\n"
"s_waitcnt lgkmcnt(6)\n"
"v_lshlrev_b32 v17, 16, v17\n"
"v_mul_f32 v16, s61, v16\n"
"s_waitcnt lgkmcnt(5)\n"
"v_lshlrev_b32 v18, 16, v18\n"
"v_mul_f32 v17, s61, v17\n"
"s_waitcnt lgkmcnt(4)\n"
"v_lshlrev_b32 v12, 16, v12\n"
"v_mul_f32 v18, s61, v18\n"
"s_waitcnt lgkmcnt(3)\n"
"v_lshlrev_b32 v20, 16, v20\n"
"v_mul_f32 v12, s61, v12\n"
"s_waitcnt lgkmcnt(2)\n"
"v_lshlrev_b32 v21, 16, v21\n"
"v_mul_f32 v20, s61, v20\n"
"s_waitcnt lgkmcnt(1)\n"
"v_lshlrev_b32 v22, 16, v22\n"
"v_mul_f32 v21, s61, v21\n"
"s_waitcnt lgkmcnt(0)\n"
"v_lshlrev_b32 v23, 16, v23\n"
"v_mul_f32 v22, s61, v22\n"
"v_mul_f32 v23, s61, v23\n"
"v_mov_b32 v178, v13\n"
"v_mov_b32 v179, v14\n"
"v_mov_b32 v180, v15\n"
"v_mov_b32 v181, v16\n"
"v_mov_b32 v182, v17\n"
"v_mov_b32 v183, v18\n"
"s_bfm_b64 exec, 40, 0\n"
"ds_write_b128 v8, v[180:183] offset:0\n"
"ds_write_b128 v8, v[14:17] offset:64\n"
"ds_write_b128 v8, v[178:181] offset:128\n"
"ds_write_b128 v8, v[12:15] offset:192\n"
"ds_write_b128 v8, v[20:23] offset:256\n"
"s_mov_b64 exec, -1\n"
"s_waitcnt lgkmcnt(0)\n"
"s_add_u32 s42, s46, 37248\n"
"s_addc_u32 s43, s47, 0\n"
"s_add_u32 m0, s50, 4294945792\n"
"s_nop 0\n"
"global_load_lds_dwordx4 v0, s[42:43]\n"
"s_add_u32 m0, s50, 4294946816\n"
"s_nop 0\n"
"global_load_lds_dwordx4 v1, s[42:43]\n"
"s_add_u32 m0, s50, 4294947840\n"
"s_nop 0\n"
"global_load_lds_dwordx4 v2, s[42:43]\n"
"s_add_u32 s42, s46, 74112\n"
"s_addc_u32 s43, s47, 0\n"
"s_add_u32 m0, s50, 49152\n"
"s_nop 0\n"
"global_load_lds_dwordx4 v0, s[42:43]\n"
"s_add_u32 m0, s50, 50176\n"
"s_nop 0\n"
"global_load_lds_dwordx4 v1, s[42:43]\n"
"s_add_u32 m0, s50, 51200\n"
"s_nop 0\n"
"global_load_lds_dwordx4 v2, s[42:43]\n"
"s_barrier\n"
"s_waitcnt vmcnt(6)\n"
"ds_read_b64_tr_b16 v[8:9], v3 offset:4096\n"
"ds_read_b64_tr_b16 v[10:11], v3 offset:4864\n"
"ds_read_b128 v[178:181], v4 offset:24576\n"
"ds_read_b64_tr_b16 v[12:13], v3 offset:4128\n"
"ds_read_b64_tr_b16 v[14:15], v3 offset:4896\n"
"ds_read_b64_tr_b16 v[16:17], v3 offset:4160\n"
"ds_read_b64_tr_b16 v[18:19], v3 offset:4928\n"
"ds_read_b128 v[182:185], v4 offset:30720\n"
"ds_read_b128 v[186:189], v4 offset:36864\n"
"s_waitcnt lgkmcnt(6)\n"
"v_mfma_f32_16x16x32_bf16 v[196:199], v[8:11], v[178:181], 0\n"
"s_waitcnt lgkmcnt(4)\n"
"v_mfma_f32_16x16x32_bf16 v[190:193], v[12:15], v[178:181], 0\n"
"s_waitcnt lgkmcnt(2)\n"
"v_mfma_f32_16x16x32_bf16 v[212:215], v[16:19], v[178:181], 0\n"
"s_waitcnt lgkmcnt(1)\n"
"ds_read_b128 v[216:219], v4 offset:43008\n"
"v_mfma_f32_16x16x32_bf16 v[202:205], v[8:11], v[182:185], 0\n"
"v_mfma_f32_16x16x32_bf16 v[220:223], v[12:15], v[182:185], 0\n"
"v_mfma_f32_16x16x32_bf16 v[236:239], v[16:19], v[182:185], 0\n"
"s_waitcnt lgkmcnt(1)\n"
"ds_read_b128 v[240:243], v4 offset:49152\n"
"v_mfma_f32_16x16x32_bf16 v[226:229], v[8:11], v[186:189], 0\n"
"v_mfma_f32_16x16x32_bf16 v[244:247], v[12:15], v[186:189], 0\n"
"v_mfma_f32_16x16x32_bf16 v[248:251], v[16:19], v[186:189], 0\n"
"s_waitcnt lgkmcnt(1)\n"
"ds_read_b128 v[252:255], v4 offset:55296\n"
"v_mfma_f32_16x16x32_bf16 v[232:235], v[8:11], v[216:219], 0\n"
"v_mfma_f32_16x16x32_bf16 v[182:185], v[12:15], v[216:219], 0\n"
"v_mfma_f32_16x16x32_bf16 v[84:87], v[16:19], v[216:219], 0\n"
"s_waitcnt vmcnt(3)\n"
"ds_read_b64_tr_b16 v[20:21], v3 offset:7168\n"
"s_waitcnt lgkmcnt(2)\n"
"v_mfma_f32_16x16x32_bf16 v[208:211], v[8:11], v[240:243], 0\n"
"ds_read_b64_tr_b16 v[22:23], v3 offset:7936\n"
"v_mfma_f32_16x16x32_bf16 v[186:189], v[12:15], v[240:243], 0\n"
"ds_read_b128 v[36:39], v5 offset:24576\n"
"v_mfma_f32_16x16x32_bf16 v[216:219], v[16:19], v[240:243], 0\n"
"s_waitcnt lgkmcnt(3)\n"
"ds_read_b64_tr_b16 v[28:29], v3 offset:7200\n"
"v_mfma_f32_16x16x32_bf16 v[178:181], v[8:11], v[252:255], 0\n"
"ds_read_b64_tr_b16 v[30:31], v3 offset:7968\n"
"v_mfma_f32_16x16x32_bf16 v[88:91], v[12:15], v[252:255], 0\n"
"ds_read_b64_tr_b16 v[32:33], v3 offset:7232\n"
"ds_read_b64_tr_b16 v[34:35], v3 offset:8000\n"
"v_mfma_f32_16x16x32_bf16 v[240:243], v[16:19], v[252:255], 0\n"
"s_waitcnt lgkmcnt(4)\n"
"ds_read_b128 v[40:43], v5 offset:30720\n"
"ds_read_b128 v[44:47], v5 offset:36864\n"
"v_mfma_f32_16x16x32_bf16 v[196:199], v[20:23], v[36:39], v[196:199]\n"
"s_waitcnt lgkmcnt(4)\n"
"v_mfma_f32_16x16x32_bf16 v[190:193], v[28:31], v[36:39], v[190:193]\n"
"s_waitcnt lgkmcnt(2)\n"
"v_mfma_f32_16x16x32_bf16 v[212:215], v[32:35], v[36:39], v[212:215]\n"
"s_waitcnt lgkmcnt(1)\n"
"ds_read_b128 v[48:51], v5 offset:43008\n"
"v_mfma_f32_16x16x32_bf16 v[202:205], v[20:23], v[40:43], v[202:205]\n"
"v_mfma_f32_16x16x32_bf16 v[220:223], v[28:31], v[40:43], v[220:223]\n"
"v_mfma_f32_16x16x32_bf16 v[236:239], v[32:35], v[40:43], v[236:239]\n"
"s_waitcnt lgkmcnt(1)\n"
"ds_read_b128 v[252:255], v5 offset:49152\n"
"v_mfma_f32_16x16x32_bf16 v[226:229], v[20:23], v[44:47], v[226:229]\n"
"v_mfma_f32_16x16x32_bf16 v[244:247], v[28:31], v[44:47], v[244:247]\n"
"v_mfma_f32_16x16x32_bf16 v[248:251], v[32:35], v[44:47], v[248:251]\n"
"s_waitcnt lgkmcnt(1)\n"
"ds_read_b128 v[52:55], v5 offset:55296\n"
"v_mfma_f32_16x16x32_bf16 v[232:235], v[20:23], v[48:51], v[232:235]\n"
"v_mfma_f32_16x16x32_bf16 v[182:185], v[28:31], v[48:51], v[182:185]\n"
"v_mfma_f32_16x16x32_bf16 v[84:87], v[32:35], v[48:51], v[84:87]\n"
"s_waitcnt vmcnt(0)\n"
"ds_read_b64_tr_b16 v[36:37], v27 offset:0\n"
"s_waitcnt lgkmcnt(2)\n"
"v_mfma_f32_16x16x32_bf16 v[208:211], v[20:23], v[252:255], v[208:211]\n"
"ds_read_b64_tr_b16 v[38:39], v27 offset:768\n"
"v_mfma_f32_16x16x32_bf16 v[186:189], v[28:31], v[252:255], v[186:189]\n"
"ds_read_b128 v[56:59], v4 offset:26624\n"
"v_mfma_f32_16x16x32_bf16 v[216:219], v[32:35], v[252:255], v[216:219]\n"
"s_waitcnt lgkmcnt(3)\n"
"ds_read_b64_tr_b16 v[40:41], v27 offset:32\n"
"v_mfma_f32_16x16x32_bf16 v[178:181], v[20:23], v[52:55], v[178:181]\n"
"ds_read_b64_tr_b16 v[42:43], v27 offset:800\n"
"v_mfma_f32_16x16x32_bf16 v[88:91], v[28:31], v[52:55], v[88:91]\n"
"ds_read_b64_tr_b16 v[44:45], v27 offset:64\n"
"ds_read_b64_tr_b16 v[46:47], v27 offset:832\n"
"v_mfma_f32_16x16x32_bf16 v[240:243], v[32:35], v[52:55], v[240:243]\n"
"s_waitcnt lgkmcnt(4)\n"
"ds_read_b128 v[252:255], v4 offset:32768\n"
"ds_read_b128 v[60:63], v4 offset:38912\n"
"v_mfma_f32_16x16x32_bf16 v[196:199], v[36:39], v[56:59], v[196:199]\n"
"s_waitcnt lgkmcnt(4)\n"
"v_mfma_f32_16x16x32_bf16 v[190:193], v[40:43], v[56:59], v[190:193]\n"
"s_waitcnt lgkmcnt(2)\n"
"v_mfma_f32_16x16x32_bf16 v[212:215], v[44:47], v[56:59], v[212:215]\n"
"s_waitcnt lgkmcnt(1)\n"
"ds_read_b128 v[64:67], v4 offset:45056\n"
"v_mfma_f32_16x16x32_bf16 v[202:205], v[36:39], v[252:255], v[202:205]\n"
"v_mfma_f32_16x16x32_bf16 v[220:223], v[40:43], v[252:255], v[220:223]\n"
"v_mfma_f32_16x16x32_bf16 v[236:239], v[44:47], v[252:255], v[236:239]\n"
"s_waitcnt lgkmcnt(1)\n"
"ds_read_b128 v[68:71], v4 offset:51200\n"
"v_mfma_f32_16x16x32_bf16 v[226:229], v[36:39], v[60:63], v[226:229]\n"
"v_mfma_f32_16x16x32_bf16 v[244:247], v[40:43], v[60:63], v[244:247]\n"
"v_mfma_f32_16x16x32_bf16 v[248:251], v[44:47], v[60:63], v[248:251]\n"
"s_waitcnt lgkmcnt(1)\n"
"ds_read_b128 v[72:75], v4 offset:57344\n"
"v_mfma_f32_16x16x32_bf16 v[232:235], v[36:39], v[64:67], v[232:235]\n"
"v_mfma_f32_16x16x32_bf16 v[182:185], v[40:43], v[64:67], v[182:185]\n"
"v_mfma_f32_16x16x32_bf16 v[84:87], v[44:47], v[64:67], v[84:87]\n"
"s_barrier\n"
"ds_read_b64_tr_b16 v[48:49], v27 offset:3072\n"
"s_waitcnt lgkmcnt(2)\n"
"v_mfma_f32_16x16x32_bf16 v[208:211], v[36:39], v[68:71], v[208:211]\n"
"ds_read_b64_tr_b16 v[50:51], v27 offset:3840\n"
"v_mfma_f32_16x16x32_bf16 v[186:189], v[40:43], v[68:71], v[186:189]\n"
"ds_read_b128 v[252:255], v5 offset:26624\n"
"v_mfma_f32_16x16x32_bf16 v[216:219], v[44:47], v[68:71], v[216:219]\n"
"s_waitcnt lgkmcnt(3)\n"
"ds_read_b64_tr_b16 v[52:53], v27 offset:3104\n"
"v_mfma_f32_16x16x32_bf16 v[178:181], v[36:39], v[72:75], v[178:181]\n"
"ds_read_b64_tr_b16 v[54:55], v27 offset:3872\n"
"v_mfma_f32_16x16x32_bf16 v[88:91], v[40:43], v[72:75], v[88:91]\n"
"ds_read_b64_tr_b16 v[56:57], v27 offset:3136\n"
"ds_read_b64_tr_b16 v[58:59], v27 offset:3904\n"
"v_mfma_f32_16x16x32_bf16 v[240:243], v[44:47], v[72:75], v[240:243]\n"
"s_waitcnt lgkmcnt(4)\n"
"ds_read_b128 v[60:63], v5 offset:32768\n"
"ds_read_b128 v[64:67], v5 offset:38912\n"
"v_mfma_f32_16x16x32_bf16 v[196:199], v[48:51], v[252:255], v[196:199]\n"
"s_waitcnt lgkmcnt(4)\n"
"v_mfma_f32_16x16x32_bf16 v[190:193], v[52:55], v[252:255], v[190:193]\n"
"s_waitcnt lgkmcnt(2)\n"
"v_mfma_f32_16x16x32_bf16 v[212:215], v[56:59], v[252:255], v[212:215]\n"
"s_waitcnt lgkmcnt(1)\n"
"ds_read_b128 v[68:71], v5 offset:45056\n"
"v_mfma_f32_16x16x32_bf16 v[202:205], v[48:51], v[60:63], v[202:205]\n"
"v_mfma_f32_16x16x32_bf16 v[220:223], v[52:55], v[60:63], v[220:223]\n"
"v_mfma_f32_16x16x32_bf16 v[236:239], v[56:59], v[60:63], v[236:239]\n"
"s_waitcnt lgkmcnt(1)\n"
"ds_read_b128 v[72:75], v5 offset:51200\n"
"v_mfma_f32_16x16x32_bf16 v[226:229], v[48:51], v[64:67], v[226:229]\n"
"v_mfma_f32_16x16x32_bf16 v[244:247], v[52:55], v[64:67], v[244:247]\n"
"v_mfma_f32_16x16x32_bf16 v[248:251], v[56:59], v[64:67], v[248:251]\n"
"s_waitcnt lgkmcnt(1)\n"
"ds_read_b128 v[252:255], v5 offset:57344\n"
"v_mfma_f32_16x16x32_bf16 v[232:235], v[48:51], v[68:71], v[232:235]\n"
"v_mfma_f32_16x16x32_bf16 v[182:185], v[52:55], v[68:71], v[182:185]\n"
"v_mfma_f32_16x16x32_bf16 v[84:87], v[56:59], v[68:71], v[84:87]\n"
"s_barrier\n"
"ds_read_b64_tr_b16 v[60:61], v27 offset:24576\n"
"s_waitcnt lgkmcnt(2)\n"
"v_mfma_f32_16x16x32_bf16 v[208:211], v[48:51], v[72:75], v[208:211]\n"
"ds_read_b64_tr_b16 v[62:63], v27 offset:25344\n"
"v_mfma_f32_16x16x32_bf16 v[186:189], v[52:55], v[72:75], v[186:189]\n"
"ds_read_b128 v[92:95], v4 offset:28672\n"
"v_mfma_f32_16x16x32_bf16 v[216:219], v[56:59], v[72:75], v[216:219]\n"
"s_waitcnt lgkmcnt(3)\n"
"ds_read_b64_tr_b16 v[64:65], v27 offset:24608\n"
"v_mfma_f32_16x16x32_bf16 v[178:181], v[48:51], v[252:255], v[178:181]\n"
"ds_read_b64_tr_b16 v[66:67], v27 offset:25376\n"
"v_mfma_f32_16x16x32_bf16 v[88:91], v[52:55], v[252:255], v[88:91]\n"
"ds_read_b64_tr_b16 v[68:69], v27 offset:24640\n"
"ds_read_b64_tr_b16 v[70:71], v27 offset:25408\n"
"v_mfma_f32_16x16x32_bf16 v[240:243], v[56:59], v[252:255], v[240:243]\n"
"s_waitcnt lgkmcnt(4)\n"
"ds_read_b128 v[96:99], v4 offset:34816\n"
"ds_read_b128 v[100:103], v4 offset:40960\n"
"v_mfma_f32_16x16x32_bf16 v[196:199], v[60:63], v[92:95], v[196:199]\n"
"s_waitcnt lgkmcnt(4)\n"
"ds_read_b64_tr_b16 v[72:73], v27 offset:27648\n"
"ds_read_b64_tr_b16 v[74:75], v27 offset:28416\n"
"ds_read_b64_tr_b16 v[76:77], v27 offset:27680\n"
"v_mfma_f32_16x16x32_bf16 v[190:193], v[64:67], v[92:95], v[190:193]\n"
"s_waitcnt lgkmcnt(5)\n"
"ds_read_b64_tr_b16 v[78:79], v27 offset:28448\n"
"ds_read_b64_tr_b16 v[80:81], v27 offset:27712\n"
"v_mfma_f32_16x16x32_bf16 v[212:215], v[68:71], v[92:95], v[212:215]\n"
"s_waitcnt lgkmcnt(6)\n"
"ds_read_b64_tr_b16 v[82:83], v27 offset:28480\n"
"v_mfma_f32_16x16x32_bf16 v[202:205], v[60:63], v[96:99], v[202:205]\n"
"ds_read_b128 v[252:255], v4 offset:47104\n"
"v_mfma_f32_16x16x32_bf16 v[220:223], v[64:67], v[96:99], v[220:223]\n"
"v_mfma_f32_16x16x32_bf16 v[236:239], v[68:71], v[96:99], v[236:239]\n"
"s_waitcnt lgkmcnt(7)\n"
"ds_read_b128 v[104:107], v5 offset:28672\n"
"v_mfma_f32_16x16x32_bf16 v[226:229], v[60:63], v[100:103], v[226:229]\n"
"ds_read_b128 v[92:95], v5 offset:34816\n"
"v_mfma_f32_16x16x32_bf16 v[244:247], v[64:67], v[100:103], v[244:247]\n"
"s_waitcnt lgkmcnt(3)\n"
"ds_read_b128 v[108:111], v5 offset:40960\n"
"s_barrier\n"
"ds_read_b128 v[96:99], v5 offset:47104\n"
"v_mfma_f32_16x16x32_bf16 v[248:251], v[68:71], v[100:103], v[248:251]\n"
"s_waitcnt lgkmcnt(4)\n"
"ds_read_b128 v[112:115], v4 offset:53248\n"
"ds_read_b128 v[116:119], v5 offset:53248\n"
"v_mfma_f32_16x16x32_bf16 v[232:235], v[60:63], v[252:255], v[232:235]\n"
"ds_read_b128 v[120:123], v4 offset:59392\n"
"ds_read_b128 v[124:127], v5 offset:59392\n"
"v_mfma_f32_16x16x32_bf16 v[182:185], v[64:67], v[252:255], v[182:185]\n"
"v_mfma_f32_16x16x32_bf16 v[84:87], v[68:71], v[252:255], v[84:87]\n"
"s_waitcnt lgkmcnt(3)\n"
"v_mfma_f32_16x16x32_bf16 v[208:211], v[60:63], v[112:115], v[208:211]\n"
"v_mfma_f32_16x16x32_bf16 v[186:189], v[64:67], v[112:115], v[186:189]\n"
"v_mfma_f32_16x16x32_bf16 v[216:219], v[68:71], v[112:115], v[216:219]\n"
"s_waitcnt lgkmcnt(1)\n"
"v_mfma_f32_16x16x32_bf16 v[178:181], v[60:63], v[120:123], v[178:181]\n"
"v_mfma_f32_16x16x32_bf16 v[88:91], v[64:67], v[120:123], v[88:91]\n"
"v_mfma_f32_16x16x32_bf16 v[240:243], v[68:71], v[120:123], v[240:243]\n"
"v_mfma_f32_16x16x32_bf16 v[196:199], v[72:75], v[104:107], v[196:199]\n"
"v_mfma_f32_16x16x32_bf16 v[190:193], v[76:79], v[104:107], v[190:193]\n"
"v_mfma_f32_16x16x32_bf16 v[212:215], v[80:83], v[104:107], v[212:215]\n"
"v_mfma_f32_16x16x32_bf16 v[202:205], v[72:75], v[92:95], v[202:205]\n"
"v_mfma_f32_16x16x32_bf16 v[220:223], v[76:79], v[92:95], v[220:223]\n"
"v_mfma_f32_16x16x32_bf16 v[236:239], v[80:83], v[92:95], v[236:239]\n"
"v_mfma_f32_16x16x32_bf16 v[226:229], v[72:75], v[108:111], v[226:229]\n"
"v_mfma_f32_16x16x32_bf16 v[244:247], v[76:79], v[108:111], v[244:247]\n"
"v_mfma_f32_16x16x32_bf16 v[248:251], v[80:83], v[108:111], v[248:251]\n"
"v_cvt_pk_bf16_f32 v196, v196, v197\n"
"v_cvt_pk_bf16_f32 v197, v198, v199\n"
"v_cvt_pk_bf16_f32 v198, v190, v191\n"
"v_cvt_pk_bf16_f32 v199, v192, v193\n"
"v_cvt_pk_bf16_f32 v200, v212, v213\n"
"v_cvt_pk_bf16_f32 v201, v214, v215\n"
"v_cvt_pk_bf16_f32 v202, v202, v203\n"
"v_cvt_pk_bf16_f32 v203, v204, v205\n"
"v_cvt_pk_bf16_f32 v204, v220, v221\n"
"v_cvt_pk_bf16_f32 v205, v222, v223\n"
"v_cvt_pk_bf16_f32 v206, v236, v237\n"
"v_cvt_pk_bf16_f32 v207, v238, v239\n"
"v_cvt_pk_bf16_f32 v226, v226, v227\n"
"v_cvt_pk_bf16_f32 v227, v228, v229\n"
"v_mfma_f32_16x16x32_bf16 v[232:235], v[72:75], v[96:99], v[232:235]\n"
"v_cvt_pk_bf16_f32 v228, v244, v245\n"
"v_cvt_pk_bf16_f32 v229, v246, v247\n"
"v_mfma_f32_16x16x32_bf16 v[182:185], v[76:79], v[96:99], v[182:185]\n"
"v_cvt_pk_bf16_f32 v230, v248, v249\n"
"v_cvt_pk_bf16_f32 v231, v250, v251\n"
"v_mfma_f32_16x16x32_bf16 v[84:87], v[80:83], v[96:99], v[84:87]\n"
"v_mfma_f32_16x16x32_bf16 v[208:211], v[72:75], v[116:119], v[208:211]\n"
"v_mfma_f32_16x16x32_bf16 v[186:189], v[76:79], v[116:119], v[186:189]\n"
"v_cvt_pk_bf16_f32 v232, v232, v233\n"
"v_cvt_pk_bf16_f32 v233, v234, v235\n"
"v_mfma_f32_16x16x32_bf16 v[216:219], v[80:83], v[116:119], v[216:219]\n"
"s_waitcnt lgkmcnt(0)\n"
"v_cvt_pk_bf16_f32 v234, v182, v183\n"
"v_cvt_pk_bf16_f32 v235, v184, v185\n"
"v_mfma_f32_16x16x32_bf16 v[178:181], v[72:75], v[124:127], v[178:181]\n"
"v_cvt_pk_bf16_f32 v236, v84, v85\n"
"v_cvt_pk_bf16_f32 v237, v86, v87\n"
"v_cvt_pk_bf16_f32 v208, v208, v209\n"
"v_cvt_pk_bf16_f32 v209, v210, v211\n"
"v_cvt_pk_bf16_f32 v210, v186, v187\n"
"v_cvt_pk_bf16_f32 v211, v188, v189\n"
"v_mfma_f32_16x16x32_bf16 v[88:91], v[76:79], v[124:127], v[88:91]\n"
"v_cvt_pk_bf16_f32 v212, v216, v217\n"
"v_cvt_pk_bf16_f32 v213, v218, v219\n"
"v_mfma_f32_16x16x32_bf16 v[240:243], v[80:83], v[124:127], v[240:243]\n"
"s_barrier\n"
"s_add_u32 s42, s46, 0\n"
"s_addc_u32 s43, s47, 0\n"
"s_add_u32 m0, s50, 24576\n"
"s_nop 0\n"
"global_load_lds_dwordx4 v0, s[42:43]\n"
"s_add_u32 m0, s50, 25600\n"
"s_nop 0\n"
"global_load_lds_dwordx4 v1, s[42:43]\n"
"s_add_u32 m0, s50, 26624\n"
"s_nop 0\n"
"global_load_lds_dwordx4 v2, s[42:43]\n"
"s_barrier\n"
"s_add_u32 s42, s46, 36864\n"
"s_addc_u32 s43, s47, 0\n"
"s_add_u32 m0, s50, 27648\n"
"s_nop 0\n"
"global_load_lds_dwordx4 v0, s[42:43]\n"
"s_add_u32 m0, s50, 28672\n"
"s_nop 0\n"
"global_load_lds_dwordx4 v1, s[42:43]\n"
"s_add_u32 m0, s50, 29696\n"
"s_nop 0\n"
"global_load_lds_dwordx4 v2, s[42:43]\n"
"s_add_u32 s42, s46, 73728\n"
"s_addc_u32 s43, s47, 0\n"
"s_add_u32 m0, s50, 4294942720\n"
"s_nop 0\n"
"global_load_lds_dwordx4 v0, s[42:43]\n"
"s_add_u32 m0, s50, 4294943744\n"
"s_nop 0\n"
"global_load_lds_dwordx4 v1, s[42:43]\n"
"s_add_u32 m0, s50, 4294944768\n"
"s_nop 0\n"
"global_load_lds_dwordx4 v2, s[42:43]\n"
"s_add_u32 s42, s46, 110592\n"
"s_addc_u32 s43, s47, 0\n"
"s_add_u32 m0, s50, 4294945792\n"
"s_nop 0\n"
"global_load_lds_dwordx4 v0, s[42:43]\n"
"s_add_u32 m0, s50, 4294946816\n"
"s_nop 0\n"
"global_load_lds_dwordx4 v1, s[42:43]\n"
"s_add_u32 m0, s50, 4294947840\n"
"s_nop 0\n"
"global_load_lds_dwordx4 v2, s[42:43]\n"
"s_add_u32 s42, s46, 147456\n"
"s_addc_u32 s43, s47, 0\n"
"s_add_u32 m0, s50, 49152\n"
"s_nop 0\n"
"global_load_lds_dwordx4 v0, s[42:43]\n"
"s_add_u32 m0, s50, 50176\n"
"s_nop 0\n"
"global_load_lds_dwordx4 v1, s[42:43]\n"
"s_add_u32 m0, s50, 51200\n"
"s_nop 0\n"
"global_load_lds_dwordx4 v2, s[42:43]\n"
"s_add_u32 s42, s46, 184320\n"
"s_addc_u32 s43, s47, 0\n"
"s_add_u32 m0, s50, 52224\n"
"s_nop 0\n"
"global_load_lds_dwordx4 v0, s[42:43]\n"
"s_add_u32 m0, s50, 53248\n"
"s_nop 0\n"
"global_load_lds_dwordx4 v1, s[42:43]\n"
"s_add_u32 m0, s50, 54272\n"
"s_nop 0\n"
"global_load_lds_dwordx4 v2, s[42:43]\n"
"s_barrier\n"
"ds_read_b128 v[184:187], v4 offset:6144\n"
"s_waitcnt vmcnt(15)\n"
"ds_read_b128 v[188:191], v5 offset:6144\n"
"s_waitcnt vmcnt(12)\n"
"v_cvt_pk_bf16_f32 v178, v178, v179\n"
"v_cvt_pk_bf16_f32 v179, v180, v181\n"
"v_cvt_pk_bf16_f32 v180, v88, v89\n"
"v_cvt_pk_bf16_f32 v181, v90, v91\n"
"v_cvt_pk_bf16_f32 v182, v240, v241\n"
"v_cvt_pk_bf16_f32 v183, v242, v243\n"
"ds_read_b64_tr_b16 v[84:85], v3 offset:53248\n"
"s_sub_u32 m0, s50, 28672\n"
"ds_read_b64_tr_b16 v[86:87], v3 offset:54016\n"
"ds_read_b64_tr_b16 v[88:89], v3 offset:53280\n"
"ds_read_b64_tr_b16 v[90:91], v3 offset:54048\n"
"s_waitcnt lgkmcnt(5)\n"
"v_mfma_f32_16x16x32_bf16 v[238:241], v[8:11], v[184:187], 0\n"
"ds_read_b64_tr_b16 v[92:93], v3 offset:53312\n"
"v_mfma_f32_16x16x32_bf16 v[192:195], v[12:15], v[184:187], 0\n"
"ds_read_b128 v[214:217], v4 offset:8192\n"
"v_mfma_f32_16x16x32_bf16 v[218:221], v[16:19], v[184:187], 0\n"
"s_waitcnt lgkmcnt(6)\n"
"ds_read_b64_tr_b16 v[94:95], v3 offset:54080\n"
"v_mfma_f32_16x16x32_bf16 v[238:241], v[20:23], v[188:191], v[238:241]\n"
"s_waitcnt vmcnt(9)\n"
"ds_read_b64_tr_b16 v[96:97], v3 offset:56320\n"
"v_mfma_f32_16x16x32_bf16 v[192:195], v[28:31], v[188:191], v[192:195]\n"
"ds_read_b64_tr_b16 v[98:99], v3 offset:57088\n"
"ds_read_b64_tr_b16 v[100:101], v3 offset:56352\n"
"v_mfma_f32_16x16x32_bf16 v[218:221], v[32:35], v[188:191], v[218:221]\n"
"s_waitcnt lgkmcnt(4)\n"
"ds_read_b64_tr_b16 v[102:103], v3 offset:57120\n"
"ds_read_b64_tr_b16 v[222:223], v3 offset:56384\n"
"v_mfma_f32_16x16x32_bf16 v[238:241], v[36:39], v[214:217], v[238:241]\n"
"ds_read_b64_tr_b16 v[224:225], v3 offset:57152\n"
"ds_read_b128 v[184:187], v5 offset:8192\n"
"v_mfma_f32_16x16x32_bf16 v[192:195], v[40:43], v[214:217], v[192:195]\n"
"s_waitcnt vmcnt(6)\n"
"ds_read_b64_tr_b16 v[104:105], v3 offset:4096\n"
"v_mfma_f32_16x16x32_bf16 v[218:221], v[44:47], v[214:217], v[218:221]\n"
"ds_read_b64_tr_b16 v[106:107], v3 offset:4864\n"
"ds_read_b64_tr_b16 v[108:109], v3 offset:4128\n"
"ds_read_b64_tr_b16 v[110:111], v3 offset:4896\n"
"ds_read_b64_tr_b16 v[244:245], v3 offset:4160\n"
"ds_read_b64_tr_b16 v[246:247], v3 offset:4928\n"
"ds_read_b128 v[188:191], v4 offset:10240\n"
"ds_read_b64_tr_b16 v[112:113], v3 offset:7168\n"
"s_waitcnt lgkmcnt(8)\n"
"v_mfma_f32_16x16x32_bf16 v[238:241], v[48:51], v[184:187], v[238:241]\n"
"ds_read_b64_tr_b16 v[114:115], v3 offset:7936\n"
"v_mfma_f32_16x16x32_bf16 v[192:195], v[52:55], v[184:187], v[192:195]\n"
"ds_read_b128 v[214:217], v5 offset:10240\n"
"v_mfma_f32_16x16x32_bf16 v[218:221], v[56:59], v[184:187], v[218:221]\n"
"s_waitcnt vmcnt(3)\n"
"ds_read_b64_tr_b16 v[116:117], v3 offset:7200\n"
"ds_read_b64_tr_b16 v[118:119], v3 offset:7968\n"
"ds_read_b64_tr_b16 v[248:249], v3 offset:7232\n"
"ds_read_b64_tr_b16 v[250:251], v3 offset:8000\n"
"s_waitcnt lgkmcnt(7)\n"
"v_mfma_f32_16x16x32_bf16 v[238:241], v[60:63], v[188:191], v[238:241]\n"
"ds_read_b64_tr_b16 v[0:1], v27 offset:0\n"
"v_mfma_f32_16x16x32_bf16 v[192:195], v[64:67], v[188:191], v[192:195]\n"
"ds_read_b128 v[184:187], v4 offset:0\n"
"v_mfma_f32_16x16x32_bf16 v[218:221], v[68:71], v[188:191], v[218:221]\n"
"s_waitcnt lgkmcnt(6)\n"
"ds_read_b64_tr_b16 v[2:3], v27 offset:768\n"
"v_mfma_f32_16x16x32_bf16 v[238:241], v[72:75], v[214:217], v[238:241]\n"
"s_waitcnt vmcnt(0)\n"
"ds_read_b64_tr_b16 v[120:121], v27 offset:32\n"
"v_mfma_f32_16x16x32_bf16 v[192:195], v[76:79], v[214:217], v[192:195]\n"
"ds_read_b64_tr_b16 v[122:123], v27 offset:800\n"
"ds_read_b64_tr_b16 v[252:253], v27 offset:64\n"
"v_mfma_f32_16x16x32_bf16 v[218:221], v[80:83], v[214:217], v[218:221]\n"
"ds_read_b64_tr_b16 v[254:255], v27 offset:832\n"
"ds_read_b128 v[188:191], v5 offset:0\n"
"v_cvt_pk_bf16_f32 v238, v238, v239\n"
"v_cvt_pk_bf16_f32 v239, v240, v241\n"
"s_waitcnt lgkmcnt(6)\n"
"v_mfma_f32_16x16x32_bf16 v[132:135], v[84:87], v[184:187], 0\n"
"ds_read_b64_tr_b16 v[124:125], v27 offset:3072\n"
"v_cvt_pk_bf16_f32 v240, v192, v193\n"
"v_cvt_pk_bf16_f32 v241, v194, v195\n"
"v_mfma_f32_16x16x32_bf16 v[214:217], v[88:91], v[184:187], 0\n"
"ds_read_b64_tr_b16 v[126:127], v27 offset:3840\n"
"v_cvt_pk_bf16_f32 v242, v218, v219\n"
"v_cvt_pk_bf16_f32 v243, v220, v221\n"
"v_mfma_f32_16x16x32_bf16 v[136:139], v[92:95], v[184:187], 0\n"
"s_waitcnt lgkmcnt(2)\n"
"ds_read_b64_tr_b16 v[128:129], v27 offset:3104\n"
"ds_read_b64_tr_b16 v[130:131], v27 offset:3872\n"
"ds_read_b64_tr_b16 v[140:141], v27 offset:3136\n"
"v_mfma_f32_16x16x32_bf16 v[132:135], v[96:99], v[188:191], v[132:135]\n"
"ds_read_b64_tr_b16 v[142:143], v27 offset:3904\n"
"ds_read_b128 v[192:195], v4 offset:2048\n"
"v_mfma_f32_16x16x32_bf16 v[214:217], v[100:103], v[188:191], v[214:217]\n"
"ds_read_b128 v[184:187], v5 offset:2048\n"
"v_mfma_f32_16x16x32_bf16 v[136:139], v[222:225], v[188:191], v[136:139]\n"
"s_waitcnt lgkmcnt(2)\n"
"s_barrier\n"
"s_waitcnt lgkmcnt(1)\n"
"v_mfma_f32_16x16x32_bf16 v[132:135], v[104:107], v[192:195], v[132:135]\n"
"v_mfma_f32_16x16x32_bf16 v[214:217], v[108:111], v[192:195], v[214:217]\n"
"ds_read_b128 v[218:221], v4 offset:4096\n"
"v_mfma_f32_16x16x32_bf16 v[136:139], v[244:247], v[192:195], v[136:139]\n"
"s_waitcnt lgkmcnt(1)\n"
"v_mfma_f32_16x16x32_bf16 v[132:135], v[112:115], v[184:187], v[132:135]\n"
"v_mfma_f32_16x16x32_bf16 v[214:217], v[116:119], v[184:187], v[214:217]\n"
"ds_read_b128 v[188:191], v5 offset:4096\n"
"v_mfma_f32_16x16x32_bf16 v[136:139], v[248:251], v[184:187], v[136:139]\n"
"s_waitcnt lgkmcnt(1)\n"
"v_mfma_f32_16x16x32_bf16 v[132:135], v[0:3], v[218:221], v[132:135]\n"
"v_mfma_f32_16x16x32_bf16 v[214:217], v[120:123], v[218:221], v[214:217]\n"
"ds_read_b128 v[192:195], v4 offset:12288\n"
"v_mfma_f32_16x16x32_bf16 v[136:139], v[252:255], v[218:221], v[136:139]\n"
"s_waitcnt lgkmcnt(1)\n"
"v_mfma_f32_16x16x32_bf16 v[132:135], v[124:127], v[188:191], v[132:135]\n"
"v_mfma_f32_16x16x32_bf16 v[214:217], v[128:131], v[188:191], v[214:217]\n"
"ds_read_b128 v[144:147], v5 offset:12288\n"
"v_mfma_f32_16x16x32_bf16 v[136:139], v[140:143], v[188:191], v[136:139]\n"
"s_waitcnt lgkmcnt(1)\n"
"v_mfma_f32_16x16x32_bf16 v[184:187], v[8:11], v[192:195], 0\n"
"v_mfma_f32_16x16x32_bf16 v[148:151], v[12:15], v[192:195], 0\n"
"ds_read_b128 v[218:221], v4 offset:14336\n"
"v_mfma_f32_16x16x32_bf16 v[152:155], v[16:19], v[192:195], 0\n"
"s_waitcnt lgkmcnt(1)\n"
"v_mfma_f32_16x16x32_bf16 v[184:187], v[20:23], v[144:147], v[184:187]\n"
"v_mul_f32 v132, s60, v132\n"
"v_mul_f32 v133, s60, v133\n"
"v_mfma_f32_16x16x32_bf16 v[148:151], v[28:31], v[144:147], v[148:151]\n"
"ds_read_b128 v[188:191], v5 offset:14336\n"
"v_cvt_pk_bf16_f32 v132, v132, v133\n"
"v_mfma_f32_16x16x32_bf16 v[152:155], v[32:35], v[144:147], v[152:155]\n"
"v_mul_f32 v134, s60, v134\n"
"v_mul_f32 v135, s60, v135\n"
"v_cvt_pk_bf16_f32 v133, v134, v135\n"
"v_mul_f32 v214, s60, v214\n"
"s_waitcnt lgkmcnt(1)\n"
"v_mfma_f32_16x16x32_bf16 v[184:187], v[36:39], v[218:221], v[184:187]\n"
"v_mul_f32 v215, s60, v215\n"
"v_mul_f32 v216, s60, v216\n"
"v_mfma_f32_16x16x32_bf16 v[148:151], v[40:43], v[218:221], v[148:151]\n"
"ds_read_b128 v[144:147], v4 offset:16384\n"
"v_mul_f32 v217, s60, v217\n"
"v_mfma_f32_16x16x32_bf16 v[152:155], v[44:47], v[218:221], v[152:155]\n"
"s_waitcnt lgkmcnt(1)\n"
"v_cvt_pk_bf16_f32 v134, v214, v215\n"
"v_cvt_pk_bf16_f32 v135, v216, v217\n"
"v_mfma_f32_16x16x32_bf16 v[184:187], v[48:51], v[188:191], v[184:187]\n"
"ds_write_b128 v6, v[222:225] offset:5120\n"
"v_mfma_f32_16x16x32_bf16 v[148:151], v[52:55], v[188:191], v[148:151]\n"
"ds_read_b128 v[156:159], v5 offset:16384\n"
"v_mul_f32 v136, s60, v136\n"
"v_mfma_f32_16x16x32_bf16 v[152:155], v[56:59], v[188:191], v[152:155]\n"
"s_waitcnt lgkmcnt(2)\n"
"v_mul_f32 v137, s60, v137\n"
"v_mul_f32 v138, s60, v138\n"
"v_mfma_f32_16x16x32_bf16 v[184:187], v[60:63], v[144:147], v[184:187]\n"
"v_mul_f32 v139, s60, v139\n"
"v_cvt_pk_bf16_f32 v26, v136, v137\n"
"v_mfma_f32_16x16x32_bf16 v[148:151], v[64:67], v[144:147], v[148:151]\n"
"ds_read_b128 v[214:217], v4 offset:18432\n"
"v_cvt_pk_bf16_f32 v27, v138, v139\n"
"v_mfma_f32_16x16x32_bf16 v[152:155], v[68:71], v[144:147], v[152:155]\n"
"s_waitcnt lgkmcnt(1)\n"
"v_mfma_f32_16x16x32_bf16 v[184:187], v[72:75], v[156:159], v[184:187]\n"
"v_mfma_f32_16x16x32_bf16 v[148:151], v[76:79], v[156:159], v[148:151]\n"
"ds_read_b128 v[218:221], v5 offset:18432\n"
"v_mfma_f32_16x16x32_bf16 v[152:155], v[80:83], v[156:159], v[152:155]\n"
"s_waitcnt lgkmcnt(1)\n"
"v_mfma_f32_16x16x32_bf16 v[190:193], v[8:11], v[214:217], 0\n"
"v_mfma_f32_16x16x32_bf16 v[136:139], v[12:15], v[214:217], 0\n"
"ds_read_b128 v[144:147], v4 offset:20480\n"
"v_mfma_f32_16x16x32_bf16 v[160:163], v[16:19], v[214:217], 0\n"
"s_waitcnt lgkmcnt(1)\n"
"v_mfma_f32_16x16x32_bf16 v[190:193], v[20:23], v[218:221], v[190:193]\n"
"ds_write_b128 v6, v[244:247] offset:6144\n"
"v_cvt_pk_bf16_f32 v184, v184, v185\n"
"v_cvt_pk_bf16_f32 v185, v186, v187\n"
"v_cvt_pk_bf16_f32 v186, v148, v149\n"
"v_cvt_pk_bf16_f32 v187, v150, v151\n"
"v_cvt_pk_bf16_f32 v188, v152, v153\n"
"v_cvt_pk_bf16_f32 v189, v154, v155\n"
"v_mfma_f32_16x16x32_bf16 v[136:139], v[28:31], v[218:221], v[136:139]\n"
"ds_read_b128 v[214:217], v5 offset:20480\n"
"v_bfe_u32 v195, v6, 2, 6\n"
"v_mfma_f32_16x16x32_bf16 v[160:163], v[32:35], v[218:221], v[160:163]\n"
"s_waitcnt lgkmcnt(2)\n"
"ds_read_b32 v195, v195 offset:1216\n"
"v_mfma_f32_16x16x32_bf16 v[190:193], v[36:39], v[144:147], v[190:193]\n"
"s_add_u32 s68, s50, 0\n"
"v_mfma_f32_16x16x32_bf16 v[136:139], v[40:43], v[144:147], v[136:139]\n"
"ds_read_b128 v[148:151], v4 offset:22528\n"
"v_mfma_f32_16x16x32_bf16 v[160:163], v[44:47], v[144:147], v[160:163]\n"
"s_waitcnt lgkmcnt(2)\n"
"v_mfma_f32_16x16x32_bf16 v[190:193], v[48:51], v[214:217], v[190:193]\n"
"v_mfma_f32_16x16x32_bf16 v[136:139], v[52:55], v[214:217], v[136:139]\n"
"s_add_u32 s69, s50, 1040\n"
"ds_read_b128 v[218:221], v5 offset:22528\n"
"s_waitcnt lgkmcnt(2)\n"
"v_add_u32_sdwa v177, v195, v7 dst_sel:DWORD dst_unused:UNUSED_PAD src0_sel:WORD_0 src1_sel:DWORD\n"
"v_mfma_f32_16x16x32_bf16 v[160:163], v[56:59], v[214:217], v[160:163]\n"
"s_waitcnt lgkmcnt(1)\n"
"v_max_u32 v176, s68, v177\n"
"v_mfma_f32_16x16x32_bf16 v[190:193], v[60:63], v[148:151], v[190:193]\n"
"v_mfma_f32_16x16x32_bf16 v[136:139], v[64:67], v[148:151], v[136:139]\n"
"ds_read_b128 v[144:147], v176 offset:16\n"
"v_min_u32 v175, s69, v177\n"
"v_mfma_f32_16x16x32_bf16 v[160:163], v[68:71], v[148:151], v[160:163]\n"
"s_waitcnt lgkmcnt(1)\n"
"v_mfma_f32_16x16x32_bf16 v[190:193], v[72:75], v[218:221], v[190:193]\n"
"ds_read_b128 v[214:217], v176 offset:32\n"
"v_mfma_f32_16x16x32_bf16 v[136:139], v[76:79], v[218:221], v[136:139]\n"
"s_add_u32 s70, s50, 4294966992\n"
"ds_write_b128 v6, v[248:251] offset:7168\n"
"v_mfma_f32_16x16x32_bf16 v[160:163], v[80:83], v[218:221], v[160:163]\n"
"s_waitcnt lgkmcnt(2)\n"
"ds_read_b128 v[148:151], v175 offset:1344\n"
"v_mfma_f32_16x16x32_bf16 v[144:147], v[202:205], v[132:135], v[144:147]\n"
"v_mfma_f32_16x16x32_bf16 v[144:147], v[204:207], v[24:27], v[144:147]\n"
"ds_read_b128 v[152:155], v175 offset:1360\n"
"v_cvt_pk_bf16_f32 v190, v190, v191\n"
"v_cvt_pk_bf16_f32 v191, v192, v193\n"
"v_cvt_pk_bf16_f32 v192, v136, v137\n"
"v_cvt_pk_bf16_f32 v193, v138, v139\n"
"v_cvt_pk_bf16_f32 v194, v160, v161\n"
"v_cvt_pk_bf16_f32 v195, v162, v163\n"
"s_waitcnt lgkmcnt(3)\n"
"v_mfma_f32_16x16x32_bf16 v[214:217], v[208:211], v[132:135], v[214:217]\n"
"v_max3_f32 v220, v144, v145, v146\n"
"v_max_f32 v220, v220, v147\n"
"v_mfma_f32_16x16x32_bf16 v[214:217], v[210:213], v[24:27], v[214:217]\n"
"s_waitcnt lgkmcnt(1)\n"
"ds_read_b128 v[136:139], v176 offset:0\n"
"v_min_u32 v174, s70, v177\n"
"v_mfma_f32_16x16x32_bf16 v[148:151], v[226:229], v[132:135], v[148:151]\n"
"v_mfma_f32_16x16x32_bf16 v[148:151], v[228:231], v[24:27], v[148:151]\n"
"s_waitcnt lgkmcnt(1)\n"
"ds_read_b128 v[156:159], v174 offset:2688\n"
"s_nop 0\n"
"v_max3_f32 v220, v220, v214, v215\n"
"v_max3_f32 v220, v220, v216, v217\n"
"v_mfma_f32_16x16x32_bf16 v[152:155], v[232:235], v[132:135], v[152:155]\n"
"v_mfma_f32_16x16x32_bf16 v[152:155], v[234:237], v[24:27], v[152:155]\n"
"s_waitcnt lgkmcnt(1)\n"
"ds_read_b128 v[144:147], v175 offset:1376\n"
"v_max3_f32 v220, v220, v148, v149\n"
"v_max3_f32 v220, v220, v150, v151\n"
"v_mfma_f32_16x16x32_bf16 v[136:139], v[196:199], v[132:135], v[136:139]\n"
"v_mfma_f32_16x16x32_bf16 v[136:139], v[198:201], v[24:27], v[136:139]\n"
"ds_read_b128 v[214:217], v174 offset:2704\n"
"ds_write_b128 v6, v[252:255] offset:8192\n"
"v_max3_f32 v220, v220, v152, v153\n"
"v_max3_f32 v220, v220, v154, v155\n"
"s_waitcnt lgkmcnt(3)\n"
"v_mfma_f32_16x16x32_bf16 v[156:159], v[178:181], v[132:135], v[156:159]\n"
"v_mfma_f32_16x16x32_bf16 v[156:159], v[180:183], v[24:27], v[156:159]\n"
"s_waitcnt lgkmcnt(2)\n"
"ds_read_b128 v[148:151], v174 offset:2720\n"
"v_max3_f32 v220, v220, v136, v137\n"
"v_max3_f32 v220, v220, v138, v139\n"
"v_mfma_f32_16x16x32_bf16 v[144:147], v[238:241], v[132:135], v[144:147]\n"
"v_mfma_f32_16x16x32_bf16 v[144:147], v[240:243], v[24:27], v[144:147]\n"
"s_waitcnt lgkmcnt(2)\n"
"ds_read_b128 v[152:155], v176 offset:16\n"
"v_max3_f32 v220, v220, v156, v157\n"
"v_max3_f32 v220, v220, v158, v159\n"
"v_mfma_f32_16x16x32_bf16 v[214:217], v[184:187], v[132:135], v[214:217]\n"
"v_mfma_f32_16x16x32_bf16 v[214:217], v[186:189], v[24:27], v[214:217]\n"
"s_waitcnt lgkmcnt(1)\n"
"ds_read_b128 v[136:139], v176 offset:32\n"
"v_max3_f32 v220, v220, v144, v145\n"
"v_max3_f32 v220, v220, v146, v147\n"
"v_mfma_f32_16x16x32_bf16 v[148:151], v[190:193], v[132:135], v[148:151]\n"
"v_mfma_f32_16x16x32_bf16 v[148:151], v[192:195], v[24:27], v[148:151]\n"
"s_waitcnt lgkmcnt(1)\n"
"ds_read_b128 v[156:159], v175 offset:1344\n"
"v_max3_f32 v220, v220, v214, v215\n"
"v_max3_f32 v220, v220, v216, v217\n"
"v_mfma_f32_16x16x32_bf16 v[152:155], v[202:205], v[132:135], v[152:155]\n"
"v_mfma_f32_16x16x32_bf16 v[152:155], v[204:207], v[24:27], v[152:155]\n"
"ds_read_b128 v[144:147], v175 offset:1360\n"
"s_nop 0\n"
"v_max3_f32 v220, v220, v148, v149\n"
"v_max3_f32 v220, v220, v150, v151\n"
"v_mov_b32 v221, v220\n"
"s_nop 1\n"
"v_permlane16_swap_b32 v220, v221\n"
"v_max_f32 v220, v220, v221\n"
"v_mov_b32 v221, v220\n"
"s_nop 1\n"
"v_permlane32_swap_b32 v220, v221\n"
"v_max_f32 v220, v220, v221\n"
"v_sub_f32 v152, v152, v220\n"
"v_exp_f32 v152, v152\n"
"v_sub_f32 v153, v153, v220\n"
"v_exp_f32 v153, v153\n"
"v_sub_f32 v154, v154, v220\n"
"v_exp_f32 v154, v154\n"
"v_sub_f32 v155, v155, v220\n"
"v_exp_f32 v155, v155\n"
"s_waitcnt lgkmcnt(2)\n"
"v_mfma_f32_16x16x32_bf16 v[136:139], v[208:211], v[132:135], v[136:139]\n"
"v_cvt_pk_bf16_f32 v152, v152, v153\n"
"v_cvt_pk_bf16_f32 v153, v154, v155\n"
"v_mfma_f32_16x16x32_bf16 v[136:139], v[210:213], v[24:27], v[136:139]\n"
"ds_write_addtid_b32 v152 offset:53248\n"
"ds_read_b128 v[214:217], v176 offset:0\n"
"ds_write_addtid_b32 v153 offset:53504\n"
"s_waitcnt lgkmcnt(4)\n"
"v_mfma_f32_16x16x32_bf16 v[156:159], v[226:229], v[132:135], v[156:159]\n"
"ds_write_b128 v6, v[140:143] offset:9216\n"
"v_mfma_f32_16x16x32_bf16 v[156:159], v[228:231], v[24:27], v[156:159]\n"
"ds_read_b128 v[148:151], v174 offset:2688\n"
"v_sub_f32 v136, v136, v220\n"
"v_exp_f32 v136, v136\n"
"v_sub_f32 v137, v137, v220\n"
"v_exp_f32 v137, v137\n"
"s_nop 0\n"
"v_cvt_pk_bf16_f32 v136, v136, v137\n"
"ds_write_addtid_b32 v136 offset:53760\n"
"s_waitcnt lgkmcnt(6)\n"
"v_mfma_f32_16x16x32_bf16 v[144:147], v[232:235], v[132:135], v[144:147]\n"
"v_sub_f32 v156, v156, v220\n"
"v_exp_f32 v156, v156\n"
"v_sub_f32 v157, v157, v220\n"
"v_exp_f32 v157, v157\n"
"v_sub_f32 v158, v158, v220\n"
"v_exp_f32 v158, v158\n"
"v_sub_f32 v159, v159, v220\n"
"v_exp_f32 v159, v159\n"
"v_cvt_pk_bf16_f32 v156, v156, v157\n"
"v_cvt_pk_bf16_f32 v157, v158, v159\n"
"v_mfma_f32_16x16x32_bf16 v[144:147], v[234:237], v[24:27], v[144:147]\n"
"ds_write_addtid_b32 v156 offset:54272\n"
"ds_write_addtid_b32 v157 offset:54528\n"
"s_waitcnt lgkmcnt(6)\n"
"v_mfma_f32_16x16x32_bf16 v[214:217], v[196:199], v[132:135], v[214:217]\n"
"ds_read_b128 v[136:139], v175 offset:1376\n"
"v_mfma_f32_16x16x32_bf16 v[214:217], v[198:201], v[24:27], v[214:217]\n"
"ds_read_b128 v[152:155], v174 offset:2704\n"
"s_nop 0\n"
"v_sub_f32 v144, v144, v220\n"
"v_exp_f32 v144, v144\n"
"v_sub_f32 v145, v145, v220\n"
"v_exp_f32 v145, v145\n"
"v_sub_f32 v146, v146, v220\n"
"v_exp_f32 v146, v146\n"
"v_sub_f32 v147, v147, v220\n"
"v_exp_f32 v147, v147\n"
"v_cvt_pk_bf16_f32 v144, v144, v145\n"
"v_sub_f32 v214, v214, v220\n"
"v_exp_f32 v214, v214\n"
"v_cvt_pk_bf16_f32 v145, v146, v147\n"
"v_sub_f32 v215, v215, v220\n"
"v_exp_f32 v215, v215\n"
"v_sub_f32 v216, v216, v220\n"
"v_exp_f32 v216, v216\n"
"v_sub_f32 v217, v217, v220\n"
"v_exp_f32 v217, v217\n"
"v_cvt_pk_bf16_f32 v214, v214, v215\n"
"v_cvt_pk_bf16_f32 v215, v216, v217\n"
"ds_write_addtid_b32 v144 offset:54784\n"
"ds_write_addtid_b32 v145 offset:55040\n"
"s_waitcnt lgkmcnt(7)\n"
"v_mfma_f32_16x16x32_bf16 v[148:151], v[178:181], v[132:135], v[148:151]\n"
"v_mfma_f32_16x16x32_bf16 v[148:151], v[180:183], v[24:27], v[148:151]\n"
"s_waitcnt lgkmcnt(0)\n"
"ds_read_b128 v[196:199], v174 offset:2720\n"
"s_barrier\n"
"ds_write_addtid_b32 v214 offset:58880\n"
"ds_write_addtid_b32 v215 offset:59136\n"
"v_mfma_f32_16x16x32_bf16 v[136:139], v[238:241], v[132:135], v[136:139]\n"
"v_mfma_f32_16x16x32_bf16 v[136:139], v[240:243], v[24:27], v[136:139]\n"
"ds_read_b128 v[216:219], v4 offset:24576\n"
"v_sub_f32 v148, v148, v220\n"
"v_exp_f32 v148, v148\n"
"v_sub_f32 v149, v149, v220\n"
"v_exp_f32 v149, v149\n"
"v_sub_f32 v150, v150, v220\n"
"v_exp_f32 v150, v150\n"
"v_sub_f32 v151, v151, v220\n"
"v_exp_f32 v151, v151\n"
"v_cvt_pk_bf16_f32 v148, v148, v149\n"
"v_cvt_pk_bf16_f32 v149, v150, v151\n"
"v_sub_f32 v136, v136, v220\n"
"v_exp_f32 v136, v136\n"
"v_sub_f32 v137, v137, v220\n"
"v_exp_f32 v137, v137\n"
"ds_write_addtid_b32 v148 offset:57344\n"
"v_cvt_pk_bf16_f32 v136, v136, v137\n"
"ds_write_addtid_b32 v149 offset:57600\n"
"v_mfma_f32_16x16x32_bf16 v[152:155], v[184:187], v[132:135], v[152:155]\n"
"ds_write_addtid_b32 v136 offset:33792\n"
"v_mfma_f32_16x16x32_bf16 v[152:155], v[186:189], v[24:27], v[152:155]\n"
"s_waitcnt lgkmcnt(6)\n"
"v_mfma_f32_16x16x32_bf16 v[196:199], v[190:193], v[132:135], v[196:199]\n"
"ds_read_b128 v[144:147], v5 offset:24576\n"
"v_mfma_f32_16x16x32_bf16 v[196:199], v[192:195], v[24:27], v[196:199]\n"
"s_waitcnt lgkmcnt(4)\n"
"v_mfma_f32_16x16x32_bf16 v[148:151], v[84:87], v[216:219], 0\n"
"v_mfma_f32_16x16x32_bf16 v[156:159], v[88:91], v[216:219], 0\n"
"ds_read_b128 v[136:139], v4 offset:26624\n"
"v_mfma_f32_16x16x32_bf16 v[160:163], v[92:95], v[216:219], 0\n"
"s_waitcnt lgkmcnt(1)\n"
"v_mfma_f32_16x16x32_bf16 v[148:151], v[96:99], v[144:147], v[148:151]\n"
"v_sub_f32 v152, v152, v220\n"
"v_exp_f32 v152, v152\n"
"v_sub_f32 v153, v153, v220\n"
"v_exp_f32 v153, v153\n"
"v_sub_f32 v154, v154, v220\n"
"v_exp_f32 v154, v154\n"
"v_sub_f32 v155, v155, v220\n"
"v_exp_f32 v155, v155\n"
"v_cvt_pk_bf16_f32 v152, v152, v153\n"
"v_cvt_pk_bf16_f32 v153, v154, v155\n"
"v_mfma_f32_16x16x32_bf16 v[156:159], v[100:103], v[144:147], v[156:159]\n"
"ds_write_addtid_b32 v152 offset:57856\n"
"ds_write_addtid_b32 v153 offset:58112\n"
"v_mfma_f32_16x16x32_bf16 v[160:163], v[222:225], v[144:147], v[160:163]\n"
"s_waitcnt lgkmcnt(2)\n"
"ds_read_b128 v[198:201], v5 offset:26624\n"
"v_mfma_f32_16x16x32_bf16 v[148:151], v[104:107], v[136:139], v[148:151]\n"
"v_sub_f32 v196, v196, v220\n"
"v_exp_f32 v196, v196\n"
"v_mfma_f32_16x16x32_bf16 v[156:159], v[108:111], v[136:139], v[156:159]\n"
"ds_read_b128 v[132:135], v4 offset:28672\n"
"v_sub_f32 v197, v197, v220\n"
"v_exp_f32 v197, v197\n"
"v_mfma_f32_16x16x32_bf16 v[160:163], v[244:247], v[136:139], v[160:163]\n"
"v_cvt_pk_bf16_f32 v196, v196, v197\n"
"ds_write_addtid_b32 v196 offset:58368\n"
"s_waitcnt lgkmcnt(2)\n"
"v_mfma_f32_16x16x32_bf16 v[148:151], v[112:115], v[198:201], v[148:151]\n"
"v_mfma_f32_16x16x32_bf16 v[156:159], v[116:119], v[198:201], v[156:159]\n"
"ds_read_b128 v[218:221], v5 offset:28672\n"
"v_mfma_f32_16x16x32_bf16 v[160:163], v[248:251], v[198:201], v[160:163]\n"
"s_waitcnt lgkmcnt(2)\n"
"v_mfma_f32_16x16x32_bf16 v[148:151], v[0:3], v[132:135], v[148:151]\n"
"s_waitcnt lgkmcnt(1)\n"
"s_barrier\n"
"v_mfma_f32_16x16x32_bf16 v[156:159], v[120:123], v[132:135], v[156:159]\n"
"ds_read_b128 v[222:225], v4 offset:36864\n"
"v_mfma_f32_16x16x32_bf16 v[160:163], v[252:255], v[132:135], v[160:163]\n"
"s_waitcnt lgkmcnt(1)\n"
"v_mfma_f32_16x16x32_bf16 v[148:151], v[124:127], v[218:221], v[148:151]\n"
"v_mfma_f32_16x16x32_bf16 v[156:159], v[128:131], v[218:221], v[156:159]\n"
"ds_read_b128 v[196:199], v5 offset:36864\n"
"v_mfma_f32_16x16x32_bf16 v[160:163], v[140:143], v[218:221], v[160:163]\n"
"s_waitcnt lgkmcnt(1)\n"
"v_mfma_f32_16x16x32_bf16 v[214:217], v[8:11], v[222:225], 0\n"
"v_mfma_f32_16x16x32_bf16 v[248:251], v[12:15], v[222:225], 0\n"
"ds_read_b128 v[244:247], v4 offset:38912\n"
"v_mfma_f32_16x16x32_bf16 v[252:255], v[16:19], v[222:225], 0\n"
"s_waitcnt lgkmcnt(1)\n"
"v_mfma_f32_16x16x32_bf16 v[214:217], v[20:23], v[196:199], v[214:217]\n"
"v_mul_f32 v148, s60, v148\n"
"v_mul_f32 v149, s60, v149\n"
"v_mfma_f32_16x16x32_bf16 v[248:251], v[28:31], v[196:199], v[248:251]\n"
"ds_read_b128 v[218:221], v5 offset:38912\n"
"v_cvt_pk_bf16_f32 v148, v148, v149\n"
"v_mfma_f32_16x16x32_bf16 v[252:255], v[32:35], v[196:199], v[252:255]\n"
"s_waitcnt lgkmcnt(1)\n"
"v_mul_f32 v150, s60, v150\n"
"v_mul_f32 v151, s60, v151\n"
"v_mfma_f32_16x16x32_bf16 v[214:217], v[36:39], v[244:247], v[214:217]\n"
"v_cvt_pk_bf16_f32 v149, v150, v151\n"
"v_mul_f32 v156, s60, v156\n"
"v_mfma_f32_16x16x32_bf16 v[248:251], v[40:43], v[244:247], v[248:251]\n"
"ds_read_b128 v[196:199], v4 offset:40960\n"
"v_mul_f32 v157, s60, v157\n"
"v_mfma_f32_16x16x32_bf16 v[252:255], v[44:47], v[244:247], v[252:255]\n"
"v_mul_f32 v158, s60, v158\n"
"v_mul_f32 v159, s60, v159\n"
"v_cvt_pk_bf16_f32 v150, v156, v157\n"
"v_cvt_pk_bf16_f32 v151, v158, v159\n"
"s_waitcnt lgkmcnt(1)\n"
"v_mfma_f32_16x16x32_bf16 v[214:217], v[48:51], v[218:221], v[214:217]\n"
"v_mul_f32 v160, s60, v160\n"
"v_mul_f32 v161, s60, v161\n"
"v_mfma_f32_16x16x32_bf16 v[248:251], v[52:55], v[218:221], v[248:251]\n"
"ds_read_b128 v[222:225], v5 offset:40960\n"
"v_mul_f32 v162, s60, v162\n"
"v_mfma_f32_16x16x32_bf16 v[252:255], v[56:59], v[218:221], v[252:255]\n"
"s_waitcnt lgkmcnt(1)\n"
"v_mul_f32 v163, s60, v163\n"
"v_cvt_pk_bf16_f32 v26, v160, v161\n"
"v_mfma_f32_16x16x32_bf16 v[214:217], v[60:63], v[196:199], v[214:217]\n"
"v_cvt_pk_bf16_f32 v27, v162, v163\n"
"v_mfma_f32_16x16x32_bf16 v[248:251], v[64:67], v[196:199], v[248:251]\n"
"ds_read_b128 v[132:135], v4 offset:43008\n"
"v_mfma_f32_16x16x32_bf16 v[252:255], v[68:71], v[196:199], v[252:255]\n"
"s_waitcnt lgkmcnt(1)\n"
"v_mfma_f32_16x16x32_bf16 v[214:217], v[72:75], v[222:225], v[214:217]\n"
"v_mfma_f32_16x16x32_bf16 v[248:251], v[76:79], v[222:225], v[248:251]\n"
"ds_read_b128 v[136:139], v5 offset:43008\n"
"v_mfma_f32_16x16x32_bf16 v[252:255], v[80:83], v[222:225], v[252:255]\n"
"s_waitcnt lgkmcnt(1)\n"
"v_mfma_f32_16x16x32_bf16 v[244:247], v[8:11], v[132:135], 0\n"
"v_mfma_f32_16x16x32_bf16 v[140:143], v[12:15], v[132:135], 0\n"
"ds_read_b128 v[196:199], v4 offset:45056\n"
"v_mfma_f32_16x16x32_bf16 v[144:147], v[16:19], v[132:135], 0\n"
"s_waitcnt lgkmcnt(1)\n"
"v_mfma_f32_16x16x32_bf16 v[244:247], v[20:23], v[136:139], v[244:247]\n"
"v_cvt_pk_bf16_f32 v214, v214, v215\n"
"v_cvt_pk_bf16_f32 v215, v216, v217\n"
"v_cvt_pk_bf16_f32 v216, v248, v249\n"
"v_cvt_pk_bf16_f32 v217, v250, v251\n"
"v_cvt_pk_bf16_f32 v218, v252, v253\n"
"v_cvt_pk_bf16_f32 v219, v254, v255\n"
"v_mfma_f32_16x16x32_bf16 v[140:143], v[28:31], v[136:139], v[140:143]\n"
"ds_read_b128 v[220:223], v5 offset:45056\n"
"v_bfe_u32 v225, v6, 2, 6\n"
"v_mfma_f32_16x16x32_bf16 v[144:147], v[32:35], v[136:139], v[144:147]\n"
"s_waitcnt lgkmcnt(1)\n"
"ds_read_b32 v225, v225 offset:1280\n"
"v_mfma_f32_16x16x32_bf16 v[244:247], v[36:39], v[196:199], v[244:247]\n"
"s_add_u32 s71, s50, 4294967280\n"
"v_mfma_f32_16x16x32_bf16 v[140:143], v[40:43], v[196:199], v[140:143]\n"
"ds_read_b128 v[248:251], v4 offset:47104\n"
"v_mfma_f32_16x16x32_bf16 v[144:147], v[44:47], v[196:199], v[144:147]\n"
"s_waitcnt lgkmcnt(2)\n"
"v_mfma_f32_16x16x32_bf16 v[244:247], v[48:51], v[220:223], v[244:247]\n"
"v_mfma_f32_16x16x32_bf16 v[140:143], v[52:55], v[220:223], v[140:143]\n"
"s_add_u32 s72, s50, 1024\n"
"ds_read_b128 v[252:255], v5 offset:47104\n"
"s_waitcnt lgkmcnt(2)\n"
"v_add_u32_sdwa v224, v225, v7 dst_sel:DWORD dst_unused:UNUSED_PAD src0_sel:WORD_0 src1_sel:DWORD\n"
"v_mfma_f32_16x16x32_bf16 v[144:147], v[56:59], v[220:223], v[144:147]\n"
"s_waitcnt lgkmcnt(1)\n"
"v_max_u32 v201, s71, v224\n"
"v_mfma_f32_16x16x32_bf16 v[244:247], v[60:63], v[248:251], v[244:247]\n"
"v_mfma_f32_16x16x32_bf16 v[140:143], v[64:67], v[248:251], v[140:143]\n"
"ds_read_b128 v[196:199], v201 offset:16\n"
"v_min_u32 v200, s72, v224\n"
"v_mfma_f32_16x16x32_bf16 v[144:147], v[68:71], v[248:251], v[144:147]\n"
"s_waitcnt lgkmcnt(1)\n"
"v_mfma_f32_16x16x32_bf16 v[244:247], v[72:75], v[252:255], v[244:247]\n"
"ds_read_b128 v[220:223], v201 offset:32\n"
"v_mfma_f32_16x16x32_bf16 v[140:143], v[76:79], v[252:255], v[140:143]\n"
"v_mfma_f32_16x16x32_bf16 v[144:147], v[80:83], v[252:255], v[144:147]\n"
"s_waitcnt lgkmcnt(1)\n"
"ds_read_b128 v[132:135], v200 offset:1376\n"
"v_mfma_f32_16x16x32_bf16 v[196:199], v[202:205], v[148:151], v[196:199]\n"
"v_mfma_f32_16x16x32_bf16 v[196:199], v[204:207], v[24:27], v[196:199]\n"
"s_waitcnt lgkmcnt(1)\n"
"ds_read_b128 v[136:139], v200 offset:1360\n"
"v_mfma_f32_16x16x32_bf16 v[220:223], v[208:211], v[148:151], v[220:223]\n"
"v_cvt_pk_bf16_f32 v244, v244, v245\n"
"v_cvt_pk_bf16_f32 v245, v246, v247\n"
"v_cvt_pk_bf16_f32 v246, v140, v141\n"
"v_cvt_pk_bf16_f32 v247, v142, v143\n"
"v_cvt_pk_bf16_f32 v248, v144, v145\n"
"v_cvt_pk_bf16_f32 v249, v146, v147\n"
"v_max3_f32 v250, v196, v197, v198\n"
"v_max_f32 v250, v250, v199\n"
"v_mfma_f32_16x16x32_bf16 v[220:223], v[210:213], v[24:27], v[220:223]\n"
"s_waitcnt lgkmcnt(1)\n"
"ds_read_b128 v[252:255], v201 offset:48\n"
"v_mfma_f32_16x16x32_bf16 v[132:135], v[238:241], v[148:151], v[132:135]\n"
"v_mfma_f32_16x16x32_bf16 v[132:135], v[240:243], v[24:27], v[132:135]\n"
"s_waitcnt lgkmcnt(1)\n"
"ds_read_b128 v[140:143], v200 offset:1392\n"
"s_nop 1\n"
"v_max3_f32 v250, v250, v220, v221\n"
"v_max3_f32 v250, v250, v222, v223\n"
"v_mfma_f32_16x16x32_bf16 v[136:139], v[232:235], v[148:151], v[136:139]\n"
"v_mfma_f32_16x16x32_bf16 v[136:139], v[234:237], v[24:27], v[136:139]\n"
"s_waitcnt lgkmcnt(1)\n"
"ds_read_b128 v[196:199], v201 offset:16\n"
"v_max3_f32 v250, v250, v132, v133\n"
"v_max3_f32 v250, v250, v134, v135\n"
"v_mfma_f32_16x16x32_bf16 v[252:255], v[214:217], v[148:151], v[252:255]\n"
"v_mfma_f32_16x16x32_bf16 v[252:255], v[216:219], v[24:27], v[252:255]\n"
"s_waitcnt lgkmcnt(1)\n"
"ds_read_b128 v[220:223], v201 offset:32\n"
"v_max3_f32 v250, v250, v136, v137\n"
"v_max3_f32 v250, v250, v138, v139\n"
"v_mfma_f32_16x16x32_bf16 v[140:143], v[244:247], v[148:151], v[140:143]\n"
"v_mfma_f32_16x16x32_bf16 v[140:143], v[246:249], v[24:27], v[140:143]\n"
"s_waitcnt lgkmcnt(1)\n"
"ds_read_b128 v[132:135], v200 offset:1376\n"
"v_max3_f32 v250, v250, v252, v253\n"
"v_max3_f32 v250, v250, v254, v255\n"
"v_mfma_f32_16x16x32_bf16 v[196:199], v[202:205], v[148:151], v[196:199]\n"
"v_mfma_f32_16x16x32_bf16 v[196:199], v[204:207], v[24:27], v[196:199]\n"
"ds_read_b128 v[136:139], v200 offset:1360\n"
"s_nop 0\n"
"v_max3_f32 v250, v250, v140, v141\n"
"v_max3_f32 v250, v250, v142, v143\n"
"v_mov_b32 v251, v250\n"
"s_nop 1\n"
"v_permlane16_swap_b32 v250, v251\n"
"v_max_f32 v250, v250, v251\n"
"v_mov_b32 v251, v250\n"
"s_nop 1\n"
"v_permlane32_swap_b32 v250, v251\n"
"v_max_f32 v250, v250, v251\n"
"v_sub_f32 v196, v196, v250\n"
"v_exp_f32 v196, v196\n"
"v_sub_f32 v197, v197, v250\n"
"v_exp_f32 v197, v197\n"
"v_sub_f32 v198, v198, v250\n"
"v_exp_f32 v198, v198\n"
"v_sub_f32 v199, v199, v250\n"
"v_exp_f32 v199, v199\n"
"s_waitcnt lgkmcnt(2)\n"
"v_mfma_f32_16x16x32_bf16 v[220:223], v[208:211], v[148:151], v[220:223]\n"
"v_cvt_pk_bf16_f32 v196, v196, v197\n"
"v_cvt_pk_bf16_f32 v197, v198, v199\n"
"v_mfma_f32_16x16x32_bf16 v[220:223], v[210:213], v[24:27], v[220:223]\n"
"ds_write_addtid_b32 v196 offset:55296\n"
"ds_read_b128 v[252:255], v201 offset:48\n"
"ds_write_addtid_b32 v197 offset:55552\n"
"s_waitcnt lgkmcnt(4)\n"
"v_mfma_f32_16x16x32_bf16 v[132:135], v[238:241], v[148:151], v[132:135]\n"
"v_mfma_f32_16x16x32_bf16 v[132:135], v[240:243], v[24:27], v[132:135]\n"
"ds_read_b128 v[140:143], v200 offset:1392\n"
"s_nop 0\n"
"v_sub_f32 v220, v220, v250\n"
"v_exp_f32 v220, v220\n"
"v_sub_f32 v221, v221, v250\n"
"v_exp_f32 v221, v221\n"
"v_sub_f32 v222, v222, v250\n"
"v_exp_f32 v222, v222\n"
"v_sub_f32 v223, v223, v250\n"
"v_exp_f32 v223, v223\n"
"v_sub_f32 v132, v132, v250\n"
"v_exp_f32 v132, v132\n"
"v_sub_f32 v133, v133, v250\n"
"v_exp_f32 v133, v133\n"
"v_sub_f32 v134, v134, v250\n"
"v_exp_f32 v134, v134\n"
"v_sub_f32 v135, v135, v250\n"
"v_exp_f32 v135, v135\n"
"v_cvt_pk_bf16_f32 v220, v220, v221\n"
"v_cvt_pk_bf16_f32 v132, v132, v133\n"
"v_cvt_pk_bf16_f32 v221, v222, v223\n"
"v_cvt_pk_bf16_f32 v133, v134, v135\n"
"ds_write_addtid_b32 v220 offset:55808\n"
"ds_write_addtid_b32 v132 offset:56320\n"
"ds_write_addtid_b32 v221 offset:56064\n"
"s_waitcnt lgkmcnt(7)\n"
"v_mfma_f32_16x16x32_bf16 v[136:139], v[232:235], v[148:151], v[136:139]\n"
"ds_write_addtid_b32 v133 offset:56576\n"
"v_mfma_f32_16x16x32_bf16 v[136:139], v[234:237], v[24:27], v[136:139]\n"
"s_waitcnt lgkmcnt(6)\n"
"ds_read_b128 v[196:199], v4 offset:30720\n"
"v_mfma_f32_16x16x32_bf16 v[252:255], v[214:217], v[148:151], v[252:255]\n"
"v_mfma_f32_16x16x32_bf16 v[252:255], v[216:219], v[24:27], v[252:255]\n"
"s_waitcnt lgkmcnt(1)\n"
"s_barrier\n"
"s_nop 1\n"
"v_sub_f32 v136, v136, v250\n"
"v_exp_f32 v136, v136\n"
"v_sub_f32 v137, v137, v250\n"
"v_exp_f32 v137, v137\n"
"v_sub_f32 v138, v138, v250\n"
"v_exp_f32 v138, v138\n"
"v_sub_f32 v139, v139, v250\n"
"v_exp_f32 v139, v139\n"
"v_cvt_pk_bf16_f32 v136, v136, v137\n"
"v_cvt_pk_bf16_f32 v137, v138, v139\n"
"v_sub_f32 v252, v252, v250\n"
"v_exp_f32 v252, v252\n"
"v_sub_f32 v253, v253, v250\n"
"v_exp_f32 v253, v253\n"
"v_sub_f32 v254, v254, v250\n"
"v_exp_f32 v254, v254\n"
"v_sub_f32 v255, v255, v250\n"
"v_exp_f32 v255, v255\n"
"v_cvt_pk_bf16_f32 v252, v252, v253\n"
"v_cvt_pk_bf16_f32 v253, v254, v255\n"
"ds_write_addtid_b32 v136 offset:57856\n"
"ds_write_addtid_b32 v137 offset:58112\n"
"v_mfma_f32_16x16x32_bf16 v[140:143], v[244:247], v[148:151], v[140:143]\n"
"ds_read_b128 v[220:223], v5 offset:30720\n"
"v_mfma_f32_16x16x32_bf16 v[140:143], v[246:249], v[24:27], v[140:143]\n"
"ds_write_addtid_b32 v252 offset:57344\n"
"s_waitcnt lgkmcnt(4)\n"
"v_mfma_f32_16x16x32_bf16 v[132:135], v[84:87], v[196:199], 0\n"
"ds_read_b128 v[144:147], v6 offset:5120\n"
"v_mfma_f32_16x16x32_bf16 v[136:139], v[88:91], v[196:199], 0\n"
"ds_read_b128 v[148:151], v4 offset:32768\n"
"v_mfma_f32_16x16x32_bf16 v[152:155], v[92:95], v[196:199], 0\n"
"ds_write_addtid_b32 v253 offset:57600\n"
"s_waitcnt lgkmcnt(4)\n"
"v_mfma_f32_16x16x32_bf16 v[132:135], v[96:99], v[220:223], v[132:135]\n"
"ds_read_b128 v[156:159], v6 offset:6144\n"
"v_mfma_f32_16x16x32_bf16 v[136:139], v[100:103], v[220:223], v[136:139]\n"
"s_waitcnt lgkmcnt(3)\n"
"ds_read_b128 v[160:163], v5 offset:32768\n"
"v_mfma_f32_16x16x32_bf16 v[152:155], v[144:147], v[220:223], v[152:155]\n"
"s_waitcnt lgkmcnt(3)\n"
"v_sub_f32 v140, v140, v250\n"
"v_exp_f32 v140, v140\n"
"v_mfma_f32_16x16x32_bf16 v[132:135], v[104:107], v[148:151], v[132:135]\n"
"ds_read_b128 v[196:199], v6 offset:7168\n"
"v_sub_f32 v141, v141, v250\n"
"v_exp_f32 v141, v141\n"
"v_sub_f32 v142, v142, v250\n"
"v_exp_f32 v142, v142\n"
"v_mfma_f32_16x16x32_bf16 v[136:139], v[108:111], v[148:151], v[136:139]\n"
"ds_read_b128 v[220:223], v4 offset:34816\n"
"v_cvt_pk_bf16_f32 v140, v140, v141\n"
"v_sub_f32 v143, v143, v250\n"
"v_exp_f32 v143, v143\n"
"s_waitcnt lgkmcnt(3)\n"
"v_mfma_f32_16x16x32_bf16 v[152:155], v[156:159], v[148:151], v[152:155]\n"
"ds_write_addtid_b32 v140 offset:56832\n"
"v_cvt_pk_bf16_f32 v141, v142, v143\n"
"s_waitcnt lgkmcnt(3)\n"
"v_mfma_f32_16x16x32_bf16 v[132:135], v[112:115], v[160:163], v[132:135]\n"
"ds_write_addtid_b32 v141 offset:57088\n"
"v_mfma_f32_16x16x32_bf16 v[136:139], v[116:119], v[160:163], v[136:139]\n"
"s_waitcnt lgkmcnt(3)\n"
"ds_read_b128 v[250:253], v6 offset:8192\n"
"v_mfma_f32_16x16x32_bf16 v[152:155], v[196:199], v[160:163], v[152:155]\n"
"s_waitcnt lgkmcnt(3)\n"
"ds_read_b128 v[144:147], v5 offset:34816\n"
"v_mfma_f32_16x16x32_bf16 v[132:135], v[0:3], v[220:223], v[132:135]\n"
"ds_read_b128 v[148:151], v6 offset:9216\n"
"v_mfma_f32_16x16x32_bf16 v[136:139], v[120:123], v[220:223], v[136:139]\n"
"s_waitcnt lgkmcnt(3)\n"
"ds_read_b128 v[140:143], v4 offset:49152\n"
"s_barrier\n"
"s_waitcnt lgkmcnt(3)\n"
"v_mfma_f32_16x16x32_bf16 v[152:155], v[250:253], v[220:223], v[152:155]\n"
"s_waitcnt lgkmcnt(2)\n"
"v_mfma_f32_16x16x32_bf16 v[132:135], v[124:127], v[144:147], v[132:135]\n"
"v_mfma_f32_16x16x32_bf16 v[136:139], v[128:131], v[144:147], v[136:139]\n"
"s_waitcnt lgkmcnt(1)\n"
"ds_read_b128 v[156:159], v5 offset:49152\n"
"v_mfma_f32_16x16x32_bf16 v[152:155], v[148:151], v[144:147], v[152:155]\n"
"s_waitcnt lgkmcnt(1)\n"
"v_mfma_f32_16x16x32_bf16 v[196:199], v[8:11], v[140:143], 0\n"
"v_mfma_f32_16x16x32_bf16 v[160:163], v[12:15], v[140:143], 0\n"
"ds_read_b128 v[220:223], v4 offset:51200\n"
"v_mfma_f32_16x16x32_bf16 v[250:253], v[16:19], v[140:143], 0\n"
"s_waitcnt lgkmcnt(1)\n"
"v_mfma_f32_16x16x32_bf16 v[196:199], v[20:23], v[156:159], v[196:199]\n"
"v_mul_f32 v132, s60, v132\n"
"v_mul_f32 v133, s60, v133\n"
"v_mfma_f32_16x16x32_bf16 v[160:163], v[28:31], v[156:159], v[160:163]\n"
"ds_read_b128 v[144:147], v5 offset:51200\n"
"v_cvt_pk_bf16_f32 v132, v132, v133\n"
"v_mfma_f32_16x16x32_bf16 v[250:253], v[32:35], v[156:159], v[250:253]\n"
"s_waitcnt lgkmcnt(1)\n"
"v_mul_f32 v134, s60, v134\n"
"v_mul_f32 v135, s60, v135\n"
"v_mfma_f32_16x16x32_bf16 v[196:199], v[36:39], v[220:223], v[196:199]\n"
"v_cvt_pk_bf16_f32 v133, v134, v135\n"
"v_mul_f32 v136, s60, v136\n"
"v_mfma_f32_16x16x32_bf16 v[160:163], v[40:43], v[220:223], v[160:163]\n"
"ds_read_b128 v[140:143], v4 offset:53248\n"
"v_mul_f32 v137, s60, v137\n"
"v_mfma_f32_16x16x32_bf16 v[250:253], v[44:47], v[220:223], v[250:253]\n"
"v_mul_f32 v138, s60, v138\n"
"v_mul_f32 v139, s60, v139\n"
"v_cvt_pk_bf16_f32 v134, v136, v137\n"
"v_cvt_pk_bf16_f32 v135, v138, v139\n"
"s_waitcnt lgkmcnt(1)\n"
"v_mfma_f32_16x16x32_bf16 v[196:199], v[48:51], v[144:147], v[196:199]\n"
"v_mul_f32 v152, s60, v152\n"
"v_mul_f32 v153, s60, v153\n"
"v_mfma_f32_16x16x32_bf16 v[160:163], v[52:55], v[144:147], v[160:163]\n"
"ds_read_b128 v[148:151], v5 offset:53248\n"
"v_mul_f32 v154, s60, v154\n"
"v_mfma_f32_16x16x32_bf16 v[250:253], v[56:59], v[144:147], v[250:253]\n"
"s_waitcnt lgkmcnt(1)\n"
"v_mul_f32 v155, s60, v155\n"
"v_cvt_pk_bf16_f32 v26, v152, v153\n"
"v_mfma_f32_16x16x32_bf16 v[196:199], v[60:63], v[140:143], v[196:199]\n"
"v_cvt_pk_bf16_f32 v27, v154, v155\n"
"v_mfma_f32_16x16x32_bf16 v[160:163], v[64:67], v[140:143], v[160:163]\n"
"ds_read_b128 v[136:139], v4 offset:55296\n"
"v_mfma_f32_16x16x32_bf16 v[250:253], v[68:71], v[140:143], v[250:253]\n"
"s_waitcnt lgkmcnt(1)\n"
"v_mfma_f32_16x16x32_bf16 v[196:199], v[72:75], v[148:151], v[196:199]\n"
"v_mfma_f32_16x16x32_bf16 v[160:163], v[76:79], v[148:151], v[160:163]\n"
"ds_read_b128 v[144:147], v5 offset:55296\n"
"v_mfma_f32_16x16x32_bf16 v[250:253], v[80:83], v[148:151], v[250:253]\n"
"s_waitcnt lgkmcnt(1)\n"
"v_mfma_f32_16x16x32_bf16 v[220:223], v[8:11], v[136:139], 0\n"
"v_mfma_f32_16x16x32_bf16 v[152:155], v[12:15], v[136:139], 0\n"
"ds_read_b128 v[140:143], v4 offset:57344\n"
"v_mfma_f32_16x16x32_bf16 v[156:159], v[16:19], v[136:139], 0\n"
"s_waitcnt lgkmcnt(1)\n"
"v_mfma_f32_16x16x32_bf16 v[220:223], v[20:23], v[144:147], v[220:223]\n"
"v_cvt_pk_bf16_f32 v196, v196, v197\n"
"v_cvt_pk_bf16_f32 v197, v198, v199\n"
"v_cvt_pk_bf16_f32 v198, v160, v161\n"
"v_cvt_pk_bf16_f32 v199, v162, v163\n"
"v_cvt_pk_bf16_f32 v200, v250, v251\n"
"v_cvt_pk_bf16_f32 v201, v252, v253\n"
"v_mfma_f32_16x16x32_bf16 v[152:155], v[28:31], v[144:147], v[152:155]\n"
"ds_read_b128 v[136:139], v5 offset:57344\n"
"v_bfe_u32 v225, v6, 2, 6\n"
"v_mfma_f32_16x16x32_bf16 v[156:159], v[32:35], v[144:147], v[156:159]\n"
"s_waitcnt lgkmcnt(1)\n"
"ds_read_b32 v225, v225 offset:1344\n"
"v_mfma_f32_16x16x32_bf16 v[220:223], v[36:39], v[140:143], v[220:223]\n"
"v_mfma_f32_16x16x32_bf16 v[152:155], v[40:43], v[140:143], v[152:155]\n"
"s_add_u32 s69, s50, 4294966976\n"
"ds_read_b128 v[250:253], v4 offset:59392\n"
"v_mfma_f32_16x16x32_bf16 v[156:159], v[44:47], v[140:143], v[156:159]\n"
"s_waitcnt lgkmcnt(2)\n"
"v_mfma_f32_16x16x32_bf16 v[220:223], v[48:51], v[136:139], v[220:223]\n"
"v_mfma_f32_16x16x32_bf16 v[152:155], v[52:55], v[136:139], v[152:155]\n"
"s_waitcnt lgkmcnt(1)\n"
"ds_read_b128 v[144:147], v5 offset:59392\n"
"v_add_u32_sdwa v224, v225, v7 dst_sel:DWORD dst_unused:UNUSED_PAD src0_sel:WORD_0 src1_sel:DWORD\n"
"v_mfma_f32_16x16x32_bf16 v[156:159], v[56:59], v[136:139], v[156:159]\n"
"s_add_u32 s73, s50, 4294967280\n"
"v_min_u32 v177, s69, v224\n"
"s_waitcnt lgkmcnt(1)\n"
"v_mfma_f32_16x16x32_bf16 v[220:223], v[60:63], v[250:253], v[220:223]\n"
"v_mfma_f32_16x16x32_bf16 v[152:155], v[64:67], v[250:253], v[152:155]\n"
"ds_read_b128 v[140:143], v177 offset:2704\n"
"v_max_u32 v176, s73, v224\n"
"v_mfma_f32_16x16x32_bf16 v[156:159], v[68:71], v[250:253], v[156:159]\n"
"s_waitcnt lgkmcnt(1)\n"
"v_mfma_f32_16x16x32_bf16 v[220:223], v[72:75], v[144:147], v[220:223]\n"
"s_add_u32 s68, s50, 4294965936\n"
"ds_read_b128 v[136:139], v177 offset:2720\n"
"v_mfma_f32_16x16x32_bf16 v[152:155], v[76:79], v[144:147], v[152:155]\n"
"v_mfma_f32_16x16x32_bf16 v[156:159], v[80:83], v[144:147], v[156:159]\n"
"s_waitcnt lgkmcnt(1)\n"
"ds_read_b128 v[148:151], v176 offset:32\n"
"v_max_u32 v251, s68, v224\n"
"v_mfma_f32_16x16x32_bf16 v[140:143], v[184:187], v[132:135], v[140:143]\n"
"v_mfma_f32_16x16x32_bf16 v[140:143], v[186:189], v[24:27], v[140:143]\n"
"ds_read_b128 v[252:255], v176 offset:48\n"
"v_cvt_pk_bf16_f32 v220, v220, v221\n"
"v_cvt_pk_bf16_f32 v221, v222, v223\n"
"v_cvt_pk_bf16_f32 v222, v152, v153\n"
"v_cvt_pk_bf16_f32 v223, v154, v155\n"
"v_cvt_pk_bf16_f32 v224, v156, v157\n"
"v_cvt_pk_bf16_f32 v225, v158, v159\n"
"s_waitcnt lgkmcnt(2)\n"
"v_mfma_f32_16x16x32_bf16 v[136:139], v[190:193], v[132:135], v[136:139]\n"
"v_max3_f32 v174, v140, v141, v142\n"
"v_max_f32 v174, v174, v143\n"
"v_mfma_f32_16x16x32_bf16 v[136:139], v[192:195], v[24:27], v[136:139]\n"
"s_waitcnt lgkmcnt(1)\n"
"ds_read_b128 v[144:147], v251 offset:1392\n"
"v_mfma_f32_16x16x32_bf16 v[148:151], v[208:211], v[132:135], v[148:151]\n"
"v_mfma_f32_16x16x32_bf16 v[148:151], v[210:213], v[24:27], v[148:151]\n"
"s_waitcnt lgkmcnt(1)\n"
"ds_read_b128 v[152:155], v176 offset:16\n"
"s_nop 1\n"
"v_max3_f32 v174, v174, v136, v137\n"
"v_max3_f32 v174, v174, v138, v139\n"
"v_mfma_f32_16x16x32_bf16 v[252:255], v[214:217], v[132:135], v[252:255]\n"
"v_mfma_f32_16x16x32_bf16 v[252:255], v[216:219], v[24:27], v[252:255]\n"
"s_waitcnt lgkmcnt(1)\n"
"ds_read_b128 v[140:143], v251 offset:1360\n"
"v_max3_f32 v174, v174, v148, v149\n"
"v_max3_f32 v174, v174, v150, v151\n"
"v_mfma_f32_16x16x32_bf16 v[144:147], v[244:247], v[132:135], v[144:147]\n"
"v_mfma_f32_16x16x32_bf16 v[144:147], v[246:249], v[24:27], v[144:147]\n"
"s_waitcnt lgkmcnt(1)\n"
"ds_read_b128 v[136:139], v251 offset:1376\n"
"v_max3_f32 v174, v174, v252, v253\n"
"v_max3_f32 v174, v174, v254, v255\n"
"v_mfma_f32_16x16x32_bf16 v[152:155], v[202:205], v[132:135], v[152:155]\n"
"v_mfma_f32_16x16x32_bf16 v[152:155], v[204:207], v[24:27], v[152:155]\n"
"s_waitcnt lgkmcnt(1)\n"
"ds_read_b128 v[148:151], v177 offset:2736\n"
"v_max3_f32 v174, v174, v144, v145\n"
"v_max3_f32 v174, v174, v146, v147\n"
"v_mfma_f32_16x16x32_bf16 v[140:143], v[232:235], v[132:135], v[140:143]\n"
"v_mfma_f32_16x16x32_bf16 v[140:143], v[234:237], v[24:27], v[140:143]\n"
"s_waitcnt lgkmcnt(1)\n"
"ds_read_b128 v[252:255], v177 offset:2704\n"
"v_max3_f32 v174, v174, v152, v153\n"
"v_max3_f32 v174, v174, v154, v155\n"
"v_mfma_f32_16x16x32_bf16 v[136:139], v[238:241], v[132:135], v[136:139]\n"
"v_mfma_f32_16x16x32_bf16 v[136:139], v[240:243], v[24:27], v[136:139]\n"
"s_waitcnt lgkmcnt(1)\n"
"ds_read_b128 v[144:147], v177 offset:2720\n"
"v_max3_f32 v174, v174, v140, v141\n"
"v_max3_f32 v174, v174, v142, v143\n"
"v_mfma_f32_16x16x32_bf16 v[148:151], v[196:199], v[132:135], v[148:151]\n"
"v_mfma_f32_16x16x32_bf16 v[148:151], v[198:201], v[24:27], v[148:151]\n"
"s_waitcnt lgkmcnt(1)\n"
"ds_read_b128 v[152:155], v176 offset:32\n"
"v_max3_f32 v174, v174, v136, v137\n"
"v_max3_f32 v174, v174, v138, v139\n"
"v_mfma_f32_16x16x32_bf16 v[252:255], v[184:187], v[132:135], v[252:255]\n"
"v_mfma_f32_16x16x32_bf16 v[252:255], v[186:189], v[24:27], v[252:255]\n"
"ds_read_b128 v[140:143], v176 offset:48\n"
"s_nop 0\n"
"v_max3_f32 v174, v174, v148, v149\n"
"v_max3_f32 v174, v174, v150, v151\n"
"v_mov_b32 v175, v174\n"
"s_nop 1\n"
"v_permlane16_swap_b32 v174, v175\n"
"v_max_f32 v174, v174, v175\n"
"v_mov_b32 v175, v174\n"
"s_nop 1\n"
"v_permlane32_swap_b32 v174, v175\n"
"v_max_f32 v174, v174, v175\n"
"v_sub_f32 v252, v252, v174\n"
"v_exp_f32 v252, v252\n"
"v_sub_f32 v253, v253, v174\n"
"v_exp_f32 v253, v253\n"
"v_sub_f32 v254, v254, v174\n"
"v_exp_f32 v254, v254\n"
"v_sub_f32 v255, v255, v174\n"
"v_exp_f32 v255, v255\n"
"s_waitcnt lgkmcnt(2)\n"
"v_mfma_f32_16x16x32_bf16 v[144:147], v[190:193], v[132:135], v[144:147]\n"
"v_cvt_pk_bf16_f32 v252, v252, v253\n"
"v_cvt_pk_bf16_f32 v253, v254, v255\n"
"v_mfma_f32_16x16x32_bf16 v[144:147], v[192:195], v[24:27], v[144:147]\n"
"ds_write_addtid_b32 v252 offset:53248\n"
"ds_read_b128 v[136:139], v251 offset:1392\n"
"ds_write_addtid_b32 v253 offset:53504\n"
"s_waitcnt lgkmcnt(4)\n"
"v_mfma_f32_16x16x32_bf16 v[152:155], v[208:211], v[132:135], v[152:155]\n"
"v_mfma_f32_16x16x32_bf16 v[152:155], v[210:213], v[24:27], v[152:155]\n"
"ds_read_b128 v[148:151], v176 offset:16\n"
"s_nop 0\n"
"v_sub_f32 v144, v144, v174\n"
"v_exp_f32 v144, v144\n"
"v_sub_f32 v145, v145, v174\n"
"v_exp_f32 v145, v145\n"
"v_sub_f32 v146, v146, v174\n"
"v_exp_f32 v146, v146\n"
"v_sub_f32 v147, v147, v174\n"
"v_exp_f32 v147, v147\n"
"v_sub_f32 v152, v152, v174\n"
"v_exp_f32 v152, v152\n"
"v_sub_f32 v153, v153, v174\n"
"v_exp_f32 v153, v153\n"
"v_sub_f32 v154, v154, v174\n"
"v_exp_f32 v154, v154\n"
"v_sub_f32 v155, v155, v174\n"
"v_exp_f32 v155, v155\n"
"v_cvt_pk_bf16_f32 v144, v144, v145\n"
"v_cvt_pk_bf16_f32 v145, v146, v147\n"
"v_cvt_pk_bf16_f32 v152, v152, v153\n"
"v_cvt_pk_bf16_f32 v153, v154, v155\n"
"ds_write_addtid_b32 v144 offset:53760\n"
"ds_write_addtid_b32 v145 offset:54016\n"
"s_waitcnt lgkmcnt(6)\n"
"v_mfma_f32_16x16x32_bf16 v[140:143], v[214:217], v[132:135], v[140:143]\n"
"ds_write_addtid_b32 v152 offset:54272\n"
"v_mfma_f32_16x16x32_bf16 v[140:143], v[216:219], v[24:27], v[140:143]\n"
"ds_read_b128 v[252:255], v251 offset:1360\n"
"ds_write_addtid_b32 v153 offset:54528\n"
"s_waitcnt lgkmcnt(7)\n"
"v_mfma_f32_16x16x32_bf16 v[136:139], v[244:247], v[132:135], v[136:139]\n"
"v_mfma_f32_16x16x32_bf16 v[136:139], v[246:249], v[24:27], v[136:139]\n"
"ds_read_b128 v[144:147], v251 offset:1376\n"
"s_nop 1\n"
"v_sub_f32 v140, v140, v174\n"
"v_exp_f32 v140, v140\n"
"v_sub_f32 v141, v141, v174\n"
"v_exp_f32 v141, v141\n"
"v_sub_f32 v142, v142, v174\n"
"v_exp_f32 v142, v142\n"
"v_sub_f32 v143, v143, v174\n"
"v_exp_f32 v143, v143\n"
"v_sub_f32 v136, v136, v174\n"
"v_exp_f32 v136, v136\n"
"v_sub_f32 v137, v137, v174\n"
"v_exp_f32 v137, v137\n"
"v_sub_f32 v138, v138, v174\n"
"v_exp_f32 v138, v138\n"
"v_sub_f32 v139, v139, v174\n"
"v_exp_f32 v139, v139\n"
"v_cvt_pk_bf16_f32 v140, v140, v141\n"
"v_cvt_pk_bf16_f32 v136, v136, v137\n"
"v_cvt_pk_bf16_f32 v141, v142, v143\n"
"v_cvt_pk_bf16_f32 v137, v138, v139\n"
"ds_write_addtid_b32 v140 offset:54784\n"
"ds_write_addtid_b32 v136 offset:33792\n"
"ds_write_addtid_b32 v141 offset:55040\n"
"s_waitcnt lgkmcnt(9)\n"
"v_mfma_f32_16x16x32_bf16 v[148:151], v[202:205], v[132:135], v[148:151]\n"
"ds_write_addtid_b32 v137 offset:34048\n"
"v_mfma_f32_16x16x32_bf16 v[148:151], v[204:207], v[24:27], v[148:151]\n"
"s_waitcnt lgkmcnt(6)\n"
"ds_read_b128 v[152:155], v177 offset:2736\n"
"v_mfma_f32_16x16x32_bf16 v[252:255], v[232:235], v[132:135], v[252:255]\n"
"v_mfma_f32_16x16x32_bf16 v[252:255], v[234:237], v[24:27], v[252:255]\n"
"s_waitcnt lgkmcnt(1)\n"
"ds_read_b128 v[140:143], v4 offset:12288\n"
"s_barrier\n"
"s_nop 0\n"
"v_sub_f32 v148, v148, v174\n"
"v_exp_f32 v148, v148\n"
"v_sub_f32 v149, v149, v174\n"
"v_exp_f32 v149, v149\n"
"v_sub_f32 v150, v150, v174\n"
"v_exp_f32 v150, v150\n"
"v_sub_f32 v151, v151, v174\n"
"v_exp_f32 v151, v151\n"
"v_sub_f32 v252, v252, v174\n"
"v_exp_f32 v252, v252\n"
"v_sub_f32 v253, v253, v174\n"
"v_exp_f32 v253, v253\n"
"v_cvt_pk_bf16_f32 v148, v148, v149\n"
"v_cvt_pk_bf16_f32 v149, v150, v151\n"
"v_sub_f32 v254, v254, v174\n"
"v_exp_f32 v254, v254\n"
"v_sub_f32 v255, v255, v174\n"
"v_exp_f32 v255, v255\n"
"v_cvt_pk_bf16_f32 v252, v252, v253\n"
"v_cvt_pk_bf16_f32 v253, v254, v255\n"
"ds_write_addtid_b32 v148 offset:57856\n"
"ds_write_addtid_b32 v149 offset:58112\n"
"v_mfma_f32_16x16x32_bf16 v[144:147], v[238:241], v[132:135], v[144:147]\n"
"ds_write_addtid_b32 v252 offset:58368\n"
"v_mfma_f32_16x16x32_bf16 v[144:147], v[240:243], v[24:27], v[144:147]\n"
"ds_write_addtid_b32 v253 offset:58624\n"
"s_waitcnt lgkmcnt(5)\n"
"v_mfma_f32_16x16x32_bf16 v[152:155], v[196:199], v[132:135], v[152:155]\n"
"ds_read_b128 v[202:205], v5 offset:12288\n"
"v_mfma_f32_16x16x32_bf16 v[152:155], v[198:201], v[24:27], v[152:155]\n"
"s_waitcnt lgkmcnt(5)\n"
"v_mfma_f32_16x16x32_bf16 v[136:139], v[84:87], v[140:143], 0\n"
"ds_read_b128 v[148:151], v6 offset:5120\n"
"v_mfma_f32_16x16x32_bf16 v[156:159], v[88:91], v[140:143], 0\n"
"ds_read_b128 v[250:253], v4 offset:14336\n"
"v_sub_f32 v144, v144, v174\n"
"v_exp_f32 v144, v144\n"
"v_sub_f32 v145, v145, v174\n"
"v_exp_f32 v145, v145\n"
"v_sub_f32 v146, v146, v174\n"
"v_exp_f32 v146, v146\n"
"v_sub_f32 v147, v147, v174\n"
"v_exp_f32 v147, v147\n"
"v_mfma_f32_16x16x32_bf16 v[132:135], v[92:95], v[140:143], 0\n"
"s_waitcnt lgkmcnt(2)\n"
"v_cvt_pk_bf16_f32 v144, v144, v145\n"
"v_cvt_pk_bf16_f32 v145, v146, v147\n"
"v_mfma_f32_16x16x32_bf16 v[136:139], v[96:99], v[202:205], v[136:139]\n"
"ds_write_addtid_b32 v144 offset:58880\n"
"v_mfma_f32_16x16x32_bf16 v[156:159], v[100:103], v[202:205], v[156:159]\n"
"ds_write_addtid_b32 v145 offset:59136\n"
"s_waitcnt lgkmcnt(3)\n"
"v_mfma_f32_16x16x32_bf16 v[132:135], v[148:151], v[202:205], v[132:135]\n"
"s_waitcnt lgkmcnt(2)\n"
"ds_read_b128 v[140:143], v6 offset:6144\n"
"v_sub_f32 v152, v152, v174\n"
"v_exp_f32 v152, v152\n"
"v_mfma_f32_16x16x32_bf16 v[136:139], v[104:107], v[250:253], v[136:139]\n"
"ds_read_b128 v[160:163], v5 offset:14336\n"
"v_sub_f32 v153, v153, v174\n"
"v_exp_f32 v153, v153\n"
"v_mfma_f32_16x16x32_bf16 v[156:159], v[108:111], v[250:253], v[156:159]\n"
"ds_read_b128 v[202:205], v6 offset:7168\n"
"v_sub_f32 v154, v154, v174\n"
"v_exp_f32 v154, v154\n"
"v_sub_f32 v155, v155, v174\n"
"v_exp_f32 v155, v155\n"
"v_cvt_pk_bf16_f32 v152, v152, v153\n"
"s_waitcnt lgkmcnt(2)\n"
"v_mfma_f32_16x16x32_bf16 v[132:135], v[140:143], v[250:253], v[132:135]\n"
"s_waitcnt lgkmcnt(1)\n"
"ds_read_b128 v[144:147], v4 offset:16384\n"
"v_cvt_pk_bf16_f32 v153, v154, v155\n"
"v_mfma_f32_16x16x32_bf16 v[136:139], v[112:115], v[160:163], v[136:139]\n"
"ds_write_addtid_b32 v152 offset:57344\n"
"v_mfma_f32_16x16x32_bf16 v[156:159], v[116:119], v[160:163], v[156:159]\n"
"ds_write_addtid_b32 v153 offset:57600\n"
"s_waitcnt lgkmcnt(3)\n"
"v_mfma_f32_16x16x32_bf16 v[132:135], v[202:205], v[160:163], v[132:135]\n"
"s_waitcnt lgkmcnt(2)\n"
"ds_read_b128 v[140:143], v6 offset:8192\n"
"ds_read_b128 v[148:151], v5 offset:16384\n"
"v_mfma_f32_16x16x32_bf16 v[136:139], v[0:3], v[144:147], v[136:139]\n"
"ds_read_b128 v[164:167], v6 offset:9216\n"
"v_mfma_f32_16x16x32_bf16 v[156:159], v[120:123], v[144:147], v[156:159]\n"
"s_waitcnt lgkmcnt(3)\n"
"s_barrier\n"
"s_waitcnt lgkmcnt(2)\n"
"v_mfma_f32_16x16x32_bf16 v[132:135], v[140:143], v[144:147], v[132:135]\n"
"s_waitcnt lgkmcnt(1)\n"
"ds_read_b128 v[202:205], v4 offset:18432\n"
"v_mfma_f32_16x16x32_bf16 v[136:139], v[124:127], v[148:151], v[136:139]\n"
"v_mfma_f32_16x16x32_bf16 v[156:159], v[128:131], v[148:151], v[156:159]\n"
"s_waitcnt lgkmcnt(1)\n"
"v_mfma_f32_16x16x32_bf16 v[132:135], v[164:167], v[148:151], v[132:135]\n"
"s_waitcnt lgkmcnt(0)\n"
"ds_read_b128 v[152:155], v5 offset:18432\n"
"v_mfma_f32_16x16x32_bf16 v[250:253], v[8:11], v[202:205], 0\n"
"v_mfma_f32_16x16x32_bf16 v[140:143], v[12:15], v[202:205], 0\n"
"v_mfma_f32_16x16x32_bf16 v[144:147], v[16:19], v[202:205], 0\n"
"s_waitcnt lgkmcnt(0)\n"
"ds_read_b128 v[160:163], v4 offset:20480\n"
"v_mfma_f32_16x16x32_bf16 v[250:253], v[20:23], v[152:155], v[250:253]\n"
"v_mul_f32 v136, s60, v136\n"
"v_mul_f32 v137, s60, v137\n"
"v_mfma_f32_16x16x32_bf16 v[140:143], v[28:31], v[152:155], v[140:143]\n"
"v_cvt_pk_bf16_f32 v136, v136, v137\n"
"v_mul_f32 v138, s60, v138\n"
"v_mfma_f32_16x16x32_bf16 v[144:147], v[32:35], v[152:155], v[144:147]\n"
"ds_read_b128 v[202:205], v5 offset:20480\n"
"v_mul_f32 v139, s60, v139\n"
"v_cvt_pk_bf16_f32 v137, v138, v139\n"
"v_mul_f32 v156, s60, v156\n"
"v_mul_f32 v157, s60, v157\n"
"v_mul_f32 v158, s60, v158\n"
"v_mul_f32 v159, s60, v159\n"
"v_cvt_pk_bf16_f32 v138, v156, v157\n"
"s_waitcnt lgkmcnt(1)\n"
"v_mfma_f32_16x16x32_bf16 v[250:253], v[36:39], v[160:163], v[250:253]\n"
"v_cvt_pk_bf16_f32 v139, v158, v159\n"
"v_mul_f32 v132, s60, v132\n"
"v_mfma_f32_16x16x32_bf16 v[140:143], v[40:43], v[160:163], v[140:143]\n"
"v_mul_f32 v133, s60, v133\n"
"v_mul_f32 v134, s60, v134\n"
"v_mfma_f32_16x16x32_bf16 v[144:147], v[44:47], v[160:163], v[144:147]\n"
"s_waitcnt lgkmcnt(0)\n"
"ds_read_b128 v[148:151], v4 offset:22528\n"
"v_mul_f32 v135, s60, v135\n"
"v_mfma_f32_16x16x32_bf16 v[250:253], v[48:51], v[202:205], v[250:253]\n"
"v_cvt_pk_bf16_f32 v26, v132, v133\n"
"v_cvt_pk_bf16_f32 v27, v134, v135\n"
"v_mfma_f32_16x16x32_bf16 v[140:143], v[52:55], v[202:205], v[140:143]\n"
"v_mfma_f32_16x16x32_bf16 v[144:147], v[56:59], v[202:205], v[144:147]\n"
"s_waitcnt lgkmcnt(0)\n"
"ds_read_b128 v[152:155], v5 offset:22528\n"
"v_mfma_f32_16x16x32_bf16 v[250:253], v[60:63], v[148:151], v[250:253]\n"
"v_mfma_f32_16x16x32_bf16 v[140:143], v[64:67], v[148:151], v[140:143]\n"
"v_mfma_f32_16x16x32_bf16 v[144:147], v[68:71], v[148:151], v[144:147]\n"
"s_waitcnt lgkmcnt(0)\n"
"ds_read_b128 v[132:135], v4 offset:36864\n"
"v_mfma_f32_16x16x32_bf16 v[250:253], v[72:75], v[152:155], v[250:253]\n"
"v_mfma_f32_16x16x32_bf16 v[140:143], v[76:79], v[152:155], v[140:143]\n"
"v_mfma_f32_16x16x32_bf16 v[144:147], v[80:83], v[152:155], v[144:147]\n"
"s_waitcnt lgkmcnt(0)\n"
"ds_read_b128 v[156:159], v5 offset:36864\n"
"v_mfma_f32_16x16x32_bf16 v[202:205], v[8:11], v[132:135], 0\n"
"v_mfma_f32_16x16x32_bf16 v[148:151], v[12:15], v[132:135], 0\n"
"v_mfma_f32_16x16x32_bf16 v[160:163], v[16:19], v[132:135], 0\n"
"s_waitcnt lgkmcnt(0)\n"
"ds_read_b128 v[164:167], v4 offset:38912\n"
"v_mfma_f32_16x16x32_bf16 v[202:205], v[20:23], v[156:159], v[202:205]\n"
"v_cvt_pk_bf16_f32 v250, v250, v251\n"
"v_cvt_pk_bf16_f32 v251, v252, v253\n"
"v_cvt_pk_bf16_f32 v252, v140, v141\n"
"v_cvt_pk_bf16_f32 v253, v142, v143\n"
"v_cvt_pk_bf16_f32 v254, v144, v145\n"
"v_cvt_pk_bf16_f32 v255, v146, v147\n"
"v_mfma_f32_16x16x32_bf16 v[148:151], v[28:31], v[156:159], v[148:151]\n"
"v_mfma_f32_16x16x32_bf16 v[160:163], v[32:35], v[156:159], v[160:163]\n"
"s_waitcnt lgkmcnt(0)\n"
"ds_read_b128 v[132:135], v5 offset:38912\n"
"v_mfma_f32_16x16x32_bf16 v[202:205], v[36:39], v[164:167], v[202:205]\n"
"v_bfe_u32 v177, v6, 2, 6\n"
"v_mfma_f32_16x16x32_bf16 v[148:151], v[40:43], v[164:167], v[148:151]\n"
"ds_read_b32 v177, v177 offset:1408\n"
"v_mfma_f32_16x16x32_bf16 v[160:163], v[44:47], v[164:167], v[160:163]\n"
"s_add_u32 s70, s50, 4294967264\n"
"ds_read_b128 v[140:143], v4 offset:40960\n"
"s_waitcnt lgkmcnt(2)\n"
"v_mfma_f32_16x16x32_bf16 v[202:205], v[48:51], v[132:135], v[202:205]\n"
"v_mfma_f32_16x16x32_bf16 v[148:151], v[52:55], v[132:135], v[148:151]\n"
"v_mfma_f32_16x16x32_bf16 v[160:163], v[56:59], v[132:135], v[160:163]\n"
"s_waitcnt lgkmcnt(1)\n"
"ds_read_b128 v[144:147], v5 offset:40960\n"
"v_add_u32_sdwa v176, v177, v7 dst_sel:DWORD dst_unused:UNUSED_PAD src0_sel:WORD_0 src1_sel:DWORD\n"
"s_nop 0\n"
"v_max_u32 v175, s70, v176\n"
"v_add_u32_sdwa v177, sext(v177), v176 dst_sel:DWORD dst_unused:UNUSED_PAD src0_sel:WORD_1 src1_sel:DWORD\n"
"s_waitcnt lgkmcnt(1)\n"
"v_mfma_f32_16x16x32_bf16 v[202:205], v[60:63], v[140:143], v[202:205]\n"
"s_add_u32 s72, s50, 4294966960\n"
"v_max_u32 v174, s70, v177\n"
"v_mfma_f32_16x16x32_bf16 v[148:151], v[64:67], v[140:143], v[148:151]\n"
"v_mfma_f32_16x16x32_bf16 v[160:163], v[68:71], v[140:143], v[160:163]\n"
"s_waitcnt lgkmcnt(0)\n"
"ds_read_b128 v[132:135], v175 offset:48\n"
"v_min_u32 v173, s72, v176\n"
"v_mfma_f32_16x16x32_bf16 v[202:205], v[72:75], v[144:147], v[202:205]\n"
"v_mfma_f32_16x16x32_bf16 v[148:151], v[76:79], v[144:147], v[148:151]\n"
"ds_read_b128 v[152:155], v174 offset:64\n"
"v_mfma_f32_16x16x32_bf16 v[160:163], v[80:83], v[144:147], v[160:163]\n"
"s_waitcnt lgkmcnt(1)\n"
"v_mfma_f32_16x16x32_bf16 v[132:135], v[214:217], v[136:139], v[132:135]\n"
"s_add_u32 s71, s50, 1008\n"
"ds_read_b128 v[140:143], v173 offset:2720\n"
"v_mfma_f32_16x16x32_bf16 v[132:135], v[216:219], v[24:27], v[132:135]\n"
"s_waitcnt lgkmcnt(1)\n"
"v_mfma_f32_16x16x32_bf16 v[152:155], v[220:223], v[136:139], v[152:155]\n"
"ds_read_b128 v[156:159], v173 offset:2736\n"
"v_cvt_pk_bf16_f32 v202, v202, v203\n"
"v_cvt_pk_bf16_f32 v203, v204, v205\n"
"v_cvt_pk_bf16_f32 v204, v148, v149\n"
"v_cvt_pk_bf16_f32 v205, v150, v151\n"
"v_cvt_pk_bf16_f32 v206, v160, v161\n"
"v_cvt_pk_bf16_f32 v207, v162, v163\n"
"v_mfma_f32_16x16x32_bf16 v[152:155], v[222:225], v[24:27], v[152:155]\n"
"s_waitcnt lgkmcnt(1)\n"
"v_max3_f32 v170, v132, v133, v134\n"
"v_max_f32 v170, v170, v135\n"
"v_mfma_f32_16x16x32_bf16 v[140:143], v[190:193], v[136:139], v[140:143]\n"
"ds_read_b128 v[144:147], v175 offset:32\n"
"v_min_u32 v169, s71, v176\n"
"v_mfma_f32_16x16x32_bf16 v[140:143], v[192:195], v[24:27], v[140:143]\n"
"s_waitcnt lgkmcnt(1)\n"
"v_max3_f32 v170, v170, v152, v153\n"
"v_max3_f32 v170, v170, v154, v155\n"
"v_mfma_f32_16x16x32_bf16 v[156:159], v[196:199], v[136:139], v[156:159]\n"
"ds_read_b128 v[132:135], v169 offset:1376\n"
"v_min_u32 v168, s71, v177\n"
"v_mfma_f32_16x16x32_bf16 v[156:159], v[198:201], v[24:27], v[156:159]\n"
"s_waitcnt lgkmcnt(1)\n"
"v_max3_f32 v170, v170, v140, v141\n"
"v_max3_f32 v170, v170, v142, v143\n"
"v_mfma_f32_16x16x32_bf16 v[144:147], v[208:211], v[136:139], v[144:147]\n"
"ds_read_b128 v[148:151], v169 offset:1392\n"
"v_min_u32 v176, s72, v177\n"
"v_mfma_f32_16x16x32_bf16 v[144:147], v[210:213], v[24:27], v[144:147]\n"
"s_waitcnt lgkmcnt(1)\n"
"v_max3_f32 v170, v170, v156, v157\n"
"v_max3_f32 v170, v170, v158, v159\n"
"v_mfma_f32_16x16x32_bf16 v[132:135], v[238:241], v[136:139], v[132:135]\n"
"ds_read_b128 v[140:143], v168 offset:1408\n"
"v_mfma_f32_16x16x32_bf16 v[132:135], v[240:243], v[24:27], v[132:135]\n"
"s_waitcnt lgkmcnt(1)\n"
"s_nop 0\n"
"v_max3_f32 v170, v170, v144, v145\n"
"v_max3_f32 v170, v170, v146, v147\n"
"v_mfma_f32_16x16x32_bf16 v[148:151], v[244:247], v[136:139], v[148:151]\n"
"ds_read_b128 v[152:155], v176 offset:2752\n"
"v_mfma_f32_16x16x32_bf16 v[148:151], v[246:249], v[24:27], v[148:151]\n"
"s_waitcnt lgkmcnt(1)\n"
"v_max3_f32 v170, v170, v132, v133\n"
"v_max3_f32 v170, v170, v134, v135\n"
"v_mfma_f32_16x16x32_bf16 v[140:143], v[250:253], v[136:139], v[140:143]\n"
"ds_read_b128 v[144:147], v175 offset:48\n"
"v_mfma_f32_16x16x32_bf16 v[140:143], v[252:255], v[24:27], v[140:143]\n"
"s_waitcnt lgkmcnt(1)\n"
"s_nop 0\n"
"v_max3_f32 v170, v170, v148, v149\n"
"v_max3_f32 v170, v170, v150, v151\n"
"v_mfma_f32_16x16x32_bf16 v[152:155], v[202:205], v[136:139], v[152:155]\n"
"ds_read_b128 v[132:135], v174 offset:64\n"
"v_mfma_f32_16x16x32_bf16 v[152:155], v[204:207], v[24:27], v[152:155]\n"
"s_waitcnt lgkmcnt(1)\n"
"v_max3_f32 v170, v170, v140, v141\n"
"v_max3_f32 v170, v170, v142, v143\n"
"v_mfma_f32_16x16x32_bf16 v[144:147], v[214:217], v[136:139], v[144:147]\n"
"ds_read_b128 v[148:151], v173 offset:2720\n"
"v_mfma_f32_16x16x32_bf16 v[144:147], v[216:219], v[24:27], v[144:147]\n"
"s_nop 1\n"
"v_max3_f32 v170, v170, v152, v153\n"
"v_max3_f32 v170, v170, v154, v155\n"
"v_mov_b32 v171, v170\n"
"s_nop 1\n"
"v_permlane16_swap_b32 v170, v171\n"
"v_max_f32 v170, v170, v171\n"
"v_mov_b32 v171, v170\n"
"s_nop 1\n"
"v_permlane32_swap_b32 v170, v171\n"
"v_max_f32 v170, v170, v171\n"
"v_sub_f32 v144, v144, v170\n"
"v_exp_f32 v144, v144\n"
"v_sub_f32 v145, v145, v170\n"
"v_exp_f32 v145, v145\n"
"v_sub_f32 v146, v146, v170\n"
"v_exp_f32 v146, v146\n"
"v_sub_f32 v147, v147, v170\n"
"v_exp_f32 v147, v147\n"
"s_waitcnt lgkmcnt(1)\n"
"v_mfma_f32_16x16x32_bf16 v[132:135], v[220:223], v[136:139], v[132:135]\n"
"ds_read_b128 v[214:217], v173 offset:2736\n"
"v_cvt_pk_bf16_f32 v144, v144, v145\n"
"v_mfma_f32_16x16x32_bf16 v[132:135], v[222:225], v[24:27], v[132:135]\n"
"ds_write_addtid_b32 v144 offset:55296\n"
"v_cvt_pk_bf16_f32 v145, v146, v147\n"
"ds_write_addtid_b32 v145 offset:55552\n"
"s_waitcnt lgkmcnt(3)\n"
"v_mfma_f32_16x16x32_bf16 v[148:151], v[190:193], v[136:139], v[148:151]\n"
"ds_read_b128 v[218:221], v175 offset:32\n"
"v_mfma_f32_16x16x32_bf16 v[148:151], v[192:195], v[24:27], v[148:151]\n"
"s_nop 0\n"
"v_sub_f32 v132, v132, v170\n"
"v_exp_f32 v132, v132\n"
"v_sub_f32 v133, v133, v170\n"
"v_exp_f32 v133, v133\n"
"v_sub_f32 v134, v134, v170\n"
"v_exp_f32 v134, v134\n"
"v_sub_f32 v135, v135, v170\n"
"v_exp_f32 v135, v135\n"
"v_sub_f32 v150, v150, v170\n"
"v_exp_f32 v150, v150\n"
"v_sub_f32 v151, v151, v170\n"
"v_exp_f32 v151, v151\n"
"v_cvt_pk_bf16_f32 v132, v132, v133\n"
"v_cvt_pk_bf16_f32 v133, v134, v135\n"
"v_cvt_pk_bf16_f32 v149, v150, v151\n"
"ds_write_addtid_b32 v132 offset:55808\n"
"ds_write_addtid_b32 v133 offset:56064\n"
"s_waitcnt lgkmcnt(5)\n"
"v_mfma_f32_16x16x32_bf16 v[214:217], v[196:199], v[136:139], v[214:217]\n"
"ds_read_b128 v[222:225], v169 offset:1376\n"
"v_mfma_f32_16x16x32_bf16 v[214:217], v[198:201], v[24:27], v[214:217]\n"
"ds_write_addtid_b32 v149 offset:56576\n"
"s_waitcnt lgkmcnt(4)\n"
"v_mfma_f32_16x16x32_bf16 v[218:221], v[208:211], v[136:139], v[218:221]\n"
"ds_read_b128 v[140:143], v169 offset:1392\n"
"v_mfma_f32_16x16x32_bf16 v[218:221], v[210:213], v[24:27], v[218:221]\n"
"s_nop 2\n"
"v_sub_f32 v214, v214, v170\n"
"v_exp_f32 v214, v214\n"
"v_sub_f32 v215, v215, v170\n"
"v_exp_f32 v215, v215\n"
"v_sub_f32 v216, v216, v170\n"
"v_exp_f32 v216, v216\n"
"v_sub_f32 v217, v217, v170\n"
"v_exp_f32 v217, v217\n"
"v_cvt_pk_bf16_f32 v214, v214, v215\n"
"v_cvt_pk_bf16_f32 v215, v216, v217\n"
"v_sub_f32 v220, v220, v170\n"
"v_exp_f32 v220, v220\n"
"v_sub_f32 v221, v221, v170\n"
"v_exp_f32 v221, v221\n"
"ds_write_addtid_b32 v214 offset:56832\n"
"v_cvt_pk_bf16_f32 v219, v220, v221\n"
"ds_write_addtid_b32 v215 offset:57088\n"
"s_waitcnt lgkmcnt(4)\n"
"v_mfma_f32_16x16x32_bf16 v[222:225], v[238:241], v[136:139], v[222:225]\n"
"ds_read_b128 v[208:211], v168 offset:1408\n"
"v_mfma_f32_16x16x32_bf16 v[222:225], v[240:243], v[24:27], v[222:225]\n"
"s_waitcnt lgkmcnt(1)\n"
"s_barrier\n"
"ds_write_addtid_b32 v219 offset:58112\n"
"v_mfma_f32_16x16x32_bf16 v[140:143], v[244:247], v[136:139], v[140:143]\n"
"ds_read_b128 v[212:215], v176 offset:2752\n"
"v_mfma_f32_16x16x32_bf16 v[140:143], v[246:249], v[24:27], v[140:143]\n"
"s_nop 1\n"
"v_sub_f32 v224, v224, v170\n"
"v_exp_f32 v224, v224\n"
"v_sub_f32 v225, v225, v170\n"
"v_exp_f32 v225, v225\n"
"s_nop 0\n"
"v_cvt_pk_bf16_f32 v223, v224, v225\n"
"ds_write_addtid_b32 v223 offset:58624\n"
"s_waitcnt lgkmcnt(3)\n"
"v_mfma_f32_16x16x32_bf16 v[208:211], v[250:253], v[136:139], v[208:211]\n"
"ds_read_b128 v[216:219], v4 offset:43008\n"
"v_sub_f32 v140, v140, v170\n"
"v_exp_f32 v140, v140\n"
"v_sub_f32 v141, v141, v170\n"
"v_exp_f32 v141, v141\n"
"v_sub_f32 v142, v142, v170\n"
"v_exp_f32 v142, v142\n"
"v_sub_f32 v143, v143, v170\n"
"v_exp_f32 v143, v143\n"
"v_mfma_f32_16x16x32_bf16 v[208:211], v[252:255], v[24:27], v[208:211]\n"
"v_cvt_pk_bf16_f32 v140, v140, v141\n"
"v_cvt_pk_bf16_f32 v141, v142, v143\n"
"ds_write_addtid_b32 v140 offset:58880\n"
"ds_write_addtid_b32 v141 offset:59136\n"
"s_waitcnt lgkmcnt(4)\n"
"v_mfma_f32_16x16x32_bf16 v[212:215], v[202:205], v[136:139], v[212:215]\n"
"v_mfma_f32_16x16x32_bf16 v[212:215], v[204:207], v[24:27], v[212:215]\n"
"s_waitcnt lgkmcnt(2)\n"
"ds_read_b128 v[220:223], v5 offset:43008\n"
"v_mfma_f32_16x16x32_bf16 v[132:135], v[84:87], v[216:219], 0\n"
"v_sub_f32 v208, v208, v170\n"
"v_exp_f32 v208, v208\n"
"v_sub_f32 v209, v209, v170\n"
"v_exp_f32 v209, v209\n"
"v_mfma_f32_16x16x32_bf16 v[136:139], v[88:91], v[216:219], 0\n"
"ds_read_b128 v[140:143], v6 offset:5120\n"
"v_cvt_pk_bf16_f32 v208, v208, v209\n"
"v_sub_f32 v210, v210, v170\n"
"v_exp_f32 v210, v210\n"
"v_sub_f32 v211, v211, v170\n"
"v_exp_f32 v211, v211\n"
"v_mfma_f32_16x16x32_bf16 v[144:147], v[92:95], v[216:219], 0\n"
"s_waitcnt lgkmcnt(1)\n"
"ds_read_b128 v[148:151], v4 offset:45056\n"
"v_cvt_pk_bf16_f32 v209, v210, v211\n"
"v_mfma_f32_16x16x32_bf16 v[132:135], v[96:99], v[220:223], v[132:135]\n"
"ds_write_addtid_b32 v208 offset:34304\n"
"v_mfma_f32_16x16x32_bf16 v[136:139], v[100:103], v[220:223], v[136:139]\n"
"ds_write_addtid_b32 v209 offset:34560\n"
"s_waitcnt lgkmcnt(3)\n"
"v_mfma_f32_16x16x32_bf16 v[144:147], v[140:143], v[220:223], v[144:147]\n"
"ds_read_b128 v[216:219], v6 offset:6144\n"
"v_sub_f32 v212, v212, v170\n"
"v_exp_f32 v212, v212\n"
"v_sub_f32 v213, v213, v170\n"
"v_exp_f32 v213, v213\n"
"s_waitcnt lgkmcnt(3)\n"
"v_mfma_f32_16x16x32_bf16 v[132:135], v[104:107], v[148:151], v[132:135]\n"
"ds_read_b128 v[208:211], v5 offset:45056\n"
"v_cvt_pk_bf16_f32 v212, v212, v213\n"
"v_mfma_f32_16x16x32_bf16 v[136:139], v[108:111], v[148:151], v[136:139]\n"
"ds_read_b128 v[220:223], v6 offset:7168\n"
"v_sub_f32 v214, v214, v170\n"
"v_exp_f32 v214, v214\n"
"v_sub_f32 v215, v215, v170\n"
"v_exp_f32 v215, v215\n"
"s_waitcnt lgkmcnt(2)\n"
"v_mfma_f32_16x16x32_bf16 v[144:147], v[216:219], v[148:151], v[144:147]\n"
"ds_read_b128 v[140:143], v4 offset:47104\n"
"v_cvt_pk_bf16_f32 v213, v214, v215\n"
"ds_write_addtid_b32 v212 offset:57344\n"
"ds_write_addtid_b32 v213 offset:57600\n"
"s_waitcnt lgkmcnt(4)\n"
"v_mfma_f32_16x16x32_bf16 v[132:135], v[112:115], v[208:211], v[132:135]\n"
"v_mfma_f32_16x16x32_bf16 v[136:139], v[116:119], v[208:211], v[136:139]\n"
"s_waitcnt lgkmcnt(3)\n"
"ds_read_b128 v[216:219], v6 offset:8192\n"
"v_mfma_f32_16x16x32_bf16 v[144:147], v[220:223], v[208:211], v[144:147]\n"
"s_waitcnt lgkmcnt(3)\n"
"ds_read_b128 v[148:151], v5 offset:47104\n"
"v_mfma_f32_16x16x32_bf16 v[132:135], v[0:3], v[140:143], v[132:135]\n"
"s_waitcnt lgkmcnt(2)\n"
"s_barrier\n"
"v_mfma_f32_16x16x32_bf16 v[136:139], v[120:123], v[140:143], v[136:139]\n"
"s_waitcnt lgkmcnt(1)\n"
"ds_read_b128 v[212:215], v6 offset:9216\n"
"v_mfma_f32_16x16x32_bf16 v[144:147], v[216:219], v[140:143], v[144:147]\n"
"s_waitcnt lgkmcnt(1)\n"
"ds_read_b128 v[220:223], v4 offset:24576\n"
"v_mfma_f32_16x16x32_bf16 v[132:135], v[124:127], v[148:151], v[132:135]\n"
"v_mfma_f32_16x16x32_bf16 v[136:139], v[128:131], v[148:151], v[136:139]\n"
"s_waitcnt lgkmcnt(1)\n"
"v_mfma_f32_16x16x32_bf16 v[144:147], v[212:215], v[148:151], v[144:147]\n"
"s_waitcnt lgkmcnt(0)\n"
"ds_read_b128 v[152:155], v5 offset:24576\n"
"v_mfma_f32_16x16x32_bf16 v[208:211], v[8:11], v[220:223], 0\n"
"v_mfma_f32_16x16x32_bf16 v[140:143], v[12:15], v[220:223], 0\n"
"v_mfma_f32_16x16x32_bf16 v[156:159], v[16:19], v[220:223], 0\n"
"s_waitcnt lgkmcnt(0)\n"
"ds_read_b128 v[216:219], v4 offset:26624\n"
"v_mfma_f32_16x16x32_bf16 v[208:211], v[20:23], v[152:155], v[208:211]\n"
"v_mul_f32 v132, s60, v132\n"
"v_mul_f32 v133, s60, v133\n"
"v_mfma_f32_16x16x32_bf16 v[140:143], v[28:31], v[152:155], v[140:143]\n"
"v_cvt_pk_bf16_f32 v132, v132, v133\n"
"v_mul_f32 v134, s60, v134\n"
"v_mfma_f32_16x16x32_bf16 v[156:159], v[32:35], v[152:155], v[156:159]\n"
"ds_read_b128 v[212:215], v5 offset:26624\n"
"v_mul_f32 v135, s60, v135\n"
"v_cvt_pk_bf16_f32 v133, v134, v135\n"
"v_mul_f32 v136, s60, v136\n"
"v_mul_f32 v137, s60, v137\n"
"v_mul_f32 v138, s60, v138\n"
"v_mul_f32 v139, s60, v139\n"
"s_waitcnt lgkmcnt(1)\n"
"v_mfma_f32_16x16x32_bf16 v[208:211], v[36:39], v[216:219], v[208:211]\n"
"v_cvt_pk_bf16_f32 v134, v136, v137\n"
"v_cvt_pk_bf16_f32 v135, v138, v139\n"
"v_mfma_f32_16x16x32_bf16 v[140:143], v[40:43], v[216:219], v[140:143]\n"
"v_mul_f32 v144, s60, v144\n"
"v_mul_f32 v145, s60, v145\n"
"v_mfma_f32_16x16x32_bf16 v[156:159], v[44:47], v[216:219], v[156:159]\n"
"s_waitcnt lgkmcnt(0)\n"
"ds_read_b128 v[220:223], v4 offset:28672\n"
"v_mul_f32 v146, s60, v146\n"
"v_mul_f32 v147, s60, v147\n"
"v_mfma_f32_16x16x32_bf16 v[208:211], v[48:51], v[212:215], v[208:211]\n"
"v_cvt_pk_bf16_f32 v26, v144, v145\n"
"v_cvt_pk_bf16_f32 v27, v146, v147\n"
"v_mfma_f32_16x16x32_bf16 v[140:143], v[52:55], v[212:215], v[140:143]\n"
"v_mfma_f32_16x16x32_bf16 v[156:159], v[56:59], v[212:215], v[156:159]\n"
"s_waitcnt lgkmcnt(0)\n"
"ds_read_b128 v[136:139], v5 offset:28672\n"
"v_mfma_f32_16x16x32_bf16 v[208:211], v[60:63], v[220:223], v[208:211]\n"
"v_mfma_f32_16x16x32_bf16 v[140:143], v[64:67], v[220:223], v[140:143]\n"
"v_mfma_f32_16x16x32_bf16 v[156:159], v[68:71], v[220:223], v[156:159]\n"
"s_waitcnt lgkmcnt(0)\n"
"ds_read_b128 v[144:147], v4 offset:30720\n"
"v_mfma_f32_16x16x32_bf16 v[208:211], v[72:75], v[136:139], v[208:211]\n"
"v_mfma_f32_16x16x32_bf16 v[140:143], v[76:79], v[136:139], v[140:143]\n"
"v_mfma_f32_16x16x32_bf16 v[156:159], v[80:83], v[136:139], v[156:159]\n"
"s_waitcnt lgkmcnt(0)\n"
"ds_read_b128 v[148:151], v5 offset:30720\n"
"v_mfma_f32_16x16x32_bf16 v[214:217], v[8:11], v[144:147], 0\n"
"v_mfma_f32_16x16x32_bf16 v[220:223], v[12:15], v[144:147], 0\n"
"v_mfma_f32_16x16x32_bf16 v[152:155], v[16:19], v[144:147], 0\n"
"s_waitcnt lgkmcnt(0)\n"
"ds_read_b128 v[160:163], v4 offset:32768\n"
"v_mfma_f32_16x16x32_bf16 v[214:217], v[20:23], v[148:151], v[214:217]\n"
"v_cvt_pk_bf16_f32 v208, v208, v209\n"
"v_cvt_pk_bf16_f32 v209, v210, v211\n"
"v_cvt_pk_bf16_f32 v210, v140, v141\n"
"v_cvt_pk_bf16_f32 v211, v142, v143\n"
"v_cvt_pk_bf16_f32 v212, v156, v157\n"
"v_cvt_pk_bf16_f32 v213, v158, v159\n"
"v_mfma_f32_16x16x32_bf16 v[220:223], v[28:31], v[148:151], v[220:223]\n"
"v_mfma_f32_16x16x32_bf16 v[152:155], v[32:35], v[148:151], v[152:155]\n"
"s_waitcnt lgkmcnt(0)\n"
"ds_read_b128 v[136:139], v5 offset:32768\n"
"v_mfma_f32_16x16x32_bf16 v[214:217], v[36:39], v[160:163], v[214:217]\n"
"v_bfe_u32 v225, v6, 2, 6\n"
"v_mfma_f32_16x16x32_bf16 v[220:223], v[40:43], v[160:163], v[220:223]\n"
"ds_read_b32 v225, v225 offset:1472\n"
"v_mfma_f32_16x16x32_bf16 v[152:155], v[44:47], v[160:163], v[152:155]\n"
"s_add_u32 s68, s50, 4294966992\n"
"ds_read_b128 v[140:143], v4 offset:34816\n"
"s_waitcnt lgkmcnt(2)\n"
"v_mfma_f32_16x16x32_bf16 v[214:217], v[48:51], v[136:139], v[214:217]\n"
"v_mfma_f32_16x16x32_bf16 v[220:223], v[52:55], v[136:139], v[220:223]\n"
"v_mfma_f32_16x16x32_bf16 v[152:155], v[56:59], v[136:139], v[152:155]\n"
"s_waitcnt lgkmcnt(1)\n"
"ds_read_b128 v[144:147], v5 offset:34816\n"
"v_add_u32_sdwa v224, v225, v7 dst_sel:DWORD dst_unused:UNUSED_PAD src0_sel:WORD_0 src1_sel:DWORD\n"
"s_nop 0\n"
"v_min_u32 v177, s68, v224\n"
"s_waitcnt lgkmcnt(1)\n"
"v_mfma_f32_16x16x32_bf16 v[214:217], v[60:63], v[140:143], v[214:217]\n"
"s_add_u32 s73, s50, 4294965952\n"
"v_mfma_f32_16x16x32_bf16 v[220:223], v[64:67], v[140:143], v[220:223]\n"
"v_mfma_f32_16x16x32_bf16 v[152:155], v[68:71], v[140:143], v[152:155]\n"
"s_waitcnt lgkmcnt(0)\n"
"ds_read_b128 v[136:139], v177 offset:2688\n"
"v_max_u32 v176, s73, v224\n"
"v_mfma_f32_16x16x32_bf16 v[214:217], v[72:75], v[144:147], v[214:217]\n"
"v_mfma_f32_16x16x32_bf16 v[220:223], v[76:79], v[144:147], v[220:223]\n"
"ds_read_b128 v[148:151], v177 offset:2704\n"
"v_mfma_f32_16x16x32_bf16 v[152:155], v[80:83], v[144:147], v[152:155]\n"
"s_waitcnt lgkmcnt(1)\n"
"v_mfma_f32_16x16x32_bf16 v[136:139], v[178:181], v[132:135], v[136:139]\n"
"ds_read_b128 v[140:143], v176 offset:1344\n"
"v_mfma_f32_16x16x32_bf16 v[136:139], v[180:183], v[24:27], v[136:139]\n"
"s_waitcnt lgkmcnt(1)\n"
"v_mfma_f32_16x16x32_bf16 v[148:151], v[184:187], v[132:135], v[148:151]\n"
"ds_read_b128 v[156:159], v176 offset:1360\n"
"v_cvt_pk_bf16_f32 v214, v214, v215\n"
"v_cvt_pk_bf16_f32 v215, v216, v217\n"
"v_cvt_pk_bf16_f32 v216, v220, v221\n"
"v_cvt_pk_bf16_f32 v217, v222, v223\n"
"v_cvt_pk_bf16_f32 v218, v152, v153\n"
"v_cvt_pk_bf16_f32 v219, v154, v155\n"
"v_mfma_f32_16x16x32_bf16 v[148:151], v[186:189], v[24:27], v[148:151]\n"
"s_waitcnt lgkmcnt(1)\n"
"v_max3_f32 v224, v136, v137, v138\n"
"v_max_f32 v224, v224, v139\n"
"v_mfma_f32_16x16x32_bf16 v[140:143], v[226:229], v[132:135], v[140:143]\n"
"ds_read_b128 v[220:223], v176 offset:1376\n"
"v_mfma_f32_16x16x32_bf16 v[140:143], v[228:231], v[24:27], v[140:143]\n"
"s_waitcnt lgkmcnt(1)\n"
"s_nop 0\n"
"v_max3_f32 v224, v224, v148, v149\n"
"v_max3_f32 v224, v224, v150, v151\n"
"v_mfma_f32_16x16x32_bf16 v[156:159], v[232:235], v[132:135], v[156:159]\n"
"ds_read_b128 v[136:139], v177 offset:2720\n"
"v_mfma_f32_16x16x32_bf16 v[156:159], v[234:237], v[24:27], v[156:159]\n"
"s_waitcnt lgkmcnt(1)\n"
"v_max3_f32 v224, v224, v140, v141\n"
"v_max3_f32 v224, v224, v142, v143\n"
"v_mfma_f32_16x16x32_bf16 v[220:223], v[238:241], v[132:135], v[220:223]\n"
"ds_read_b128 v[144:147], v177 offset:2688\n"
"v_mfma_f32_16x16x32_bf16 v[220:223], v[240:243], v[24:27], v[220:223]\n"
"s_waitcnt lgkmcnt(1)\n"
"s_nop 0\n"
"v_max3_f32 v224, v224, v156, v157\n"
"v_max3_f32 v224, v224, v158, v159\n"
"v_mfma_f32_16x16x32_bf16 v[136:139], v[190:193], v[132:135], v[136:139]\n"
"ds_read_b128 v[140:143], v177 offset:2704\n"
"v_mfma_f32_16x16x32_bf16 v[136:139], v[192:195], v[24:27], v[136:139]\n"
"s_waitcnt lgkmcnt(1)\n"
"v_max3_f32 v224, v224, v220, v221\n"
"v_max3_f32 v224, v224, v222, v223\n"
"v_mfma_f32_16x16x32_bf16 v[144:147], v[178:181], v[132:135], v[144:147]\n"
"ds_read_b128 v[148:151], v176 offset:1344\n"
"v_mfma_f32_16x16x32_bf16 v[144:147], v[180:183], v[24:27], v[144:147]\n"
"s_nop 1\n"
"v_max3_f32 v224, v224, v136, v137\n"
"v_max3_f32 v224, v224, v138, v139\n"
"v_mov_b32 v225, v224\n"
"s_nop 1\n"
"v_permlane16_swap_b32 v224, v225\n"
"v_max_f32 v224, v224, v225\n"
"v_mov_b32 v225, v224\n"
"s_nop 1\n"
"v_permlane32_swap_b32 v224, v225\n"
"v_max_f32 v224, v224, v225\n"
"v_sub_f32 v144, v144, v224\n"
"v_exp_f32 v144, v144\n"
"v_sub_f32 v145, v145, v224\n"
"v_exp_f32 v145, v145\n"
"v_sub_f32 v146, v146, v224\n"
"v_exp_f32 v146, v146\n"
"v_sub_f32 v147, v147, v224\n"
"v_exp_f32 v147, v147\n"
"s_waitcnt lgkmcnt(1)\n"
"v_mfma_f32_16x16x32_bf16 v[140:143], v[184:187], v[132:135], v[140:143]\n"
"ds_read_b128 v[220:223], v176 offset:1360\n"
"v_cvt_pk_bf16_f32 v144, v144, v145\n"
"v_mfma_f32_16x16x32_bf16 v[140:143], v[186:189], v[24:27], v[140:143]\n"
"ds_write_addtid_b32 v144 offset:53248\n"
"v_cvt_pk_bf16_f32 v145, v146, v147\n"
"ds_write_addtid_b32 v145 offset:53504\n"
"s_waitcnt lgkmcnt(3)\n"
"v_mfma_f32_16x16x32_bf16 v[148:151], v[226:229], v[132:135], v[148:151]\n"
"ds_read_b128 v[136:139], v176 offset:1376\n"
"v_mfma_f32_16x16x32_bf16 v[148:151], v[228:231], v[24:27], v[148:151]\n"
"s_nop 0\n"
"v_sub_f32 v140, v140, v224\n"
"v_exp_f32 v140, v140\n"
"v_sub_f32 v141, v141, v224\n"
"v_exp_f32 v141, v141\n"
"v_sub_f32 v142, v142, v224\n"
"v_exp_f32 v142, v142\n"
"v_sub_f32 v143, v143, v224\n"
"v_exp_f32 v143, v143\n"
"v_sub_f32 v148, v148, v224\n"
"v_exp_f32 v148, v148\n"
"v_sub_f32 v149, v149, v224\n"
"v_exp_f32 v149, v149\n"
"v_sub_f32 v150, v150, v224\n"
"v_exp_f32 v150, v150\n"
"v_sub_f32 v151, v151, v224\n"
"v_exp_f32 v151, v151\n"
"v_cvt_pk_bf16_f32 v140, v140, v141\n"
"v_cvt_pk_bf16_f32 v148, v148, v149\n"
"v_cvt_pk_bf16_f32 v141, v142, v143\n"
"v_cvt_pk_bf16_f32 v149, v150, v151\n"
"ds_write_addtid_b32 v140 offset:53760\n"
"ds_write_addtid_b32 v148 offset:54272\n"
"ds_write_addtid_b32 v141 offset:54016\n"
"s_waitcnt lgkmcnt(6)\n"
"v_mfma_f32_16x16x32_bf16 v[220:223], v[232:235], v[132:135], v[220:223]\n"
"ds_write_addtid_b32 v149 offset:54528\n"
"v_mfma_f32_16x16x32_bf16 v[220:223], v[234:237], v[24:27], v[220:223]\n"
"s_waitcnt lgkmcnt(4)\n"
"ds_read_b128 v[144:147], v177 offset:2720\n"
"v_mfma_f32_16x16x32_bf16 v[136:139], v[238:241], v[132:135], v[136:139]\n"
"ds_read_b128 v[152:155], v4 offset:55296\n"
"v_mfma_f32_16x16x32_bf16 v[136:139], v[240:243], v[24:27], v[136:139]\n"
"s_waitcnt lgkmcnt(2)\n"
"s_barrier\n"
"s_nop 0\n"
"v_sub_f32 v220, v220, v224\n"
"v_exp_f32 v220, v220\n"
"v_sub_f32 v221, v221, v224\n"
"v_exp_f32 v221, v221\n"
"v_sub_f32 v222, v222, v224\n"
"v_exp_f32 v222, v222\n"
"v_sub_f32 v223, v223, v224\n"
"v_exp_f32 v223, v223\n"
"v_cvt_pk_bf16_f32 v220, v220, v221\n"
"v_cvt_pk_bf16_f32 v221, v222, v223\n"
"v_sub_f32 v136, v136, v224\n"
"v_exp_f32 v136, v136\n"
"v_sub_f32 v137, v137, v224\n"
"v_exp_f32 v137, v137\n"
"v_sub_f32 v138, v138, v224\n"
"v_exp_f32 v138, v138\n"
"v_sub_f32 v139, v139, v224\n"
"v_exp_f32 v139, v139\n"
"v_cvt_pk_bf16_f32 v136, v136, v137\n"
"v_cvt_pk_bf16_f32 v137, v138, v139\n"
"ds_write_addtid_b32 v220 offset:54784\n"
"ds_write_addtid_b32 v221 offset:55040\n"
"s_waitcnt lgkmcnt(3)\n"
"v_mfma_f32_16x16x32_bf16 v[144:147], v[190:193], v[132:135], v[144:147]\n"
"ds_write_addtid_b32 v136 offset:57856\n"
"v_mfma_f32_16x16x32_bf16 v[144:147], v[192:195], v[24:27], v[144:147]\n"
"s_waitcnt lgkmcnt(3)\n"
"ds_read_b128 v[140:143], v5 offset:55296\n"
"v_mfma_f32_16x16x32_bf16 v[148:151], v[84:87], v[152:155], 0\n"
"ds_write_addtid_b32 v137 offset:58112\n"
"v_mfma_f32_16x16x32_bf16 v[156:159], v[88:91], v[152:155], 0\n"
"ds_read_b128 v[220:223], v6 offset:5120\n"
"v_mfma_f32_16x16x32_bf16 v[132:135], v[92:95], v[152:155], 0\n"
"s_waitcnt lgkmcnt(2)\n"
"ds_read_b128 v[160:163], v4 offset:57344\n"
"v_mfma_f32_16x16x32_bf16 v[148:151], v[96:99], v[140:143], v[148:151]\n"
"v_sub_f32 v144, v144, v224\n"
"v_exp_f32 v144, v144\n"
"v_mfma_f32_16x16x32_bf16 v[156:159], v[100:103], v[140:143], v[156:159]\n"
"s_waitcnt lgkmcnt(1)\n"
"ds_read_b128 v[136:139], v6 offset:6144\n"
"v_mfma_f32_16x16x32_bf16 v[132:135], v[220:223], v[140:143], v[132:135]\n"
"s_waitcnt lgkmcnt(1)\n"
"ds_read_b128 v[152:155], v5 offset:57344\n"
"v_mfma_f32_16x16x32_bf16 v[148:151], v[104:107], v[160:163], v[148:151]\n"
"v_sub_f32 v145, v145, v224\n"
"v_exp_f32 v145, v145\n"
"v_mfma_f32_16x16x32_bf16 v[156:159], v[108:111], v[160:163], v[156:159]\n"
"ds_read_b128 v[164:167], v6 offset:7168\n"
"v_sub_f32 v146, v146, v224\n"
"v_exp_f32 v146, v146\n"
"v_sub_f32 v147, v147, v224\n"
"v_exp_f32 v147, v147\n"
"s_waitcnt lgkmcnt(2)\n"
"v_mfma_f32_16x16x32_bf16 v[132:135], v[136:139], v[160:163], v[132:135]\n"
"s_waitcnt lgkmcnt(1)\n"
"ds_read_b128 v[140:143], v4 offset:59392\n"
"v_cvt_pk_bf16_f32 v144, v144, v145\n"
"v_cvt_pk_bf16_f32 v145, v146, v147\n"
"v_mfma_f32_16x16x32_bf16 v[148:151], v[112:115], v[152:155], v[148:151]\n"
"ds_write_addtid_b32 v144 offset:57344\n"
"v_mfma_f32_16x16x32_bf16 v[156:159], v[116:119], v[152:155], v[156:159]\n"
"ds_write_addtid_b32 v145 offset:57600\n"
"s_waitcnt lgkmcnt(3)\n"
"v_mfma_f32_16x16x32_bf16 v[132:135], v[164:167], v[152:155], v[132:135]\n"
"s_waitcnt lgkmcnt(2)\n"
"ds_read_b128 v[136:139], v6 offset:8192\n"
"ds_read_b128 v[160:163], v5 offset:59392\n"
"v_mfma_f32_16x16x32_bf16 v[148:151], v[0:3], v[140:143], v[148:151]\n"
"v_mfma_f32_16x16x32_bf16 v[156:159], v[120:123], v[140:143], v[156:159]\n"
"s_waitcnt lgkmcnt(2)\n"
"s_barrier\n"
"ds_read_b128 v[144:147], v6 offset:9216\n"
"s_waitcnt lgkmcnt(2)\n"
"v_mfma_f32_16x16x32_bf16 v[132:135], v[136:139], v[140:143], v[132:135]\n"
"s_waitcnt lgkmcnt(1)\n"
"v_mfma_f32_16x16x32_bf16 v[148:151], v[124:127], v[160:163], v[148:151]\n"
"ds_read_b128 v[152:155], v4 offset:49152\n"
"v_mfma_f32_16x16x32_bf16 v[156:159], v[128:131], v[160:163], v[156:159]\n"
"s_waitcnt lgkmcnt(1)\n"
"v_mfma_f32_16x16x32_bf16 v[132:135], v[144:147], v[160:163], v[132:135]\n"
"s_waitcnt lgkmcnt(0)\n"
"v_mfma_f32_16x16x32_bf16 v[220:223], v[8:11], v[152:155], 0\n"
"ds_read_b128 v[164:167], v5 offset:49152\n"
"v_mfma_f32_16x16x32_bf16 v[136:139], v[12:15], v[152:155], 0\n"
"v_mfma_f32_16x16x32_bf16 v[140:143], v[16:19], v[152:155], 0\n"
"s_waitcnt lgkmcnt(0)\n"
"v_mfma_f32_16x16x32_bf16 v[220:223], v[20:23], v[164:167], v[220:223]\n"
"ds_read_b128 v[168:171], v4 offset:51200\n"
"v_mul_f32 v148, s60, v148\n"
"v_mfma_f32_16x16x32_bf16 v[136:139], v[28:31], v[164:167], v[136:139]\n"
"v_mul_f32 v149, s60, v149\n"
"v_cvt_pk_bf16_f32 v148, v148, v149\n"
"v_mfma_f32_16x16x32_bf16 v[140:143], v[32:35], v[164:167], v[140:143]\n"
"v_mul_f32 v150, s60, v150\n"
"v_mul_f32 v151, s60, v151\n"
"v_cvt_pk_bf16_f32 v149, v150, v151\n"
"v_mul_f32 v156, s60, v156\n"
"v_mul_f32 v157, s60, v157\n"
"v_mul_f32 v158, s60, v158\n"
"v_mul_f32 v159, s60, v159\n"
"v_cvt_pk_bf16_f32 v150, v156, v157\n"
"v_cvt_pk_bf16_f32 v151, v158, v159\n"
"v_mul_f32 v132, s60, v132\n"
"v_mul_f32 v133, s60, v133\n"
"v_mul_f32 v134, s60, v134\n"
"s_waitcnt lgkmcnt(0)\n"
"v_mfma_f32_16x16x32_bf16 v[220:223], v[36:39], v[168:171], v[220:223]\n"
"ds_read_b128 v[144:147], v5 offset:51200\n"
"v_mul_f32 v135, s60, v135\n"
"v_mfma_f32_16x16x32_bf16 v[136:139], v[40:43], v[168:171], v[136:139]\n"
"v_cvt_pk_bf16_f32 v26, v132, v133\n"
"v_cvt_pk_bf16_f32 v27, v134, v135\n"
"v_mfma_f32_16x16x32_bf16 v[140:143], v[44:47], v[168:171], v[140:143]\n"
"s_waitcnt lgkmcnt(0)\n"
"v_bfe_u32 v225, v6, 2, 6\n"
"ds_read_b32 v225, v225 offset:1536\n"
"v_mfma_f32_16x16x32_bf16 v[220:223], v[48:51], v[144:147], v[220:223]\n"
"ds_read_b128 v[152:155], v4 offset:53248\n"
"v_mfma_f32_16x16x32_bf16 v[136:139], v[52:55], v[144:147], v[136:139]\n"
"s_add_u32 s70, s50, 4294964608\n"
"v_mfma_f32_16x16x32_bf16 v[140:143], v[56:59], v[144:147], v[140:143]\n"
"s_waitcnt lgkmcnt(1)\n"
"v_add_u32_sdwa v177, v225, v7 dst_sel:DWORD dst_unused:UNUSED_PAD src0_sel:WORD_0 src1_sel:DWORD\n"
"s_waitcnt lgkmcnt(0)\n"
"v_mfma_f32_16x16x32_bf16 v[220:223], v[60:63], v[152:155], v[220:223]\n"
"ds_read_b128 v[132:135], v5 offset:53248\n"
"v_max_u32 v176, s70, v177\n"
"v_mfma_f32_16x16x32_bf16 v[136:139], v[64:67], v[152:155], v[136:139]\n"
"v_mfma_f32_16x16x32_bf16 v[140:143], v[68:71], v[152:155], v[140:143]\n"
"s_add_u32 s71, s50, 4294965648\n"
"s_waitcnt lgkmcnt(0)\n"
"v_mfma_f32_16x16x32_bf16 v[220:223], v[72:75], v[132:135], v[220:223]\n"
"ds_read_b128 v[144:147], v176 offset:2688\n"
"v_mfma_f32_16x16x32_bf16 v[136:139], v[76:79], v[132:135], v[136:139]\n"
"v_min_u32 v175, s71, v177\n"
"v_mfma_f32_16x16x32_bf16 v[140:143], v[80:83], v[132:135], v[140:143]\n"
"s_waitcnt lgkmcnt(0)\n"
"ds_read_b128 v[152:155], v176 offset:2704\n"
"v_mfma_f32_16x16x32_bf16 v[144:147], v[178:181], v[148:151], v[144:147]\n"
"v_mfma_f32_16x16x32_bf16 v[144:147], v[180:183], v[24:27], v[144:147]\n"
"ds_read_b128 v[156:159], v175 offset:4032\n"
"v_cvt_pk_bf16_f32 v220, v220, v221\n"
"v_cvt_pk_bf16_f32 v221, v222, v223\n"
"v_cvt_pk_bf16_f32 v222, v136, v137\n"
"v_cvt_pk_bf16_f32 v223, v138, v139\n"
"v_cvt_pk_bf16_f32 v224, v140, v141\n"
"v_cvt_pk_bf16_f32 v225, v142, v143\n"
"s_waitcnt lgkmcnt(1)\n"
"v_mfma_f32_16x16x32_bf16 v[152:155], v[184:187], v[148:151], v[152:155]\n"
"s_add_u32 s69, s50, 4294965952\n"
"v_max3_f32 v172, v144, v145, v146\n"
"v_max_f32 v172, v172, v147\n"
"v_mfma_f32_16x16x32_bf16 v[152:155], v[186:189], v[24:27], v[152:155]\n"
"s_waitcnt lgkmcnt(0)\n"
"ds_read_b128 v[132:135], v175 offset:4048\n"
"v_mfma_f32_16x16x32_bf16 v[156:159], v[208:211], v[148:151], v[156:159]\n"
"v_max_u32 v174, s69, v177\n"
"v_mfma_f32_16x16x32_bf16 v[156:159], v[210:213], v[24:27], v[156:159]\n"
"s_waitcnt lgkmcnt(0)\n"
"ds_read_b128 v[136:139], v176 offset:2720\n"
"s_nop 0\n"
"v_max3_f32 v172, v172, v152, v153\n"
"v_max3_f32 v172, v172, v154, v155\n"
"v_mfma_f32_16x16x32_bf16 v[132:135], v[214:217], v[148:151], v[132:135]\n"
"v_mfma_f32_16x16x32_bf16 v[132:135], v[216:219], v[24:27], v[132:135]\n"
"s_waitcnt lgkmcnt(0)\n"
"ds_read_b128 v[140:143], v174 offset:1344\n"
"v_max3_f32 v172, v172, v156, v157\n"
"v_max3_f32 v172, v172, v158, v159\n"
"v_mfma_f32_16x16x32_bf16 v[136:139], v[190:193], v[148:151], v[136:139]\n"
"v_mfma_f32_16x16x32_bf16 v[136:139], v[192:195], v[24:27], v[136:139]\n"
"s_waitcnt lgkmcnt(0)\n"
"ds_read_b128 v[144:147], v174 offset:1360\n"
"v_max3_f32 v172, v172, v132, v133\n"
"v_max3_f32 v172, v172, v134, v135\n"
"v_mfma_f32_16x16x32_bf16 v[140:143], v[226:229], v[148:151], v[140:143]\n"
"v_mfma_f32_16x16x32_bf16 v[140:143], v[228:231], v[24:27], v[140:143]\n"
"s_waitcnt lgkmcnt(0)\n"
"ds_read_b128 v[152:155], v174 offset:1376\n"
"v_max3_f32 v172, v172, v136, v137\n"
"v_max3_f32 v172, v172, v138, v139\n"
"v_mfma_f32_16x16x32_bf16 v[144:147], v[232:235], v[148:151], v[144:147]\n"
"v_mfma_f32_16x16x32_bf16 v[144:147], v[234:237], v[24:27], v[144:147]\n"
"s_waitcnt lgkmcnt(0)\n"
"ds_read_b128 v[132:135], v175 offset:4064\n"
"v_max3_f32 v172, v172, v140, v141\n"
"v_max3_f32 v172, v172, v142, v143\n"
"v_mfma_f32_16x16x32_bf16 v[152:155], v[238:241], v[148:151], v[152:155]\n"
"v_mfma_f32_16x16x32_bf16 v[152:155], v[240:243], v[24:27], v[152:155]\n"
"s_waitcnt lgkmcnt(0)\n"
"ds_read_b128 v[136:139], v176 offset:2688\n"
"v_max3_f32 v172, v172, v144, v145\n"
"v_max3_f32 v172, v172, v146, v147\n"
"v_mfma_f32_16x16x32_bf16 v[132:135], v[220:223], v[148:151], v[132:135]\n"
"v_mfma_f32_16x16x32_bf16 v[132:135], v[222:225], v[24:27], v[132:135]\n"
"s_waitcnt lgkmcnt(0)\n"
"ds_read_b128 v[140:143], v176 offset:2704\n"
"v_max3_f32 v172, v172, v152, v153\n"
"v_max3_f32 v172, v172, v154, v155\n"
"v_mfma_f32_16x16x32_bf16 v[136:139], v[178:181], v[148:151], v[136:139]\n"
"v_mfma_f32_16x16x32_bf16 v[136:139], v[180:183], v[24:27], v[136:139]\n"
"ds_read_b128 v[144:147], v175 offset:4032\n"
"s_nop 0\n"
"v_max3_f32 v172, v172, v132, v133\n"
"v_max3_f32 v172, v172, v134, v135\n"
"v_mov_b32 v173, v172\n"
"s_nop 1\n"
"v_permlane16_swap_b32 v172, v173\n"
"v_max_f32 v172, v172, v173\n"
"v_mov_b32 v173, v172\n"
"s_nop 1\n"
"v_permlane32_swap_b32 v172, v173\n"
"v_max_f32 v172, v172, v173\n"
"v_sub_f32 v136, v136, v172\n"
"v_exp_f32 v136, v136\n"
"v_sub_f32 v137, v137, v172\n"
"v_exp_f32 v137, v137\n"
"v_sub_f32 v138, v138, v172\n"
"v_exp_f32 v138, v138\n"
"v_sub_f32 v139, v139, v172\n"
"v_exp_f32 v139, v139\n"
"s_waitcnt lgkmcnt(1)\n"
"v_mfma_f32_16x16x32_bf16 v[140:143], v[184:187], v[148:151], v[140:143]\n"
"v_cvt_pk_bf16_f32 v136, v136, v137\n"
"v_cvt_pk_bf16_f32 v137, v138, v139\n"
"v_mfma_f32_16x16x32_bf16 v[140:143], v[186:189], v[24:27], v[140:143]\n"
"ds_read_b128 v[132:135], v175 offset:4048\n"
"ds_write_addtid_b32 v136 offset:55296\n"
"ds_write_addtid_b32 v137 offset:55552\n"
"s_waitcnt lgkmcnt(3)\n"
"v_mfma_f32_16x16x32_bf16 v[144:147], v[208:211], v[148:151], v[144:147]\n"
"v_mfma_f32_16x16x32_bf16 v[144:147], v[210:213], v[24:27], v[144:147]\n"
"ds_read_b128 v[152:155], v176 offset:2720\n"
"s_nop 0\n"
"v_sub_f32 v140, v140, v172\n"
"v_exp_f32 v140, v140\n"
"v_sub_f32 v141, v141, v172\n"
"v_exp_f32 v141, v141\n"
"v_sub_f32 v142, v142, v172\n"
"v_exp_f32 v142, v142\n"
"v_sub_f32 v143, v143, v172\n"
"v_exp_f32 v143, v143\n"
"v_sub_f32 v144, v144, v172\n"
"v_exp_f32 v144, v144\n"
"v_sub_f32 v145, v145, v172\n"
"v_exp_f32 v145, v145\n"
"v_cvt_pk_bf16_f32 v140, v140, v141\n"
"v_sub_f32 v146, v146, v172\n"
"v_exp_f32 v146, v146\n"
"v_cvt_pk_bf16_f32 v141, v142, v143\n"
"v_sub_f32 v147, v147, v172\n"
"v_exp_f32 v147, v147\n"
"v_cvt_pk_bf16_f32 v144, v144, v145\n"
"v_cvt_pk_bf16_f32 v145, v146, v147\n"
"ds_write_addtid_b32 v140 offset:55808\n"
"ds_write_addtid_b32 v141 offset:56064\n"
"s_waitcnt lgkmcnt(5)\n"
"v_mfma_f32_16x16x32_bf16 v[132:135], v[214:217], v[148:151], v[132:135]\n"
"ds_write_addtid_b32 v144 offset:56320\n"
"v_mfma_f32_16x16x32_bf16 v[132:135], v[216:219], v[24:27], v[132:135]\n"
"ds_read_b128 v[136:139], v174 offset:1344\n"
"ds_write_addtid_b32 v145 offset:56576\n"
"s_waitcnt lgkmcnt(5)\n"
"v_mfma_f32_16x16x32_bf16 v[152:155], v[190:193], v[148:151], v[152:155]\n"
"v_mfma_f32_16x16x32_bf16 v[152:155], v[192:195], v[24:27], v[152:155]\n"
"ds_read_b128 v[140:143], v174 offset:1360\n"
"s_nop 1\n"
"v_sub_f32 v132, v132, v172\n"
"v_exp_f32 v132, v132\n"
"v_sub_f32 v133, v133, v172\n"
"v_exp_f32 v133, v133\n"
"v_sub_f32 v134, v134, v172\n"
"v_exp_f32 v134, v134\n"
"v_sub_f32 v135, v135, v172\n"
"v_exp_f32 v135, v135\n"
"v_sub_f32 v152, v152, v172\n"
"v_exp_f32 v152, v152\n"
"v_cvt_pk_bf16_f32 v132, v132, v133\n"
"v_cvt_pk_bf16_f32 v133, v134, v135\n"
"v_sub_f32 v153, v153, v172\n"
"v_exp_f32 v153, v153\n"
"ds_write_addtid_b32 v132 offset:56832\n"
"v_cvt_pk_bf16_f32 v152, v152, v153\n"
"ds_write_addtid_b32 v133 offset:57088\n"
"s_waitcnt lgkmcnt(4)\n"
"v_mfma_f32_16x16x32_bf16 v[136:139], v[226:229], v[148:151], v[136:139]\n"
"ds_write_addtid_b32 v152 offset:34304\n"
"v_mfma_f32_16x16x32_bf16 v[136:139], v[228:231], v[24:27], v[136:139]\n"
"s_waitcnt lgkmcnt(3)\n"
"ds_read_b128 v[144:147], v174 offset:1376\n"
"v_mfma_f32_16x16x32_bf16 v[140:143], v[232:235], v[148:151], v[140:143]\n"
"v_mfma_f32_16x16x32_bf16 v[140:143], v[234:237], v[24:27], v[140:143]\n"
"s_waitcnt lgkmcnt(1)\n"
"ds_read_b128 v[132:135], v175 offset:4064\n"
"s_barrier\n"
"s_nop 0\n"
"v_sub_f32 v136, v136, v172\n"
"v_exp_f32 v136, v136\n"
"v_sub_f32 v137, v137, v172\n"
"v_exp_f32 v137, v137\n"
"v_sub_f32 v138, v138, v172\n"
"v_exp_f32 v138, v138\n"
"v_sub_f32 v139, v139, v172\n"
"v_exp_f32 v139, v139\n"
"v_sub_f32 v140, v140, v172\n"
"v_exp_f32 v140, v140\n"
"v_cvt_pk_bf16_f32 v136, v136, v137\n"
"v_cvt_pk_bf16_f32 v137, v138, v139\n"
"v_sub_f32 v141, v141, v172\n"
"v_exp_f32 v141, v141\n"
"v_sub_f32 v142, v142, v172\n"
"v_exp_f32 v142, v142\n"
"v_sub_f32 v143, v143, v172\n"
"v_exp_f32 v143, v143\n"
"v_cvt_pk_bf16_f32 v140, v140, v141\n"
"v_cvt_pk_bf16_f32 v141, v142, v143\n"
"ds_write_addtid_b32 v136 offset:57856\n"
"ds_write_addtid_b32 v137 offset:58112\n"
"s_waitcnt lgkmcnt(3)\n"
"v_mfma_f32_16x16x32_bf16 v[144:147], v[238:241], v[148:151], v[144:147]\n"
"ds_write_addtid_b32 v140 offset:58368\n"
"v_mfma_f32_16x16x32_bf16 v[144:147], v[240:243], v[24:27], v[144:147]\n"
"ds_read_b128 v[226:229], v4 offset:18432\n"
"ds_write_addtid_b32 v141 offset:58624\n"
"s_waitcnt lgkmcnt(5)\n"
"v_mfma_f32_16x16x32_bf16 v[132:135], v[220:223], v[148:151], v[132:135]\n"
"v_mfma_f32_16x16x32_bf16 v[132:135], v[222:225], v[24:27], v[132:135]\n"
"s_waitcnt lgkmcnt(1)\n"
"v_mfma_f32_16x16x32_bf16 v[136:139], v[84:87], v[226:229], 0\n"
"ds_read_b128 v[152:155], v5 offset:18432\n"
"v_mfma_f32_16x16x32_bf16 v[156:159], v[88:91], v[226:229], 0\n"
"v_mfma_f32_16x16x32_bf16 v[160:163], v[92:95], v[226:229], 0\n"
"ds_read_b128 v[140:143], v6 offset:5120\n"
"v_sub_f32 v144, v144, v172\n"
"v_exp_f32 v144, v144\n"
"v_sub_f32 v145, v145, v172\n"
"v_exp_f32 v145, v145\n"
"v_sub_f32 v132, v132, v172\n"
"v_exp_f32 v132, v132\n"
"v_cvt_pk_bf16_f32 v144, v144, v145\n"
"s_waitcnt lgkmcnt(1)\n"
"v_mfma_f32_16x16x32_bf16 v[136:139], v[96:99], v[152:155], v[136:139]\n"
"ds_read_b128 v[226:229], v4 offset:20480\n"
"v_mfma_f32_16x16x32_bf16 v[156:159], v[100:103], v[152:155], v[156:159]\n"
"ds_write_addtid_b32 v144 offset:58880\n"
"s_waitcnt lgkmcnt(2)\n"
"v_mfma_f32_16x16x32_bf16 v[160:163], v[140:143], v[152:155], v[160:163]\n"
"s_waitcnt lgkmcnt(1)\n"
"ds_read_b128 v[148:151], v6 offset:6144\n"
"v_sub_f32 v133, v133, v172\n"
"v_exp_f32 v133, v133\n"
"v_mfma_f32_16x16x32_bf16 v[136:139], v[104:107], v[226:229], v[136:139]\n"
"ds_read_b128 v[164:167], v5 offset:20480\n"
"v_cvt_pk_bf16_f32 v132, v132, v133\n"
"v_mfma_f32_16x16x32_bf16 v[156:159], v[108:111], v[226:229], v[156:159]\n"
"ds_write_addtid_b32 v132 offset:57344\n"
"s_waitcnt lgkmcnt(2)\n"
"v_mfma_f32_16x16x32_bf16 v[160:163], v[148:151], v[226:229], v[160:163]\n"
"s_waitcnt lgkmcnt(1)\n"
"ds_read_b128 v[140:143], v6 offset:7168\n"
"v_mfma_f32_16x16x32_bf16 v[136:139], v[112:115], v[164:167], v[136:139]\n"
"s_waitcnt lgkmcnt(1)\n"
"s_barrier\n"
"ds_read_b128 v[144:147], v4 offset:22528\n"
"v_mfma_f32_16x16x32_bf16 v[156:159], v[116:119], v[164:167], v[156:159]\n"
"s_waitcnt lgkmcnt(1)\n"
"v_mfma_f32_16x16x32_bf16 v[160:163], v[140:143], v[164:167], v[160:163]\n"
"s_waitcnt lgkmcnt(0)\n"
"ds_read_b128 v[132:135], v6 offset:8192\n"
"v_mfma_f32_16x16x32_bf16 v[136:139], v[0:3], v[144:147], v[136:139]\n"
"ds_read_b128 v[148:151], v5 offset:22528\n"
"v_mfma_f32_16x16x32_bf16 v[156:159], v[120:123], v[144:147], v[156:159]\n"
"s_waitcnt lgkmcnt(1)\n"
"v_mfma_f32_16x16x32_bf16 v[160:163], v[132:135], v[144:147], v[160:163]\n"
"s_waitcnt lgkmcnt(0)\n"
"ds_read_b128 v[152:155], v6 offset:9216\n"
"v_mfma_f32_16x16x32_bf16 v[136:139], v[124:127], v[148:151], v[136:139]\n"
"ds_read_b128 v[140:143], v4 offset:12288\n"
"v_mfma_f32_16x16x32_bf16 v[156:159], v[128:131], v[148:151], v[156:159]\n"
"s_waitcnt lgkmcnt(1)\n"
"v_mfma_f32_16x16x32_bf16 v[160:163], v[152:155], v[148:151], v[160:163]\n"
"s_waitcnt lgkmcnt(0)\n"
"v_mfma_f32_16x16x32_bf16 v[226:229], v[8:11], v[140:143], 0\n"
"ds_read_b128 v[132:135], v5 offset:12288\n"
"v_mfma_f32_16x16x32_bf16 v[144:147], v[12:15], v[140:143], 0\n"
"v_mfma_f32_16x16x32_bf16 v[164:167], v[16:19], v[140:143], 0\n"
"s_waitcnt lgkmcnt(0)\n"
"v_mfma_f32_16x16x32_bf16 v[226:229], v[20:23], v[132:135], v[226:229]\n"
"ds_read_b128 v[168:171], v4 offset:14336\n"
"v_mul_f32 v136, s60, v136\n"
"v_mfma_f32_16x16x32_bf16 v[144:147], v[28:31], v[132:135], v[144:147]\n"
"v_mul_f32 v137, s60, v137\n"
"v_cvt_pk_bf16_f32 v136, v136, v137\n"
"v_mfma_f32_16x16x32_bf16 v[164:167], v[32:35], v[132:135], v[164:167]\n"
"v_mul_f32 v138, s60, v138\n"
"v_mul_f32 v139, s60, v139\n"
"v_cvt_pk_bf16_f32 v137, v138, v139\n"
"v_mul_f32 v156, s60, v156\n"
"v_mul_f32 v157, s60, v157\n"
"v_mul_f32 v158, s60, v158\n"
"v_mul_f32 v159, s60, v159\n"
"v_cvt_pk_bf16_f32 v138, v156, v157\n"
"v_cvt_pk_bf16_f32 v139, v158, v159\n"
"v_mul_f32 v160, s60, v160\n"
"v_mul_f32 v161, s60, v161\n"
"v_mul_f32 v162, s60, v162\n"
"s_waitcnt lgkmcnt(0)\n"
"v_mfma_f32_16x16x32_bf16 v[226:229], v[36:39], v[168:171], v[226:229]\n"
"ds_read_b128 v[132:135], v5 offset:14336\n"
"v_mul_f32 v163, s60, v163\n"
"v_mfma_f32_16x16x32_bf16 v[144:147], v[40:43], v[168:171], v[144:147]\n"
"v_cvt_pk_bf16_f32 v26, v160, v161\n"
"v_cvt_pk_bf16_f32 v27, v162, v163\n"
"v_mfma_f32_16x16x32_bf16 v[164:167], v[44:47], v[168:171], v[164:167]\n"
"s_waitcnt lgkmcnt(0)\n"
"v_bfe_u32 v231, v6, 2, 6\n"
"ds_read_b32 v231, v231 offset:1600\n"
"v_mfma_f32_16x16x32_bf16 v[226:229], v[48:51], v[132:135], v[226:229]\n"
"ds_read_b128 v[140:143], v4 offset:16384\n"
"v_mfma_f32_16x16x32_bf16 v[144:147], v[52:55], v[132:135], v[144:147]\n"
"s_add_u32 s73, s50, 4294966976\n"
"v_mfma_f32_16x16x32_bf16 v[164:167], v[56:59], v[132:135], v[164:167]\n"
"s_waitcnt lgkmcnt(1)\n"
"v_add_u32_sdwa v177, v231, v7 dst_sel:DWORD dst_unused:UNUSED_PAD src0_sel:WORD_0 src1_sel:DWORD\n"
"s_waitcnt lgkmcnt(0)\n"
"v_mfma_f32_16x16x32_bf16 v[226:229], v[60:63], v[140:143], v[226:229]\n"
"ds_read_b128 v[148:151], v5 offset:16384\n"
"v_min_u32 v176, s73, v177\n"
"v_mfma_f32_16x16x32_bf16 v[144:147], v[64:67], v[140:143], v[144:147]\n"
"v_mfma_f32_16x16x32_bf16 v[164:167], v[68:71], v[140:143], v[164:167]\n"
"s_add_u32 s68, s50, 4294965632\n"
"s_waitcnt lgkmcnt(0)\n"
"v_mfma_f32_16x16x32_bf16 v[226:229], v[72:75], v[148:151], v[226:229]\n"
"ds_read_b128 v[132:135], v176 offset:2704\n"
"v_mfma_f32_16x16x32_bf16 v[144:147], v[76:79], v[148:151], v[144:147]\n"
"v_min_u32 v175, s68, v177\n"
"v_mfma_f32_16x16x32_bf16 v[164:167], v[80:83], v[148:151], v[164:167]\n"
"s_waitcnt lgkmcnt(0)\n"
"ds_read_b128 v[140:143], v176 offset:2720\n"
"v_mfma_f32_16x16x32_bf16 v[132:135], v[184:187], v[136:139], v[132:135]\n"
"v_mfma_f32_16x16x32_bf16 v[132:135], v[186:189], v[24:27], v[132:135]\n"
"ds_read_b128 v[152:155], v175 offset:4048\n"
"v_cvt_pk_bf16_f32 v226, v226, v227\n"
"v_cvt_pk_bf16_f32 v227, v228, v229\n"
"v_cvt_pk_bf16_f32 v228, v144, v145\n"
"v_cvt_pk_bf16_f32 v229, v146, v147\n"
"v_cvt_pk_bf16_f32 v230, v164, v165\n"
"v_cvt_pk_bf16_f32 v231, v166, v167\n"
"s_waitcnt lgkmcnt(1)\n"
"v_mfma_f32_16x16x32_bf16 v[140:143], v[190:193], v[136:139], v[140:143]\n"
"s_add_u32 s72, s50, 4294965936\n"
"v_max3_f32 v172, v132, v133, v134\n"
"v_max_f32 v172, v172, v135\n"
"v_mfma_f32_16x16x32_bf16 v[140:143], v[192:195], v[24:27], v[140:143]\n"
"s_waitcnt lgkmcnt(0)\n"
"ds_read_b128 v[144:147], v175 offset:4064\n"
"v_mfma_f32_16x16x32_bf16 v[152:155], v[214:217], v[136:139], v[152:155]\n"
"v_max_u32 v174, s72, v177\n"
"v_mfma_f32_16x16x32_bf16 v[152:155], v[216:219], v[24:27], v[152:155]\n"
"s_waitcnt lgkmcnt(0)\n"
"ds_read_b128 v[148:151], v176 offset:2736\n"
"s_nop 0\n"
"v_max3_f32 v172, v172, v140, v141\n"
"v_max3_f32 v172, v172, v142, v143\n"
"v_mfma_f32_16x16x32_bf16 v[144:147], v[220:223], v[136:139], v[144:147]\n"
"v_mfma_f32_16x16x32_bf16 v[144:147], v[222:225], v[24:27], v[144:147]\n"
"s_waitcnt lgkmcnt(0)\n"
"ds_read_b128 v[132:135], v174 offset:1360\n"
"v_max3_f32 v172, v172, v152, v153\n"
"v_max3_f32 v172, v172, v154, v155\n"
"v_mfma_f32_16x16x32_bf16 v[148:151], v[196:199], v[136:139], v[148:151]\n"
"v_mfma_f32_16x16x32_bf16 v[148:151], v[198:201], v[24:27], v[148:151]\n"
"s_waitcnt lgkmcnt(0)\n"
"ds_read_b128 v[140:143], v174 offset:1376\n"
"v_max3_f32 v172, v172, v144, v145\n"
"v_max3_f32 v172, v172, v146, v147\n"
"v_mfma_f32_16x16x32_bf16 v[132:135], v[232:235], v[136:139], v[132:135]\n"
"v_mfma_f32_16x16x32_bf16 v[132:135], v[234:237], v[24:27], v[132:135]\n"
"s_waitcnt lgkmcnt(0)\n"
"ds_read_b128 v[152:155], v174 offset:1392\n"
"v_max3_f32 v172, v172, v148, v149\n"
"v_max3_f32 v172, v172, v150, v151\n"
"v_mfma_f32_16x16x32_bf16 v[140:143], v[238:241], v[136:139], v[140:143]\n"
"v_mfma_f32_16x16x32_bf16 v[140:143], v[240:243], v[24:27], v[140:143]\n"
"s_waitcnt lgkmcnt(0)\n"
"ds_read_b128 v[144:147], v175 offset:4080\n"
"v_max3_f32 v172, v172, v132, v133\n"
"v_max3_f32 v172, v172, v134, v135\n"
"v_mfma_f32_16x16x32_bf16 v[152:155], v[244:247], v[136:139], v[152:155]\n"
"v_mfma_f32_16x16x32_bf16 v[152:155], v[246:249], v[24:27], v[152:155]\n"
"s_waitcnt lgkmcnt(0)\n"
"ds_read_b128 v[148:151], v176 offset:2704\n"
"v_max3_f32 v172, v172, v140, v141\n"
"v_max3_f32 v172, v172, v142, v143\n"
"v_mfma_f32_16x16x32_bf16 v[144:147], v[226:229], v[136:139], v[144:147]\n"
"v_mfma_f32_16x16x32_bf16 v[144:147], v[228:231], v[24:27], v[144:147]\n"
"s_waitcnt lgkmcnt(0)\n"
"ds_read_b128 v[132:135], v176 offset:2720\n"
"v_max3_f32 v172, v172, v152, v153\n"
"v_max3_f32 v172, v172, v154, v155\n"
"v_mfma_f32_16x16x32_bf16 v[148:151], v[184:187], v[136:139], v[148:151]\n"
"v_mfma_f32_16x16x32_bf16 v[148:151], v[186:189], v[24:27], v[148:151]\n"
"ds_read_b128 v[140:143], v175 offset:4048\n"
"s_nop 0\n"
"v_max3_f32 v172, v172, v144, v145\n"
"v_max3_f32 v172, v172, v146, v147\n"
"v_mov_b32 v173, v172\n"
"s_nop 1\n"
"v_permlane16_swap_b32 v172, v173\n"
"v_max_f32 v172, v172, v173\n"
"v_mov_b32 v173, v172\n"
"s_nop 1\n"
"v_permlane32_swap_b32 v172, v173\n"
"v_max_f32 v172, v172, v173\n"
"v_sub_f32 v148, v148, v172\n"
"v_exp_f32 v148, v148\n"
"v_sub_f32 v149, v149, v172\n"
"v_exp_f32 v149, v149\n"
"v_sub_f32 v150, v150, v172\n"
"v_exp_f32 v150, v150\n"
"v_sub_f32 v151, v151, v172\n"
"v_exp_f32 v151, v151\n"
"s_waitcnt lgkmcnt(1)\n"
"v_mfma_f32_16x16x32_bf16 v[132:135], v[190:193], v[136:139], v[132:135]\n"
"v_cvt_pk_bf16_f32 v148, v148, v149\n"
"v_cvt_pk_bf16_f32 v149, v150, v151\n"
"v_mfma_f32_16x16x32_bf16 v[132:135], v[192:195], v[24:27], v[132:135]\n"
"ds_read_b128 v[144:147], v175 offset:4064\n"
"ds_write_addtid_b32 v148 offset:53248\n"
"ds_write_addtid_b32 v149 offset:53504\n"
"s_waitcnt lgkmcnt(3)\n"
"v_mfma_f32_16x16x32_bf16 v[140:143], v[214:217], v[136:139], v[140:143]\n"
"v_mfma_f32_16x16x32_bf16 v[140:143], v[216:219], v[24:27], v[140:143]\n"
"ds_read_b128 v[152:155], v176 offset:2736\n"
"s_nop 0\n"
"v_sub_f32 v132, v132, v172\n"
"v_exp_f32 v132, v132\n"
"v_sub_f32 v133, v133, v172\n"
"v_exp_f32 v133, v133\n"
"v_sub_f32 v134, v134, v172\n"
"v_exp_f32 v134, v134\n"
"v_sub_f32 v135, v135, v172\n"
"v_exp_f32 v135, v135\n"
"v_sub_f32 v140, v140, v172\n"
"v_exp_f32 v140, v140\n"
"v_sub_f32 v141, v141, v172\n"
"v_exp_f32 v141, v141\n"
"v_sub_f32 v142, v142, v172\n"
"v_exp_f32 v142, v142\n"
"v_sub_f32 v143, v143, v172\n"
"v_exp_f32 v143, v143\n"
"v_cvt_pk_bf16_f32 v132, v132, v133\n"
"v_cvt_pk_bf16_f32 v133, v134, v135\n"
"v_cvt_pk_bf16_f32 v140, v140, v141\n"
"v_cvt_pk_bf16_f32 v141, v142, v143\n"
"ds_write_addtid_b32 v132 offset:53760\n"
"ds_write_addtid_b32 v133 offset:54016\n"
"s_waitcnt lgkmcnt(5)\n"
"v_mfma_f32_16x16x32_bf16 v[144:147], v[220:223], v[136:139], v[144:147]\n"
"ds_write_addtid_b32 v140 offset:54272\n"
"v_mfma_f32_16x16x32_bf16 v[144:147], v[222:225], v[24:27], v[144:147]\n"
"ds_read_b128 v[148:151], v174 offset:1360\n"
"ds_write_addtid_b32 v141 offset:54528\n"
"s_waitcnt lgkmcnt(5)\n"
"v_mfma_f32_16x16x32_bf16 v[152:155], v[196:199], v[136:139], v[152:155]\n"
"v_mfma_f32_16x16x32_bf16 v[152:155], v[198:201], v[24:27], v[152:155]\n"
"ds_read_b128 v[132:135], v174 offset:1376\n"
"s_nop 1\n"
"v_sub_f32 v144, v144, v172\n"
"v_exp_f32 v144, v144\n"
"v_sub_f32 v145, v145, v172\n"
"v_exp_f32 v145, v145\n"
"v_sub_f32 v146, v146, v172\n"
"v_exp_f32 v146, v146\n"
"v_sub_f32 v147, v147, v172\n"
"v_exp_f32 v147, v147\n"
"v_sub_f32 v152, v152, v172\n"
"v_exp_f32 v152, v152\n"
"v_sub_f32 v153, v153, v172\n"
"v_exp_f32 v153, v153\n"
"v_sub_f32 v154, v154, v172\n"
"v_exp_f32 v154, v154\n"
"v_cvt_pk_bf16_f32 v144, v144, v145\n"
"v_cvt_pk_bf16_f32 v145, v146, v147\n"
"v_sub_f32 v155, v155, v172\n"
"v_exp_f32 v155, v155\n"
"v_cvt_pk_bf16_f32 v152, v152, v153\n"
"v_cvt_pk_bf16_f32 v153, v154, v155\n"
"ds_write_addtid_b32 v144 offset:54784\n"
"ds_write_addtid_b32 v145 offset:55040\n"
"s_waitcnt lgkmcnt(4)\n"
"v_mfma_f32_16x16x32_bf16 v[148:151], v[232:235], v[136:139], v[148:151]\n"
"ds_write_addtid_b32 v152 offset:33792\n"
"v_mfma_f32_16x16x32_bf16 v[148:151], v[234:237], v[24:27], v[148:151]\n"
"ds_write_addtid_b32 v153 offset:34048\n"
"s_waitcnt lgkmcnt(4)\n"
"v_mfma_f32_16x16x32_bf16 v[132:135], v[238:241], v[136:139], v[132:135]\n"
"ds_read_b128 v[140:143], v174 offset:1392\n"
"v_mfma_f32_16x16x32_bf16 v[132:135], v[240:243], v[24:27], v[132:135]\n"
"s_waitcnt lgkmcnt(1)\n"
"ds_read_b128 v[144:147], v175 offset:4080\n"
"s_barrier\n"
"v_sub_f32 v148, v148, v172\n"
"v_exp_f32 v148, v148\n"
"v_sub_f32 v149, v149, v172\n"
"v_exp_f32 v149, v149\n"
"v_sub_f32 v150, v150, v172\n"
"v_exp_f32 v150, v150\n"
"v_sub_f32 v151, v151, v172\n"
"v_exp_f32 v151, v151\n"
"v_sub_f32 v132, v132, v172\n"
"v_exp_f32 v132, v132\n"
"v_cvt_pk_bf16_f32 v148, v148, v149\n"
"v_cvt_pk_bf16_f32 v149, v150, v151\n"
"v_sub_f32 v133, v133, v172\n"
"v_exp_f32 v133, v133\n"
"v_sub_f32 v134, v134, v172\n"
"v_exp_f32 v134, v134\n"
"v_sub_f32 v135, v135, v172\n"
"v_exp_f32 v135, v135\n"
"v_cvt_pk_bf16_f32 v132, v132, v133\n"
"v_cvt_pk_bf16_f32 v133, v134, v135\n"
"ds_write_addtid_b32 v148 offset:57856\n"
"ds_write_addtid_b32 v149 offset:58112\n"
"s_waitcnt lgkmcnt(3)\n"
"v_mfma_f32_16x16x32_bf16 v[140:143], v[244:247], v[136:139], v[140:143]\n"
"ds_write_addtid_b32 v132 offset:58368\n"
"v_mfma_f32_16x16x32_bf16 v[140:143], v[246:249], v[24:27], v[140:143]\n"
"ds_read_b128 v[232:235], v4 offset:43008\n"
"ds_write_addtid_b32 v133 offset:58624\n"
"s_waitcnt lgkmcnt(5)\n"
"v_mfma_f32_16x16x32_bf16 v[144:147], v[226:229], v[136:139], v[144:147]\n"
"v_mfma_f32_16x16x32_bf16 v[144:147], v[228:231], v[24:27], v[144:147]\n"
"s_waitcnt lgkmcnt(1)\n"
"v_mfma_f32_16x16x32_bf16 v[148:151], v[84:87], v[232:235], 0\n"
"ds_read_b128 v[152:155], v5 offset:43008\n"
"v_mfma_f32_16x16x32_bf16 v[156:159], v[88:91], v[232:235], 0\n"
"v_mfma_f32_16x16x32_bf16 v[160:163], v[92:95], v[232:235], 0\n"
"ds_read_b128 v[132:135], v6 offset:5120\n"
"v_sub_f32 v140, v140, v172\n"
"v_exp_f32 v140, v140\n"
"v_sub_f32 v141, v141, v172\n"
"v_exp_f32 v141, v141\n"
"v_sub_f32 v142, v142, v172\n"
"v_exp_f32 v142, v142\n"
"v_cvt_pk_bf16_f32 v140, v140, v141\n"
"v_sub_f32 v143, v143, v172\n"
"v_exp_f32 v143, v143\n"
"s_waitcnt lgkmcnt(1)\n"
"v_mfma_f32_16x16x32_bf16 v[148:151], v[96:99], v[152:155], v[148:151]\n"
"ds_write_addtid_b32 v140 offset:58880\n"
"v_cvt_pk_bf16_f32 v141, v142, v143\n"
"v_mfma_f32_16x16x32_bf16 v[156:159], v[100:103], v[152:155], v[156:159]\n"
"ds_read_b128 v[232:235], v4 offset:45056\n"
"ds_write_addtid_b32 v141 offset:59136\n"
"s_waitcnt lgkmcnt(3)\n"
"v_mfma_f32_16x16x32_bf16 v[160:163], v[132:135], v[152:155], v[160:163]\n"
"ds_read_b128 v[136:139], v6 offset:6144\n"
"v_sub_f32 v144, v144, v172\n"
"v_exp_f32 v144, v144\n"
"v_sub_f32 v145, v145, v172\n"
"v_exp_f32 v145, v145\n"
"v_sub_f32 v146, v146, v172\n"
"v_exp_f32 v146, v146\n"
"v_sub_f32 v147, v147, v172\n"
"v_exp_f32 v147, v147\n"
"v_cvt_pk_bf16_f32 v144, v144, v145\n"
"v_cvt_pk_bf16_f32 v145, v146, v147\n"
"s_waitcnt lgkmcnt(2)\n"
"v_mfma_f32_16x16x32_bf16 v[148:151], v[104:107], v[232:235], v[148:151]\n"
"ds_read_b128 v[132:135], v5 offset:45056\n"
"v_mfma_f32_16x16x32_bf16 v[156:159], v[108:111], v[232:235], v[156:159]\n"
"ds_write_addtid_b32 v144 offset:57344\n"
"s_waitcnt lgkmcnt(2)\n"
"v_mfma_f32_16x16x32_bf16 v[160:163], v[136:139], v[232:235], v[160:163]\n"
"ds_read_b128 v[140:143], v6 offset:7168\n"
"ds_write_addtid_b32 v145 offset:57600\n"
"s_waitcnt lgkmcnt(3)\n"
"v_mfma_f32_16x16x32_bf16 v[148:151], v[112:115], v[132:135], v[148:151]\n"
"ds_read_b128 v[152:155], v4 offset:47104\n"
"v_mfma_f32_16x16x32_bf16 v[156:159], v[116:119], v[132:135], v[156:159]\n"
"s_waitcnt lgkmcnt(1)\n"
"s_barrier\n"
"v_mfma_f32_16x16x32_bf16 v[160:163], v[140:143], v[132:135], v[160:163]\n"
"s_waitcnt lgkmcnt(0)\n"
"ds_read_b128 v[136:139], v6 offset:8192\n"
"v_mfma_f32_16x16x32_bf16 v[148:151], v[0:3], v[152:155], v[148:151]\n"
"ds_read_b128 v[144:147], v5 offset:47104\n"
"v_mfma_f32_16x16x32_bf16 v[156:159], v[120:123], v[152:155], v[156:159]\n"
"s_waitcnt lgkmcnt(1)\n"
"v_mfma_f32_16x16x32_bf16 v[160:163], v[136:139], v[152:155], v[160:163]\n"
"s_waitcnt lgkmcnt(0)\n"
"ds_read_b128 v[164:167], v6 offset:9216\n"
"v_mfma_f32_16x16x32_bf16 v[148:151], v[124:127], v[144:147], v[148:151]\n"
"ds_read_b128 v[132:135], v4 offset:36864\n"
"v_mfma_f32_16x16x32_bf16 v[156:159], v[128:131], v[144:147], v[156:159]\n"
"s_waitcnt lgkmcnt(1)\n"
"v_mfma_f32_16x16x32_bf16 v[160:163], v[164:167], v[144:147], v[160:163]\n"
"s_waitcnt lgkmcnt(0)\n"
"v_mfma_f32_16x16x32_bf16 v[232:235], v[8:11], v[132:135], 0\n"
"ds_read_b128 v[136:139], v5 offset:36864\n"
"v_mfma_f32_16x16x32_bf16 v[140:143], v[12:15], v[132:135], 0\n"
"v_mfma_f32_16x16x32_bf16 v[152:155], v[16:19], v[132:135], 0\n"
"s_waitcnt lgkmcnt(0)\n"
"v_mfma_f32_16x16x32_bf16 v[232:235], v[20:23], v[136:139], v[232:235]\n"
"ds_read_b128 v[168:171], v4 offset:38912\n"
"v_mul_f32 v148, s60, v148\n"
"v_mfma_f32_16x16x32_bf16 v[140:143], v[28:31], v[136:139], v[140:143]\n"
"v_mul_f32 v149, s60, v149\n"
"v_cvt_pk_bf16_f32 v148, v148, v149\n"
"v_mfma_f32_16x16x32_bf16 v[152:155], v[32:35], v[136:139], v[152:155]\n"
"v_mul_f32 v150, s60, v150\n"
"v_mul_f32 v151, s60, v151\n"
"v_cvt_pk_bf16_f32 v149, v150, v151\n"
"v_mul_f32 v156, s60, v156\n"
"v_mul_f32 v157, s60, v157\n"
"v_mul_f32 v158, s60, v158\n"
"v_mul_f32 v159, s60, v159\n"
"v_cvt_pk_bf16_f32 v150, v156, v157\n"
"v_cvt_pk_bf16_f32 v151, v158, v159\n"
"v_mul_f32 v160, s60, v160\n"
"v_mul_f32 v161, s60, v161\n"
"v_mul_f32 v162, s60, v162\n"
"s_waitcnt lgkmcnt(0)\n"
"v_mfma_f32_16x16x32_bf16 v[232:235], v[36:39], v[168:171], v[232:235]\n"
"ds_read_b128 v[132:135], v5 offset:38912\n"
"v_mul_f32 v163, s60, v163\n"
"v_mfma_f32_16x16x32_bf16 v[140:143], v[40:43], v[168:171], v[140:143]\n"
"v_cvt_pk_bf16_f32 v26, v160, v161\n"
"v_cvt_pk_bf16_f32 v27, v162, v163\n"
"v_mfma_f32_16x16x32_bf16 v[152:155], v[44:47], v[168:171], v[152:155]\n"
"s_waitcnt lgkmcnt(0)\n"
"v_bfe_u32 v177, v6, 2, 6\n"
"ds_read_b32 v177, v177 offset:1664\n"
"v_mfma_f32_16x16x32_bf16 v[232:235], v[48:51], v[132:135], v[232:235]\n"
"ds_read_b128 v[136:139], v4 offset:40960\n"
"v_mfma_f32_16x16x32_bf16 v[140:143], v[52:55], v[132:135], v[140:143]\n"
"s_add_u32 s69, s50, 4294965920\n"
"v_mfma_f32_16x16x32_bf16 v[152:155], v[56:59], v[132:135], v[152:155]\n"
"s_waitcnt lgkmcnt(1)\n"
"v_add_u32_sdwa v176, v177, v7 dst_sel:DWORD dst_unused:UNUSED_PAD src0_sel:WORD_0 src1_sel:DWORD\n"
"s_nop 0\n"
"v_add_u32_sdwa v177, sext(v177), v176 dst_sel:DWORD dst_unused:UNUSED_PAD src0_sel:WORD_1 src1_sel:DWORD\n"
"s_waitcnt lgkmcnt(0)\n"
"v_mfma_f32_16x16x32_bf16 v[232:235], v[60:63], v[136:139], v[232:235]\n"
"ds_read_b128 v[144:147], v5 offset:40960\n"
"v_max_u32 v175, s69, v176\n"
"v_mfma_f32_16x16x32_bf16 v[140:143], v[64:67], v[136:139], v[140:143]\n"
"v_mfma_f32_16x16x32_bf16 v[152:155], v[68:71], v[136:139], v[152:155]\n"
"s_add_u32 s70, s50, 4294966960\n"
"v_max_u32 v174, s69, v177\n"
"s_waitcnt lgkmcnt(0)\n"
"v_mfma_f32_16x16x32_bf16 v[232:235], v[72:75], v[144:147], v[232:235]\n"
"ds_read_b128 v[132:135], v175 offset:1392\n"
"v_mfma_f32_16x16x32_bf16 v[140:143], v[76:79], v[144:147], v[140:143]\n"
"v_min_u32 v173, s70, v176\n"
"v_mfma_f32_16x16x32_bf16 v[152:155], v[80:83], v[144:147], v[152:155]\n"
"s_waitcnt lgkmcnt(0)\n"
"ds_read_b128 v[136:139], v174 offset:1408\n"
"v_mfma_f32_16x16x32_bf16 v[132:135], v[244:247], v[148:151], v[132:135]\n"
"v_mfma_f32_16x16x32_bf16 v[132:135], v[246:249], v[24:27], v[132:135]\n"
"ds_read_b128 v[156:159], v173 offset:2720\n"
"v_cvt_pk_bf16_f32 v232, v232, v233\n"
"v_cvt_pk_bf16_f32 v233, v234, v235\n"
"v_cvt_pk_bf16_f32 v234, v140, v141\n"
"v_cvt_pk_bf16_f32 v235, v142, v143\n"
"v_cvt_pk_bf16_f32 v236, v152, v153\n"
"v_cvt_pk_bf16_f32 v237, v154, v155\n"
"s_waitcnt lgkmcnt(1)\n"
"v_mfma_f32_16x16x32_bf16 v[136:139], v[250:253], v[148:151], v[136:139]\n"
"v_max3_f32 v170, v132, v133, v134\n"
"v_max_f32 v170, v170, v135\n"
"v_mfma_f32_16x16x32_bf16 v[136:139], v[252:255], v[24:27], v[136:139]\n"
"s_waitcnt lgkmcnt(0)\n"
"ds_read_b128 v[140:143], v173 offset:2736\n"
"v_min_u32 v172, s70, v177\n"
"v_mfma_f32_16x16x32_bf16 v[156:159], v[190:193], v[148:151], v[156:159]\n"
"s_add_u32 s71, s50, 4294965616\n"
"v_mfma_f32_16x16x32_bf16 v[156:159], v[192:195], v[24:27], v[156:159]\n"
"s_waitcnt lgkmcnt(0)\n"
"ds_read_b128 v[132:135], v172 offset:2752\n"
"v_max3_f32 v170, v170, v136, v137\n"
"v_max3_f32 v170, v170, v138, v139\n"
"v_mfma_f32_16x16x32_bf16 v[140:143], v[196:199], v[148:151], v[140:143]\n"
"v_min_u32 v169, s71, v176\n"
"v_mfma_f32_16x16x32_bf16 v[140:143], v[198:201], v[24:27], v[140:143]\n"
"s_waitcnt lgkmcnt(0)\n"
"ds_read_b128 v[144:147], v175 offset:1376\n"
"v_max3_f32 v170, v170, v156, v157\n"
"v_max3_f32 v170, v170, v158, v159\n"
"v_mfma_f32_16x16x32_bf16 v[132:135], v[202:205], v[148:151], v[132:135]\n"
"v_mfma_f32_16x16x32_bf16 v[132:135], v[204:207], v[24:27], v[132:135]\n"
"s_waitcnt lgkmcnt(0)\n"
"ds_read_b128 v[136:139], v169 offset:4064\n"
"v_max3_f32 v170, v170, v140, v141\n"
"v_max3_f32 v170, v170, v142, v143\n"
"v_mfma_f32_16x16x32_bf16 v[144:147], v[238:241], v[148:151], v[144:147]\n"
"v_min_u32 v168, s71, v177\n"
"v_mfma_f32_16x16x32_bf16 v[144:147], v[240:243], v[24:27], v[144:147]\n"
"s_waitcnt lgkmcnt(0)\n"
"ds_read_b128 v[152:155], v169 offset:4080\n"
"v_max3_f32 v170, v170, v132, v133\n"
"v_max3_f32 v170, v170, v134, v135\n"
"v_mfma_f32_16x16x32_bf16 v[136:139], v[220:223], v[148:151], v[136:139]\n"
"v_mfma_f32_16x16x32_bf16 v[136:139], v[222:225], v[24:27], v[136:139]\n"
"s_waitcnt lgkmcnt(0)\n"
"ds_read_b128 v[140:143], v168 offset:4096\n"
"v_max3_f32 v170, v170, v144, v145\n"
"v_max3_f32 v170, v170, v146, v147\n"
"v_mfma_f32_16x16x32_bf16 v[152:155], v[226:229], v[148:151], v[152:155]\n"
"v_mfma_f32_16x16x32_bf16 v[152:155], v[228:231], v[24:27], v[152:155]\n"
"s_waitcnt lgkmcnt(0)\n"
"ds_read_b128 v[132:135], v175 offset:1392\n"
"v_max3_f32 v170, v170, v136, v137\n"
"v_max3_f32 v170, v170, v138, v139\n"
"v_mfma_f32_16x16x32_bf16 v[140:143], v[232:235], v[148:151], v[140:143]\n"
"v_mfma_f32_16x16x32_bf16 v[140:143], v[234:237], v[24:27], v[140:143]\n"
"s_waitcnt lgkmcnt(0)\n"
"ds_read_b128 v[144:147], v174 offset:1408\n"
"v_max3_f32 v170, v170, v152, v153\n"
"v_max3_f32 v170, v170, v154, v155\n"
"v_mfma_f32_16x16x32_bf16 v[132:135], v[244:247], v[148:151], v[132:135]\n"
"v_mfma_f32_16x16x32_bf16 v[132:135], v[246:249], v[24:27], v[132:135]\n"
"ds_read_b128 v[136:139], v173 offset:2720\n"
"s_nop 0\n"
"v_max3_f32 v170, v170, v140, v141\n"
"v_max3_f32 v170, v170, v142, v143\n"
"v_mov_b32 v171, v170\n"
"s_nop 1\n"
"v_permlane16_swap_b32 v170, v171\n"
"v_max_f32 v170, v170, v171\n"
"v_mov_b32 v171, v170\n"
"s_nop 1\n"
"v_permlane32_swap_b32 v170, v171\n"
"v_max_f32 v170, v170, v171\n"
"v_sub_f32 v132, v132, v170\n"
"v_exp_f32 v132, v132\n"
"v_sub_f32 v133, v133, v170\n"
"v_exp_f32 v133, v133\n"
"v_sub_f32 v134, v134, v170\n"
"v_exp_f32 v134, v134\n"
"v_sub_f32 v135, v135, v170\n"
"v_exp_f32 v135, v135\n"
"s_waitcnt lgkmcnt(1)\n"
"v_mfma_f32_16x16x32_bf16 v[144:147], v[250:253], v[148:151], v[144:147]\n"
"v_cvt_pk_bf16_f32 v132, v132, v133\n"
"v_cvt_pk_bf16_f32 v133, v134, v135\n"
"v_mfma_f32_16x16x32_bf16 v[144:147], v[252:255], v[24:27], v[144:147]\n"
"ds_read_b128 v[244:247], v173 offset:2736\n"
"ds_write_addtid_b32 v132 offset:55296\n"
"ds_write_addtid_b32 v133 offset:55552\n"
"s_waitcnt lgkmcnt(3)\n"
"v_mfma_f32_16x16x32_bf16 v[136:139], v[190:193], v[148:151], v[136:139]\n"
"v_mfma_f32_16x16x32_bf16 v[136:139], v[192:195], v[24:27], v[136:139]\n"
"ds_read_b128 v[248:251], v172 offset:2752\n"
"s_nop 0\n"
"v_sub_f32 v144, v144, v170\n"
"v_exp_f32 v144, v144\n"
"v_sub_f32 v145, v145, v170\n"
"v_exp_f32 v145, v145\n"
"v_sub_f32 v146, v146, v170\n"
"v_exp_f32 v146, v146\n"
"v_sub_f32 v147, v147, v170\n"
"v_exp_f32 v147, v147\n"
"v_sub_f32 v138, v138, v170\n"
"v_exp_f32 v138, v138\n"
"v_sub_f32 v139, v139, v170\n"
"v_exp_f32 v139, v139\n"
"v_cvt_pk_bf16_f32 v144, v144, v145\n"
"v_cvt_pk_bf16_f32 v145, v146, v147\n"
"v_cvt_pk_bf16_f32 v137, v138, v139\n"
"ds_write_addtid_b32 v144 offset:55808\n"
"ds_write_addtid_b32 v145 offset:56064\n"
"s_waitcnt lgkmcnt(5)\n"
"v_mfma_f32_16x16x32_bf16 v[244:247], v[196:199], v[148:151], v[244:247]\n"
"ds_write_addtid_b32 v137 offset:56576\n"
"v_mfma_f32_16x16x32_bf16 v[244:247], v[198:201], v[24:27], v[244:247]\n"
"s_waitcnt lgkmcnt(3)\n"
"ds_read_b128 v[252:255], v175 offset:1376\n"
"v_mfma_f32_16x16x32_bf16 v[248:251], v[202:205], v[148:151], v[248:251]\n"
"v_mfma_f32_16x16x32_bf16 v[248:251], v[204:207], v[24:27], v[248:251]\n"
"ds_read_b128 v[132:135], v169 offset:4064\n"
"s_nop 2\n"
"v_sub_f32 v244, v244, v170\n"
"v_exp_f32 v244, v244\n"
"v_sub_f32 v245, v245, v170\n"
"v_exp_f32 v245, v245\n"
"v_sub_f32 v246, v246, v170\n"
"v_exp_f32 v246, v246\n"
"v_sub_f32 v247, v247, v170\n"
"v_exp_f32 v247, v247\n"
"v_sub_f32 v248, v248, v170\n"
"v_exp_f32 v248, v248\n"
"v_sub_f32 v249, v249, v170\n"
"v_exp_f32 v249, v249\n"
"v_cvt_pk_bf16_f32 v244, v244, v245\n"
"v_sub_f32 v250, v250, v170\n"
"v_exp_f32 v250, v250\n"
"v_cvt_pk_bf16_f32 v245, v246, v247\n"
"v_sub_f32 v251, v251, v170\n"
"v_exp_f32 v251, v251\n"
"v_cvt_pk_bf16_f32 v248, v248, v249\n"
"v_cvt_pk_bf16_f32 v249, v250, v251\n"
"ds_write_addtid_b32 v244 offset:56832\n"
"ds_write_addtid_b32 v245 offset:57088\n"
"s_waitcnt lgkmcnt(3)\n"
"v_mfma_f32_16x16x32_bf16 v[252:255], v[238:241], v[148:151], v[252:255]\n"
"ds_write_addtid_b32 v248 offset:34304\n"
"v_mfma_f32_16x16x32_bf16 v[252:255], v[240:243], v[24:27], v[252:255]\n"
"ds_read_b128 v[136:139], v169 offset:4080\n"
"ds_write_addtid_b32 v249 offset:34560\n"
"s_waitcnt lgkmcnt(5)\n"
"v_mfma_f32_16x16x32_bf16 v[132:135], v[220:223], v[148:151], v[132:135]\n"
"v_mfma_f32_16x16x32_bf16 v[132:135], v[222:225], v[24:27], v[132:135]\n"
"s_waitcnt lgkmcnt(0)\n"
"ds_read_b128 v[244:247], v168 offset:4096\n"
"s_barrier\n"
"v_sub_f32 v254, v254, v170\n"
"v_exp_f32 v254, v254\n"
"v_sub_f32 v255, v255, v170\n"
"v_exp_f32 v255, v255\n"
"s_nop 0\n"
"v_cvt_pk_bf16_f32 v253, v254, v255\n"
"ds_write_addtid_b32 v253 offset:59136\n"
"v_mfma_f32_16x16x32_bf16 v[136:139], v[226:229], v[148:151], v[136:139]\n"
"v_sub_f32 v134, v134, v170\n"
"v_exp_f32 v134, v134\n"
"v_sub_f32 v135, v135, v170\n"
"v_exp_f32 v135, v135\n"
"v_mfma_f32_16x16x32_bf16 v[136:139], v[228:231], v[24:27], v[136:139]\n"
"ds_read_b128 v[238:241], v4 offset:24576\n"
"v_cvt_pk_bf16_f32 v133, v134, v135\n"
"ds_write_addtid_b32 v133 offset:57600\n"
"s_waitcnt lgkmcnt(3)\n"
"v_mfma_f32_16x16x32_bf16 v[244:247], v[232:235], v[148:151], v[244:247]\n"
"v_mfma_f32_16x16x32_bf16 v[244:247], v[234:237], v[24:27], v[244:247]\n"
"s_waitcnt lgkmcnt(1)\n"
"v_mfma_f32_16x16x32_bf16 v[250:253], v[84:87], v[238:241], 0\n"
"ds_read_b128 v[140:143], v5 offset:24576\n"
"v_mfma_f32_16x16x32_bf16 v[144:147], v[88:91], v[238:241], 0\n"
"v_sub_f32 v136, v136, v170\n"
"v_exp_f32 v136, v136\n"
"v_sub_f32 v137, v137, v170\n"
"v_exp_f32 v137, v137\n"
"v_sub_f32 v138, v138, v170\n"
"v_exp_f32 v138, v138\n"
"v_mfma_f32_16x16x32_bf16 v[132:135], v[92:95], v[238:241], 0\n"
"ds_read_b128 v[148:151], v6 offset:5120\n"
"v_sub_f32 v139, v139, v170\n"
"v_exp_f32 v139, v139\n"
"v_cvt_pk_bf16_f32 v136, v136, v137\n"
"v_cvt_pk_bf16_f32 v137, v138, v139\n"
"s_waitcnt lgkmcnt(1)\n"
"v_mfma_f32_16x16x32_bf16 v[250:253], v[96:99], v[140:143], v[250:253]\n"
"ds_write_addtid_b32 v136 offset:57856\n"
"v_mfma_f32_16x16x32_bf16 v[144:147], v[100:103], v[140:143], v[144:147]\n"
"ds_read_b128 v[238:241], v4 offset:26624\n"
"v_sub_f32 v244, v244, v170\n"
"v_exp_f32 v244, v244\n"
"v_sub_f32 v245, v245, v170\n"
"v_exp_f32 v245, v245\n"
"s_waitcnt lgkmcnt(2)\n"
"v_mfma_f32_16x16x32_bf16 v[132:135], v[148:151], v[140:143], v[132:135]\n"
"ds_write_addtid_b32 v137 offset:58112\n"
"v_sub_f32 v246, v246, v170\n"
"v_exp_f32 v246, v246\n"
"v_sub_f32 v247, v247, v170\n"
"v_exp_f32 v247, v247\n"
"v_cvt_pk_bf16_f32 v244, v244, v245\n"
"v_cvt_pk_bf16_f32 v245, v246, v247\n"
"s_waitcnt lgkmcnt(1)\n"
"v_mfma_f32_16x16x32_bf16 v[250:253], v[104:107], v[238:241], v[250:253]\n"
"ds_read_b128 v[136:139], v6 offset:6144\n"
"v_mfma_f32_16x16x32_bf16 v[144:147], v[108:111], v[238:241], v[144:147]\n"
"ds_read_b128 v[140:143], v5 offset:26624\n"
"ds_write_addtid_b32 v244 offset:58368\n"
"ds_write_addtid_b32 v245 offset:58624\n"
"s_waitcnt lgkmcnt(3)\n"
"v_mfma_f32_16x16x32_bf16 v[132:135], v[136:139], v[238:241], v[132:135]\n"
"s_waitcnt lgkmcnt(2)\n"
"ds_read_b128 v[246:249], v6 offset:7168\n"
"v_mfma_f32_16x16x32_bf16 v[250:253], v[112:115], v[140:143], v[250:253]\n"
"s_waitcnt lgkmcnt(1)\n"
"s_barrier\n"
"ds_read_b128 v[148:151], v4 offset:28672\n"
"v_mfma_f32_16x16x32_bf16 v[144:147], v[116:119], v[140:143], v[144:147]\n"
"s_waitcnt lgkmcnt(1)\n"
"v_mfma_f32_16x16x32_bf16 v[132:135], v[246:249], v[140:143], v[132:135]\n"
"s_waitcnt lgkmcnt(0)\n"
"ds_read_b128 v[238:241], v6 offset:8192\n"
"v_mfma_f32_16x16x32_bf16 v[250:253], v[0:3], v[148:151], v[250:253]\n"
"ds_read_b128 v[242:245], v5 offset:28672\n"
"v_mfma_f32_16x16x32_bf16 v[144:147], v[120:123], v[148:151], v[144:147]\n"
"s_waitcnt lgkmcnt(1)\n"
"v_mfma_f32_16x16x32_bf16 v[132:135], v[238:241], v[148:151], v[132:135]\n"
"s_waitcnt lgkmcnt(0)\n"
"ds_read_b128 v[136:139], v6 offset:9216\n"
"v_mfma_f32_16x16x32_bf16 v[250:253], v[124:127], v[242:245], v[250:253]\n"
"ds_read_b128 v[246:249], v4 offset:30720\n"
"v_mfma_f32_16x16x32_bf16 v[144:147], v[128:131], v[242:245], v[144:147]\n"
"s_waitcnt lgkmcnt(1)\n"
"v_mfma_f32_16x16x32_bf16 v[132:135], v[136:139], v[242:245], v[132:135]\n"
"s_waitcnt lgkmcnt(0)\n"
"ds_read_b128 v[140:143], v5 offset:30720\n"
"v_mfma_f32_16x16x32_bf16 v[238:241], v[8:11], v[246:249], 0\n"
"v_mfma_f32_16x16x32_bf16 v[148:151], v[12:15], v[246:249], 0\n"
"v_mfma_f32_16x16x32_bf16 v[152:155], v[16:19], v[246:249], 0\n"
"ds_read_b128 v[156:159], v4 offset:32768\n"
"v_mul_f32 v250, s60, v250\n"
"v_mul_f32 v251, s60, v251\n"
"v_cvt_pk_bf16_f32 v250, v250, v251\n"
"v_mul_f32 v252, s60, v252\n"
"v_mul_f32 v253, s60, v253\n"
"v_cvt_pk_bf16_f32 v251, v252, v253\n"
"v_mul_f32 v144, s60, v144\n"
"s_waitcnt lgkmcnt(1)\n"
"v_mfma_f32_16x16x32_bf16 v[238:241], v[20:23], v[140:143], v[238:241]\n"
"v_mul_f32 v145, s60, v145\n"
"v_mul_f32 v146, s60, v146\n"
"v_mfma_f32_16x16x32_bf16 v[148:151], v[28:31], v[140:143], v[148:151]\n"
"v_mul_f32 v147, s60, v147\n"
"v_cvt_pk_bf16_f32 v252, v144, v145\n"
"v_mfma_f32_16x16x32_bf16 v[152:155], v[32:35], v[140:143], v[152:155]\n"
"s_waitcnt lgkmcnt(0)\n"
"ds_read_b128 v[242:245], v5 offset:32768\n"
"v_cvt_pk_bf16_f32 v253, v146, v147\n"
"v_mfma_f32_16x16x32_bf16 v[238:241], v[36:39], v[156:159], v[238:241]\n"
"v_mul_f32 v132, s60, v132\n"
"v_mul_f32 v133, s60, v133\n"
"v_mfma_f32_16x16x32_bf16 v[148:151], v[40:43], v[156:159], v[148:151]\n"
"v_mul_f32 v134, s60, v134\n"
"v_mul_f32 v135, s60, v135\n"
"v_mfma_f32_16x16x32_bf16 v[152:155], v[44:47], v[156:159], v[152:155]\n"
"s_waitcnt lgkmcnt(0)\n"
"ds_read_b128 v[136:139], v4 offset:34816\n"
"v_cvt_pk_bf16_f32 v26, v132, v133\n"
"v_cvt_pk_bf16_f32 v27, v134, v135\n"
"v_mfma_f32_16x16x32_bf16 v[238:241], v[48:51], v[242:245], v[238:241]\n"
"v_mfma_f32_16x16x32_bf16 v[148:151], v[52:55], v[242:245], v[148:151]\n"
"v_mfma_f32_16x16x32_bf16 v[152:155], v[56:59], v[242:245], v[152:155]\n"
"s_waitcnt lgkmcnt(0)\n"
"ds_read_b128 v[140:143], v5 offset:34816\n"
"v_mfma_f32_16x16x32_bf16 v[238:241], v[60:63], v[136:139], v[238:241]\n"
"v_mfma_f32_16x16x32_bf16 v[148:151], v[64:67], v[136:139], v[148:151]\n"
"v_mfma_f32_16x16x32_bf16 v[152:155], v[68:71], v[136:139], v[152:155]\n"
"s_waitcnt lgkmcnt(0)\n"
"ds_read_b128 v[132:135], v4 offset:49152\n"
"v_mfma_f32_16x16x32_bf16 v[238:241], v[72:75], v[140:143], v[238:241]\n"
"v_mfma_f32_16x16x32_bf16 v[148:151], v[76:79], v[140:143], v[148:151]\n"
"v_mfma_f32_16x16x32_bf16 v[152:155], v[80:83], v[140:143], v[152:155]\n"
"s_waitcnt lgkmcnt(0)\n"
"ds_read_b128 v[144:147], v5 offset:49152\n"
"v_mfma_f32_16x16x32_bf16 v[244:247], v[8:11], v[132:135], 0\n"
"v_mfma_f32_16x16x32_bf16 v[136:139], v[12:15], v[132:135], 0\n"
"v_mfma_f32_16x16x32_bf16 v[156:159], v[16:19], v[132:135], 0\n"
"s_waitcnt lgkmcnt(0)\n"
"ds_read_b128 v[160:163], v4 offset:51200\n"
"v_mfma_f32_16x16x32_bf16 v[244:247], v[20:23], v[144:147], v[244:247]\n"
"v_cvt_pk_bf16_f32 v238, v238, v239\n"
"v_cvt_pk_bf16_f32 v239, v240, v241\n"
"v_cvt_pk_bf16_f32 v240, v148, v149\n"
"v_cvt_pk_bf16_f32 v241, v150, v151\n"
"v_cvt_pk_bf16_f32 v242, v152, v153\n"
"v_cvt_pk_bf16_f32 v243, v154, v155\n"
"v_mfma_f32_16x16x32_bf16 v[136:139], v[28:31], v[144:147], v[136:139]\n"
"v_mfma_f32_16x16x32_bf16 v[156:159], v[32:35], v[144:147], v[156:159]\n"
"s_waitcnt lgkmcnt(0)\n"
"ds_read_b128 v[132:135], v5 offset:51200\n"
"v_mfma_f32_16x16x32_bf16 v[244:247], v[36:39], v[160:163], v[244:247]\n"
"v_bfe_u32 v255, v6, 2, 6\n"
"v_mfma_f32_16x16x32_bf16 v[136:139], v[40:43], v[160:163], v[136:139]\n"
"ds_read_b32 v255, v255 offset:1728\n"
"v_mfma_f32_16x16x32_bf16 v[156:159], v[44:47], v[160:163], v[156:159]\n"
"s_add_u32 s72, s50, 4294964592\n"
"ds_read_b128 v[140:143], v4 offset:53248\n"
"s_waitcnt lgkmcnt(2)\n"
"v_mfma_f32_16x16x32_bf16 v[244:247], v[48:51], v[132:135], v[244:247]\n"
"v_mfma_f32_16x16x32_bf16 v[136:139], v[52:55], v[132:135], v[136:139]\n"
"v_mfma_f32_16x16x32_bf16 v[156:159], v[56:59], v[132:135], v[156:159]\n"
"s_waitcnt lgkmcnt(1)\n"
"ds_read_b128 v[144:147], v5 offset:53248\n"
"v_add_u32_sdwa v254, v255, v7 dst_sel:DWORD dst_unused:UNUSED_PAD src0_sel:WORD_0 src1_sel:DWORD\n"
"s_waitcnt lgkmcnt(1)\n"
"v_mfma_f32_16x16x32_bf16 v[244:247], v[60:63], v[140:143], v[244:247]\n"
"v_max_u32 v177, s72, v254\n"
"v_add_u32_sdwa v255, sext(v255), v254 dst_sel:DWORD dst_unused:UNUSED_PAD src0_sel:WORD_1 src1_sel:DWORD\n"
"v_mfma_f32_16x16x32_bf16 v[136:139], v[64:67], v[140:143], v[136:139]\n"
"v_mfma_f32_16x16x32_bf16 v[156:159], v[68:71], v[140:143], v[156:159]\n"
"s_waitcnt lgkmcnt(0)\n"
"ds_read_b128 v[132:135], v177 offset:2704\n"
"v_mfma_f32_16x16x32_bf16 v[244:247], v[72:75], v[144:147], v[244:247]\n"
"v_mfma_f32_16x16x32_bf16 v[136:139], v[76:79], v[144:147], v[136:139]\n"
"s_add_u32 s73, s50, 4294965632\n"
"ds_read_b128 v[148:151], v177 offset:2720\n"
"v_max_u32 v176, s72, v255\n"
"v_mfma_f32_16x16x32_bf16 v[156:159], v[80:83], v[144:147], v[156:159]\n"
"s_waitcnt lgkmcnt(1)\n"
"v_mfma_f32_16x16x32_bf16 v[132:135], v[184:187], v[250:253], v[132:135]\n"
"ds_read_b128 v[140:143], v177 offset:2736\n"
"v_min_u32 v175, s73, v254\n"
"v_cvt_pk_bf16_f32 v244, v244, v245\n"
"v_cvt_pk_bf16_f32 v245, v246, v247\n"
"v_cvt_pk_bf16_f32 v246, v136, v137\n"
"v_cvt_pk_bf16_f32 v247, v138, v139\n"
"v_mfma_f32_16x16x32_bf16 v[132:135], v[186:189], v[24:27], v[132:135]\n"
"s_waitcnt lgkmcnt(1)\n"
"v_cvt_pk_bf16_f32 v248, v156, v157\n"
"v_cvt_pk_bf16_f32 v249, v158, v159\n"
"v_mfma_f32_16x16x32_bf16 v[148:151], v[190:193], v[250:253], v[148:151]\n"
"ds_read_b128 v[144:147], v176 offset:2752\n"
"v_mfma_f32_16x16x32_bf16 v[148:151], v[192:195], v[24:27], v[148:151]\n"
"s_waitcnt lgkmcnt(1)\n"
"s_nop 0\n"
"v_max3_f32 v254, v132, v133, v134\n"
"v_max_f32 v254, v254, v135\n"
"v_mfma_f32_16x16x32_bf16 v[140:143], v[196:199], v[250:253], v[140:143]\n"
"ds_read_b128 v[136:139], v175 offset:4048\n"
"v_mfma_f32_16x16x32_bf16 v[140:143], v[198:201], v[24:27], v[140:143]\n"
"s_waitcnt lgkmcnt(1)\n"
"v_max3_f32 v254, v254, v148, v149\n"
"v_max3_f32 v254, v254, v150, v151\n"
"v_mfma_f32_16x16x32_bf16 v[144:147], v[202:205], v[250:253], v[144:147]\n"
"ds_read_b128 v[132:135], v175 offset:4064\n"
"v_min_u32 v174, s73, v255\n"
"v_mfma_f32_16x16x32_bf16 v[144:147], v[204:207], v[24:27], v[144:147]\n"
"s_waitcnt lgkmcnt(1)\n"
"v_max3_f32 v254, v254, v140, v141\n"
"v_max3_f32 v254, v254, v142, v143\n"
"v_mfma_f32_16x16x32_bf16 v[136:139], v[214:217], v[250:253], v[136:139]\n"
"ds_read_b128 v[148:151], v175 offset:4080\n"
"v_mfma_f32_16x16x32_bf16 v[136:139], v[216:219], v[24:27], v[136:139]\n"
"s_waitcnt lgkmcnt(1)\n"
"s_nop 0\n"
"v_max3_f32 v254, v254, v144, v145\n"
"v_max3_f32 v254, v254, v146, v147\n"
"v_mfma_f32_16x16x32_bf16 v[132:135], v[220:223], v[250:253], v[132:135]\n"
"ds_read_b128 v[140:143], v174 offset:4096\n"
"v_mfma_f32_16x16x32_bf16 v[132:135], v[222:225], v[24:27], v[132:135]\n"
"s_waitcnt lgkmcnt(1)\n"
"v_max3_f32 v254, v254, v136, v137\n"
"v_max3_f32 v254, v254, v138, v139\n"
"v_mfma_f32_16x16x32_bf16 v[148:151], v[226:229], v[250:253], v[148:151]\n"
"ds_read_b128 v[144:147], v177 offset:2704\n"
"v_mfma_f32_16x16x32_bf16 v[148:151], v[228:231], v[24:27], v[148:151]\n"
"s_waitcnt lgkmcnt(1)\n"
"s_nop 0\n"
"v_max3_f32 v254, v254, v132, v133\n"
"v_max3_f32 v254, v254, v134, v135\n"
"v_mfma_f32_16x16x32_bf16 v[140:143], v[232:235], v[250:253], v[140:143]\n"
"ds_read_b128 v[136:139], v177 offset:2720\n"
"v_mfma_f32_16x16x32_bf16 v[140:143], v[234:237], v[24:27], v[140:143]\n"
"s_waitcnt lgkmcnt(1)\n"
"v_max3_f32 v254, v254, v148, v149\n"
"v_max3_f32 v254, v254, v150, v151\n"
"v_mfma_f32_16x16x32_bf16 v[144:147], v[184:187], v[250:253], v[144:147]\n"
"ds_read_b128 v[132:135], v177 offset:2736\n"
"v_mfma_f32_16x16x32_bf16 v[144:147], v[186:189], v[24:27], v[144:147]\n"
"s_nop 1\n"
"v_max3_f32 v254, v254, v140, v141\n"
"v_max3_f32 v254, v254, v142, v143\n"
"v_mov_b32 v255, v254\n"
"s_nop 1\n"
"v_permlane16_swap_b32 v254, v255\n"
"v_max_f32 v254, v254, v255\n"
"v_mov_b32 v255, v254\n"
"s_nop 1\n"
"v_permlane32_swap_b32 v254, v255\n"
"v_max_f32 v254, v254, v255\n"
"v_sub_f32 v146, v146, v254\n"
"v_exp_f32 v146, v146\n"
"v_sub_f32 v147, v147, v254\n"
"v_exp_f32 v147, v147\n"
"s_waitcnt lgkmcnt(1)\n"
"v_mfma_f32_16x16x32_bf16 v[136:139], v[190:193], v[250:253], v[136:139]\n"
"ds_read_b128 v[140:143], v176 offset:2752\n"
"v_cvt_pk_bf16_f32 v145, v146, v147\n"
"v_mfma_f32_16x16x32_bf16 v[136:139], v[192:195], v[24:27], v[136:139]\n"
"ds_write_addtid_b32 v145 offset:53504\n"
"s_waitcnt lgkmcnt(2)\n"
"v_mfma_f32_16x16x32_bf16 v[132:135], v[196:199], v[250:253], v[132:135]\n"
"ds_read_b128 v[148:151], v175 offset:4048\n"
"v_mfma_f32_16x16x32_bf16 v[132:135], v[198:201], v[24:27], v[132:135]\n"
"s_nop 2\n"
"v_sub_f32 v136, v136, v254\n"
"v_exp_f32 v136, v136\n"
"v_sub_f32 v137, v137, v254\n"
"v_exp_f32 v137, v137\n"
"v_sub_f32 v138, v138, v254\n"
"v_exp_f32 v138, v138\n"
"v_sub_f32 v139, v139, v254\n"
"v_exp_f32 v139, v139\n"
"v_sub_f32 v132, v132, v254\n"
"v_exp_f32 v132, v132\n"
"v_sub_f32 v133, v133, v254\n"
"v_exp_f32 v133, v133\n"
"v_sub_f32 v134, v134, v254\n"
"v_exp_f32 v134, v134\n"
"v_sub_f32 v135, v135, v254\n"
"v_exp_f32 v135, v135\n"
"v_cvt_pk_bf16_f32 v136, v136, v137\n"
"v_cvt_pk_bf16_f32 v137, v138, v139\n"
"v_cvt_pk_bf16_f32 v132, v132, v133\n"
"v_cvt_pk_bf16_f32 v133, v134, v135\n"
"ds_write_addtid_b32 v136 offset:53760\n"
"ds_write_addtid_b32 v137 offset:54016\n"
"s_waitcnt lgkmcnt(4)\n"
"v_mfma_f32_16x16x32_bf16 v[140:143], v[202:205], v[250:253], v[140:143]\n"
"ds_read_b128 v[144:147], v175 offset:4064\n"
"v_mfma_f32_16x16x32_bf16 v[140:143], v[204:207], v[24:27], v[140:143]\n"
"ds_write_addtid_b32 v132 offset:54272\n"
"ds_write_addtid_b32 v133 offset:54528\n"
"s_waitcnt lgkmcnt(5)\n"
"v_mfma_f32_16x16x32_bf16 v[148:151], v[214:217], v[250:253], v[148:151]\n"
"ds_read_b128 v[136:139], v175 offset:4080\n"
"v_mfma_f32_16x16x32_bf16 v[148:151], v[216:219], v[24:27], v[148:151]\n"
"s_nop 1\n"
"v_sub_f32 v140, v140, v254\n"
"v_exp_f32 v140, v140\n"
"v_sub_f32 v141, v141, v254\n"
"v_exp_f32 v141, v141\n"
"v_sub_f32 v142, v142, v254\n"
"v_exp_f32 v142, v142\n"
"v_sub_f32 v143, v143, v254\n"
"v_exp_f32 v143, v143\n"
"v_cvt_pk_bf16_f32 v140, v140, v141\n"
"v_cvt_pk_bf16_f32 v141, v142, v143\n"
"v_sub_f32 v150, v150, v254\n"
"v_exp_f32 v150, v150\n"
"v_sub_f32 v151, v151, v254\n"
"v_exp_f32 v151, v151\n"
"ds_write_addtid_b32 v140 offset:54784\n"
"v_cvt_pk_bf16_f32 v149, v150, v151\n"
"ds_write_addtid_b32 v141 offset:55040\n"
"s_waitcnt lgkmcnt(5)\n"
"v_mfma_f32_16x16x32_bf16 v[144:147], v[220:223], v[250:253], v[144:147]\n"
"ds_read_b128 v[132:135], v174 offset:4096\n"
"v_mfma_f32_16x16x32_bf16 v[144:147], v[222:225], v[24:27], v[144:147]\n"
"s_waitcnt lgkmcnt(1)\n"
"s_barrier\n"
"ds_write_addtid_b32 v149 offset:57600\n"
"v_mfma_f32_16x16x32_bf16 v[136:139], v[226:229], v[250:253], v[136:139]\n"
"ds_read_b128 v[140:143], v4 offset:55296\n"
"v_mfma_f32_16x16x32_bf16 v[136:139], v[228:231], v[24:27], v[136:139]\n"
"s_nop 1\n"
"v_sub_f32 v144, v144, v254\n"
"v_exp_f32 v144, v144\n"
"v_sub_f32 v145, v145, v254\n"
"v_exp_f32 v145, v145\n"
"v_sub_f32 v146, v146, v254\n"
"v_exp_f32 v146, v146\n"
"v_sub_f32 v147, v147, v254\n"
"v_exp_f32 v147, v147\n"
"v_cvt_pk_bf16_f32 v144, v144, v145\n"
"v_cvt_pk_bf16_f32 v145, v146, v147\n"
"v_sub_f32 v136, v136, v254\n"
"v_exp_f32 v136, v136\n"
"v_sub_f32 v137, v137, v254\n"
"v_exp_f32 v137, v137\n"
"v_sub_f32 v138, v138, v254\n"
"v_exp_f32 v138, v138\n"
"v_sub_f32 v139, v139, v254\n"
"v_exp_f32 v139, v139\n"
"v_cvt_pk_bf16_f32 v136, v136, v137\n"
"v_cvt_pk_bf16_f32 v137, v138, v139\n"
"ds_write_addtid_b32 v144 offset:57856\n"
"ds_write_addtid_b32 v145 offset:58112\n"
"s_waitcnt lgkmcnt(4)\n"
"v_mfma_f32_16x16x32_bf16 v[132:135], v[232:235], v[250:253], v[132:135]\n"
"ds_write_addtid_b32 v136 offset:58368\n"
"v_mfma_f32_16x16x32_bf16 v[132:135], v[234:237], v[24:27], v[132:135]\n"
"s_waitcnt lgkmcnt(3)\n"
"ds_read_b128 v[148:151], v5 offset:55296\n"
"v_mfma_f32_16x16x32_bf16 v[152:155], v[84:87], v[140:143], 0\n"
"ds_write_addtid_b32 v137 offset:58624\n"
"v_mfma_f32_16x16x32_bf16 v[144:147], v[88:91], v[140:143], 0\n"
"ds_read_b128 v[250:253], v6 offset:5120\n"
"v_mfma_f32_16x16x32_bf16 v[156:159], v[92:95], v[140:143], 0\n"
"s_waitcnt lgkmcnt(2)\n"
"ds_read_b128 v[160:163], v4 offset:57344\n"
"v_mfma_f32_16x16x32_bf16 v[152:155], v[96:99], v[148:151], v[152:155]\n"
"v_sub_f32 v132, v132, v254\n"
"v_exp_f32 v132, v132\n"
"v_mfma_f32_16x16x32_bf16 v[144:147], v[100:103], v[148:151], v[144:147]\n"
"s_waitcnt lgkmcnt(1)\n"
"ds_read_b128 v[136:139], v6 offset:6144\n"
"v_mfma_f32_16x16x32_bf16 v[156:159], v[250:253], v[148:151], v[156:159]\n"
"s_waitcnt lgkmcnt(1)\n"
"ds_read_b128 v[140:143], v5 offset:57344\n"
"v_mfma_f32_16x16x32_bf16 v[152:155], v[104:107], v[160:163], v[152:155]\n"
"v_sub_f32 v133, v133, v254\n"
"v_exp_f32 v133, v133\n"
"v_mfma_f32_16x16x32_bf16 v[144:147], v[108:111], v[160:163], v[144:147]\n"
"ds_read_b128 v[164:167], v6 offset:7168\n"
"v_sub_f32 v134, v134, v254\n"
"v_exp_f32 v134, v134\n"
"v_sub_f32 v135, v135, v254\n"
"v_exp_f32 v135, v135\n"
"s_waitcnt lgkmcnt(2)\n"
"v_mfma_f32_16x16x32_bf16 v[156:159], v[136:139], v[160:163], v[156:159]\n"
"s_waitcnt lgkmcnt(1)\n"
"ds_read_b128 v[148:151], v4 offset:59392\n"
"v_cvt_pk_bf16_f32 v132, v132, v133\n"
"v_cvt_pk_bf16_f32 v133, v134, v135\n"
"v_mfma_f32_16x16x32_bf16 v[152:155], v[112:115], v[140:143], v[152:155]\n"
"ds_write_addtid_b32 v132 offset:58880\n"
"v_mfma_f32_16x16x32_bf16 v[144:147], v[116:119], v[140:143], v[144:147]\n"
"ds_write_addtid_b32 v133 offset:59136\n"
"s_waitcnt lgkmcnt(3)\n"
"v_mfma_f32_16x16x32_bf16 v[156:159], v[164:167], v[140:143], v[156:159]\n"
"s_waitcnt lgkmcnt(2)\n"
"ds_read_b128 v[136:139], v6 offset:8192\n"
"ds_read_b128 v[160:163], v5 offset:59392\n"
"v_mfma_f32_16x16x32_bf16 v[152:155], v[0:3], v[148:151], v[152:155]\n"
"v_mfma_f32_16x16x32_bf16 v[144:147], v[120:123], v[148:151], v[144:147]\n"
"s_waitcnt lgkmcnt(2)\n"
"s_barrier\n"
"ds_read_b128 v[132:135], v6 offset:9216\n"
"s_waitcnt lgkmcnt(2)\n"
"v_mfma_f32_16x16x32_bf16 v[156:159], v[136:139], v[148:151], v[156:159]\n"
"s_waitcnt lgkmcnt(1)\n"
"v_mfma_f32_16x16x32_bf16 v[152:155], v[124:127], v[160:163], v[152:155]\n"
"ds_read_b128 v[140:143], v4 offset:12288\n"
"v_mfma_f32_16x16x32_bf16 v[144:147], v[128:131], v[160:163], v[144:147]\n"
"s_waitcnt lgkmcnt(1)\n"
"v_mfma_f32_16x16x32_bf16 v[156:159], v[132:135], v[160:163], v[156:159]\n"
"s_waitcnt lgkmcnt(0)\n"
"v_mfma_f32_16x16x32_bf16 v[250:253], v[8:11], v[140:143], 0\n"
"ds_read_b128 v[164:167], v5 offset:12288\n"
"v_mfma_f32_16x16x32_bf16 v[136:139], v[12:15], v[140:143], 0\n"
"v_mfma_f32_16x16x32_bf16 v[148:151], v[16:19], v[140:143], 0\n"
"s_waitcnt lgkmcnt(0)\n"
"v_mfma_f32_16x16x32_bf16 v[250:253], v[20:23], v[164:167], v[250:253]\n"
"ds_read_b128 v[168:171], v4 offset:14336\n"
"v_mul_f32 v152, s60, v152\n"
"v_mfma_f32_16x16x32_bf16 v[136:139], v[28:31], v[164:167], v[136:139]\n"
"v_mul_f32 v153, s60, v153\n"
"v_cvt_pk_bf16_f32 v152, v152, v153\n"
"v_mfma_f32_16x16x32_bf16 v[148:151], v[32:35], v[164:167], v[148:151]\n"
"v_mul_f32 v154, s60, v154\n"
"v_mul_f32 v155, s60, v155\n"
"v_cvt_pk_bf16_f32 v153, v154, v155\n"
"v_mul_f32 v144, s60, v144\n"
"v_mul_f32 v145, s60, v145\n"
"v_mul_f32 v146, s60, v146\n"
"v_mul_f32 v147, s60, v147\n"
"v_cvt_pk_bf16_f32 v154, v144, v145\n"
"v_cvt_pk_bf16_f32 v155, v146, v147\n"
"v_mul_f32 v156, s60, v156\n"
"v_mul_f32 v157, s60, v157\n"
"v_mul_f32 v158, s60, v158\n"
"s_waitcnt lgkmcnt(0)\n"
"v_mfma_f32_16x16x32_bf16 v[250:253], v[36:39], v[168:171], v[250:253]\n"
"ds_read_b128 v[132:135], v5 offset:14336\n"
"v_mul_f32 v159, s60, v159\n"
"v_mfma_f32_16x16x32_bf16 v[136:139], v[40:43], v[168:171], v[136:139]\n"
"v_cvt_pk_bf16_f32 v26, v156, v157\n"
"v_cvt_pk_bf16_f32 v27, v158, v159\n"
"v_mfma_f32_16x16x32_bf16 v[148:151], v[44:47], v[168:171], v[148:151]\n"
"s_waitcnt lgkmcnt(0)\n"
"v_bfe_u32 v255, v6, 2, 6\n"
"ds_read_b32 v255, v255 offset:1792\n"
"v_mfma_f32_16x16x32_bf16 v[250:253], v[48:51], v[132:135], v[250:253]\n"
"ds_read_b128 v[140:143], v4 offset:16384\n"
"v_mfma_f32_16x16x32_bf16 v[136:139], v[52:55], v[132:135], v[136:139]\n"
"s_add_u32 s68, s50, 4294964608\n"
"v_mfma_f32_16x16x32_bf16 v[148:151], v[56:59], v[132:135], v[148:151]\n"
"s_waitcnt lgkmcnt(1)\n"
"v_add_u32_sdwa v177, v255, v7 dst_sel:DWORD dst_unused:UNUSED_PAD src0_sel:WORD_0 src1_sel:DWORD\n"
"s_waitcnt lgkmcnt(0)\n"
"v_mfma_f32_16x16x32_bf16 v[250:253], v[60:63], v[140:143], v[250:253]\n"
"s_add_u32 s69, s50, 4294963264\n"
"ds_read_b128 v[144:147], v5 offset:16384\n"
"v_max_u32 v176, s68, v177\n"
"v_mfma_f32_16x16x32_bf16 v[136:139], v[64:67], v[140:143], v[136:139]\n"
"v_mfma_f32_16x16x32_bf16 v[148:151], v[68:71], v[140:143], v[148:151]\n"
"s_waitcnt lgkmcnt(0)\n"
"v_max_u32 v175, s69, v177\n"
"v_mfma_f32_16x16x32_bf16 v[250:253], v[72:75], v[144:147], v[250:253]\n"
"ds_read_b128 v[132:135], v176 offset:2688\n"
"v_mfma_f32_16x16x32_bf16 v[136:139], v[76:79], v[144:147], v[136:139]\n"
"v_mfma_f32_16x16x32_bf16 v[148:151], v[80:83], v[144:147], v[148:151]\n"
"s_add_u32 s70, s50, 4294964304\n"
"ds_read_b128 v[140:143], v175 offset:4032\n"
"s_waitcnt lgkmcnt(1)\n"
"v_mfma_f32_16x16x32_bf16 v[132:135], v[178:181], v[152:155], v[132:135]\n"
"v_mfma_f32_16x16x32_bf16 v[132:135], v[180:183], v[24:27], v[132:135]\n"
"ds_read_b128 v[156:159], v176 offset:2704\n"
"v_cvt_pk_bf16_f32 v250, v250, v251\n"
"v_cvt_pk_bf16_f32 v251, v252, v253\n"
"v_cvt_pk_bf16_f32 v252, v136, v137\n"
"v_cvt_pk_bf16_f32 v253, v138, v139\n"
"v_cvt_pk_bf16_f32 v254, v148, v149\n"
"v_cvt_pk_bf16_f32 v255, v150, v151\n"
"s_waitcnt lgkmcnt(1)\n"
"v_mfma_f32_16x16x32_bf16 v[140:143], v[208:211], v[152:155], v[140:143]\n"
"v_max3_f32 v172, v132, v133, v134\n"
"v_max_f32 v172, v172, v135\n"
"v_mfma_f32_16x16x32_bf16 v[140:143], v[210:213], v[24:27], v[140:143]\n"
"ds_read_b128 v[136:139], v175 offset:4048\n"
"v_min_u32 v174, s70, v177\n"
"s_mov_b64 exec, s[52:53]\n"
"v_add_u32 v174, 1040, v174\n"
"s_mov_b64 exec, -1\n"
"s_waitcnt lgkmcnt(1)\n"
"v_mfma_f32_16x16x32_bf16 v[156:159], v[184:187], v[152:155], v[156:159]\n"
"v_mfma_f32_16x16x32_bf16 v[156:159], v[186:189], v[24:27], v[156:159]\n"
"s_waitcnt lgkmcnt(0)\n"
"ds_read_b128 v[132:135], v174 offset:5376\n"
"v_max3_f32 v172, v172, v140, v141\n"
"v_max3_f32 v172, v172, v142, v143\n"
"v_mfma_f32_16x16x32_bf16 v[136:139], v[214:217], v[152:155], v[136:139]\n"
"v_mfma_f32_16x16x32_bf16 v[136:139], v[216:219], v[24:27], v[136:139]\n"
"s_waitcnt lgkmcnt(0)\n"
"ds_read_b128 v[144:147], v176 offset:2720\n"
"v_max3_f32 v172, v172, v156, v157\n"
"v_max3_f32 v172, v172, v158, v159\n"
"v_mfma_f32_16x16x32_bf16 v[132:135], v[238:241], v[152:155], v[132:135]\n"
"v_mfma_f32_16x16x32_bf16 v[132:135], v[240:243], v[24:27], v[132:135]\n"
"s_waitcnt lgkmcnt(0)\n"
"ds_read_b128 v[140:143], v175 offset:4064\n"
"v_max3_f32 v172, v172, v136, v137\n"
"v_max3_f32 v172, v172, v138, v139\n"
"v_mfma_f32_16x16x32_bf16 v[144:147], v[190:193], v[152:155], v[144:147]\n"
"v_mfma_f32_16x16x32_bf16 v[144:147], v[192:195], v[24:27], v[144:147]\n"
"s_waitcnt lgkmcnt(0)\n"
"ds_read_b128 v[148:151], v174 offset:5392\n"
"v_max3_f32 v172, v172, v132, v133\n"
"v_max3_f32 v172, v172, v134, v135\n"
"v_mfma_f32_16x16x32_bf16 v[140:143], v[220:223], v[152:155], v[140:143]\n"
"v_mfma_f32_16x16x32_bf16 v[140:143], v[222:225], v[24:27], v[140:143]\n"
"s_waitcnt lgkmcnt(0)\n"
"ds_read_b128 v[136:139], v174 offset:5408\n"
"v_max3_f32 v172, v172, v144, v145\n"
"v_max3_f32 v172, v172, v146, v147\n"
"v_mfma_f32_16x16x32_bf16 v[148:151], v[244:247], v[152:155], v[148:151]\n"
"v_mfma_f32_16x16x32_bf16 v[148:151], v[246:249], v[24:27], v[148:151]\n"
"s_waitcnt lgkmcnt(0)\n"
"ds_read_b128 v[132:135], v176 offset:2688\n"
"v_max3_f32 v172, v172, v140, v141\n"
"v_max3_f32 v172, v172, v142, v143\n"
"v_mfma_f32_16x16x32_bf16 v[136:139], v[250:253], v[152:155], v[136:139]\n"
"v_mfma_f32_16x16x32_bf16 v[136:139], v[252:255], v[24:27], v[136:139]\n"
"s_waitcnt lgkmcnt(0)\n"
"ds_read_b128 v[144:147], v175 offset:4032\n"
"v_max3_f32 v172, v172, v148, v149\n"
"v_max3_f32 v172, v172, v150, v151\n"
"v_mfma_f32_16x16x32_bf16 v[132:135], v[178:181], v[152:155], v[132:135]\n"
"v_mfma_f32_16x16x32_bf16 v[132:135], v[180:183], v[24:27], v[132:135]\n"
"ds_read_b128 v[140:143], v176 offset:2704\n"
"s_nop 0\n"
"v_max3_f32 v172, v172, v136, v137\n"
"v_max3_f32 v172, v172, v138, v139\n"
"v_mov_b32 v173, v172\n"
"s_nop 1\n"
"v_permlane16_swap_b32 v172, v173\n"
"v_max_f32 v172, v172, v173\n"
"v_mov_b32 v173, v172\n"
"s_nop 1\n"
"v_permlane32_swap_b32 v172, v173\n"
"v_max_f32 v172, v172, v173\n"
"v_sub_f32 v132, v132, v172\n"
"v_exp_f32 v132, v132\n"
"v_sub_f32 v133, v133, v172\n"
"v_exp_f32 v133, v133\n"
"v_sub_f32 v134, v134, v172\n"
"v_exp_f32 v134, v134\n"
"v_sub_f32 v135, v135, v172\n"
"v_exp_f32 v135, v135\n"
"s_waitcnt lgkmcnt(1)\n"
"v_mfma_f32_16x16x32_bf16 v[144:147], v[208:211], v[152:155], v[144:147]\n"
"v_cvt_pk_bf16_f32 v132, v132, v133\n"
"v_cvt_pk_bf16_f32 v133, v134, v135\n"
"v_mfma_f32_16x16x32_bf16 v[144:147], v[210:213], v[24:27], v[144:147]\n"
"ds_read_b128 v[178:181], v175 offset:4048\n"
"ds_write_addtid_b32 v132 offset:55296\n"
"ds_write_addtid_b32 v133 offset:55552\n"
"s_waitcnt lgkmcnt(3)\n"
"v_mfma_f32_16x16x32_bf16 v[140:143], v[184:187], v[152:155], v[140:143]\n"
"v_mfma_f32_16x16x32_bf16 v[140:143], v[186:189], v[24:27], v[140:143]\n"
"ds_read_b128 v[136:139], v174 offset:5376\n"
"s_nop 0\n"
"v_sub_f32 v144, v144, v172\n"
"v_exp_f32 v144, v144\n"
"v_sub_f32 v145, v145, v172\n"
"v_exp_f32 v145, v145\n"
"v_sub_f32 v146, v146, v172\n"
"v_exp_f32 v146, v146\n"
"v_sub_f32 v147, v147, v172\n"
"v_exp_f32 v147, v147\n"
"v_sub_f32 v140, v140, v172\n"
"v_exp_f32 v140, v140\n"
"v_sub_f32 v141, v141, v172\n"
"v_exp_f32 v141, v141\n"
"v_sub_f32 v142, v142, v172\n"
"v_exp_f32 v142, v142\n"
"v_cvt_pk_bf16_f32 v144, v144, v145\n"
"v_sub_f32 v143, v143, v172\n"
"v_exp_f32 v143, v143\n"
"v_cvt_pk_bf16_f32 v145, v146, v147\n"
"v_cvt_pk_bf16_f32 v140, v140, v141\n"
"v_cvt_pk_bf16_f32 v141, v142, v143\n"
"ds_write_addtid_b32 v144 offset:56320\n"
"ds_write_addtid_b32 v145 offset:56576\n"
"s_waitcnt lgkmcnt(5)\n"
"v_mfma_f32_16x16x32_bf16 v[178:181], v[214:217], v[152:155], v[178:181]\n"
"ds_write_addtid_b32 v140 offset:55808\n"
"v_mfma_f32_16x16x32_bf16 v[178:181], v[216:219], v[24:27], v[178:181]\n"
"ds_read_b128 v[208:211], v176 offset:2720\n"
"ds_write_addtid_b32 v141 offset:56064\n"
"s_waitcnt lgkmcnt(5)\n"
"v_mfma_f32_16x16x32_bf16 v[136:139], v[238:241], v[152:155], v[136:139]\n"
"v_mfma_f32_16x16x32_bf16 v[136:139], v[240:243], v[24:27], v[136:139]\n"
"ds_read_b128 v[132:135], v175 offset:4064\n"
"s_nop 1\n"
"v_sub_f32 v178, v178, v172\n"
"v_exp_f32 v178, v178\n"
"v_sub_f32 v179, v179, v172\n"
"v_exp_f32 v179, v179\n"
"v_sub_f32 v180, v180, v172\n"
"v_exp_f32 v180, v180\n"
"v_sub_f32 v181, v181, v172\n"
"v_exp_f32 v181, v181\n"
"v_cvt_pk_bf16_f32 v178, v178, v179\n"
"v_cvt_pk_bf16_f32 v179, v180, v181\n"
"v_sub_f32 v136, v136, v172\n"
"v_exp_f32 v136, v136\n"
"v_sub_f32 v137, v137, v172\n"
"v_exp_f32 v137, v137\n"
"v_sub_f32 v138, v138, v172\n"
"v_exp_f32 v138, v138\n"
"v_sub_f32 v139, v139, v172\n"
"v_exp_f32 v139, v139\n"
"v_cvt_pk_bf16_f32 v136, v136, v137\n"
"v_cvt_pk_bf16_f32 v137, v138, v139\n"
"ds_write_addtid_b32 v178 offset:56832\n"
"ds_write_addtid_b32 v179 offset:57088\n"
"s_waitcnt lgkmcnt(4)\n"
"v_mfma_f32_16x16x32_bf16 v[208:211], v[190:193], v[152:155], v[208:211]\n"
"v_mfma_f32_16x16x32_bf16 v[208:211], v[192:195], v[24:27], v[208:211]\n"
"s_waitcnt lgkmcnt(0)\n"
"ds_read_b128 v[180:183], v174 offset:5392\n"
"s_barrier\n"
"ds_write_addtid_b32 v136 offset:57344\n"
"ds_write_addtid_b32 v137 offset:57600\n"
"v_mfma_f32_16x16x32_bf16 v[132:135], v[220:223], v[152:155], v[132:135]\n"
"v_mfma_f32_16x16x32_bf16 v[132:135], v[222:225], v[24:27], v[132:135]\n"
"s_nop 0\n"
"ds_read_b128 v[210:213], v174 offset:5408\n"
"v_sub_f32 v208, v208, v172\n"
"v_exp_f32 v208, v208\n"
"v_sub_f32 v209, v209, v172\n"
"v_exp_f32 v209, v209\n"
"s_nop 0\n"
"v_cvt_pk_bf16_f32 v208, v208, v209\n"
"ds_write_addtid_b32 v208 offset:58368\n"
"s_waitcnt lgkmcnt(4)\n"
"v_mfma_f32_16x16x32_bf16 v[180:183], v[244:247], v[152:155], v[180:183]\n"
"v_sub_f32 v132, v132, v172\n"
"v_exp_f32 v132, v132\n"
"v_sub_f32 v133, v133, v172\n"
"v_exp_f32 v133, v133\n"
"v_mfma_f32_16x16x32_bf16 v[180:183], v[246:249], v[24:27], v[180:183]\n"
"ds_read_b128 v[238:241], v4 offset:18432\n"
"v_cvt_pk_bf16_f32 v132, v132, v133\n"
"ds_write_addtid_b32 v132 offset:58880\n"
"s_waitcnt lgkmcnt(3)\n"
"v_mfma_f32_16x16x32_bf16 v[210:213], v[250:253], v[152:155], v[210:213]\n"
"v_mfma_f32_16x16x32_bf16 v[210:213], v[252:255], v[24:27], v[210:213]\n"
"s_waitcnt lgkmcnt(1)\n"
"v_mfma_f32_16x16x32_bf16 v[136:139], v[84:87], v[238:241], 0\n"
"ds_read_b128 v[140:143], v5 offset:18432\n"
"v_mfma_f32_16x16x32_bf16 v[144:147], v[88:91], v[238:241], 0\n"
"v_sub_f32 v180, v180, v172\n"
"v_exp_f32 v180, v180\n"
"v_sub_f32 v181, v181, v172\n"
"v_exp_f32 v181, v181\n"
"v_sub_f32 v182, v182, v172\n"
"v_exp_f32 v182, v182\n"
"v_mfma_f32_16x16x32_bf16 v[132:135], v[92:95], v[238:241], 0\n"
"ds_read_b128 v[148:151], v6 offset:5120\n"
"v_sub_f32 v183, v183, v172\n"
"v_exp_f32 v183, v183\n"
"v_cvt_pk_bf16_f32 v180, v180, v181\n"
"v_cvt_pk_bf16_f32 v181, v182, v183\n"
"s_waitcnt lgkmcnt(1)\n"
"v_mfma_f32_16x16x32_bf16 v[136:139], v[96:99], v[140:143], v[136:139]\n"
"ds_write_addtid_b32 v180 offset:57856\n"
"v_mfma_f32_16x16x32_bf16 v[144:147], v[100:103], v[140:143], v[144:147]\n"
"ds_read_b128 v[238:241], v4 offset:20480\n"
"v_sub_f32 v210, v210, v172\n"
"v_exp_f32 v210, v210\n"
"v_sub_f32 v211, v211, v172\n"
"v_exp_f32 v211, v211\n"
"s_waitcnt lgkmcnt(2)\n"
"v_mfma_f32_16x16x32_bf16 v[132:135], v[148:151], v[140:143], v[132:135]\n"
"ds_write_addtid_b32 v181 offset:58112\n"
"v_cvt_pk_bf16_f32 v210, v210, v211\n"
"ds_read_b128 v[152:155], v6 offset:6144\n"
"ds_write_addtid_b32 v210 offset:34304\n"
"s_waitcnt lgkmcnt(3)\n"
"v_mfma_f32_16x16x32_bf16 v[136:139], v[104:107], v[238:241], v[136:139]\n"
"ds_read_b128 v[156:159], v5 offset:20480\n"
"v_mfma_f32_16x16x32_bf16 v[144:147], v[108:111], v[238:241], v[144:147]\n"
"s_waitcnt lgkmcnt(1)\n"
"s_barrier\n"
"v_mfma_f32_16x16x32_bf16 v[132:135], v[152:155], v[238:241], v[132:135]\n"
"s_waitcnt lgkmcnt(0)\n"
"ds_read_b128 v[178:181], v6 offset:7168\n"
"v_mfma_f32_16x16x32_bf16 v[136:139], v[112:115], v[156:159], v[136:139]\n"
"ds_read_b128 v[208:211], v4 offset:22528\n"
"v_mfma_f32_16x16x32_bf16 v[144:147], v[116:119], v[156:159], v[144:147]\n"
"s_waitcnt lgkmcnt(1)\n"
"v_mfma_f32_16x16x32_bf16 v[132:135], v[178:181], v[156:159], v[132:135]\n"
"s_waitcnt lgkmcnt(0)\n"
"ds_read_b128 v[140:143], v6 offset:8192\n"
"v_mfma_f32_16x16x32_bf16 v[136:139], v[0:3], v[208:211], v[136:139]\n"
"ds_read_b128 v[238:241], v5 offset:22528\n"
"v_mfma_f32_16x16x32_bf16 v[144:147], v[120:123], v[208:211], v[144:147]\n"
"s_waitcnt lgkmcnt(1)\n"
"v_mfma_f32_16x16x32_bf16 v[132:135], v[140:143], v[208:211], v[132:135]\n"
"s_waitcnt lgkmcnt(0)\n"
"ds_read_b128 v[148:151], v6 offset:9216\n"
"ds_read_b128 v[152:155], v4 offset:36864\n"
"v_mfma_f32_16x16x32_bf16 v[136:139], v[124:127], v[238:241], v[136:139]\n"
"v_mfma_f32_16x16x32_bf16 v[144:147], v[128:131], v[238:241], v[144:147]\n"
"s_waitcnt lgkmcnt(1)\n"
"v_mfma_f32_16x16x32_bf16 v[132:135], v[148:151], v[238:241], v[132:135]\n"
"s_waitcnt lgkmcnt(0)\n"
"ds_read_b128 v[156:159], v5 offset:36864\n"
"v_mfma_f32_16x16x32_bf16 v[178:181], v[8:11], v[152:155], 0\n"
"v_mfma_f32_16x16x32_bf16 v[208:211], v[12:15], v[152:155], 0\n"
"v_mfma_f32_16x16x32_bf16 v[140:143], v[16:19], v[152:155], 0\n"
"s_waitcnt lgkmcnt(0)\n"
"ds_read_b128 v[160:163], v4 offset:38912\n"
"v_mfma_f32_16x16x32_bf16 v[178:181], v[20:23], v[156:159], v[178:181]\n"
"v_mul_f32 v136, s60, v136\n"
"v_mul_f32 v137, s60, v137\n"
"v_mfma_f32_16x16x32_bf16 v[208:211], v[28:31], v[156:159], v[208:211]\n"
"v_cvt_pk_bf16_f32 v136, v136, v137\n"
"v_mul_f32 v138, s60, v138\n"
"v_mfma_f32_16x16x32_bf16 v[140:143], v[32:35], v[156:159], v[140:143]\n"
"s_waitcnt lgkmcnt(0)\n"
"ds_read_b128 v[238:241], v5 offset:38912\n"
"v_mul_f32 v139, s60, v139\n"
"v_mfma_f32_16x16x32_bf16 v[178:181], v[36:39], v[160:163], v[178:181]\n"
"v_cvt_pk_bf16_f32 v137, v138, v139\n"
"v_mul_f32 v144, s60, v144\n"
"v_mfma_f32_16x16x32_bf16 v[208:211], v[40:43], v[160:163], v[208:211]\n"
"v_bfe_u32 v243, v6, 2, 6\n"
"v_mul_f32 v145, s60, v145\n"
"v_mfma_f32_16x16x32_bf16 v[140:143], v[44:47], v[160:163], v[140:143]\n"
"ds_read_b128 v[148:151], v4 offset:40960\n"
"v_mul_f32 v146, s60, v146\n"
"v_mul_f32 v147, s60, v147\n"
"v_cvt_pk_bf16_f32 v138, v144, v145\n"
"v_cvt_pk_bf16_f32 v139, v146, v147\n"
"v_mul_f32 v132, s60, v132\n"
"v_mul_f32 v133, s60, v133\n"
"v_mul_f32 v134, s60, v134\n"
"s_waitcnt lgkmcnt(1)\n"
"v_mfma_f32_16x16x32_bf16 v[178:181], v[48:51], v[238:241], v[178:181]\n"
"s_add_u32 s71, s50, 4294964592\n"
"ds_read_b32 v243, v243 offset:1856\n"
"v_mul_f32 v135, s60, v135\n"
"v_mfma_f32_16x16x32_bf16 v[208:211], v[52:55], v[238:241], v[208:211]\n"
"v_cvt_pk_bf16_f32 v26, v132, v133\n"
"v_cvt_pk_bf16_f32 v27, v134, v135\n"
"v_mfma_f32_16x16x32_bf16 v[140:143], v[56:59], v[238:241], v[140:143]\n"
"s_waitcnt lgkmcnt(1)\n"
"ds_read_b128 v[144:147], v5 offset:40960\n"
"v_mfma_f32_16x16x32_bf16 v[178:181], v[60:63], v[148:151], v[178:181]\n"
"v_mfma_f32_16x16x32_bf16 v[208:211], v[64:67], v[148:151], v[208:211]\n"
"s_add_u32 s72, s50, 4294965632\n"
"v_mfma_f32_16x16x32_bf16 v[140:143], v[68:71], v[148:151], v[140:143]\n"
"s_waitcnt lgkmcnt(1)\n"
"v_add_u32_sdwa v213, v243, v7 dst_sel:DWORD dst_unused:UNUSED_PAD src0_sel:WORD_0 src1_sel:DWORD\n"
"s_nop 0\n"
"v_max_u32 v212, s71, v213\n"
"ds_read_b128 v[238:241], v212 offset:2720\n"
"s_waitcnt lgkmcnt(1)\n"
"v_mfma_f32_16x16x32_bf16 v[178:181], v[72:75], v[144:147], v[178:181]\n"
"v_min_u32 v177, s72, v213\n"
"v_mfma_f32_16x16x32_bf16 v[208:211], v[76:79], v[144:147], v[208:211]\n"
"ds_read_b128 v[132:135], v212 offset:2736\n"
"v_mfma_f32_16x16x32_bf16 v[140:143], v[80:83], v[144:147], v[140:143]\n"
"s_waitcnt lgkmcnt(1)\n"
"v_mfma_f32_16x16x32_bf16 v[238:241], v[190:193], v[136:139], v[238:241]\n"
"ds_read_b128 v[148:151], v177 offset:4048\n"
"v_mfma_f32_16x16x32_bf16 v[238:241], v[192:195], v[24:27], v[238:241]\n"
"s_waitcnt lgkmcnt(1)\n"
"v_mfma_f32_16x16x32_bf16 v[132:135], v[196:199], v[136:139], v[132:135]\n"
"s_add_u32 s73, s50, 4294964288\n"
"ds_read_b128 v[152:155], v177 offset:4064\n"
"v_cvt_pk_bf16_f32 v178, v178, v179\n"
"v_cvt_pk_bf16_f32 v179, v180, v181\n"
"v_cvt_pk_bf16_f32 v180, v208, v209\n"
"v_cvt_pk_bf16_f32 v181, v210, v211\n"
"v_cvt_pk_bf16_f32 v182, v140, v141\n"
"v_cvt_pk_bf16_f32 v183, v142, v143\n"
"v_mfma_f32_16x16x32_bf16 v[132:135], v[198:201], v[24:27], v[132:135]\n"
"s_waitcnt lgkmcnt(1)\n"
"v_max3_f32 v242, v238, v239, v240\n"
"v_max_f32 v242, v242, v241\n"
"v_mfma_f32_16x16x32_bf16 v[148:151], v[214:217], v[136:139], v[148:151]\n"
"ds_read_b128 v[208:211], v177 offset:4080\n"
"v_min_u32 v176, s73, v213\n"
"v_mfma_f32_16x16x32_bf16 v[148:151], v[216:219], v[24:27], v[148:151]\n"
"s_mov_b64 exec, s[52:53]\n"
"v_add_u32 v176, 1040, v176\n"
"s_mov_b64 exec, -1\n"
"v_max3_f32 v242, v242, v132, v133\n"
"v_max3_f32 v242, v242, v134, v135\n"
"s_waitcnt lgkmcnt(1)\n"
"v_mfma_f32_16x16x32_bf16 v[152:155], v[220:223], v[136:139], v[152:155]\n"
"ds_read_b128 v[238:241], v212 offset:2704\n"
"v_mfma_f32_16x16x32_bf16 v[152:155], v[222:225], v[24:27], v[152:155]\n"
"s_waitcnt lgkmcnt(1)\n"
"v_max3_f32 v242, v242, v148, v149\n"
"v_max3_f32 v242, v242, v150, v151\n"
"v_mfma_f32_16x16x32_bf16 v[208:211], v[226:229], v[136:139], v[208:211]\n"
"ds_read_b128 v[132:135], v176 offset:5392\n"
"v_mfma_f32_16x16x32_bf16 v[208:211], v[228:231], v[24:27], v[208:211]\n"
"s_waitcnt lgkmcnt(1)\n"
"s_nop 0\n"
"v_max3_f32 v242, v242, v152, v153\n"
"v_max3_f32 v242, v242, v154, v155\n"
"v_mfma_f32_16x16x32_bf16 v[238:241], v[184:187], v[136:139], v[238:241]\n"
"ds_read_b128 v[140:143], v176 offset:5408\n"
"v_mfma_f32_16x16x32_bf16 v[238:241], v[186:189], v[24:27], v[238:241]\n"
"s_waitcnt lgkmcnt(1)\n"
"v_max3_f32 v242, v242, v208, v209\n"
"v_max3_f32 v242, v242, v210, v211\n"
"v_mfma_f32_16x16x32_bf16 v[132:135], v[244:247], v[136:139], v[132:135]\n"
"ds_read_b128 v[144:147], v176 offset:5424\n"
"v_mfma_f32_16x16x32_bf16 v[132:135], v[246:249], v[24:27], v[132:135]\n"
"s_waitcnt lgkmcnt(1)\n"
"s_nop 0\n"
"v_max3_f32 v242, v242, v238, v239\n"
"v_max3_f32 v242, v242, v240, v241\n"
"v_mfma_f32_16x16x32_bf16 v[140:143], v[250:253], v[136:139], v[140:143]\n"
"ds_read_b128 v[208:211], v212 offset:2720\n"
"v_mfma_f32_16x16x32_bf16 v[140:143], v[252:255], v[24:27], v[140:143]\n"
"s_waitcnt lgkmcnt(1)\n"
"v_max3_f32 v242, v242, v132, v133\n"
"v_max3_f32 v242, v242, v134, v135\n"
"v_mfma_f32_16x16x32_bf16 v[144:147], v[178:181], v[136:139], v[144:147]\n"
"ds_read_b128 v[238:241], v212 offset:2736\n"
"v_mfma_f32_16x16x32_bf16 v[144:147], v[180:183], v[24:27], v[144:147]\n"
"s_waitcnt lgkmcnt(1)\n"
"s_nop 0\n"
"v_max3_f32 v242, v242, v140, v141\n"
"v_max3_f32 v242, v242, v142, v143\n"
"v_mfma_f32_16x16x32_bf16 v[208:211], v[190:193], v[136:139], v[208:211]\n"
"ds_read_b128 v[132:135], v177 offset:4048\n"
"v_mfma_f32_16x16x32_bf16 v[208:211], v[192:195], v[24:27], v[208:211]\n"
"s_nop 0\n"
"v_max3_f32 v242, v242, v144, v145\n"
"v_max3_f32 v242, v242, v146, v147\n"
"v_mov_b32 v243, v242\n"
"s_nop 1\n"
"v_permlane16_swap_b32 v242, v243\n"
"v_max_f32 v242, v242, v243\n"
"v_mov_b32 v243, v242\n"
"s_nop 1\n"
"v_permlane32_swap_b32 v242, v243\n"
"v_max_f32 v242, v242, v243\n"
"v_sub_f32 v208, v208, v242\n"
"v_exp_f32 v208, v208\n"
"v_sub_f32 v209, v209, v242\n"
"v_exp_f32 v209, v209\n"
"v_sub_f32 v210, v210, v242\n"
"v_exp_f32 v210, v210\n"
"v_sub_f32 v211, v211, v242\n"
"v_exp_f32 v211, v211\n"
"s_waitcnt lgkmcnt(1)\n"
"v_mfma_f32_16x16x32_bf16 v[238:241], v[196:199], v[136:139], v[238:241]\n"
"ds_read_b128 v[140:143], v177 offset:4064\n"
"v_cvt_pk_bf16_f32 v208, v208, v209\n"
"v_mfma_f32_16x16x32_bf16 v[238:241], v[198:201], v[24:27], v[238:241]\n"
"ds_write_addtid_b32 v208 offset:53248\n"
"v_cvt_pk_bf16_f32 v209, v210, v211\n"
"ds_write_addtid_b32 v209 offset:53504\n"
"s_waitcnt lgkmcnt(3)\n"
"v_mfma_f32_16x16x32_bf16 v[132:135], v[214:217], v[136:139], v[132:135]\n"
"ds_read_b128 v[144:147], v177 offset:4080\n"
"v_mfma_f32_16x16x32_bf16 v[132:135], v[216:219], v[24:27], v[132:135]\n"
"s_nop 0\n"
"v_sub_f32 v238, v238, v242\n"
"v_exp_f32 v238, v238\n"
"v_sub_f32 v239, v239, v242\n"
"v_exp_f32 v239, v239\n"
"v_sub_f32 v240, v240, v242\n"
"v_exp_f32 v240, v240\n"
"v_sub_f32 v241, v241, v242\n"
"v_exp_f32 v241, v241\n"
"v_sub_f32 v132, v132, v242\n"
"v_exp_f32 v132, v132\n"
"v_sub_f32 v133, v133, v242\n"
"v_exp_f32 v133, v133\n"
"v_sub_f32 v134, v134, v242\n"
"v_exp_f32 v134, v134\n"
"v_sub_f32 v135, v135, v242\n"
"v_exp_f32 v135, v135\n"
"v_cvt_pk_bf16_f32 v238, v238, v239\n"
"v_cvt_pk_bf16_f32 v239, v240, v241\n"
"v_cvt_pk_bf16_f32 v132, v132, v133\n"
"v_cvt_pk_bf16_f32 v133, v134, v135\n"
"ds_write_addtid_b32 v238 offset:53760\n"
"ds_write_addtid_b32 v239 offset:54016\n"
"s_waitcnt lgkmcnt(5)\n"
"v_mfma_f32_16x16x32_bf16 v[140:143], v[220:223], v[136:139], v[140:143]\n"
"ds_read_b128 v[208:211], v212 offset:2704\n"
"v_mfma_f32_16x16x32_bf16 v[140:143], v[222:225], v[24:27], v[140:143]\n"
"ds_write_addtid_b32 v132 offset:54272\n"
"ds_write_addtid_b32 v133 offset:54528\n"
"s_waitcnt lgkmcnt(5)\n"
"v_mfma_f32_16x16x32_bf16 v[144:147], v[226:229], v[136:139], v[144:147]\n"
"ds_read_b128 v[238:241], v176 offset:5392\n"
"v_mfma_f32_16x16x32_bf16 v[144:147], v[228:231], v[24:27], v[144:147]\n"
"s_nop 1\n"
"v_sub_f32 v140, v140, v242\n"
"v_exp_f32 v140, v140\n"
"v_sub_f32 v141, v141, v242\n"
"v_exp_f32 v141, v141\n"
"v_sub_f32 v142, v142, v242\n"
"v_exp_f32 v142, v142\n"
"v_sub_f32 v143, v143, v242\n"
"v_exp_f32 v143, v143\n"
"v_sub_f32 v144, v144, v242\n"
"v_exp_f32 v144, v144\n"
"v_sub_f32 v145, v145, v242\n"
"v_exp_f32 v145, v145\n"
"v_sub_f32 v146, v146, v242\n"
"v_exp_f32 v146, v146\n"
"v_cvt_pk_bf16_f32 v140, v140, v141\n"
"v_sub_f32 v147, v147, v242\n"
"v_exp_f32 v147, v147\n"
"v_cvt_pk_bf16_f32 v141, v142, v143\n"
"v_cvt_pk_bf16_f32 v144, v144, v145\n"
"v_cvt_pk_bf16_f32 v145, v146, v147\n"
"ds_write_addtid_b32 v140 offset:54784\n"
"ds_write_addtid_b32 v141 offset:55040\n"
"s_waitcnt lgkmcnt(5)\n"
"v_mfma_f32_16x16x32_bf16 v[208:211], v[184:187], v[136:139], v[208:211]\n"
"ds_write_addtid_b32 v144 offset:33792\n"
"v_mfma_f32_16x16x32_bf16 v[208:211], v[186:189], v[24:27], v[208:211]\n"
"ds_write_addtid_b32 v145 offset:34048\n"
"ds_read_b128 v[132:135], v176 offset:5408\n"
"s_waitcnt lgkmcnt(5)\n"
"v_mfma_f32_16x16x32_bf16 v[238:241], v[244:247], v[136:139], v[238:241]\n"
"ds_read_b128 v[140:143], v176 offset:5424\n"
"v_mfma_f32_16x16x32_bf16 v[238:241], v[246:249], v[24:27], v[238:241]\n"
"s_waitcnt lgkmcnt(2)\n"
"s_barrier\n"
"v_sub_f32 v208, v208, v242\n"
"v_exp_f32 v208, v208\n"
"v_sub_f32 v209, v209, v242\n"
"v_exp_f32 v209, v209\n"
"v_sub_f32 v210, v210, v242\n"
"v_exp_f32 v210, v210\n"
"v_sub_f32 v211, v211, v242\n"
"v_exp_f32 v211, v211\n"
"v_sub_f32 v238, v238, v242\n"
"v_exp_f32 v238, v238\n"
"v_cvt_pk_bf16_f32 v208, v208, v209\n"
"v_sub_f32 v239, v239, v242\n"
"v_exp_f32 v239, v239\n"
"v_cvt_pk_bf16_f32 v209, v210, v211\n"
"v_sub_f32 v240, v240, v242\n"
"v_exp_f32 v240, v240\n"
"v_sub_f32 v241, v241, v242\n"
"v_exp_f32 v241, v241\n"
"v_cvt_pk_bf16_f32 v238, v238, v239\n"
"v_cvt_pk_bf16_f32 v239, v240, v241\n"
"ds_write_addtid_b32 v208 offset:57856\n"
"ds_write_addtid_b32 v209 offset:58112\n"
"s_waitcnt lgkmcnt(3)\n"
"v_mfma_f32_16x16x32_bf16 v[132:135], v[250:253], v[136:139], v[132:135]\n"
"ds_read_b128 v[184:187], v4 offset:43008\n"
"v_mfma_f32_16x16x32_bf16 v[132:135], v[252:255], v[24:27], v[132:135]\n"
"ds_write_addtid_b32 v238 offset:58368\n"
"ds_write_addtid_b32 v239 offset:58624\n"
"s_waitcnt lgkmcnt(5)\n"
"v_mfma_f32_16x16x32_bf16 v[140:143], v[178:181], v[136:139], v[140:143]\n"
"v_mfma_f32_16x16x32_bf16 v[140:143], v[180:183], v[24:27], v[140:143]\n"
"s_waitcnt lgkmcnt(2)\n"
"ds_read_b128 v[208:211], v5 offset:43008\n"
"v_mfma_f32_16x16x32_bf16 v[144:147], v[84:87], v[184:187], 0\n"
"v_mfma_f32_16x16x32_bf16 v[148:151], v[88:91], v[184:187], 0\n"
"ds_read_b128 v[152:155], v6 offset:5120\n"
"v_sub_f32 v132, v132, v242\n"
"v_exp_f32 v132, v132\n"
"v_sub_f32 v133, v133, v242\n"
"v_exp_f32 v133, v133\n"
"v_sub_f32 v134, v134, v242\n"
"v_exp_f32 v134, v134\n"
"v_mfma_f32_16x16x32_bf16 v[238:241], v[92:95], v[184:187], 0\n"
"ds_read_b128 v[136:139], v4 offset:45056\n"
"v_cvt_pk_bf16_f32 v132, v132, v133\n"
"v_sub_f32 v135, v135, v242\n"
"v_exp_f32 v135, v135\n"
"s_waitcnt lgkmcnt(2)\n"
"v_mfma_f32_16x16x32_bf16 v[144:147], v[96:99], v[208:211], v[144:147]\n"
"ds_write_addtid_b32 v132 offset:58880\n"
"v_cvt_pk_bf16_f32 v133, v134, v135\n"
"v_mfma_f32_16x16x32_bf16 v[148:151], v[100:103], v[208:211], v[148:151]\n"
"ds_write_addtid_b32 v133 offset:59136\n"
"s_waitcnt lgkmcnt(3)\n"
"v_mfma_f32_16x16x32_bf16 v[238:241], v[152:155], v[208:211], v[238:241]\n"
"s_waitcnt lgkmcnt(2)\n"
"ds_read_b128 v[184:187], v6 offset:6144\n"
"v_sub_f32 v140, v140, v242\n"
"v_exp_f32 v140, v140\n"
"v_mfma_f32_16x16x32_bf16 v[144:147], v[104:107], v[136:139], v[144:147]\n"
"ds_read_b128 v[156:159], v5 offset:45056\n"
"v_sub_f32 v141, v141, v242\n"
"v_exp_f32 v141, v141\n"
"v_mfma_f32_16x16x32_bf16 v[148:151], v[108:111], v[136:139], v[148:151]\n"
"ds_read_b128 v[208:211], v6 offset:7168\n"
"v_sub_f32 v142, v142, v242\n"
"v_exp_f32 v142, v142\n"
"v_sub_f32 v143, v143, v242\n"
"v_exp_f32 v143, v143\n"
"s_waitcnt lgkmcnt(2)\n"
"v_mfma_f32_16x16x32_bf16 v[238:241], v[184:187], v[136:139], v[238:241]\n"
"s_waitcnt lgkmcnt(1)\n"
"ds_read_b128 v[132:135], v4 offset:47104\n"
"v_cvt_pk_bf16_f32 v140, v140, v141\n"
"v_cvt_pk_bf16_f32 v141, v142, v143\n"
"v_mfma_f32_16x16x32_bf16 v[144:147], v[112:115], v[156:159], v[144:147]\n"
"ds_write_addtid_b32 v140 offset:57344\n"
"v_mfma_f32_16x16x32_bf16 v[148:151], v[116:119], v[156:159], v[148:151]\n"
"ds_write_addtid_b32 v141 offset:57600\n"
"s_waitcnt lgkmcnt(3)\n"
"v_mfma_f32_16x16x32_bf16 v[238:241], v[208:211], v[156:159], v[238:241]\n"
"s_waitcnt lgkmcnt(2)\n"
"ds_read_b128 v[136:139], v6 offset:8192\n"
"ds_read_b128 v[152:155], v5 offset:47104\n"
"v_mfma_f32_16x16x32_bf16 v[144:147], v[0:3], v[132:135], v[144:147]\n"
"v_mfma_f32_16x16x32_bf16 v[148:151], v[120:123], v[132:135], v[148:151]\n"
"s_waitcnt lgkmcnt(2)\n"
"ds_read_b128 v[160:163], v6 offset:9216\n"
"s_barrier\n"
"s_waitcnt lgkmcnt(2)\n"
"v_mfma_f32_16x16x32_bf16 v[238:241], v[136:139], v[132:135], v[238:241]\n"
"s_waitcnt lgkmcnt(1)\n"
"ds_read_b128 v[208:211], v4 offset:24576\n"
"v_mfma_f32_16x16x32_bf16 v[144:147], v[124:127], v[152:155], v[144:147]\n"
"v_mfma_f32_16x16x32_bf16 v[148:151], v[128:131], v[152:155], v[148:151]\n"
"s_waitcnt lgkmcnt(1)\n"
"v_mfma_f32_16x16x32_bf16 v[238:241], v[160:163], v[152:155], v[238:241]\n"
"s_waitcnt lgkmcnt(0)\n"
"ds_read_b128 v[140:143], v5 offset:24576\n"
"v_mfma_f32_16x16x32_bf16 v[184:187], v[8:11], v[208:211], 0\n"
"v_mfma_f32_16x16x32_bf16 v[132:135], v[12:15], v[208:211], 0\n"
"v_mfma_f32_16x16x32_bf16 v[136:139], v[16:19], v[208:211], 0\n"
"s_waitcnt lgkmcnt(0)\n"
"ds_read_b128 v[156:159], v4 offset:26624\n"
"v_mfma_f32_16x16x32_bf16 v[184:187], v[20:23], v[140:143], v[184:187]\n"
"v_mul_f32 v144, s60, v144\n"
"v_mul_f32 v145, s60, v145\n"
"v_mfma_f32_16x16x32_bf16 v[132:135], v[28:31], v[140:143], v[132:135]\n"
"v_cvt_pk_bf16_f32 v144, v144, v145\n"
"v_mul_f32 v146, s60, v146\n"
"v_mfma_f32_16x16x32_bf16 v[136:139], v[32:35], v[140:143], v[136:139]\n"
"ds_read_b128 v[208:211], v5 offset:26624\n"
"v_mul_f32 v147, s60, v147\n"
"v_cvt_pk_bf16_f32 v145, v146, v147\n"
"v_mul_f32 v148, s60, v148\n"
"v_mul_f32 v149, s60, v149\n"
"v_mul_f32 v150, s60, v150\n"
"v_mul_f32 v151, s60, v151\n"
"v_cvt_pk_bf16_f32 v146, v148, v149\n"
"s_waitcnt lgkmcnt(1)\n"
"v_mfma_f32_16x16x32_bf16 v[184:187], v[36:39], v[156:159], v[184:187]\n"
"v_cvt_pk_bf16_f32 v147, v150, v151\n"
"v_mul_f32 v238, s60, v238\n"
"v_mfma_f32_16x16x32_bf16 v[132:135], v[40:43], v[156:159], v[132:135]\n"
"v_bfe_u32 v213, v6, 2, 6\n"
"v_mul_f32 v239, s60, v239\n"
"v_mfma_f32_16x16x32_bf16 v[136:139], v[44:47], v[156:159], v[136:139]\n"
"s_waitcnt lgkmcnt(0)\n"
"ds_read_b128 v[8:11], v4 offset:28672\n"
"v_mul_f32 v240, s60, v240\n"
"v_mfma_f32_16x16x32_bf16 v[184:187], v[48:51], v[208:211], v[184:187]\n"
"s_add_u32 s68, s50, 4294964576\n"
"ds_read_b32 v213, v213 offset:1920\n"
"v_mul_f32 v241, s60, v241\n"
"v_mfma_f32_16x16x32_bf16 v[132:135], v[52:55], v[208:211], v[132:135]\n"
"v_cvt_pk_bf16_f32 v26, v238, v239\n"
"v_cvt_pk_bf16_f32 v27, v240, v241\n"
"v_mfma_f32_16x16x32_bf16 v[136:139], v[56:59], v[208:211], v[136:139]\n"
"s_waitcnt lgkmcnt(1)\n"
"ds_read_b128 v[12:15], v5 offset:28672\n"
"v_mfma_f32_16x16x32_bf16 v[184:187], v[60:63], v[8:11], v[184:187]\n"
"v_mfma_f32_16x16x32_bf16 v[132:135], v[64:67], v[8:11], v[132:135]\n"
"s_waitcnt lgkmcnt(1)\n"
"v_add_u32_sdwa v212, v213, v7 dst_sel:DWORD dst_unused:UNUSED_PAD src0_sel:WORD_0 src1_sel:DWORD\n"
"s_nop 0\n"
"v_max_u32 v239, s68, v212\n"
"v_mfma_f32_16x16x32_bf16 v[136:139], v[68:71], v[8:11], v[136:139]\n"
"s_waitcnt lgkmcnt(0)\n"
"ds_read_b128 v[208:211], v239 offset:2720\n"
"v_add_u32_sdwa v213, sext(v213), v212 dst_sel:DWORD dst_unused:UNUSED_PAD src0_sel:WORD_1 src1_sel:DWORD\n"
"v_mfma_f32_16x16x32_bf16 v[184:187], v[72:75], v[12:15], v[184:187]\n"
"s_add_u32 s69, s50, 4294963232\n"
"v_max_u32 v238, s68, v213\n"
"v_mfma_f32_16x16x32_bf16 v[132:135], v[76:79], v[12:15], v[132:135]\n"
"s_add_u32 s70, s50, 4294964272\n"
"ds_read_b128 v[240:243], v239 offset:2736\n"
"v_mfma_f32_16x16x32_bf16 v[136:139], v[80:83], v[12:15], v[136:139]\n"
"s_waitcnt lgkmcnt(1)\n"
"v_max_u32 v177, s69, v213\n"
"v_mfma_f32_16x16x32_bf16 v[208:211], v[190:193], v[144:147], v[208:211]\n"
"ds_read_b128 v[8:11], v238 offset:2752\n"
"v_cvt_pk_bf16_f32 v184, v184, v185\n"
"v_cvt_pk_bf16_f32 v185, v186, v187\n"
"v_cvt_pk_bf16_f32 v186, v132, v133\n"
"v_cvt_pk_bf16_f32 v187, v134, v135\n"
"v_mfma_f32_16x16x32_bf16 v[208:211], v[192:195], v[24:27], v[208:211]\n"
"s_waitcnt lgkmcnt(1)\n"
"v_cvt_pk_bf16_f32 v188, v136, v137\n"
"v_cvt_pk_bf16_f32 v189, v138, v139\n"
"v_mfma_f32_16x16x32_bf16 v[240:243], v[196:199], v[144:147], v[240:243]\n"
"ds_read_b128 v[12:15], v177 offset:4096\n"
"v_min_u32 v176, s70, v212\n"
"v_mfma_f32_16x16x32_bf16 v[240:243], v[198:201], v[24:27], v[240:243]\n"
"v_min_u32 v175, s70, v213\n"
"s_mov_b64 exec, s[52:53]\n"
"v_add_u32 v176, 1040, v176\n"
"v_add_u32 v175, 1040, v175\n"
"s_mov_b64 exec, -1\n"
"v_max3_f32 v172, v208, v209, v210\n"
"v_max_f32 v172, v172, v211\n"
"s_waitcnt lgkmcnt(1)\n"
"v_mfma_f32_16x16x32_bf16 v[8:11], v[202:205], v[144:147], v[8:11]\n"
"ds_read_b128 v[16:19], v176 offset:5408\n"
"v_max_u32 v174, s69, v212\n"
"v_mfma_f32_16x16x32_bf16 v[8:11], v[204:207], v[24:27], v[8:11]\n"
"s_waitcnt lgkmcnt(1)\n"
"v_max3_f32 v172, v172, v240, v241\n"
"v_max3_f32 v172, v172, v242, v243\n"
"v_mfma_f32_16x16x32_bf16 v[12:15], v[232:235], v[144:147], v[12:15]\n"
"ds_read_b128 v[208:211], v174 offset:4064\n"
"v_mfma_f32_16x16x32_bf16 v[12:15], v[234:237], v[24:27], v[12:15]\n"
"s_waitcnt lgkmcnt(1)\n"
"s_nop 0\n"
"v_max3_f32 v172, v172, v8, v9\n"
"v_max3_f32 v172, v172, v10, v11\n"
"v_mfma_f32_16x16x32_bf16 v[16:19], v[250:253], v[144:147], v[16:19]\n"
"ds_read_b128 v[240:243], v174 offset:4080\n"
"v_mfma_f32_16x16x32_bf16 v[16:19], v[252:255], v[24:27], v[16:19]\n"
"s_waitcnt lgkmcnt(1)\n"
"v_max3_f32 v172, v172, v12, v13\n"
"v_max3_f32 v172, v172, v14, v15\n"
"v_mfma_f32_16x16x32_bf16 v[208:211], v[220:223], v[144:147], v[208:211]\n"
"ds_read_b128 v[8:11], v176 offset:5424\n"
"v_mfma_f32_16x16x32_bf16 v[208:211], v[222:225], v[24:27], v[208:211]\n"
"s_waitcnt lgkmcnt(1)\n"
"s_nop 0\n"
"v_max3_f32 v172, v172, v16, v17\n"
"v_max3_f32 v172, v172, v18, v19\n"
"v_mfma_f32_16x16x32_bf16 v[240:243], v[226:229], v[144:147], v[240:243]\n"
"ds_read_b128 v[12:15], v175 offset:5440\n"
"v_mfma_f32_16x16x32_bf16 v[240:243], v[228:231], v[24:27], v[240:243]\n"
"s_waitcnt lgkmcnt(1)\n"
"v_max3_f32 v172, v172, v208, v209\n"
"v_max3_f32 v172, v172, v210, v211\n"
"v_mfma_f32_16x16x32_bf16 v[8:11], v[178:181], v[144:147], v[8:11]\n"
"ds_read_b128 v[16:19], v239 offset:2720\n"
"v_mfma_f32_16x16x32_bf16 v[8:11], v[180:183], v[24:27], v[8:11]\n"
"s_waitcnt lgkmcnt(1)\n"
"s_nop 0\n"
"v_max3_f32 v172, v172, v240, v241\n"
"v_max3_f32 v172, v172, v242, v243\n"
"v_mfma_f32_16x16x32_bf16 v[12:15], v[184:187], v[144:147], v[12:15]\n"
"ds_read_b128 v[208:211], v239 offset:2736\n"
"v_mfma_f32_16x16x32_bf16 v[12:15], v[186:189], v[24:27], v[12:15]\n"
"s_waitcnt lgkmcnt(1)\n"
"v_max3_f32 v172, v172, v8, v9\n"
"v_max3_f32 v172, v172, v10, v11\n"
"v_mfma_f32_16x16x32_bf16 v[16:19], v[190:193], v[144:147], v[16:19]\n"
"ds_read_b128 v[240:243], v238 offset:2752\n"
"v_mfma_f32_16x16x32_bf16 v[16:19], v[192:195], v[24:27], v[16:19]\n"
"s_nop 1\n"
"v_max3_f32 v172, v172, v12, v13\n"
"v_max3_f32 v172, v172, v14, v15\n"
"v_mov_b32 v173, v172\n"
"s_nop 1\n"
"v_permlane16_swap_b32 v172, v173\n"
"v_max_f32 v172, v172, v173\n"
"v_mov_b32 v173, v172\n"
"s_nop 1\n"
"v_permlane32_swap_b32 v172, v173\n"
"v_max_f32 v172, v172, v173\n"
"v_sub_f32 v18, v18, v172\n"
"v_exp_f32 v18, v18\n"
"v_sub_f32 v19, v19, v172\n"
"v_exp_f32 v19, v19\n"
"s_waitcnt lgkmcnt(1)\n"
"v_mfma_f32_16x16x32_bf16 v[208:211], v[196:199], v[144:147], v[208:211]\n"
"ds_read_b128 v[190:193], v177 offset:4096\n"
"v_cvt_pk_bf16_f32 v17, v18, v19\n"
"v_mfma_f32_16x16x32_bf16 v[208:211], v[198:201], v[24:27], v[208:211]\n"
"ds_write_addtid_b32 v17 offset:55552\n"
"s_waitcnt lgkmcnt(2)\n"
"v_mfma_f32_16x16x32_bf16 v[240:243], v[202:205], v[144:147], v[240:243]\n"
"ds_read_b128 v[8:11], v176 offset:5408\n"
"v_mfma_f32_16x16x32_bf16 v[240:243], v[204:207], v[24:27], v[240:243]\n"
"s_nop 2\n"
"v_sub_f32 v208, v208, v172\n"
"v_exp_f32 v208, v208\n"
"v_sub_f32 v209, v209, v172\n"
"v_exp_f32 v209, v209\n"
"v_sub_f32 v210, v210, v172\n"
"v_exp_f32 v210, v210\n"
"v_sub_f32 v211, v211, v172\n"
"v_exp_f32 v211, v211\n"
"v_sub_f32 v240, v240, v172\n"
"v_exp_f32 v240, v240\n"
"v_sub_f32 v241, v241, v172\n"
"v_exp_f32 v241, v241\n"
"v_sub_f32 v242, v242, v172\n"
"v_exp_f32 v242, v242\n"
"v_cvt_pk_bf16_f32 v208, v208, v209\n"
"v_cvt_pk_bf16_f32 v209, v210, v211\n"
"v_sub_f32 v243, v243, v172\n"
"v_exp_f32 v243, v243\n"
"v_cvt_pk_bf16_f32 v240, v240, v241\n"
"v_cvt_pk_bf16_f32 v241, v242, v243\n"
"ds_write_addtid_b32 v208 offset:55808\n"
"ds_write_addtid_b32 v209 offset:56064\n"
"s_waitcnt lgkmcnt(4)\n"
"v_mfma_f32_16x16x32_bf16 v[190:193], v[232:235], v[144:147], v[190:193]\n"
"ds_read_b128 v[194:197], v174 offset:4064\n"
"v_mfma_f32_16x16x32_bf16 v[190:193], v[234:237], v[24:27], v[190:193]\n"
"ds_write_addtid_b32 v240 offset:56320\n"
"ds_write_addtid_b32 v241 offset:56576\n"
"s_waitcnt lgkmcnt(5)\n"
"v_mfma_f32_16x16x32_bf16 v[8:11], v[250:253], v[144:147], v[8:11]\n"
"ds_read_b128 v[198:201], v174 offset:4080\n"
"v_mfma_f32_16x16x32_bf16 v[8:11], v[252:255], v[24:27], v[8:11]\n"
"s_nop 1\n"
"v_sub_f32 v190, v190, v172\n"
"v_exp_f32 v190, v190\n"
"v_sub_f32 v191, v191, v172\n"
"v_exp_f32 v191, v191\n"
"v_sub_f32 v192, v192, v172\n"
"v_exp_f32 v192, v192\n"
"v_sub_f32 v193, v193, v172\n"
"v_exp_f32 v193, v193\n"
"v_sub_f32 v10, v10, v172\n"
"v_exp_f32 v10, v10\n"
"v_cvt_pk_bf16_f32 v190, v190, v191\n"
"v_cvt_pk_bf16_f32 v191, v192, v193\n"
"v_sub_f32 v11, v11, v172\n"
"v_exp_f32 v11, v11\n"
"ds_write_addtid_b32 v190 offset:56832\n"
"v_cvt_pk_bf16_f32 v9, v10, v11\n"
"ds_write_addtid_b32 v191 offset:57088\n"
"s_waitcnt lgkmcnt(5)\n"
"v_mfma_f32_16x16x32_bf16 v[194:197], v[220:223], v[144:147], v[194:197]\n"
"ds_read_b128 v[202:205], v176 offset:5424\n"
"v_mfma_f32_16x16x32_bf16 v[194:197], v[222:225], v[24:27], v[194:197]\n"
"ds_write_addtid_b32 v9 offset:34560\n"
"s_waitcnt lgkmcnt(4)\n"
"v_mfma_f32_16x16x32_bf16 v[198:201], v[226:229], v[144:147], v[198:201]\n"
"ds_read_b128 v[206:209], v175 offset:5440\n"
"v_mfma_f32_16x16x32_bf16 v[198:201], v[228:231], v[24:27], v[198:201]\n"
"s_waitcnt lgkmcnt(1)\n"
"s_barrier\n"
"s_nop 0\n"
"v_sub_f32 v196, v196, v172\n"
"v_exp_f32 v196, v196\n"
"v_sub_f32 v197, v197, v172\n"
"v_exp_f32 v197, v197\n"
"s_nop 0\n"
"v_cvt_pk_bf16_f32 v195, v196, v197\n"
"ds_write_addtid_b32 v195 offset:58624\n"
"v_mfma_f32_16x16x32_bf16 v[202:205], v[178:181], v[144:147], v[202:205]\n"
"ds_read_b128 v[190:193], v4 offset:30720\n"
"v_sub_f32 v198, v198, v172\n"
"v_exp_f32 v198, v198\n"
"v_sub_f32 v199, v199, v172\n"
"v_exp_f32 v199, v199\n"
"v_sub_f32 v200, v200, v172\n"
"v_exp_f32 v200, v200\n"
"v_sub_f32 v201, v201, v172\n"
"v_exp_f32 v201, v201\n"
"v_mfma_f32_16x16x32_bf16 v[202:205], v[180:183], v[24:27], v[202:205]\n"
"v_cvt_pk_bf16_f32 v198, v198, v199\n"
"v_cvt_pk_bf16_f32 v199, v200, v201\n"
"ds_write_addtid_b32 v198 offset:58880\n"
"ds_write_addtid_b32 v199 offset:59136\n"
"s_waitcnt lgkmcnt(4)\n"
"v_mfma_f32_16x16x32_bf16 v[206:209], v[184:187], v[144:147], v[206:209]\n"
"v_mfma_f32_16x16x32_bf16 v[206:209], v[186:189], v[24:27], v[206:209]\n"
"s_waitcnt lgkmcnt(2)\n"
"ds_read_b128 v[194:197], v5 offset:30720\n"
"v_mfma_f32_16x16x32_bf16 v[210:213], v[84:87], v[190:193], 0\n"
"v_sub_f32 v202, v202, v172\n"
"v_exp_f32 v202, v202\n"
"v_sub_f32 v203, v203, v172\n"
"v_exp_f32 v203, v203\n"
"v_mfma_f32_16x16x32_bf16 v[184:187], v[88:91], v[190:193], 0\n"
"ds_read_b128 v[198:201], v6 offset:5120\n"
"v_cvt_pk_bf16_f32 v202, v202, v203\n"
"v_sub_f32 v204, v204, v172\n"
"v_exp_f32 v204, v204\n"
"v_mfma_f32_16x16x32_bf16 v[232:235], v[92:95], v[190:193], 0\n"
"s_waitcnt lgkmcnt(1)\n"
"ds_read_b128 v[236:239], v4 offset:32768\n"
"v_sub_f32 v205, v205, v172\n"
"v_exp_f32 v205, v205\n"
"v_mfma_f32_16x16x32_bf16 v[210:213], v[96:99], v[194:197], v[210:213]\n"
"ds_write_addtid_b32 v202 offset:57344\n"
"v_cvt_pk_bf16_f32 v203, v204, v205\n"
"v_mfma_f32_16x16x32_bf16 v[184:187], v[100:103], v[194:197], v[184:187]\n"
"ds_write_addtid_b32 v203 offset:57600\n"
"ds_read_b128 v[188:191], v6 offset:6144\n"
"s_waitcnt lgkmcnt(4)\n"
"v_mfma_f32_16x16x32_bf16 v[232:235], v[198:201], v[194:197], v[232:235]\n"
"s_waitcnt lgkmcnt(3)\n"
"ds_read_b128 v[240:243], v5 offset:32768\n"
"v_sub_f32 v206, v206, v172\n"
"v_exp_f32 v206, v206\n"
"v_mfma_f32_16x16x32_bf16 v[210:213], v[104:107], v[236:239], v[210:213]\n"
"v_sub_f32 v207, v207, v172\n"
"v_exp_f32 v207, v207\n"
"v_mfma_f32_16x16x32_bf16 v[184:187], v[108:111], v[236:239], v[184:187]\n"
"ds_read_b128 v[192:195], v6 offset:7168\n"
"v_sub_f32 v208, v208, v172\n"
"v_exp_f32 v208, v208\n"
"v_cvt_pk_bf16_f32 v206, v206, v207\n"
"s_waitcnt lgkmcnt(2)\n"
"v_mfma_f32_16x16x32_bf16 v[232:235], v[188:191], v[236:239], v[232:235]\n"
"ds_read_b128 v[196:199], v4 offset:34816\n"
"v_bfe_u32 v4, v6, 2, 6\n"
"v_sub_f32 v209, v209, v172\n"
"v_exp_f32 v209, v209\n"
"s_waitcnt lgkmcnt(2)\n"
"v_mfma_f32_16x16x32_bf16 v[210:213], v[112:115], v[240:243], v[210:213]\n"
"s_add_u32 s71, s50, 4294963248\n"
"ds_write_addtid_b32 v206 offset:57856\n"
"v_cvt_pk_bf16_f32 v207, v208, v209\n"
"v_mfma_f32_16x16x32_bf16 v[184:187], v[116:119], v[240:243], v[184:187]\n"
"ds_write_addtid_b32 v207 offset:58112\n"
"s_waitcnt lgkmcnt(3)\n"
"v_mfma_f32_16x16x32_bf16 v[232:235], v[192:195], v[240:243], v[232:235]\n"
"s_waitcnt lgkmcnt(2)\n"
"ds_read_b32 v4, v4 offset:1984\n"
"ds_read_b128 v[188:191], v6 offset:8192\n"
"ds_read_b128 v[200:203], v5 offset:34816\n"
"v_mfma_f32_16x16x32_bf16 v[210:213], v[0:3], v[196:199], v[210:213]\n"
"s_add_u32 s72, s50, 4294964288\n"
"v_mfma_f32_16x16x32_bf16 v[184:187], v[120:123], v[196:199], v[184:187]\n"
"s_waitcnt lgkmcnt(2)\n"
"ds_read_b128 v[192:195], v6 offset:9216\n"
"v_add_u32_sdwa v5, v4, v7 dst_sel:DWORD dst_unused:UNUSED_PAD src0_sel:WORD_0 src1_sel:DWORD\n"
"s_nop 0\n"
"v_max_u32 v209, s71, v5\n"
"s_waitcnt lgkmcnt(2)\n"
"v_mfma_f32_16x16x32_bf16 v[232:235], v[188:191], v[196:199], v[232:235]\n"
"s_waitcnt lgkmcnt(1)\n"
"ds_read_b128 v[204:207], v209 offset:4048\n"
"v_min_u32 v208, s72, v5\n"
"v_mfma_f32_16x16x32_bf16 v[210:213], v[124:127], v[200:203], v[210:213]\n"
"s_mov_b64 exec, s[52:53]\n"
"v_add_u32 v208, 1040, v208\n"
"s_mov_b64 exec, -1\n"
"v_mfma_f32_16x16x32_bf16 v[184:187], v[128:131], v[200:203], v[184:187]\n"
"s_waitcnt lgkmcnt(1)\n"
"s_barrier\n"
"ds_read_b128 v[188:191], v209 offset:4064\n"
"v_mfma_f32_16x16x32_bf16 v[232:235], v[192:195], v[200:203], v[232:235]\n"
"v_mul_f32 v210, s60, v210\n"
"v_mul_f32 v211, s60, v211\n"
"v_cvt_pk_bf16_f32 v210, v210, v211\n"
"v_mul_f32 v212, s60, v212\n"
"v_mul_f32 v213, s60, v213\n"
"v_cvt_pk_bf16_f32 v211, v212, v213\n"
"v_mul_f32 v184, s60, v184\n"
"v_mul_f32 v185, s60, v185\n"
"v_mul_f32 v186, s60, v186\n"
"v_mul_f32 v187, s60, v187\n"
"v_cvt_pk_bf16_f32 v212, v184, v185\n"
"v_cvt_pk_bf16_f32 v213, v186, v187\n"
"v_mul_f32 v232, s60, v232\n"
"v_mul_f32 v233, s60, v233\n"
"s_waitcnt lgkmcnt(1)\n"
"v_mfma_f32_16x16x32_bf16 v[204:207], v[214:217], v[210:213], v[204:207]\n"
"ds_read_b128 v[192:195], v208 offset:5392\n"
"v_mul_f32 v234, s60, v234\n"
"v_mul_f32 v235, s60, v235\n"
"v_cvt_pk_bf16_f32 v26, v232, v233\n"
"v_cvt_pk_bf16_f32 v27, v234, v235\n"
"s_nop 1\n"
"v_mfma_f32_16x16x32_bf16 v[204:207], v[216:219], v[24:27], v[204:207]\n"
"s_waitcnt lgkmcnt(1)\n"
"v_mfma_f32_16x16x32_bf16 v[188:191], v[220:223], v[210:213], v[188:191]\n"
"ds_read_b128 v[184:187], v209 offset:4080\n"
"v_mfma_f32_16x16x32_bf16 v[188:191], v[222:225], v[24:27], v[188:191]\n"
"s_waitcnt lgkmcnt(1)\n"
"s_nop 2\n"
"v_max3_f32 v176, v204, v205, v206\n"
"v_max_f32 v176, v176, v207\n"
"v_mfma_f32_16x16x32_bf16 v[192:195], v[244:247], v[210:213], v[192:195]\n"
"ds_read_b128 v[196:199], v208 offset:5408\n"
"v_mfma_f32_16x16x32_bf16 v[192:195], v[246:249], v[24:27], v[192:195]\n"
"s_waitcnt lgkmcnt(1)\n"
"v_max3_f32 v176, v176, v188, v189\n"
"v_max3_f32 v176, v176, v190, v191\n"
"v_mfma_f32_16x16x32_bf16 v[184:187], v[226:229], v[210:213], v[184:187]\n"
"ds_read_b128 v[200:203], v208 offset:5424\n"
"v_mfma_f32_16x16x32_bf16 v[184:187], v[228:231], v[24:27], v[184:187]\n"
"s_waitcnt lgkmcnt(1)\n"
"s_nop 0\n"
"v_max3_f32 v176, v176, v192, v193\n"
"v_max3_f32 v176, v176, v194, v195\n"
"v_mfma_f32_16x16x32_bf16 v[196:199], v[250:253], v[210:213], v[196:199]\n"
"ds_read_b128 v[188:191], v209 offset:4048\n"
"v_mfma_f32_16x16x32_bf16 v[196:199], v[252:255], v[24:27], v[196:199]\n"
"s_waitcnt lgkmcnt(1)\n"
"v_max3_f32 v176, v176, v184, v185\n"
"v_max3_f32 v176, v176, v186, v187\n"
"v_mfma_f32_16x16x32_bf16 v[200:203], v[178:181], v[210:213], v[200:203]\n"
"ds_read_b128 v[192:195], v209 offset:4064\n"
"v_mfma_f32_16x16x32_bf16 v[200:203], v[180:183], v[24:27], v[200:203]\n"
"s_waitcnt lgkmcnt(1)\n"
"s_nop 0\n"
"v_max3_f32 v176, v176, v196, v197\n"
"v_max3_f32 v176, v176, v198, v199\n"
"v_mfma_f32_16x16x32_bf16 v[188:191], v[214:217], v[210:213], v[188:191]\n"
"ds_read_b128 v[184:187], v208 offset:5392\n"
"v_mfma_f32_16x16x32_bf16 v[188:191], v[216:219], v[24:27], v[188:191]\n"
"s_nop 0\n"
"v_max3_f32 v176, v176, v200, v201\n"
"v_max3_f32 v176, v176, v202, v203\n"
"v_mov_b32 v177, v176\n"
"s_nop 1\n"
"v_permlane16_swap_b32 v176, v177\n"
"v_max_f32 v176, v176, v177\n"
"v_mov_b32 v177, v176\n"
"s_nop 1\n"
"v_permlane32_swap_b32 v176, v177\n"
"v_max_f32 v176, v176, v177\n"
"v_sub_f32 v188, v188, v176\n"
"v_exp_f32 v188, v188\n"
"v_sub_f32 v189, v189, v176\n"
"v_exp_f32 v189, v189\n"
"v_sub_f32 v190, v190, v176\n"
"v_exp_f32 v190, v190\n"
"v_sub_f32 v191, v191, v176\n"
"v_exp_f32 v191, v191\n"
"s_waitcnt lgkmcnt(1)\n"
"v_mfma_f32_16x16x32_bf16 v[192:195], v[220:223], v[210:213], v[192:195]\n"
"ds_read_b128 v[196:199], v209 offset:4080\n"
"v_cvt_pk_bf16_f32 v188, v188, v189\n"
"v_mfma_f32_16x16x32_bf16 v[192:195], v[222:225], v[24:27], v[192:195]\n"
"ds_write_addtid_b32 v188 offset:53248\n"
"v_cvt_pk_bf16_f32 v189, v190, v191\n"
"ds_write_addtid_b32 v189 offset:53504\n"
"s_waitcnt lgkmcnt(3)\n"
"v_mfma_f32_16x16x32_bf16 v[184:187], v[244:247], v[210:213], v[184:187]\n"
"ds_read_b128 v[200:203], v208 offset:5408\n"
"v_mfma_f32_16x16x32_bf16 v[184:187], v[246:249], v[24:27], v[184:187]\n"
"s_nop 0\n"
"v_sub_f32 v192, v192, v176\n"
"v_exp_f32 v192, v192\n"
"v_sub_f32 v193, v193, v176\n"
"v_exp_f32 v193, v193\n"
"v_sub_f32 v194, v194, v176\n"
"v_exp_f32 v194, v194\n"
"v_sub_f32 v195, v195, v176\n"
"v_exp_f32 v195, v195\n"
"v_sub_f32 v184, v184, v176\n"
"v_exp_f32 v184, v184\n"
"v_sub_f32 v185, v185, v176\n"
"v_exp_f32 v185, v185\n"
"v_cvt_pk_bf16_f32 v192, v192, v193\n"
"v_sub_f32 v186, v186, v176\n"
"v_exp_f32 v186, v186\n"
"v_cvt_pk_bf16_f32 v193, v194, v195\n"
"v_sub_f32 v187, v187, v176\n"
"v_exp_f32 v187, v187\n"
"v_cvt_pk_bf16_f32 v184, v184, v185\n"
"v_cvt_pk_bf16_f32 v185, v186, v187\n"
"ds_write_addtid_b32 v192 offset:53760\n"
"ds_write_addtid_b32 v193 offset:54016\n"
"s_waitcnt lgkmcnt(5)\n"
"v_mfma_f32_16x16x32_bf16 v[196:199], v[226:229], v[210:213], v[196:199]\n"
"ds_read_b128 v[188:191], v208 offset:5424\n"
"v_mfma_f32_16x16x32_bf16 v[196:199], v[228:231], v[24:27], v[196:199]\n"
"ds_write_addtid_b32 v184 offset:54272\n"
"ds_write_addtid_b32 v185 offset:54528\n"
"s_waitcnt lgkmcnt(5)\n"
"v_mfma_f32_16x16x32_bf16 v[200:203], v[250:253], v[210:213], v[200:203]\n"
"v_mfma_f32_16x16x32_bf16 v[200:203], v[252:255], v[24:27], v[200:203]\n"
"s_nop 2\n"
"v_sub_f32 v196, v196, v176\n"
"v_exp_f32 v196, v196\n"
"v_sub_f32 v197, v197, v176\n"
"v_exp_f32 v197, v197\n"
"v_sub_f32 v198, v198, v176\n"
"v_exp_f32 v198, v198\n"
"v_sub_f32 v199, v199, v176\n"
"v_exp_f32 v199, v199\n"
"v_cvt_pk_bf16_f32 v196, v196, v197\n"
"v_cvt_pk_bf16_f32 v197, v198, v199\n"
"v_sub_f32 v200, v200, v176\n"
"v_exp_f32 v200, v200\n"
"v_sub_f32 v201, v201, v176\n"
"v_exp_f32 v201, v201\n"
"v_sub_f32 v202, v202, v176\n"
"v_exp_f32 v202, v202\n"
"v_sub_f32 v203, v203, v176\n"
"v_exp_f32 v203, v203\n"
"v_cvt_pk_bf16_f32 v200, v200, v201\n"
"v_cvt_pk_bf16_f32 v201, v202, v203\n"
"ds_write_addtid_b32 v196 offset:4096\n"
"ds_write_addtid_b32 v197 offset:4352\n"
"s_waitcnt lgkmcnt(4)\n"
"v_mfma_f32_16x16x32_bf16 v[188:191], v[178:181], v[210:213], v[188:191]\n"
"ds_write_addtid_b32 v200 offset:54784\n"
"v_mfma_f32_16x16x32_bf16 v[188:191], v[180:183], v[24:27], v[188:191]\n"
"ds_write_addtid_b32 v201 offset:55040\n"
"s_nop 6\n"
"v_sub_f32 v188, v188, v176\n"
"v_exp_f32 v188, v188\n"
"v_sub_f32 v189, v189, v176\n"
"v_exp_f32 v189, v189\n"
"s_nop 0\n"
"v_cvt_pk_bf16_f32 v188, v188, v189\n"
"ds_write_addtid_b32 v188 offset:4608\n"
"v_sub_f32 v190, v190, v176\n"
"v_exp_f32 v190, v190\n"
"v_sub_f32 v191, v191, v176\n"
"v_exp_f32 v191, v191\n"
"s_nop 0\n"
"v_cvt_pk_bf16_f32 v189, v190, v191\n"
"ds_write_addtid_b32 v189 offset:4864\n"
"s_waitcnt lgkmcnt(0)\n"
"s_barrier\n"
"s_barrier\n"
"s_barrier\n"
    ,
"s_add_u32 s50, s54, 53248\n"
"s_lshr_b32 s41, s40, 1\n"
"s_sub_u32 s55, 0, s41\n"
"s_mul_i32 s51, s41, 4096\n"
"s_lshl_b32 s41, s40, 10\n"
"s_add_u32 s51, s51, s41\n"
"s_lshl_b32 s65, s40, 2\n"
"s_add_u32 s65, s65, 32192\n"
"s_andn2_b32 s41, s45, 7\n"
"s_add_u32 s56, s56, s41\n"
"s_addc_u32 s57, s57, 0\n"
"s_add_u32 s66, s66, s41\n"
"s_addc_u32 s67, s67, 0\n"
"s_mul_i32 s68, s40, 1552\n"
"s_add_u32 s68, s68, 57344\n"
"s_setprio 0\n"
"s_movk_i32 s40, 768\n"
"s_mov_b64 s[62:63], 0\n"
"s_movk_i32 s64, 0x207\n"
"s_add_u32 s58, s56, 128\n"
"s_addc_u32 s59, s57, 0\n"
"s_add_u32 s60, s56, 256\n"
"s_addc_u32 s61, s57, 0\n"
"v_subrev_u32 v179, 24576, v10\n"
"s_waitcnt vmcnt(0)\n"
"s_barrier\n"
"s_add_u32 s42, s46, 110976\n"
"s_addc_u32 s43, s47, 0\n"
"s_add_u32 m0, s50, 27648\n"
"s_nop 0\n"
"global_load_lds_dwordx4 v0, s[42:43]\n"
"s_add_u32 m0, s50, 28672\n"
"s_nop 0\n"
"global_load_lds_dwordx4 v1, s[42:43]\n"
"s_add_u32 m0, s50, 29696\n"
"s_nop 0\n"
"global_load_lds_dwordx4 v2, s[42:43]\n"
"s_add_u32 s42, s46, 147840\n"
"s_addc_u32 s43, s47, 0\n"
"s_add_u32 m0, s50, 49152\n"
"s_nop 0\n"
"global_load_lds_dwordx4 v0, s[42:43]\n"
"s_add_u32 m0, s50, 50176\n"
"s_nop 0\n"
"global_load_lds_dwordx4 v1, s[42:43]\n"
"s_add_u32 m0, s50, 51200\n"
"s_nop 0\n"
"global_load_lds_dwordx4 v2, s[42:43]\n"
"s_add_u32 s42, s46, 184704\n"
"s_addc_u32 s43, s47, 0\n"
"s_add_u32 m0, s50, 52224\n"
"s_nop 0\n"
"global_load_lds_dwordx4 v0, s[42:43]\n"
"s_add_u32 m0, s50, 53248\n"
"s_nop 0\n"
"global_load_lds_dwordx4 v1, s[42:43]\n"
"s_add_u32 m0, s50, 54272\n"
"s_nop 0\n"
"global_load_lds_dwordx4 v2, s[42:43]\n"
"s_add_u32 s42, s46, 768\n"
"s_addc_u32 s43, s47, 0\n"
"s_add_u32 m0, s50, 0\n"
"s_nop 0\n"
"global_load_lds_dwordx4 v0, s[42:43]\n"
"s_add_u32 m0, s50, 1024\n"
"s_nop 0\n"
"global_load_lds_dwordx4 v1, s[42:43]\n"
"s_add_u32 m0, s50, 2048\n"
"s_nop 0\n"
"global_load_lds_dwordx4 v2, s[42:43]\n"
"s_waitcnt vmcnt(9)\n"
"s_barrier\n"
"s_add_u32 s42, s46, 37632\n"
"s_addc_u32 s43, s47, 0\n"
"s_add_u32 m0, s50, 3072\n"
"s_nop 0\n"
"global_load_lds_dwordx4 v0, s[42:43]\n"
"s_add_u32 m0, s50, 4096\n"
"s_nop 0\n"
"global_load_lds_dwordx4 v1, s[42:43]\n"
"s_add_u32 m0, s50, 5120\n"
"s_nop 0\n"
"global_load_lds_dwordx4 v2, s[42:43]\n"
"s_add_u32 s42, s46, 74496\n"
"s_addc_u32 s43, s47, 0\n"
"s_add_u32 m0, s50, 4294918144\n"
"s_nop 0\n"
"global_load_lds_dwordx4 v0, s[42:43]\n"
"s_add_u32 m0, s50, 4294919168\n"
"s_nop 0\n"
"global_load_lds_dwordx4 v1, s[42:43]\n"
"s_add_u32 m0, s50, 4294920192\n"
"s_nop 0\n"
"global_load_lds_dwordx4 v2, s[42:43]\n"
"s_waitcnt vmcnt(9)\n"
"s_barrier\n"
"s_add_u32 s42, s46, 111360\n"
"s_addc_u32 s43, s47, 0\n"
"s_add_u32 m0, s50, 4294921216\n"
"s_nop 0\n"
"global_load_lds_dwordx4 v0, s[42:43]\n"
"s_add_u32 m0, s50, 4294922240\n"
"s_nop 0\n"
"global_load_lds_dwordx4 v1, s[42:43]\n"
"s_add_u32 m0, s50, 4294923264\n"
"s_nop 0\n"
"global_load_lds_dwordx4 v2, s[42:43]\n"
"s_barrier\n"
"s_add_u32 s42, s46, 148224\n"
"s_addc_u32 s43, s47, 0\n"
"s_add_u32 m0, s50, 24576\n"
"s_nop 0\n"
"global_load_lds_dwordx4 v0, s[42:43]\n"
"s_add_u32 m0, s50, 25600\n"
"s_nop 0\n"
"global_load_lds_dwordx4 v1, s[42:43]\n"
"s_add_u32 m0, s50, 26624\n"
"s_nop 0\n"
"global_load_lds_dwordx4 v2, s[42:43]\n"
"s_add_u32 s42, s46, 185088\n"
"s_addc_u32 s43, s47, 0\n"
"s_add_u32 m0, s50, 27648\n"
"s_nop 0\n"
"global_load_lds_dwordx4 v0, s[42:43]\n"
"s_add_u32 m0, s50, 28672\n"
"s_nop 0\n"
"global_load_lds_dwordx4 v1, s[42:43]\n"
"s_add_u32 m0, s50, 29696\n"
"s_nop 0\n"
"global_load_lds_dwordx4 v2, s[42:43]\n"
"s_mov_b32 m0, 3584\n"
"s_bfm_b64 exec, 6, 0\n"
"global_load_lds_dword v22, s[46:47]\n"
"s_mov_b64 exec, 31\n"
"global_load_lds_dword v23, s[48:49]\n"
"s_mov_b64 exec, -1\n"
"s_waitcnt vmcnt(17)\n"
"ds_read_b64_tr_b16 v[26:27], v10 offset:29504\n"
"s_andn2_b32 s41, 576, s55\n"
"v_add_u32 v178, s41, v15\n"
"ds_read_u16 v178, v178 offset:224\n"
"ds_read_b64_tr_b16 v[28:29], v10 offset:28672\n"
"ds_read_b64_tr_b16 v[30:31], v10 offset:29440\n"
"ds_read_b128 v[180:183], v12 offset:24576\n"
"ds_read_b64_tr_b16 v[32:33], v10 offset:28704\n"
"ds_read_b64_tr_b16 v[34:35], v10 offset:29472\n"
"ds_read_b64_tr_b16 v[24:25], v10 offset:28736\n"
"ds_read_b128 v[184:187], v12 offset:30720\n"
"ds_read_b128 v[188:191], v12 offset:36864\n"
"ds_read_u16 v23, v21 offset:1152\n"
"s_waitcnt lgkmcnt(9)\n"
"v_mad_u32_u24 v178, v178, s40, v16\n"
"s_add_u32 m0, s51, 102400\n"
"s_nop 0\n"
"global_load_lds_dwordx4 v178, s[56:57] nt\n"
"s_add_u32 m0, s51, 104448\n"
"s_nop 0\n"
"global_load_lds_dwordx4 v178, s[58:59] nt\n"
"s_add_u32 m0, s51, 106496\n"
"s_nop 0\n"
"global_load_lds_dwordx4 v178, s[60:61] nt\n"
"s_waitcnt vmcnt(17)\n"
"ds_read_b64_tr_b16 v[38:39], v10 offset:32576\n"
"s_and_b32 s41, 32, s55\n"
"v_add_u32 v178, s41, v15\n"
"ds_read_u16 v178, v178 offset:352\n"
"s_waitcnt lgkmcnt(2)\n"
"v_mad_u32_u24 v22, v23, s40, v18\n"
"v_mad_u32_u24 v23, v23, s40, v19\n"
"ds_read_b64_tr_b16 v[40:41], v10 offset:31744\n"
"ds_read_b64_tr_b16 v[42:43], v10 offset:32512\n"
"ds_read_b64_tr_b16 v[44:45], v10 offset:31776\n"
"ds_read_b64_tr_b16 v[46:47], v10 offset:32544\n"
"ds_read_b64_tr_b16 v[36:37], v10 offset:31808\n"
"ds_read_u16 v177, v21 offset:1184\n"
"s_waitcnt lgkmcnt(6)\n"
"v_mad_u32_u24 v178, v178, s40, v16\n"
"s_add_u32 m0, s51, 114688\n"
"s_nop 0\n"
"global_load_lds_dwordx4 v178, s[56:57] nt\n"
"s_add_u32 m0, s51, 116736\n"
"s_nop 0\n"
"global_load_lds_dwordx4 v178, s[58:59] nt\n"
"s_add_u32 m0, s51, 118784\n"
"s_nop 0\n"
"global_load_lds_dwordx4 v178, s[60:61] nt\n"
"v_mfma_f32_16x16x32_bf16 v[192:195], v[180:183], v[28:31], 0\n"
"s_waitcnt vmcnt(17)\n"
"ds_read_b64_tr_b16 v[48:49], v179 offset:4096\n"
"v_mfma_f32_16x16x32_bf16 v[196:199], v[180:183], v[32:35], 0\n"
"s_waitcnt lgkmcnt(1)\n"
"ds_read_b128 v[200:203], v13 offset:24576\n"
"v_mad_u32_u24 v176, v177, s40, v18\n"
"v_mfma_f32_16x16x32_bf16 v[204:207], v[180:183], v[24:27], 0\n"
"ds_read_b64_tr_b16 v[50:51], v179 offset:4864\n"
"v_mad_u32_u24 v177, v177, s40, v19\n"
"v_mfma_f32_16x16x32_bf16 v[180:183], v[184:187], v[28:31], 0\n"
"ds_read_b64_tr_b16 v[52:53], v179 offset:4128\n"
"v_mfma_f32_16x16x32_bf16 v[214:217], v[184:187], v[32:35], 0\n"
"ds_read_b128 v[208:211], v13 offset:30720\n"
"v_mfma_f32_16x16x32_bf16 v[218:221], v[184:187], v[24:27], 0\n"
"ds_read_b64_tr_b16 v[54:55], v179 offset:4896\n"
"v_mfma_f32_16x16x32_bf16 v[222:225], v[188:191], v[28:31], 0\n"
"ds_read_b64_tr_b16 v[56:57], v179 offset:4160\n"
"v_mfma_f32_16x16x32_bf16 v[226:229], v[188:191], v[32:35], 0\n"
"ds_read_b128 v[184:187], v13 offset:36864\n"
"v_mfma_f32_16x16x32_bf16 v[230:233], v[188:191], v[24:27], 0\n"
"s_waitcnt lgkmcnt(6)\n"
"ds_read_b64_tr_b16 v[58:59], v179 offset:4928\n"
"v_mfma_f32_16x16x32_bf16 v[192:195], v[200:203], v[40:43], v[192:195]\n"
"v_mfma_f32_16x16x32_bf16 v[196:199], v[200:203], v[44:47], v[196:199]\n"
"ds_read_b128 v[188:191], v12 offset:26624\n"
"v_mfma_f32_16x16x32_bf16 v[204:207], v[200:203], v[36:39], v[204:207]\n"
"s_waitcnt lgkmcnt(5)\n"
"v_mfma_f32_16x16x32_bf16 v[180:183], v[208:211], v[40:43], v[180:183]\n"
"v_mfma_f32_16x16x32_bf16 v[214:217], v[208:211], v[44:47], v[214:217]\n"
"ds_read_b128 v[200:203], v12 offset:32768\n"
"v_mfma_f32_16x16x32_bf16 v[218:221], v[208:211], v[36:39], v[218:221]\n"
"s_waitcnt lgkmcnt(3)\n"
"v_mfma_f32_16x16x32_bf16 v[222:225], v[184:187], v[40:43], v[222:225]\n"
"s_waitcnt vmcnt(14)\n"
"ds_read_b64_tr_b16 v[60:61], v179 offset:7168\n"
"v_mfma_f32_16x16x32_bf16 v[226:229], v[184:187], v[44:47], v[226:229]\n"
"ds_read_b128 v[208:211], v12 offset:38912\n"
"v_mfma_f32_16x16x32_bf16 v[230:233], v[184:187], v[36:39], v[230:233]\n"
"s_waitcnt lgkmcnt(3)\n"
"ds_read_b64_tr_b16 v[62:63], v179 offset:7936\n"
"v_mfma_f32_16x16x32_bf16 v[192:195], v[188:191], v[48:51], v[192:195]\n"
"ds_read_b64_tr_b16 v[64:65], v179 offset:7200\n"
"v_mfma_f32_16x16x32_bf16 v[196:199], v[188:191], v[52:55], v[196:199]\n"
"ds_read_b128 v[184:187], v13 offset:26624\n"
"v_mfma_f32_16x16x32_bf16 v[204:207], v[188:191], v[56:59], v[204:207]\n"
"s_waitcnt lgkmcnt(5)\n"
"ds_read_b64_tr_b16 v[66:67], v179 offset:7968\n"
"v_mfma_f32_16x16x32_bf16 v[180:183], v[200:203], v[48:51], v[180:183]\n"
"ds_read_b64_tr_b16 v[68:69], v179 offset:7232\n"
"v_mfma_f32_16x16x32_bf16 v[214:217], v[200:203], v[52:55], v[214:217]\n"
"ds_read_b64_tr_b16 v[70:71], v179 offset:8000\n"
"v_mfma_f32_16x16x32_bf16 v[218:221], v[200:203], v[56:59], v[218:221]\n"
"s_waitcnt lgkmcnt(6)\n"
"ds_read_b128 v[188:191], v13 offset:32768\n"
"v_mfma_f32_16x16x32_bf16 v[222:225], v[208:211], v[48:51], v[222:225]\n"
"s_waitcnt vmcnt(11)\n"
"ds_read_b64_tr_b16 v[72:73], v10 offset:53248\n"
"v_mfma_f32_16x16x32_bf16 v[226:229], v[208:211], v[52:55], v[226:229]\n"
"ds_read_b128 v[200:203], v13 offset:38912\n"
"v_mfma_f32_16x16x32_bf16 v[230:233], v[208:211], v[56:59], v[230:233]\n"
"s_waitcnt lgkmcnt(6)\n"
"ds_read_b64_tr_b16 v[74:75], v10 offset:54016\n"
"v_mfma_f32_16x16x32_bf16 v[192:195], v[184:187], v[60:63], v[192:195]\n"
"s_waitcnt lgkmcnt(4)\n"
"ds_read_b64_tr_b16 v[76:77], v10 offset:53280\n"
"s_barrier\n"
"v_mfma_f32_16x16x32_bf16 v[196:199], v[184:187], v[64:67], v[196:199]\n"
"ds_read_b128 v[208:211], v12 offset:28672\n"
"v_mfma_f32_16x16x32_bf16 v[204:207], v[184:187], v[68:71], v[204:207]\n"
"s_waitcnt lgkmcnt(5)\n"
"ds_read_b64_tr_b16 v[78:79], v10 offset:54048\n"
"v_mfma_f32_16x16x32_bf16 v[180:183], v[188:191], v[60:63], v[180:183]\n"
"ds_read_b64_tr_b16 v[80:81], v10 offset:53312\n"
"v_mfma_f32_16x16x32_bf16 v[214:217], v[188:191], v[64:67], v[214:217]\n"
"ds_read_b64_tr_b16 v[82:83], v10 offset:54080\n"
"v_mfma_f32_16x16x32_bf16 v[218:221], v[188:191], v[68:71], v[218:221]\n"
"s_waitcnt lgkmcnt(6)\n"
"ds_read_b128 v[184:187], v12 offset:34816\n"
"v_mfma_f32_16x16x32_bf16 v[222:225], v[200:203], v[60:63], v[222:225]\n"
"s_waitcnt vmcnt(8)\n"
"ds_read_b64_tr_b16 v[84:85], v10 offset:56320\n"
"v_mfma_f32_16x16x32_bf16 v[226:229], v[200:203], v[64:67], v[226:229]\n"
"ds_read_b128 v[188:191], v12 offset:40960\n"
"v_mfma_f32_16x16x32_bf16 v[230:233], v[200:203], v[68:71], v[230:233]\n"
"s_waitcnt lgkmcnt(6)\n"
"ds_read_b64_tr_b16 v[86:87], v10 offset:57088\n"
"v_mfma_f32_16x16x32_bf16 v[192:195], v[208:211], v[72:75], v[192:195]\n"
"s_waitcnt lgkmcnt(6)\n"
"ds_read_b64_tr_b16 v[88:89], v10 offset:56352\n"
"v_mfma_f32_16x16x32_bf16 v[196:199], v[208:211], v[76:79], v[196:199]\n"
"s_waitcnt lgkmcnt(5)\n"
"ds_read_b128 v[200:203], v13 offset:28672\n"
"v_mfma_f32_16x16x32_bf16 v[204:207], v[208:211], v[80:83], v[204:207]\n"
"s_waitcnt lgkmcnt(5)\n"
"ds_read_b64_tr_b16 v[90:91], v10 offset:57120\n"
"v_mfma_f32_16x16x32_bf16 v[180:183], v[184:187], v[72:75], v[180:183]\n"
"ds_read_b64_tr_b16 v[92:93], v10 offset:56384\n"
"v_mfma_f32_16x16x32_bf16 v[214:217], v[184:187], v[76:79], v[214:217]\n"
"ds_read_b64_tr_b16 v[94:95], v10 offset:57152\n"
"v_mfma_f32_16x16x32_bf16 v[218:221], v[184:187], v[80:83], v[218:221]\n"
"s_waitcnt lgkmcnt(6)\n"
"ds_read_b128 v[184:187], v13 offset:34816\n"
"v_mfma_f32_16x16x32_bf16 v[222:225], v[188:191], v[72:75], v[222:225]\n"
"v_mfma_f32_16x16x32_bf16 v[226:229], v[188:191], v[76:79], v[226:229]\n"
"ds_read_b128 v[238:241], v13 offset:40960\n"
"v_mfma_f32_16x16x32_bf16 v[230:233], v[188:191], v[80:83], v[230:233]\n"
"s_waitcnt lgkmcnt(5)\n"
"v_mfma_f32_16x16x32_bf16 v[192:195], v[200:203], v[84:87], v[192:195]\n"
"s_waitcnt lgkmcnt(2)\n"
"s_barrier\n"
"v_mfma_f32_16x16x32_bf16 v[196:199], v[200:203], v[88:91], v[196:199]\n"
"ds_read_b128 v[188:191], v12 offset:43008\n"
"v_mfma_f32_16x16x32_bf16 v[204:207], v[200:203], v[92:95], v[204:207]\n"
"s_waitcnt lgkmcnt(2)\n"
"v_mfma_f32_16x16x32_bf16 v[180:183], v[184:187], v[84:87], v[180:183]\n"
"v_mfma_f32_16x16x32_bf16 v[214:217], v[184:187], v[88:91], v[214:217]\n"
"ds_read_b128 v[200:203], v13 offset:43008\n"
"v_mfma_f32_16x16x32_bf16 v[218:221], v[184:187], v[92:95], v[218:221]\n"
"s_waitcnt lgkmcnt(2)\n"
"v_cvt_pk_bf16_f32 v184, v192, v193\n"
"v_cvt_pk_bf16_f32 v185, v194, v195\n"
"v_mfma_f32_16x16x32_bf16 v[222:225], v[238:241], v[84:87], v[222:225]\n"
"v_cvt_pk_bf16_f32 v210, v196, v197\n"
"v_cvt_pk_bf16_f32 v211, v198, v199\n"
"v_mfma_f32_16x16x32_bf16 v[226:229], v[238:241], v[88:91], v[226:229]\n"
"ds_read_b128 v[196:199], v12 offset:45056\n"
"v_cvt_pk_bf16_f32 v236, v204, v205\n"
"v_cvt_pk_bf16_f32 v237, v206, v207\n"
"v_mfma_f32_16x16x32_bf16 v[230:233], v[238:241], v[92:95], v[230:233]\n"
"s_waitcnt lgkmcnt(2)\n"
"v_cvt_pk_bf16_f32 v186, v180, v181\n"
"v_cvt_pk_bf16_f32 v187, v182, v183\n"
"v_mfma_f32_16x16x32_bf16 v[178:181], v[188:191], v[28:31], 0\n"
"v_cvt_pk_bf16_f32 v212, v214, v215\n"
"v_cvt_pk_bf16_f32 v213, v216, v217\n"
"v_mfma_f32_16x16x32_bf16 v[204:207], v[188:191], v[32:35], 0\n"
"ds_read_b128 v[214:217], v13 offset:45056\n"
"v_cvt_pk_bf16_f32 v238, v218, v219\n"
"v_cvt_pk_bf16_f32 v239, v220, v221\n"
"v_mfma_f32_16x16x32_bf16 v[240:243], v[188:191], v[24:27], 0\n"
"s_waitcnt lgkmcnt(2)\n"
"v_cvt_pk_bf16_f32 v194, v222, v223\n"
"v_cvt_pk_bf16_f32 v195, v224, v225\n"
"v_mfma_f32_16x16x32_bf16 v[178:181], v[200:203], v[40:43], v[178:181]\n"
"v_cvt_pk_bf16_f32 v220, v226, v227\n"
"v_cvt_pk_bf16_f32 v221, v228, v229\n"
"v_mfma_f32_16x16x32_bf16 v[204:207], v[200:203], v[44:47], v[204:207]\n"
"ds_read_b128 v[188:191], v12 offset:47104\n"
"v_cvt_pk_bf16_f32 v246, v230, v231\n"
"v_cvt_pk_bf16_f32 v247, v232, v233\n"
"v_mfma_f32_16x16x32_bf16 v[240:243], v[200:203], v[36:39], v[240:243]\n"
"s_waitcnt lgkmcnt(2)\n"
"v_mfma_f32_16x16x32_bf16 v[178:181], v[196:199], v[48:51], v[178:181]\n"
"v_mfma_f32_16x16x32_bf16 v[204:207], v[196:199], v[52:55], v[204:207]\n"
"ds_read_b128 v[200:203], v13 offset:47104\n"
"v_mfma_f32_16x16x32_bf16 v[240:243], v[196:199], v[56:59], v[240:243]\n"
"s_waitcnt lgkmcnt(2)\n"
"v_mfma_f32_16x16x32_bf16 v[178:181], v[214:217], v[60:63], v[178:181]\n"
"v_mfma_f32_16x16x32_bf16 v[204:207], v[214:217], v[64:67], v[204:207]\n"
"ds_read_b128 v[196:199], v12 offset:49152\n"
"v_mfma_f32_16x16x32_bf16 v[240:243], v[214:217], v[68:71], v[240:243]\n"
"s_waitcnt lgkmcnt(2)\n"
"v_mfma_f32_16x16x32_bf16 v[178:181], v[188:191], v[72:75], v[178:181]\n"
"v_mfma_f32_16x16x32_bf16 v[204:207], v[188:191], v[76:79], v[204:207]\n"
"ds_read_b128 v[214:217], v13 offset:49152\n"
"v_mfma_f32_16x16x32_bf16 v[240:243], v[188:191], v[80:83], v[240:243]\n"
"s_waitcnt lgkmcnt(2)\n"
"v_mfma_f32_16x16x32_bf16 v[178:181], v[200:203], v[84:87], v[178:181]\n"
"v_mfma_f32_16x16x32_bf16 v[204:207], v[200:203], v[88:91], v[204:207]\n"
"ds_read_b128 v[188:191], v12 offset:51200\n"
"v_mfma_f32_16x16x32_bf16 v[240:243], v[200:203], v[92:95], v[240:243]\n"
"s_waitcnt lgkmcnt(2)\n"
"v_mfma_f32_16x16x32_bf16 v[200:203], v[196:199], v[28:31], 0\n"
"v_mfma_f32_16x16x32_bf16 v[224:227], v[196:199], v[32:35], 0\n"
"ds_read_b128 v[228:231], v13 offset:51200\n"
"v_mfma_f32_16x16x32_bf16 v[232:235], v[196:199], v[24:27], 0\n"
"s_waitcnt lgkmcnt(2)\n"
"v_mfma_f32_16x16x32_bf16 v[200:203], v[214:217], v[40:43], v[200:203]\n"
"v_cvt_pk_bf16_f32 v196, v178, v179\n"
"v_cvt_pk_bf16_f32 v197, v180, v181\n"
"v_cvt_pk_bf16_f32 v222, v204, v205\n"
"v_cvt_pk_bf16_f32 v223, v206, v207\n"
"v_mfma_f32_16x16x32_bf16 v[224:227], v[214:217], v[44:47], v[224:227]\n"
"ds_read_b128 v[178:181], v12 offset:53248\n"
"v_cvt_pk_bf16_f32 v248, v240, v241\n"
"v_cvt_pk_bf16_f32 v249, v242, v243\n"
"v_mfma_f32_16x16x32_bf16 v[232:235], v[214:217], v[36:39], v[232:235]\n"
"s_waitcnt lgkmcnt(2)\n"
"v_mfma_f32_16x16x32_bf16 v[200:203], v[188:191], v[48:51], v[200:203]\n"
"v_mfma_f32_16x16x32_bf16 v[224:227], v[188:191], v[52:55], v[224:227]\n"
"ds_read_b128 v[204:207], v13 offset:53248\n"
"v_mfma_f32_16x16x32_bf16 v[232:235], v[188:191], v[56:59], v[232:235]\n"
"s_waitcnt lgkmcnt(2)\n"
"v_mfma_f32_16x16x32_bf16 v[200:203], v[228:231], v[60:63], v[200:203]\n"
"v_mfma_f32_16x16x32_bf16 v[224:227], v[228:231], v[64:67], v[224:227]\n"
"ds_read_b128 v[188:191], v12 offset:55296\n"
"v_mfma_f32_16x16x32_bf16 v[232:235], v[228:231], v[68:71], v[232:235]\n"
"s_waitcnt lgkmcnt(2)\n"
"v_mfma_f32_16x16x32_bf16 v[200:203], v[178:181], v[72:75], v[200:203]\n"
"v_mfma_f32_16x16x32_bf16 v[224:227], v[178:181], v[76:79], v[224:227]\n"
"ds_read_b128 v[216:219], v13 offset:55296\n"
"v_mfma_f32_16x16x32_bf16 v[232:235], v[178:181], v[80:83], v[232:235]\n"
"s_waitcnt lgkmcnt(2)\n"
"v_mfma_f32_16x16x32_bf16 v[200:203], v[204:207], v[84:87], v[200:203]\n"
"v_mfma_f32_16x16x32_bf16 v[224:227], v[204:207], v[88:91], v[224:227]\n"
"ds_read_b128 v[178:181], v12 offset:57344\n"
"v_mfma_f32_16x16x32_bf16 v[232:235], v[204:207], v[92:95], v[232:235]\n"
"s_waitcnt lgkmcnt(2)\n"
"v_mfma_f32_16x16x32_bf16 v[204:207], v[188:191], v[28:31], 0\n"
"v_mfma_f32_16x16x32_bf16 v[228:231], v[188:191], v[32:35], 0\n"
"ds_read_b128 v[242:245], v13 offset:57344\n"
"v_mfma_f32_16x16x32_bf16 v[250:253], v[188:191], v[24:27], 0\n"
"s_waitcnt lgkmcnt(2)\n"
"v_mfma_f32_16x16x32_bf16 v[204:207], v[216:219], v[40:43], v[204:207]\n"
"v_cvt_pk_bf16_f32 v188, v200, v201\n"
"v_cvt_pk_bf16_f32 v189, v202, v203\n"
"v_cvt_pk_bf16_f32 v214, v224, v225\n"
"v_cvt_pk_bf16_f32 v215, v226, v227\n"
"v_mfma_f32_16x16x32_bf16 v[228:231], v[216:219], v[44:47], v[228:231]\n"
"ds_read_b128 v[190:193], v12 offset:59392\n"
"v_cvt_pk_bf16_f32 v240, v232, v233\n"
"v_cvt_pk_bf16_f32 v241, v234, v235\n"
"v_mfma_f32_16x16x32_bf16 v[250:253], v[216:219], v[36:39], v[250:253]\n"
"s_waitcnt lgkmcnt(2)\n"
"v_mfma_f32_16x16x32_bf16 v[204:207], v[178:181], v[48:51], v[204:207]\n"
"v_mfma_f32_16x16x32_bf16 v[228:231], v[178:181], v[52:55], v[228:231]\n"
"ds_read_b128 v[198:201], v13 offset:59392\n"
"v_mfma_f32_16x16x32_bf16 v[250:253], v[178:181], v[56:59], v[250:253]\n"
"s_waitcnt lgkmcnt(2)\n"
"v_mfma_f32_16x16x32_bf16 v[204:207], v[242:245], v[60:63], v[204:207]\n"
"v_mfma_f32_16x16x32_bf16 v[228:231], v[242:245], v[64:67], v[228:231]\n"
"v_mfma_f32_16x16x32_bf16 v[250:253], v[242:245], v[68:71], v[250:253]\n"
"s_waitcnt lgkmcnt(1)\n"
"v_mfma_f32_16x16x32_bf16 v[204:207], v[190:193], v[72:75], v[204:207]\n"
"v_mfma_f32_16x16x32_bf16 v[228:231], v[190:193], v[76:79], v[228:231]\n"
"v_mfma_f32_16x16x32_bf16 v[250:253], v[190:193], v[80:83], v[250:253]\n"
"s_and_b32 s41, 32, s55\n"
"v_add_u32 v2, s41, v15\n"
"s_waitcnt lgkmcnt(0)\n"
"v_mfma_f32_16x16x32_bf16 v[204:207], v[198:201], v[84:87], v[204:207]\n"
"ds_read_u16 v2, v2 offset:832\n"
"v_mfma_f32_16x16x32_bf16 v[228:231], v[198:201], v[88:91], v[228:231]\n"
"s_waitcnt vmcnt(0)\n"
"s_barrier\n"
"ds_read_b128 v[180:183], v12 offset:6144\n"
"v_mfma_f32_16x16x32_bf16 v[250:253], v[198:201], v[92:95], v[250:253]\n"
"s_waitcnt lgkmcnt(1)\n"
"ds_read_b128 v[190:193], v13 offset:6144\n"
"v_mad_u32_u24 v2, v2, s40, v16\n"
"s_waitcnt lgkmcnt(1)\n"
"v_mfma_f32_16x16x32_bf16 v[200:203], v[180:183], v[28:31], 0\n"
"v_cvt_pk_bf16_f32 v178, v204, v205\n"
"v_cvt_pk_bf16_f32 v179, v206, v207\n"
"v_mfma_f32_16x16x32_bf16 v[206:209], v[180:183], v[32:35], 0\n"
"s_add_u32 m0, s51, 126976\n"
"s_nop 0\n"
"global_load_lds_dwordx4 v2, s[56:57] nt\n"
"ds_read_b128 v[216:219], v12 offset:8192\n"
"v_mfma_f32_16x16x32_bf16 v[232:235], v[180:183], v[24:27], 0\n"
"s_add_u32 m0, s51, 129024\n"
"s_nop 0\n"
"global_load_lds_dwordx4 v2, s[58:59] nt\n"
"s_waitcnt lgkmcnt(1)\n"
"v_mfma_f32_16x16x32_bf16 v[200:203], v[190:193], v[40:43], v[200:203]\n"
"s_add_u32 m0, s51, 131072\n"
"s_nop 0\n"
"global_load_lds_dwordx4 v2, s[60:61] nt\n"
"v_cvt_pk_bf16_f32 v204, v228, v229\n"
"v_cvt_pk_bf16_f32 v205, v230, v231\n"
"v_mfma_f32_16x16x32_bf16 v[206:209], v[190:193], v[44:47], v[206:209]\n"
"ds_read_b128 v[180:183], v13 offset:8192\n"
"v_cvt_pk_bf16_f32 v230, v250, v251\n"
"v_cvt_pk_bf16_f32 v231, v252, v253\n"
"v_mfma_f32_16x16x32_bf16 v[232:235], v[190:193], v[36:39], v[232:235]\n"
"s_and_b32 s41, 160, s55\n"
"v_add_u32 v2, s41, v15\n"
"s_waitcnt lgkmcnt(1)\n"
"v_mfma_f32_16x16x32_bf16 v[200:203], v[216:219], v[48:51], v[200:203]\n"
"ds_read_u16 v2, v2 offset:96\n"
"v_mfma_f32_16x16x32_bf16 v[206:209], v[216:219], v[52:55], v[206:209]\n"
"ds_read_b128 v[190:193], v12 offset:10240\n"
"v_mfma_f32_16x16x32_bf16 v[232:235], v[216:219], v[56:59], v[232:235]\n"
"s_waitcnt lgkmcnt(2)\n"
"v_mfma_f32_16x16x32_bf16 v[200:203], v[180:183], v[60:63], v[200:203]\n"
"v_mfma_f32_16x16x32_bf16 v[206:209], v[180:183], v[64:67], v[206:209]\n"
"ds_read_b128 v[216:219], v13 offset:10240\n"
"v_mfma_f32_16x16x32_bf16 v[232:235], v[180:183], v[68:71], v[232:235]\n"
"s_waitcnt lgkmcnt(2)\n"
"v_mad_u32_u24 v2, v2, s40, v16\n"
"s_waitcnt lgkmcnt(1)\n"
"v_mfma_f32_16x16x32_bf16 v[200:203], v[190:193], v[72:75], v[200:203]\n"
"s_add_u32 m0, s51, 139264\n"
"s_nop 0\n"
"global_load_lds_dwordx4 v2, s[56:57] nt\n"
"v_mfma_f32_16x16x32_bf16 v[206:209], v[190:193], v[76:79], v[206:209]\n"
"s_add_u32 m0, s51, 141312\n"
"s_nop 0\n"
"global_load_lds_dwordx4 v2, s[58:59] nt\n"
"ds_read_b128 v[180:183], v12 offset:12288\n"
"v_mfma_f32_16x16x32_bf16 v[232:235], v[190:193], v[80:83], v[232:235]\n"
"s_add_u32 m0, s51, 143360\n"
"s_nop 0\n"
"global_load_lds_dwordx4 v2, s[60:61] nt\n"
"s_waitcnt lgkmcnt(1)\n"
"v_mfma_f32_16x16x32_bf16 v[200:203], v[216:219], v[84:87], v[200:203]\n"
"s_andn2_b32 s41, 288, s55\n"
"v_add_u32 v2, s41, v15\n"
"v_mfma_f32_16x16x32_bf16 v[206:209], v[216:219], v[88:91], v[206:209]\n"
"ds_read_u16 v2, v2 offset:128\n"
"v_mfma_f32_16x16x32_bf16 v[232:235], v[216:219], v[92:95], v[232:235]\n"
"s_waitcnt lgkmcnt(1)\n"
"ds_read_b128 v[190:193], v13 offset:12288\n"
"v_mfma_f32_16x16x32_bf16 v[216:219], v[180:183], v[28:31], 0\n"
"v_mfma_f32_16x16x32_bf16 v[226:229], v[180:183], v[32:35], 0\n"
"ds_read_b128 v[242:245], v12 offset:14336\n"
"v_cvt_pk_bf16_f32 v198, v200, v201\n"
"v_mfma_f32_16x16x32_bf16 v[252:255], v[180:183], v[24:27], 0\n"
"s_waitcnt lgkmcnt(2)\n"
"v_cvt_pk_bf16_f32 v199, v202, v203\n"
"v_mad_u32_u24 v2, v2, s40, v16\n"
"v_cvt_pk_bf16_f32 v224, v206, v207\n"
"v_cvt_pk_bf16_f32 v225, v208, v209\n"
"v_cvt_pk_bf16_f32 v250, v232, v233\n"
"s_waitcnt lgkmcnt(1)\n"
"v_mfma_f32_16x16x32_bf16 v[216:219], v[190:193], v[40:43], v[216:219]\n"
"s_add_u32 m0, s51, 151552\n"
"s_nop 0\n"
"global_load_lds_dwordx4 v2, s[56:57] nt\n"
"v_mfma_f32_16x16x32_bf16 v[226:229], v[190:193], v[44:47], v[226:229]\n"
"s_add_u32 m0, s51, 153600\n"
"s_nop 0\n"
"global_load_lds_dwordx4 v2, s[58:59] nt\n"
"ds_read_b128 v[180:183], v13 offset:14336\n"
"v_cvt_pk_bf16_f32 v251, v234, v235\n"
"v_mfma_f32_16x16x32_bf16 v[252:255], v[190:193], v[36:39], v[252:255]\n"
"s_add_u32 m0, s51, 155648\n"
"s_nop 0\n"
"global_load_lds_dwordx4 v2, s[60:61] nt\n"
"s_waitcnt lgkmcnt(1)\n"
"v_mfma_f32_16x16x32_bf16 v[216:219], v[242:245], v[48:51], v[216:219]\n"
"s_barrier\n"
"s_add_u32 s42, s48, 0\n"
"s_addc_u32 s43, s49, 0\n"
"s_add_u32 m0, s50, 24576\n"
"s_nop 0\n"
"global_load_lds_dwordx4 v3, s[42:43]\n"
"v_mfma_f32_16x16x32_bf16 v[226:229], v[242:245], v[52:55], v[226:229]\n"
"ds_read_b128 v[190:193], v12 offset:16384\n"
"v_mfma_f32_16x16x32_bf16 v[252:255], v[242:245], v[56:59], v[252:255]\n"
"s_add_u32 m0, s50, 25600\n"
"s_nop 0\n"
"global_load_lds_dwordx4 v4, s[42:43]\n"
"s_waitcnt lgkmcnt(1)\n"
"v_mfma_f32_16x16x32_bf16 v[216:219], v[180:183], v[60:63], v[216:219]\n"
"v_mfma_f32_16x16x32_bf16 v[226:229], v[180:183], v[64:67], v[226:229]\n"
"ds_read_b128 v[200:203], v13 offset:16384\n"
"v_mfma_f32_16x16x32_bf16 v[252:255], v[180:183], v[68:71], v[252:255]\n"
"s_add_u32 m0, s50, 26624\n"
"s_nop 0\n"
"global_load_lds_dwordx4 v5, s[42:43]\n"
"s_waitcnt lgkmcnt(1)\n"
"v_mfma_f32_16x16x32_bf16 v[216:219], v[190:193], v[72:75], v[216:219]\n"
"v_mfma_f32_16x16x32_bf16 v[226:229], v[190:193], v[76:79], v[226:229]\n"
"ds_read_b128 v[180:183], v12 offset:18432\n"
"v_mfma_f32_16x16x32_bf16 v[252:255], v[190:193], v[80:83], v[252:255]\n"
"s_add_u32 s42, s48, 18432\n"
"s_addc_u32 s43, s49, 0\n"
"s_add_u32 m0, s50, 27648\n"
"s_nop 0\n"
"global_load_lds_dwordx4 v3, s[42:43]\n"
"s_waitcnt lgkmcnt(1)\n"
"v_mfma_f32_16x16x32_bf16 v[216:219], v[200:203], v[84:87], v[216:219]\n"
"v_mfma_f32_16x16x32_bf16 v[226:229], v[200:203], v[88:91], v[226:229]\n"
"ds_read_b128 v[190:193], v13 offset:18432\n"
"s_add_u32 m0, s50, 28672\n"
"s_nop 0\n"
"global_load_lds_dwordx4 v4, s[42:43]\n"
"v_mfma_f32_16x16x32_bf16 v[252:255], v[200:203], v[92:95], v[252:255]\n"
"s_waitcnt lgkmcnt(1)\n"
"v_mfma_f32_16x16x32_bf16 v[200:203], v[180:183], v[28:31], 0\n"
"v_mfma_f32_16x16x32_bf16 v[242:245], v[180:183], v[32:35], 0\n"
"s_add_u32 m0, s50, 29696\n"
"s_nop 0\n"
"global_load_lds_dwordx4 v5, s[42:43]\n"
"ds_read_b128 v[96:99], v12 offset:20480\n"
"v_mfma_f32_16x16x32_bf16 v[112:115], v[180:183], v[24:27], 0\n"
"s_waitcnt lgkmcnt(1)\n"
"v_cvt_pk_bf16_f32 v180, v216, v217\n"
"v_cvt_pk_bf16_f32 v181, v218, v219\n"
"v_mfma_f32_16x16x32_bf16 v[200:203], v[190:193], v[40:43], v[200:203]\n"
"v_cvt_pk_bf16_f32 v206, v226, v227\n"
"v_cvt_pk_bf16_f32 v207, v228, v229\n"
"v_mfma_f32_16x16x32_bf16 v[242:245], v[190:193], v[44:47], v[242:245]\n"
"ds_read_b128 v[216:219], v13 offset:20480\n"
"v_cvt_pk_bf16_f32 v232, v252, v253\n"
"v_cvt_pk_bf16_f32 v233, v254, v255\n"
"v_mfma_f32_16x16x32_bf16 v[112:115], v[190:193], v[36:39], v[112:115]\n"
"s_waitcnt lgkmcnt(1)\n"
"v_mfma_f32_16x16x32_bf16 v[200:203], v[96:99], v[48:51], v[200:203]\n"
"v_mfma_f32_16x16x32_bf16 v[242:245], v[96:99], v[52:55], v[242:245]\n"
"ds_read_b128 v[190:193], v12 offset:22528\n"
"v_mfma_f32_16x16x32_bf16 v[112:115], v[96:99], v[56:59], v[112:115]\n"
"s_waitcnt vmcnt(12)\n"
"s_barrier\n"
"s_waitcnt vmcnt(3)\n"
"ds_read_b64_tr_b16 v[96:97], v11 offset:53248\n"
"s_waitcnt lgkmcnt(2)\n"
"v_mfma_f32_16x16x32_bf16 v[200:203], v[216:219], v[60:63], v[200:203]\n"
"ds_read_b64_tr_b16 v[98:99], v11 offset:54016\n"
"ds_read_b64_tr_b16 v[100:101], v11 offset:53256\n"
"v_mfma_f32_16x16x32_bf16 v[242:245], v[216:219], v[64:67], v[242:245]\n"
"ds_read_b64_tr_b16 v[102:103], v11 offset:54024\n"
"ds_read_b64_tr_b16 v[226:227], v10 offset:53312\n"
"v_mfma_f32_16x16x32_bf16 v[112:115], v[216:219], v[68:71], v[112:115]\n"
"ds_read_b64_tr_b16 v[228:229], v10 offset:54080\n"
"s_waitcnt vmcnt(0)\n"
"ds_read_b64_tr_b16 v[104:105], v11 offset:56320\n"
"s_waitcnt lgkmcnt(7)\n"
"v_mfma_f32_16x16x32_bf16 v[200:203], v[190:193], v[72:75], v[200:203]\n"
"ds_read_b64_tr_b16 v[106:107], v11 offset:57088\n"
"ds_read_b64_tr_b16 v[108:109], v11 offset:56328\n"
"v_mfma_f32_16x16x32_bf16 v[242:245], v[190:193], v[76:79], v[242:245]\n"
"ds_read_b64_tr_b16 v[110:111], v11 offset:57096\n"
"v_mov_b32 v216, 0x3f803f80\n"
"v_mfma_f32_16x16x32_bf16 v[112:115], v[190:193], v[80:83], v[112:115]\n"
"ds_read_b64_tr_b16 v[252:253], v10 offset:56384\n"
"v_mov_b32 v217, 0x3f803f80\n"
"v_mov_b32 v218, 0x3f803f80\n"
"v_mov_b32 v219, 0x3f803f80\n"
"v_mov_b32 v193, 0\n"
"s_waitcnt lgkmcnt(5)\n"
"s_add_u32 s42, s48, 36864\n"
"s_addc_u32 s43, s49, 0\n"
"s_add_u32 m0, s50, 24576\n"
"s_nop 0\n"
"global_load_lds_dwordx4 v3, s[42:43]\n"
"ds_read_b64_tr_b16 v[254:255], v10 offset:57152\n"
"ds_read_b128 v[116:119], v13 offset:22528\n"
"s_add_u32 m0, s50, 25600\n"
"s_nop 0\n"
"global_load_lds_dwordx4 v4, s[42:43]\n"
"s_add_u32 m0, s50, 26624\n"
"s_nop 0\n"
"global_load_lds_dwordx4 v5, s[42:43]\n"
"s_waitcnt lgkmcnt(0)\n"
"v_mfma_f32_16x16x32_bf16 v[200:203], v[116:119], v[84:87], v[200:203]\n"
"v_mfma_f32_16x16x32_bf16 v[242:245], v[116:119], v[88:91], v[242:245]\n"
"v_mfma_f32_16x16x32_bf16 v[112:115], v[116:119], v[92:95], v[112:115]\n"
"s_add_u32 s42, s48, 55296\n"
"s_addc_u32 s43, s49, 0\n"
"s_add_u32 m0, s50, 27648\n"
"s_nop 0\n"
"global_load_lds_dwordx4 v3, s[42:43]\n"
"s_add_u32 m0, s50, 28672\n"
"s_nop 0\n"
"global_load_lds_dwordx4 v4, s[42:43]\n"
"v_cvt_pk_bf16_f32 v182, v200, v201\n"
"v_cvt_pk_bf16_f32 v183, v202, v203\n"
"v_cvt_pk_bf16_f32 v208, v242, v243\n"
"v_cvt_pk_bf16_f32 v209, v244, v245\n"
"v_cvt_pk_bf16_f32 v234, v112, v113\n"
"v_cvt_pk_bf16_f32 v235, v114, v115\n"
"s_add_u32 m0, s50, 29696\n"
"s_nop 0\n"
"global_load_lds_dwordx4 v5, s[42:43]\n"
"s_waitcnt vmcnt(15)\n"
"s_barrier\n"
"ds_read2st64_b32 v[190:191], v9 offset0:112 offset1:113\n"
"ds_read_b32 v192, v9 offset:29184\n"
"ds_read2st64_b32 v[200:201], v9 offset0:116 offset1:117\n"
"ds_read2st64_b32 v[202:203], v9 offset0:118 offset1:119\n"
"s_waitcnt lgkmcnt(2)\n"
"v_mfma_f32_16x16x32_bf16 v[242:245], v[216:219], v[190:193], 0\n"
"s_waitcnt vmcnt(3)\n"
"ds_read_b64_tr_b16 v[0:1], v11 offset:53248\n"
"v_mfma_f32_16x16x32_bf16 v[136:139], v[186:189], v[190:193], 0\n"
"ds_read_b64_tr_b16 v[2:3], v11 offset:54016\n"
"v_mfma_f32_16x16x32_bf16 v[124:127], v[212:215], v[190:193], 0\n"
"ds_read_b64_tr_b16 v[112:113], v11 offset:53256\n"
"v_mfma_f32_16x16x32_bf16 v[128:131], v[238:241], v[190:193], 0\n"
"s_waitcnt lgkmcnt(3)\n"
"ds_read_b64_tr_b16 v[114:115], v11 offset:54024\n"
"v_mfma_f32_16x16x32_bf16 v[242:245], v[216:219], v[200:203], v[242:245]\n"
"ds_read_b64_tr_b16 v[140:141], v10 offset:53312\n"
"ds_read2st64_b32 v[190:191], v9 offset0:128 offset1:129\n"
"v_mfma_f32_16x16x32_bf16 v[136:139], v[194:197], v[200:203], v[136:139]\n"
"ds_read_b64_tr_b16 v[142:143], v10 offset:54080\n"
"ds_read2st64_b32 v[192:193], v9 offset0:130 offset1:131\n"
"v_mfma_f32_16x16x32_bf16 v[124:127], v[220:223], v[200:203], v[124:127]\n"
"s_waitcnt vmcnt(0)\n"
"ds_read_b64_tr_b16 v[116:117], v11 offset:56320\n"
"v_mfma_f32_16x16x32_bf16 v[128:131], v[246:249], v[200:203], v[128:131]\n"
"s_waitcnt lgkmcnt(2)\n"
"ds_read_b64_tr_b16 v[118:119], v11 offset:57088\n"
"ds_read_b32 v200, v9 offset:33792\n"
"ds_read2st64_b32 v[202:203], v9 offset0:134 offset1:135\n"
"s_add_u32 s42, s48, 12288\n"
"s_addc_u32 s43, s49, 0\n"
"s_add_u32 m0, s50, 24576\n"
"s_nop 0\n"
"global_load_lds_dwordx4 v6, s[42:43]\n"
"s_waitcnt lgkmcnt(4)\n"
"v_mfma_f32_16x16x32_bf16 v[242:245], v[216:219], v[190:193], v[242:245]\n"
"ds_read_b64_tr_b16 v[120:121], v11 offset:56328\n"
"v_mov_b32 v201, 0\n"
"v_mfma_f32_16x16x32_bf16 v[136:139], v[178:181], v[190:193], v[136:139]\n"
"ds_read_b64_tr_b16 v[122:123], v11 offset:57096\n"
"v_mov_b32 v135, 0\n"
"v_mfma_f32_16x16x32_bf16 v[124:127], v[204:207], v[190:193], v[124:127]\n"
"s_add_u32 m0, s50, 25600\n"
"s_nop 0\n"
"global_load_lds_dwordx4 v7, s[42:43]\n"
"ds_read_b64_tr_b16 v[144:145], v10 offset:56384\n"
"v_mfma_f32_16x16x32_bf16 v[128:131], v[230:233], v[190:193], v[128:131]\n"
"s_waitcnt lgkmcnt(3)\n"
"ds_read_b64_tr_b16 v[146:147], v10 offset:57152\n"
"v_mov_b32 v132, 0\n"
"v_mfma_f32_16x16x32_bf16 v[242:245], v[216:219], v[200:203], v[242:245]\n"
"ds_read_b32 v134, v9 offset:9216\n"
"v_mov_b32 v133, 0\n"
"s_add_u32 m0, s50, 26624\n"
"s_nop 0\n"
"global_load_lds_dwordx4 v8, s[42:43]\n"
"s_waitcnt lgkmcnt(1)\n"
"s_add_u32 s42, s48, 49152\n"
"s_addc_u32 s43, s49, 0\n"
"s_add_u32 m0, s50, 27648\n"
"s_nop 0\n"
"global_load_lds_dwordx4 v6, s[42:43]\n"
"s_waitcnt lgkmcnt(0)\n"
"v_mfma_f32_16x16x32_bf16 v[242:245], v[216:219], v[132:135], v[242:245]\n"
"v_mfma_f32_16x16x32_bf16 v[128:131], v[234:237], v[200:203], v[128:131]\n"
"s_add_u32 m0, s50, 28672\n"
"s_nop 0\n"
"global_load_lds_dwordx4 v7, s[42:43]\n"
"v_mfma_f32_16x16x32_bf16 v[128:131], v[248:251], v[132:135], v[128:131]\n"
"ds_read_b128 v[190:193], v12 offset:36864\n"
"v_mfma_f32_16x16x32_bf16 v[136:139], v[182:185], v[200:203], v[136:139]\n"
"v_mfma_f32_16x16x32_bf16 v[136:139], v[196:199], v[132:135], v[136:139]\n"
"s_add_u32 m0, s50, 29696\n"
"s_nop 0\n"
"global_load_lds_dwordx4 v8, s[42:43]\n"
"s_andn2_b32 s41, 608, s55\n"
"v_add_u32 v8, s41, v15\n"
"v_rcp_f32 v243, v242\n"
"v_mfma_f32_16x16x32_bf16 v[124:127], v[208:211], v[200:203], v[124:127]\n"
"ds_read_u16 v8, v8 offset:288\n"
"v_mul_f32 v128, v128, v243\n"
"v_mfma_f32_16x16x32_bf16 v[124:127], v[222:225], v[132:135], v[124:127]\n"
"s_waitcnt lgkmcnt(1)\n"
"ds_read_b128 v[200:203], v13 offset:36864\n"
"v_mul_f32 v129, v129, v243\n"
"v_cvt_pk_bf16_f32 v244, v128, v129\n"
"v_mfma_f32_16x16x32_bf16 v[216:219], v[190:193], v[28:31], 0\n"
"v_mul_f32 v130, v130, v243\n"
"v_mul_f32 v131, v131, v243\n"
"v_mfma_f32_16x16x32_bf16 v[132:135], v[190:193], v[32:35], 0\n"
"ds_read_b128 v[4:7], v12 offset:38912\n"
"v_cvt_pk_bf16_f32 v245, v130, v131\n"
"v_mfma_f32_16x16x32_bf16 v[128:131], v[190:193], v[24:27], 0\n"
"s_waitcnt lgkmcnt(2)\n"
"v_mul_f32 v136, v136, v243\n"
"v_mul_f32 v137, v137, v243\n"
"v_cvt_pk_bf16_f32 v136, v136, v137\n"
"v_mad_u32_u24 v8, v8, s40, v16\n"
"v_mul_f32 v138, v138, v243\n"
"v_mul_f32 v139, v139, v243\n"
"v_cvt_pk_bf16_f32 v137, v138, v139\n"
"s_waitcnt lgkmcnt(1)\n"
"v_mfma_f32_16x16x32_bf16 v[216:219], v[200:203], v[40:43], v[216:219]\n"
"s_add_u32 m0, s51, 114688\n"
"s_nop 0\n"
"global_load_lds_dwordx4 v8, s[56:57] nt\n"
"v_mfma_f32_16x16x32_bf16 v[132:135], v[200:203], v[44:47], v[132:135]\n"
"ds_read_b128 v[190:193], v13 offset:38912\n"
"v_mul_f32 v124, v124, v243\n"
"v_mfma_f32_16x16x32_bf16 v[128:131], v[200:203], v[36:39], v[128:131]\n"
"s_waitcnt lgkmcnt(1)\n"
"v_mul_f32 v125, v125, v243\n"
"v_cvt_pk_bf16_f32 v138, v124, v125\n"
"v_mfma_f32_16x16x32_bf16 v[216:219], v[4:7], v[48:51], v[216:219]\n"
"v_mul_f32 v126, v126, v243\n"
"v_mul_f32 v127, v127, v243\n"
"v_mfma_f32_16x16x32_bf16 v[132:135], v[4:7], v[52:55], v[132:135]\n"
"ds_read_b128 v[200:203], v12 offset:40960\n"
"v_cvt_pk_bf16_f32 v139, v126, v127\n"
"v_mfma_f32_16x16x32_bf16 v[128:131], v[4:7], v[56:59], v[128:131]\n"
"s_add_u32 m0, s51, 116736\n"
"s_nop 0\n"
"global_load_lds_dwordx4 v8, s[58:59] nt\n"
"s_waitcnt lgkmcnt(1)\n"
"v_mfma_f32_16x16x32_bf16 v[216:219], v[190:193], v[60:63], v[216:219]\n"
"v_mfma_f32_16x16x32_bf16 v[132:135], v[190:193], v[64:67], v[132:135]\n"
"ds_read_b128 v[4:7], v13 offset:40960\n"
"v_mfma_f32_16x16x32_bf16 v[128:131], v[190:193], v[68:71], v[128:131]\n"
"s_waitcnt lgkmcnt(1)\n"
"v_mfma_f32_16x16x32_bf16 v[216:219], v[200:203], v[72:75], v[216:219]\n"
"v_mfma_f32_16x16x32_bf16 v[132:135], v[200:203], v[76:79], v[132:135]\n"
"ds_read_b128 v[190:193], v12 offset:43008\n"
"v_mfma_f32_16x16x32_bf16 v[128:131], v[200:203], v[80:83], v[128:131]\n"
"s_add_u32 m0, s51, 118784\n"
"s_nop 0\n"
"global_load_lds_dwordx4 v8, s[60:61] nt\n"
"s_waitcnt lgkmcnt(1)\n"
"v_mfma_f32_16x16x32_bf16 v[216:219], v[4:7], v[84:87], v[216:219]\n"
"v_mfma_f32_16x16x32_bf16 v[132:135], v[4:7], v[88:91], v[132:135]\n"
"ds_read_b128 v[200:203], v13 offset:43008\n"
"v_mfma_f32_16x16x32_bf16 v[128:131], v[4:7], v[92:95], v[128:131]\n"
"s_waitcnt lgkmcnt(1)\n"
"v_mfma_f32_16x16x32_bf16 v[148:151], v[190:193], v[28:31], 0\n"
"v_mfma_f32_16x16x32_bf16 v[152:155], v[190:193], v[32:35], 0\n"
"ds_read_b128 v[156:159], v12 offset:45056\n"
"v_mfma_f32_16x16x32_bf16 v[160:163], v[190:193], v[24:27], 0\n"
"s_waitcnt vmcnt(21)\n"
"s_barrier\n"
"s_waitcnt vmcnt(6)\n"
"ds_read_b64_tr_b16 v[4:5], v11 offset:53248\n"
"v_cvt_pk_bf16_f32 v190, v216, v217\n"
"v_cvt_pk_bf16_f32 v191, v218, v219\n"
"s_waitcnt lgkmcnt(2)\n"
"v_mfma_f32_16x16x32_bf16 v[148:151], v[200:203], v[40:43], v[148:151]\n"
"s_mov_b32 m0, s54\n"
"ds_read_b64_tr_b16 v[6:7], v11 offset:54016\n"
"v_cvt_pk_bf16_f32 v216, v132, v133\n"
"v_cvt_pk_bf16_f32 v217, v134, v135\n"
"v_mfma_f32_16x16x32_bf16 v[152:155], v[200:203], v[44:47], v[152:155]\n"
"ds_read_b64_tr_b16 v[124:125], v11 offset:53256\n"
"v_cvt_pk_bf16_f32 v242, v128, v129\n"
"v_cvt_pk_bf16_f32 v243, v130, v131\n"
"v_mfma_f32_16x16x32_bf16 v[160:163], v[200:203], v[36:39], v[160:163]\n"
"s_waitcnt lgkmcnt(3)\n"
"ds_read_b64_tr_b16 v[126:127], v11 offset:54024\n"
"ds_read_b64_tr_b16 v[200:201], v10 offset:53312\n"
"v_mfma_f32_16x16x32_bf16 v[148:151], v[156:159], v[48:51], v[148:151]\n"
"ds_read_b64_tr_b16 v[202:203], v10 offset:54080\n"
"s_waitcnt vmcnt(3)\n"
"ds_read_b64_tr_b16 v[128:129], v11 offset:56320\n"
"v_mfma_f32_16x16x32_bf16 v[152:155], v[156:159], v[52:55], v[152:155]\n"
"ds_read_b64_tr_b16 v[130:131], v11 offset:57088\n"
"ds_read_b64_tr_b16 v[132:133], v11 offset:56328\n"
"v_mfma_f32_16x16x32_bf16 v[160:163], v[156:159], v[56:59], v[160:163]\n"
"ds_read_b64_tr_b16 v[134:135], v11 offset:57096\n"
"ds_read_b64_tr_b16 v[156:157], v10 offset:56384\n"
"ds_read_b64_tr_b16 v[158:159], v10 offset:57152\n"
"ds_read_b128 v[164:167], v13 offset:45056\n"
"ds_write_addtid_b32 v244 offset:33280\n"
"ds_write_addtid_b32 v245 offset:33536\n"
"ds_write_addtid_b32 v136 offset:32256\n"
"ds_read_b128 v[168:171], v12 offset:47104\n"
"ds_write_addtid_b32 v137 offset:32512\n"
"ds_write_addtid_b32 v138 offset:32768\n"
"ds_write_addtid_b32 v139 offset:33024\n"
"s_waitcnt lgkmcnt(8)\n"
"ds_write_b128 v17, v[226:229] offset:53248\n"
"s_waitcnt lgkmcnt(8)\n"
"v_mfma_f32_16x16x32_bf16 v[148:151], v[164:167], v[60:63], v[148:151]\n"
"v_mfma_f32_16x16x32_bf16 v[152:155], v[164:167], v[64:67], v[152:155]\n"
"ds_write_b128 v17, v[252:255] offset:54272\n"
"v_mfma_f32_16x16x32_bf16 v[160:163], v[164:167], v[68:71], v[160:163]\n"
"s_waitcnt lgkmcnt(5)\n"
"ds_read_b128 v[226:229], v13 offset:47104\n"
"v_mfma_f32_16x16x32_bf16 v[148:151], v[168:171], v[72:75], v[148:151]\n"
"ds_write_b128 v17, v[140:143] offset:55296\n"
"v_mfma_f32_16x16x32_bf16 v[152:155], v[168:171], v[76:79], v[152:155]\n"
"v_mov_b32 v136, 0x3f803f80\n"
"v_mov_b32 v137, 0x3f803f80\n"
"v_mfma_f32_16x16x32_bf16 v[160:163], v[168:171], v[80:83], v[160:163]\n"
"s_and_b32 s41, 480, s55\n"
"v_add_u32 v8, s41, v15\n"
"v_mov_b32 v138, 0x3f803f80\n"
"v_mov_b32 v139, 0x3f803f80\n"
"ds_write_b128 v17, v[144:147] offset:56320\n"
"s_waitcnt lgkmcnt(2)\n"
"v_mfma_f32_16x16x32_bf16 v[148:151], v[226:229], v[84:87], v[148:151]\n"
"s_waitcnt vmcnt(0)\n"
"s_barrier\n"
"ds_read2st64_b32 v[252:253], v9 offset0:120 offset1:121\n"
"ds_read2st64_b32 v[254:255], v9 offset0:122 offset1:123\n"
"v_mfma_f32_16x16x32_bf16 v[152:155], v[226:229], v[88:91], v[152:155]\n"
"ds_write_b128 v17, v[200:203] offset:57344\n"
"v_mfma_f32_16x16x32_bf16 v[160:163], v[226:229], v[92:95], v[160:163]\n"
"ds_read_u16 v8, v8 offset:448\n"
"ds_read2st64_b32 v[140:141], v9 offset0:124 offset1:125\n"
"v_cvt_pk_bf16_f32 v200, v148, v149\n"
"v_cvt_pk_bf16_f32 v201, v150, v151\n"
"ds_read2st64_b32 v[142:143], v9 offset0:126 offset1:127\n"
"ds_write_b128 v17, v[156:159] offset:58368\n"
"s_waitcnt lgkmcnt(5)\n"
"v_mfma_f32_16x16x32_bf16 v[144:147], v[136:139], v[252:255], 0\n"
"ds_read2st64_b32 v[150:151], v9 offset0:128 offset1:129\n"
"v_cvt_pk_bf16_f32 v226, v152, v153\n"
"v_mfma_f32_16x16x32_bf16 v[156:159], v[186:189], v[252:255], 0\n"
"s_waitcnt lgkmcnt(4)\n"
"ds_read2st64_b32 v[152:153], v9 offset0:130 offset1:131\n"
"v_mad_u32_u24 v8, v8, s40, v16\n"
"v_mfma_f32_16x16x32_bf16 v[164:167], v[212:215], v[252:255], 0\n"
"s_add_u32 m0, s51, 139264\n"
"s_nop 0\n"
"global_load_lds_dwordx4 v8, s[56:57] nt\n"
"v_mfma_f32_16x16x32_bf16 v[168:171], v[238:241], v[252:255], 0\n"
"s_waitcnt lgkmcnt(3)\n"
"v_cvt_pk_bf16_f32 v227, v154, v155\n"
"v_cvt_pk_bf16_f32 v252, v160, v161\n"
"v_mfma_f32_16x16x32_bf16 v[144:147], v[136:139], v[140:143], v[144:147]\n"
"v_cvt_pk_bf16_f32 v253, v162, v163\n"
"v_mov_b32 v148, 0\n"
"v_mov_b32 v149, 0\n"
"v_mov_b32 v154, 0\n"
"v_mov_b32 v155, 0\n"
"s_waitcnt lgkmcnt(0)\n"
"v_mfma_f32_16x16x32_bf16 v[144:147], v[136:139], v[150:153], v[144:147]\n"
"v_mfma_f32_16x16x32_bf16 v[168:171], v[250:253], v[140:143], v[168:171]\n"
"v_mfma_f32_16x16x32_bf16 v[168:171], v[240:243], v[148:151], v[168:171]\n"
"s_add_u32 m0, s51, 141312\n"
"s_nop 0\n"
"global_load_lds_dwordx4 v8, s[58:59] nt\n"
"v_mfma_f32_16x16x32_bf16 v[168:171], v[248:251], v[152:155], v[168:171]\n"
"v_mfma_f32_16x16x32_bf16 v[156:159], v[198:201], v[140:143], v[156:159]\n"
"v_mfma_f32_16x16x32_bf16 v[156:159], v[188:191], v[148:151], v[156:159]\n"
"ds_read2st64_b32 v[136:137], v14 offset0:126 offset1:127\n"
"v_mfma_f32_16x16x32_bf16 v[156:159], v[196:199], v[152:155], v[156:159]\n"
"ds_read2st64_b32 v[138:139], v14 offset0:128 offset1:129\n"
"v_rcp_f32 v255, v144\n"
"v_mfma_f32_16x16x32_bf16 v[164:167], v[224:227], v[140:143], v[164:167]\n"
"ds_read_b128 v[140:143], v17 offset:53248\n"
"v_mul_f32 v168, v168, v255\n"
"v_mfma_f32_16x16x32_bf16 v[164:167], v[214:217], v[148:151], v[164:167]\n"
"s_add_u32 m0, s51, 143360\n"
"s_nop 0\n"
"global_load_lds_dwordx4 v8, s[60:61] nt\n"
"ds_read2st64_b32 v[144:145], v14 offset0:150 offset1:151\n"
"v_mfma_f32_16x16x32_bf16 v[164:167], v[222:225], v[152:155], v[164:167]\n"
"ds_read2st64_b32 v[146:147], v14 offset0:152 offset1:153\n"
"v_mul_f32 v169, v169, v255\n"
"v_cvt_pk_bf16_f32 v244, v168, v169\n"
"v_mul_f32 v170, v170, v255\n"
"v_mul_f32 v171, v171, v255\n"
"v_cvt_pk_bf16_f32 v245, v170, v171\n"
"s_waitcnt lgkmcnt(3)\n"
"v_mfma_f32_16x16x32_bf16 v[148:151], v[96:99], v[136:139], 0\n"
"ds_read_b128 v[152:155], v17 offset:54272\n"
"v_mul_f32 v156, v156, v255\n"
"v_mfma_f32_16x16x32_bf16 v[160:163], v[100:103], v[136:139], 0\n"
"s_waitcnt lgkmcnt(3)\n"
"ds_read2st64_b32 v[168:169], v14 offset0:174 offset1:175\n"
"v_mul_f32 v157, v157, v255\n"
"v_mfma_f32_16x16x32_bf16 v[172:175], v[140:143], v[136:139], 0\n"
"ds_read2st64_b32 v[170:171], v14 offset0:176 offset1:177\n"
"v_cvt_pk_bf16_f32 v156, v156, v157\n"
"v_mul_f32 v158, v158, v255\n"
"v_mul_f32 v159, v159, v255\n"
"s_waitcnt lgkmcnt(3)\n"
"v_mfma_f32_16x16x32_bf16 v[148:151], v[104:107], v[144:147], v[148:151]\n"
"ds_read_b128 v[136:139], v17 offset:55296\n"
"v_cvt_pk_bf16_f32 v157, v158, v159\n"
"v_mfma_f32_16x16x32_bf16 v[160:163], v[108:111], v[144:147], v[160:163]\n"
"s_waitcnt lgkmcnt(3)\n"
"ds_read2st64_b32 v[140:141], v14 offset0:198 offset1:199\n"
"v_mul_f32 v164, v164, v255\n"
"v_mfma_f32_16x16x32_bf16 v[172:175], v[152:155], v[144:147], v[172:175]\n"
"ds_read2st64_b32 v[142:143], v14 offset0:200 offset1:201\n"
"v_mul_f32 v165, v165, v255\n"
"v_cvt_pk_bf16_f32 v158, v164, v165\n"
"v_mul_f32 v166, v166, v255\n"
"v_mul_f32 v167, v167, v255\n"
"v_cvt_pk_bf16_f32 v159, v166, v167\n"
"s_waitcnt lgkmcnt(3)\n"
"v_mfma_f32_16x16x32_bf16 v[148:151], v[0:3], v[168:171], v[148:151]\n"
"ds_read_b128 v[144:147], v17 offset:56320\n"
"v_mfma_f32_16x16x32_bf16 v[160:163], v[112:115], v[168:171], v[160:163]\n"
"s_waitcnt lgkmcnt(3)\n"
"ds_read2st64_b32 v[152:153], v14 offset0:130 offset1:131\n"
"v_mfma_f32_16x16x32_bf16 v[172:175], v[136:139], v[168:171], v[172:175]\n"
"s_waitcnt lgkmcnt(2)\n"
"ds_read2st64_b32 v[154:155], v14 offset0:154 offset1:155\n"
"v_mfma_f32_16x16x32_bf16 v[148:151], v[116:119], v[140:143], v[148:151]\n"
"ds_read_b128 v[136:139], v17 offset:57344\n"
"v_mfma_f32_16x16x32_bf16 v[160:163], v[120:123], v[140:143], v[160:163]\n"
"s_waitcnt lgkmcnt(3)\n"
"ds_read2st64_b32 v[164:165], v14 offset0:178 offset1:179\n"
"v_mfma_f32_16x16x32_bf16 v[172:175], v[144:147], v[140:143], v[172:175]\n"
"s_waitcnt lgkmcnt(2)\n"
"ds_read2st64_b32 v[166:167], v14 offset0:202 offset1:203\n"
"v_mfma_f32_16x16x32_bf16 v[148:151], v[4:7], v[152:155], v[148:151]\n"
"ds_read_b128 v[140:143], v17 offset:58368\n"
"v_mfma_f32_16x16x32_bf16 v[160:163], v[124:127], v[152:155], v[160:163]\n"
"s_mov_b32 m0, s54\n"
"ds_read_b128 v[144:147], v12 offset:49152\n"
"s_waitcnt lgkmcnt(4)\n"
"v_mfma_f32_16x16x32_bf16 v[172:175], v[136:139], v[152:155], v[172:175]\n"
"s_waitcnt lgkmcnt(2)\n"
"v_mfma_f32_16x16x32_bf16 v[148:151], v[128:131], v[164:167], v[148:151]\n"
"v_mfma_f32_16x16x32_bf16 v[160:163], v[132:135], v[164:167], v[160:163]\n"
"s_barrier\n"
"ds_write_addtid_b32 v244 offset:33280\n"
"s_waitcnt lgkmcnt(2)\n"
"v_mfma_f32_16x16x32_bf16 v[172:175], v[140:143], v[164:167], v[172:175]\n"
"ds_write_addtid_b32 v245 offset:33536\n"
"ds_write_addtid_b32 v156 offset:32256\n"
"s_waitcnt lgkmcnt(3)\n"
"v_mfma_f32_16x16x32_bf16 v[136:139], v[144:147], v[28:31], 0\n"
"ds_write_addtid_b32 v157 offset:32512\n"
"v_cvt_pk_bf16_f32 v148, v148, v149\n"
"v_cvt_pk_bf16_f32 v149, v150, v151\n"
"v_cvt_pk_bf16_f32 v150, v160, v161\n"
"v_cvt_pk_bf16_f32 v151, v162, v163\n"
"v_cvt_pk_bf16_f32 v172, v172, v173\n"
"v_cvt_pk_bf16_f32 v173, v174, v175\n"
"v_mfma_f32_16x16x32_bf16 v[140:143], v[144:147], v[32:35], 0\n"
"ds_write_addtid_b32 v158 offset:32768\n"
"ds_write_addtid_b32 v159 offset:33024\n"
"s_mov_b32 m0, s68\n"
"ds_read_b128 v[152:155], v13 offset:49152\n"
"v_mfma_f32_16x16x32_bf16 v[156:159], v[144:147], v[24:27], 0\n"
"ds_write_addtid_b32 v148 offset:45056\n"
"ds_write_addtid_b32 v149 offset:45312\n"
"ds_write_addtid_b32 v150 offset:45568\n"
"ds_write_addtid_b32 v151 offset:45824\n"
"ds_write_addtid_b32 v172 offset:46080\n"
"ds_write_addtid_b32 v173 offset:46336\n"
"s_and_b32 s41, 32, s55\n"
"v_add_u32 v8, s41, v15\n"
"ds_read_u16 v8, v8 offset:480\n"
"ds_read_b128 v[144:147], v12 offset:51200\n"
"s_waitcnt lgkmcnt(8)\n"
"v_mfma_f32_16x16x32_bf16 v[136:139], v[152:155], v[40:43], v[136:139]\n"
"v_mfma_f32_16x16x32_bf16 v[140:143], v[152:155], v[44:47], v[140:143]\n"
"ds_read_b128 v[148:151], v13 offset:51200\n"
"v_mfma_f32_16x16x32_bf16 v[156:159], v[152:155], v[36:39], v[156:159]\n"
"s_waitcnt lgkmcnt(2)\n"
"v_mad_u32_u24 v8, v8, s40, v16\n"
"s_waitcnt lgkmcnt(1)\n"
"v_mfma_f32_16x16x32_bf16 v[136:139], v[144:147], v[48:51], v[136:139]\n"
"s_add_u32 m0, s51, 126976\n"
"s_nop 0\n"
"global_load_lds_dwordx4 v8, s[56:57] nt\n"
"v_mfma_f32_16x16x32_bf16 v[140:143], v[144:147], v[52:55], v[140:143]\n"
"ds_read_b128 v[152:155], v12 offset:53248\n"
"v_mfma_f32_16x16x32_bf16 v[156:159], v[144:147], v[56:59], v[156:159]\n"
"s_waitcnt lgkmcnt(1)\n"
"v_mfma_f32_16x16x32_bf16 v[136:139], v[148:151], v[60:63], v[136:139]\n"
"v_mfma_f32_16x16x32_bf16 v[140:143], v[148:151], v[64:67], v[140:143]\n"
"ds_read_b128 v[144:147], v13 offset:53248\n"
"v_mfma_f32_16x16x32_bf16 v[156:159], v[148:151], v[68:71], v[156:159]\n"
"s_waitcnt lgkmcnt(1)\n"
"v_mfma_f32_16x16x32_bf16 v[136:139], v[152:155], v[72:75], v[136:139]\n"
"s_add_u32 m0, s51, 129024\n"
"s_nop 0\n"
"global_load_lds_dwordx4 v8, s[58:59] nt\n"
"v_mfma_f32_16x16x32_bf16 v[140:143], v[152:155], v[76:79], v[140:143]\n"
"ds_read_b128 v[148:151], v12 offset:55296\n"
"v_mfma_f32_16x16x32_bf16 v[156:159], v[152:155], v[80:83], v[156:159]\n"
"s_waitcnt lgkmcnt(1)\n"
"v_mfma_f32_16x16x32_bf16 v[136:139], v[144:147], v[84:87], v[136:139]\n"
"v_mfma_f32_16x16x32_bf16 v[140:143], v[144:147], v[88:91], v[140:143]\n"
"ds_read_b128 v[152:155], v13 offset:55296\n"
"v_mfma_f32_16x16x32_bf16 v[156:159], v[144:147], v[92:95], v[156:159]\n"
"s_waitcnt lgkmcnt(1)\n"
"v_mfma_f32_16x16x32_bf16 v[144:147], v[148:151], v[28:31], 0\n"
"s_add_u32 m0, s51, 131072\n"
"s_nop 0\n"
"global_load_lds_dwordx4 v8, s[60:61] nt\n"
"v_mfma_f32_16x16x32_bf16 v[160:163], v[148:151], v[32:35], 0\n"
"ds_read_b128 v[164:167], v12 offset:57344\n"
"v_cvt_pk_bf16_f32 v184, v136, v137\n"
"v_mfma_f32_16x16x32_bf16 v[168:171], v[148:151], v[24:27], 0\n"
"s_waitcnt lgkmcnt(1)\n"
"v_cvt_pk_bf16_f32 v185, v138, v139\n"
"v_cvt_pk_bf16_f32 v210, v140, v141\n"
"v_mfma_f32_16x16x32_bf16 v[144:147], v[152:155], v[40:43], v[144:147]\n"
"v_cvt_pk_bf16_f32 v211, v142, v143\n"
"v_cvt_pk_bf16_f32 v236, v156, v157\n"
"v_mfma_f32_16x16x32_bf16 v[160:163], v[152:155], v[44:47], v[160:163]\n"
"ds_read_b128 v[136:139], v13 offset:57344\n"
"v_cvt_pk_bf16_f32 v237, v158, v159\n"
"v_mfma_f32_16x16x32_bf16 v[168:171], v[152:155], v[36:39], v[168:171]\n"
"s_waitcnt lgkmcnt(1)\n"
"v_mfma_f32_16x16x32_bf16 v[144:147], v[164:167], v[48:51], v[144:147]\n"
"v_mfma_f32_16x16x32_bf16 v[160:163], v[164:167], v[52:55], v[160:163]\n"
"ds_read_b128 v[140:143], v12 offset:59392\n"
"v_mfma_f32_16x16x32_bf16 v[168:171], v[164:167], v[56:59], v[168:171]\n"
"s_waitcnt lgkmcnt(1)\n"
"v_mfma_f32_16x16x32_bf16 v[144:147], v[136:139], v[60:63], v[144:147]\n"
"v_mfma_f32_16x16x32_bf16 v[160:163], v[136:139], v[64:67], v[160:163]\n"
"ds_read_b128 v[148:151], v13 offset:59392\n"
"v_mfma_f32_16x16x32_bf16 v[168:171], v[136:139], v[68:71], v[168:171]\n"
"s_waitcnt lgkmcnt(1)\n"
"v_mfma_f32_16x16x32_bf16 v[144:147], v[140:143], v[72:75], v[144:147]\n"
"v_mfma_f32_16x16x32_bf16 v[160:163], v[140:143], v[76:79], v[160:163]\n"
"v_mov_b32 v136, 0x3f803f80\n"
"v_mov_b32 v137, 0x3f803f80\n"
"v_mfma_f32_16x16x32_bf16 v[168:171], v[140:143], v[80:83], v[168:171]\n"
"s_waitcnt lgkmcnt(0)\n"
"v_mov_b32 v138, 0x3f803f80\n"
"v_mov_b32 v139, 0x3f803f80\n"
"v_mfma_f32_16x16x32_bf16 v[144:147], v[148:151], v[84:87], v[144:147]\n"
"s_waitcnt vmcnt(3)\n"
"s_barrier\n"
"ds_read2st64_b32 v[140:141], v9 offset0:112 offset1:113\n"
"ds_read2st64_b32 v[142:143], v9 offset0:114 offset1:115\n"
"v_mfma_f32_16x16x32_bf16 v[160:163], v[148:151], v[88:91], v[160:163]\n"
"s_and_b32 s41, 416, s55\n"
"v_add_u32 v8, s41, v15\n"
"v_mfma_f32_16x16x32_bf16 v[168:171], v[148:151], v[92:95], v[168:171]\n"
"ds_read2st64_b32 v[148:149], v9 offset0:116 offset1:117\n"
"v_cvt_pk_bf16_f32 v192, v144, v145\n"
"v_cvt_pk_bf16_f32 v193, v146, v147\n"
"ds_read2st64_b32 v[150:151], v9 offset0:118 offset1:119\n"
"ds_read_u16 v8, v8 offset:544\n"
"v_cvt_pk_bf16_f32 v218, v160, v161\n"
"v_cvt_pk_bf16_f32 v219, v162, v163\n"
"s_waitcnt lgkmcnt(3)\n"
"v_mfma_f32_16x16x32_bf16 v[144:147], v[136:139], v[140:143], 0\n"
"v_cvt_pk_bf16_f32 v244, v168, v169\n"
"v_cvt_pk_bf16_f32 v245, v170, v171\n"
"v_mfma_f32_16x16x32_bf16 v[152:155], v[180:183], v[140:143], 0\n"
"v_mfma_f32_16x16x32_bf16 v[156:159], v[206:209], v[140:143], 0\n"
"ds_read_u16 v255, v21 offset:800\n"
"v_mfma_f32_16x16x32_bf16 v[160:163], v[232:235], v[140:143], 0\n"
"s_waitcnt lgkmcnt(1)\n"
"ds_read2st64_b32 v[140:141], v20 offset0:0 offset1:1\n"
"v_mad_u32_u24 v8, v8, s40, v16\n"
"v_mfma_f32_16x16x32_bf16 v[144:147], v[136:139], v[148:151], v[144:147]\n"
"s_add_u32 m0, s51, 151552\n"
"s_nop 0\n"
"global_load_lds_dwordx4 v8, s[56:57] nt\n"
"ds_read2st64_b32 v[142:143], v20 offset0:2 offset1:3\n"
"v_mfma_f32_16x16x32_bf16 v[152:155], v[188:191], v[148:151], v[152:155]\n"
"ds_read2st64_b32 v[164:165], v9 offset0:128 offset1:129\n"
"ds_read2st64_b32 v[166:167], v9 offset0:130 offset1:131\n"
"v_mfma_f32_16x16x32_bf16 v[156:159], v[214:217], v[148:151], v[156:159]\n"
"s_waitcnt lgkmcnt(4)\n"
"ds_read2st64_b32 v[228:229], v20 offset0:4 offset1:5\n"
"v_mad_u32_u24 v254, v255, s40, v18\n"
"v_mfma_f32_16x16x32_bf16 v[160:163], v[240:243], v[148:151], v[160:163]\n"
"s_waitcnt lgkmcnt(3)\n"
"ds_read2st64_b32 v[148:149], v9 offset0:132 offset1:133\n"
"v_mad_u32_u24 v255, v255, s40, v19\n"
"ds_read2st64_b32 v[150:151], v9 offset0:134 offset1:135\n"
"global_store_dwordx4 v254, v[140:143], s[66:67] sc1\n"
"s_waitcnt lgkmcnt(3)\n"
"v_mfma_f32_16x16x32_bf16 v[144:147], v[136:139], v[164:167], v[144:147]\n"
"s_add_u32 m0, s51, 153600\n"
"s_nop 0\n"
"global_load_lds_dwordx4 v8, s[58:59] nt\n"
"ds_read2st64_b32 v[142:143], v9 offset0:36 offset1:37\n"
"v_mfma_f32_16x16x32_bf16 v[152:155], v[184:187], v[164:167], v[152:155]\n"
"s_waitcnt lgkmcnt(3)\n"
"global_store_dwordx2 v255, v[228:229], s[66:67] offset:256 sc1\n"
"v_mfma_f32_16x16x32_bf16 v[156:159], v[210:213], v[164:167], v[156:159]\n"
"v_mov_b32 v140, 0\n"
"v_mov_b32 v141, 0\n"
"v_mfma_f32_16x16x32_bf16 v[160:163], v[236:239], v[164:167], v[160:163]\n"
"s_waitcnt lgkmcnt(1)\n"
"v_mfma_f32_16x16x32_bf16 v[144:147], v[136:139], v[148:151], v[144:147]\n"
"s_waitcnt lgkmcnt(0)\n"
"v_mfma_f32_16x16x32_bf16 v[144:147], v[136:139], v[140:143], v[144:147]\n"
"s_add_u32 m0, s51, 155648\n"
"s_nop 0\n"
"global_load_lds_dwordx4 v8, s[60:61] nt\n"
"v_mfma_f32_16x16x32_bf16 v[160:163], v[248:251], v[148:151], v[160:163]\n"
"v_mfma_f32_16x16x32_bf16 v[160:163], v[250:253], v[140:143], v[160:163]\n"
"ds_read2st64_b32 v[136:137], v14 offset0:126 offset1:127\n"
"v_mfma_f32_16x16x32_bf16 v[152:155], v[196:199], v[148:151], v[152:155]\n"
"ds_read2st64_b32 v[138:139], v14 offset0:128 offset1:129\n"
"v_mfma_f32_16x16x32_bf16 v[152:155], v[198:201], v[140:143], v[152:155]\n"
"ds_read_b128 v[164:167], v17 offset:53248\n"
"v_rcp_f32 v8, v144\n"
"v_mfma_f32_16x16x32_bf16 v[156:159], v[222:225], v[148:151], v[156:159]\n"
"ds_read2st64_b32 v[144:145], v14 offset0:150 offset1:151\n"
"v_mul_f32 v160, v160, v8\n"
"v_mfma_f32_16x16x32_bf16 v[156:159], v[224:227], v[140:143], v[156:159]\n"
"ds_read2st64_b32 v[146:147], v14 offset0:152 offset1:153\n"
"v_mul_f32 v161, v161, v8\n"
"v_cvt_pk_bf16_f32 v254, v160, v161\n"
"v_mul_f32 v162, v162, v8\n"
"v_mul_f32 v163, v163, v8\n"
"v_cvt_pk_bf16_f32 v255, v162, v163\n"
"v_mul_f32 v152, v152, v8\n"
"s_waitcnt lgkmcnt(3)\n"
"v_mfma_f32_16x16x32_bf16 v[140:143], v[96:99], v[136:139], 0\n"
"ds_read_b128 v[148:151], v17 offset:54272\n"
"v_mul_f32 v153, v153, v8\n"
"v_mfma_f32_16x16x32_bf16 v[160:163], v[100:103], v[136:139], 0\n"
"s_waitcnt lgkmcnt(3)\n"
"ds_read2st64_b32 v[168:169], v14 offset0:174 offset1:175\n"
"v_cvt_pk_bf16_f32 v152, v152, v153\n"
"v_mfma_f32_16x16x32_bf16 v[172:175], v[164:167], v[136:139], 0\n"
"s_waitcnt lgkmcnt(2)\n"
"ds_read2st64_b32 v[170:171], v14 offset0:176 offset1:177\n"
"v_mul_f32 v154, v154, v8\n"
"v_mul_f32 v155, v155, v8\n"
"v_mfma_f32_16x16x32_bf16 v[140:143], v[104:107], v[144:147], v[140:143]\n"
"ds_read_b128 v[136:139], v17 offset:55296\n"
"v_cvt_pk_bf16_f32 v153, v154, v155\n"
"v_mfma_f32_16x16x32_bf16 v[160:163], v[108:111], v[144:147], v[160:163]\n"
"s_waitcnt lgkmcnt(3)\n"
"ds_read2st64_b32 v[164:165], v14 offset0:198 offset1:199\n"
"v_mul_f32 v156, v156, v8\n"
"v_mul_f32 v157, v157, v8\n"
"v_mfma_f32_16x16x32_bf16 v[172:175], v[148:151], v[144:147], v[172:175]\n"
"ds_read2st64_b32 v[166:167], v14 offset0:200 offset1:201\n"
"v_cvt_pk_bf16_f32 v154, v156, v157\n"
"v_mul_f32 v158, v158, v8\n"
"v_mul_f32 v159, v159, v8\n"
"v_cvt_pk_bf16_f32 v155, v158, v159\n"
"s_waitcnt lgkmcnt(3)\n"
"v_mfma_f32_16x16x32_bf16 v[140:143], v[0:3], v[168:171], v[140:143]\n"
"ds_read_b128 v[144:147], v17 offset:56320\n"
"v_mfma_f32_16x16x32_bf16 v[160:163], v[112:115], v[168:171], v[160:163]\n"
"s_waitcnt lgkmcnt(3)\n"
"ds_read2st64_b32 v[148:149], v14 offset0:130 offset1:131\n"
"v_mfma_f32_16x16x32_bf16 v[172:175], v[136:139], v[168:171], v[172:175]\n"
"s_waitcnt lgkmcnt(2)\n"
"ds_read2st64_b32 v[150:151], v14 offset0:154 offset1:155\n"
"v_mfma_f32_16x16x32_bf16 v[140:143], v[116:119], v[164:167], v[140:143]\n"
"ds_read_b128 v[136:139], v17 offset:57344\n"
"v_mfma_f32_16x16x32_bf16 v[160:163], v[120:123], v[164:167], v[160:163]\n"
"s_waitcnt lgkmcnt(3)\n"
"ds_read2st64_b32 v[156:157], v14 offset0:178 offset1:179\n"
"v_mfma_f32_16x16x32_bf16 v[172:175], v[144:147], v[164:167], v[172:175]\n"
"s_waitcnt lgkmcnt(2)\n"
"ds_read2st64_b32 v[158:159], v14 offset0:202 offset1:203\n"
"v_mfma_f32_16x16x32_bf16 v[140:143], v[4:7], v[148:151], v[140:143]\n"
"ds_read_b128 v[144:147], v17 offset:58368\n"
"v_mfma_f32_16x16x32_bf16 v[160:163], v[124:127], v[148:151], v[160:163]\n"
"s_waitcnt lgkmcnt(3)\n"
"ds_read_b128 v[164:167], v12 offset:18432\n"
"v_mfma_f32_16x16x32_bf16 v[172:175], v[136:139], v[148:151], v[172:175]\n"
"s_waitcnt lgkmcnt(2)\n"
"v_mfma_f32_16x16x32_bf16 v[140:143], v[128:131], v[156:159], v[140:143]\n"
"v_mfma_f32_16x16x32_bf16 v[160:163], v[132:135], v[156:159], v[160:163]\n"
"s_barrier\n"
"ds_read_b128 v[136:139], v13 offset:18432\n"
"s_waitcnt lgkmcnt(2)\n"
"v_mfma_f32_16x16x32_bf16 v[172:175], v[144:147], v[156:159], v[172:175]\n"
"s_waitcnt lgkmcnt(1)\n"
"v_mfma_f32_16x16x32_bf16 v[144:147], v[164:167], v[28:31], 0\n"
"v_mfma_f32_16x16x32_bf16 v[148:151], v[164:167], v[32:35], 0\n"
"ds_read_b128 v[156:159], v12 offset:20480\n"
"v_mfma_f32_16x16x32_bf16 v[168:171], v[164:167], v[24:27], 0\n"
"v_cvt_pk_bf16_f32 v140, v140, v141\n"
"v_cvt_pk_bf16_f32 v141, v142, v143\n"
"v_cvt_pk_bf16_f32 v142, v160, v161\n"
"v_cvt_pk_bf16_f32 v143, v162, v163\n"
"s_waitcnt lgkmcnt(1)\n"
"v_mfma_f32_16x16x32_bf16 v[144:147], v[136:139], v[40:43], v[144:147]\n"
"s_mov_b32 m0, s54\n"
"v_cvt_pk_bf16_f32 v172, v172, v173\n"
"v_cvt_pk_bf16_f32 v173, v174, v175\n"
"v_mfma_f32_16x16x32_bf16 v[148:151], v[136:139], v[44:47], v[148:151]\n"
"ds_read_b128 v[160:163], v13 offset:20480\n"
"v_mfma_f32_16x16x32_bf16 v[168:171], v[136:139], v[36:39], v[168:171]\n"
"ds_write_addtid_b32 v254 offset:33280\n"
"s_waitcnt lgkmcnt(2)\n"
"v_mfma_f32_16x16x32_bf16 v[144:147], v[156:159], v[48:51], v[144:147]\n"
"ds_write_addtid_b32 v255 offset:33536\n"
"v_mfma_f32_16x16x32_bf16 v[148:151], v[156:159], v[52:55], v[148:151]\n"
"ds_write_addtid_b32 v152 offset:32256\n"
"v_mfma_f32_16x16x32_bf16 v[168:171], v[156:159], v[56:59], v[168:171]\n"
"ds_read_b128 v[136:139], v12 offset:22528\n"
"ds_write_addtid_b32 v153 offset:32512\n"
"s_waitcnt lgkmcnt(5)\n"
"v_mfma_f32_16x16x32_bf16 v[144:147], v[160:163], v[60:63], v[144:147]\n"
"ds_write_addtid_b32 v154 offset:32768\n"
"v_mfma_f32_16x16x32_bf16 v[148:151], v[160:163], v[64:67], v[148:151]\n"
"ds_write_addtid_b32 v155 offset:33024\n"
"s_mov_b32 m0, s68\n"
"ds_read_b128 v[152:155], v13 offset:22528\n"
"v_mfma_f32_16x16x32_bf16 v[168:171], v[160:163], v[68:71], v[168:171]\n"
"ds_write_addtid_b32 v140 offset:45056\n"
"ds_write_addtid_b32 v141 offset:45312\n"
"ds_write_addtid_b32 v142 offset:45568\n"
"s_waitcnt lgkmcnt(7)\n"
"v_mfma_f32_16x16x32_bf16 v[144:147], v[136:139], v[72:75], v[144:147]\n"
"ds_write_addtid_b32 v143 offset:45824\n"
"v_mfma_f32_16x16x32_bf16 v[148:151], v[136:139], v[76:79], v[148:151]\n"
"ds_read_b128 v[140:143], v12 offset:36864\n"
"v_mfma_f32_16x16x32_bf16 v[168:171], v[136:139], v[80:83], v[168:171]\n"
"ds_write_addtid_b32 v172 offset:46080\n"
"s_waitcnt lgkmcnt(6)\n"
"v_mfma_f32_16x16x32_bf16 v[144:147], v[152:155], v[84:87], v[144:147]\n"
"ds_write_addtid_b32 v173 offset:46336\n"
"v_mfma_f32_16x16x32_bf16 v[148:151], v[152:155], v[88:91], v[148:151]\n"
"ds_read_b128 v[136:139], v13 offset:36864\n"
"v_mfma_f32_16x16x32_bf16 v[168:171], v[152:155], v[92:95], v[168:171]\n"
"s_waitcnt lgkmcnt(3)\n"
"v_mfma_f32_16x16x32_bf16 v[152:155], v[140:143], v[28:31], 0\n"
"v_mfma_f32_16x16x32_bf16 v[156:159], v[140:143], v[32:35], 0\n"
"ds_read_b128 v[160:163], v12 offset:38912\n"
"v_mfma_f32_16x16x32_bf16 v[164:167], v[140:143], v[24:27], 0\n"
"s_waitcnt lgkmcnt(1)\n"
"v_cvt_pk_bf16_f32 v202, v144, v145\n"
"v_cvt_pk_bf16_f32 v203, v146, v147\n"
"v_mfma_f32_16x16x32_bf16 v[152:155], v[136:139], v[40:43], v[152:155]\n"
"v_cvt_pk_bf16_f32 v228, v148, v149\n"
"v_cvt_pk_bf16_f32 v229, v150, v151\n"
"v_mfma_f32_16x16x32_bf16 v[156:159], v[136:139], v[44:47], v[156:159]\n"
"ds_read_b128 v[140:143], v13 offset:38912\n"
"v_cvt_pk_bf16_f32 v254, v168, v169\n"
"v_cvt_pk_bf16_f32 v255, v170, v171\n"
"v_mfma_f32_16x16x32_bf16 v[164:167], v[136:139], v[36:39], v[164:167]\n"
"s_waitcnt lgkmcnt(1)\n"
"v_mfma_f32_16x16x32_bf16 v[152:155], v[160:163], v[48:51], v[152:155]\n"
"v_mfma_f32_16x16x32_bf16 v[156:159], v[160:163], v[52:55], v[156:159]\n"
"ds_read_b128 v[136:139], v12 offset:40960\n"
"v_mfma_f32_16x16x32_bf16 v[164:167], v[160:163], v[56:59], v[164:167]\n"
"s_waitcnt lgkmcnt(1)\n"
"v_mfma_f32_16x16x32_bf16 v[152:155], v[140:143], v[60:63], v[152:155]\n"
"v_mfma_f32_16x16x32_bf16 v[156:159], v[140:143], v[64:67], v[156:159]\n"
"ds_read_b128 v[144:147], v13 offset:40960\n"
"v_mfma_f32_16x16x32_bf16 v[164:167], v[140:143], v[68:71], v[164:167]\n"
"s_waitcnt lgkmcnt(1)\n"
"v_mfma_f32_16x16x32_bf16 v[152:155], v[136:139], v[72:75], v[152:155]\n"
"v_mfma_f32_16x16x32_bf16 v[156:159], v[136:139], v[76:79], v[156:159]\n"
"v_mov_b32 v140, 0x3f803f80\n"
"v_mov_b32 v141, 0x3f803f80\n"
"v_mfma_f32_16x16x32_bf16 v[164:167], v[136:139], v[80:83], v[164:167]\n"
"s_waitcnt lgkmcnt(0)\n"
"v_mov_b32 v142, 0x3f803f80\n"
"v_mov_b32 v143, 0x3f803f80\n"
"v_mfma_f32_16x16x32_bf16 v[152:155], v[144:147], v[84:87], v[152:155]\n"
"s_waitcnt vmcnt(0)\n"
"s_barrier\n"
"ds_read2st64_b32 v[136:137], v9 offset0:120 offset1:121\n"
"ds_read2st64_b32 v[138:139], v9 offset0:122 offset1:123\n"
"v_mfma_f32_16x16x32_bf16 v[156:159], v[144:147], v[88:91], v[156:159]\n"
"s_and_b32 s41, 416, s55\n"
"v_add_u32 v8, s41, v15\n"
"v_mfma_f32_16x16x32_bf16 v[164:167], v[144:147], v[92:95], v[164:167]\n"
"ds_read_b32 v145, v9 offset:32000\n"
"v_cvt_pk_bf16_f32 v186, v152, v153\n"
"v_cvt_pk_bf16_f32 v187, v154, v155\n"
"ds_read2st64_b32 v[146:147], v9 offset0:126 offset1:127\n"
"ds_read_u16 v8, v8 offset:576\n"
"v_cvt_pk_bf16_f32 v212, v156, v157\n"
"v_cvt_pk_bf16_f32 v213, v158, v159\n"
"s_waitcnt lgkmcnt(3)\n"
"v_mfma_f32_16x16x32_bf16 v[148:151], v[140:143], v[136:139], 0\n"
"v_mov_b32 v144, 0\n"
"v_cvt_pk_bf16_f32 v238, v164, v165\n"
"v_mfma_f32_16x16x32_bf16 v[152:155], v[190:193], v[136:139], 0\n"
"v_cvt_pk_bf16_f32 v239, v166, v167\n"
"v_mfma_f32_16x16x32_bf16 v[190:193], v[216:219], v[136:139], 0\n"
"ds_read_u16 v11, v21 offset:832\n"
"v_mfma_f32_16x16x32_bf16 v[216:219], v[242:245], v[136:139], 0\n"
"s_waitcnt lgkmcnt(1)\n"
"ds_read2st64_b32 v[242:243], v20 offset0:0 offset1:1\n"
"v_mad_u32_u24 v8, v8, s40, v16\n"
"v_mfma_f32_16x16x32_bf16 v[148:151], v[140:143], v[144:147], v[148:151]\n"
"s_add_u32 m0, s51, 114688\n"
"s_nop 0\n"
"global_load_lds_dwordx4 v8, s[56:57] nt\n"
"ds_read2st64_b32 v[244:245], v20 offset0:2 offset1:3\n"
"v_mfma_f32_16x16x32_bf16 v[152:155], v[182:185], v[144:147], v[152:155]\n"
"ds_read2st64_b32 v[136:137], v9 offset0:128 offset1:129\n"
"v_mov_b32 v138, 0\n"
"v_mfma_f32_16x16x32_bf16 v[190:193], v[208:211], v[144:147], v[190:193]\n"
"ds_read_b32 v139, v9 offset:33536\n"
"v_mfma_f32_16x16x32_bf16 v[216:219], v[234:237], v[144:147], v[216:219]\n"
"s_waitcnt lgkmcnt(4)\n"
"ds_read2st64_b32 v[174:175], v20 offset0:4 offset1:5\n"
"v_mad_u32_u24 v10, v11, s40, v18\n"
"v_mad_u32_u24 v11, v11, s40, v19\n"
"ds_read_b32 v145, v9 offset:34048\n"
"ds_read2st64_b32 v[146:147], v9 offset0:134 offset1:135\n"
"s_waitcnt lgkmcnt(5)\n"
"global_store_dwordx4 v10, v[242:245], s[66:67] sc1\n"
"s_waitcnt lgkmcnt(3)\n"
"v_mfma_f32_16x16x32_bf16 v[148:151], v[140:143], v[136:139], v[148:151]\n"
"s_add_u32 m0, s51, 116736\n"
"s_nop 0\n"
"global_load_lds_dwordx4 v8, s[58:59] nt\n"
"ds_read2st64_b32 v[244:245], v9 offset0:38 offset1:39\n"
"v_mfma_f32_16x16x32_bf16 v[152:155], v[186:189], v[136:139], v[152:155]\n"
"s_waitcnt lgkmcnt(3)\n"
"global_store_dwordx2 v11, v[174:175], s[66:67] offset:256 sc1\n"
"v_mfma_f32_16x16x32_bf16 v[190:193], v[212:215], v[136:139], v[190:193]\n"
"v_mov_b32 v144, 0\n"
"v_mov_b32 v242, 0\n"
"v_mfma_f32_16x16x32_bf16 v[216:219], v[238:241], v[136:139], v[216:219]\n"
"s_waitcnt lgkmcnt(1)\n"
"v_mov_b32 v243, 0\n"
"v_mfma_f32_16x16x32_bf16 v[148:151], v[140:143], v[144:147], v[148:151]\n"
"s_waitcnt lgkmcnt(0)\n"
"v_mfma_f32_16x16x32_bf16 v[148:151], v[140:143], v[242:245], v[148:151]\n"
"s_add_u32 m0, s51, 118784\n"
"s_nop 0\n"
"global_load_lds_dwordx4 v8, s[60:61] nt\n"
"v_mfma_f32_16x16x32_bf16 v[216:219], v[250:253], v[144:147], v[216:219]\n"
"v_mfma_f32_16x16x32_bf16 v[216:219], v[252:255], v[242:245], v[216:219]\n"
"ds_read2st64_b32 v[136:137], v14 offset0:126 offset1:127\n"
"v_mfma_f32_16x16x32_bf16 v[152:155], v[198:201], v[144:147], v[152:155]\n"
"ds_read2st64_b32 v[138:139], v14 offset0:128 offset1:129\n"
"v_mfma_f32_16x16x32_bf16 v[152:155], v[200:203], v[242:245], v[152:155]\n"
"ds_read_b128 v[140:143], v17 offset:53248\n"
"v_rcp_f32 v8, v148\n"
"v_mfma_f32_16x16x32_bf16 v[190:193], v[224:227], v[144:147], v[190:193]\n"
"ds_read2st64_b32 v[240:241], v14 offset0:150 offset1:151\n"
"v_mul_f32 v216, v216, v8\n"
"v_mfma_f32_16x16x32_bf16 v[190:193], v[226:229], v[242:245], v[190:193]\n"
"ds_read2st64_b32 v[242:243], v14 offset0:152 offset1:153\n"
"v_mul_f32 v217, v217, v8\n"
"v_cvt_pk_bf16_f32 v244, v216, v217\n"
"v_mul_f32 v218, v218, v8\n"
"v_mul_f32 v219, v219, v8\n"
"v_cvt_pk_bf16_f32 v245, v218, v219\n"
"v_mul_f32 v152, v152, v8\n"
"s_waitcnt lgkmcnt(3)\n"
"v_mfma_f32_16x16x32_bf16 v[214:217], v[96:99], v[136:139], 0\n"
"ds_read_b128 v[144:147], v17 offset:54272\n"
"v_mul_f32 v153, v153, v8\n"
"v_mfma_f32_16x16x32_bf16 v[148:151], v[100:103], v[136:139], 0\n"
"s_waitcnt lgkmcnt(3)\n"
"ds_read2st64_b32 v[156:157], v14 offset0:174 offset1:175\n"
"v_cvt_pk_bf16_f32 v152, v152, v153\n"
"v_mfma_f32_16x16x32_bf16 v[160:163], v[140:143], v[136:139], 0\n"
"s_waitcnt lgkmcnt(2)\n"
"ds_read2st64_b32 v[158:159], v14 offset0:176 offset1:177\n"
"v_mul_f32 v154, v154, v8\n"
"v_mul_f32 v155, v155, v8\n"
"v_mfma_f32_16x16x32_bf16 v[214:217], v[104:107], v[240:243], v[214:217]\n"
"ds_read_b128 v[136:139], v17 offset:55296\n"
"v_cvt_pk_bf16_f32 v153, v154, v155\n"
"v_mfma_f32_16x16x32_bf16 v[148:151], v[108:111], v[240:243], v[148:151]\n"
"s_waitcnt lgkmcnt(3)\n"
"ds_read2st64_b32 v[140:141], v14 offset0:198 offset1:199\n"
"v_mul_f32 v190, v190, v8\n"
"v_mul_f32 v191, v191, v8\n"
"v_mfma_f32_16x16x32_bf16 v[160:163], v[144:147], v[240:243], v[160:163]\n"
"ds_read2st64_b32 v[142:143], v14 offset0:200 offset1:201\n"
"v_cvt_pk_bf16_f32 v154, v190, v191\n"
"v_mul_f32 v192, v192, v8\n"
"v_mul_f32 v193, v193, v8\n"
"v_cvt_pk_bf16_f32 v155, v192, v193\n"
"s_waitcnt lgkmcnt(3)\n"
"v_mfma_f32_16x16x32_bf16 v[214:217], v[0:3], v[156:159], v[214:217]\n"
"ds_read_b128 v[188:191], v17 offset:56320\n"
"v_mfma_f32_16x16x32_bf16 v[148:151], v[112:115], v[156:159], v[148:151]\n"
"s_waitcnt lgkmcnt(3)\n"
"ds_read2st64_b32 v[240:241], v14 offset0:130 offset1:131\n"
"v_mfma_f32_16x16x32_bf16 v[160:163], v[136:139], v[156:159], v[160:163]\n"
"s_waitcnt lgkmcnt(2)\n"
"ds_read2st64_b32 v[242:243], v14 offset0:154 offset1:155\n"
"v_mfma_f32_16x16x32_bf16 v[214:217], v[116:119], v[140:143], v[214:217]\n"
"ds_read_b128 v[136:139], v17 offset:57344\n"
"v_mfma_f32_16x16x32_bf16 v[148:151], v[120:123], v[140:143], v[148:151]\n"
"s_waitcnt lgkmcnt(3)\n"
"ds_read2st64_b32 v[144:145], v14 offset0:178 offset1:179\n"
"v_mfma_f32_16x16x32_bf16 v[160:163], v[188:191], v[140:143], v[160:163]\n"
"s_waitcnt lgkmcnt(2)\n"
"ds_read2st64_b32 v[146:147], v14 offset0:202 offset1:203\n"
"v_mfma_f32_16x16x32_bf16 v[214:217], v[4:7], v[240:243], v[214:217]\n"
"ds_read_b128 v[188:191], v17 offset:58368\n"
"v_mfma_f32_16x16x32_bf16 v[148:151], v[124:127], v[240:243], v[148:151]\n"
"s_waitcnt lgkmcnt(3)\n"
"ds_read_b128 v[140:143], v12 offset:24576\n"
"v_mfma_f32_16x16x32_bf16 v[160:163], v[136:139], v[240:243], v[160:163]\n"
"s_waitcnt lgkmcnt(2)\n"
"v_mfma_f32_16x16x32_bf16 v[214:217], v[128:131], v[144:147], v[214:217]\n"
"v_mfma_f32_16x16x32_bf16 v[148:151], v[132:135], v[144:147], v[148:151]\n"
"s_barrier\n"
"ds_read_b128 v[240:243], v13 offset:24576\n"
"s_mov_b32 m0, s54\n"
"s_waitcnt lgkmcnt(2)\n"
"v_mfma_f32_16x16x32_bf16 v[160:163], v[188:191], v[144:147], v[160:163]\n"
"ds_write_addtid_b32 v244 offset:33280\n"
"s_waitcnt lgkmcnt(2)\n"
"v_mfma_f32_16x16x32_bf16 v[190:193], v[140:143], v[28:31], 0\n"
"ds_write_addtid_b32 v245 offset:33536\n"
"v_cvt_pk_bf16_f32 v214, v214, v215\n"
"v_cvt_pk_bf16_f32 v215, v216, v217\n"
"v_cvt_pk_bf16_f32 v216, v148, v149\n"
"v_cvt_pk_bf16_f32 v217, v150, v151\n"
"v_mfma_f32_16x16x32_bf16 v[136:139], v[140:143], v[32:35], 0\n"
"ds_write_addtid_b32 v152 offset:32256\n"
"v_cvt_pk_bf16_f32 v160, v160, v161\n"
"v_cvt_pk_bf16_f32 v161, v162, v163\n"
"v_mfma_f32_16x16x32_bf16 v[144:147], v[140:143], v[24:27], 0\n"
"ds_write_addtid_b32 v153 offset:32512\n"
"ds_write_addtid_b32 v154 offset:32768\n"
"s_waitcnt lgkmcnt(5)\n"
"v_mfma_f32_16x16x32_bf16 v[190:193], v[240:243], v[40:43], v[190:193]\n"
"ds_write_addtid_b32 v155 offset:33024\n"
"s_mov_b32 m0, s68\n"
"ds_read_b128 v[140:143], v12 offset:26624\n"
"v_mfma_f32_16x16x32_bf16 v[136:139], v[240:243], v[44:47], v[136:139]\n"
"ds_write_addtid_b32 v214 offset:45056\n"
"ds_write_addtid_b32 v215 offset:45312\n"
"v_mfma_f32_16x16x32_bf16 v[144:147], v[240:243], v[36:39], v[144:147]\n"
"ds_write_addtid_b32 v216 offset:45568\n"
"ds_write_addtid_b32 v217 offset:45824\n"
"ds_write_addtid_b32 v160 offset:46080\n"
"ds_write_addtid_b32 v161 offset:46336\n"
"s_and_b32 s41, 416, s55\n"
"v_add_u32 v8, s41, v15\n"
"ds_read_u16 v8, v8 offset:608\n"
"s_waitcnt lgkmcnt(7)\n"
"v_mfma_f32_16x16x32_bf16 v[190:193], v[140:143], v[48:51], v[190:193]\n"
"ds_read_b128 v[214:217], v13 offset:26624\n"
"v_mfma_f32_16x16x32_bf16 v[136:139], v[140:143], v[52:55], v[136:139]\n"
"ds_read_b128 v[240:243], v12 offset:28672\n"
"v_mfma_f32_16x16x32_bf16 v[144:147], v[140:143], v[56:59], v[144:147]\n"
"s_waitcnt lgkmcnt(2)\n"
"v_mad_u32_u24 v8, v8, s40, v16\n"
"s_add_u32 m0, s51, 139264\n"
"s_nop 0\n"
"global_load_lds_dwordx4 v8, s[56:57] nt\n"
"s_waitcnt lgkmcnt(1)\n"
"v_mfma_f32_16x16x32_bf16 v[190:193], v[214:217], v[60:63], v[190:193]\n"
"v_mfma_f32_16x16x32_bf16 v[136:139], v[214:217], v[64:67], v[136:139]\n"
"ds_read_b128 v[140:143], v13 offset:28672\n"
"v_mfma_f32_16x16x32_bf16 v[144:147], v[214:217], v[68:71], v[144:147]\n"
"s_waitcnt lgkmcnt(1)\n"
"v_mfma_f32_16x16x32_bf16 v[190:193], v[240:243], v[72:75], v[190:193]\n"
"v_mfma_f32_16x16x32_bf16 v[136:139], v[240:243], v[76:79], v[136:139]\n"
"ds_read_b128 v[214:217], v12 offset:30720\n"
"v_mfma_f32_16x16x32_bf16 v[144:147], v[240:243], v[80:83], v[144:147]\n"
"s_add_u32 m0, s51, 141312\n"
"s_nop 0\n"
"global_load_lds_dwordx4 v8, s[58:59] nt\n"
"s_waitcnt lgkmcnt(1)\n"
"v_mfma_f32_16x16x32_bf16 v[190:193], v[140:143], v[84:87], v[190:193]\n"
"v_mfma_f32_16x16x32_bf16 v[136:139], v[140:143], v[88:91], v[136:139]\n"
"ds_read_b128 v[242:245], v13 offset:30720\n"
"v_mfma_f32_16x16x32_bf16 v[144:147], v[140:143], v[92:95], v[144:147]\n"
"s_waitcnt lgkmcnt(1)\n"
"v_mfma_f32_16x16x32_bf16 v[140:143], v[214:217], v[28:31], 0\n"
"v_mfma_f32_16x16x32_bf16 v[148:151], v[214:217], v[32:35], 0\n"
"ds_read_b128 v[152:155], v12 offset:32768\n"
"s_add_u32 m0, s51, 143360\n"
"s_nop 0\n"
"global_load_lds_dwordx4 v8, s[60:61] nt\n"
"v_mfma_f32_16x16x32_bf16 v[156:159], v[214:217], v[24:27], 0\n"
"s_waitcnt lgkmcnt(1)\n"
"v_cvt_pk_bf16_f32 v188, v190, v191\n"
"v_cvt_pk_bf16_f32 v189, v192, v193\n"
"v_mfma_f32_16x16x32_bf16 v[140:143], v[242:245], v[40:43], v[140:143]\n"
"v_cvt_pk_bf16_f32 v214, v136, v137\n"
"v_cvt_pk_bf16_f32 v215, v138, v139\n"
"v_mfma_f32_16x16x32_bf16 v[148:151], v[242:245], v[44:47], v[148:151]\n"
"ds_read_b128 v[190:193], v13 offset:32768\n"
"v_cvt_pk_bf16_f32 v240, v144, v145\n"
"v_cvt_pk_bf16_f32 v241, v146, v147\n"
"v_mfma_f32_16x16x32_bf16 v[156:159], v[242:245], v[36:39], v[156:159]\n"
"s_waitcnt lgkmcnt(1)\n"
"v_mfma_f32_16x16x32_bf16 v[140:143], v[152:155], v[48:51], v[140:143]\n"
"v_mfma_f32_16x16x32_bf16 v[148:151], v[152:155], v[52:55], v[148:151]\n"
"ds_read_b128 v[216:219], v12 offset:34816\n"
"v_mfma_f32_16x16x32_bf16 v[156:159], v[152:155], v[56:59], v[156:159]\n"
"s_waitcnt lgkmcnt(1)\n"
"v_mfma_f32_16x16x32_bf16 v[140:143], v[190:193], v[60:63], v[140:143]\n"
"v_mfma_f32_16x16x32_bf16 v[148:151], v[190:193], v[64:67], v[148:151]\n"
"ds_read_b128 v[242:245], v13 offset:34816\n"
"v_mfma_f32_16x16x32_bf16 v[156:159], v[190:193], v[68:71], v[156:159]\n"
"s_waitcnt lgkmcnt(1)\n"
"v_mfma_f32_16x16x32_bf16 v[140:143], v[216:219], v[72:75], v[140:143]\n"
"v_mfma_f32_16x16x32_bf16 v[148:151], v[216:219], v[76:79], v[148:151]\n"
"v_mov_b32 v136, 0x3f803f80\n"
"v_mov_b32 v137, 0x3f803f80\n"
"v_mfma_f32_16x16x32_bf16 v[156:159], v[216:219], v[80:83], v[156:159]\n"
"s_waitcnt lgkmcnt(0)\n"
"v_mov_b32 v138, 0x3f803f80\n"
"v_mov_b32 v139, 0x3f803f80\n"
"v_mfma_f32_16x16x32_bf16 v[140:143], v[242:245], v[84:87], v[140:143]\n"
"s_barrier\n"
"ds_read2st64_b32 v[144:145], v9 offset0:112 offset1:113\n"
"ds_read2st64_b32 v[146:147], v9 offset0:114 offset1:115\n"
"v_mfma_f32_16x16x32_bf16 v[148:151], v[242:245], v[88:91], v[148:151]\n"
"s_andn2_b32 s41, 416, s55\n"
"v_add_u32 v8, s41, v15\n"
"v_mfma_f32_16x16x32_bf16 v[156:159], v[242:245], v[92:95], v[156:159]\n"
"ds_read2st64_b32 v[152:153], v9 offset0:116 offset1:117\n"
"v_cvt_pk_bf16_f32 v190, v140, v141\n"
"v_cvt_pk_bf16_f32 v191, v142, v143\n"
"ds_read2st64_b32 v[154:155], v9 offset0:118 offset1:119\n"
"ds_read_u16 v8, v8 offset:640\n"
"v_cvt_pk_bf16_f32 v216, v148, v149\n"
"v_cvt_pk_bf16_f32 v217, v150, v151\n"
"s_waitcnt lgkmcnt(3)\n"
"v_mfma_f32_16x16x32_bf16 v[140:143], v[136:139], v[144:147], 0\n"
"ds_read2st64_b32 v[162:163], v9 offset0:128 offset1:129\n"
"v_cvt_pk_bf16_f32 v242, v156, v157\n"
"v_mfma_f32_16x16x32_bf16 v[148:151], v[178:181], v[144:147], 0\n"
"ds_read2st64_b32 v[164:165], v9 offset0:130 offset1:131\n"
"v_cvt_pk_bf16_f32 v243, v158, v159\n"
"v_mfma_f32_16x16x32_bf16 v[156:159], v[204:207], v[144:147], 0\n"
"ds_read_u16 v245, v21 offset:864\n"
"v_mfma_f32_16x16x32_bf16 v[168:171], v[230:233], v[144:147], 0\n"
"s_waitcnt lgkmcnt(3)\n"
"ds_read2st64_b32 v[144:145], v20 offset0:0 offset1:1\n"
"v_mad_u32_u24 v8, v8, s40, v16\n"
"v_mfma_f32_16x16x32_bf16 v[140:143], v[136:139], v[152:155], v[140:143]\n"
"s_add_u32 m0, s51, 126976\n"
"s_nop 0\n"
"global_load_lds_dwordx4 v8, s[56:57] nt\n"
"ds_read2st64_b32 v[146:147], v20 offset0:2 offset1:3\n"
"v_mov_b32 v160, 0\n"
"v_mov_b32 v161, 0\n"
"v_mov_b32 v166, 0\n"
"v_mov_b32 v167, 0\n"
"s_waitcnt lgkmcnt(3)\n"
"v_mfma_f32_16x16x32_bf16 v[140:143], v[136:139], v[162:165], v[140:143]\n"
"v_mfma_f32_16x16x32_bf16 v[168:171], v[246:249], v[152:155], v[168:171]\n"
"s_waitcnt lgkmcnt(2)\n"
"ds_read2st64_b32 v[218:219], v20 offset0:4 offset1:5\n"
"v_mad_u32_u24 v244, v245, s40, v18\n"
"v_mfma_f32_16x16x32_bf16 v[168:171], v[232:235], v[160:163], v[168:171]\n"
"v_mad_u32_u24 v245, v245, s40, v19\n"
"v_mfma_f32_16x16x32_bf16 v[168:171], v[250:253], v[164:167], v[168:171]\n"
"s_waitcnt lgkmcnt(1)\n"
"global_store_dwordx4 v244, v[144:147], s[66:67] sc1\n"
"v_mfma_f32_16x16x32_bf16 v[148:151], v[194:197], v[152:155], v[148:151]\n"
"s_add_u32 m0, s51, 129024\n"
"s_nop 0\n"
"global_load_lds_dwordx4 v8, s[58:59] nt\n"
"v_mfma_f32_16x16x32_bf16 v[148:151], v[180:183], v[160:163], v[148:151]\n"
"ds_read2st64_b32 v[136:137], v14 offset0:126 offset1:127\n"
"v_mfma_f32_16x16x32_bf16 v[148:151], v[198:201], v[164:167], v[148:151]\n"
"ds_read2st64_b32 v[138:139], v14 offset0:128 offset1:129\n"
"v_rcp_f32 v244, v140\n"
"v_mfma_f32_16x16x32_bf16 v[156:159], v[220:223], v[152:155], v[156:159]\n"
"s_waitcnt lgkmcnt(2)\n"
"global_store_dwordx2 v245, v[218:219], s[66:67] offset:256 sc1\n"
"ds_read_b128 v[140:143], v17 offset:53248\n"
"v_mfma_f32_16x16x32_bf16 v[156:159], v[206:209], v[160:163], v[156:159]\n"
"ds_read2st64_b32 v[144:145], v14 offset0:150 offset1:151\n"
"v_mul_f32 v168, v168, v244\n"
"v_mfma_f32_16x16x32_bf16 v[156:159], v[224:227], v[164:167], v[156:159]\n"
"ds_read2st64_b32 v[146:147], v14 offset0:152 offset1:153\n"
"v_mul_f32 v169, v169, v244\n"
"v_cvt_pk_bf16_f32 v218, v168, v169\n"
"v_mul_f32 v170, v170, v244\n"
"v_mul_f32 v171, v171, v244\n"
"s_waitcnt lgkmcnt(3)\n"
"v_mfma_f32_16x16x32_bf16 v[152:155], v[96:99], v[136:139], 0\n"
"s_add_u32 m0, s51, 131072\n"
"s_nop 0\n"
"global_load_lds_dwordx4 v8, s[60:61] nt\n"
"ds_read_b128 v[160:163], v17 offset:54272\n"
"v_mfma_f32_16x16x32_bf16 v[164:167], v[100:103], v[136:139], 0\n"
"s_waitcnt lgkmcnt(3)\n"
"ds_read2st64_b32 v[168:169], v14 offset0:174 offset1:175\n"
"v_cvt_pk_bf16_f32 v219, v170, v171\n"
"v_mfma_f32_16x16x32_bf16 v[172:175], v[140:143], v[136:139], 0\n"
"ds_read2st64_b32 v[170:171], v14 offset0:176 offset1:177\n"
"v_mul_f32 v148, v148, v244\n"
"v_mul_f32 v149, v149, v244\n"
"v_cvt_pk_bf16_f32 v148, v148, v149\n"
"s_waitcnt lgkmcnt(3)\n"
"v_mfma_f32_16x16x32_bf16 v[152:155], v[104:107], v[144:147], v[152:155]\n"
"ds_read_b128 v[136:139], v17 offset:55296\n"
"v_mul_f32 v150, v150, v244\n"
"v_mfma_f32_16x16x32_bf16 v[164:167], v[108:111], v[144:147], v[164:167]\n"
"s_waitcnt lgkmcnt(3)\n"
"ds_read2st64_b32 v[140:141], v14 offset0:198 offset1:199\n"
"v_mul_f32 v151, v151, v244\n"
"v_cvt_pk_bf16_f32 v149, v150, v151\n"
"v_mfma_f32_16x16x32_bf16 v[172:175], v[160:163], v[144:147], v[172:175]\n"
"ds_read2st64_b32 v[142:143], v14 offset0:200 offset1:201\n"
"v_mul_f32 v156, v156, v244\n"
"v_mul_f32 v157, v157, v244\n"
"v_cvt_pk_bf16_f32 v150, v156, v157\n"
"v_mul_f32 v158, v158, v244\n"
"s_waitcnt lgkmcnt(3)\n"
"v_mfma_f32_16x16x32_bf16 v[152:155], v[0:3], v[168:171], v[152:155]\n"
"ds_read_b128 v[144:147], v17 offset:56320\n"
"v_mul_f32 v159, v159, v244\n"
"v_mfma_f32_16x16x32_bf16 v[164:167], v[112:115], v[168:171], v[164:167]\n"
"s_waitcnt lgkmcnt(3)\n"
"ds_read2st64_b32 v[156:157], v14 offset0:130 offset1:131\n"
"v_cvt_pk_bf16_f32 v151, v158, v159\n"
"v_mfma_f32_16x16x32_bf16 v[172:175], v[136:139], v[168:171], v[172:175]\n"
"s_waitcnt lgkmcnt(2)\n"
"ds_read2st64_b32 v[158:159], v14 offset0:154 offset1:155\n"
"v_mfma_f32_16x16x32_bf16 v[152:155], v[116:119], v[140:143], v[152:155]\n"
"ds_read_b128 v[136:139], v17 offset:57344\n"
"v_mfma_f32_16x16x32_bf16 v[164:167], v[120:123], v[140:143], v[164:167]\n"
"s_waitcnt lgkmcnt(3)\n"
"ds_read2st64_b32 v[160:161], v14 offset0:178 offset1:179\n"
"v_mfma_f32_16x16x32_bf16 v[172:175], v[144:147], v[140:143], v[172:175]\n"
"s_waitcnt lgkmcnt(2)\n"
"ds_read2st64_b32 v[162:163], v14 offset0:202 offset1:203\n"
"v_mfma_f32_16x16x32_bf16 v[152:155], v[4:7], v[156:159], v[152:155]\n"
"ds_read_b128 v[140:143], v17 offset:58368\n"
"v_mfma_f32_16x16x32_bf16 v[164:167], v[124:127], v[156:159], v[164:167]\n"
"s_waitcnt lgkmcnt(3)\n"
"ds_read_b128 v[144:147], v12 offset:49152\n"
"v_mfma_f32_16x16x32_bf16 v[172:175], v[136:139], v[156:159], v[172:175]\n"
"s_waitcnt lgkmcnt(2)\n"
"v_mfma_f32_16x16x32_bf16 v[152:155], v[128:131], v[160:163], v[152:155]\n"
"v_mfma_f32_16x16x32_bf16 v[164:167], v[132:135], v[160:163], v[164:167]\n"
"s_waitcnt vmcnt(8)\n"
"s_barrier\n"
"ds_read_b128 v[136:139], v13 offset:49152\n"
"s_waitcnt lgkmcnt(2)\n"
"v_mfma_f32_16x16x32_bf16 v[172:175], v[140:143], v[160:163], v[172:175]\n"
"s_waitcnt lgkmcnt(1)\n"
"v_mfma_f32_16x16x32_bf16 v[140:143], v[144:147], v[28:31], 0\n"
"v_mfma_f32_16x16x32_bf16 v[156:159], v[144:147], v[32:35], 0\n"
"ds_read_b128 v[160:163], v12 offset:51200\n"
"v_mfma_f32_16x16x32_bf16 v[168:171], v[144:147], v[24:27], 0\n"
"v_cvt_pk_bf16_f32 v152, v152, v153\n"
"v_cvt_pk_bf16_f32 v153, v154, v155\n"
"v_cvt_pk_bf16_f32 v154, v164, v165\n"
"v_cvt_pk_bf16_f32 v155, v166, v167\n"
"s_waitcnt lgkmcnt(1)\n"
"v_mfma_f32_16x16x32_bf16 v[140:143], v[136:139], v[40:43], v[140:143]\n"
"s_mov_b32 m0, s54\n"
"v_cvt_pk_bf16_f32 v172, v172, v173\n"
"v_cvt_pk_bf16_f32 v173, v174, v175\n"
"v_mfma_f32_16x16x32_bf16 v[156:159], v[136:139], v[44:47], v[156:159]\n"
"ds_read_b128 v[144:147], v13 offset:51200\n"
"v_mfma_f32_16x16x32_bf16 v[168:171], v[136:139], v[36:39], v[168:171]\n"
"ds_write_addtid_b32 v218 offset:33280\n"
"s_waitcnt lgkmcnt(2)\n"
"v_mfma_f32_16x16x32_bf16 v[140:143], v[160:163], v[48:51], v[140:143]\n"
"ds_write_addtid_b32 v219 offset:33536\n"
"v_mfma_f32_16x16x32_bf16 v[156:159], v[160:163], v[52:55], v[156:159]\n"
"ds_write_addtid_b32 v148 offset:32256\n"
"v_mfma_f32_16x16x32_bf16 v[168:171], v[160:163], v[56:59], v[168:171]\n"
"ds_read_b128 v[136:139], v12 offset:53248\n"
"ds_write_addtid_b32 v149 offset:32512\n"
"s_waitcnt lgkmcnt(5)\n"
"v_mfma_f32_16x16x32_bf16 v[140:143], v[144:147], v[60:63], v[140:143]\n"
"ds_write_addtid_b32 v150 offset:32768\n"
"v_mfma_f32_16x16x32_bf16 v[156:159], v[144:147], v[64:67], v[156:159]\n"
"ds_write_addtid_b32 v151 offset:33024\n"
"s_mov_b32 m0, s68\n"
"ds_read_b128 v[148:151], v13 offset:53248\n"
"v_mfma_f32_16x16x32_bf16 v[168:171], v[144:147], v[68:71], v[168:171]\n"
"ds_write_addtid_b32 v152 offset:45056\n"
"ds_write_addtid_b32 v153 offset:45312\n"
"ds_write_addtid_b32 v154 offset:45568\n"
"s_waitcnt lgkmcnt(7)\n"
"v_mfma_f32_16x16x32_bf16 v[140:143], v[136:139], v[72:75], v[140:143]\n"
"ds_write_addtid_b32 v155 offset:45824\n"
"ds_write_addtid_b32 v172 offset:46080\n"
"v_mfma_f32_16x16x32_bf16 v[156:159], v[136:139], v[76:79], v[156:159]\n"
"ds_write_addtid_b32 v173 offset:46336\n"
"v_mov_b32 v144, 0x3f803f80\n"
"v_mfma_f32_16x16x32_bf16 v[168:171], v[136:139], v[80:83], v[168:171]\n"
"s_waitcnt lgkmcnt(6)\n"
"v_mov_b32 v145, 0x3f803f80\n"
"v_mov_b32 v146, 0x3f803f80\n"
"v_mfma_f32_16x16x32_bf16 v[140:143], v[148:151], v[84:87], v[140:143]\n"
"v_mov_b32 v147, 0x3f803f80\n"
"v_mfma_f32_16x16x32_bf16 v[156:159], v[148:151], v[88:91], v[156:159]\n"
"v_mfma_f32_16x16x32_bf16 v[168:171], v[148:151], v[92:95], v[168:171]\n"
"s_waitcnt lgkmcnt(0)\n"
"s_barrier\n"
"ds_read2st64_b32 v[136:137], v9 offset0:120 offset1:121\n"
"ds_read2st64_b32 v[138:139], v9 offset0:122 offset1:123\n"
"s_waitcnt lgkmcnt(0)\n"
"v_mfma_f32_16x16x32_bf16 v[148:151], v[144:147], v[136:139], 0\n"
"ds_read2st64_b32 v[152:153], v9 offset0:124 offset1:125\n"
"v_cvt_pk_bf16_f32 v192, v140, v141\n"
"v_mfma_f32_16x16x32_bf16 v[160:163], v[178:181], v[136:139], 0\n"
"ds_read2st64_b32 v[154:155], v9 offset0:126 offset1:127\n"
"v_cvt_pk_bf16_f32 v193, v142, v143\n"
"v_mfma_f32_16x16x32_bf16 v[140:143], v[204:207], v[136:139], 0\n"
"s_and_b32 s41, 416, s55\n"
"v_add_u32 v8, s41, v15\n"
"v_cvt_pk_bf16_f32 v218, v156, v157\n"
"v_mfma_f32_16x16x32_bf16 v[164:167], v[230:233], v[136:139], 0\n"
"ds_read_u16 v11, v21 offset:896\n"
"v_cvt_pk_bf16_f32 v219, v158, v159\n"
"v_cvt_pk_bf16_f32 v244, v168, v169\n"
"v_cvt_pk_bf16_f32 v245, v170, v171\n"
"ds_read2st64_b32 v[136:137], v20 offset0:0 offset1:1\n"
"ds_read2st64_b32 v[138:139], v20 offset0:2 offset1:3\n"
"ds_read_u16 v8, v8 offset:672\n"
"s_waitcnt lgkmcnt(4)\n"
"v_mfma_f32_16x16x32_bf16 v[148:151], v[144:147], v[152:155], v[148:151]\n"
"ds_read_b32 v156, v9 offset:32768\n"
"v_mfma_f32_16x16x32_bf16 v[160:163], v[188:191], v[152:155], v[160:163]\n"
"ds_read2st64_b32 v[158:159], v9 offset0:130 offset1:131\n"
"v_mov_b32 v157, 0\n"
"v_mfma_f32_16x16x32_bf16 v[140:143], v[214:217], v[152:155], v[140:143]\n"
"s_waitcnt lgkmcnt(5)\n"
"v_mad_u32_u24 v10, v11, s40, v18\n"
"v_mad_u32_u24 v11, v11, s40, v19\n"
"v_mfma_f32_16x16x32_bf16 v[164:167], v[240:243], v[152:155], v[164:167]\n"
"s_waitcnt lgkmcnt(3)\n"
"ds_read2st64_b32 v[174:175], v20 offset0:4 offset1:5\n"
"global_store_dwordx4 v10, v[136:139], s[66:67] sc1\n"
"s_waitcnt lgkmcnt(3)\n"
"v_mad_u32_u24 v8, v8, s40, v16\n"
"s_add_u32 m0, s51, 151552\n"
"s_nop 0\n"
"global_load_lds_dwordx4 v8, s[56:57] nt\n"
"s_waitcnt lgkmcnt(1)\n"
"v_mfma_f32_16x16x32_bf16 v[148:151], v[144:147], v[156:159], v[148:151]\n"
"ds_read2st64_b32 v[136:137], v9 offset0:132 offset1:133\n"
"v_mfma_f32_16x16x32_bf16 v[160:163], v[192:195], v[156:159], v[160:163]\n"
"ds_read_b32 v138, v9 offset:34304\n"
"v_mov_b32 v139, 0\n"
"v_mfma_f32_16x16x32_bf16 v[140:143], v[218:221], v[156:159], v[140:143]\n"
"ds_read_b32 v154, v9 offset:9728\n"
"v_mov_b32 v155, 0\n"
"v_mfma_f32_16x16x32_bf16 v[164:167], v[244:247], v[156:159], v[164:167]\n"
"s_waitcnt lgkmcnt(3)\n"
"global_store_dwordx2 v11, v[174:175], s[66:67] offset:256 sc1\n"
"v_mov_b32 v152, 0\n"
"v_mov_b32 v153, 0\n"
"s_add_u32 m0, s51, 153600\n"
"s_nop 0\n"
"global_load_lds_dwordx4 v8, s[58:59] nt\n"
"s_waitcnt lgkmcnt(1)\n"
"v_mfma_f32_16x16x32_bf16 v[148:151], v[144:147], v[136:139], v[148:151]\n"
"s_waitcnt lgkmcnt(0)\n"
"v_mfma_f32_16x16x32_bf16 v[148:151], v[144:147], v[152:155], v[148:151]\n"
"v_mfma_f32_16x16x32_bf16 v[164:167], v[248:251], v[136:139], v[164:167]\n"
"v_mfma_f32_16x16x32_bf16 v[164:167], v[232:235], v[152:155], v[164:167]\n"
"v_mfma_f32_16x16x32_bf16 v[160:163], v[196:199], v[136:139], v[160:163]\n"
"ds_read2st64_b32 v[144:145], v14 offset0:126 offset1:127\n"
"v_mfma_f32_16x16x32_bf16 v[160:163], v[180:183], v[152:155], v[160:163]\n"
"s_add_u32 m0, s51, 155648\n"
"s_nop 0\n"
"global_load_lds_dwordx4 v8, s[60:61] nt\n"
"ds_read2st64_b32 v[146:147], v14 offset0:128 offset1:129\n"
"v_rcp_f32 v8, v148\n"
"v_mfma_f32_16x16x32_bf16 v[140:143], v[222:225], v[136:139], v[140:143]\n"
"ds_read_b128 v[136:139], v17 offset:53248\n"
"v_mul_f32 v164, v164, v8\n"
"v_mfma_f32_16x16x32_bf16 v[140:143], v[206:209], v[152:155], v[140:143]\n"
"ds_read2st64_b32 v[148:149], v14 offset0:150 offset1:151\n"
"v_mul_f32 v165, v165, v8\n"
"v_cvt_pk_bf16_f32 v246, v164, v165\n"
"v_mul_f32 v166, v166, v8\n"
"v_mul_f32 v167, v167, v8\n"
"v_cvt_pk_bf16_f32 v247, v166, v167\n"
"v_mul_f32 v160, v160, v8\n"
"v_mul_f32 v161, v161, v8\n"
"v_cvt_pk_bf16_f32 v160, v160, v161\n"
"v_mul_f32 v162, v162, v8\n"
"v_mul_f32 v163, v163, v8\n"
"s_waitcnt lgkmcnt(2)\n"
"v_mfma_f32_16x16x32_bf16 v[152:155], v[96:99], v[144:147], 0\n"
"ds_read2st64_b32 v[150:151], v14 offset0:152 offset1:153\n"
"v_cvt_pk_bf16_f32 v161, v162, v163\n"
"v_mfma_f32_16x16x32_bf16 v[156:159], v[100:103], v[144:147], 0\n"
"s_waitcnt lgkmcnt(2)\n"
"ds_read_b128 v[164:167], v17 offset:54272\n"
"v_mul_f32 v140, v140, v8\n"
"v_mfma_f32_16x16x32_bf16 v[168:171], v[136:139], v[144:147], 0\n"
"ds_read2st64_b32 v[136:137], v14 offset0:174 offset1:175\n"
"v_mul_f32 v141, v141, v8\n"
"v_cvt_pk_bf16_f32 v162, v140, v141\n"
"v_mul_f32 v142, v142, v8\n"
"v_mul_f32 v143, v143, v8\n"
"v_cvt_pk_bf16_f32 v163, v142, v143\n"
"ds_read2st64_b32 v[138:139], v14 offset0:176 offset1:177\n"
"s_waitcnt lgkmcnt(3)\n"
"v_mfma_f32_16x16x32_bf16 v[152:155], v[104:107], v[148:151], v[152:155]\n"
"v_mfma_f32_16x16x32_bf16 v[156:159], v[108:111], v[148:151], v[156:159]\n"
"s_waitcnt lgkmcnt(2)\n"
"ds_read_b128 v[140:143], v17 offset:55296\n"
"v_mfma_f32_16x16x32_bf16 v[168:171], v[164:167], v[148:151], v[168:171]\n"
"s_waitcnt lgkmcnt(1)\n"
"ds_read2st64_b32 v[144:145], v14 offset0:198 offset1:199\n"
"ds_read2st64_b32 v[146:147], v14 offset0:200 offset1:201\n"
"v_mfma_f32_16x16x32_bf16 v[152:155], v[0:3], v[136:139], v[152:155]\n"
"v_mfma_f32_16x16x32_bf16 v[156:159], v[112:115], v[136:139], v[156:159]\n"
"s_waitcnt lgkmcnt(2)\n"
"ds_read_b128 v[148:151], v17 offset:56320\n"
"v_mfma_f32_16x16x32_bf16 v[168:171], v[140:143], v[136:139], v[168:171]\n"
"s_waitcnt lgkmcnt(1)\n"
"ds_read2st64_b32 v[136:137], v14 offset0:130 offset1:131\n"
"ds_read2st64_b32 v[138:139], v14 offset0:154 offset1:155\n"
"v_mfma_f32_16x16x32_bf16 v[152:155], v[116:119], v[144:147], v[152:155]\n"
"v_mfma_f32_16x16x32_bf16 v[156:159], v[120:123], v[144:147], v[156:159]\n"
"s_waitcnt lgkmcnt(2)\n"
"ds_read_b128 v[140:143], v17 offset:57344\n"
"v_mfma_f32_16x16x32_bf16 v[168:171], v[148:151], v[144:147], v[168:171]\n"
"s_waitcnt lgkmcnt(1)\n"
"ds_read2st64_b32 v[144:145], v14 offset0:178 offset1:179\n"
"ds_read2st64_b32 v[146:147], v14 offset0:202 offset1:203\n"
"v_mfma_f32_16x16x32_bf16 v[152:155], v[4:7], v[136:139], v[152:155]\n"
"v_mfma_f32_16x16x32_bf16 v[156:159], v[124:127], v[136:139], v[156:159]\n"
"s_waitcnt lgkmcnt(2)\n"
"ds_read_b128 v[148:151], v17 offset:58368\n"
"v_mfma_f32_16x16x32_bf16 v[168:171], v[140:143], v[136:139], v[168:171]\n"
"s_waitcnt lgkmcnt(1)\n"
"ds_read_b128 v[136:139], v12 offset:12288\n"
"v_mfma_f32_16x16x32_bf16 v[152:155], v[128:131], v[144:147], v[152:155]\n"
"v_mfma_f32_16x16x32_bf16 v[156:159], v[132:135], v[144:147], v[156:159]\n"
"s_waitcnt vmcnt(10)\n"
"s_barrier\n"
"s_waitcnt lgkmcnt(1)\n"
"v_mfma_f32_16x16x32_bf16 v[168:171], v[148:151], v[144:147], v[168:171]\n"
"s_waitcnt lgkmcnt(0)\n"
"ds_read_b128 v[140:143], v13 offset:12288\n"
"v_mfma_f32_16x16x32_bf16 v[144:147], v[136:139], v[28:31], 0\n"
"v_mfma_f32_16x16x32_bf16 v[148:151], v[136:139], v[32:35], 0\n"
"v_mfma_f32_16x16x32_bf16 v[164:167], v[136:139], v[24:27], 0\n"
"ds_read_b128 v[136:139], v12 offset:14336\n"
"v_cvt_pk_bf16_f32 v152, v152, v153\n"
"v_cvt_pk_bf16_f32 v153, v154, v155\n"
"v_cvt_pk_bf16_f32 v154, v156, v157\n"
"v_cvt_pk_bf16_f32 v168, v168, v169\n"
"v_cvt_pk_bf16_f32 v155, v158, v159\n"
"v_cvt_pk_bf16_f32 v169, v170, v171\n"
"s_waitcnt lgkmcnt(1)\n"
"v_mfma_f32_16x16x32_bf16 v[144:147], v[140:143], v[40:43], v[144:147]\n"
"s_mov_b32 m0, s54\n"
"v_mfma_f32_16x16x32_bf16 v[148:151], v[140:143], v[44:47], v[148:151]\n"
"ds_write_addtid_b32 v246 offset:33280\n"
"v_mfma_f32_16x16x32_bf16 v[164:167], v[140:143], v[36:39], v[164:167]\n"
"s_waitcnt lgkmcnt(1)\n"
"ds_read_b128 v[140:143], v13 offset:14336\n"
"v_mfma_f32_16x16x32_bf16 v[144:147], v[136:139], v[48:51], v[144:147]\n"
"ds_write_addtid_b32 v247 offset:33536\n"
"v_mfma_f32_16x16x32_bf16 v[148:151], v[136:139], v[52:55], v[148:151]\n"
"ds_write_addtid_b32 v160 offset:32256\n"
"v_mfma_f32_16x16x32_bf16 v[164:167], v[136:139], v[56:59], v[164:167]\n"
"ds_write_addtid_b32 v161 offset:32512\n"
"ds_read_b128 v[136:139], v12 offset:16384\n"
"ds_write_addtid_b32 v162 offset:32768\n"
"ds_write_addtid_b32 v163 offset:33024\n"
"s_waitcnt lgkmcnt(6)\n"
"v_mfma_f32_16x16x32_bf16 v[144:147], v[140:143], v[60:63], v[144:147]\n"
"s_mov_b32 m0, s68\n"
"v_mfma_f32_16x16x32_bf16 v[148:151], v[140:143], v[64:67], v[148:151]\n"
"ds_write_addtid_b32 v152 offset:45056\n"
"v_mfma_f32_16x16x32_bf16 v[164:167], v[140:143], v[68:71], v[164:167]\n"
"ds_read_b128 v[140:143], v13 offset:16384\n"
"ds_write_addtid_b32 v153 offset:45312\n"
"s_waitcnt lgkmcnt(5)\n"
"v_mfma_f32_16x16x32_bf16 v[144:147], v[136:139], v[72:75], v[144:147]\n"
"ds_write_addtid_b32 v154 offset:45568\n"
"v_mfma_f32_16x16x32_bf16 v[148:151], v[136:139], v[76:79], v[148:151]\n"
"ds_write_addtid_b32 v155 offset:45824\n"
"ds_write_addtid_b32 v168 offset:46080\n"
"v_mfma_f32_16x16x32_bf16 v[164:167], v[136:139], v[80:83], v[164:167]\n"
"ds_write_addtid_b32 v169 offset:46336\n"
"v_mov_b32 v136, 0x3f803f80\n"
"v_mov_b32 v137, 0x3f803f80\n"
"v_mov_b32 v138, 0x3f803f80\n"
"v_mov_b32 v139, 0x3f803f80\n"
"s_waitcnt lgkmcnt(5)\n"
"v_mfma_f32_16x16x32_bf16 v[144:147], v[140:143], v[84:87], v[144:147]\n"
"v_mfma_f32_16x16x32_bf16 v[148:151], v[140:143], v[88:91], v[148:151]\n"
"v_mfma_f32_16x16x32_bf16 v[164:167], v[140:143], v[92:95], v[164:167]\n"
"s_waitcnt lgkmcnt(0)\n"
"s_barrier\n"
"ds_read2st64_b32 v[140:141], v9 offset0:112 offset1:113\n"
"ds_read2st64_b32 v[142:143], v9 offset0:114 offset1:115\n"
"s_waitcnt lgkmcnt(0)\n"
"v_mfma_f32_16x16x32_bf16 v[152:155], v[136:139], v[140:143], 0\n"
"ds_read2st64_b32 v[156:157], v9 offset0:116 offset1:117\n"
"v_mfma_f32_16x16x32_bf16 v[160:163], v[180:183], v[140:143], 0\n"
"ds_read2st64_b32 v[158:159], v9 offset0:118 offset1:119\n"
"v_cvt_pk_bf16_f32 v194, v144, v145\n"
"v_mfma_f32_16x16x32_bf16 v[168:171], v[206:209], v[140:143], 0\n"
"s_and_b32 s41, 416, s55\n"
"v_add_u32 v8, s41, v15\n"
"v_cvt_pk_bf16_f32 v195, v146, v147\n"
"v_mfma_f32_16x16x32_bf16 v[144:147], v[232:235], v[140:143], 0\n"
"ds_read_u16 v11, v21 offset:928\n"
"v_cvt_pk_bf16_f32 v220, v148, v149\n"
"v_cvt_pk_bf16_f32 v221, v150, v151\n"
"v_cvt_pk_bf16_f32 v246, v164, v165\n"
"v_cvt_pk_bf16_f32 v247, v166, v167\n"
"ds_read2st64_b32 v[140:141], v20 offset0:0 offset1:1\n"
"ds_read2st64_b32 v[142:143], v20 offset0:2 offset1:3\n"
"ds_read_u16 v8, v8 offset:704\n"
"s_waitcnt lgkmcnt(4)\n"
"v_mfma_f32_16x16x32_bf16 v[152:155], v[136:139], v[156:159], v[152:155]\n"
"ds_read2st64_b32 v[148:149], v9 offset0:128 offset1:129\n"
"v_mfma_f32_16x16x32_bf16 v[160:163], v[190:193], v[156:159], v[160:163]\n"
"ds_read2st64_b32 v[150:151], v9 offset0:130 offset1:131\n"
"v_mfma_f32_16x16x32_bf16 v[168:171], v[216:219], v[156:159], v[168:171]\n"
"s_waitcnt lgkmcnt(5)\n"
"v_mad_u32_u24 v10, v11, s40, v18\n"
"v_mad_u32_u24 v11, v11, s40, v19\n"
"v_mfma_f32_16x16x32_bf16 v[144:147], v[242:245], v[156:159], v[144:147]\n"
"s_waitcnt lgkmcnt(3)\n"
"ds_read2st64_b32 v[174:175], v20 offset0:4 offset1:5\n"
"global_store_dwordx4 v10, v[140:143], s[66:67] sc1\n"
"s_waitcnt lgkmcnt(3)\n"
"v_mad_u32_u24 v8, v8, s40, v16\n"
"s_waitcnt lgkmcnt(1)\n"
"v_mfma_f32_16x16x32_bf16 v[152:155], v[136:139], v[148:151], v[152:155]\n"
"s_add_u32 m0, s51, 114688\n"
"s_nop 0\n"
"global_load_lds_dwordx4 v8, s[56:57] nt\n"
"ds_read2st64_b32 v[140:141], v9 offset0:132 offset1:133\n"
"v_mfma_f32_16x16x32_bf16 v[160:163], v[194:197], v[148:151], v[160:163]\n"
"ds_read2st64_b32 v[142:143], v9 offset0:134 offset1:135\n"
"v_mfma_f32_16x16x32_bf16 v[168:171], v[220:223], v[148:151], v[168:171]\n"
"ds_read2st64_b32 v[158:159], v9 offset0:36 offset1:37\n"
"v_mov_b32 v156, 0\n"
"v_mfma_f32_16x16x32_bf16 v[144:147], v[246:249], v[148:151], v[144:147]\n"
"s_waitcnt lgkmcnt(3)\n"
"global_store_dwordx2 v11, v[174:175], s[66:67] offset:256 sc1\n"
"v_mov_b32 v157, 0\n"
"s_waitcnt lgkmcnt(1)\n"
"v_mfma_f32_16x16x32_bf16 v[152:155], v[136:139], v[140:143], v[152:155]\n"
"s_add_u32 m0, s51, 116736\n"
"s_nop 0\n"
"global_load_lds_dwordx4 v8, s[58:59] nt\n"
"s_waitcnt lgkmcnt(0)\n"
"v_mfma_f32_16x16x32_bf16 v[152:155], v[136:139], v[156:159], v[152:155]\n"
"v_mfma_f32_16x16x32_bf16 v[144:147], v[250:253], v[140:143], v[144:147]\n"
"v_mfma_f32_16x16x32_bf16 v[144:147], v[234:237], v[156:159], v[144:147]\n"
"v_mfma_f32_16x16x32_bf16 v[160:163], v[198:201], v[140:143], v[160:163]\n"
"ds_read2st64_b32 v[136:137], v14 offset0:126 offset1:127\n"
"v_mfma_f32_16x16x32_bf16 v[160:163], v[182:185], v[156:159], v[160:163]\n"
"ds_read2st64_b32 v[138:139], v14 offset0:128 offset1:129\n"
"v_mfma_f32_16x16x32_bf16 v[168:171], v[224:227], v[140:143], v[168:171]\n"
"s_add_u32 m0, s51, 118784\n"
"s_nop 0\n"
"global_load_lds_dwordx4 v8, s[60:61] nt\n"
"v_rcp_f32 v8, v152\n"
"v_mfma_f32_16x16x32_bf16 v[168:171], v[208:211], v[156:159], v[168:171]\n"
"ds_read_b128 v[140:143], v17 offset:53248\n"
"v_mul_f32 v144, v144, v8\n"
"v_mul_f32 v145, v145, v8\n"
"v_cvt_pk_bf16_f32 v248, v144, v145\n"
"v_mul_f32 v146, v146, v8\n"
"v_mul_f32 v147, v147, v8\n"
"v_cvt_pk_bf16_f32 v249, v146, v147\n"
"v_mul_f32 v160, v160, v8\n"
"v_mul_f32 v161, v161, v8\n"
"v_cvt_pk_bf16_f32 v160, v160, v161\n"
"s_waitcnt lgkmcnt(1)\n"
"v_mfma_f32_16x16x32_bf16 v[144:147], v[96:99], v[136:139], 0\n"
"ds_read2st64_b32 v[148:149], v14 offset0:150 offset1:151\n"
"v_mul_f32 v162, v162, v8\n"
"v_mfma_f32_16x16x32_bf16 v[152:155], v[100:103], v[136:139], 0\n"
"ds_read2st64_b32 v[150:151], v14 offset0:152 offset1:153\n"
"v_mul_f32 v163, v163, v8\n"
"v_cvt_pk_bf16_f32 v161, v162, v163\n"
"v_mul_f32 v168, v168, v8\n"
"s_waitcnt lgkmcnt(2)\n"
"v_mfma_f32_16x16x32_bf16 v[156:159], v[140:143], v[136:139], 0\n"
"ds_read_b128 v[136:139], v17 offset:54272\n"
"v_mul_f32 v169, v169, v8\n"
"v_cvt_pk_bf16_f32 v162, v168, v169\n"
"v_mul_f32 v170, v170, v8\n"
"v_mul_f32 v171, v171, v8\n"
"v_cvt_pk_bf16_f32 v163, v170, v171\n"
"ds_read2st64_b32 v[140:141], v14 offset0:174 offset1:175\n"
"ds_read2st64_b32 v[142:143], v14 offset0:176 offset1:177\n"
"s_waitcnt lgkmcnt(3)\n"
"v_mfma_f32_16x16x32_bf16 v[144:147], v[104:107], v[148:151], v[144:147]\n"
"v_mfma_f32_16x16x32_bf16 v[152:155], v[108:111], v[148:151], v[152:155]\n"
"s_waitcnt lgkmcnt(2)\n"
"ds_read_b128 v[164:167], v17 offset:55296\n"
"v_mfma_f32_16x16x32_bf16 v[156:159], v[136:139], v[148:151], v[156:159]\n"
"s_waitcnt lgkmcnt(1)\n"
"ds_read2st64_b32 v[136:137], v14 offset0:198 offset1:199\n"
"ds_read2st64_b32 v[138:139], v14 offset0:200 offset1:201\n"
"v_mfma_f32_16x16x32_bf16 v[144:147], v[0:3], v[140:143], v[144:147]\n"
"v_mfma_f32_16x16x32_bf16 v[152:155], v[112:115], v[140:143], v[152:155]\n"
"s_waitcnt lgkmcnt(2)\n"
"ds_read_b128 v[148:151], v17 offset:56320\n"
"v_mfma_f32_16x16x32_bf16 v[156:159], v[164:167], v[140:143], v[156:159]\n"
"s_waitcnt lgkmcnt(1)\n"
"ds_read2st64_b32 v[140:141], v14 offset0:130 offset1:131\n"
"ds_read2st64_b32 v[142:143], v14 offset0:154 offset1:155\n"
"v_mfma_f32_16x16x32_bf16 v[144:147], v[116:119], v[136:139], v[144:147]\n"
"v_mfma_f32_16x16x32_bf16 v[152:155], v[120:123], v[136:139], v[152:155]\n"
"s_waitcnt lgkmcnt(2)\n"
"ds_read_b128 v[164:167], v17 offset:57344\n"
"v_mfma_f32_16x16x32_bf16 v[156:159], v[148:151], v[136:139], v[156:159]\n"
"s_waitcnt lgkmcnt(1)\n"
"ds_read2st64_b32 v[136:137], v14 offset0:178 offset1:179\n"
"ds_read2st64_b32 v[138:139], v14 offset0:202 offset1:203\n"
"v_mfma_f32_16x16x32_bf16 v[144:147], v[4:7], v[140:143], v[144:147]\n"
"v_mfma_f32_16x16x32_bf16 v[152:155], v[124:127], v[140:143], v[152:155]\n"
"s_waitcnt lgkmcnt(2)\n"
"ds_read_b128 v[148:151], v17 offset:58368\n"
"v_mfma_f32_16x16x32_bf16 v[156:159], v[164:167], v[140:143], v[156:159]\n"
"s_waitcnt lgkmcnt(1)\n"
"ds_read_b128 v[140:143], v12 offset:36864\n"
"v_mfma_f32_16x16x32_bf16 v[144:147], v[128:131], v[136:139], v[144:147]\n"
"v_mfma_f32_16x16x32_bf16 v[152:155], v[132:135], v[136:139], v[152:155]\n"
"s_waitcnt vmcnt(10)\n"
"s_barrier\n"
"s_waitcnt lgkmcnt(1)\n"
"v_mfma_f32_16x16x32_bf16 v[156:159], v[148:151], v[136:139], v[156:159]\n"
"s_waitcnt lgkmcnt(0)\n"
"ds_read_b128 v[136:139], v13 offset:36864\n"
"v_mfma_f32_16x16x32_bf16 v[148:151], v[140:143], v[28:31], 0\n"
"v_mfma_f32_16x16x32_bf16 v[164:167], v[140:143], v[32:35], 0\n"
"v_mfma_f32_16x16x32_bf16 v[168:171], v[140:143], v[24:27], 0\n"
"ds_read_b128 v[140:143], v12 offset:38912\n"
"v_cvt_pk_bf16_f32 v144, v144, v145\n"
"v_cvt_pk_bf16_f32 v145, v146, v147\n"
"v_cvt_pk_bf16_f32 v146, v152, v153\n"
"v_cvt_pk_bf16_f32 v156, v156, v157\n"
"v_cvt_pk_bf16_f32 v147, v154, v155\n"
"v_cvt_pk_bf16_f32 v157, v158, v159\n"
"s_waitcnt lgkmcnt(1)\n"
"v_mfma_f32_16x16x32_bf16 v[148:151], v[136:139], v[40:43], v[148:151]\n"
"s_mov_b32 m0, s54\n"
"v_mfma_f32_16x16x32_bf16 v[164:167], v[136:139], v[44:47], v[164:167]\n"
"ds_write_addtid_b32 v248 offset:33280\n"
"v_mfma_f32_16x16x32_bf16 v[168:171], v[136:139], v[36:39], v[168:171]\n"
"s_waitcnt lgkmcnt(1)\n"
"ds_read_b128 v[136:139], v13 offset:38912\n"
"v_mfma_f32_16x16x32_bf16 v[148:151], v[140:143], v[48:51], v[148:151]\n"
"ds_write_addtid_b32 v249 offset:33536\n"
"v_mfma_f32_16x16x32_bf16 v[164:167], v[140:143], v[52:55], v[164:167]\n"
"ds_write_addtid_b32 v160 offset:32256\n"
"v_mfma_f32_16x16x32_bf16 v[168:171], v[140:143], v[56:59], v[168:171]\n"
"ds_write_addtid_b32 v161 offset:32512\n"
"ds_read_b128 v[140:143], v12 offset:40960\n"
"ds_write_addtid_b32 v162 offset:32768\n"
"ds_write_addtid_b32 v163 offset:33024\n"
"s_waitcnt lgkmcnt(6)\n"
"v_mfma_f32_16x16x32_bf16 v[148:151], v[136:139], v[60:63], v[148:151]\n"
"s_mov_b32 m0, s68\n"
"v_mfma_f32_16x16x32_bf16 v[164:167], v[136:139], v[64:67], v[164:167]\n"
"ds_write_addtid_b32 v144 offset:45056\n"
"v_mfma_f32_16x16x32_bf16 v[168:171], v[136:139], v[68:71], v[168:171]\n"
"ds_read_b128 v[136:139], v13 offset:40960\n"
"ds_write_addtid_b32 v145 offset:45312\n"
"s_waitcnt lgkmcnt(5)\n"
"v_mfma_f32_16x16x32_bf16 v[148:151], v[140:143], v[72:75], v[148:151]\n"
"ds_write_addtid_b32 v146 offset:45568\n"
"v_mfma_f32_16x16x32_bf16 v[164:167], v[140:143], v[76:79], v[164:167]\n"
"ds_write_addtid_b32 v147 offset:45824\n"
"ds_write_addtid_b32 v156 offset:46080\n"
"v_mfma_f32_16x16x32_bf16 v[168:171], v[140:143], v[80:83], v[168:171]\n"
"ds_write_addtid_b32 v157 offset:46336\n"
"v_mov_b32 v140, 0x3f803f80\n"
"v_mov_b32 v141, 0x3f803f80\n"
"v_mov_b32 v142, 0x3f803f80\n"
"v_mov_b32 v143, 0x3f803f80\n"
"s_waitcnt lgkmcnt(5)\n"
"v_mfma_f32_16x16x32_bf16 v[148:151], v[136:139], v[84:87], v[148:151]\n"
"v_mfma_f32_16x16x32_bf16 v[164:167], v[136:139], v[88:91], v[164:167]\n"
"v_mfma_f32_16x16x32_bf16 v[168:171], v[136:139], v[92:95], v[168:171]\n"
"s_waitcnt vmcnt(5)\n"
"s_waitcnt lgkmcnt(0)\n"
"s_barrier\n"
"ds_read2st64_b32 v[136:137], v9 offset0:120 offset1:121\n"
"ds_read2st64_b32 v[138:139], v9 offset0:122 offset1:123\n"
"s_waitcnt lgkmcnt(0)\n"
"v_mfma_f32_16x16x32_bf16 v[144:147], v[140:143], v[136:139], 0\n"
"ds_read_b32 v153, v9 offset:32000\n"
"v_mfma_f32_16x16x32_bf16 v[156:159], v[200:203], v[136:139], 0\n"
"ds_read2st64_b32 v[154:155], v9 offset0:126 offset1:127\n"
"v_cvt_pk_bf16_f32 v196, v148, v149\n"
"v_mfma_f32_16x16x32_bf16 v[200:203], v[226:229], v[136:139], 0\n"
"s_and_b32 s41, 416, s55\n"
"v_add_u32 v8, s41, v15\n"
"v_cvt_pk_bf16_f32 v197, v150, v151\n"
"v_mfma_f32_16x16x32_bf16 v[226:229], v[252:255], v[136:139], 0\n"
"ds_read_u16 v11, v21 offset:960\n"
"v_mov_b32 v152, 0\n"
"v_cvt_pk_bf16_f32 v222, v164, v165\n"
"v_cvt_pk_bf16_f32 v223, v166, v167\n"
"v_cvt_pk_bf16_f32 v248, v168, v169\n"
"v_cvt_pk_bf16_f32 v249, v170, v171\n"
"ds_read2st64_b32 v[252:253], v20 offset0:0 offset1:1\n"
"ds_read2st64_b32 v[254:255], v20 offset0:2 offset1:3\n"
"s_waitcnt lgkmcnt(3)\n"
"v_mfma_f32_16x16x32_bf16 v[144:147], v[140:143], v[152:155], v[144:147]\n"
"ds_read_b32 v137, v9 offset:33024\n"
"v_mfma_f32_16x16x32_bf16 v[156:159], v[182:185], v[152:155], v[156:159]\n"
"ds_read2st64_b32 v[138:139], v9 offset0:130 offset1:131\n"
"v_mov_b32 v136, 0\n"
"v_mfma_f32_16x16x32_bf16 v[200:203], v[208:211], v[152:155], v[200:203]\n"
"s_waitcnt lgkmcnt(4)\n"
"ds_read_u16 v8, v8 offset:736\n"
"v_mad_u32_u24 v10, v11, s40, v18\n"
"v_mfma_f32_16x16x32_bf16 v[226:229], v[234:237], v[152:155], v[226:229]\n"
"s_waitcnt lgkmcnt(3)\n"
"ds_read2st64_b32 v[174:175], v20 offset0:4 offset1:5\n"
"v_mad_u32_u24 v11, v11, s40, v19\n"
"global_store_dwordx4 v10, v[252:255], s[66:67] sc1\n"
"s_waitcnt lgkmcnt(2)\n"
"v_mfma_f32_16x16x32_bf16 v[144:147], v[140:143], v[136:139], v[144:147]\n"
"s_waitcnt lgkmcnt(1)\n"
"ds_read2st64_b32 v[252:253], v9 offset0:132 offset1:133\n"
"v_mad_u32_u24 v8, v8, s40, v16\n"
"v_mfma_f32_16x16x32_bf16 v[156:159], v[192:195], v[136:139], v[156:159]\n"
"s_add_u32 m0, s51, 139264\n"
"s_nop 0\n"
"global_load_lds_dwordx4 v8, s[56:57] nt\n"
"ds_read_b32 v255, v9 offset:34560\n"
"v_mfma_f32_16x16x32_bf16 v[200:203], v[218:221], v[136:139], v[200:203]\n"
"ds_read2st64_b32 v[150:151], v9 offset0:38 offset1:39\n"
"v_mov_b32 v254, 0\n"
"v_mfma_f32_16x16x32_bf16 v[226:229], v[244:247], v[136:139], v[226:229]\n"
"s_waitcnt lgkmcnt(3)\n"
"global_store_dwordx2 v11, v[174:175], s[66:67] offset:256 sc1\n"
"v_mov_b32 v148, 0\n"
"v_mov_b32 v149, 0\n"
"s_waitcnt lgkmcnt(1)\n"
"v_mfma_f32_16x16x32_bf16 v[144:147], v[140:143], v[252:255], v[144:147]\n"
"s_add_u32 m0, s51, 141312\n"
"s_nop 0\n"
"global_load_lds_dwordx4 v8, s[58:59] nt\n"
"s_waitcnt lgkmcnt(0)\n"
"v_mfma_f32_16x16x32_bf16 v[144:147], v[140:143], v[148:151], v[144:147]\n"
"v_mfma_f32_16x16x32_bf16 v[226:229], v[248:251], v[252:255], v[226:229]\n"
"v_mfma_f32_16x16x32_bf16 v[226:229], v[236:239], v[148:151], v[226:229]\n"
"v_mfma_f32_16x16x32_bf16 v[156:159], v[196:199], v[252:255], v[156:159]\n"
"ds_read2st64_b32 v[136:137], v14 offset0:126 offset1:127\n"
"v_mfma_f32_16x16x32_bf16 v[156:159], v[184:187], v[148:151], v[156:159]\n"
"ds_read2st64_b32 v[138:139], v14 offset0:128 offset1:129\n"
"v_mfma_f32_16x16x32_bf16 v[200:203], v[222:225], v[252:255], v[200:203]\n"
"s_add_u32 m0, s51, 143360\n"
"s_nop 0\n"
"global_load_lds_dwordx4 v8, s[60:61] nt\n"
"v_rcp_f32 v8, v144\n"
"v_mfma_f32_16x16x32_bf16 v[200:203], v[210:213], v[148:151], v[200:203]\n"
"ds_read_b128 v[250:253], v17 offset:53248\n"
"v_mul_f32 v226, v226, v8\n"
"v_mul_f32 v227, v227, v8\n"
"v_cvt_pk_bf16_f32 v198, v226, v227\n"
"v_mul_f32 v228, v228, v8\n"
"v_mul_f32 v229, v229, v8\n"
"v_cvt_pk_bf16_f32 v199, v228, v229\n"
"v_mul_f32 v156, v156, v8\n"
"v_mul_f32 v157, v157, v8\n"
"v_cvt_pk_bf16_f32 v156, v156, v157\n"
"s_waitcnt lgkmcnt(1)\n"
"v_mfma_f32_16x16x32_bf16 v[224:227], v[96:99], v[136:139], 0\n"
"ds_read2st64_b32 v[140:141], v14 offset0:150 offset1:151\n"
"v_mul_f32 v158, v158, v8\n"
"v_mfma_f32_16x16x32_bf16 v[144:147], v[100:103], v[136:139], 0\n"
"ds_read2st64_b32 v[142:143], v14 offset0:152 offset1:153\n"
"v_mul_f32 v159, v159, v8\n"
"v_cvt_pk_bf16_f32 v157, v158, v159\n"
"v_mul_f32 v200, v200, v8\n"
"s_waitcnt lgkmcnt(2)\n"
"v_mfma_f32_16x16x32_bf16 v[148:151], v[250:253], v[136:139], 0\n"
"ds_read_b128 v[250:253], v17 offset:54272\n"
"v_mul_f32 v201, v201, v8\n"
"v_cvt_pk_bf16_f32 v158, v200, v201\n"
"v_mul_f32 v202, v202, v8\n"
"v_mul_f32 v203, v203, v8\n"
"v_cvt_pk_bf16_f32 v159, v202, v203\n"
"ds_read2st64_b32 v[200:201], v14 offset0:174 offset1:175\n"
"ds_read2st64_b32 v[202:203], v14 offset0:176 offset1:177\n"
"s_waitcnt lgkmcnt(3)\n"
"v_mfma_f32_16x16x32_bf16 v[224:227], v[104:107], v[140:143], v[224:227]\n"
"v_mfma_f32_16x16x32_bf16 v[144:147], v[108:111], v[140:143], v[144:147]\n"
"s_waitcnt lgkmcnt(2)\n"
"ds_read_b128 v[136:139], v17 offset:55296\n"
"v_mfma_f32_16x16x32_bf16 v[148:151], v[250:253], v[140:143], v[148:151]\n"
"s_waitcnt lgkmcnt(1)\n"
"ds_read2st64_b32 v[250:251], v14 offset0:198 offset1:199\n"
"ds_read2st64_b32 v[252:253], v14 offset0:200 offset1:201\n"
"v_mfma_f32_16x16x32_bf16 v[224:227], v[0:3], v[200:203], v[224:227]\n"
"v_mfma_f32_16x16x32_bf16 v[144:147], v[112:115], v[200:203], v[144:147]\n"
"s_waitcnt lgkmcnt(2)\n"
"ds_read_b128 v[140:143], v17 offset:56320\n"
"v_mfma_f32_16x16x32_bf16 v[148:151], v[136:139], v[200:203], v[148:151]\n"
"s_waitcnt lgkmcnt(1)\n"
"ds_read2st64_b32 v[200:201], v14 offset0:130 offset1:131\n"
"ds_read2st64_b32 v[202:203], v14 offset0:154 offset1:155\n"
"v_mfma_f32_16x16x32_bf16 v[224:227], v[116:119], v[250:253], v[224:227]\n"
"v_mfma_f32_16x16x32_bf16 v[144:147], v[120:123], v[250:253], v[144:147]\n"
"s_waitcnt lgkmcnt(2)\n"
"ds_read_b128 v[136:139], v17 offset:57344\n"
"v_mfma_f32_16x16x32_bf16 v[148:151], v[140:143], v[250:253], v[148:151]\n"
"s_waitcnt lgkmcnt(1)\n"
"ds_read2st64_b32 v[250:251], v14 offset0:178 offset1:179\n"
"ds_read2st64_b32 v[252:253], v14 offset0:202 offset1:203\n"
"v_mfma_f32_16x16x32_bf16 v[224:227], v[4:7], v[200:203], v[224:227]\n"
"v_mfma_f32_16x16x32_bf16 v[144:147], v[124:127], v[200:203], v[144:147]\n"
"s_waitcnt lgkmcnt(2)\n"
"ds_read_b128 v[140:143], v17 offset:58368\n"
"v_mfma_f32_16x16x32_bf16 v[148:151], v[136:139], v[200:203], v[148:151]\n"
"s_waitcnt lgkmcnt(1)\n"
"ds_read_b128 v[200:203], v12 offset:30720\n"
"v_mfma_f32_16x16x32_bf16 v[224:227], v[128:131], v[250:253], v[224:227]\n"
"v_mfma_f32_16x16x32_bf16 v[144:147], v[132:135], v[250:253], v[144:147]\n"
"s_barrier\n"
"s_waitcnt lgkmcnt(1)\n"
"v_mfma_f32_16x16x32_bf16 v[148:151], v[140:143], v[250:253], v[148:151]\n"
"s_waitcnt lgkmcnt(0)\n"
"ds_read_b128 v[250:253], v13 offset:30720\n"
"v_mfma_f32_16x16x32_bf16 v[136:139], v[200:203], v[28:31], 0\n"
"v_mfma_f32_16x16x32_bf16 v[140:143], v[200:203], v[32:35], 0\n"
"v_mfma_f32_16x16x32_bf16 v[152:155], v[200:203], v[24:27], 0\n"
"ds_read_b128 v[200:203], v12 offset:32768\n"
"v_cvt_pk_bf16_f32 v224, v224, v225\n"
"v_cvt_pk_bf16_f32 v225, v226, v227\n"
"v_cvt_pk_bf16_f32 v226, v144, v145\n"
"v_cvt_pk_bf16_f32 v227, v146, v147\n"
"v_cvt_pk_bf16_f32 v148, v148, v149\n"
"v_cvt_pk_bf16_f32 v149, v150, v151\n"
"s_waitcnt lgkmcnt(1)\n"
"v_mfma_f32_16x16x32_bf16 v[136:139], v[250:253], v[40:43], v[136:139]\n"
"s_mov_b32 m0, s54\n"
"v_mfma_f32_16x16x32_bf16 v[140:143], v[250:253], v[44:47], v[140:143]\n"
"ds_write_addtid_b32 v198 offset:33280\n"
"v_mfma_f32_16x16x32_bf16 v[152:155], v[250:253], v[36:39], v[152:155]\n"
"s_waitcnt lgkmcnt(1)\n"
"ds_read_b128 v[250:253], v13 offset:32768\n"
"v_mfma_f32_16x16x32_bf16 v[136:139], v[200:203], v[48:51], v[136:139]\n"
"ds_write_addtid_b32 v199 offset:33536\n"
"v_mfma_f32_16x16x32_bf16 v[140:143], v[200:203], v[52:55], v[140:143]\n"
"ds_write_addtid_b32 v156 offset:32256\n"
"v_mfma_f32_16x16x32_bf16 v[152:155], v[200:203], v[56:59], v[152:155]\n"
"ds_write_addtid_b32 v157 offset:32512\n"
"ds_read_b128 v[198:201], v12 offset:34816\n"
"ds_write_addtid_b32 v158 offset:32768\n"
"ds_write_addtid_b32 v159 offset:33024\n"
"s_waitcnt lgkmcnt(6)\n"
"v_mfma_f32_16x16x32_bf16 v[136:139], v[250:253], v[60:63], v[136:139]\n"
"s_mov_b32 m0, s68\n"
"v_mfma_f32_16x16x32_bf16 v[140:143], v[250:253], v[64:67], v[140:143]\n"
"ds_write_addtid_b32 v224 offset:45056\n"
"v_mfma_f32_16x16x32_bf16 v[152:155], v[250:253], v[68:71], v[152:155]\n"
"ds_read_b128 v[250:253], v13 offset:34816\n"
"ds_write_addtid_b32 v225 offset:45312\n"
"s_waitcnt lgkmcnt(5)\n"
"v_mfma_f32_16x16x32_bf16 v[136:139], v[198:201], v[72:75], v[136:139]\n"
"ds_write_addtid_b32 v226 offset:45568\n"
"v_mfma_f32_16x16x32_bf16 v[140:143], v[198:201], v[76:79], v[140:143]\n"
"ds_write_addtid_b32 v227 offset:45824\n"
"v_mfma_f32_16x16x32_bf16 v[152:155], v[198:201], v[80:83], v[152:155]\n"
"ds_read_b128 v[198:201], v12 offset:49152\n"
"ds_write_addtid_b32 v148 offset:46080\n"
"ds_write_addtid_b32 v149 offset:46336\n"
"s_waitcnt lgkmcnt(6)\n"
"v_mfma_f32_16x16x32_bf16 v[136:139], v[250:253], v[84:87], v[136:139]\n"
"v_mfma_f32_16x16x32_bf16 v[140:143], v[250:253], v[88:91], v[140:143]\n"
"v_mfma_f32_16x16x32_bf16 v[152:155], v[250:253], v[92:95], v[152:155]\n"
"s_waitcnt lgkmcnt(2)\n"
"ds_read_b128 v[226:229], v13 offset:49152\n"
"v_mfma_f32_16x16x32_bf16 v[252:255], v[198:201], v[28:31], 0\n"
"v_mfma_f32_16x16x32_bf16 v[144:147], v[198:201], v[32:35], 0\n"
"v_mfma_f32_16x16x32_bf16 v[148:151], v[198:201], v[24:27], 0\n"
"s_waitcnt lgkmcnt(0)\n"
"ds_read_b128 v[200:203], v12 offset:51200\n"
"v_mfma_f32_16x16x32_bf16 v[252:255], v[226:229], v[40:43], v[252:255]\n"
"v_cvt_pk_bf16_f32 v198, v136, v137\n"
"v_cvt_pk_bf16_f32 v199, v138, v139\n"
"v_cvt_pk_bf16_f32 v224, v140, v141\n"
"v_cvt_pk_bf16_f32 v225, v142, v143\n"
"v_mfma_f32_16x16x32_bf16 v[144:147], v[226:229], v[44:47], v[144:147]\n"
"v_cvt_pk_bf16_f32 v250, v152, v153\n"
"v_cvt_pk_bf16_f32 v251, v154, v155\n"
"v_mfma_f32_16x16x32_bf16 v[148:151], v[226:229], v[36:39], v[148:151]\n"
"s_waitcnt lgkmcnt(0)\n"
"ds_read_b128 v[226:229], v13 offset:51200\n"
"v_mfma_f32_16x16x32_bf16 v[252:255], v[200:203], v[48:51], v[252:255]\n"
"v_mfma_f32_16x16x32_bf16 v[144:147], v[200:203], v[52:55], v[144:147]\n"
"v_mfma_f32_16x16x32_bf16 v[148:151], v[200:203], v[56:59], v[148:151]\n"
"s_waitcnt lgkmcnt(0)\n"
"ds_read_b128 v[200:203], v12 offset:53248\n"
"v_mfma_f32_16x16x32_bf16 v[252:255], v[226:229], v[60:63], v[252:255]\n"
"v_mfma_f32_16x16x32_bf16 v[144:147], v[226:229], v[64:67], v[144:147]\n"
"v_mfma_f32_16x16x32_bf16 v[148:151], v[226:229], v[68:71], v[148:151]\n"
"s_waitcnt lgkmcnt(0)\n"
"ds_read_b128 v[226:229], v13 offset:53248\n"
"v_mfma_f32_16x16x32_bf16 v[252:255], v[200:203], v[72:75], v[252:255]\n"
"v_mfma_f32_16x16x32_bf16 v[144:147], v[200:203], v[76:79], v[144:147]\n"
"v_mfma_f32_16x16x32_bf16 v[148:151], v[200:203], v[80:83], v[148:151]\n"
"v_mov_b32 v136, 0x3f803f80\n"
"v_mov_b32 v137, 0x3f803f80\n"
"v_mov_b32 v138, 0x3f803f80\n"
"v_mov_b32 v139, 0x3f803f80\n"
"s_waitcnt lgkmcnt(0)\n"
"v_mfma_f32_16x16x32_bf16 v[252:255], v[226:229], v[84:87], v[252:255]\n"
"s_waitcnt vmcnt(5)\n"
"s_barrier\n"
"ds_read_b32 v141, v9 offset:28928\n"
"v_mov_b32 v140, 0\n"
"v_mfma_f32_16x16x32_bf16 v[144:147], v[226:229], v[88:91], v[144:147]\n"
"s_and_b32 s41, 416, s55\n"
"v_add_u32 v8, s41, v15\n"
"v_mfma_f32_16x16x32_bf16 v[148:151], v[226:229], v[92:95], v[148:151]\n"
"ds_read2st64_b32 v[142:143], v9 offset0:114 offset1:115\n"
"v_cvt_pk_bf16_f32 v200, v252, v253\n"
"v_cvt_pk_bf16_f32 v201, v254, v255\n"
"ds_read_u16 v8, v8 offset:768\n"
"s_waitcnt lgkmcnt(1)\n"
"v_mfma_f32_16x16x32_bf16 v[152:155], v[136:139], v[140:143], 0\n"
"ds_read2st64_b32 v[156:157], v9 offset0:116 offset1:117\n"
"v_cvt_pk_bf16_f32 v226, v144, v145\n"
"v_mfma_f32_16x16x32_bf16 v[160:163], v[180:183], v[140:143], 0\n"
"ds_read2st64_b32 v[158:159], v9 offset0:118 offset1:119\n"
"v_cvt_pk_bf16_f32 v227, v146, v147\n"
"v_mfma_f32_16x16x32_bf16 v[144:147], v[206:209], v[140:143], 0\n"
"ds_read_u16 v15, v21 offset:992\n"
"v_cvt_pk_bf16_f32 v252, v148, v149\n"
"v_mfma_f32_16x16x32_bf16 v[164:167], v[232:235], v[140:143], 0\n"
"s_waitcnt lgkmcnt(3)\n"
"ds_read2st64_b32 v[140:141], v20 offset0:0 offset1:1\n"
"v_cvt_pk_bf16_f32 v253, v150, v151\n"
"v_mad_u32_u24 v8, v8, s40, v16\n"
"s_add_u32 m0, s51, 126976\n"
"s_nop 0\n"
"global_load_lds_dwordx4 v8, s[56:57] nt\n"
"ds_read2st64_b32 v[142:143], v20 offset0:2 offset1:3\n"
"ds_read_b32 v149, v9 offset:33024\n"
"ds_read2st64_b32 v[150:151], v9 offset0:130 offset1:131\n"
"s_waitcnt lgkmcnt(5)\n"
"v_mfma_f32_16x16x32_bf16 v[152:155], v[136:139], v[156:159], v[152:155]\n"
"ds_read2st64_b32 v[168:169], v9 offset0:132 offset1:133\n"
"v_mov_b32 v148, 0\n"
"v_mfma_f32_16x16x32_bf16 v[160:163], v[184:187], v[156:159], v[160:163]\n"
"s_waitcnt lgkmcnt(5)\n"
"ds_read2st64_b32 v[170:171], v9 offset0:134 offset1:135\n"
"v_mad_u32_u24 v16, v15, s40, v18\n"
"v_mfma_f32_16x16x32_bf16 v[144:147], v[210:213], v[156:159], v[144:147]\n"
"ds_read2st64_b32 v[254:255], v20 offset0:4 offset1:5\n"
"v_mad_u32_u24 v15, v15, s40, v19\n"
"v_mfma_f32_16x16x32_bf16 v[164:167], v[236:239], v[156:159], v[164:167]\n"
"s_add_u32 m0, s51, 129024\n"
"s_nop 0\n"
"global_load_lds_dwordx4 v8, s[58:59] nt\n"
"s_waitcnt lgkmcnt(5)\n"
"global_store_dwordx4 v16, v[140:143], s[66:67] sc1\n"
"s_waitcnt lgkmcnt(3)\n"
"v_mfma_f32_16x16x32_bf16 v[152:155], v[136:139], v[148:151], v[152:155]\n"
"s_waitcnt lgkmcnt(1)\n"
"v_mfma_f32_16x16x32_bf16 v[152:155], v[136:139], v[168:171], v[152:155]\n"
"v_mfma_f32_16x16x32_bf16 v[164:167], v[242:245], v[148:151], v[164:167]\n"
"s_waitcnt lgkmcnt(0)\n"
"global_store_dwordx2 v15, v[254:255], s[66:67] offset:256 sc1\n"
"v_mfma_f32_16x16x32_bf16 v[164:167], v[246:249], v[168:171], v[164:167]\n"
"ds_read2st64_b32 v[136:137], v14 offset0:126 offset1:127\n"
"v_mfma_f32_16x16x32_bf16 v[160:163], v[190:193], v[148:151], v[160:163]\n"
"s_add_u32 m0, s51, 131072\n"
"s_nop 0\n"
"global_load_lds_dwordx4 v8, s[60:61] nt\n"
"ds_read2st64_b32 v[138:139], v14 offset0:128 offset1:129\n"
"v_rcp_f32 v16, v152\n"
"v_mfma_f32_16x16x32_bf16 v[160:163], v[194:197], v[168:171], v[160:163]\n"
"ds_read_b128 v[140:143], v17 offset:53248\n"
"v_mul_f32 v164, v164, v16\n"
"v_mfma_f32_16x16x32_bf16 v[144:147], v[216:219], v[148:151], v[144:147]\n"
"ds_read2st64_b32 v[148:149], v14 offset0:150 offset1:151\n"
"v_mul_f32 v165, v165, v16\n"
"v_mfma_f32_16x16x32_bf16 v[144:147], v[220:223], v[168:171], v[144:147]\n"
"ds_read2st64_b32 v[150:151], v14 offset0:152 offset1:153\n"
"v_cvt_pk_bf16_f32 v254, v164, v165\n"
"v_mul_f32 v166, v166, v16\n"
"v_mul_f32 v167, v167, v16\n"
"v_cvt_pk_bf16_f32 v255, v166, v167\n"
"v_mul_f32 v160, v160, v16\n"
"v_mul_f32 v161, v161, v16\n"
"s_waitcnt lgkmcnt(3)\n"
"v_mfma_f32_16x16x32_bf16 v[152:155], v[96:99], v[136:139], 0\n"
"ds_read_b128 v[156:159], v17 offset:54272\n"
"v_cvt_pk_bf16_f32 v160, v160, v161\n"
"v_mfma_f32_16x16x32_bf16 v[164:167], v[100:103], v[136:139], 0\n"
"s_waitcnt lgkmcnt(3)\n"
"ds_read2st64_b32 v[168:169], v14 offset0:174 offset1:175\n"
"v_mul_f32 v162, v162, v16\n"
"v_mfma_f32_16x16x32_bf16 v[172:175], v[140:143], v[136:139], 0\n"
"s_waitcnt lgkmcnt(2)\n"
"ds_read2st64_b32 v[170:171], v14 offset0:176 offset1:177\n"
"v_mul_f32 v163, v163, v16\n"
"v_cvt_pk_bf16_f32 v161, v162, v163\n"
"v_mfma_f32_16x16x32_bf16 v[152:155], v[104:107], v[148:151], v[152:155]\n"
"ds_read_b128 v[136:139], v17 offset:55296\n"
"v_mul_f32 v144, v144, v16\n"
"v_mfma_f32_16x16x32_bf16 v[164:167], v[108:111], v[148:151], v[164:167]\n"
"s_waitcnt lgkmcnt(3)\n"
"ds_read2st64_b32 v[140:141], v14 offset0:198 offset1:199\n"
"v_mul_f32 v145, v145, v16\n"
"v_cvt_pk_bf16_f32 v162, v144, v145\n"
"v_mfma_f32_16x16x32_bf16 v[172:175], v[156:159], v[148:151], v[172:175]\n"
"ds_read2st64_b32 v[142:143], v14 offset0:200 offset1:201\n"
"v_mul_f32 v146, v146, v16\n"
"v_mul_f32 v147, v147, v16\n"
"v_cvt_pk_bf16_f32 v163, v146, v147\n"
"s_waitcnt lgkmcnt(3)\n"
"v_mfma_f32_16x16x32_bf16 v[152:155], v[0:3], v[168:171], v[152:155]\n"
"ds_read_b128 v[144:147], v17 offset:56320\n"
"v_mfma_f32_16x16x32_bf16 v[164:167], v[112:115], v[168:171], v[164:167]\n"
"s_waitcnt lgkmcnt(3)\n"
"ds_read2st64_b32 v[148:149], v14 offset0:130 offset1:131\n"
"v_mfma_f32_16x16x32_bf16 v[172:175], v[136:139], v[168:171], v[172:175]\n"
"s_waitcnt lgkmcnt(2)\n"
"ds_read2st64_b32 v[150:151], v14 offset0:154 offset1:155\n"
"v_mfma_f32_16x16x32_bf16 v[152:155], v[116:119], v[140:143], v[152:155]\n"
"ds_read_b128 v[136:139], v17 offset:57344\n"
"v_mfma_f32_16x16x32_bf16 v[164:167], v[120:123], v[140:143], v[164:167]\n"
"s_waitcnt lgkmcnt(3)\n"
"ds_read2st64_b32 v[156:157], v14 offset0:178 offset1:179\n"
"v_mfma_f32_16x16x32_bf16 v[172:175], v[144:147], v[140:143], v[172:175]\n"
"s_waitcnt lgkmcnt(2)\n"
"ds_read2st64_b32 v[158:159], v14 offset0:202 offset1:203\n"
"v_mfma_f32_16x16x32_bf16 v[152:155], v[4:7], v[148:151], v[152:155]\n"
"ds_read_b128 v[140:143], v17 offset:58368\n"
"v_mfma_f32_16x16x32_bf16 v[164:167], v[124:127], v[148:151], v[164:167]\n"
"s_waitcnt lgkmcnt(3)\n"
"ds_read_b128 v[144:147], v12 offset:12288\n"
"v_mfma_f32_16x16x32_bf16 v[172:175], v[136:139], v[148:151], v[172:175]\n"
"s_waitcnt lgkmcnt(2)\n"
"v_mfma_f32_16x16x32_bf16 v[152:155], v[128:131], v[156:159], v[152:155]\n"
"v_mfma_f32_16x16x32_bf16 v[164:167], v[132:135], v[156:159], v[164:167]\n"
"s_barrier\n"
"ds_read_b128 v[136:139], v13 offset:12288\n"
"s_waitcnt lgkmcnt(2)\n"
"v_mfma_f32_16x16x32_bf16 v[172:175], v[140:143], v[156:159], v[172:175]\n"
"s_waitcnt lgkmcnt(1)\n"
"v_mfma_f32_16x16x32_bf16 v[140:143], v[144:147], v[28:31], 0\n"
"v_mfma_f32_16x16x32_bf16 v[148:151], v[144:147], v[32:35], 0\n"
"ds_read_b128 v[156:159], v12 offset:14336\n"
"v_mfma_f32_16x16x32_bf16 v[168:171], v[144:147], v[24:27], 0\n"
"v_cvt_pk_bf16_f32 v152, v152, v153\n"
"v_cvt_pk_bf16_f32 v153, v154, v155\n"
"v_cvt_pk_bf16_f32 v154, v164, v165\n"
"v_cvt_pk_bf16_f32 v155, v166, v167\n"
"s_waitcnt lgkmcnt(1)\n"
"v_mfma_f32_16x16x32_bf16 v[140:143], v[136:139], v[40:43], v[140:143]\n"
"s_mov_b32 m0, s54\n"
"v_cvt_pk_bf16_f32 v172, v172, v173\n"
"v_cvt_pk_bf16_f32 v173, v174, v175\n"
"v_mfma_f32_16x16x32_bf16 v[148:151], v[136:139], v[44:47], v[148:151]\n"
"ds_read_b128 v[144:147], v13 offset:14336\n"
"v_mfma_f32_16x16x32_bf16 v[168:171], v[136:139], v[36:39], v[168:171]\n"
"ds_write_addtid_b32 v254 offset:33280\n"
"s_waitcnt lgkmcnt(2)\n"
"v_mfma_f32_16x16x32_bf16 v[140:143], v[156:159], v[48:51], v[140:143]\n"
"ds_write_addtid_b32 v255 offset:33536\n"
"v_mfma_f32_16x16x32_bf16 v[148:151], v[156:159], v[52:55], v[148:151]\n"
"ds_write_addtid_b32 v160 offset:32256\n"
"v_mfma_f32_16x16x32_bf16 v[168:171], v[156:159], v[56:59], v[168:171]\n"
"ds_read_b128 v[136:139], v12 offset:16384\n"
"ds_write_addtid_b32 v161 offset:32512\n"
"s_waitcnt lgkmcnt(5)\n"
"v_mfma_f32_16x16x32_bf16 v[140:143], v[144:147], v[60:63], v[140:143]\n"
"ds_write_addtid_b32 v162 offset:32768\n"
"v_mfma_f32_16x16x32_bf16 v[148:151], v[144:147], v[64:67], v[148:151]\n"
"ds_write_addtid_b32 v163 offset:33024\n"
"s_mov_b32 m0, s68\n"
"ds_read_b128 v[156:159], v13 offset:16384\n"
"v_mfma_f32_16x16x32_bf16 v[168:171], v[144:147], v[68:71], v[168:171]\n"
"ds_write_addtid_b32 v152 offset:45056\n"
"ds_write_addtid_b32 v153 offset:45312\n"
"ds_write_addtid_b32 v154 offset:45568\n"
"s_waitcnt lgkmcnt(7)\n"
"v_mfma_f32_16x16x32_bf16 v[140:143], v[136:139], v[72:75], v[140:143]\n"
"ds_write_addtid_b32 v155 offset:45824\n"
"ds_write_addtid_b32 v172 offset:46080\n"
"v_mfma_f32_16x16x32_bf16 v[148:151], v[136:139], v[76:79], v[148:151]\n"
"ds_write_addtid_b32 v173 offset:46336\n"
"v_mov_b32 v144, 0x3f803f80\n"
"v_mfma_f32_16x16x32_bf16 v[168:171], v[136:139], v[80:83], v[168:171]\n"
"s_waitcnt lgkmcnt(6)\n"
"v_mov_b32 v145, 0x3f803f80\n"
"v_mov_b32 v146, 0x3f803f80\n"
"v_mfma_f32_16x16x32_bf16 v[140:143], v[156:159], v[84:87], v[140:143]\n"
"v_mov_b32 v147, 0x3f803f80\n"
"v_mfma_f32_16x16x32_bf16 v[148:151], v[156:159], v[88:91], v[148:151]\n"
"v_mfma_f32_16x16x32_bf16 v[168:171], v[156:159], v[92:95], v[168:171]\n"
"s_waitcnt vmcnt(5)\n"
"s_waitcnt lgkmcnt(0)\n"
"s_barrier\n"
"ds_read2st64_b32 v[136:137], v9 offset0:120 offset1:121\n"
"ds_read2st64_b32 v[138:139], v9 offset0:122 offset1:123\n"
"s_waitcnt lgkmcnt(0)\n"
"v_mfma_f32_16x16x32_bf16 v[152:155], v[144:147], v[136:139], 0\n"
"ds_read2st64_b32 v[156:157], v9 offset0:124 offset1:125\n"
"v_cvt_pk_bf16_f32 v202, v140, v141\n"
"v_mfma_f32_16x16x32_bf16 v[160:163], v[178:181], v[136:139], 0\n"
"ds_read2st64_b32 v[158:159], v9 offset0:126 offset1:127\n"
"v_cvt_pk_bf16_f32 v203, v142, v143\n"
"v_mfma_f32_16x16x32_bf16 v[140:143], v[204:207], v[136:139], 0\n"
"v_cvt_pk_bf16_f32 v228, v148, v149\n"
"v_cvt_pk_bf16_f32 v229, v150, v151\n"
"v_mfma_f32_16x16x32_bf16 v[148:151], v[230:233], v[136:139], 0\n"
"ds_read_u16 v16, v21 offset:1024\n"
"v_cvt_pk_bf16_f32 v254, v168, v169\n"
"v_cvt_pk_bf16_f32 v255, v170, v171\n"
"ds_read2st64_b32 v[136:137], v20 offset0:0 offset1:1\n"
"ds_read2st64_b32 v[138:139], v20 offset0:2 offset1:3\n"
"s_waitcnt lgkmcnt(3)\n"
"v_mfma_f32_16x16x32_bf16 v[152:155], v[144:147], v[156:159], v[152:155]\n"
"ds_read2st64_b32 v[164:165], v9 offset0:128 offset1:129\n"
"v_mfma_f32_16x16x32_bf16 v[160:163], v[188:191], v[156:159], v[160:163]\n"
"ds_read2st64_b32 v[166:167], v9 offset0:130 offset1:131\n"
"v_mfma_f32_16x16x32_bf16 v[140:143], v[214:217], v[156:159], v[140:143]\n"
"s_waitcnt lgkmcnt(4)\n"
"v_mad_u32_u24 v15, v16, s40, v18\n"
"v_mad_u32_u24 v16, v16, s40, v19\n"
"v_mfma_f32_16x16x32_bf16 v[148:151], v[240:243], v[156:159], v[148:151]\n"
"s_waitcnt lgkmcnt(2)\n"
"ds_read2st64_b32 v[240:241], v20 offset0:4 offset1:5\n"
"global_store_dwordx4 v15, v[136:139], s[66:67] sc1\n"
"s_waitcnt lgkmcnt(1)\n"
"v_mfma_f32_16x16x32_bf16 v[152:155], v[144:147], v[164:167], v[152:155]\n"
"ds_read_b32 v170, v9 offset:33792\n"
"v_mfma_f32_16x16x32_bf16 v[160:163], v[198:201], v[164:167], v[160:163]\n"
"ds_read_b32 v172, v9 offset:34304\n"
"v_mov_b32 v171, 0\n"
"v_mfma_f32_16x16x32_bf16 v[140:143], v[224:227], v[164:167], v[140:143]\n"
"ds_read_b32 v138, v9 offset:9728\n"
"v_mov_b32 v173, 0\n"
"v_mfma_f32_16x16x32_bf16 v[148:151], v[250:253], v[164:167], v[148:151]\n"
"s_waitcnt lgkmcnt(3)\n"
"global_store_dwordx2 v16, v[240:241], s[66:67] offset:256 sc1\n"
"v_mov_b32 v139, 0\n"
"v_mov_b32 v136, 0\n"
"v_mov_b32 v137, 0\n"
"v_mov_b32 v168, 0\n"
"v_mov_b32 v169, 0\n"
"s_waitcnt lgkmcnt(1)\n"
"v_mfma_f32_16x16x32_bf16 v[152:155], v[144:147], v[170:173], v[152:155]\n"
"s_waitcnt lgkmcnt(0)\n"
"v_mov_b32 v174, 0\n"
"v_mov_b32 v175, 0\n"
"v_mfma_f32_16x16x32_bf16 v[152:155], v[144:147], v[136:139], v[152:155]\n"
"v_mfma_f32_16x16x32_bf16 v[148:151], v[232:235], v[168:171], v[148:151]\n"
"v_mfma_f32_16x16x32_bf16 v[148:151], v[244:247], v[172:175], v[148:151]\n"
"v_mfma_f32_16x16x32_bf16 v[148:151], v[252:255], v[136:139], v[148:151]\n"
"v_mfma_f32_16x16x32_bf16 v[160:163], v[180:183], v[168:171], v[160:163]\n"
"v_mfma_f32_16x16x32_bf16 v[160:163], v[192:195], v[172:175], v[160:163]\n"
"v_mfma_f32_16x16x32_bf16 v[160:163], v[200:203], v[136:139], v[160:163]\n"
"ds_read2st64_b32 v[144:145], v14 offset0:126 offset1:127\n"
"v_mfma_f32_16x16x32_bf16 v[140:143], v[206:209], v[168:171], v[140:143]\n"
"ds_read2st64_b32 v[146:147], v14 offset0:128 offset1:129\n"
"v_rcp_f32 v16, v152\n"
"v_mfma_f32_16x16x32_bf16 v[140:143], v[218:221], v[172:175], v[140:143]\n"
"ds_read_b128 v[152:155], v17 offset:53248\n"
"v_mul_f32 v148, v148, v16\n"
"v_mfma_f32_16x16x32_bf16 v[140:143], v[226:229], v[136:139], v[140:143]\n"
"ds_read2st64_b32 v[136:137], v14 offset0:150 offset1:151\n"
"v_mul_f32 v149, v149, v16\n"
"v_cvt_pk_bf16_f32 v240, v148, v149\n"
"v_mul_f32 v150, v150, v16\n"
"v_mul_f32 v151, v151, v16\n"
"v_cvt_pk_bf16_f32 v241, v150, v151\n"
"v_mul_f32 v160, v160, v16\n"
"v_mul_f32 v161, v161, v16\n"
"v_cvt_pk_bf16_f32 v160, v160, v161\n"
"v_mul_f32 v162, v162, v16\n"
"v_mul_f32 v163, v163, v16\n"
"s_waitcnt lgkmcnt(2)\n"
"v_mfma_f32_16x16x32_bf16 v[148:151], v[96:99], v[144:147], 0\n"
"ds_read2st64_b32 v[138:139], v14 offset0:152 offset1:153\n"
"v_cvt_pk_bf16_f32 v161, v162, v163\n"
"v_mfma_f32_16x16x32_bf16 v[156:159], v[100:103], v[144:147], 0\n"
"s_waitcnt lgkmcnt(2)\n"
"ds_read_b128 v[164:167], v17 offset:54272\n"
"v_mul_f32 v140, v140, v16\n"
"v_mfma_f32_16x16x32_bf16 v[168:171], v[152:155], v[144:147], 0\n"
"ds_read2st64_b32 v[144:145], v14 offset0:174 offset1:175\n"
"v_mul_f32 v141, v141, v16\n"
"v_cvt_pk_bf16_f32 v162, v140, v141\n"
"v_mul_f32 v142, v142, v16\n"
"v_mul_f32 v143, v143, v16\n"
"v_cvt_pk_bf16_f32 v163, v142, v143\n"
"ds_read2st64_b32 v[146:147], v14 offset0:176 offset1:177\n"
"s_waitcnt lgkmcnt(3)\n"
"v_mfma_f32_16x16x32_bf16 v[148:151], v[104:107], v[136:139], v[148:151]\n"
"v_mfma_f32_16x16x32_bf16 v[156:159], v[108:111], v[136:139], v[156:159]\n"
"s_waitcnt lgkmcnt(2)\n"
"ds_read_b128 v[140:143], v17 offset:55296\n"
"v_mfma_f32_16x16x32_bf16 v[168:171], v[164:167], v[136:139], v[168:171]\n"
"s_waitcnt lgkmcnt(1)\n"
"ds_read2st64_b32 v[136:137], v14 offset0:198 offset1:199\n"
"ds_read2st64_b32 v[138:139], v14 offset0:200 offset1:201\n"
"v_mfma_f32_16x16x32_bf16 v[148:151], v[0:3], v[144:147], v[148:151]\n"
"v_mfma_f32_16x16x32_bf16 v[156:159], v[112:115], v[144:147], v[156:159]\n"
"s_waitcnt lgkmcnt(2)\n"
"ds_read_b128 v[152:155], v17 offset:56320\n"
"v_mfma_f32_16x16x32_bf16 v[168:171], v[140:143], v[144:147], v[168:171]\n"
"s_waitcnt lgkmcnt(1)\n"
"ds_read2st64_b32 v[140:141], v14 offset0:130 offset1:131\n"
"ds_read2st64_b32 v[142:143], v14 offset0:154 offset1:155\n"
"v_mfma_f32_16x16x32_bf16 v[148:151], v[116:119], v[136:139], v[148:151]\n"
"v_mfma_f32_16x16x32_bf16 v[156:159], v[120:123], v[136:139], v[156:159]\n"
"s_waitcnt lgkmcnt(2)\n"
"ds_read_b128 v[144:147], v17 offset:57344\n"
"v_mfma_f32_16x16x32_bf16 v[168:171], v[152:155], v[136:139], v[168:171]\n"
"s_waitcnt lgkmcnt(1)\n"
"ds_read2st64_b32 v[136:137], v14 offset0:178 offset1:179\n"
"ds_read2st64_b32 v[138:139], v14 offset0:202 offset1:203\n"
"v_mfma_f32_16x16x32_bf16 v[148:151], v[4:7], v[140:143], v[148:151]\n"
"v_mfma_f32_16x16x32_bf16 v[156:159], v[124:127], v[140:143], v[156:159]\n"
"s_waitcnt lgkmcnt(2)\n"
"ds_read_b128 v[152:155], v17 offset:58368\n"
"v_mfma_f32_16x16x32_bf16 v[168:171], v[144:147], v[140:143], v[168:171]\n"
"s_waitcnt lgkmcnt(1)\n"
"ds_read_b128 v[140:143], v12 offset:36864\n"
"v_mfma_f32_16x16x32_bf16 v[148:151], v[128:131], v[136:139], v[148:151]\n"
"v_mfma_f32_16x16x32_bf16 v[156:159], v[132:135], v[136:139], v[156:159]\n"
"s_barrier\n"
"s_waitcnt lgkmcnt(1)\n"
"v_mfma_f32_16x16x32_bf16 v[168:171], v[152:155], v[136:139], v[168:171]\n"
"s_waitcnt lgkmcnt(0)\n"
"ds_read_b128 v[136:139], v13 offset:36864\n"
"v_mfma_f32_16x16x32_bf16 v[144:147], v[140:143], v[28:31], 0\n"
"v_mfma_f32_16x16x32_bf16 v[152:155], v[140:143], v[32:35], 0\n"
"v_mfma_f32_16x16x32_bf16 v[164:167], v[140:143], v[24:27], 0\n"
"ds_read_b128 v[140:143], v12 offset:38912\n"
"v_cvt_pk_bf16_f32 v148, v148, v149\n"
"v_cvt_pk_bf16_f32 v149, v150, v151\n"
"v_cvt_pk_bf16_f32 v150, v156, v157\n"
"v_cvt_pk_bf16_f32 v168, v168, v169\n"
"v_cvt_pk_bf16_f32 v151, v158, v159\n"
"v_cvt_pk_bf16_f32 v169, v170, v171\n"
"s_waitcnt lgkmcnt(1)\n"
"v_mfma_f32_16x16x32_bf16 v[144:147], v[136:139], v[40:43], v[144:147]\n"
"s_mov_b32 m0, s54\n"
"v_mfma_f32_16x16x32_bf16 v[152:155], v[136:139], v[44:47], v[152:155]\n"
"ds_write_addtid_b32 v240 offset:33280\n"
"v_mfma_f32_16x16x32_bf16 v[164:167], v[136:139], v[36:39], v[164:167]\n"
"s_waitcnt lgkmcnt(1)\n"
"ds_read_b128 v[136:139], v13 offset:38912\n"
"v_mfma_f32_16x16x32_bf16 v[144:147], v[140:143], v[48:51], v[144:147]\n"
"ds_write_addtid_b32 v241 offset:33536\n"
"v_mfma_f32_16x16x32_bf16 v[152:155], v[140:143], v[52:55], v[152:155]\n"
"ds_write_addtid_b32 v160 offset:32256\n"
"v_mfma_f32_16x16x32_bf16 v[164:167], v[140:143], v[56:59], v[164:167]\n"
"ds_write_addtid_b32 v161 offset:32512\n"
"ds_read_b128 v[140:143], v12 offset:40960\n"
"ds_write_addtid_b32 v162 offset:32768\n"
"ds_write_addtid_b32 v163 offset:33024\n"
"s_waitcnt lgkmcnt(6)\n"
"v_mfma_f32_16x16x32_bf16 v[144:147], v[136:139], v[60:63], v[144:147]\n"
"s_mov_b32 m0, s68\n"
"v_mfma_f32_16x16x32_bf16 v[152:155], v[136:139], v[64:67], v[152:155]\n"
"ds_write_addtid_b32 v148 offset:45056\n"
"v_mfma_f32_16x16x32_bf16 v[164:167], v[136:139], v[68:71], v[164:167]\n"
"ds_read_b128 v[136:139], v13 offset:40960\n"
"ds_write_addtid_b32 v149 offset:45312\n"
"s_waitcnt lgkmcnt(5)\n"
"v_mfma_f32_16x16x32_bf16 v[144:147], v[140:143], v[72:75], v[144:147]\n"
"ds_write_addtid_b32 v150 offset:45568\n"
"v_mfma_f32_16x16x32_bf16 v[152:155], v[140:143], v[76:79], v[152:155]\n"
"ds_write_addtid_b32 v151 offset:45824\n"
"ds_write_addtid_b32 v168 offset:46080\n"
"v_mfma_f32_16x16x32_bf16 v[164:167], v[140:143], v[80:83], v[164:167]\n"
"ds_write_addtid_b32 v169 offset:46336\n"
"v_mov_b32 v140, 0x3f803f80\n"
"v_mov_b32 v141, 0x3f803f80\n"
"v_mov_b32 v142, 0x3f803f80\n"
"v_mov_b32 v143, 0x3f803f80\n"
"s_waitcnt lgkmcnt(5)\n"
"v_mfma_f32_16x16x32_bf16 v[144:147], v[136:139], v[84:87], v[144:147]\n"
"v_mfma_f32_16x16x32_bf16 v[152:155], v[136:139], v[88:91], v[152:155]\n"
"v_mfma_f32_16x16x32_bf16 v[164:167], v[136:139], v[92:95], v[164:167]\n"
"s_waitcnt vmcnt(2)\n"
"s_waitcnt lgkmcnt(0)\n"
"s_barrier\n"
"ds_read2st64_b32 v[136:137], v9 offset0:112 offset1:113\n"
"ds_read2st64_b32 v[138:139], v9 offset0:114 offset1:115\n"
"s_waitcnt lgkmcnt(0)\n"
"v_mfma_f32_16x16x32_bf16 v[148:151], v[140:143], v[136:139], 0\n"
"ds_read2st64_b32 v[156:157], v9 offset0:116 offset1:117\n"
"v_mfma_f32_16x16x32_bf16 v[160:163], v[182:185], v[136:139], 0\n"
"ds_read2st64_b32 v[158:159], v9 offset0:118 offset1:119\n"
"v_cvt_pk_bf16_f32 v178, v144, v145\n"
"v_mfma_f32_16x16x32_bf16 v[168:171], v[208:211], v[136:139], 0\n"
"ds_read_u16 v16, v21 offset:1056\n"
"v_cvt_pk_bf16_f32 v179, v146, v147\n"
"v_mfma_f32_16x16x32_bf16 v[144:147], v[234:237], v[136:139], 0\n"
"ds_read2st64_b32 v[136:137], v20 offset0:0 offset1:1\n"
"v_cvt_pk_bf16_f32 v204, v152, v153\n"
"v_cvt_pk_bf16_f32 v205, v154, v155\n"
"v_cvt_pk_bf16_f32 v230, v164, v165\n"
"v_cvt_pk_bf16_f32 v231, v166, v167\n"
"ds_read2st64_b32 v[138:139], v20 offset0:2 offset1:3\n"
"ds_read2st64_b32 v[152:153], v9 offset0:128 offset1:129\n"
"ds_read2st64_b32 v[154:155], v9 offset0:130 offset1:131\n"
"s_waitcnt lgkmcnt(5)\n"
"v_mfma_f32_16x16x32_bf16 v[148:151], v[140:143], v[156:159], v[148:151]\n"
"v_mfma_f32_16x16x32_bf16 v[160:163], v[190:193], v[156:159], v[160:163]\n"
"s_waitcnt lgkmcnt(4)\n"
"v_mad_u32_u24 v15, v16, s40, v18\n"
"v_mad_u32_u24 v16, v16, s40, v19\n"
"v_mfma_f32_16x16x32_bf16 v[168:171], v[216:219], v[156:159], v[168:171]\n"
"ds_read2st64_b32 v[240:241], v20 offset0:4 offset1:5\n"
"v_mfma_f32_16x16x32_bf16 v[144:147], v[242:245], v[156:159], v[144:147]\n"
"s_waitcnt lgkmcnt(1)\n"
"ds_read2st64_b32 v[156:157], v9 offset0:132 offset1:133\n"
"ds_read2st64_b32 v[158:159], v9 offset0:134 offset1:135\n"
"v_mfma_f32_16x16x32_bf16 v[148:151], v[140:143], v[152:155], v[148:151]\n"
"global_store_dwordx4 v15, v[136:139], s[66:67] sc1\n"
"s_nop 1\n"
"ds_read2st64_b32 v[138:139], v9 offset0:36 offset1:37\n"
"v_mfma_f32_16x16x32_bf16 v[160:163], v[178:181], v[152:155], v[160:163]\n"
"v_mov_b32 v136, 0\n"
"v_mov_b32 v137, 0\n"
"v_mfma_f32_16x16x32_bf16 v[168:171], v[204:207], v[152:155], v[168:171]\n"
"s_waitcnt lgkmcnt(3)\n"
"global_store_dwordx2 v16, v[240:241], s[66:67] offset:256 sc1\n"
"v_mfma_f32_16x16x32_bf16 v[144:147], v[230:233], v[152:155], v[144:147]\n"
"s_waitcnt lgkmcnt(1)\n"
"v_mfma_f32_16x16x32_bf16 v[148:151], v[140:143], v[156:159], v[148:151]\n"
"s_waitcnt lgkmcnt(0)\n"
"v_mfma_f32_16x16x32_bf16 v[148:151], v[140:143], v[136:139], v[148:151]\n"
"v_mfma_f32_16x16x32_bf16 v[144:147], v[252:255], v[156:159], v[144:147]\n"
"v_mfma_f32_16x16x32_bf16 v[144:147], v[244:247], v[136:139], v[144:147]\n"
"ds_read2st64_b32 v[140:141], v14 offset0:126 offset1:127\n"
"v_mfma_f32_16x16x32_bf16 v[160:163], v[200:203], v[156:159], v[160:163]\n"
"ds_read2st64_b32 v[142:143], v14 offset0:128 offset1:129\n"
"v_mfma_f32_16x16x32_bf16 v[160:163], v[192:195], v[136:139], v[160:163]\n"
"ds_read_b128 v[152:155], v17 offset:53248\n"
"v_mfma_f32_16x16x32_bf16 v[168:171], v[226:229], v[156:159], v[168:171]\n"
"ds_read2st64_b32 v[156:157], v14 offset0:150 offset1:151\n"
"v_rcp_f32 v16, v148\n"
"v_mfma_f32_16x16x32_bf16 v[168:171], v[218:221], v[136:139], v[168:171]\n"
"ds_read2st64_b32 v[158:159], v14 offset0:152 offset1:153\n"
"v_mul_f32 v144, v144, v16\n"
"v_mul_f32 v145, v145, v16\n"
"v_cvt_pk_bf16_f32 v240, v144, v145\n"
"v_mul_f32 v146, v146, v16\n"
"v_mul_f32 v147, v147, v16\n"
"v_cvt_pk_bf16_f32 v241, v146, v147\n"
"s_waitcnt lgkmcnt(3)\n"
"v_mfma_f32_16x16x32_bf16 v[136:139], v[96:99], v[140:143], 0\n"
"ds_read_b128 v[144:147], v17 offset:54272\n"
"v_mul_f32 v160, v160, v16\n"
"v_mfma_f32_16x16x32_bf16 v[148:151], v[100:103], v[140:143], 0\n"
"s_waitcnt lgkmcnt(3)\n"
"ds_read2st64_b32 v[164:165], v14 offset0:174 offset1:175\n"
"v_mul_f32 v161, v161, v16\n"
"v_mfma_f32_16x16x32_bf16 v[172:175], v[152:155], v[140:143], 0\n"
"s_waitcnt lgkmcnt(2)\n"
"ds_read2st64_b32 v[166:167], v14 offset0:176 offset1:177\n"
"v_cvt_pk_bf16_f32 v160, v160, v161\n"
"v_mul_f32 v162, v162, v16\n"
"v_mfma_f32_16x16x32_bf16 v[136:139], v[104:107], v[156:159], v[136:139]\n"
"ds_read_b128 v[140:143], v17 offset:55296\n"
"v_mul_f32 v163, v163, v16\n"
"v_mfma_f32_16x16x32_bf16 v[148:151], v[108:111], v[156:159], v[148:151]\n"
"s_waitcnt lgkmcnt(3)\n"
"ds_read2st64_b32 v[152:153], v14 offset0:198 offset1:199\n"
"v_cvt_pk_bf16_f32 v161, v162, v163\n"
"v_mul_f32 v168, v168, v16\n"
"v_mfma_f32_16x16x32_bf16 v[172:175], v[144:147], v[156:159], v[172:175]\n"
"ds_read2st64_b32 v[154:155], v14 offset0:200 offset1:201\n"
"v_mul_f32 v169, v169, v16\n"
"v_cvt_pk_bf16_f32 v162, v168, v169\n"
"v_mul_f32 v170, v170, v16\n"
"v_mul_f32 v171, v171, v16\n"
"v_cvt_pk_bf16_f32 v163, v170, v171\n"
"s_waitcnt lgkmcnt(3)\n"
"v_mfma_f32_16x16x32_bf16 v[136:139], v[0:3], v[164:167], v[136:139]\n"
"ds_read_b128 v[144:147], v17 offset:56320\n"
"v_mfma_f32_16x16x32_bf16 v[148:151], v[112:115], v[164:167], v[148:151]\n"
"s_waitcnt lgkmcnt(3)\n"
"ds_read2st64_b32 v[156:157], v14 offset0:130 offset1:131\n"
"v_mfma_f32_16x16x32_bf16 v[172:175], v[140:143], v[164:167], v[172:175]\n"
"s_waitcnt lgkmcnt(2)\n"
"ds_read2st64_b32 v[158:159], v14 offset0:154 offset1:155\n"
"v_mfma_f32_16x16x32_bf16 v[136:139], v[116:119], v[152:155], v[136:139]\n"
"ds_read_b128 v[140:143], v17 offset:57344\n"
"v_mfma_f32_16x16x32_bf16 v[148:151], v[120:123], v[152:155], v[148:151]\n"
"s_waitcnt lgkmcnt(3)\n"
"ds_read2st64_b32 v[164:165], v14 offset0:178 offset1:179\n"
"v_mfma_f32_16x16x32_bf16 v[172:175], v[144:147], v[152:155], v[172:175]\n"
"s_waitcnt lgkmcnt(2)\n"
"ds_read2st64_b32 v[166:167], v14 offset0:202 offset1:203\n"
"v_mfma_f32_16x16x32_bf16 v[136:139], v[4:7], v[156:159], v[136:139]\n"
"ds_read_b128 v[144:147], v17 offset:58368\n"
"v_mfma_f32_16x16x32_bf16 v[148:151], v[124:127], v[156:159], v[148:151]\n"
"s_waitcnt lgkmcnt(3)\n"
"ds_read_b128 v[152:155], v12 offset:24576\n"
"v_mfma_f32_16x16x32_bf16 v[172:175], v[140:143], v[156:159], v[172:175]\n"
"s_waitcnt lgkmcnt(2)\n"
"v_mfma_f32_16x16x32_bf16 v[136:139], v[128:131], v[164:167], v[136:139]\n"
"v_mfma_f32_16x16x32_bf16 v[148:151], v[132:135], v[164:167], v[148:151]\n"
"s_barrier\n"
"ds_read_b128 v[140:143], v13 offset:24576\n"
"s_waitcnt lgkmcnt(2)\n"
"v_mfma_f32_16x16x32_bf16 v[172:175], v[144:147], v[164:167], v[172:175]\n"
"s_waitcnt lgkmcnt(1)\n"
"v_mfma_f32_16x16x32_bf16 v[144:147], v[152:155], v[28:31], 0\n"
"v_mfma_f32_16x16x32_bf16 v[28:31], v[152:155], v[32:35], 0\n"
"ds_read_b128 v[32:35], v12 offset:26624\n"
"v_mfma_f32_16x16x32_bf16 v[156:159], v[152:155], v[24:27], 0\n"
"v_cvt_pk_bf16_f32 v136, v136, v137\n"
"v_cvt_pk_bf16_f32 v137, v138, v139\n"
"v_cvt_pk_bf16_f32 v138, v148, v149\n"
"v_cvt_pk_bf16_f32 v139, v150, v151\n"
"s_waitcnt lgkmcnt(1)\n"
"v_mfma_f32_16x16x32_bf16 v[144:147], v[140:143], v[40:43], v[144:147]\n"
"s_mov_b32 m0, s54\n"
"v_cvt_pk_bf16_f32 v172, v172, v173\n"
"v_cvt_pk_bf16_f32 v173, v174, v175\n"
"v_mfma_f32_16x16x32_bf16 v[28:31], v[140:143], v[44:47], v[28:31]\n"
"ds_read_b128 v[24:27], v13 offset:26624\n"
"v_mfma_f32_16x16x32_bf16 v[156:159], v[140:143], v[36:39], v[156:159]\n"
"ds_write_addtid_b32 v240 offset:33280\n"
"s_waitcnt lgkmcnt(2)\n"
"v_mfma_f32_16x16x32_bf16 v[144:147], v[32:35], v[48:51], v[144:147]\n"
"ds_write_addtid_b32 v241 offset:33536\n"
"v_mfma_f32_16x16x32_bf16 v[28:31], v[32:35], v[52:55], v[28:31]\n"
"ds_write_addtid_b32 v160 offset:32256\n"
"v_mfma_f32_16x16x32_bf16 v[156:159], v[32:35], v[56:59], v[156:159]\n"
"ds_read_b128 v[32:35], v12 offset:28672\n"
"ds_write_addtid_b32 v161 offset:32512\n"
"s_waitcnt lgkmcnt(5)\n"
"v_mfma_f32_16x16x32_bf16 v[144:147], v[24:27], v[60:63], v[144:147]\n"
"ds_write_addtid_b32 v162 offset:32768\n"
"v_mfma_f32_16x16x32_bf16 v[28:31], v[24:27], v[64:67], v[28:31]\n"
"ds_write_addtid_b32 v163 offset:33024\n"
"s_mov_b32 m0, s68\n"
"ds_read_b128 v[36:39], v13 offset:28672\n"
"v_mfma_f32_16x16x32_bf16 v[156:159], v[24:27], v[68:71], v[156:159]\n"
"ds_write_addtid_b32 v136 offset:45056\n"
"ds_write_addtid_b32 v137 offset:45312\n"
"ds_write_addtid_b32 v138 offset:45568\n"
"s_waitcnt lgkmcnt(7)\n"
"v_mfma_f32_16x16x32_bf16 v[144:147], v[32:35], v[72:75], v[144:147]\n"
"ds_write_addtid_b32 v139 offset:45824\n"
"ds_write_addtid_b32 v172 offset:46080\n"
"v_mfma_f32_16x16x32_bf16 v[28:31], v[32:35], v[76:79], v[28:31]\n"
"ds_write_addtid_b32 v173 offset:46336\n"
"v_mov_b32 v24, 0x3f803f80\n"
"v_mfma_f32_16x16x32_bf16 v[156:159], v[32:35], v[80:83], v[156:159]\n"
"s_waitcnt lgkmcnt(6)\n"
"v_mov_b32 v25, 0x3f803f80\n"
"v_mov_b32 v26, 0x3f803f80\n"
"v_mfma_f32_16x16x32_bf16 v[144:147], v[36:39], v[84:87], v[144:147]\n"
"v_mov_b32 v27, 0x3f803f80\n"
"v_mov_b32 v32, 0\n"
"v_mfma_f32_16x16x32_bf16 v[28:31], v[36:39], v[88:91], v[28:31]\n"
"v_mfma_f32_16x16x32_bf16 v[156:159], v[36:39], v[92:95], v[156:159]\n"
"s_waitcnt lgkmcnt(0)\n"
"s_barrier\n"
"ds_read_b32 v33, v9 offset:30976\n"
"ds_read2st64_b32 v[34:35], v9 offset0:122 offset1:123\n"
"ds_read2st64_b32 v[36:37], v9 offset0:128 offset1:129\n"
"v_cvt_pk_bf16_f32 v180, v144, v145\n"
"v_cvt_pk_bf16_f32 v181, v146, v147\n"
"ds_read2st64_b32 v[38:39], v9 offset0:130 offset1:131\n"
"v_cvt_pk_bf16_f32 v206, v28, v29\n"
"v_cvt_pk_bf16_f32 v207, v30, v31\n"
"v_cvt_pk_bf16_f32 v232, v156, v157\n"
"v_cvt_pk_bf16_f32 v233, v158, v159\n"
"s_waitcnt lgkmcnt(2)\n"
"v_mfma_f32_16x16x32_bf16 v[28:31], v[24:27], v[32:35], 0\n"
"v_mfma_f32_16x16x32_bf16 v[40:43], v[182:185], v[32:35], 0\n"
"v_mfma_f32_16x16x32_bf16 v[44:47], v[208:211], v[32:35], 0\n"
"ds_read_u16 v16, v21 offset:1088\n"
"v_mfma_f32_16x16x32_bf16 v[48:51], v[234:237], v[32:35], 0\n"
"s_waitcnt lgkmcnt(1)\n"
"ds_read2st64_b32 v[32:33], v20 offset0:0 offset1:1\n"
"ds_read2st64_b32 v[34:35], v20 offset0:2 offset1:3\n"
"v_mfma_f32_16x16x32_bf16 v[28:31], v[24:27], v[36:39], v[28:31]\n"
"ds_read_b32 v53, v9 offset:34048\n"
"v_mov_b32 v52, 0\n"
"v_mfma_f32_16x16x32_bf16 v[40:43], v[178:181], v[36:39], v[40:43]\n"
"ds_read2st64_b32 v[54:55], v9 offset0:134 offset1:135\n"
"v_mfma_f32_16x16x32_bf16 v[44:47], v[204:207], v[36:39], v[44:47]\n"
"ds_read2st64_b32 v[240:241], v20 offset0:4 offset1:5\n"
"v_mfma_f32_16x16x32_bf16 v[48:51], v[230:233], v[36:39], v[48:51]\n"
"s_waitcnt lgkmcnt(5)\n"
"ds_read2st64_b32 v[58:59], v9 offset0:124 offset1:125\n"
"v_mad_u32_u24 v15, v16, s40, v18\n"
"v_mad_u32_u24 v16, v16, s40, v19\n"
"s_waitcnt lgkmcnt(4)\n"
"global_store_dwordx4 v15, v[32:35], s[66:67] sc1\n"
"ds_read2st64_b32 v[60:61], v9 offset0:126 offset1:127\n"
"s_waitcnt lgkmcnt(3)\n"
"v_mfma_f32_16x16x32_bf16 v[28:31], v[24:27], v[52:55], v[28:31]\n"
"ds_read_b32 v35, v9 offset:9984\n"
"v_mfma_f32_16x16x32_bf16 v[40:43], v[192:195], v[52:55], v[40:43]\n"
"s_waitcnt lgkmcnt(3)\n"
"global_store_dwordx2 v16, v[240:241], s[66:67] offset:256 sc1\n"
"v_mfma_f32_16x16x32_bf16 v[44:47], v[218:221], v[52:55], v[44:47]\n"
"v_mov_b32 v34, 0\n"
"v_mov_b32 v32, 0\n"
"v_mfma_f32_16x16x32_bf16 v[48:51], v[244:247], v[52:55], v[48:51]\n"
"s_waitcnt lgkmcnt(1)\n"
"v_mov_b32 v33, 0\n"
"v_mov_b32 v56, 0\n"
"v_mov_b32 v57, 0\n"
"v_mfma_f32_16x16x32_bf16 v[28:31], v[24:27], v[58:61], v[28:31]\n"
"s_waitcnt lgkmcnt(0)\n"
"v_mov_b32 v62, 0\n"
"v_mov_b32 v63, 0\n"
"v_mfma_f32_16x16x32_bf16 v[28:31], v[24:27], v[32:35], v[28:31]\n"
"v_mfma_f32_16x16x32_bf16 v[48:51], v[236:239], v[56:59], v[48:51]\n"
"v_mfma_f32_16x16x32_bf16 v[48:51], v[248:251], v[60:63], v[48:51]\n"
"v_mfma_f32_16x16x32_bf16 v[48:51], v[252:255], v[32:35], v[48:51]\n"
"v_mfma_f32_16x16x32_bf16 v[40:43], v[184:187], v[56:59], v[40:43]\n"
"v_mfma_f32_16x16x32_bf16 v[40:43], v[196:199], v[60:63], v[40:43]\n"
"ds_read2st64_b32 v[182:183], v14 offset0:126 offset1:127\n"
"v_mfma_f32_16x16x32_bf16 v[40:43], v[200:203], v[32:35], v[40:43]\n"
"ds_read2st64_b32 v[184:185], v14 offset0:128 offset1:129\n"
"v_rcp_f32 v16, v28\n"
"v_mfma_f32_16x16x32_bf16 v[44:47], v[210:213], v[56:59], v[44:47]\n"
"ds_read_b128 v[186:189], v17 offset:53248\n"
"v_mul_f32 v48, v48, v16\n"
"v_mfma_f32_16x16x32_bf16 v[44:47], v[222:225], v[60:63], v[44:47]\n"
"ds_read2st64_b32 v[196:197], v14 offset0:150 offset1:151\n"
"v_mul_f32 v49, v49, v16\n"
"v_mfma_f32_16x16x32_bf16 v[44:47], v[226:229], v[32:35], v[44:47]\n"
"ds_read2st64_b32 v[198:199], v14 offset0:152 offset1:153\n"
"v_cvt_pk_bf16_f32 v240, v48, v49\n"
"v_mul_f32 v50, v50, v16\n"
"v_mul_f32 v51, v51, v16\n"
"v_cvt_pk_bf16_f32 v241, v50, v51\n"
"v_mul_f32 v40, v40, v16\n"
"v_mul_f32 v41, v41, v16\n"
"s_waitcnt lgkmcnt(3)\n"
"v_mfma_f32_16x16x32_bf16 v[208:211], v[96:99], v[182:185], 0\n"
"ds_read_b128 v[212:215], v17 offset:54272\n"
"v_cvt_pk_bf16_f32 v40, v40, v41\n"
"v_mfma_f32_16x16x32_bf16 v[222:225], v[100:103], v[182:185], 0\n"
"s_waitcnt lgkmcnt(3)\n"
"ds_read2st64_b32 v[234:235], v14 offset0:174 offset1:175\n"
"v_mul_f32 v42, v42, v16\n"
"v_mfma_f32_16x16x32_bf16 v[248:251], v[186:189], v[182:185], 0\n"
"s_waitcnt lgkmcnt(2)\n"
"ds_read2st64_b32 v[236:237], v14 offset0:176 offset1:177\n"
"v_mul_f32 v43, v43, v16\n"
"v_cvt_pk_bf16_f32 v41, v42, v43\n"
"v_mfma_f32_16x16x32_bf16 v[208:211], v[104:107], v[196:199], v[208:211]\n"
"ds_read_b128 v[182:185], v17 offset:55296\n"
"v_mul_f32 v44, v44, v16\n"
"v_mfma_f32_16x16x32_bf16 v[222:225], v[108:111], v[196:199], v[222:225]\n"
"s_waitcnt lgkmcnt(3)\n"
"ds_read2st64_b32 v[186:187], v14 offset0:198 offset1:199\n"
"v_mul_f32 v45, v45, v16\n"
"v_cvt_pk_bf16_f32 v42, v44, v45\n"
"v_mfma_f32_16x16x32_bf16 v[248:251], v[212:215], v[196:199], v[248:251]\n"
"ds_read2st64_b32 v[188:189], v14 offset0:200 offset1:201\n"
"v_mul_f32 v46, v46, v16\n"
"v_mul_f32 v47, v47, v16\n"
"v_cvt_pk_bf16_f32 v43, v46, v47\n"
"s_waitcnt lgkmcnt(3)\n"
"v_mfma_f32_16x16x32_bf16 v[208:211], v[0:3], v[234:237], v[208:211]\n"
"s_mov_b32 m0, s54\n"
"ds_read_b128 v[196:199], v17 offset:56320\n"
"v_mfma_f32_16x16x32_bf16 v[222:225], v[112:115], v[234:237], v[222:225]\n"
"s_waitcnt lgkmcnt(3)\n"
"ds_read2st64_b32 v[212:213], v14 offset0:130 offset1:131\n"
"v_mfma_f32_16x16x32_bf16 v[248:251], v[182:185], v[234:237], v[248:251]\n"
"s_waitcnt lgkmcnt(2)\n"
"ds_read2st64_b32 v[214:215], v14 offset0:154 offset1:155\n"
"ds_read2st64_b32 v[182:183], v14 offset0:178 offset1:179\n"
"v_mfma_f32_16x16x32_bf16 v[208:211], v[116:119], v[186:189], v[208:211]\n"
"ds_read2st64_b32 v[184:185], v14 offset0:202 offset1:203\n"
"v_mfma_f32_16x16x32_bf16 v[222:225], v[120:123], v[186:189], v[222:225]\n"
"ds_write_addtid_b32 v240 offset:7168\n"
"ds_write_addtid_b32 v241 offset:7424\n"
"s_waitcnt lgkmcnt(6)\n"
"v_mfma_f32_16x16x32_bf16 v[248:251], v[196:199], v[186:189], v[248:251]\n"
"ds_write_addtid_b32 v40 offset:6144\n"
"ds_write_addtid_b32 v41 offset:6400\n"
"ds_write_addtid_b32 v42 offset:6656\n"
"s_waitcnt lgkmcnt(7)\n"
"v_mfma_f32_16x16x32_bf16 v[208:211], v[4:7], v[212:215], v[208:211]\n"
"ds_write_addtid_b32 v43 offset:6912\n"
"ds_read_b128 v[186:189], v17 offset:57344\n"
"v_mfma_f32_16x16x32_bf16 v[222:225], v[124:127], v[212:215], v[222:225]\n"
"ds_read_b128 v[196:199], v17 offset:58368\n"
"v_mov_b32 v234, 0x3f803f80\n"
"v_mov_b32 v235, 0x3f803f80\n"
"v_mov_b32 v236, 0x3f803f80\n"
"v_mov_b32 v237, 0x3f803f80\n"
"s_waitcnt lgkmcnt(2)\n"
"s_barrier\n"
"ds_read2st64_b32 v[238:239], v9 offset0:112 offset1:113\n"
"ds_read2st64_b32 v[240:241], v9 offset0:114 offset1:115\n"
"s_waitcnt lgkmcnt(3)\n"
"v_mfma_f32_16x16x32_bf16 v[248:251], v[186:189], v[212:215], v[248:251]\n"
"v_mfma_f32_16x16x32_bf16 v[208:211], v[128:131], v[182:185], v[208:211]\n"
"v_mfma_f32_16x16x32_bf16 v[222:225], v[132:135], v[182:185], v[222:225]\n"
"s_waitcnt lgkmcnt(2)\n"
"v_subrev_u32 v16, 24576, v9\n"
"v_mfma_f32_16x16x32_bf16 v[248:251], v[196:199], v[182:185], v[248:251]\n"
"s_waitcnt lgkmcnt(0)\n"
"ds_read2st64_b32 v[182:183], v9 offset0:116 offset1:117\n"
"ds_read2st64_b32 v[184:185], v9 offset0:118 offset1:119\n"
"v_mfma_f32_16x16x32_bf16 v[186:189], v[234:237], v[238:241], 0\n"
"ds_read2st64_b32 v[26:27], v16 offset0:16 offset1:17\n"
"v_mfma_f32_16x16x32_bf16 v[196:199], v[190:193], v[238:241], 0\n"
"ds_read2st64_b32 v[28:29], v16 offset0:18 offset1:19\n"
"v_cvt_pk_bf16_f32 v208, v208, v209\n"
"v_mfma_f32_16x16x32_bf16 v[212:215], v[216:219], v[238:241], 0\n"
"v_cvt_pk_bf16_f32 v209, v210, v211\n"
"v_cvt_pk_bf16_f32 v210, v222, v223\n"
"v_mfma_f32_16x16x32_bf16 v[8:11], v[242:245], v[238:241], 0\n"
"v_mov_b32 v24, 0\n"
"v_mov_b32 v25, 0\n"
"v_cvt_pk_bf16_f32 v211, v224, v225\n"
"v_cvt_pk_bf16_f32 v248, v248, v249\n"
"v_cvt_pk_bf16_f32 v249, v250, v251\n"
"s_waitcnt lgkmcnt(2)\n"
"v_mfma_f32_16x16x32_bf16 v[186:189], v[234:237], v[182:185], v[186:189]\n"
"s_waitcnt lgkmcnt(0)\n"
"v_mov_b32 v30, 0\n"
"v_mov_b32 v31, 0\n"
"v_mfma_f32_16x16x32_bf16 v[186:189], v[234:237], v[26:29], v[186:189]\n"
"v_mfma_f32_16x16x32_bf16 v[8:11], v[252:255], v[182:185], v[8:11]\n"
"v_mfma_f32_16x16x32_bf16 v[8:11], v[244:247], v[24:27], v[8:11]\n"
"v_mfma_f32_16x16x32_bf16 v[8:11], v[230:233], v[28:31], v[8:11]\n"
"s_mov_b32 m0, s54\n"
"v_mfma_f32_16x16x32_bf16 v[196:199], v[200:203], v[182:185], v[196:199]\n"
"v_mfma_f32_16x16x32_bf16 v[196:199], v[192:195], v[24:27], v[196:199]\n"
"s_nop 1\n"
"ds_read2st64_b32 v[188:189], v14 offset0:24 offset1:25\n"
"v_mfma_f32_16x16x32_bf16 v[196:199], v[178:181], v[28:31], v[196:199]\n"
"ds_read2st64_b32 v[190:191], v14 offset0:26 offset1:27\n"
"v_rcp_f32 v16, v186\n"
"v_mfma_f32_16x16x32_bf16 v[212:215], v[226:229], v[182:185], v[212:215]\n"
"ds_read_b128 v[178:181], v17 offset:53248\n"
"v_mul_f32 v8, v8, v16\n"
"v_mul_f32 v9, v9, v16\n"
"v_mul_f32 v10, v10, v16\n"
"v_mul_f32 v11, v11, v16\n"
"v_mul_f32 v196, v196, v16\n"
"v_mul_f32 v197, v197, v16\n"
"v_cvt_pk_bf16_f32 v196, v196, v197\n"
"v_mul_f32 v198, v198, v16\n"
"v_mul_f32 v199, v199, v16\n"
"v_cvt_pk_bf16_f32 v197, v198, v199\n"
"v_cvt_pk_bf16_f32 v250, v8, v9\n"
"v_cvt_pk_bf16_f32 v251, v10, v11\n"
"v_mfma_f32_16x16x32_bf16 v[212:215], v[218:221], v[24:27], v[212:215]\n"
"ds_write_addtid_b32 v250 offset:33280\n"
"ds_write_addtid_b32 v251 offset:33536\n"
"v_mfma_f32_16x16x32_bf16 v[212:215], v[204:207], v[28:31], v[212:215]\n"
"ds_write_addtid_b32 v196 offset:32256\n"
"ds_write_addtid_b32 v197 offset:32512\n"
"s_waitcnt lgkmcnt(5)\n"
"v_mfma_f32_16x16x32_bf16 v[182:185], v[96:99], v[188:191], 0\n"
"ds_read2st64_b32 v[192:193], v14 offset0:48 offset1:49\n"
"ds_read2st64_b32 v[194:195], v14 offset0:50 offset1:51\n"
"v_mfma_f32_16x16x32_bf16 v[200:203], v[100:103], v[188:191], 0\n"
"s_waitcnt lgkmcnt(6)\n"
"ds_read_b128 v[204:207], v17 offset:54272\n"
"v_mfma_f32_16x16x32_bf16 v[216:219], v[178:181], v[188:191], 0\n"
"ds_read2st64_b32 v[178:179], v14 offset0:72 offset1:73\n"
"v_mul_f32 v212, v212, v16\n"
"v_mul_f32 v213, v213, v16\n"
"v_mul_f32 v214, v214, v16\n"
"v_mul_f32 v215, v215, v16\n"
"v_cvt_pk_bf16_f32 v198, v212, v213\n"
"v_cvt_pk_bf16_f32 v199, v214, v215\n"
"ds_write_addtid_b32 v198 offset:32768\n"
"ds_write_addtid_b32 v199 offset:33024\n"
"s_mov_b32 m0, s68\n"
"ds_read2st64_b32 v[180:181], v14 offset0:74 offset1:75\n"
"s_waitcnt lgkmcnt(5)\n"
"v_mfma_f32_16x16x32_bf16 v[182:185], v[104:107], v[192:195], v[182:185]\n"
"ds_write_addtid_b32 v208 offset:45056\n"
"ds_write_addtid_b32 v209 offset:45312\n"
"v_mfma_f32_16x16x32_bf16 v[200:203], v[108:111], v[192:195], v[200:203]\n"
"ds_write_addtid_b32 v210 offset:45568\n"
"ds_write_addtid_b32 v211 offset:45824\n"
"s_waitcnt lgkmcnt(8)\n"
"v_mfma_f32_16x16x32_bf16 v[216:219], v[204:207], v[192:195], v[216:219]\n"
"ds_write_addtid_b32 v248 offset:46080\n"
"ds_write_addtid_b32 v249 offset:46336\n"
"ds_read_b128 v[186:189], v17 offset:55296\n"
"ds_read2st64_b32 v[190:191], v14 offset0:96 offset1:97\n"
"s_waitcnt lgkmcnt(8)\n"
"v_mfma_f32_16x16x32_bf16 v[182:185], v[0:3], v[178:181], v[182:185]\n"
"ds_read2st64_b32 v[192:193], v14 offset0:98 offset1:99\n"
"v_mfma_f32_16x16x32_bf16 v[200:203], v[112:115], v[178:181], v[200:203]\n"
"s_waitcnt lgkmcnt(2)\n"
"ds_read_b128 v[194:197], v17 offset:56320\n"
"ds_read2st64_b32 v[204:205], v14 offset0:28 offset1:29\n"
"ds_read2st64_b32 v[206:207], v14 offset0:52 offset1:53\n"
"v_mfma_f32_16x16x32_bf16 v[216:219], v[186:189], v[178:181], v[216:219]\n"
"s_waitcnt lgkmcnt(3)\n"
"ds_read2st64_b32 v[178:179], v14 offset0:76 offset1:77\n"
"ds_read2st64_b32 v[180:181], v14 offset0:100 offset1:101\n"
"v_mfma_f32_16x16x32_bf16 v[182:185], v[116:119], v[190:193], v[182:185]\n"
"ds_read_b128 v[186:189], v17 offset:57344\n"
"v_mfma_f32_16x16x32_bf16 v[200:203], v[120:123], v[190:193], v[200:203]\n"
"s_waitcnt lgkmcnt(5)\n"
"v_mfma_f32_16x16x32_bf16 v[216:219], v[194:197], v[190:193], v[216:219]\n"
"s_waitcnt lgkmcnt(3)\n"
"v_mfma_f32_16x16x32_bf16 v[182:185], v[4:7], v[204:207], v[182:185]\n"
"ds_read_b128 v[190:193], v17 offset:58368\n"
"v_mfma_f32_16x16x32_bf16 v[200:203], v[124:127], v[204:207], v[200:203]\n"
"s_waitcnt lgkmcnt(2)\n"
"s_barrier\n"
"ds_read2st64_b32 v[194:195], v14 offset0:126 offset1:127\n"
"s_waitcnt lgkmcnt(2)\n"
"v_mfma_f32_16x16x32_bf16 v[216:219], v[186:189], v[204:207], v[216:219]\n"
"ds_read2st64_b32 v[196:197], v14 offset0:128 offset1:129\n"
"v_mfma_f32_16x16x32_bf16 v[182:185], v[128:131], v[178:181], v[182:185]\n"
"ds_read_b128 v[186:189], v17 offset:53248\n"
"v_mfma_f32_16x16x32_bf16 v[200:203], v[132:135], v[178:181], v[200:203]\n"
"s_waitcnt lgkmcnt(3)\n"
"ds_read2st64_b32 v[204:205], v14 offset0:150 offset1:151\n"
"v_mfma_f32_16x16x32_bf16 v[216:219], v[190:193], v[178:181], v[216:219]\n"
"s_waitcnt lgkmcnt(2)\n"
"ds_read2st64_b32 v[206:207], v14 offset0:152 offset1:153\n"
"v_mfma_f32_16x16x32_bf16 v[178:181], v[96:99], v[194:197], 0\n"
"ds_read_b128 v[190:193], v17 offset:54272\n"
"v_mfma_f32_16x16x32_bf16 v[208:211], v[100:103], v[194:197], 0\n"
"s_waitcnt lgkmcnt(3)\n"
"ds_read2st64_b32 v[212:213], v14 offset0:174 offset1:175\n"
"v_cvt_pk_bf16_f32 v182, v182, v183\n"
"v_mfma_f32_16x16x32_bf16 v[220:223], v[186:189], v[194:197], 0\n"
"s_waitcnt lgkmcnt(2)\n"
"ds_read2st64_b32 v[214:215], v14 offset0:176 offset1:177\n"
"v_cvt_pk_bf16_f32 v183, v184, v185\n"
"v_mfma_f32_16x16x32_bf16 v[178:181], v[104:107], v[204:207], v[178:181]\n"
"ds_read_u16 v16, v21 offset:1120\n"
"v_cvt_pk_bf16_f32 v184, v200, v201\n"
"v_mfma_f32_16x16x32_bf16 v[208:211], v[108:111], v[204:207], v[208:211]\n"
"ds_read2st64_b32 v[186:187], v20 offset0:0 offset1:1\n"
"v_cvt_pk_bf16_f32 v185, v202, v203\n"
"v_cvt_pk_bf16_f32 v216, v216, v217\n"
"v_cvt_pk_bf16_f32 v217, v218, v219\n"
"s_waitcnt lgkmcnt(4)\n"
"v_mfma_f32_16x16x32_bf16 v[220:223], v[190:193], v[204:207], v[220:223]\n"
"ds_read2st64_b32 v[188:189], v20 offset0:2 offset1:3\n"
"ds_read_b128 v[190:193], v17 offset:55296\n"
"ds_read2st64_b32 v[194:195], v14 offset0:198 offset1:199\n"
"ds_read2st64_b32 v[196:197], v14 offset0:200 offset1:201\n"
"s_waitcnt lgkmcnt(6)\n"
"v_mfma_f32_16x16x32_bf16 v[178:181], v[0:3], v[212:215], v[178:181]\n"
"s_waitcnt lgkmcnt(5)\n"
"ds_read2st64_b32 v[218:219], v20 offset0:4 offset1:5\n"
"v_mad_u32_u24 v21, v16, s40, v18\n"
"v_mfma_f32_16x16x32_bf16 v[208:211], v[112:115], v[212:215], v[208:211]\n"
"ds_read_b128 v[198:201], v17 offset:56320\n"
"v_mad_u32_u24 v16, v16, s40, v19\n"
"ds_read2st64_b32 v[202:203], v14 offset0:130 offset1:131\n"
"ds_read2st64_b32 v[204:205], v14 offset0:154 offset1:155\n"
"s_waitcnt lgkmcnt(6)\n"
"v_mfma_f32_16x16x32_bf16 v[220:223], v[190:193], v[212:215], v[220:223]\n"
"s_mov_b32 m0, s68\n"
"global_store_dwordx4 v21, v[186:189], s[66:67] sc1\n"
"s_waitcnt lgkmcnt(4)\n"
"v_mfma_f32_16x16x32_bf16 v[178:181], v[116:119], v[194:197], v[178:181]\n"
"ds_read_b128 v[186:189], v17 offset:57344\n"
"v_mfma_f32_16x16x32_bf16 v[208:211], v[120:123], v[194:197], v[208:211]\n"
"s_waitcnt lgkmcnt(3)\n"
"ds_read2st64_b32 v[190:191], v14 offset0:178 offset1:179\n"
"v_mfma_f32_16x16x32_bf16 v[220:223], v[198:201], v[194:197], v[220:223]\n"
"s_waitcnt lgkmcnt(2)\n"
"ds_read2st64_b32 v[192:193], v14 offset0:202 offset1:203\n"
"v_mfma_f32_16x16x32_bf16 v[178:181], v[4:7], v[202:205], v[178:181]\n"
"global_store_dwordx2 v16, v[218:219], s[66:67] offset:256 sc1\n"
"ds_write_addtid_b32 v182 offset:51456\n"
"v_mfma_f32_16x16x32_bf16 v[208:211], v[124:127], v[202:205], v[208:211]\n"
"ds_write_addtid_b32 v183 offset:51712\n"
"ds_read_b128 v[194:197], v17 offset:58368\n"
"s_waitcnt lgkmcnt(5)\n"
"v_mfma_f32_16x16x32_bf16 v[220:223], v[186:189], v[202:205], v[220:223]\n"
"ds_write_addtid_b32 v184 offset:51968\n"
"ds_write_addtid_b32 v185 offset:52224\n"
"ds_write_addtid_b32 v216 offset:52480\n"
"s_waitcnt lgkmcnt(6)\n"
"v_mfma_f32_16x16x32_bf16 v[178:181], v[128:131], v[190:193], v[178:181]\n"
"ds_write_addtid_b32 v217 offset:52736\n"
"v_mfma_f32_16x16x32_bf16 v[208:211], v[132:135], v[190:193], v[208:211]\n"
"s_waitcnt lgkmcnt(4)\n"
"v_mfma_f32_16x16x32_bf16 v[220:223], v[194:197], v[190:193], v[220:223]\n"
"s_nop 3\n"
"v_cvt_pk_bf16_f32 v178, v178, v179\n"
"ds_write_addtid_b32 v178 offset:57856\n"
"v_cvt_pk_bf16_f32 v179, v180, v181\n"
"ds_write_addtid_b32 v179 offset:58112\n"
"v_cvt_pk_bf16_f32 v180, v208, v209\n"
"ds_write_addtid_b32 v180 offset:58368\n"
"v_cvt_pk_bf16_f32 v181, v210, v211\n"
"ds_write_addtid_b32 v181 offset:58624\n"
"v_cvt_pk_bf16_f32 v220, v220, v221\n"
"v_cvt_pk_bf16_f32 v221, v222, v223\n"
"ds_write_addtid_b32 v220 offset:58880\n"
"ds_write_addtid_b32 v221 offset:59136\n"
"s_waitcnt lgkmcnt(0)\n"
"s_barrier\n"
"ds_read2st64_b32 v[178:179], v20 offset0:25 offset1:26\n"
"ds_read2st64_b32 v[180:181], v20 offset0:27 offset1:28\n"
"ds_read2st64_b32 v[182:183], v20 offset0:29 offset1:30\n"
"ds_read2st64_b32 v[184:185], v20 offset0:50 offset1:51\n"
"ds_read2st64_b32 v[186:187], v20 offset0:52 offset1:53\n"
"ds_read2st64_b32 v[254:255], v20 offset0:54 offset1:55\n"
"s_waitcnt lgkmcnt(4)\n"
"global_store_dwordx4 v22, v[178:181], s[66:67] sc1\n"
"s_waitcnt lgkmcnt(3)\n"
"global_store_dwordx2 v23, v[182:183], s[66:67] offset:256 sc1\n"
"s_waitcnt lgkmcnt(1)\n"
"global_store_dwordx4 v176, v[184:187], s[66:67] sc1\n"
"s_waitcnt lgkmcnt(0)\n"
"global_store_dwordx2 v177, v[254:255], s[66:67] offset:256 sc1\n"
    );
  }
}

extern "C" void kernel_launch(void* const* d_in, const int* in_sizes, int n_in,
                              void* d_out, int out_size, void* d_ws, size_t ws_size,
                              hipStream_t stream) {
  dinat_fused<<<256, 512, 0, stream>>>((const uint16_t*)d_in[0], (const uint16_t*)d_in[1], (const uint16_t*)d_in[2],
                                       (const uint16_t*)d_in[3], (uint16_t*)d_out
  );
}
```
